```python
import torch

def _softmax(x, axis=-1):
    return torch.softmax(x, dim=axis)

def _gelu(x):
    return torch.nn.functional.gelu(x, approximate='tanh')

def _silu(x):
    return torch.nn.functional.silu(x)

def _sigmoid(x):
    return torch.sigmoid(x)

def _layernorm(x, axis=-1, eps=1e-05):
    m = x.mean(dim=axis, keepdim=True)
    v = x.var(dim=axis, keepdim=True, unbiased=False)
    return (x - m) / torch.sqrt(v + eps)

def _scatter_add(tgt, idx, src, axis=0):
    return tgt.clone().index_add_(axis, idx, src)

def _segment_sum(src, seg, n):
    out = torch.zeros((n,) + src.shape[1:], dtype=src.dtype)
    return out.index_add_(0, seg, src)

def _pad(x, pw):
    flat = tuple((v for pair in reversed(pw) for v in pair))
    return torch.nn.functional.pad(x, flat)
kernel_name = 'gated_mix__memory_aug_recurrent_mem_tr__sliding_local_dilated__prompt'

def setup_inputs():

    _w = lambda a: (lambda t: t.to(torch.bfloat16) if t.is_floating_point() else t)(torch.as_tensor(a))

    def _A_setup():
        B, S, Dm = (1, 2048, 640)
        H, Dh = (32, 192)
        M, seg = (8, 32)
        seg = min(seg, S)
        S = S // seg * seg
        return {'x': _w((lambda *s: torch.randn(*s, dtype=torch.bfloat16) * 0.05)(B, S, Dm)), 'mem0': _w((lambda *s: torch.randn(*s, dtype=torch.bfloat16) * 1.6)(B, M, Dm)), 'Wq': _w((lambda *s: torch.randn(*s, dtype=torch.bfloat16) * 0.8)(Dm, H, Dh) * Dm ** (-0.5)), 'Wk': _w((lambda *s: torch.randn(*s, dtype=torch.bfloat16) * 0.8)(Dm, H, Dh) * Dm ** (-0.5)), 'Wv': _w((lambda *s: torch.randn(*s, dtype=torch.bfloat16) * 1.6)(Dm, H, Dh) * Dm ** (-0.5)), 'Wo': _w((lambda *s: torch.randn(*s, dtype=torch.bfloat16) * 1.6)(H, Dh, Dm) * (H * Dh) ** (-0.5)), 'seg_len': seg}
    _A_in = _A_setup()

    def _B_setup():
        B, S, D = (1, 2048, 640)
        return {'x': _w((lambda *s: torch.randn(*s, dtype=torch.bfloat16) * 0.05)(B, S, D)), 'wqkv': _w((lambda *s: torch.randn(*s, dtype=torch.bfloat16) * 0.8)(D, 3 * D)), 'wo': _w((lambda *s: torch.randn(*s, dtype=torch.bfloat16) * 0.05)(D, D))}
    _B_in = _B_setup()
    return {'A_x': _A_in['x'], 'A_mem0': _A_in['mem0'], 'A_Wq': _A_in['Wq'], 'A_Wk': _A_in['Wk'], 'A_Wv': _A_in['Wv'], 'A_Wo': _A_in['Wo'], 'A_seg_len': _A_in['seg_len'], 'B_x': _B_in['x'], 'B_wqkv': _B_in['wqkv'], 'B_wo': _B_in['wo'], 'gate_w': _w((lambda *s: torch.randn(*s, dtype=torch.bfloat16) * 0.05)(640) * 16.0)}

def reference(A_x, A_mem0, A_Wq, A_Wk, A_Wv, A_Wo, A_seg_len, B_x, B_wqkv, B_wo, gate_w):

    def _A_ref(x, mem0, Wq, Wk, Wv, Wo, seg_len):
        B, S, Dm = x.shape
        H, Dh = (Wq.shape[1], Wq.shape[2])
        M = mem0.shape[1]
        scale = Dh ** (-0.5)
        n_seg = S // seg_len
        L = M + seg_len
        idx = torch.arange(L)
        mask = (idx[:, None] < M) | (idx[:, None] >= idx[None, :])
        mem = mem0
        outs = []
        for s in range(n_seg):
            xs = x[:, s * seg_len:(s + 1) * seg_len, :].clone()
            h = torch.concatenate([mem, xs], axis=1)
            q = torch.einsum('btd,dhe->bhte', h, Wq)
            k = torch.einsum('btd,dhe->bhte', h, Wk)
            v = torch.einsum('btd,dhe->bhte', h, Wv)
            scores = torch.einsum('bhqe,bhke->bhqk', q, k) * scale
            scores = torch.where(mask[None, None], scores, -1000000000.0)
            p = _softmax(scores, axis=-1)
            o = torch.einsum('bhqk,bhke->bhqe', p, v)
            o = torch.einsum('bhte,hed->btd', o, Wo)
            mem = o[:, :M, :].clone()
            outs.append(o[:, M:, :])
        return torch.concatenate(outs, axis=1)

    def _B_ref(x, wqkv, wo):
        B, S, D = x.shape
        H = 32
        Dh = D // H
        W, dil = (8, 2)
        qkv = (lambda x, *d: x.permute(*d))((x @ wqkv).reshape(B, S, 3, H, Dh), 2, 0, 3, 1, 4)
        q, k, v = (qkv[0], qkv[1], qkv[2])
        scale = Dh ** (-0.5)
        pos = torch.arange(S)
        off = torch.arange(W) * dil
        idx = pos[:, None] - off[None, :]
        mask = idx >= 0
        idx_c = torch.where(mask, idx, 0).reshape(-1)
        kg = k[:, :, idx_c, :].reshape(B, H, S, W, Dh)
        vg = v[:, :, idx_c, :].reshape(B, H, S, W, Dh)
        logits = torch.einsum('bhsd,bhswd->bhsw', q, kg) * scale
        logits = torch.where(mask[None, None, :, :], logits, -1000000000.0)
        attn = _softmax(logits, axis=-1)
        o = torch.einsum('bhsw,bhswd->bhsd', attn, vg)
        return (lambda x, *d: x.permute(*d))(o, 0, 2, 1, 3).reshape(B, S, D) @ wo
    x = A_x
    g = _sigmoid((x * gate_w).sum(-1, keepdims=True))
    a = _A_ref(x, A_mem0, A_Wq, A_Wk, A_Wv, A_Wo, A_seg_len)
    b = _B_ref(x, B_wqkv, B_wo)
    return g * a + (1 - g) * b
```

```cpp
#include <hip/hip_runtime.h>
#pragma clang diagnostic ignored "-Wint-to-pointer-cast"

typedef unsigned short u16;
typedef unsigned int u32;
typedef unsigned long long u64;
typedef __bf16 bf16x8 __attribute__((ext_vector_type(8)));
typedef __bf16 bf16x16 __attribute__((ext_vector_type(16)));
typedef short s16x4 __attribute__((ext_vector_type(4)));
typedef float f32x4 __attribute__((ext_vector_type(4)));
typedef unsigned u32x4 __attribute__((ext_vector_type(4)));
typedef unsigned u32x2 __attribute__((ext_vector_type(2)));
typedef unsigned u32x8 __attribute__((ext_vector_type(8)));
typedef unsigned u32x16 __attribute__((ext_vector_type(16)));
typedef unsigned u32x32 __attribute__((ext_vector_type(32)));
#define DI __device__ __forceinline__
#define LDSP(T, a) ((__attribute__((address_space(3))) T*)(a))

constexpr int S_LEN = 2048, DM = 640, NH = 32, DH = 192, NSEG = 64, HD = NH * DH;
#define NSAMP 4
constexpr int NREG = 13;
constexpr int NV = 80, NVB = 40;
constexpr int NS = 64;
#define CFG 218, 145, 108, 30, 62, 12, 199, 134, 48, 182, 120, 6

constexpr size_t U_STRIDE = NV * 4096, D_STRIDE = NV * 16384;
constexpr size_t HO_STRIDE = 65536;
constexpr size_t OFF_ST = NSEG * HO_STRIDE;
enum { F_ACK = 0, F_ONE = 3, F_VD = 16, F_QH = 96, F_QKVB = 104, F_BA = 112, F_BO = 120, F_CUR = 128, F_PROG = 136 - 8 * 62, F_UD = 152, F_PX = 160, F_K = 224, F_END = 512, F_Q = 520, F_N = 592 };
constexpr size_t OFF_LAT = OFF_ST + F_N * 128;
constexpr size_t OFF_KX = 5u << 20;
constexpr size_t OFF_QX = OFF_KX + (size_t)S_LEN * HD * 2;
constexpr size_t OFF_D = OFF_QX + (size_t)S_LEN * HD * 2;
constexpr size_t OFF_U = (OFF_D + NSEG * D_STRIDE + 0x1fffff) & ~(size_t)0x1fffff;
constexpr size_t OFF_KM = OFF_U + (22u << 20);
constexpr size_t OFF_PX = OFF_KM + (size_t)NSEG * NH * 3072;
constexpr size_t OFF_QKVB = OFF_PX + (size_t)NSEG * 81920;
constexpr size_t OFF_OB = OFF_QKVB + (size_t)S_LEN * 1920 * 2;
constexpr size_t OFF_BOUT = OFF_OB + (size_t)S_LEN * DM * 2;
constexpr size_t OFF_GATE = OFF_BOUT + (size_t)S_LEN * DM * 2;
constexpr size_t OFF_XF = OFF_GATE + S_LEN * 4;
constexpr size_t WS_END = OFF_XF + (size_t)S_LEN * DM * 2;
static_assert(WS_END + (2u << 20) <= (256u << 20), "workspace");

typedef __bf16 bf16x2 __attribute__((ext_vector_type(2)));
typedef float f32x2 __attribute__((ext_vector_type(2)));
DI u32 cvtpk(float lo, float hi) { return __builtin_bit_cast(u32, __builtin_convertvector(f32x2{lo, hi}, bf16x2)); }
DI float rbf(float f) { return __uint_as_float(cvtpk(f, f) << 16); }
DI f32x4 mfma16(u32x4 a, u32x4 b, f32x4 c) {
    return __builtin_amdgcn_mfma_f32_16x16x32_bf16(__builtin_bit_cast(bf16x8, a), __builtin_bit_cast(bf16x8, b), c, 0, 0, 0);
}
DI u32x2 trread(u32 a) {
    return __builtin_bit_cast(u32x2, __builtin_amdgcn_ds_read_tr16_b64_v4i16(LDSP(s16x4, a)));
}
DI void glds16c(const char* sbase, u32 voff, u32 lds) {
    u32 sv;
    asm volatile("s_mov_b32 %0, m0\n\ts_mov_b32 m0, %3\n\ts_nop 0\n\tglobal_load_lds_dwordx4 %1, %2 sc1\n\ts_mov_b32 m0, %0"
                 : "=&s"(sv) : "v"(voff), "s"(sbase), "s"(lds) : "memory");
}
template <int IMM>
DI void gld16(u32x4& r, const char* sbase, u32 voff) {
    asm volatile("s_nop 4\n\tglobal_load_dwordx4 %0, %1, %2 offset:%3" : "=v"(r) : "v"(voff), "s"(sbase), "i"(IMM) : "memory");
}
DI void gst16(char* sbase, u32 voff, u32x4 v) {
    asm volatile("global_store_dwordx4 %0, %1, %2 sc1\n\ts_nop 1" : : "v"(voff), "v"(v), "s"(sbase) : "memory");
}
#define OST " nt"
DI void gst16p(char* sbase, u32 voff, u32x4 v) {
    asm volatile("s_nop 4\n\tglobal_store_dwordx4 %0, %1, %2" OST "\n\ts_nop 1" : : "v"(voff), "v"(v), "s"(sbase) : "memory");
}
DI void gst8(char* sbase, u32 voff, u32x2 v) {
    asm volatile("global_store_dwordx2 %0, %1, %2 sc1\n\ts_nop 1" : : "v"(voff), "v"(v), "s"(sbase) : "memory");
}
template <int N>
DI void waitvm() { asm volatile("s_waitcnt vmcnt(%0)" : : "i"(N) : "memory"); }
DI void barrier() { asm volatile("s_waitcnt lgkmcnt(0)\n\ts_barrier" : : : "memory"); }
#define PIN() __builtin_amdgcn_sched_barrier(0)
DI int uni(int v) { return __builtin_amdgcn_readfirstlane(v); }
DI int lanex() {
    int t;
    asm volatile("v_mbcnt_lo_u32_b32 %0, -1, 0\n\tv_mbcnt_hi_u32_b32 %0, -1, %0" : "=v"(t));
    __builtin_assume(t >= 0 && t < 64);
    return t;
}
DI void flag_ld(u32& r, const char* fl, int idx) {
    asm volatile("s_nop 4\n\tglobal_load_dword %0, %1, %2 sc1" : "=v"(r) : "v"(idx * 128), "s"(fl) : "memory");
}
DI void flag_ldm(u32& r, const char* fl, int idx, bool on) {
    asm volatile("v_mov_b32 %0, 1000\n\ts_mov_b32 exec_lo, %3\n\ts_mov_b32 exec_hi, 0\n\tglobal_load_dword %0, %1, %2 sc1\n\ts_mov_b64 exec, -1" : "=&v"(r) : "v"(idx * 128), "s"(fl), "s"(uni(on ? 1 : 0)) : "memory");
}
DI void flag_wait(const char* fl, int idx, int target) {
#pragma nounroll
    for (int it = 0; it < (1 << 16); ++it) {
        u32 v;
        flag_ld(v, fl, idx);
        asm volatile("s_waitcnt vmcnt(0)" : "+v"(v) : : "memory");
        if (uni((int)(v + 1u)) >= target) break;
        __builtin_amdgcn_s_sleep(6);
    }
}
DI void flag_add(char* fl, int idx, int nrep) {
    const int lane = lanex();
    if (lane < nrep) asm volatile("s_nop 4\n\tglobal_atomic_add %0, %1, %2 sc1" : : "v"((idx + lane) * 128), "v"(1), "s"(fl) : "memory");
}
DI void acquire() { asm volatile("buffer_inv sc1\n\ts_waitcnt vmcnt(0)" : : : "memory"); }

template <int V> struct IC { static constexpr int v = V; };
template <int B, int E, class Fn>
DI void sfor(Fn&& fn) { if constexpr (B < E) { fn(IC<B>{}); sfor<B + 1, E>(fn); } }
enum { T_QK = 0, T_VD = 1, T_BL = 3 };
constexpr int GBUF = 24576, GRING = 6;
struct GemmArgs {
    const char *w1, *w1b;
    const char* w2;
    const char* x;
    char *out, *outb;
    int ldw;
    char* sig;
    int type, np1, tn;
    int sV, sP, sQ;
    int xb;
};
DI const char* unip(const char* p) {
    const u64 v = (u64)p;
    return (const char*)(((u64)(u32)uni((int)(v >> 32)) << 32) | (u32)uni((int)v));
}
template <int Z>
DI void mf1(f32x4& c, const u32x4& a, const u32x4& b) {
    if constexpr (Z) asm volatile("v_mfma_f32_16x16x32_bf16 %0, %1, %2, 0" : "=&v"(c) : "v"(a), "a"(b));
    else asm volatile("v_mfma_f32_16x16x32_bf16 %0, %1, %2, %0" : "+v"(c) : "v"(a), "a"(b));
}
template <int Z>
DI void mf2(f32x4& c, const u32x4& a, const u32x4& b) {
    if constexpr (Z) asm volatile("v_mfma_f32_16x16x32_bf16 %0, %1, %2, 0" : "=&v"(c) : "v"(a), "v"(b));
    else asm volatile("v_mfma_f32_16x16x32_bf16 %0, %1, %2, %0" : "+v"(c) : "v"(a), "v"(b));
}
template <int IMM>
DI void gld16a(u32x4& r, const char* sbase, u32 voff) {
    asm volatile("global_load_dwordx4 %0, %1, %2 offset:%3" : "+a"(r) : "v"(voff), "s"(sbase), "i"(IMM) : "memory");
}
DI void setm0(u32 v) { asm volatile("s_mov_b32 m0, %0" : : "s"(v) : "memory"); }
template <int IMM>
DI void gldsm(const char* sbase, u32 voff) {
    asm volatile("global_load_lds_dwordx4 %0, %1 offset:%2" : : "v"(voff), "s"(sbase), "i"(IMM) : "memory");
}
DI void gst16n(char* sbase, u32 voff, u32x4 v) {
    asm volatile("global_store_dwordx4 %0, %1, %2 sc1" : : "v"(voff), "v"(v), "s"(sbase) : "memory");
}
template <int N>
DI void waitbar() { asm volatile("s_waitcnt vmcnt(%0)\n\ts_barrier" : : "i"(N) : "memory"); }
#define LGKM(n) __builtin_amdgcn_s_waitcnt(0xC07F | ((n) << 8))

DI int xhalf() { u32 x; asm volatile("s_getreg_b32 %0, hwreg(HW_REG_XCC_ID)" : "=s"(x)); return (int)((x >> 2) & 1); }
struct Sync { u32x2 r, g, e; };
DI void sync_ld(Sync& y, const char* ws) {
    asm volatile("s_nop 4\n\tglobal_load_dwordx2 %0, %3, %5 sc1\n\tglobal_load_dwordx2 %1, %4, %5 offset:512 sc1\n\tglobal_load_dwordx2 %2, %4, %5 offset:1024 sc1"
                 : "=&v"(y.r), "=&v"(y.g), "=&v"(y.e) : "v"((lanex() & 31) * 8), "v"(0), "s"(ws + OFF_LAT) : "memory");
}
DI u64 sync_tag(u32x2 e) { return (((u64)e[1] << 32 | e[0]) + 1) << 1; }
DI int sync_end(Sync& y, const char* ws, u32& phi, bool ack) {
    asm volatile("" : "+v"(y.r), "+v"(y.g), "+v"(y.e));
#pragma nounroll
    for (int it = 0; it < (1 << 16); ++it) {
        const u64 t = sync_tag(y.e);
        if (__builtin_amdgcn_ballot_w64((y.r[0] & ~1u) != (u32)t || y.r[1] != (u32)(t >> 32) || y.g[0] != (u32)t) == 0) break;
        __builtin_amdgcn_s_sleep(24);
        sync_ld(y, ws);
        asm volatile("s_waitcnt vmcnt(0)" : "+v"(y.r), "+v"(y.g), "+v"(y.e) : : "memory");
    }
    if (ack) flag_add((char*)ws + OFF_ST, F_ACK, 1);
    phi = (u32)uni((int)y.g[1]);
    const u32 b = (u32)__builtin_amdgcn_ballot_w64(y.r[0] & 1), all = __builtin_popcount(b) > 16;
    u32 q = 0;
#pragma unroll
    for (int r = 0; r < NREG; ++r) {
        const u32 c = r < 2 ? __builtin_popcount(b & (0x55u << r)) + all : 2 * __builtin_popcount((b >> (4 + 2 * r)) & 3) + all;
        q |= ((c > 2) ^ (u32)((r < 2 ? r : r + (OFF_U >> 21)) & 1)) << r;
    }
    return uni((int)(q ^ 3u * xhalf()));
}
DI void pace(int on) {
    asm volatile("s_cmp_ge_i32 %0, 0\n\ts_cbranch_scc1 .Lp%=\n\ts_waitcnt vmcnt(0)\n.Lp%=:" : : "s"(on) : "memory", "scc");
}
DI void early_add(char* ctr, int c) {
    asm volatile("s_cmp_lg_u32 %1, 1\n\ts_cbranch_scc1 .Le%=\n\ts_mov_b64 exec, 1\n\ts_nop 1\n\tglobal_atomic_add %2, %3, %0 sc1\n\ts_mov_b64 exec, -1\n.Le%=:"
                 : : "s"(ctr), "s"(c), "v"(0), "v"(1) : "memory", "scc");
}
#define XL 2
template <int NT>
DI void gemm_task(const GemmArgs& g, Sync& y, const char* ws, u32 lds, int w) {
    constexpr int FD = 5, NX = 2 * NT, OPS = NX + 6, BI = 24 - FD;
    const int lane = lanex();
    const int n = lane & 15, q = lane >> 4, qq = (lane >> 2) & 3, pp = lane & 3;
    const bool p2 = g.type == T_VD;
    const int rw = lane >> 3, sp = (lane >> 1) & 3;
    const u32 v2b = (u32)(rw * 1280 + 16 * (lane & 1) + 5120);
    auto v2o = [&](int i) { return v2b + (u32)(((sp ^ (rw >> 1)) << 5) + i * 9216); };
    u32 wv[6];
#pragma unroll
    for (int i = 0; i < 6; ++i)
        wv[i] = (u32)((rw + 8 * (i / 3)) * g.ldw + ((sp ^ (((rw >> 1) & 1) + 2 * (i / 3))) << 5) + 16 * (lane & 1) + (i % 3) * 128 + 5120 - 1024 * i);
    const char* const wp2 = g.w2 + (size_t)(48 * w) * 1280 - 2048;
    const char* wp = unip(g.w1 + (size_t)(16 * w) * g.ldw - 2048);
    int wstep = uni(64 * g.ldw);
    u32 wdst = lds + (u32)w * 6144 + 3072;
    const u32 wdlast = wdst + (GRING - 1) * GBUF;
    auto wring = [&]() { wdst = wdst == wdlast ? wdst - (GRING - 1) * GBUF : wdst + GBUF; };
    auto wload = [&](int i) {
        setm0(wdst);
        wring();
        asm volatile("s_nop 0");
        sfor<0, 6>([&](auto I) { gldsm<1024 * (I.v - 3)>(wp, wv[I.v]); });
        wp += wstep;
    };
    u32x4 vf[NT][6];
    u32x4 fr[FD + 1];
    u32x4 xr[5][NX];
    auto xtie = [&](u32x4 (&x)[NX]) {
#pragma unroll
        for (int i = 0; i < NX; i += 4) asm volatile("" : "+a"(x[i]), "+a"(x[i + 1]), "+a"(x[i + 2]), "+a"(x[i + 3]));
    };
#pragma unroll
    for (int i = 0; i < 5 * NX; ++i) asm volatile("" : "=a"(xr[i / NX][i % NX]));
    u32x2 flo;
    u32 c[4], slot = 0;
    int sd = GBUF;
    auto sring = [&]() { sd = slot == (GRING - 1) * GBUF ? -(GRING - 1) * GBUF : GBUF; slot += (u32)sd; };
    {
        u32 xo[NT];
#pragma unroll
        for (int tt = 0; tt < NT; ++tt) {
            const int tok = (g.type == T_VD) ? 8 * (n >> 2) + 4 * (tt & 1) + (n & 3) : 16 * (tt & 1) + n;
            xo[tt] = (u32)((NT / 2 * w + (tt >> 1)) * 40960 + (g.xb ? q * 512 + tok * 16 : q * 16 + tok * 1280));
        }
        f32x4 acc[12][NT];
        const bool rm = NT == 4 && g.type == T_BL;
        const int sw = (qq >> 1) + 2 * (q & 1);
#pragma unroll
        for (int t = 0; t < 4; ++t)
            c[t] = lds + q * 3072 + qq * 128 + (rm ? ((((t & 2) + (pp >> 1)) ^ sw) << 5) + 16 * (pp & 1) + 8 * ((pp >> 1) ^ (t & 1)) : ((t ^ sw) << 5) + 8 * pp);
        auto rlo = [&](int f) { flo = trread(c[f & 3] + (f / 12) * 12288 + ((f % 12) >> 2) * 1024); };
        auto rhi = [&](int f) {
            const u32x2 hi = trread(c[f & 3] + (f / 12) * 12288 + ((f % 12) >> 2) * 1024 + 512);
            fr[f % (FD + 1)] = u32x4{flo[0], flo[1], hi[0], hi[1]};
        };
        const int xk = uni(g.xb ? 2048 : 64);
        const char *xp = g.x, *xq = xp + xk;
        sfor<0, 4>([&](auto J) {
            if constexpr (J.v < XL) { sfor<0, NX>([&](auto I) { gld16a<0>(xr[J.v][I.v], I.v & 1 ? xq : xp, xo[I.v >> 1]); }); xp += 2 * xk; xq += 2 * xk; }
            wload(J.v);
        });
        waitbar<18 + (XL - 1) * NX>();
        xtie(xr[0]);
#pragma unroll
        for (int i = 0; i < FD; ++i) { rlo(i); rhi(i); }
        char* po = g.out;
#pragma nounroll
        for (int ph = g.np1; ph > 0; --ph) {
            sfor<0, 10>([&](auto J) {
                constexpr int j = J.v;
                u32x4 (&xc)[NX] = xr[j % 5];
                u32x4 (&xn)[NX] = xr[(j + XL) % 5];
                sfor<0, 24>([&](auto I) {
                    constexpr int idx = I.v, kk = idx / 12, T = idx % 12, z = j == 0 && kk == 0, f = (idx + FD) % 24;
                    constexpr int mo = NT == 4 ? (idx < BI && idx % 4 != 2 ? idx - (idx + 1) / 4 : -1) : (idx < BI && idx % 2 == 0 ? idx / 2 : -1);
                    auto mem = [&]() {
                        if constexpr (mo >= NX) gldsm<1024 * (mo - NX - 3)>(wp, wv[mo - NX]);
                        else if constexpr (mo >= 0) gld16a<0>(xn[mo], mo & 1 ? xq : xp, xo[mo >> 1]);
                    };
                    auto misc = [&]() {
                        if constexpr (idx == 1) wring();
                        if constexpr (idx == 13) sring();
                        if constexpr (j == 1 && idx == 22) early_add(g.sig, ph + w);
                        if constexpr (idx >= 15 && idx < BI) { c[idx - 15] += (u32)sd; asm volatile("" : "+v"(c[idx - 15])); }
                        const bool more = ph > 1;
                        if constexpr (j != 5) {
                            if constexpr (idx == 20) wp += wstep;
                        } else {
                            if constexpr (idx == 19) { wp = more ? g.w1b + (size_t)(16 * w) * g.ldw - 2048 : wp2; wstep = more ? wstep : (p2 ? 128 : 0); }
                            if constexpr (idx >= 20 && idx < 23) {
#pragma unroll
                                for (int i = 2 * (idx - 20); i < 2 * (idx - 19); ++i) { wv[i] = more ? wv[i] : (p2 ? v2o(i) : 5120u); asm volatile("" : "+v"(wv[i])); }
                            }
                        }
                        if constexpr (j != 9 - XL) {
                            if constexpr (idx == 21) xp += 2 * xk;
                            if constexpr (idx == 22) xq += 2 * xk;
                        } else if constexpr (idx == 23) {
                            xp = g.x; xq = xp + xk;
#pragma unroll
                            for (int i = 0; i < NT; ++i) { xo[i] = more ? xo[i] : 0u; asm volatile("" : "+v"(xo[i])); }
                        }
                    };
                    if constexpr (idx == BI) { waitbar<6 + (XL - 1) * OPS>(); xtie(xr[(j + 1) % 5]); }
                    if constexpr (idx % 2 == 0) LGKM(6);
                    PIN();
                    const u32x4 a = fr[idx % (FD + 1)];
                    mf1<z>(acc[T][0], a, xc[kk]); PIN();
                    rlo(f);
                    if constexpr (idx == 0) setm0(wdst);
                    if constexpr (NT == 2) rhi(f);
                    PIN();
                    mf1<z>(acc[T][1], a, xc[2 + kk]); PIN();
                    if constexpr (NT == 4) {
                        rhi(f); PIN();
                        mf1<z>(acc[T][2], a, xc[4 + kk]); PIN();
                        mem(); PIN();
                        mf1<z>(acc[T][3], a, xc[6 + kk]); PIN();
                    } else { mem(); PIN(); }
                    misc();
                    PIN();
                });
            });
            asm volatile("s_nop 7\n\ts_nop 1");
            PIN();
#pragma unroll
            for (int tt = 0; tt < NT; ++tt)
#pragma unroll
                for (int ks = 0; ks < 6; ++ks)
                    vf[tt][ks] = u32x4{cvtpk(acc[2 * ks][tt][0], acc[2 * ks][tt][1]), cvtpk(acc[2 * ks][tt][2], acc[2 * ks][tt][3]),
                                       cvtpk(acc[2 * ks + 1][tt][0], acc[2 * ks + 1][tt][1]), cvtpk(acc[2 * ks + 1][tt][2], acc[2 * ks + 1][tt][3])};
            if (g.type == T_QK) {
#pragma unroll
                for (int tt = 0; tt < NT; ++tt)
#pragma unroll
                    for (int ks = 0; ks < 6; ++ks)
                        gst16(po + (size_t)(NT / 2 * w + (tt >> 1)) * (NH * 12288) + (tt & 1) * 6144 + ks * 1024, lane * 16,
                              u32x4{vf[tt][ks][0], vf[tt][ks][2], vf[tt][ks][1], vf[tt][ks][3]});
            } else if (rm) {
                const bool sx = q >= 2;
#pragma unroll
                for (int tt = 0; tt < NT; ++tt)
#pragma unroll
                    for (int ks = 0; ks < 6; ++ks) {
                        const u32x4 v = vf[tt][ks];
                        gst16(po + (size_t)(2 * w + (tt >> 1)) * g.sV + (tt & 1) * 256 + ks * 2048, (u32)(q * 512 + n * 16),
                              u32x4{sx ? v[2] : v[0], sx ? v[3] : v[1], sx ? v[0] : v[2], sx ? v[1] : v[3]});
                    }
            }
            po = g.outb;
            PIN();
        }
    }
    if (p2) {
        constexpr int HI = 2048;
        const int r = 4 * (q & 1) + qq, sw = r >> 1;
#pragma unroll
        for (int t = 0; t < 4; ++t) c[t] = lds + slot + (q >> 1) * 1024 + r * 128 + ((t ^ sw) << 5) + 8 * pp;
        sring();
        u32 so = (u32)((n >> 3) * g.sV + q * g.sQ + (n & 7) * 16);
        char* const o0 = g.out + (size_t)(NT / 2 * w) * g.sP;
        char* const o1 = o0 + g.sP;
        const int sv2 = 2 * g.sV;
        auto rlo = [&](int f) { flo = trread(c[f / 6] + (f % 6) * 4096); };
        auto rhi = [&](int f) {
            const u32x2 hi = trread(c[f / 6] + (f % 6) * 4096 + HI);
            fr[f % (FD + 1)] = u32x4{flo[0], flo[1], hi[0], hi[1]};
        };
        f32x4 a2[2][NT];
        u32 e[NT / 2][4];
        auto cv = [&](f32x4 (&a)[NT], int i) { e[i >> 2][i & 3] = cvtpk(a[i >> 1][2 * (i & 1)], a[i >> 1][2 * (i & 1) + 1]); asm volatile("" : "+v"(e[i >> 2][i & 3])); };
        auto st = [&](int pl) { gst16n(pl ? o1 : o0, so, u32x4{e[pl][0], e[pl][1], e[pl][2], e[pl][3]}); };
        PIN();
#pragma unroll
        for (int i = 0; i < FD; ++i) { rlo(i); rhi(i); }
#pragma unroll
        for (int tt = 0; tt < NT; ++tt) a2[1][tt] = f32x4{0.f, 0.f, 0.f, 0.f};
#pragma nounroll
        for (int t = 0; t < g.tn; ++t) {
            sfor<0, 24>([&](auto I) {
                constexpr int idx = I.v, cb = idx / 6, ks = idx % 6, f = (idx + FD) % 24;
                f32x4 (&ac)[NT] = a2[cb & 1];
                f32x4 (&ap)[NT] = a2[(cb & 1) ^ 1];
                constexpr int mo = cb < 2 ? (ks == 0 ? 3 * cb : (ks >= 4 ? 3 * cb + ks - 3 : -1)) : -1;
                auto mem = [&]() {
                    if constexpr (mo >= 0) gldsm<1024 * (mo - 3)>(wp, wv[mo]);
                    if constexpr (ks == 3 || (NT == 4 && ks == 2)) { if (cb > 0 || t > 0) st(NT == 4 ? ks - 2 : 0); }
                };
                auto misc = [&]() {
                    if constexpr (idx == 12) wring();
                    if constexpr (idx == 23) sring();
                    if constexpr (idx == 16) { wp += t < g.tn - 5 ? 128 : 0; }
                    if constexpr (ks == 0) { c[cb] += (u32)sd; asm volatile("" : "+v"(c[cb])); }
                    if constexpr (ks == 4) { so += (cb > 0 || t > 0) ? (u32)sv2 : 0u; asm volatile("" : "+v"(so)); }
                };
                if constexpr (idx == BI) waitbar<NT == 4 ? 38 : 28>();
                if constexpr (idx % 2 == 0) LGKM(6);
                PIN();
                const u32x4 b = fr[idx % (FD + 1)];
                mf2<ks == 0>(ac[0], vf[0][ks], b); PIN();
                rlo(f);
                if constexpr (idx == 0) setm0(wdst);
                if constexpr (NT == 2) rhi(f);
                if constexpr (NT == 4 && ks >= 1 && ks <= 3) cv(ap, ks == 1 ? 0 : (ks == 2 ? 4 : 7));
                PIN();
                mf2<ks == 0>(ac[1], vf[1][ks], b); PIN();
                if constexpr (NT == 4) {
                    rhi(f);
                    if constexpr (ks == 1 || ks == 2) cv(ap, ks == 1 ? 1 : 5);
                    PIN();
                    mf2<ks == 0>(ac[2], vf[2][ks], b); PIN();
                    mem();
                    if constexpr (ks == 1) { cv(ap, 2); cv(ap, 3); }
                    PIN();
                    mf2<ks == 0>(ac[3], vf[3][ks], b); PIN();
                    if constexpr (ks == 2) cv(ap, 6);
                } else {
                    if constexpr (ks == 1 || ks == 2) { cv(ap, 2 * ks - 2); cv(ap, 2 * ks - 1); }
                    mem(); PIN();
                }
                misc();
                PIN();
            });
            asm volatile("s_nop 7\n\ts_nop 1");
        }
        PIN();
#pragma unroll
        for (int i = 0; i < 2 * NT; ++i) cv(a2[1], i);
#pragma unroll
        for (int pl = 0; pl < NT / 2; ++pl) st(pl);
        asm volatile("s_nop 1");
    }
    if constexpr (NT == 2) asm volatile(".Lkm_g2:"); else asm volatile(".Lkm_g4:");
    sync_ld(y, ws);
    waitvm<0>();
    asm volatile("" : "+v"(y.r), "+v"(y.g), "+v"(y.e));
    sfor<0, 5>([&](auto I) { xtie(xr[I.v]); });
}

struct Ptrs {
    const char *x, *mem0, *wq, *wk, *wv, *wo, *wqkvb, *wob, *gw;
    char* out;
    char* ws;
};

enum { Q_H = 0, Q_VD = 1, Q_KQ = 2, Q_QKVB = 3, Q_BA = 4, Q_BO = 5, Q_PX = 6, Q_OUT = 7 };
DI void run_gemm(const Ptrs& p, int ty, int b, int i, int hf, bool raw, Sync& y, u32 lds, int w) {
    GemmArgs g;
    g.sV = g.sP = g.sQ = 0; g.w2 = p.wo; g.ldw = HD * 2; g.np1 = 1; g.tn = 10;
    g.sig = p.ws + OFF_ST + F_END * 128;
    const char* xf = raw ? p.x : p.ws + OFF_XF;
    g.xb = !raw;
    int s0 = 8 * b;
    if (ty == Q_VD) {
        s0 = b < 2 ? 4 * b : s0 - 8;
        g.type = T_VD; g.w1 = g.w1b = p.wv + i * DH * 2;
        g.w2 = p.wo + (size_t)i * DH * 1280;
        g.out = g.outb = p.ws + OFF_D + (size_t)s0 * D_STRIDE + i * 512;
        g.sV = 16384; g.sP = (int)D_STRIDE; g.sQ = 128;
        if (b == 0) { g.tn = 5; g.w2 += hf * 640; g.out += hf * 40 * 16384; }
    } else if (ty == Q_H) {
        s0 = 0;
        g.type = T_QK; g.w1 = g.w1b = (b ? p.wq : p.wk) + i * DH * 2;
        g.out = g.outb = p.ws + (b ? OFF_QX : OFF_KX) + (size_t)i * 12288;
        b = 0;
    } else if (ty == Q_KQ) {
        s0 = b < 2 ? 4 * b : s0 - 8;
        g.type = T_QK; g.w1 = p.wk + i * DH * 2; g.w1b = p.wq + i * DH * 2;
        g.out = p.ws + OFF_KX + (size_t)(s0 * NH + i) * 12288;
        g.outb = g.out + (OFF_QX - OFF_KX);
        g.np1 = 2;
        g.sig = p.ws + OFF_ST + (F_K + b * 32 + i) * 128;
    } else if (ty == Q_QKVB) {
        g.type = T_BL; g.w1 = p.wqkvb + i * 768; g.w1b = g.w1 + 384; g.ldw = 3840;
        g.sV = 1920 * 64; g.np1 = 2;
        g.out = p.ws + OFF_QKVB + (size_t)s0 * g.sV + i * 48 * 512; g.outb = g.out + 24 * 512;
    } else {
        const int c0 = min(i * DH, DM - DH);
        g.type = T_BL; g.w1 = g.w1b = p.wob + c0 * 2; g.ldw = 1280;
        xf = p.ws + OFF_OB;
        g.sV = 40960;
        g.out = g.outb = p.ws + OFF_BOUT + (size_t)s0 * 40960 + c0 * 64;
    }
    g.x = xf + (size_t)s0 * 40960;
    if (ty <= Q_KQ && b < 2) gemm_task<2>(g, y, p.ws, lds, w);
    else gemm_task<4>(g, y, p.ws, lds, w);
}
DI int pblk(int s) { return s < 8 ? s >> 2 : min(s >> 3, 7) + 1; }

template <int IMM>
DI void gld16c(u32x4& r, const char* sbase, u32 voff) {
    asm volatile("global_load_dwordx4 %0, %1, %2 offset:%3 sc1" : "=v"(r) : "v"(voff), "s"(sbase), "i"(IMM) : "memory");
}
DI void gst4c(char* sbase, u32 voff, u32 v) {
    asm volatile("global_store_dword %0, %1, %2 sc1" : : "v"(voff), "v"(v), "s"(sbase) : "memory");
}
template <int N>
DI void waitv(u32x4& r) { asm volatile("s_waitcnt vmcnt(%1)" : "+v"(r) : "i"(N) : "memory"); }
DI u32 mt() { return (u32)__builtin_amdgcn_s_memtime(); }
DI u32 rt() { return (u32)__builtin_amdgcn_s_memrealtime(); }
DI void spin_to(u32 tgt) { while ((int)(mt() - tgt) < 0) {} }
DI u32 max3u(u32 a, u32 b, u32 c) { return max(max(a, b), c); }
DI u32x4 ldsr16(u32 a) { return *LDSP(u32x4, a); }
DI void clocks(u32& r, u32& m) {
    u64 a, b;
    asm volatile("s_memrealtime %0\n\ts_memtime %1\n\ts_waitcnt lgkmcnt(0)" : "=s"(a), "=s"(b) : : "memory");
    r = (u32)a; m = (u32)b;
}
#define SLATE 20
DI void edge(u32& r, u32& m) {
    u64 a, t[9];
#define RT(i) "s_memrealtime %" #i "\n\ts_nop 1\n\t"
    asm volatile("s_memtime %0\n\t" RT(1) RT(2) RT(3) RT(4) RT(5) RT(6) RT(7) RT(8) "s_memrealtime %9\n\ts_waitcnt lgkmcnt(0)"
                 : "=&s"(a), "=&s"(t[0]), "=&s"(t[1]), "=&s"(t[2]), "=&s"(t[3]), "=&s"(t[4]), "=&s"(t[5]), "=&s"(t[6]), "=&s"(t[7]), "=&s"(t[8]) : : "memory");
    m = (u32)a;
#pragma unroll
    for (int i = 1; i < 9; ++i) m += (u32)t[i] == (u32)t[0] ? 12 : 0;
    r = (u32)t[0] + 4;
}
#define SPB 90
struct Clock {
    u32 r0, m0, r1, m1; int cpt;
    DI void init() { edge(r0, m0); r1 = r0; m1 = m0; cpt = 24 * 64; }
    DI u32 sched(u32& phi, int ofs, int G) {
        edge(r1, m1);
        int rem = (int)(phi + (u32)ofs - r1);
        if (rem < -SLATE) { const int k = (-rem / G + 1) * G; phi += (u32)k; rem += k; }
        return m1 + (u32)((min(rem, 3000) * cpt) >> 6) - SPB;
    }
    DI u32 at(u32 t) { u32 r, m; clocks(r, m); return m + (u32)((min(max((int)(t - r), 0), 3000) * cpt) >> 6); }
    DI void calib() {
        const int dr = (int)(r1 - r0);
        if (dr >= 60) { cpt = uni((int)fminf(fmaxf((float)(int)(m1 - m0) * 64.f * __builtin_amdgcn_rcpf((float)dr), 256.f), 2560.f)); r0 = r1; m0 = m1; }
    }
};
#define VISS 0
#define VISV 0
DI void follow(u32& phi, int ofs, u32 t, int G) { const int late = (int)(t - phi) - ofs; if (late > 0) phi += (u32)((late / G + 1) * G); }
DI bool unwritten(const char* base, u32 off) {
#pragma nounroll
    for (int i = 0; i < 10; ++i) {
        u32x4 t;
        gld16c<-4096>(t, base + (i + (i & 8)) * 1024, off); waitv<0>(t);
        if (__builtin_amdgcn_ballot_w64(max3u(max(t[0], t[1]), t[2], t[3]) == 0xffffffffu) != 0) return true;
    }
    return false;
}

DI size_t uslot(int sd, int s) { const int r = (s * 43) >> 8; return OFF_U + (u32)uni(s * (int)U_STRIDE + r * (int)((2u << 20) - 6 * U_STRIDE) + ((sd >> (2 + r)) & 1) * 8192); }
DI size_t mslot(int sd, int s) { return (u32)uni(s * (int)HO_STRIDE + ((sd >> (s >> 5)) & 1) * 8192 + 4096); }
struct ChainCfg { int T, cR, G, dV, fast, s1, T2, cR2, s2, T3, cR3, dU; };
constexpr int TT_OFF = 88 + 48 + 192;
DI void karg_warm() {
    u32 a[10];
    asm volatile("s_load_dword %0, %10, 0\n\t" "s_load_dword %1, %10, 64\n\t" "s_load_dword %2, %10, 128\n\t" "s_load_dword %3, %10, 192\n\t"
                 "s_load_dword %4, %10, 256\n\t" "s_load_dword %5, %10, 320\n\t" "s_load_dword %6, %10, 384\n\t" "s_load_dword %7, %10, 448\n\t"
                 "s_load_dword %8, %10, 512\n\t" "s_load_dword %9, %10, 580\n\t" "s_waitcnt lgkmcnt(0)"
                 : "=&s"(a[0]), "=&s"(a[1]), "=&s"(a[2]), "=&s"(a[3]), "=&s"(a[4]), "=&s"(a[5]), "=&s"(a[6]), "=&s"(a[7]), "=&s"(a[8]), "=&s"(a[9])
                 : "s"(__builtin_amdgcn_kernarg_segment_ptr()) : "memory");
}
DI u32 sched_ld0(int s) {
    u32 r;
    asm volatile("s_load_dword %0, %1, %2 offset:%3\n\ts_waitcnt lgkmcnt(0)" : "=s"(r) : "s"(__builtin_amdgcn_kernarg_segment_ptr()), "s"(4 * s), "n"(TT_OFF) : "memory");
    return r;
}
#define HP 0
#define HQ 0

constexpr int SL_W = 0;
constexpr int SL_IMG = 131072;
constexpr int SL_KX = SL_IMG + 6656;
constexpr int SL_PF = SL_KX + 12288 + 1024;
constexpr int S_LDS = SL_PF + 256;
DI void nap(int t) { const u32 wake = rt() + (u32)t; while ((int)(rt() - wake) < 0) __builtin_amdgcn_s_sleep(4); }
DI void pfl(const char* sbase, u32 voff, u32 lds) {
    asm volatile("s_mov_b32 m0, %2\n\ts_nop 3\n\tglobal_load_lds_dword %0, %1" : : "v"(voff), "s"(sbase), "s"(lds + SL_PF) : "memory");
}
DI void score_role(const Ptrs& p, const ChainCfg cfg, int h, u32 lds, int w) {
    const int lane = lanex(), tid = 64 * w + lane;
    const int n = lane & 15, g = lane >> 4, qq = (lane >> 2) & 3, pp = lane & 3;
    u32x32 wa[8], wv[3];
    const u32 wreg = lds + SL_W + (u32)w * 32768;
    const u32 lfa = wreg + lane * 16;
    const char* kxp = p.ws + OFF_KX + (size_t)h * 12288 + w * 3072;
    const char* const kxend = p.ws + OFF_QX;
    const u32 kxl = lds + SL_KX;
    char* const fl = p.ws + OFF_ST;
    const u32 kxw = kxl + w * 3072 - (w < 2 ? 16 : 0);
    auto kxdma = [&]() {
        asm volatile("s_mov_b32 m0, %2\n\ts_nop 3\n\tglobal_load_lds_dwordx4 %0, %1 sc1\n\tglobal_load_lds_dwordx4 %0, %1 offset:1024 sc1\n\tglobal_load_lds_dwordx4 %0, %1 offset:2048 sc1"
                     : : "v"(lanex() * 16), "s"(kxp), "s"(kxw) : "memory");
        kxp = kxp + NH * 12288 < kxend ? kxp + NH * 12288 : kxp;
    };
    u32 kfl, phi;
    int sd;
    {
        constexpr int GS = 3104;
        Sync y;
        if (w < 3) {
            gst16(p.ws + OFF_KM + ((NSEG - 1) * NH + h) * 3072 + w * 1024, lane * 16, u32x4{~0u, ~0u, ~0u, ~0u});
            gst16(p.ws + OFF_PX + (NSEG - 1) * 81920 + (w < 2 ? h * 2048 + w * 1024 : 65536 + h * 512), w < 2 ? lane * 16 : (lane * 16) & 511, u32x4{~0u, ~0u, ~0u, ~0u});
        }
        auto dvo = [&](int i) { const int ix = i * 64 + lane; return (u32)((ix / 24) * (HD * 2) + (ix % 24) * 16); };
        const u32 tra = lds + (2 * (g >> 1) + (qq >> 1)) * GS + (4 * (g & 1) + (qq & 1)) * 384 + 8 * pp;
        u32x8 pa = u32x8{dvo(0), dvo(1), dvo(2), tra + 96 * w + 32 * (w & 1), tra + 96 * w - 64 * (w & 1), tra + 32 * w, lfa, (u32)(lane & 31) * 8};
        asm volatile("s_nop 4\n\t" "s_add_u32 s42, s46, 0x0\n\t" "s_addc_u32 s43, s47, 0\n\t" "s_add_u32 m0, %18, 0x0\n\t" "s_nop 0\n\t" "global_load_lds_dwordx4 v224, s[42:43]\n\t" "s_add_u32 m0, %18, 0x400\n\t"
                     "s_nop 0\n\t" "global_load_lds_dwordx4 v225, s[42:43]\n\t" "s_add_u32 m0, %18, 0x800\n\t" "s_nop 0\n\t" "global_load_lds_dwordx4 v226, s[42:43]\n\t" "s_add_u32 s42, s46, 0x18000\n\t"
                     "s_addc_u32 s43, s47, 0\n\t" "s_add_u32 m0, %18, 0xc20\n\t" "s_nop 0\n\t" "global_load_lds_dwordx4 v224, s[42:43]\n\t" "s_add_u32 m0, %18, 0x1020\n\t" "s_nop 0\n\t"
                     "global_load_lds_dwordx4 v225, s[42:43]\n\t" "s_add_u32 m0, %18, 0x1420\n\t" "s_nop 0\n\t" "global_load_lds_dwordx4 v226, s[42:43]\n\t" "s_add_u32 s42, s46, 0xc0000\n\t" "s_addc_u32 s43, s47, 0\n\t"
                     "s_add_u32 m0, %18, 0x6100\n\t" "s_nop 0\n\t" "global_load_lds_dwordx4 v224, s[42:43]\n\t" "s_add_u32 m0, %18, 0x6500\n\t" "s_nop 0\n\t" "global_load_lds_dwordx4 v225, s[42:43]\n\t"
                     "s_add_u32 m0, %18, 0x6900\n\t" "s_nop 0\n\t" "global_load_lds_dwordx4 v226, s[42:43]\n\t" "s_add_u32 s42, s46, 0xd8000\n\t" "s_addc_u32 s43, s47, 0\n\t" "s_add_u32 m0, %18, 0x6d20\n\t" "s_nop 0\n\t"
                     "global_load_lds_dwordx4 v224, s[42:43]\n\t" "s_add_u32 m0, %18, 0x7120\n\t" "s_nop 0\n\t" "global_load_lds_dwordx4 v225, s[42:43]\n\t" "s_add_u32 m0, %18, 0x7520\n\t" "s_nop 0\n\t"
                     "global_load_lds_dwordx4 v226, s[42:43]\n\t" "s_add_u32 s42, s46, 0x180000\n\t" "s_addc_u32 s43, s47, 0\n\t" "s_add_u32 m0, %18, 0xc200\n\t" "s_nop 0\n\t" "global_load_lds_dwordx4 v224, s[42:43]\n\t"
                     "s_add_u32 m0, %18, 0xc600\n\t" "s_nop 0\n\t" "global_load_lds_dwordx4 v225, s[42:43]\n\t" "s_add_u32 m0, %18, 0xca00\n\t" "s_nop 0\n\t" "global_load_lds_dwordx4 v226, s[42:43]\n\t"
                     "s_add_u32 s42, s46, 0x198000\n\t" "s_addc_u32 s43, s47, 0\n\t" "s_add_u32 m0, %18, 0xce20\n\t" "s_nop 0\n\t" "global_load_lds_dwordx4 v224, s[42:43]\n\t" "s_add_u32 m0, %18, 0xd220\n\t" "s_nop 0\n\t"
                     "global_load_lds_dwordx4 v225, s[42:43]\n\t" "s_add_u32 m0, %18, 0xd620\n\t" "s_nop 0\n\t" "global_load_lds_dwordx4 v226, s[42:43]\n\t" "s_add_u32 s42, s46, 0x240000\n\t" "s_addc_u32 s43, s47, 0\n\t"
                     "s_add_u32 m0, %18, 0x12300\n\t" "s_nop 0\n\t" "global_load_lds_dwordx4 v224, s[42:43]\n\t" "s_add_u32 m0, %18, 0x12700\n\t" "s_nop 0\n\t" "global_load_lds_dwordx4 v225, s[42:43]\n\t"
                     "s_add_u32 m0, %18, 0x12b00\n\t" "s_nop 0\n\t" "global_load_lds_dwordx4 v226, s[42:43]\n\t" "s_add_u32 s42, s46, 0x258000\n\t" "s_addc_u32 s43, s47, 0\n\t" "s_add_u32 m0, %18, 0x12f20\n\t"
                     "s_nop 0\n\t" "global_load_lds_dwordx4 v224, s[42:43]\n\t" "s_add_u32 m0, %18, 0x13320\n\t" "s_nop 0\n\t" "global_load_lds_dwordx4 v225, s[42:43]\n\t" "s_add_u32 m0, %18, 0x13720\n\t" "s_nop 0\n\t"
                     "global_load_lds_dwordx4 v226, s[42:43]\n\t" "s_waitcnt vmcnt(12) lgkmcnt(0)\n\t" "s_barrier\n\t" "s_add_u32 s42, s46, 0x300000\n\t" "s_addc_u32 s43, s47, 0\n\t" "s_add_u32 m0, %18, 0x18400\n\t"
                     "s_nop 0\n\t" "global_load_lds_dwordx4 v224, s[42:43]\n\t" "s_add_u32 m0, %18, 0x18800\n\t" "s_nop 0\n\t" "global_load_lds_dwordx4 v225, s[42:43]\n\t" "s_add_u32 m0, %18, 0x18c00\n\t" "s_nop 0\n\t"
                     "global_load_lds_dwordx4 v226, s[42:43]\n\t" "s_add_u32 s42, s46, 0x318000\n\t" "s_addc_u32 s43, s47, 0\n\t" "s_add_u32 m0, %18, 0x19020\n\t" "s_nop 0\n\t" "global_load_lds_dwordx4 v224, s[42:43]\n\t"
                     "s_add_u32 m0, %18, 0x19420\n\t" "s_nop 0\n\t" "global_load_lds_dwordx4 v225, s[42:43]\n\t" "s_add_u32 m0, %18, 0x19820\n\t" "s_nop 0\n\t" "global_load_lds_dwordx4 v226, s[42:43]\n\t"
                     "s_add_u32 s42, s46, 0x3c0000\n\t" "s_addc_u32 s43, s47, 0\n\t" "s_add_u32 m0, %18, 0x1e500\n\t" "s_nop 0\n\t" "global_load_lds_dwordx4 v224, s[42:43]\n\t" "s_add_u32 m0, %18, 0x1e900\n\t"
                     "s_nop 0\n\t" "global_load_lds_dwordx4 v225, s[42:43]\n\t" "s_add_u32 m0, %18, 0x1ed00\n\t" "s_nop 0\n\t" "global_load_lds_dwordx4 v226, s[42:43]\n\t" "s_add_u32 s42, s46, 0x3d8000\n\t"
                     "s_addc_u32 s43, s47, 0\n\t" "s_add_u32 m0, %18, 0x1f120\n\t" "s_nop 0\n\t" "global_load_lds_dwordx4 v224, s[42:43]\n\t" "s_add_u32 m0, %18, 0x1f520\n\t" "s_nop 0\n\t"
                     "global_load_lds_dwordx4 v225, s[42:43]\n\t" "s_add_u32 m0, %18, 0x1f920\n\t" "s_nop 0\n\t" "global_load_lds_dwordx4 v226, s[42:43]\n\t" "v_add_u32 v232, 0x0, v229\n\t"
                     "ds_read_b64_tr_b16 v[8:9], v232\n\t" "ds_read_b64_tr_b16 v[10:11], v232 offset:768\n\t" "ds_read_b64_tr_b16 v[12:13], v232 offset:12416\n\t" "ds_read_b64_tr_b16 v[14:15], v232 offset:13184\n\t"
                     "ds_read_b64_tr_b16 a[96:97], v232 offset:128\n\t" "ds_read_b64_tr_b16 a[98:99], v232 offset:896\n\t" "ds_read_b64_tr_b16 a[100:101], v232 offset:12544\n\t"
                     "ds_read_b64_tr_b16 a[102:103], v232 offset:13312\n\t" "ds_read_b64_tr_b16 a[176:177], v232 offset:256\n\t" "ds_read_b64_tr_b16 a[178:179], v232 offset:1024\n\t"
                     "ds_read_b64_tr_b16 a[180:181], v232 offset:12672\n\t" "ds_read_b64_tr_b16 a[182:183], v232 offset:13440\n\t" "ds_read_b64_tr_b16 v[24:25], v232 offset:24832\n\t"
                     "ds_read_b64_tr_b16 v[26:27], v232 offset:25600\n\t" "ds_read_b64_tr_b16 v[28:29], v232 offset:37248\n\t" "ds_read_b64_tr_b16 v[30:31], v232 offset:38016\n\t"
                     "ds_read_b64_tr_b16 a[104:105], v232 offset:24960\n\t" "ds_read_b64_tr_b16 a[106:107], v232 offset:25728\n\t" "ds_read_b64_tr_b16 a[108:109], v232 offset:37376\n\t"
                     "ds_read_b64_tr_b16 a[110:111], v232 offset:38144\n\t" "ds_read_b64_tr_b16 a[184:185], v232 offset:25088\n\t" "ds_read_b64_tr_b16 a[186:187], v232 offset:25856\n\t"
                     "ds_read_b64_tr_b16 a[188:189], v232 offset:37504\n\t" "ds_read_b64_tr_b16 a[190:191], v232 offset:38272\n\t" "s_waitcnt vmcnt(12) lgkmcnt(0)\n\t" "s_barrier\n\t" "s_add_u32 s42, s46, 0x480000\n\t"
                     "s_addc_u32 s43, s47, 0\n\t" "s_add_u32 m0, %18, 0x0\n\t" "s_nop 0\n\t" "global_load_lds_dwordx4 v224, s[42:43]\n\t" "s_add_u32 m0, %18, 0x400\n\t" "s_nop 0\n\t"
                     "global_load_lds_dwordx4 v225, s[42:43]\n\t" "s_add_u32 m0, %18, 0x800\n\t" "s_nop 0\n\t" "global_load_lds_dwordx4 v226, s[42:43]\n\t" "s_add_u32 s42, s46, 0x498000\n\t" "s_addc_u32 s43, s47, 0\n\t"
                     "s_add_u32 m0, %18, 0xc20\n\t" "s_nop 0\n\t" "global_load_lds_dwordx4 v224, s[42:43]\n\t" "s_add_u32 m0, %18, 0x1020\n\t" "s_nop 0\n\t" "global_load_lds_dwordx4 v225, s[42:43]\n\t"
                     "s_add_u32 m0, %18, 0x1420\n\t" "s_nop 0\n\t" "global_load_lds_dwordx4 v226, s[42:43]\n\t" "s_add_u32 s42, s46, 0x540000\n\t" "s_addc_u32 s43, s47, 0\n\t" "s_add_u32 m0, %18, 0x6100\n\t" "s_nop 0\n\t"
                     "global_load_lds_dwordx4 v224, s[42:43]\n\t" "s_add_u32 m0, %18, 0x6500\n\t" "s_nop 0\n\t" "global_load_lds_dwordx4 v225, s[42:43]\n\t" "s_add_u32 m0, %18, 0x6900\n\t" "s_nop 0\n\t"
                     "global_load_lds_dwordx4 v226, s[42:43]\n\t" "s_add_u32 s42, s46, 0x558000\n\t" "s_addc_u32 s43, s47, 0\n\t" "s_add_u32 m0, %18, 0x6d20\n\t" "s_nop 0\n\t" "global_load_lds_dwordx4 v224, s[42:43]\n\t"
                     "s_add_u32 m0, %18, 0x7120\n\t" "s_nop 0\n\t" "global_load_lds_dwordx4 v225, s[42:43]\n\t" "s_add_u32 m0, %18, 0x7520\n\t" "s_nop 0\n\t" "global_load_lds_dwordx4 v226, s[42:43]\n\t"
                     "v_add_u32 v232, 0xc200, v229\n\t" "ds_read_b64_tr_b16 v[40:41], v232\n\t" "ds_read_b64_tr_b16 v[42:43], v232 offset:768\n\t" "ds_read_b64_tr_b16 v[44:45], v232 offset:12416\n\t"
                     "ds_read_b64_tr_b16 v[46:47], v232 offset:13184\n\t" "ds_read_b64_tr_b16 a[112:113], v232 offset:128\n\t" "ds_read_b64_tr_b16 a[114:115], v232 offset:896\n\t"
                     "ds_read_b64_tr_b16 a[116:117], v232 offset:12544\n\t" "ds_read_b64_tr_b16 a[118:119], v232 offset:13312\n\t" "ds_read_b64_tr_b16 a[192:193], v232 offset:256\n\t"
                     "ds_read_b64_tr_b16 a[194:195], v232 offset:1024\n\t" "ds_read_b64_tr_b16 a[196:197], v232 offset:12672\n\t" "ds_read_b64_tr_b16 a[198:199], v232 offset:13440\n\t"
                     "ds_read_b64_tr_b16 v[56:57], v232 offset:24832\n\t" "ds_read_b64_tr_b16 v[58:59], v232 offset:25600\n\t" "ds_read_b64_tr_b16 v[60:61], v232 offset:37248\n\t"
                     "ds_read_b64_tr_b16 v[62:63], v232 offset:38016\n\t" "ds_read_b64_tr_b16 a[120:121], v232 offset:24960\n\t" "ds_read_b64_tr_b16 a[122:123], v232 offset:25728\n\t"
                     "ds_read_b64_tr_b16 a[124:125], v232 offset:37376\n\t" "ds_read_b64_tr_b16 a[126:127], v232 offset:38144\n\t" "ds_read_b64_tr_b16 a[200:201], v232 offset:25088\n\t"
                     "ds_read_b64_tr_b16 a[202:203], v232 offset:25856\n\t" "ds_read_b64_tr_b16 a[204:205], v232 offset:37504\n\t" "ds_read_b64_tr_b16 a[206:207], v232 offset:38272\n\t" "s_waitcnt vmcnt(12) lgkmcnt(0)\n\t"
                     "s_barrier\n\t" "s_add_u32 s42, s46, 0x600000\n\t" "s_addc_u32 s43, s47, 0\n\t" "s_add_u32 m0, %18, 0xc200\n\t" "s_nop 0\n\t" "global_load_lds_dwordx4 v224, s[42:43]\n\t"
                     "s_add_u32 m0, %18, 0xc600\n\t" "s_nop 0\n\t" "global_load_lds_dwordx4 v225, s[42:43]\n\t" "s_add_u32 m0, %18, 0xca00\n\t" "s_nop 0\n\t" "global_load_lds_dwordx4 v226, s[42:43]\n\t"
                     "s_add_u32 s42, s46, 0x618000\n\t" "s_addc_u32 s43, s47, 0\n\t" "s_add_u32 m0, %18, 0xce20\n\t" "s_nop 0\n\t" "global_load_lds_dwordx4 v224, s[42:43]\n\t" "s_add_u32 m0, %18, 0xd220\n\t" "s_nop 0\n\t"
                     "global_load_lds_dwordx4 v225, s[42:43]\n\t" "s_add_u32 m0, %18, 0xd620\n\t" "s_nop 0\n\t" "global_load_lds_dwordx4 v226, s[42:43]\n\t" "s_add_u32 s42, s46, 0x6c0000\n\t" "s_addc_u32 s43, s47, 0\n\t"
                     "s_add_u32 m0, %18, 0x12300\n\t" "s_nop 0\n\t" "global_load_lds_dwordx4 v224, s[42:43]\n\t" "s_add_u32 m0, %18, 0x12700\n\t" "s_nop 0\n\t" "global_load_lds_dwordx4 v225, s[42:43]\n\t"
                     "s_add_u32 m0, %18, 0x12b00\n\t" "s_nop 0\n\t" "global_load_lds_dwordx4 v226, s[42:43]\n\t" "s_add_u32 s42, s46, 0x6d8000\n\t" "s_addc_u32 s43, s47, 0\n\t" "s_add_u32 m0, %18, 0x12f20\n\t"
                     "s_nop 0\n\t" "global_load_lds_dwordx4 v224, s[42:43]\n\t" "s_add_u32 m0, %18, 0x13320\n\t" "s_nop 0\n\t" "global_load_lds_dwordx4 v225, s[42:43]\n\t" "s_add_u32 m0, %18, 0x13720\n\t" "s_nop 0\n\t"
                     "global_load_lds_dwordx4 v226, s[42:43]\n\t" "v_add_u32 v232, 0x18400, v229\n\t" "ds_read_b64_tr_b16 v[72:73], v232\n\t" "ds_read_b64_tr_b16 v[74:75], v232 offset:768\n\t"
                     "ds_read_b64_tr_b16 v[76:77], v232 offset:12416\n\t" "ds_read_b64_tr_b16 v[78:79], v232 offset:13184\n\t" "ds_read_b64_tr_b16 a[128:129], v232 offset:128\n\t"
                     "ds_read_b64_tr_b16 a[130:131], v232 offset:896\n\t" "ds_read_b64_tr_b16 a[132:133], v232 offset:12544\n\t" "ds_read_b64_tr_b16 a[134:135], v232 offset:13312\n\t"
                     "ds_read_b64_tr_b16 a[208:209], v232 offset:256\n\t" "ds_read_b64_tr_b16 a[210:211], v232 offset:1024\n\t" "ds_read_b64_tr_b16 a[212:213], v232 offset:12672\n\t"
                     "ds_read_b64_tr_b16 a[214:215], v232 offset:13440\n\t" "ds_read_b64_tr_b16 v[88:89], v232 offset:24832\n\t" "ds_read_b64_tr_b16 v[90:91], v232 offset:25600\n\t"
                     "ds_read_b64_tr_b16 v[92:93], v232 offset:37248\n\t" "ds_read_b64_tr_b16 v[94:95], v232 offset:38016\n\t" "ds_read_b64_tr_b16 a[136:137], v232 offset:24960\n\t"
                     "ds_read_b64_tr_b16 a[138:139], v232 offset:25728\n\t" "ds_read_b64_tr_b16 a[140:141], v232 offset:37376\n\t" "ds_read_b64_tr_b16 a[142:143], v232 offset:38144\n\t"
                     "ds_read_b64_tr_b16 a[216:217], v232 offset:25088\n\t" "ds_read_b64_tr_b16 a[218:219], v232 offset:25856\n\t" "ds_read_b64_tr_b16 a[220:221], v232 offset:37504\n\t"
                     "ds_read_b64_tr_b16 a[222:223], v232 offset:38272\n\t" "s_waitcnt vmcnt(12) lgkmcnt(0)\n\t" "s_barrier\n\t" "s_add_u32 s42, s44, 0x0\n\t" "s_addc_u32 s43, s45, 0\n\t" "s_add_u32 m0, %18, 0x18400\n\t"
                     "s_nop 0\n\t" "global_load_lds_dwordx4 v224, s[42:43]\n\t" "s_add_u32 m0, %18, 0x18800\n\t" "s_nop 0\n\t" "global_load_lds_dwordx4 v225, s[42:43]\n\t" "s_add_u32 m0, %18, 0x18c00\n\t" "s_nop 0\n\t"
                     "global_load_lds_dwordx4 v226, s[42:43]\n\t" "s_add_u32 s42, s44, 0x18000\n\t" "s_addc_u32 s43, s45, 0\n\t" "s_add_u32 m0, %18, 0x19020\n\t" "s_nop 0\n\t" "global_load_lds_dwordx4 v224, s[42:43]\n\t"
                     "s_add_u32 m0, %18, 0x19420\n\t" "s_nop 0\n\t" "global_load_lds_dwordx4 v225, s[42:43]\n\t" "s_add_u32 m0, %18, 0x19820\n\t" "s_nop 0\n\t" "global_load_lds_dwordx4 v226, s[42:43]\n\t"
                     "s_add_u32 s42, s44, 0xc0000\n\t" "s_addc_u32 s43, s45, 0\n\t" "s_add_u32 m0, %18, 0x1e500\n\t" "s_nop 0\n\t" "global_load_lds_dwordx4 v224, s[42:43]\n\t" "s_add_u32 m0, %18, 0x1e900\n\t" "s_nop 0\n\t"
                     "global_load_lds_dwordx4 v225, s[42:43]\n\t" "s_add_u32 m0, %18, 0x1ed00\n\t" "s_nop 0\n\t" "global_load_lds_dwordx4 v226, s[42:43]\n\t" "s_add_u32 s42, s44, 0xd8000\n\t" "s_addc_u32 s43, s45, 0\n\t"
                     "s_add_u32 m0, %18, 0x1f120\n\t" "s_nop 0\n\t" "global_load_lds_dwordx4 v224, s[42:43]\n\t" "s_add_u32 m0, %18, 0x1f520\n\t" "s_nop 0\n\t" "global_load_lds_dwordx4 v225, s[42:43]\n\t"
                     "s_add_u32 m0, %18, 0x1f920\n\t" "s_nop 0\n\t" "global_load_lds_dwordx4 v226, s[42:43]\n\t" "v_add_u32 v232, 0x0, v229\n\t" "ds_read_b64_tr_b16 v[104:105], v232\n\t"
                     "ds_read_b64_tr_b16 v[106:107], v232 offset:768\n\t" "ds_read_b64_tr_b16 v[108:109], v232 offset:12416\n\t" "ds_read_b64_tr_b16 v[110:111], v232 offset:13184\n\t"
                     "ds_read_b64_tr_b16 a[144:145], v232 offset:128\n\t" "ds_read_b64_tr_b16 a[146:147], v232 offset:896\n\t" "ds_read_b64_tr_b16 a[148:149], v232 offset:12544\n\t"
                     "ds_read_b64_tr_b16 a[150:151], v232 offset:13312\n\t" "ds_read_b64_tr_b16 a[224:225], v232 offset:256\n\t" "ds_read_b64_tr_b16 a[226:227], v232 offset:1024\n\t"
                     "ds_read_b64_tr_b16 a[228:229], v232 offset:12672\n\t" "ds_read_b64_tr_b16 a[230:231], v232 offset:13440\n\t" "ds_read_b64_tr_b16 v[120:121], v232 offset:24832\n\t"
                     "ds_read_b64_tr_b16 v[122:123], v232 offset:25600\n\t" "ds_read_b64_tr_b16 v[124:125], v232 offset:37248\n\t" "ds_read_b64_tr_b16 v[126:127], v232 offset:38016\n\t"
                     "ds_read_b64_tr_b16 a[152:153], v232 offset:24960\n\t" "ds_read_b64_tr_b16 a[154:155], v232 offset:25728\n\t" "ds_read_b64_tr_b16 a[156:157], v232 offset:37376\n\t"
                     "ds_read_b64_tr_b16 a[158:159], v232 offset:38144\n\t" "ds_read_b64_tr_b16 a[232:233], v232 offset:25088\n\t" "ds_read_b64_tr_b16 a[234:235], v232 offset:25856\n\t"
                     "ds_read_b64_tr_b16 a[236:237], v232 offset:37504\n\t" "ds_read_b64_tr_b16 a[238:239], v232 offset:38272\n\t" "s_waitcnt vmcnt(12) lgkmcnt(0)\n\t" "s_barrier\n\t" "s_add_u32 s42, s44, 0x180000\n\t"
                     "s_addc_u32 s43, s45, 0\n\t" "s_add_u32 m0, %18, 0x0\n\t" "s_nop 0\n\t" "global_load_lds_dwordx4 v224, s[42:43]\n\t" "s_add_u32 m0, %18, 0x400\n\t" "s_nop 0\n\t"
                     "global_load_lds_dwordx4 v225, s[42:43]\n\t" "s_add_u32 m0, %18, 0x800\n\t" "s_nop 0\n\t" "global_load_lds_dwordx4 v226, s[42:43]\n\t" "s_add_u32 s42, s44, 0x198000\n\t" "s_addc_u32 s43, s45, 0\n\t"
                     "s_add_u32 m0, %18, 0xc20\n\t" "s_nop 0\n\t" "global_load_lds_dwordx4 v224, s[42:43]\n\t" "s_add_u32 m0, %18, 0x1020\n\t" "s_nop 0\n\t" "global_load_lds_dwordx4 v225, s[42:43]\n\t"
                     "s_add_u32 m0, %18, 0x1420\n\t" "s_nop 0\n\t" "global_load_lds_dwordx4 v226, s[42:43]\n\t" "s_add_u32 s42, s44, 0x240000\n\t" "s_addc_u32 s43, s45, 0\n\t" "s_add_u32 m0, %18, 0x6100\n\t" "s_nop 0\n\t"
                     "global_load_lds_dwordx4 v224, s[42:43]\n\t" "s_add_u32 m0, %18, 0x6500\n\t" "s_nop 0\n\t" "global_load_lds_dwordx4 v225, s[42:43]\n\t" "s_add_u32 m0, %18, 0x6900\n\t" "s_nop 0\n\t"
                     "global_load_lds_dwordx4 v226, s[42:43]\n\t" "s_add_u32 s42, s44, 0x258000\n\t" "s_addc_u32 s43, s45, 0\n\t" "s_add_u32 m0, %18, 0x6d20\n\t" "s_nop 0\n\t" "global_load_lds_dwordx4 v224, s[42:43]\n\t"
                     "s_add_u32 m0, %18, 0x7120\n\t" "s_nop 0\n\t" "global_load_lds_dwordx4 v225, s[42:43]\n\t" "s_add_u32 m0, %18, 0x7520\n\t" "s_nop 0\n\t" "global_load_lds_dwordx4 v226, s[42:43]\n\t"
                     "v_add_u32 v232, 0xc200, v229\n\t" "ds_read_b64_tr_b16 a[56:57], v232\n\t" "ds_read_b64_tr_b16 a[58:59], v232 offset:768\n\t" "ds_read_b64_tr_b16 a[60:61], v232 offset:12416\n\t"
                     "ds_read_b64_tr_b16 a[62:63], v232 offset:13184\n\t" "ds_read_b64_tr_b16 a[160:161], v232 offset:128\n\t" "ds_read_b64_tr_b16 a[162:163], v232 offset:896\n\t"
                     "ds_read_b64_tr_b16 a[164:165], v232 offset:12544\n\t" "ds_read_b64_tr_b16 a[166:167], v232 offset:13312\n\t" "ds_read_b64_tr_b16 a[240:241], v232 offset:256\n\t"
                     "ds_read_b64_tr_b16 a[242:243], v232 offset:1024\n\t" "ds_read_b64_tr_b16 a[244:245], v232 offset:12672\n\t" "ds_read_b64_tr_b16 a[246:247], v232 offset:13440\n\t"
                     "ds_read_b64_tr_b16 a[88:89], v232 offset:24832\n\t" "ds_read_b64_tr_b16 a[90:91], v232 offset:25600\n\t" "ds_read_b64_tr_b16 a[92:93], v232 offset:37248\n\t"
                     "ds_read_b64_tr_b16 a[94:95], v232 offset:38016\n\t" "ds_read_b64_tr_b16 a[168:169], v232 offset:24960\n\t" "ds_read_b64_tr_b16 a[170:171], v232 offset:25728\n\t"
                     "ds_read_b64_tr_b16 a[172:173], v232 offset:37376\n\t" "ds_read_b64_tr_b16 a[174:175], v232 offset:38144\n\t" "ds_read_b64_tr_b16 a[248:249], v232 offset:25088\n\t"
                     "ds_read_b64_tr_b16 a[250:251], v232 offset:25856\n\t" "ds_read_b64_tr_b16 a[252:253], v232 offset:37504\n\t" "ds_read_b64_tr_b16 a[254:255], v232 offset:38272\n\t" "s_waitcnt vmcnt(12) lgkmcnt(0)\n\t"
                     "s_barrier\n\t" "s_add_u32 s42, s44, 0x300000\n\t" "s_addc_u32 s43, s45, 0\n\t" "s_add_u32 m0, %18, 0xc200\n\t" "s_nop 0\n\t" "global_load_lds_dwordx4 v224, s[42:43]\n\t"
                     "s_add_u32 m0, %18, 0xc600\n\t" "s_nop 0\n\t" "global_load_lds_dwordx4 v225, s[42:43]\n\t" "s_add_u32 m0, %18, 0xca00\n\t" "s_nop 0\n\t" "global_load_lds_dwordx4 v226, s[42:43]\n\t"
                     "s_add_u32 s42, s44, 0x318000\n\t" "s_addc_u32 s43, s45, 0\n\t" "s_add_u32 m0, %18, 0xce20\n\t" "s_nop 0\n\t" "global_load_lds_dwordx4 v224, s[42:43]\n\t" "s_add_u32 m0, %18, 0xd220\n\t" "s_nop 0\n\t"
                     "global_load_lds_dwordx4 v225, s[42:43]\n\t" "s_add_u32 m0, %18, 0xd620\n\t" "s_nop 0\n\t" "global_load_lds_dwordx4 v226, s[42:43]\n\t" "s_add_u32 s42, s44, 0x3c0000\n\t" "s_addc_u32 s43, s45, 0\n\t"
                     "s_add_u32 m0, %18, 0x12300\n\t" "s_nop 0\n\t" "global_load_lds_dwordx4 v224, s[42:43]\n\t" "s_add_u32 m0, %18, 0x12700\n\t" "s_nop 0\n\t" "global_load_lds_dwordx4 v225, s[42:43]\n\t"
                     "s_add_u32 m0, %18, 0x12b00\n\t" "s_nop 0\n\t" "global_load_lds_dwordx4 v226, s[42:43]\n\t" "s_add_u32 s42, s44, 0x3d8000\n\t" "s_addc_u32 s43, s45, 0\n\t" "s_add_u32 m0, %18, 0x12f20\n\t"
                     "s_nop 0\n\t" "global_load_lds_dwordx4 v224, s[42:43]\n\t" "s_add_u32 m0, %18, 0x13320\n\t" "s_nop 0\n\t" "global_load_lds_dwordx4 v225, s[42:43]\n\t" "s_add_u32 m0, %18, 0x13720\n\t" "s_nop 0\n\t"
                     "global_load_lds_dwordx4 v226, s[42:43]\n\t" "v_add_u32 v232, 0x18400, v227\n\t" "v_add_u32 v233, 0x18400, v228\n\t" "ds_read_b64_tr_b16 v[128:129], v232\n\t"
                     "ds_read_b64_tr_b16 v[130:131], v232 offset:768\n\t" "ds_read_b64_tr_b16 v[132:133], v232 offset:12416\n\t" "ds_read_b64_tr_b16 v[134:135], v232 offset:13184\n\t"
                     "ds_read_b64_tr_b16 v[0:1], v232 offset:32\n\t" "ds_read_b64_tr_b16 v[2:3], v232 offset:800\n\t" "ds_read_b64_tr_b16 v[4:5], v232 offset:12448\n\t" "ds_read_b64_tr_b16 v[6:7], v232 offset:13216\n\t"
                     "ds_read_b64_tr_b16 v[136:137], v233 offset:64\n\t" "ds_read_b64_tr_b16 v[138:139], v233 offset:832\n\t" "ds_read_b64_tr_b16 v[140:141], v233 offset:12480\n\t"
                     "ds_read_b64_tr_b16 v[142:143], v233 offset:13248\n\t" "ds_read_b64_tr_b16 v[144:145], v232 offset:24832\n\t" "ds_read_b64_tr_b16 v[146:147], v232 offset:25600\n\t"
                     "ds_read_b64_tr_b16 v[148:149], v232 offset:37248\n\t" "ds_read_b64_tr_b16 v[150:151], v232 offset:38016\n\t" "ds_read_b64_tr_b16 v[16:17], v232 offset:24864\n\t"
                     "ds_read_b64_tr_b16 v[18:19], v232 offset:25632\n\t" "ds_read_b64_tr_b16 v[20:21], v232 offset:37280\n\t" "ds_read_b64_tr_b16 v[22:23], v232 offset:38048\n\t"
                     "ds_read_b64_tr_b16 v[152:153], v233 offset:24896\n\t" "ds_read_b64_tr_b16 v[154:155], v233 offset:25664\n\t" "ds_read_b64_tr_b16 v[156:157], v233 offset:37312\n\t"
                     "ds_read_b64_tr_b16 v[158:159], v233 offset:38080\n\t" "s_waitcnt vmcnt(12) lgkmcnt(0)\n\t" "s_barrier\n\t" "v_mov_b32 %14, 0\n\t" "global_load_dwordx2 %11, v231, %19 sc1\n\t"
                     "global_load_dwordx2 %12, %14, %19 offset:512 sc1\n\t" "global_load_dwordx2 %13, %14, %19 offset:1024 sc1\n\t" "s_add_u32 s42, s44, 0x480000\n\t" "s_addc_u32 s43, s45, 0\n\t"
                     "s_add_u32 m0, %18, 0x18400\n\t" "s_nop 0\n\t" "global_load_lds_dwordx4 v224, s[42:43]\n\t" "s_add_u32 m0, %18, 0x18800\n\t" "s_nop 0\n\t" "global_load_lds_dwordx4 v225, s[42:43]\n\t"
                     "s_add_u32 m0, %18, 0x18c00\n\t" "s_nop 0\n\t" "global_load_lds_dwordx4 v226, s[42:43]\n\t" "s_add_u32 s42, s44, 0x498000\n\t" "s_addc_u32 s43, s45, 0\n\t" "s_add_u32 m0, %18, 0x19020\n\t"
                     "s_nop 0\n\t" "global_load_lds_dwordx4 v224, s[42:43]\n\t" "s_add_u32 m0, %18, 0x19420\n\t" "s_nop 0\n\t" "global_load_lds_dwordx4 v225, s[42:43]\n\t" "s_add_u32 m0, %18, 0x19820\n\t" "s_nop 0\n\t"
                     "global_load_lds_dwordx4 v226, s[42:43]\n\t" "s_add_u32 s42, s44, 0x540000\n\t" "s_addc_u32 s43, s45, 0\n\t" "s_add_u32 m0, %18, 0x1e500\n\t" "s_nop 0\n\t" "global_load_lds_dwordx4 v224, s[42:43]\n\t"
                     "s_add_u32 m0, %18, 0x1e900\n\t" "s_nop 0\n\t" "global_load_lds_dwordx4 v225, s[42:43]\n\t" "s_add_u32 m0, %18, 0x1ed00\n\t" "s_nop 0\n\t" "global_load_lds_dwordx4 v226, s[42:43]\n\t"
                     "s_add_u32 s42, s44, 0x558000\n\t" "s_addc_u32 s43, s45, 0\n\t" "s_add_u32 m0, %18, 0x1f120\n\t" "s_nop 0\n\t" "global_load_lds_dwordx4 v224, s[42:43]\n\t" "s_add_u32 m0, %18, 0x1f520\n\t"
                     "s_nop 0\n\t" "global_load_lds_dwordx4 v225, s[42:43]\n\t" "s_add_u32 m0, %18, 0x1f920\n\t" "s_nop 0\n\t" "global_load_lds_dwordx4 v226, s[42:43]\n\t" "v_add_u32 v232, 0x0, v227\n\t"
                     "v_add_u32 v233, 0x0, v228\n\t" "ds_read_b64_tr_b16 v[160:161], v232\n\t" "ds_read_b64_tr_b16 v[162:163], v232 offset:768\n\t" "ds_read_b64_tr_b16 v[164:165], v232 offset:12416\n\t"
                     "ds_read_b64_tr_b16 v[166:167], v232 offset:13184\n\t" "ds_read_b64_tr_b16 v[32:33], v232 offset:32\n\t" "ds_read_b64_tr_b16 v[34:35], v232 offset:800\n\t"
                     "ds_read_b64_tr_b16 v[36:37], v232 offset:12448\n\t" "ds_read_b64_tr_b16 v[38:39], v232 offset:13216\n\t" "ds_read_b64_tr_b16 v[168:169], v233 offset:64\n\t"
                     "ds_read_b64_tr_b16 v[170:171], v233 offset:832\n\t" "ds_read_b64_tr_b16 v[172:173], v233 offset:12480\n\t" "ds_read_b64_tr_b16 v[174:175], v233 offset:13248\n\t"
                     "ds_read_b64_tr_b16 v[176:177], v232 offset:24832\n\t" "ds_read_b64_tr_b16 v[178:179], v232 offset:25600\n\t" "ds_read_b64_tr_b16 v[180:181], v232 offset:37248\n\t"
                     "ds_read_b64_tr_b16 v[182:183], v232 offset:38016\n\t" "ds_read_b64_tr_b16 v[48:49], v232 offset:24864\n\t" "ds_read_b64_tr_b16 v[50:51], v232 offset:25632\n\t"
                     "ds_read_b64_tr_b16 v[52:53], v232 offset:37280\n\t" "ds_read_b64_tr_b16 v[54:55], v232 offset:38048\n\t" "ds_read_b64_tr_b16 v[184:185], v233 offset:24896\n\t"
                     "ds_read_b64_tr_b16 v[186:187], v233 offset:25664\n\t" "ds_read_b64_tr_b16 v[188:189], v233 offset:37312\n\t" "ds_read_b64_tr_b16 v[190:191], v233 offset:38080\n\t" "s_waitcnt vmcnt(12) lgkmcnt(0)\n\t"
                     "s_barrier\n\t" "s_add_u32 s42, s44, 0x600000\n\t" "s_addc_u32 s43, s45, 0\n\t" "s_add_u32 m0, %18, 0x0\n\t" "s_nop 0\n\t" "global_load_lds_dwordx4 v224, s[42:43]\n\t" "s_add_u32 m0, %18, 0x400\n\t"
                     "s_nop 0\n\t" "global_load_lds_dwordx4 v225, s[42:43]\n\t" "s_add_u32 m0, %18, 0x800\n\t" "s_nop 0\n\t" "global_load_lds_dwordx4 v226, s[42:43]\n\t" "s_add_u32 s42, s44, 0x618000\n\t"
                     "s_addc_u32 s43, s45, 0\n\t" "s_add_u32 m0, %18, 0xc20\n\t" "s_nop 0\n\t" "global_load_lds_dwordx4 v224, s[42:43]\n\t" "s_add_u32 m0, %18, 0x1020\n\t" "s_nop 0\n\t"
                     "global_load_lds_dwordx4 v225, s[42:43]\n\t" "s_add_u32 m0, %18, 0x1420\n\t" "s_nop 0\n\t" "global_load_lds_dwordx4 v226, s[42:43]\n\t" "s_add_u32 s42, s44, 0x6c0000\n\t" "s_addc_u32 s43, s45, 0\n\t"
                     "s_add_u32 m0, %18, 0x6100\n\t" "s_nop 0\n\t" "global_load_lds_dwordx4 v224, s[42:43]\n\t" "s_add_u32 m0, %18, 0x6500\n\t" "s_nop 0\n\t" "global_load_lds_dwordx4 v225, s[42:43]\n\t"
                     "s_add_u32 m0, %18, 0x6900\n\t" "s_nop 0\n\t" "global_load_lds_dwordx4 v226, s[42:43]\n\t" "s_add_u32 s42, s44, 0x6d8000\n\t" "s_addc_u32 s43, s45, 0\n\t" "s_add_u32 m0, %18, 0x6d20\n\t" "s_nop 0\n\t"
                     "global_load_lds_dwordx4 v224, s[42:43]\n\t" "s_add_u32 m0, %18, 0x7120\n\t" "s_nop 0\n\t" "global_load_lds_dwordx4 v225, s[42:43]\n\t" "s_add_u32 m0, %18, 0x7520\n\t" "s_nop 0\n\t"
                     "global_load_lds_dwordx4 v226, s[42:43]\n\t" "global_load_dword %14, %14, %20 sc1\n\t" "v_add_u32 v232, 0xc200, v227\n\t" "v_add_u32 v233, 0xc200, v228\n\t" "ds_read_b64_tr_b16 v[192:193], v232\n\t"
                     "ds_read_b64_tr_b16 v[194:195], v232 offset:768\n\t" "ds_read_b64_tr_b16 v[196:197], v232 offset:12416\n\t" "ds_read_b64_tr_b16 v[198:199], v232 offset:13184\n\t"
                     "ds_read_b64_tr_b16 v[64:65], v232 offset:32\n\t" "ds_read_b64_tr_b16 v[66:67], v232 offset:800\n\t" "ds_read_b64_tr_b16 v[68:69], v232 offset:12448\n\t"
                     "ds_read_b64_tr_b16 v[70:71], v232 offset:13216\n\t" "ds_read_b64_tr_b16 v[200:201], v233 offset:64\n\t" "ds_read_b64_tr_b16 v[202:203], v233 offset:832\n\t"
                     "ds_read_b64_tr_b16 v[204:205], v233 offset:12480\n\t" "ds_read_b64_tr_b16 v[206:207], v233 offset:13248\n\t" "ds_read_b64_tr_b16 v[208:209], v232 offset:24832\n\t"
                     "ds_read_b64_tr_b16 v[210:211], v232 offset:25600\n\t" "ds_read_b64_tr_b16 v[212:213], v232 offset:37248\n\t" "ds_read_b64_tr_b16 v[214:215], v232 offset:38016\n\t"
                     "ds_read_b64_tr_b16 v[80:81], v232 offset:24864\n\t" "ds_read_b64_tr_b16 v[82:83], v232 offset:25632\n\t" "ds_read_b64_tr_b16 v[84:85], v232 offset:37280\n\t"
                     "ds_read_b64_tr_b16 v[86:87], v232 offset:38048\n\t" "ds_read_b64_tr_b16 v[216:217], v233 offset:24896\n\t" "ds_read_b64_tr_b16 v[218:219], v233 offset:25664\n\t"
                     "ds_read_b64_tr_b16 v[220:221], v233 offset:37312\n\t" "ds_read_b64_tr_b16 v[222:223], v233 offset:38080\n\t" "s_waitcnt vmcnt(13) lgkmcnt(0)\n\t" "s_barrier\n\t" "v_add_u32 v232, 0x18400, v227\n\t"
                     "v_add_u32 v233, 0x18400, v228\n\t" "ds_read_b64_tr_b16 a[0:1], v232\n\t" "ds_read_b64_tr_b16 a[2:3], v232 offset:768\n\t" "ds_read_b64_tr_b16 a[4:5], v232 offset:12416\n\t"
                     "ds_read_b64_tr_b16 a[6:7], v232 offset:13184\n\t" "ds_read_b64_tr_b16 v[96:97], v232 offset:32\n\t" "ds_read_b64_tr_b16 v[98:99], v232 offset:800\n\t"
                     "ds_read_b64_tr_b16 v[100:101], v232 offset:12448\n\t" "ds_read_b64_tr_b16 v[102:103], v232 offset:13216\n\t" "ds_read_b64_tr_b16 a[8:9], v233 offset:64\n\t"
                     "ds_read_b64_tr_b16 a[10:11], v233 offset:832\n\t" "ds_read_b64_tr_b16 a[12:13], v233 offset:12480\n\t" "ds_read_b64_tr_b16 a[14:15], v233 offset:13248\n\t"
                     "ds_read_b64_tr_b16 a[16:17], v232 offset:24832\n\t" "ds_read_b64_tr_b16 a[18:19], v232 offset:25600\n\t" "ds_read_b64_tr_b16 a[20:21], v232 offset:37248\n\t"
                     "ds_read_b64_tr_b16 a[22:23], v232 offset:38016\n\t" "ds_read_b64_tr_b16 v[112:113], v232 offset:24864\n\t" "ds_read_b64_tr_b16 v[114:115], v232 offset:25632\n\t"
                     "ds_read_b64_tr_b16 v[116:117], v232 offset:37280\n\t" "ds_read_b64_tr_b16 v[118:119], v232 offset:38048\n\t" "ds_read_b64_tr_b16 a[24:25], v233 offset:24896\n\t"
                     "ds_read_b64_tr_b16 a[26:27], v233 offset:25664\n\t" "ds_read_b64_tr_b16 a[28:29], v233 offset:37312\n\t" "ds_read_b64_tr_b16 a[30:31], v233 offset:38080\n\t" "s_waitcnt vmcnt(0) lgkmcnt(0)\n\t"
                     "s_barrier\n\t" "v_add_u32 v232, 0x0, v227\n\t" "v_add_u32 v233, 0x0, v228\n\t" "ds_read_b64_tr_b16 a[32:33], v232\n\t" "ds_read_b64_tr_b16 a[34:35], v232 offset:768\n\t"
                     "ds_read_b64_tr_b16 a[36:37], v232 offset:12416\n\t" "ds_read_b64_tr_b16 a[38:39], v232 offset:13184\n\t" "ds_read_b64_tr_b16 a[40:41], v232 offset:32\n\t"
                     "ds_read_b64_tr_b16 a[42:43], v232 offset:800\n\t" "ds_read_b64_tr_b16 a[44:45], v232 offset:12448\n\t" "ds_read_b64_tr_b16 a[46:47], v232 offset:13216\n\t"
                     "ds_read_b64_tr_b16 a[48:49], v233 offset:64\n\t" "ds_read_b64_tr_b16 a[50:51], v233 offset:832\n\t" "ds_read_b64_tr_b16 a[52:53], v233 offset:12480\n\t"
                     "ds_read_b64_tr_b16 a[54:55], v233 offset:13248\n\t" "ds_read_b64_tr_b16 a[64:65], v232 offset:24832\n\t" "ds_read_b64_tr_b16 a[66:67], v232 offset:25600\n\t"
                     "ds_read_b64_tr_b16 a[68:69], v232 offset:37248\n\t" "ds_read_b64_tr_b16 a[70:71], v232 offset:38016\n\t" "ds_read_b64_tr_b16 a[72:73], v232 offset:24864\n\t"
                     "ds_read_b64_tr_b16 a[74:75], v232 offset:25632\n\t" "ds_read_b64_tr_b16 a[76:77], v232 offset:37280\n\t" "ds_read_b64_tr_b16 a[78:79], v232 offset:38048\n\t"
                     "ds_read_b64_tr_b16 a[80:81], v233 offset:24896\n\t" "ds_read_b64_tr_b16 a[82:83], v233 offset:25664\n\t" "ds_read_b64_tr_b16 a[84:85], v233 offset:37312\n\t"
                     "ds_read_b64_tr_b16 a[86:87], v233 offset:38080\n\t" "s_waitcnt lgkmcnt(0)\n\t" "s_barrier\n\t" "ds_write_b128 v230, v[0:3]\n\t" "ds_write_b128 v230, v[4:7] offset:1024\n\t"
                     "ds_write_b128 v230, v[8:11] offset:2048\n\t" "ds_write_b128 v230, v[12:15] offset:3072\n\t" "ds_write_b128 v230, v[16:19] offset:4096\n\t" "ds_write_b128 v230, v[20:23] offset:5120\n\t"
                     "ds_write_b128 v230, v[24:27] offset:6144\n\t" "ds_write_b128 v230, v[28:31] offset:7168\n\t" "ds_write_b128 v230, v[32:35] offset:8192\n\t" "ds_write_b128 v230, v[36:39] offset:9216\n\t"
                     "ds_write_b128 v230, v[40:43] offset:10240\n\t" "ds_write_b128 v230, v[44:47] offset:11264\n\t" "ds_write_b128 v230, v[48:51] offset:12288\n\t" "ds_write_b128 v230, v[52:55] offset:13312\n\t"
                     "ds_write_b128 v230, v[56:59] offset:14336\n\t" "ds_write_b128 v230, v[60:63] offset:15360\n\t" "ds_write_b128 v230, v[64:67] offset:16384\n\t" "ds_write_b128 v230, v[68:71] offset:17408\n\t"
                     "ds_write_b128 v230, v[72:75] offset:18432\n\t" "ds_write_b128 v230, v[76:79] offset:19456\n\t" "ds_write_b128 v230, v[80:83] offset:20480\n\t" "ds_write_b128 v230, v[84:87] offset:21504\n\t"
                     "ds_write_b128 v230, v[88:91] offset:22528\n\t" "ds_write_b128 v230, v[92:95] offset:23552\n\t" "ds_write_b128 v230, v[96:99] offset:24576\n\t" "ds_write_b128 v230, v[100:103] offset:25600\n\t"
                     "ds_write_b128 v230, v[104:107] offset:26624\n\t" "ds_write_b128 v230, v[108:111] offset:27648\n\t" "ds_write_b128 v230, v[112:115] offset:28672\n\t" "ds_write_b128 v230, v[116:119] offset:29696\n\t"
                     "ds_write_b128 v230, v[120:123] offset:30720\n\t" "ds_write_b128 v230, v[124:127] offset:31744\n\t"
                     : "=&{a[0:31]}"(wa[0]), "=&{a[32:63]}"(wa[1]), "=&{a[64:95]}"(wa[2]), "=&{a[96:127]}"(wa[3]), "=&{a[128:159]}"(wa[4]), "=&{a[160:191]}"(wa[5]), "=&{a[192:223]}"(wa[6]), "=&{a[224:255]}"(wa[7]), "=&{v[128:159]}"(wv[0]), "=&{v[160:191]}"(wv[1]), "=&{v[192:223]}"(wv[2]), "=&v"(y.r), "=&v"(y.g), "=&v"(y.e), "=&v"(kfl)
                     : "{v[224:231]}"(pa), "{s[44:45]}"(p.wq + h * DH * 2 + (size_t)(16 * w) * (HD * 2)), "{s[46:47]}"(p.wk + h * DH * 2 + (size_t)(16 * w) * (HD * 2)),
                       "s"(lds + 2 * w * GS), "s"(p.ws + OFF_LAT), "s"(fl + (F_K + h) * 128)
                     : "v0", "v1", "v2", "v3", "v4", "v5", "v6", "v7", "v8", "v9", "v10", "v11", "v12", "v13", "v14", "v15", "v16", "v17", "v18", "v19", "v20", "v21", "v22", "v23", "v24", "v25", "v26", "v27", "v28", "v29", "v30", "v31", "v32", "v33", "v34", "v35", "v36", "v37", "v38", "v39", "v40", "v41", "v42", "v43", "v44", "v45", "v46", "v47", "v48", "v49", "v50", "v51", "v52", "v53", "v54", "v55", "v56", "v57", "v58", "v59", "v60", "v61", "v62", "v63", "v64", "v65", "v66", "v67", "v68", "v69", "v70", "v71", "v72", "v73", "v74", "v75", "v76", "v77", "v78", "v79", "v80", "v81", "v82", "v83", "v84", "v85", "v86", "v87", "v88", "v89", "v90", "v91", "v92", "v93", "v94", "v95", "v96", "v97", "v98", "v99", "v100", "v101", "v102", "v103", "v104", "v105", "v106", "v107", "v108", "v109", "v110", "v111", "v112", "v113", "v114", "v115", "v116", "v117", "v118", "v119", "v120", "v121", "v122", "v123", "v124", "v125", "v126", "v127", "v232", "v233", "s42", "s43", "scc", "memory");
        const u64 t = sync_tag(y.e);
        const bool rdy = __builtin_amdgcn_ballot_w64((y.r[0] & ~1u) != (u32)t || y.r[1] != (u32)(t >> 32) || y.g[0] != (u32)t) == 0 && uni((int)kfl) != -1;
        if (rdy) kxdma();
        sd = sync_end(y, p.ws, phi, w == 0);
        if (!rdy) { flag_wait(fl, F_K + h, 1); kxdma(); }
    }
    asm volatile("v_mov_b32 %0, 0" : "=v"(kfl));
    const u32 sidx = (lane & 2) ? 0xEEEEu : 0x4444u;
    const u32 moff = lane * 16;
    u32 kxa = kxl - 16 + (n >> 3) * 6160 + (16 * g + 4 * (n & 3) + ((n >> 1) & 2)) * 16;
    const u64 mb0 = 0xffff0000ffff0000ull;
    const int tp = (3 * w + (w & 1)) >> 1, ts = 1 + 3 * (w >> 1);
    u32 qw0 = lds + SL_IMG + tp * 512 + lane * 8, qw1 = lds + SL_IMG + ts * 512 + lane * 8 + 4 * (w & 1);
    const u32 kw0 = lds + SL_IMG + 3072 + w * 288 + n * 16 + 4 * g;
    const u32 qr0 = lds + SL_IMG + (g >> 1) * 512 + w * 128 + 64 * (g & 1) + 8 * qq;
    const u32 kr0 = lds + SL_IMG + 3072 + (qq >> 1) * 288 + g * 64 + (qq & 1) * 16 + 8 * (pp & 1);
    const u32 xso = 32768 + (h >> 4) * 16384 + (h & 15) * 512 + (w >> 1) * 256 + (n >> 3) * 64 + (n & 4) * 32 + (w & 1) * 32 + (g & 1) * 16 + (n & 3) * 4 + (g >> 1) * 8192;
    const u32 pso = (n < 8 ? 20480 + (h >> 1) * 256 + (n & 2) * 16 + (h & 1) * 8 + (n & 4) : 18432 + (h >> 2) * 256 + (h & 1) * 8 + (h & 2) * 2) + w * 64 + (g & 1) * 16 + (g >> 1) * 8192;
    const u64 psm = 0x01aa01aa01aa01aaull;
    const u32 kmo = (u32)(((n >> 3) * 4 + g) * 128 + (n & 7) * 16);
    u32x16 ad = u32x16{moff, sidx, lfa, kxa, qw0, qw1, kw0, kr0 + 1152 * w + (pp >> 1) * 576, qr0, kr0, xso, pso, 0x3dd53b94u, 0x3dd53b94u, kmo, kmo};
    asm volatile("" : "+{v[224:239]}"(ad));
    const char* memp = p.ws + mslot(sd, 0);
    char* kmp = p.ws + OFF_KM + h * 3072;
    const float kscale = 0.07216878364870322f;
    int budget = 30000;
    Clock clk; clk.init();
    for (int s = 0; s < NSEG; ++s) {
        int tries = 0;
        u64 bad, tp8, en = ~0ull;
        while (true) {
            const u32 tgt = clk.sched(phi, 0, cfg.G);
            asm volatile("ds_read_b128 v[64:67], v226\n\t" "ds_read_b128 v[68:71], v226 offset:1024\n\t" "ds_read_b128 v[72:75], v226 offset:2048\n\t" "ds_read_b128 v[76:79], v226 offset:3072\n\t"
                         "ds_read_b128 v[80:83], v226 offset:4096\n\t" "ds_read_b128 v[84:87], v226 offset:5120\n\t" "ds_read_b128 v[88:91], v226 offset:6144\n\t" "ds_read_b128 v[92:95], v226 offset:7168\n\t"
                         "ds_read_b128 v[96:99], v226 offset:8192\n\t" "ds_read_b128 v[100:103], v226 offset:9216\n\t" "ds_read_b128 v[104:107], v226 offset:10240\n\t" "ds_read_b128 v[108:111], v226 offset:11264\n\t"
                         "ds_read_b128 v[112:115], v226 offset:12288\n\t" "ds_read_b128 v[116:119], v226 offset:13312\n\t" "ds_read_b128 v[120:123], v226 offset:14336\n\t" "ds_read_b128 v[124:127], v226 offset:15360\n\t"
                         "v_mov_b64 v[40:41], 0\n\t" "v_mov_b64 v[42:43], 0\n\t" "v_mov_b64 v[44:45], 0\n\t" "v_mov_b64 v[46:47], 0\n\t" "v_mov_b64 v[48:49], 0\n\t" "v_mov_b64 v[50:51], 0\n\t" "v_mov_b64 v[52:53], 0\n\t"
                         "v_mov_b64 v[54:55], 0\n\t" "v_mov_b64 v[56:57], 0\n\t" "v_mov_b64 v[58:59], 0\n\t" "v_mov_b64 v[60:61], 0\n\t" "v_mov_b64 v[62:63], 0\n\t" "s_cmp_lg_u32 %10, 0\n\t" "s_cbranch_scc1 .Lsp_s\n\t"
                         "s_waitcnt vmcnt(0) lgkmcnt(0)\n\t" "s_barrier\n\t" ".Lsp_s:\n\t" "s_memtime vcc\n\t" "s_waitcnt lgkmcnt(0)\n\t" "s_sub_i32 s42, %2, vcc_lo\n\t" "s_cmp_gt_i32 s42, 80\n\t" "s_cbranch_scc1 .Lsp_s\n\t"
                         "s_max_i32 s42, s42, 0\n\t" "s_sub_i32 s42, 92, s42\n\t" "s_and_b32 s42, s42, -4\n\t" "s_getpc_b64 vcc\n\t" "s_add_u32 vcc_lo, vcc_lo, s42\n\t" "s_addc_u32 vcc_hi, vcc_hi, 0\n\t" "s_setpc_b64 vcc\n\t"
                         "s_nop 0\n\t" "s_nop 0\n\t" "s_nop 0\n\t" "s_nop 0\n\t" "s_nop 0\n\t" "s_nop 0\n\t" "s_nop 0\n\t" "s_nop 0\n\t" "s_nop 0\n\t" "s_nop 0\n\t" "s_nop 0\n\t" "s_nop 0\n\t" "s_nop 0\n\t" "s_nop 0\n\t"
                         "s_nop 0\n\t" "s_nop 0\n\t" "s_nop 0\n\t" "s_nop 0\n\t" "s_nop 0\n\t" "s_nop 0\n\t" "global_load_dwordx4 v[0:3], v224, %3 offset:-4096 sc1\n\t" "s_nop 10\n\t"
                         "global_load_dwordx4 v[4:7], v224, %3 offset:-3072 sc1\n\t" "s_nop 10\n\t" "global_load_dwordx4 v[8:11], v224, %3 offset:-2048 sc1\n\t" "s_nop 10\n\t"
                         "global_load_dwordx4 v[12:15], v224, %3 offset:-1024 sc1\n\t" "s_nop 10\n\t" "global_load_dwordx4 v[16:19], v224, %3 sc1\n\t" "s_nop 10\n\t" "global_load_dwordx4 v[20:23], v224, %3 offset:1024 sc1\n\t"
                         "s_nop 10\n\t" "global_load_dwordx4 v[24:27], v224, %3 offset:2048 sc1\n\t" "s_nop 10\n\t" "global_load_dwordx4 v[28:31], v224, %3 offset:3072 sc1\n\t" "s_nop 10\n\t"
                         "global_load_dwordx4 v[32:35], v224, %4 offset:-4096 sc1\n\t" "s_nop 10\n\t" "global_load_dwordx4 v[36:39], v224, %4 offset:-3072 sc1\n\t" "s_waitcnt vmcnt(9)\n\t"
                         "v_smfmac_f32_16x16x64_bf16 v[40:43], v[0:3], v[128:135], v225\n\t" "v_cmp_eq_u32 vcc, -1, v0\n\t" "v_smfmac_f32_16x16x64_bf16 v[44:47], v[0:3], v[64:71], v225\n\t" "s_and_b64 vcc, vcc, %9\n\t"
                         "s_cbranch_scc1 .Lsk_s\n\t" "ds_read_b128 v[64:67], v226 offset:16384\n\t" "v_smfmac_f32_16x16x64_bf16 v[48:51], v[0:3], v[136:143], v225\n\t" "ds_read_b128 v[68:71], v226 offset:17408\n\t"
                         "v_smfmac_f32_16x16x64_bf16 v[52:55], v[0:3], v[72:79], v225\n\t" "ds_read_b128 v[72:75], v226 offset:18432\n\t" "s_waitcnt vmcnt(8)\n\t"
                         "v_smfmac_f32_16x16x64_bf16 v[40:43], v[4:7], v[144:151], v225\n\t" "ds_read_b128 v[76:79], v226 offset:19456\n\t" "v_smfmac_f32_16x16x64_bf16 v[44:47], v[4:7], v[80:87], v225\n\t"
                         "ds_read_b128 v[80:83], v226 offset:20480\n\t" "v_smfmac_f32_16x16x64_bf16 v[48:51], v[4:7], v[152:159], v225\n\t" "ds_read_b128 v[84:87], v226 offset:21504\n\t"
                         "v_smfmac_f32_16x16x64_bf16 v[52:55], v[4:7], v[88:95], v225\n\t" "ds_read_b128 v[88:91], v226 offset:22528\n\t" "s_waitcnt vmcnt(7)\n\t"
                         "v_smfmac_f32_16x16x64_bf16 v[40:43], v[8:11], v[160:167], v225\n\t" "ds_read_b128 v[92:95], v226 offset:23552\n\t" "v_smfmac_f32_16x16x64_bf16 v[44:47], v[8:11], v[96:103], v225\n\t"
                         "ds_read_b128 v[96:99], v226 offset:24576\n\t" "v_smfmac_f32_16x16x64_bf16 v[48:51], v[8:11], v[168:175], v225\n\t" "ds_read_b128 v[100:103], v226 offset:25600\n\t"
                         "v_smfmac_f32_16x16x64_bf16 v[52:55], v[8:11], v[104:111], v225\n\t" "ds_read_b128 v[104:107], v226 offset:26624\n\t" "s_waitcnt vmcnt(6)\n\t"
                         "v_smfmac_f32_16x16x64_bf16 v[40:43], v[12:15], v[176:183], v225\n\t" "ds_read_b128 v[108:111], v226 offset:27648\n\t" "v_smfmac_f32_16x16x64_bf16 v[44:47], v[12:15], v[112:119], v225\n\t"
                         "ds_read_b128 v[112:115], v226 offset:28672\n\t" "v_smfmac_f32_16x16x64_bf16 v[48:51], v[12:15], v[184:191], v225\n\t" "ds_read_b128 v[116:119], v226 offset:29696\n\t"
                         "v_smfmac_f32_16x16x64_bf16 v[52:55], v[12:15], v[120:127], v225\n\t" "ds_read_b128 v[120:123], v226 offset:30720\n\t" "s_waitcnt vmcnt(5)\n\t"
                         "v_smfmac_f32_16x16x64_bf16 v[40:43], v[16:19], v[192:199], v225\n\t" "ds_read_b128 v[124:127], v226 offset:31744\n\t" "s_waitcnt lgkmcnt(14)\n\t"
                         "v_smfmac_f32_16x16x64_bf16 v[44:47], v[16:19], v[64:71], v225\n\t" "ds_read_b128 v[64:67], v227\n\t" "v_smfmac_f32_16x16x64_bf16 v[48:51], v[16:19], v[200:207], v225\n\t"
                         "ds_read_b128 v[68:71], v227 offset:1024\n\t" "s_waitcnt lgkmcnt(14)\n\t" "v_smfmac_f32_16x16x64_bf16 v[52:55], v[16:19], v[72:79], v225\n\t" "ds_read_b128 v[72:75], v227 offset:16\n\t"
                         "s_waitcnt vmcnt(4)\n\t" "v_smfmac_f32_16x16x64_bf16 v[40:43], v[20:23], v[208:215], v225\n\t" "ds_read_b128 v[76:79], v227 offset:1040\n\t" "s_waitcnt lgkmcnt(14)\n\t"
                         "v_smfmac_f32_16x16x64_bf16 v[44:47], v[20:23], v[80:87], v225\n\t" "ds_read_b128 v[80:83], v227 offset:2048\n\t" "v_smfmac_f32_16x16x64_bf16 v[48:51], v[20:23], v[216:223], v225\n\t"
                         "ds_read_b128 v[84:87], v227 offset:3072\n\t" "s_waitcnt lgkmcnt(14)\n\t" "v_smfmac_f32_16x16x64_bf16 v[52:55], v[20:23], v[88:95], v225\n\t" "ds_read_b128 v[88:91], v227 offset:2064\n\t"
                         "s_waitcnt vmcnt(3)\n\t" "v_smfmac_f32_16x16x64_bf16 v[40:43], v[24:27], a[0:7], v225\n\t" "ds_read_b128 v[92:95], v227 offset:3088\n\t" "s_waitcnt lgkmcnt(14)\n\t"
                         "v_smfmac_f32_16x16x64_bf16 v[44:47], v[24:27], v[96:103], v225\n\t" "ds_read_b128 v[96:99], v227 offset:4096\n\t" "v_smfmac_f32_16x16x64_bf16 v[48:51], v[24:27], a[8:15], v225\n\t"
                         "ds_read_b128 v[100:103], v227 offset:5120\n\t" "s_waitcnt lgkmcnt(14)\n\t" "v_smfmac_f32_16x16x64_bf16 v[52:55], v[24:27], v[104:111], v225\n\t" "ds_read_b128 v[104:107], v227 offset:4112\n\t"
                         "s_waitcnt vmcnt(2)\n\t" "v_smfmac_f32_16x16x64_bf16 v[40:43], v[28:31], a[16:23], v225\n\t" "ds_read_b128 v[108:111], v227 offset:5136\n\t" "s_waitcnt lgkmcnt(14)\n\t"
                         "v_smfmac_f32_16x16x64_bf16 v[44:47], v[28:31], v[112:119], v225\n\t" "v_smfmac_f32_16x16x64_bf16 v[48:51], v[28:31], a[24:31], v225\n\t" "s_waitcnt lgkmcnt(12)\n\t"
                         "v_smfmac_f32_16x16x64_bf16 v[52:55], v[28:31], v[120:127], v225\n\t" "s_waitcnt vmcnt(1)\n\t" "v_smfmac_f32_16x16x64_bf16 v[40:43], v[32:35], a[32:39], v225\n\t" "v_mov_b64 v[112:113], 0\n\t"
                         "v_smfmac_f32_16x16x64_bf16 v[44:47], v[32:35], a[40:47], v225\n\t" "v_mov_b64 v[114:115], 0\n\t" "v_smfmac_f32_16x16x64_bf16 v[48:51], v[32:35], a[48:55], v225\n\t" "v_mov_b64 v[116:117], 0\n\t"
                         "v_smfmac_f32_16x16x64_bf16 v[52:55], v[32:35], a[56:63], v225\n\t" "v_mov_b64 v[118:119], 0\n\t" "s_waitcnt vmcnt(0)\n\t" "v_smfmac_f32_16x16x64_bf16 v[40:43], v[36:39], a[64:71], v225\n\t"
                         "v_mov_b64 v[120:121], 0\n\t" "v_smfmac_f32_16x16x64_bf16 v[44:47], v[36:39], a[72:79], v225\n\t" "v_mov_b64 v[122:123], 0\n\t" "v_smfmac_f32_16x16x64_bf16 v[48:51], v[36:39], a[80:87], v225\n\t"
                         "v_mov_b64 v[124:125], 0\n\t" "v_smfmac_f32_16x16x64_bf16 v[52:55], v[36:39], a[88:95], v225\n\t" "v_add_f32 v40, v40, v42\n\t" "v_add_f32 v41, v41, v43\n\t"
                         "v_smfmac_f32_16x16x64_bf16 v[56:59], v[0:3], a[96:103], v225\n\t" "v_cvt_pk_bf16_f32 v42, v40, v41\n\t" "v_smfmac_f32_16x16x64_bf16 v[56:59], v[4:7], a[104:111], v225\n\t"
                         "v_add_f32 v44, v44, v46\n\t" "v_smfmac_f32_16x16x64_bf16 v[56:59], v[8:11], a[112:119], v225\n\t" "v_add_f32 v45, v45, v47\n\t" "v_smfmac_f32_16x16x64_bf16 v[56:59], v[12:15], a[120:127], v225\n\t"
                         "v_cvt_pk_bf16_f32 v43, v44, v45\n\t" "v_smfmac_f32_16x16x64_bf16 v[56:59], v[16:19], a[128:135], v225\n\t" "ds_write_b64 v228, v[42:43]\n\t" "v_add_f32 v48, v48, v50\n\t"
                         "v_smfmac_f32_16x16x64_bf16 v[56:59], v[20:23], a[136:143], v225\n\t" "v_add_f32 v49, v49, v51\n\t" "v_cvt_pk_bf16_f32 v46, v48, v49\n\t"
                         "v_smfmac_f32_16x16x64_bf16 v[56:59], v[24:27], a[144:151], v225\n\t" "ds_write_b32 v229, v46\n\t" "v_add_f32 v52, v52, v54\n\t" "v_smfmac_f32_16x16x64_bf16 v[56:59], v[28:31], a[152:159], v225\n\t"
                         "v_add_f32 v53, v53, v55\n\t" "v_cvt_pk_bf16_f32 v54, v52, v53\n\t" "v_smfmac_f32_16x16x64_bf16 v[56:59], v[32:35], a[160:167], v225\n\t" "ds_write_b32 v230, v54\n\t"
                         "v_smfmac_f32_16x16x64_bf16 v[56:59], v[36:39], a[168:175], v225\n\t" "v_cmp_u_f32 vcc, v40, v41\n\t" "v_smfmac_f32_16x16x64_bf16 v[60:63], v[0:3], a[176:183], v225\n\t"
                         "v_smfmac_f32_16x16x64_bf16 v[60:63], v[4:7], a[184:191], v225\n\t" "s_and_b64 vcc, vcc, %9\n\t" "s_cbranch_scc1 .Lsk_s\n\t" "v_add_f32 v56, v56, v58\n\t"
                         "v_smfmac_f32_16x16x64_bf16 v[60:63], v[8:11], a[192:199], v225\n\t" "s_waitcnt lgkmcnt(0)\n\t" "s_barrier\n\t" "v_smfmac_f32_16x16x64_bf16 v[60:63], v[12:15], a[200:207], v225\n\t"
                         "ds_read_b64_tr_b16 v[40:41], v232\n\t" "v_add_f32 v57, v57, v59\n\t" "v_smfmac_f32_16x16x64_bf16 v[60:63], v[16:19], a[208:215], v225\n\t" "ds_read_b64_tr_b16 v[42:43], v232 offset:32\n\t"
                         "v_cvt_pk_bf16_f32 v58, v56, v57\n\t" "v_smfmac_f32_16x16x64_bf16 v[60:63], v[20:23], a[216:223], v225\n\t" "ds_write_b32 v230, v58 offset:1152\n\t"
                         "v_smfmac_f32_16x16x64_bf16 v[60:63], v[24:27], a[224:231], v225\n\t" "ds_read_b64_tr_b16 v[44:45], v232 offset:1024\n\t" "ds_read_b64_tr_b16 v[46:47], v232 offset:1056\n\t"
                         "v_smfmac_f32_16x16x64_bf16 v[60:63], v[28:31], a[232:239], v225\n\t" "ds_read_b64_tr_b16 v[48:49], v232 offset:2048\n\t" "ds_read_b64_tr_b16 v[50:51], v232 offset:2080\n\t"
                         "v_smfmac_f32_16x16x64_bf16 v[60:63], v[32:35], a[240:247], v225\n\t" "ds_read_b64_tr_b16 v[0:1], v233\n\t" "ds_read_b64_tr_b16 v[2:3], v233 offset:32\n\t"
                         "v_smfmac_f32_16x16x64_bf16 v[60:63], v[36:39], a[248:255], v225\n\t" "ds_read_b64_tr_b16 v[4:5], v233 offset:576\n\t" "ds_read_b64_tr_b16 v[6:7], v233 offset:608\n\t" "s_waitcnt lgkmcnt(9)\n\t"
                         "v_smfmac_f32_16x16x64_bf16 v[112:115], v[40:43], v[64:71], v225\n\t" "s_waitcnt lgkmcnt(8)\n\t" "s_barrier\n\t" "v_smfmac_f32_16x16x64_bf16 v[116:119], v[40:43], v[72:79], v225\n\t"
                         "ds_read_b64_tr_b16 v[8:9], v233 offset:1152\n\t" "s_waitcnt lgkmcnt(7)\n\t" "v_add_f32 v60, v60, v62\n\t" "v_add_f32 v61, v61, v63\n\t"
                         "v_smfmac_f32_16x16x64_bf16 v[112:115], v[44:47], v[80:87], v225\n\t" "ds_read_b64_tr_b16 v[10:11], v233 offset:1184\n\t" "v_cvt_pk_bf16_f32 v62, v60, v61\n\t"
                         "v_smfmac_f32_16x16x64_bf16 v[116:119], v[44:47], v[88:95], v225\n\t" "ds_write_b32 v230, v62 offset:2304\n\t" "s_waitcnt lgkmcnt(7)\n\t"
                         "v_smfmac_f32_16x16x64_bf16 v[112:115], v[48:51], v[96:103], v225\n\t" "ds_read_b64_tr_b16 v[12:13], v233 offset:1728\n\t" "v_smfmac_f32_16x16x64_bf16 v[116:119], v[48:51], v[104:111], v225\n\t"
                         "ds_read_b64_tr_b16 v[14:15], v233 offset:1760\n\t" "s_waitcnt lgkmcnt(5)\n\t" "v_smfmac_f32_16x16x64_bf16 v[120:123], v[40:43], v[0:7], v225\n\t" "v_cndmask_b32 v24, v112, v113, %6\n\t"
                         "v_cndmask_b32 v25, v114, v115, %6\n\t" "v_add_f32 v24, v24, v25\n\t" "v_pk_add_f32 v[26:27], v[116:117], v[118:119]\n\t" "v_cndmask_b32 v25, v26, v27, %6\n\t" "v_max_f32 v26, v24, v25\n\t"
                         "s_waitcnt lgkmcnt(2)\n\t" "s_barrier\n\t" "ds_read_b64_tr_b16 v[16:17], v233 offset:2304\n\t" "v_max_f32_dpp v27, v26, v26 row_ror:4 row_mask:0xf bank_mask:0xf\n\t"
                         "ds_read_b64_tr_b16 v[18:19], v233 offset:2336\n\t" "v_max_f32_dpp v27, v26, v27 row_ror:8 row_mask:0xf bank_mask:0xf\n\t" "v_max_f32_dpp v27, v26, v27 row_ror:12 row_mask:0xf bank_mask:0xf\n\t"
                         "ds_read_b64_tr_b16 v[20:21], v233 offset:2880\n\t" "ds_read_b64_tr_b16 v[22:23], v233 offset:2912\n\t" "v_max_f32_dpp v30, v27, v27 row_ror:1 row_mask:0xf bank_mask:0xf\n\t"
                         "v_max_f32_dpp v30, v27, v30 row_ror:2 row_mask:0xf bank_mask:0xf\n\t" "v_max_f32_dpp v30, v27, v30 row_ror:3 row_mask:0xf bank_mask:0xf\n\t" "s_waitcnt lgkmcnt(4)\n\t"
                         "v_pk_add_f32 v[24:25], v[24:25], v[30:31] op_sel_hi:[1,0] neg_lo:[0,1] neg_hi:[0,1]\n\t" "v_pk_mul_f32 v[24:25], v[24:25], v[236:237]\n\t" "v_exp_f32 v24, v24\n\t" "v_exp_f32 v25, v25\n\t"
                         "v_mul_f32 v31, %8, v30\n\t" "v_cvt_pk_bf16_f32 v28, |v24|, |v25|\n\t" "v_smfmac_f32_16x16x64_bf16 v[120:123], v[44:47], v[8:15], v225\n\t" "global_store_dword v234, v28, %5 offset:-4096 sc1\n\t"
                         "s_waitcnt lgkmcnt(0)\n\t" "v_add_f32 v26, v24, v25\n\t" "v_smfmac_f32_16x16x64_bf16 v[120:123], v[48:51], v[16:23], v225\n\t" "s_nop 0\n\t"
                         "v_add_f32_dpp v27, v26, v26 row_ror:4 row_mask:0xf bank_mask:0xf\n\t" "v_add_f32_dpp v27, v26, v27 row_ror:8 row_mask:0xf bank_mask:0xf\n\t"
                         "v_add_f32_dpp v27, v26, v27 row_ror:12 row_mask:0xf bank_mask:0xf\n\t" "s_nop 1\n\t" "v_add_f32_dpp v29, v27, v27 row_ror:1 row_mask:0xf bank_mask:0xf\n\t"
                         "v_pk_add_f32 v[24:25], v[120:121], v[122:123]\n\t" "v_cndmask_b32 v24, v24, v25, %6\n\t" "v_max_f32 v26, v24, v30\n\t" "v_add_f32_dpp v29, v27, v29 row_ror:2 row_mask:0xf bank_mask:0xf\n\t"
                         "v_add_f32_dpp v29, v27, v29 row_ror:3 row_mask:0xf bank_mask:0xf\n\t" "v_max_f32_dpp v27, v26, v26 row_ror:1 row_mask:0xf bank_mask:0xf\n\t"
                         "v_max_f32_dpp v27, v26, v27 row_ror:2 row_mask:0xf bank_mask:0xf\n\t" "v_max_f32_dpp v27, v26, v27 row_ror:3 row_mask:0xf bank_mask:0xf\n\t" "v_mul_f32 v24, %8, v24\n\t"
                         "v_mul_f32 v29, 0x3e000000, v29\n\t" "v_max_f32_dpp v26, v27, v27 row_ror:4 row_mask:0xf bank_mask:0xf\n\t" "v_fma_f32 v24, -v26, %8, v24\n\t" "v_fma_f32 v25, -v26, %8, v31\n\t"
                         "v_exp_f32 v24, v24\n\t" "v_exp_f32 v25, v25\n\t" "s_nop 0\n\t" "v_fma_f32 v26, v25, v29, v24\n\t" "v_mov_b32_dpp v124, v24 quad_perm:[0,1,2,3] row_mask:0xf bank_mask:0x3\n\t"
                         "v_mov_b32_dpp v125, v24 quad_perm:[1,0,3,2] row_mask:0xf bank_mask:0x3\n\t" "v_add_f32_dpp v27, v26, v26 row_ror:1 row_mask:0xf bank_mask:0xf\n\t"
                         "v_add_f32_dpp v27, v26, v27 row_ror:2 row_mask:0xf bank_mask:0xf\n\t" "v_add_f32_dpp v27, v26, v27 row_ror:3 row_mask:0xf bank_mask:0xf\n\t"
                         "v_mov_b32_dpp v124, v25 quad_perm:[0,1,2,3] row_mask:0x5 bank_mask:0x4\n\t" "v_mov_b32_dpp v125, v25 quad_perm:[0,1,2,3] row_mask:0xa bank_mask:0x4\n\t"
                         "v_add_f32_dpp v26, v27, v27 row_ror:4 row_mask:0xf bank_mask:0xf\n\t" "v_rcp_f32 v26, v26\n\t" "s_mov_b64 exec, %7\n\t" "v_pk_mul_f32 v[124:125], v[124:125], v[26:27] op_sel_hi:[1,0]\n\t"
                         "v_cvt_pk_bf16_f32 v24, |v124|, |v125|\n\t" "global_store_dword v235, v24, %5 offset:-4096 sc1\n\t" "s_mov_b64 exec, -1\n\t" ".Lsk_s:\n\t" "s_memrealtime %1\n\t" "s_mov_b64 %0, vcc\n\t"
                         "s_waitcnt lgkmcnt(0)\n\t"
                         : "=&s"(bad), "=&s"(tp8)
                         : "s"(uni((int)tgt)), "s"(memp), "s"(memp + 16384), "s"((u64)memp & ~8192ull), "s"(mb0), "s"(psm), "s"(kscale * 1.4426950408889634f), "s"(en), "s"(tries), "{v[224:239]}"(ad),
                           "{a[0:31]}"(wa[0]), "{a[32:63]}"(wa[1]), "{a[64:95]}"(wa[2]), "{a[96:127]}"(wa[3]), "{a[128:159]}"(wa[4]), "{a[160:191]}"(wa[5]), "{a[192:223]}"(wa[6]), "{a[224:255]}"(wa[7]), "{v[128:159]}"(wv[0]), "{v[160:191]}"(wv[1]), "{v[192:223]}"(wv[2])
                         : "v0", "v1", "v2", "v3", "v4", "v5", "v6", "v7", "v8", "v9", "v10", "v11", "v12", "v13", "v14", "v15", "v16", "v17", "v18", "v19", "v20", "v21", "v22", "v23", "v24", "v25", "v26", "v27", "v28", "v29", "v30", "v31", "v32", "v33", "v34", "v35", "v36", "v37", "v38", "v39", "v40", "v41", "v42", "v43", "v44", "v45", "v46", "v47", "v48", "v49", "v50", "v51", "v52", "v53", "v54", "v55", "v56", "v57", "v58", "v59", "v60", "v61", "v62", "v63", "v64", "v65", "v66", "v67", "v68", "v69", "v70", "v71", "v72", "v73", "v74", "v75", "v76", "v77", "v78", "v79", "v80", "v81", "v82", "v83", "v84", "v85", "v86", "v87", "v88", "v89", "v90", "v91", "v92", "v93", "v94", "v95", "v96", "v97", "v98", "v99", "v100", "v101", "v102", "v103", "v104", "v105", "v106", "v107", "v108", "v109", "v110", "v111", "v112", "v113", "v114", "v115", "v116", "v117", "v118", "v119", "v120", "v121", "v122", "v123", "v124", "v125", "v126", "v127", "vcc", "scc", "s42", "memory");
            if (__builtin_expect(bad == 0, 1)) break;
            waitvm<0>();
            if (((++tries & 15) == 0 && !unwritten(memp, ad[0])) || --budget < 0) en = 0;
            phi += (u32)cfg.G;
        }
        const u32 tp_ = (u32)tp8;
        asm volatile("" : "+v"(kfl) : : "memory");
        if (uni((int)kfl) == -1) flag_wait(fl, F_K + pblk(s + 1) * 32 + h, 1);
        kxdma();
        flag_ldm(kfl, fl, F_K + pblk(s + 2) * 32 + h, pblk(s + 2) != pblk(s + 1));
        if (w < 3) {
            const u32x2 k0 = trread(ad[7]), k1 = trread(ad[7] + 32);
            gst16(kmp + w * 1024, ad[15], u32x4{k0[0], k0[1], k1[0], k1[1]});
        }
        clk.calib();
        const u32 tt = sched_ld0(s);
        if (s >= NSEG - 2) {
            waitvm<0>();
            barrier();
            if (w == 3) flag_add(fl, F_PROG + 8 * s + (h & 7), 1);
        }
        follow(phi, (int)(tt >> 16) - VISS, tp_, cfg.G);
        phi += tt & 0xffffu;
        memp = p.ws + mslot(sd, s + 1); kmp += NH * 3072;
    }
    if (h == 0 && w == 0) {
        flag_wait(fl, F_ACK, 256);
        Sync y;
        sync_ld(y, p.ws);
        asm volatile("s_waitcnt vmcnt(0)" : "+v"(y.r), "+v"(y.g), "+v"(y.e) : : "memory");
        const u64 t = ((u64)y.e[1] << 32 | y.e[0]) + 1;
        if (lanex() == 0) gst8(p.ws + OFF_LAT, 1024, u32x2{(u32)t, (u32)(t >> 32)});
    }
    asm volatile(".Lkm_last:\n\ts_waitcnt vmcnt(0)" : : : "memory");
}

DI void proj_role(const Ptrs& p, const ChainCfg cfg, int h, u32 lds, int w) {
    u32x32 wa[8], wv[4];
    u32 phi;
    int sd;
    {
        const int lane = lanex(), tid = 64 * w + lane;
        const int g = lane >> 4, qq = (lane >> 2) & 3, pp = lane & 3;
        constexpr int GS = 3104;
        auto dvo = [&](int i) { const int ix = i * 64 + lane; return (u32)((ix / 24) * (HD * 2) + (ix % 24) * 16); };
        auto ovo = [&](int i) { const int u = i * 64 + lane, r = u / 80; return (u32)(r * 1280 + (((u % 80) ^ (2 * (r ^ (w & 2)))) << 4)); };
        const u32 tra = lds + (2 * (g >> 1) + (qq >> 1)) * GS + (4 * (g & 1) + (qq & 1)) * 384 + 96 * w + 32 * (w & 1) + 8 * pp;
        const u32 tro = (4 * g + (qq & 1) + 16 * (qq >> 1)) * 1280 + 8 * pp + ((u32)(2 * w ^ 2 * ((4 * g + qq) & 7)) << 4);
        u32x16 pa = u32x16{dvo(0), dvo(1), dvo(2), tra, tra - 96 * (w & 1), tro, lds + (u32)((w & 1) * 24576 + (w >> 1) * 99328 + lane * 16), (u32)(lane & 31) * 8,
                           ovo(0), ovo(1), ovo(2), ovo(3), ovo(4), tro, tro, tro};
        Sync y;
        asm volatile("s_nop 4\n\t" "s_add_u32 s42, s44, 0x0\n\t" "s_addc_u32 s43, s45, 0\n\t" "s_add_u32 m0, %18, 0x0\n\t" "s_nop 0\n\t" "global_load_lds_dwordx4 v224, s[42:43]\n\t" "s_add_u32 m0, %18, 0x400\n\t"
                     "s_nop 0\n\t" "global_load_lds_dwordx4 v225, s[42:43]\n\t" "s_add_u32 m0, %18, 0x800\n\t" "s_nop 0\n\t" "global_load_lds_dwordx4 v226, s[42:43]\n\t" "s_add_u32 s42, s44, 0x18000\n\t"
                     "s_addc_u32 s43, s45, 0\n\t" "s_add_u32 m0, %18, 0xc20\n\t" "s_nop 0\n\t" "global_load_lds_dwordx4 v224, s[42:43]\n\t" "s_add_u32 m0, %18, 0x1020\n\t" "s_nop 0\n\t"
                     "global_load_lds_dwordx4 v225, s[42:43]\n\t" "s_add_u32 m0, %18, 0x1420\n\t" "s_nop 0\n\t" "global_load_lds_dwordx4 v226, s[42:43]\n\t" "s_add_u32 s42, s44, 0xc0000\n\t" "s_addc_u32 s43, s45, 0\n\t"
                     "s_add_u32 m0, %18, 0x6100\n\t" "s_nop 0\n\t" "global_load_lds_dwordx4 v224, s[42:43]\n\t" "s_add_u32 m0, %18, 0x6500\n\t" "s_nop 0\n\t" "global_load_lds_dwordx4 v225, s[42:43]\n\t"
                     "s_add_u32 m0, %18, 0x6900\n\t" "s_nop 0\n\t" "global_load_lds_dwordx4 v226, s[42:43]\n\t" "s_add_u32 s42, s44, 0xd8000\n\t" "s_addc_u32 s43, s45, 0\n\t" "s_add_u32 m0, %18, 0x6d20\n\t" "s_nop 0\n\t"
                     "global_load_lds_dwordx4 v224, s[42:43]\n\t" "s_add_u32 m0, %18, 0x7120\n\t" "s_nop 0\n\t" "global_load_lds_dwordx4 v225, s[42:43]\n\t" "s_add_u32 m0, %18, 0x7520\n\t" "s_nop 0\n\t"
                     "global_load_lds_dwordx4 v226, s[42:43]\n\t" "s_add_u32 s42, s44, 0x180000\n\t" "s_addc_u32 s43, s45, 0\n\t" "s_add_u32 m0, %18, 0xc200\n\t" "s_nop 0\n\t" "global_load_lds_dwordx4 v224, s[42:43]\n\t"
                     "s_add_u32 m0, %18, 0xc600\n\t" "s_nop 0\n\t" "global_load_lds_dwordx4 v225, s[42:43]\n\t" "s_add_u32 m0, %18, 0xca00\n\t" "s_nop 0\n\t" "global_load_lds_dwordx4 v226, s[42:43]\n\t"
                     "s_add_u32 s42, s44, 0x198000\n\t" "s_addc_u32 s43, s45, 0\n\t" "s_add_u32 m0, %18, 0xce20\n\t" "s_nop 0\n\t" "global_load_lds_dwordx4 v224, s[42:43]\n\t" "s_add_u32 m0, %18, 0xd220\n\t" "s_nop 0\n\t"
                     "global_load_lds_dwordx4 v225, s[42:43]\n\t" "s_add_u32 m0, %18, 0xd620\n\t" "s_nop 0\n\t" "global_load_lds_dwordx4 v226, s[42:43]\n\t" "s_add_u32 s42, s44, 0x240000\n\t" "s_addc_u32 s43, s45, 0\n\t"
                     "s_add_u32 m0, %18, 0x12300\n\t" "s_nop 0\n\t" "global_load_lds_dwordx4 v224, s[42:43]\n\t" "s_add_u32 m0, %18, 0x12700\n\t" "s_nop 0\n\t" "global_load_lds_dwordx4 v225, s[42:43]\n\t"
                     "s_add_u32 m0, %18, 0x12b00\n\t" "s_nop 0\n\t" "global_load_lds_dwordx4 v226, s[42:43]\n\t" "s_add_u32 s42, s44, 0x258000\n\t" "s_addc_u32 s43, s45, 0\n\t" "s_add_u32 m0, %18, 0x12f20\n\t"
                     "s_nop 0\n\t" "global_load_lds_dwordx4 v224, s[42:43]\n\t" "s_add_u32 m0, %18, 0x13320\n\t" "s_nop 0\n\t" "global_load_lds_dwordx4 v225, s[42:43]\n\t" "s_add_u32 m0, %18, 0x13720\n\t" "s_nop 0\n\t"
                     "global_load_lds_dwordx4 v226, s[42:43]\n\t" "s_waitcnt vmcnt(12) lgkmcnt(0)\n\t" "s_barrier\n\t" "s_add_u32 s42, s44, 0x300000\n\t" "s_addc_u32 s43, s45, 0\n\t" "s_add_u32 m0, %18, 0x18400\n\t"
                     "s_nop 0\n\t" "global_load_lds_dwordx4 v224, s[42:43]\n\t" "s_add_u32 m0, %18, 0x18800\n\t" "s_nop 0\n\t" "global_load_lds_dwordx4 v225, s[42:43]\n\t" "s_add_u32 m0, %18, 0x18c00\n\t" "s_nop 0\n\t"
                     "global_load_lds_dwordx4 v226, s[42:43]\n\t" "s_add_u32 s42, s44, 0x318000\n\t" "s_addc_u32 s43, s45, 0\n\t" "s_add_u32 m0, %18, 0x19020\n\t" "s_nop 0\n\t" "global_load_lds_dwordx4 v224, s[42:43]\n\t"
                     "s_add_u32 m0, %18, 0x19420\n\t" "s_nop 0\n\t" "global_load_lds_dwordx4 v225, s[42:43]\n\t" "s_add_u32 m0, %18, 0x19820\n\t" "s_nop 0\n\t" "global_load_lds_dwordx4 v226, s[42:43]\n\t"
                     "s_add_u32 s42, s44, 0x3c0000\n\t" "s_addc_u32 s43, s45, 0\n\t" "s_add_u32 m0, %18, 0x1e500\n\t" "s_nop 0\n\t" "global_load_lds_dwordx4 v224, s[42:43]\n\t" "s_add_u32 m0, %18, 0x1e900\n\t"
                     "s_nop 0\n\t" "global_load_lds_dwordx4 v225, s[42:43]\n\t" "s_add_u32 m0, %18, 0x1ed00\n\t" "s_nop 0\n\t" "global_load_lds_dwordx4 v226, s[42:43]\n\t" "s_add_u32 s42, s44, 0x3d8000\n\t"
                     "s_addc_u32 s43, s45, 0\n\t" "s_add_u32 m0, %18, 0x1f120\n\t" "s_nop 0\n\t" "global_load_lds_dwordx4 v224, s[42:43]\n\t" "s_add_u32 m0, %18, 0x1f520\n\t" "s_nop 0\n\t"
                     "global_load_lds_dwordx4 v225, s[42:43]\n\t" "s_add_u32 m0, %18, 0x1f920\n\t" "s_nop 0\n\t" "global_load_lds_dwordx4 v226, s[42:43]\n\t" "v_add_u32 v237, 0x0, v227\n\t" "v_add_u32 v238, 0x0, v228\n\t"
                     "ds_read_b64_tr_b16 a[0:1], v237\n\t" "ds_read_b64_tr_b16 a[2:3], v237 offset:768\n\t" "ds_read_b64_tr_b16 a[4:5], v237 offset:12416\n\t" "ds_read_b64_tr_b16 a[6:7], v237 offset:13184\n\t"
                     "ds_read_b64_tr_b16 v[0:1], v237 offset:32\n\t" "ds_read_b64_tr_b16 v[2:3], v237 offset:800\n\t" "ds_read_b64_tr_b16 v[4:5], v237 offset:12448\n\t" "ds_read_b64_tr_b16 v[6:7], v237 offset:13216\n\t"
                     "ds_read_b64_tr_b16 a[8:9], v238 offset:64\n\t" "ds_read_b64_tr_b16 a[10:11], v238 offset:832\n\t" "ds_read_b64_tr_b16 a[12:13], v238 offset:12480\n\t"
                     "ds_read_b64_tr_b16 a[14:15], v238 offset:13248\n\t" "ds_read_b64_tr_b16 a[16:17], v237 offset:24832\n\t" "ds_read_b64_tr_b16 a[18:19], v237 offset:25600\n\t"
                     "ds_read_b64_tr_b16 a[20:21], v237 offset:37248\n\t" "ds_read_b64_tr_b16 a[22:23], v237 offset:38016\n\t" "ds_read_b64_tr_b16 v[8:9], v237 offset:24864\n\t"
                     "ds_read_b64_tr_b16 v[10:11], v237 offset:25632\n\t" "ds_read_b64_tr_b16 v[12:13], v237 offset:37280\n\t" "ds_read_b64_tr_b16 v[14:15], v237 offset:38048\n\t"
                     "ds_read_b64_tr_b16 a[24:25], v238 offset:24896\n\t" "ds_read_b64_tr_b16 a[26:27], v238 offset:25664\n\t" "ds_read_b64_tr_b16 a[28:29], v238 offset:37312\n\t"
                     "ds_read_b64_tr_b16 a[30:31], v238 offset:38080\n\t" "s_waitcnt vmcnt(12) lgkmcnt(0)\n\t" "s_barrier\n\t" "s_add_u32 s42, s44, 0x480000\n\t" "s_addc_u32 s43, s45, 0\n\t" "s_add_u32 m0, %18, 0x0\n\t"
                     "s_nop 0\n\t" "global_load_lds_dwordx4 v224, s[42:43]\n\t" "s_add_u32 m0, %18, 0x400\n\t" "s_nop 0\n\t" "global_load_lds_dwordx4 v225, s[42:43]\n\t" "s_add_u32 m0, %18, 0x800\n\t" "s_nop 0\n\t"
                     "global_load_lds_dwordx4 v226, s[42:43]\n\t" "s_add_u32 s42, s44, 0x498000\n\t" "s_addc_u32 s43, s45, 0\n\t" "s_add_u32 m0, %18, 0xc20\n\t" "s_nop 0\n\t" "global_load_lds_dwordx4 v224, s[42:43]\n\t"
                     "s_add_u32 m0, %18, 0x1020\n\t" "s_nop 0\n\t" "global_load_lds_dwordx4 v225, s[42:43]\n\t" "s_add_u32 m0, %18, 0x1420\n\t" "s_nop 0\n\t" "global_load_lds_dwordx4 v226, s[42:43]\n\t"
                     "s_add_u32 s42, s44, 0x540000\n\t" "s_addc_u32 s43, s45, 0\n\t" "s_add_u32 m0, %18, 0x6100\n\t" "s_nop 0\n\t" "global_load_lds_dwordx4 v224, s[42:43]\n\t" "s_add_u32 m0, %18, 0x6500\n\t" "s_nop 0\n\t"
                     "global_load_lds_dwordx4 v225, s[42:43]\n\t" "s_add_u32 m0, %18, 0x6900\n\t" "s_nop 0\n\t" "global_load_lds_dwordx4 v226, s[42:43]\n\t" "s_add_u32 s42, s44, 0x558000\n\t" "s_addc_u32 s43, s45, 0\n\t"
                     "s_add_u32 m0, %18, 0x6d20\n\t" "s_nop 0\n\t" "global_load_lds_dwordx4 v224, s[42:43]\n\t" "s_add_u32 m0, %18, 0x7120\n\t" "s_nop 0\n\t" "global_load_lds_dwordx4 v225, s[42:43]\n\t"
                     "s_add_u32 m0, %18, 0x7520\n\t" "s_nop 0\n\t" "global_load_lds_dwordx4 v226, s[42:43]\n\t" "v_add_u32 v237, 0xc200, v227\n\t" "v_add_u32 v238, 0xc200, v228\n\t" "ds_read_b64_tr_b16 a[32:33], v237\n\t"
                     "ds_read_b64_tr_b16 a[34:35], v237 offset:768\n\t" "ds_read_b64_tr_b16 a[36:37], v237 offset:12416\n\t" "ds_read_b64_tr_b16 a[38:39], v237 offset:13184\n\t"
                     "ds_read_b64_tr_b16 v[16:17], v237 offset:32\n\t" "ds_read_b64_tr_b16 v[18:19], v237 offset:800\n\t" "ds_read_b64_tr_b16 v[20:21], v237 offset:12448\n\t"
                     "ds_read_b64_tr_b16 v[22:23], v237 offset:13216\n\t" "ds_read_b64_tr_b16 a[40:41], v238 offset:64\n\t" "ds_read_b64_tr_b16 a[42:43], v238 offset:832\n\t"
                     "ds_read_b64_tr_b16 a[44:45], v238 offset:12480\n\t" "ds_read_b64_tr_b16 a[46:47], v238 offset:13248\n\t" "ds_read_b64_tr_b16 a[48:49], v237 offset:24832\n\t"
                     "ds_read_b64_tr_b16 a[50:51], v237 offset:25600\n\t" "ds_read_b64_tr_b16 a[52:53], v237 offset:37248\n\t" "ds_read_b64_tr_b16 a[54:55], v237 offset:38016\n\t"
                     "ds_read_b64_tr_b16 v[24:25], v237 offset:24864\n\t" "ds_read_b64_tr_b16 v[26:27], v237 offset:25632\n\t" "ds_read_b64_tr_b16 v[28:29], v237 offset:37280\n\t"
                     "ds_read_b64_tr_b16 v[30:31], v237 offset:38048\n\t" "ds_read_b64_tr_b16 a[56:57], v238 offset:24896\n\t" "ds_read_b64_tr_b16 a[58:59], v238 offset:25664\n\t"
                     "ds_read_b64_tr_b16 a[60:61], v238 offset:37312\n\t" "ds_read_b64_tr_b16 a[62:63], v238 offset:38080\n\t" "s_waitcnt vmcnt(12) lgkmcnt(0)\n\t" "s_barrier\n\t" "s_add_u32 s42, s44, 0x600000\n\t"
                     "s_addc_u32 s43, s45, 0\n\t" "s_add_u32 m0, %18, 0xc200\n\t" "s_nop 0\n\t" "global_load_lds_dwordx4 v224, s[42:43]\n\t" "s_add_u32 m0, %18, 0xc600\n\t" "s_nop 0\n\t"
                     "global_load_lds_dwordx4 v225, s[42:43]\n\t" "s_add_u32 m0, %18, 0xca00\n\t" "s_nop 0\n\t" "global_load_lds_dwordx4 v226, s[42:43]\n\t" "s_add_u32 s42, s44, 0x618000\n\t" "s_addc_u32 s43, s45, 0\n\t"
                     "s_add_u32 m0, %18, 0xce20\n\t" "s_nop 0\n\t" "global_load_lds_dwordx4 v224, s[42:43]\n\t" "s_add_u32 m0, %18, 0xd220\n\t" "s_nop 0\n\t" "global_load_lds_dwordx4 v225, s[42:43]\n\t"
                     "s_add_u32 m0, %18, 0xd620\n\t" "s_nop 0\n\t" "global_load_lds_dwordx4 v226, s[42:43]\n\t" "s_add_u32 s42, s44, 0x6c0000\n\t" "s_addc_u32 s43, s45, 0\n\t" "s_add_u32 m0, %18, 0x12300\n\t" "s_nop 0\n\t"
                     "global_load_lds_dwordx4 v224, s[42:43]\n\t" "s_add_u32 m0, %18, 0x12700\n\t" "s_nop 0\n\t" "global_load_lds_dwordx4 v225, s[42:43]\n\t" "s_add_u32 m0, %18, 0x12b00\n\t" "s_nop 0\n\t"
                     "global_load_lds_dwordx4 v226, s[42:43]\n\t" "s_add_u32 s42, s44, 0x6d8000\n\t" "s_addc_u32 s43, s45, 0\n\t" "s_add_u32 m0, %18, 0x12f20\n\t" "s_nop 0\n\t" "global_load_lds_dwordx4 v224, s[42:43]\n\t"
                     "s_add_u32 m0, %18, 0x13320\n\t" "s_nop 0\n\t" "global_load_lds_dwordx4 v225, s[42:43]\n\t" "s_add_u32 m0, %18, 0x13720\n\t" "s_nop 0\n\t" "global_load_lds_dwordx4 v226, s[42:43]\n\t"
                     "v_add_u32 v237, 0x18400, v227\n\t" "v_add_u32 v238, 0x18400, v228\n\t" "ds_read_b64_tr_b16 a[64:65], v237\n\t" "ds_read_b64_tr_b16 a[66:67], v237 offset:768\n\t"
                     "ds_read_b64_tr_b16 a[68:69], v237 offset:12416\n\t" "ds_read_b64_tr_b16 a[70:71], v237 offset:13184\n\t" "ds_read_b64_tr_b16 v[32:33], v237 offset:32\n\t"
                     "ds_read_b64_tr_b16 v[34:35], v237 offset:800\n\t" "ds_read_b64_tr_b16 v[36:37], v237 offset:12448\n\t" "ds_read_b64_tr_b16 v[38:39], v237 offset:13216\n\t"
                     "ds_read_b64_tr_b16 a[72:73], v238 offset:64\n\t" "ds_read_b64_tr_b16 a[74:75], v238 offset:832\n\t" "ds_read_b64_tr_b16 a[76:77], v238 offset:12480\n\t"
                     "ds_read_b64_tr_b16 a[78:79], v238 offset:13248\n\t" "ds_read_b64_tr_b16 a[80:81], v237 offset:24832\n\t" "ds_read_b64_tr_b16 a[82:83], v237 offset:25600\n\t"
                     "ds_read_b64_tr_b16 a[84:85], v237 offset:37248\n\t" "ds_read_b64_tr_b16 a[86:87], v237 offset:38016\n\t" "ds_read_b64_tr_b16 v[40:41], v237 offset:24864\n\t"
                     "ds_read_b64_tr_b16 v[42:43], v237 offset:25632\n\t" "ds_read_b64_tr_b16 v[44:45], v237 offset:37280\n\t" "ds_read_b64_tr_b16 v[46:47], v237 offset:38048\n\t"
                     "ds_read_b64_tr_b16 a[88:89], v238 offset:24896\n\t" "ds_read_b64_tr_b16 a[90:91], v238 offset:25664\n\t" "ds_read_b64_tr_b16 a[92:93], v238 offset:37312\n\t"
                     "ds_read_b64_tr_b16 a[94:95], v238 offset:38080\n\t" "s_waitcnt vmcnt(12) lgkmcnt(0)\n\t" "s_barrier\n\t" "s_add_u32 s42, s46, 0x0\n\t" "s_addc_u32 s43, s47, 0\n\t" "s_add_u32 m0, %20, 0x18400\n\t"
                     "s_nop 0\n\t" "global_load_lds_dwordx4 v232, s[42:43]\n\t" "s_add_u32 m0, %20, 0x18800\n\t" "s_nop 0\n\t" "global_load_lds_dwordx4 v233, s[42:43]\n\t" "s_add_u32 m0, %20, 0x18c00\n\t" "s_nop 0\n\t"
                     "global_load_lds_dwordx4 v234, s[42:43]\n\t" "s_add_u32 m0, %20, 0x19000\n\t" "s_nop 0\n\t" "global_load_lds_dwordx4 v235, s[42:43]\n\t" "s_add_u32 m0, %20, 0x19400\n\t" "s_nop 0\n\t"
                     "global_load_lds_dwordx4 v236, s[42:43]\n\t" "s_add_u32 s42, s46, 0x1400\n\t" "s_addc_u32 s43, s47, 0\n\t" "s_add_u32 m0, %20, 0x19800\n\t" "v_xor_b32 v224, 0x80, v232\n\t"
                     "global_load_lds_dwordx4 v224, s[42:43]\n\t" "s_add_u32 m0, %20, 0x19c00\n\t" "v_xor_b32 v224, 0x80, v233\n\t" "global_load_lds_dwordx4 v224, s[42:43]\n\t" "s_add_u32 m0, %20, 0x1a000\n\t"
                     "v_xor_b32 v224, 0x80, v234\n\t" "global_load_lds_dwordx4 v224, s[42:43]\n\t" "s_add_u32 m0, %20, 0x1a400\n\t" "v_xor_b32 v224, 0x80, v235\n\t" "global_load_lds_dwordx4 v224, s[42:43]\n\t"
                     "s_add_u32 m0, %20, 0x1a800\n\t" "v_xor_b32 v224, 0x80, v236\n\t" "global_load_lds_dwordx4 v224, s[42:43]\n\t" "v_add_u32 v237, 0x0, v227\n\t" "v_add_u32 v238, 0x0, v228\n\t"
                     "ds_read_b64_tr_b16 a[96:97], v237\n\t" "ds_read_b64_tr_b16 a[98:99], v237 offset:768\n\t" "ds_read_b64_tr_b16 a[100:101], v237 offset:12416\n\t" "ds_read_b64_tr_b16 a[102:103], v237 offset:13184\n\t"
                     "ds_read_b64_tr_b16 v[48:49], v237 offset:32\n\t" "ds_read_b64_tr_b16 v[50:51], v237 offset:800\n\t" "ds_read_b64_tr_b16 v[52:53], v237 offset:12448\n\t"
                     "ds_read_b64_tr_b16 v[54:55], v237 offset:13216\n\t" "ds_read_b64_tr_b16 a[104:105], v238 offset:64\n\t" "ds_read_b64_tr_b16 a[106:107], v238 offset:832\n\t"
                     "ds_read_b64_tr_b16 a[108:109], v238 offset:12480\n\t" "ds_read_b64_tr_b16 a[110:111], v238 offset:13248\n\t" "ds_read_b64_tr_b16 a[112:113], v237 offset:24832\n\t"
                     "ds_read_b64_tr_b16 a[114:115], v237 offset:25600\n\t" "ds_read_b64_tr_b16 a[116:117], v237 offset:37248\n\t" "ds_read_b64_tr_b16 a[118:119], v237 offset:38016\n\t"
                     "ds_read_b64_tr_b16 v[56:57], v237 offset:24864\n\t" "ds_read_b64_tr_b16 v[58:59], v237 offset:25632\n\t" "ds_read_b64_tr_b16 v[60:61], v237 offset:37280\n\t"
                     "ds_read_b64_tr_b16 v[62:63], v237 offset:38048\n\t" "ds_read_b64_tr_b16 a[120:121], v238 offset:24896\n\t" "ds_read_b64_tr_b16 a[122:123], v238 offset:25664\n\t"
                     "ds_read_b64_tr_b16 a[124:125], v238 offset:37312\n\t" "ds_read_b64_tr_b16 a[126:127], v238 offset:38080\n\t" "s_waitcnt vmcnt(10) lgkmcnt(0)\n\t" "s_barrier\n\t" "s_add_u32 s42, s46, 0xa000\n\t"
                     "s_addc_u32 s43, s47, 0\n\t" "s_add_u32 m0, %20, 0x0\n\t" "s_nop 0\n\t" "global_load_lds_dwordx4 v232, s[42:43]\n\t" "s_add_u32 m0, %20, 0x400\n\t" "s_nop 0\n\t"
                     "global_load_lds_dwordx4 v233, s[42:43]\n\t" "s_add_u32 m0, %20, 0x800\n\t" "s_nop 0\n\t" "global_load_lds_dwordx4 v234, s[42:43]\n\t" "s_add_u32 m0, %20, 0xc00\n\t" "s_nop 0\n\t"
                     "global_load_lds_dwordx4 v235, s[42:43]\n\t" "s_add_u32 m0, %20, 0x1000\n\t" "s_nop 0\n\t" "global_load_lds_dwordx4 v236, s[42:43]\n\t" "s_add_u32 s42, s46, 0xb400\n\t" "s_addc_u32 s43, s47, 0\n\t"
                     "s_add_u32 m0, %20, 0x1400\n\t" "v_xor_b32 v224, 0x80, v232\n\t" "global_load_lds_dwordx4 v224, s[42:43]\n\t" "s_add_u32 m0, %20, 0x1800\n\t" "v_xor_b32 v224, 0x80, v233\n\t"
                     "global_load_lds_dwordx4 v224, s[42:43]\n\t" "s_add_u32 m0, %20, 0x1c00\n\t" "v_xor_b32 v224, 0x80, v234\n\t" "global_load_lds_dwordx4 v224, s[42:43]\n\t" "s_add_u32 m0, %20, 0x2000\n\t"
                     "v_xor_b32 v224, 0x80, v235\n\t" "global_load_lds_dwordx4 v224, s[42:43]\n\t" "s_add_u32 m0, %20, 0x2400\n\t" "v_xor_b32 v224, 0x80, v236\n\t" "global_load_lds_dwordx4 v224, s[42:43]\n\t"
                     "v_add_u32 v237, 0xc200, v227\n\t" "v_add_u32 v238, 0xc200, v228\n\t" "ds_read_b64_tr_b16 a[128:129], v237\n\t" "ds_read_b64_tr_b16 a[130:131], v237 offset:768\n\t"
                     "ds_read_b64_tr_b16 a[132:133], v237 offset:12416\n\t" "ds_read_b64_tr_b16 a[134:135], v237 offset:13184\n\t" "ds_read_b64_tr_b16 a[136:137], v237 offset:32\n\t"
                     "ds_read_b64_tr_b16 a[138:139], v237 offset:800\n\t" "ds_read_b64_tr_b16 a[140:141], v237 offset:12448\n\t" "ds_read_b64_tr_b16 a[142:143], v237 offset:13216\n\t"
                     "ds_read_b64_tr_b16 a[144:145], v238 offset:64\n\t" "ds_read_b64_tr_b16 a[146:147], v238 offset:832\n\t" "ds_read_b64_tr_b16 a[148:149], v238 offset:12480\n\t"
                     "ds_read_b64_tr_b16 a[150:151], v238 offset:13248\n\t" "ds_read_b64_tr_b16 a[152:153], v237 offset:24832\n\t" "ds_read_b64_tr_b16 a[154:155], v237 offset:25600\n\t"
                     "ds_read_b64_tr_b16 a[156:157], v237 offset:37248\n\t" "ds_read_b64_tr_b16 a[158:159], v237 offset:38016\n\t" "ds_read_b64_tr_b16 a[160:161], v237 offset:24864\n\t"
                     "ds_read_b64_tr_b16 a[162:163], v237 offset:25632\n\t" "ds_read_b64_tr_b16 a[164:165], v237 offset:37280\n\t" "ds_read_b64_tr_b16 a[166:167], v237 offset:38048\n\t"
                     "ds_read_b64_tr_b16 a[168:169], v238 offset:24896\n\t" "ds_read_b64_tr_b16 a[170:171], v238 offset:25664\n\t" "ds_read_b64_tr_b16 a[172:173], v238 offset:37312\n\t"
                     "ds_read_b64_tr_b16 a[174:175], v238 offset:38080\n\t" "s_waitcnt vmcnt(10) lgkmcnt(0)\n\t" "s_barrier\n\t" "s_add_u32 s42, s46, 0x14000\n\t" "s_addc_u32 s43, s47, 0\n\t"
                     "s_add_u32 m0, %20, 0xc200\n\t" "s_nop 0\n\t" "global_load_lds_dwordx4 v232, s[42:43]\n\t" "s_add_u32 m0, %20, 0xc600\n\t" "s_nop 0\n\t" "global_load_lds_dwordx4 v233, s[42:43]\n\t"
                     "s_add_u32 m0, %20, 0xca00\n\t" "s_nop 0\n\t" "global_load_lds_dwordx4 v234, s[42:43]\n\t" "s_add_u32 m0, %20, 0xce00\n\t" "s_nop 0\n\t" "global_load_lds_dwordx4 v235, s[42:43]\n\t"
                     "s_add_u32 m0, %20, 0xd200\n\t" "s_nop 0\n\t" "global_load_lds_dwordx4 v236, s[42:43]\n\t" "s_add_u32 s42, s46, 0x15400\n\t" "s_addc_u32 s43, s47, 0\n\t" "s_add_u32 m0, %20, 0xd600\n\t"
                     "v_xor_b32 v224, 0x80, v232\n\t" "global_load_lds_dwordx4 v224, s[42:43]\n\t" "s_add_u32 m0, %20, 0xda00\n\t" "v_xor_b32 v224, 0x80, v233\n\t" "global_load_lds_dwordx4 v224, s[42:43]\n\t"
                     "s_add_u32 m0, %20, 0xde00\n\t" "v_xor_b32 v224, 0x80, v234\n\t" "global_load_lds_dwordx4 v224, s[42:43]\n\t" "s_add_u32 m0, %20, 0xe200\n\t" "v_xor_b32 v224, 0x80, v235\n\t"
                     "global_load_lds_dwordx4 v224, s[42:43]\n\t" "s_add_u32 m0, %20, 0xe600\n\t" "v_xor_b32 v224, 0x80, v236\n\t" "global_load_lds_dwordx4 v224, s[42:43]\n\t" "s_add_u32 s42, %21, 0x18400\n\t"
                     "v_xor_b32 v237, 0x0, v229\n\t" "v_add_u32 v237, s42, v237\n\t" "v_xor_b32 v238, 0x40, v229\n\t" "v_add_u32 v238, s42, v238\n\t" "v_xor_b32 v239, 0x80, v229\n\t" "v_add_u32 v239, s42, v239\n\t"
                     "v_xor_b32 v226, 0xc0, v229\n\t" "v_add_u32 v226, s42, v226\n\t" "ds_read_b64_tr_b16 a[176:177], v237\n\t" "ds_read_b64_tr_b16 a[178:179], v238 offset:2560\n\t"
                     "ds_read_b64_tr_b16 a[200:201], v239\n\t" "ds_read_b64_tr_b16 a[202:203], v226 offset:2560\n\t" "ds_read_b64_tr_b16 a[224:225], v237 offset:256\n\t"
                     "ds_read_b64_tr_b16 a[226:227], v238 offset:2816\n\t" "ds_read_b64_tr_b16 a[248:249], v239 offset:256\n\t" "ds_read_b64_tr_b16 a[250:251], v226 offset:2816\n\t"
                     "ds_read_b64_tr_b16 v[112:113], v237 offset:512\n\t" "ds_read_b64_tr_b16 v[114:115], v238 offset:3072\n\t" "ds_read_b64_tr_b16 v[136:137], v239 offset:512\n\t"
                     "ds_read_b64_tr_b16 v[138:139], v226 offset:3072\n\t" "ds_read_b64_tr_b16 v[160:161], v237 offset:768\n\t" "ds_read_b64_tr_b16 v[162:163], v238 offset:3328\n\t"
                     "ds_read_b64_tr_b16 v[184:185], v239 offset:768\n\t" "ds_read_b64_tr_b16 v[186:187], v226 offset:3328\n\t" "ds_read_b64_tr_b16 v[208:209], v237 offset:1024\n\t"
                     "ds_read_b64_tr_b16 v[210:211], v238 offset:3584\n\t" "ds_read_b64_tr_b16 v[72:73], v239 offset:1024\n\t" "ds_read_b64_tr_b16 v[74:75], v226 offset:3584\n\t" "s_waitcnt vmcnt(10) lgkmcnt(0)\n\t"
                     "s_barrier\n\t" "s_add_u32 s42, s46, 0x1e000\n\t" "s_addc_u32 s43, s47, 0\n\t" "s_add_u32 m0, %20, 0x18400\n\t" "s_nop 0\n\t" "global_load_lds_dwordx4 v232, s[42:43]\n\t"
                     "s_add_u32 m0, %20, 0x18800\n\t" "s_nop 0\n\t" "global_load_lds_dwordx4 v233, s[42:43]\n\t" "s_add_u32 m0, %20, 0x18c00\n\t" "s_nop 0\n\t" "global_load_lds_dwordx4 v234, s[42:43]\n\t"
                     "s_add_u32 m0, %20, 0x19000\n\t" "s_nop 0\n\t" "global_load_lds_dwordx4 v235, s[42:43]\n\t" "s_add_u32 m0, %20, 0x19400\n\t" "s_nop 0\n\t" "global_load_lds_dwordx4 v236, s[42:43]\n\t"
                     "s_add_u32 s42, s46, 0x1f400\n\t" "s_addc_u32 s43, s47, 0\n\t" "s_add_u32 m0, %20, 0x19800\n\t" "v_xor_b32 v224, 0x80, v232\n\t" "global_load_lds_dwordx4 v224, s[42:43]\n\t"
                     "s_add_u32 m0, %20, 0x19c00\n\t" "v_xor_b32 v224, 0x80, v233\n\t" "global_load_lds_dwordx4 v224, s[42:43]\n\t" "s_add_u32 m0, %20, 0x1a000\n\t" "v_xor_b32 v224, 0x80, v234\n\t"
                     "global_load_lds_dwordx4 v224, s[42:43]\n\t" "s_add_u32 m0, %20, 0x1a400\n\t" "v_xor_b32 v224, 0x80, v235\n\t" "global_load_lds_dwordx4 v224, s[42:43]\n\t" "s_add_u32 m0, %20, 0x1a800\n\t"
                     "v_xor_b32 v224, 0x80, v236\n\t" "global_load_lds_dwordx4 v224, s[42:43]\n\t" "s_add_u32 s42, %21, 0x0\n\t" "v_xor_b32 v237, 0x0, v229\n\t" "v_add_u32 v237, s42, v237\n\t"
                     "v_xor_b32 v238, 0x40, v229\n\t" "v_add_u32 v238, s42, v238\n\t" "v_xor_b32 v239, 0x80, v229\n\t" "v_add_u32 v239, s42, v239\n\t" "v_xor_b32 v226, 0xc0, v229\n\t" "v_add_u32 v226, s42, v226\n\t"
                     "ds_read_b64_tr_b16 a[180:181], v237\n\t" "ds_read_b64_tr_b16 a[182:183], v238 offset:2560\n\t" "ds_read_b64_tr_b16 a[204:205], v239\n\t" "ds_read_b64_tr_b16 a[206:207], v226 offset:2560\n\t"
                     "ds_read_b64_tr_b16 a[228:229], v237 offset:256\n\t" "ds_read_b64_tr_b16 a[230:231], v238 offset:2816\n\t" "ds_read_b64_tr_b16 a[252:253], v239 offset:256\n\t"
                     "ds_read_b64_tr_b16 a[254:255], v226 offset:2816\n\t" "ds_read_b64_tr_b16 v[116:117], v237 offset:512\n\t" "ds_read_b64_tr_b16 v[118:119], v238 offset:3072\n\t"
                     "ds_read_b64_tr_b16 v[140:141], v239 offset:512\n\t" "ds_read_b64_tr_b16 v[142:143], v226 offset:3072\n\t" "ds_read_b64_tr_b16 v[164:165], v237 offset:768\n\t"
                     "ds_read_b64_tr_b16 v[166:167], v238 offset:3328\n\t" "ds_read_b64_tr_b16 v[188:189], v239 offset:768\n\t" "ds_read_b64_tr_b16 v[190:191], v226 offset:3328\n\t"
                     "ds_read_b64_tr_b16 v[212:213], v237 offset:1024\n\t" "ds_read_b64_tr_b16 v[214:215], v238 offset:3584\n\t" "ds_read_b64_tr_b16 v[76:77], v239 offset:1024\n\t"
                     "ds_read_b64_tr_b16 v[78:79], v226 offset:3584\n\t" "s_waitcnt vmcnt(10) lgkmcnt(0)\n\t" "s_barrier\n\t" "v_mov_b32 v225, 0\n\t" "global_load_dwordx2 %12, v231, %19 sc1\n\t"
                     "global_load_dwordx2 %13, v225, %19 offset:512 sc1\n\t" "global_load_dwordx2 %14, v225, %19 offset:1024 sc1\n\t" "s_add_u32 s42, s46, 0x28000\n\t" "s_addc_u32 s43, s47, 0\n\t"
                     "s_add_u32 m0, %20, 0x0\n\t" "s_nop 0\n\t" "global_load_lds_dwordx4 v232, s[42:43]\n\t" "s_add_u32 m0, %20, 0x400\n\t" "s_nop 0\n\t" "global_load_lds_dwordx4 v233, s[42:43]\n\t"
                     "s_add_u32 m0, %20, 0x800\n\t" "s_nop 0\n\t" "global_load_lds_dwordx4 v234, s[42:43]\n\t" "s_add_u32 m0, %20, 0xc00\n\t" "s_nop 0\n\t" "global_load_lds_dwordx4 v235, s[42:43]\n\t"
                     "s_add_u32 m0, %20, 0x1000\n\t" "s_nop 0\n\t" "global_load_lds_dwordx4 v236, s[42:43]\n\t" "s_add_u32 s42, s46, 0x29400\n\t" "s_addc_u32 s43, s47, 0\n\t" "s_add_u32 m0, %20, 0x1400\n\t"
                     "v_xor_b32 v224, 0x80, v232\n\t" "global_load_lds_dwordx4 v224, s[42:43]\n\t" "s_add_u32 m0, %20, 0x1800\n\t" "v_xor_b32 v224, 0x80, v233\n\t" "global_load_lds_dwordx4 v224, s[42:43]\n\t"
                     "s_add_u32 m0, %20, 0x1c00\n\t" "v_xor_b32 v224, 0x80, v234\n\t" "global_load_lds_dwordx4 v224, s[42:43]\n\t" "s_add_u32 m0, %20, 0x2000\n\t" "v_xor_b32 v224, 0x80, v235\n\t"
                     "global_load_lds_dwordx4 v224, s[42:43]\n\t" "s_add_u32 m0, %20, 0x2400\n\t" "v_xor_b32 v224, 0x80, v236\n\t" "global_load_lds_dwordx4 v224, s[42:43]\n\t" "s_add_u32 s42, %21, 0xc200\n\t"
                     "v_xor_b32 v237, 0x0, v229\n\t" "v_add_u32 v237, s42, v237\n\t" "v_xor_b32 v238, 0x40, v229\n\t" "v_add_u32 v238, s42, v238\n\t" "v_xor_b32 v239, 0x80, v229\n\t" "v_add_u32 v239, s42, v239\n\t"
                     "v_xor_b32 v226, 0xc0, v229\n\t" "v_add_u32 v226, s42, v226\n\t" "ds_read_b64_tr_b16 a[184:185], v237\n\t" "ds_read_b64_tr_b16 a[186:187], v238 offset:2560\n\t"
                     "ds_read_b64_tr_b16 a[208:209], v239\n\t" "ds_read_b64_tr_b16 a[210:211], v226 offset:2560\n\t" "ds_read_b64_tr_b16 a[232:233], v237 offset:256\n\t"
                     "ds_read_b64_tr_b16 a[234:235], v238 offset:2816\n\t" "ds_read_b64_tr_b16 v[96:97], v239 offset:256\n\t" "ds_read_b64_tr_b16 v[98:99], v226 offset:2816\n\t"
                     "ds_read_b64_tr_b16 v[120:121], v237 offset:512\n\t" "ds_read_b64_tr_b16 v[122:123], v238 offset:3072\n\t" "ds_read_b64_tr_b16 v[144:145], v239 offset:512\n\t"
                     "ds_read_b64_tr_b16 v[146:147], v226 offset:3072\n\t" "ds_read_b64_tr_b16 v[168:169], v237 offset:768\n\t" "ds_read_b64_tr_b16 v[170:171], v238 offset:3328\n\t"
                     "ds_read_b64_tr_b16 v[192:193], v239 offset:768\n\t" "ds_read_b64_tr_b16 v[194:195], v226 offset:3328\n\t" "ds_read_b64_tr_b16 v[216:217], v237 offset:1024\n\t"
                     "ds_read_b64_tr_b16 v[218:219], v238 offset:3584\n\t" "ds_read_b64_tr_b16 v[80:81], v239 offset:1024\n\t" "ds_read_b64_tr_b16 v[82:83], v226 offset:3584\n\t" "s_waitcnt vmcnt(10) lgkmcnt(0)\n\t"
                     "s_barrier\n\t" "s_add_u32 s42, s46, 0x32000\n\t" "s_addc_u32 s43, s47, 0\n\t" "s_add_u32 m0, %20, 0xc200\n\t" "s_nop 0\n\t" "global_load_lds_dwordx4 v232, s[42:43]\n\t" "s_add_u32 m0, %20, 0xc600\n\t"
                     "s_nop 0\n\t" "global_load_lds_dwordx4 v233, s[42:43]\n\t" "s_add_u32 m0, %20, 0xca00\n\t" "s_nop 0\n\t" "global_load_lds_dwordx4 v234, s[42:43]\n\t" "s_add_u32 m0, %20, 0xce00\n\t" "s_nop 0\n\t"
                     "global_load_lds_dwordx4 v235, s[42:43]\n\t" "s_add_u32 m0, %20, 0xd200\n\t" "s_nop 0\n\t" "global_load_lds_dwordx4 v236, s[42:43]\n\t" "s_add_u32 s42, s46, 0x33400\n\t" "s_addc_u32 s43, s47, 0\n\t"
                     "s_add_u32 m0, %20, 0xd600\n\t" "v_xor_b32 v224, 0x80, v232\n\t" "global_load_lds_dwordx4 v224, s[42:43]\n\t" "s_add_u32 m0, %20, 0xda00\n\t" "v_xor_b32 v224, 0x80, v233\n\t"
                     "global_load_lds_dwordx4 v224, s[42:43]\n\t" "s_add_u32 m0, %20, 0xde00\n\t" "v_xor_b32 v224, 0x80, v234\n\t" "global_load_lds_dwordx4 v224, s[42:43]\n\t" "s_add_u32 m0, %20, 0xe200\n\t"
                     "v_xor_b32 v224, 0x80, v235\n\t" "global_load_lds_dwordx4 v224, s[42:43]\n\t" "s_add_u32 m0, %20, 0xe600\n\t" "v_xor_b32 v224, 0x80, v236\n\t" "global_load_lds_dwordx4 v224, s[42:43]\n\t"
                     "s_add_u32 s42, %21, 0x18400\n\t" "v_xor_b32 v237, 0x0, v229\n\t" "v_add_u32 v237, s42, v237\n\t" "v_xor_b32 v238, 0x40, v229\n\t" "v_add_u32 v238, s42, v238\n\t" "v_xor_b32 v239, 0x80, v229\n\t"
                     "v_add_u32 v239, s42, v239\n\t" "v_xor_b32 v226, 0xc0, v229\n\t" "v_add_u32 v226, s42, v226\n\t" "ds_read_b64_tr_b16 a[188:189], v237\n\t" "ds_read_b64_tr_b16 a[190:191], v238 offset:2560\n\t"
                     "ds_read_b64_tr_b16 a[212:213], v239\n\t" "ds_read_b64_tr_b16 a[214:215], v226 offset:2560\n\t" "ds_read_b64_tr_b16 a[236:237], v237 offset:256\n\t"
                     "ds_read_b64_tr_b16 a[238:239], v238 offset:2816\n\t" "ds_read_b64_tr_b16 v[100:101], v239 offset:256\n\t" "ds_read_b64_tr_b16 v[102:103], v226 offset:2816\n\t"
                     "ds_read_b64_tr_b16 v[124:125], v237 offset:512\n\t" "ds_read_b64_tr_b16 v[126:127], v238 offset:3072\n\t" "ds_read_b64_tr_b16 v[148:149], v239 offset:512\n\t"
                     "ds_read_b64_tr_b16 v[150:151], v226 offset:3072\n\t" "ds_read_b64_tr_b16 v[172:173], v237 offset:768\n\t" "ds_read_b64_tr_b16 v[174:175], v238 offset:3328\n\t"
                     "ds_read_b64_tr_b16 v[196:197], v239 offset:768\n\t" "ds_read_b64_tr_b16 v[198:199], v226 offset:3328\n\t" "ds_read_b64_tr_b16 v[220:221], v237 offset:1024\n\t"
                     "ds_read_b64_tr_b16 v[222:223], v238 offset:3584\n\t" "ds_read_b64_tr_b16 v[84:85], v239 offset:1024\n\t" "ds_read_b64_tr_b16 v[86:87], v226 offset:3584\n\t" "s_waitcnt vmcnt(10) lgkmcnt(0)\n\t"
                     "s_barrier\n\t" "s_add_u32 s42, %21, 0x0\n\t" "v_xor_b32 v237, 0x0, v229\n\t" "v_add_u32 v237, s42, v237\n\t" "v_xor_b32 v238, 0x40, v229\n\t" "v_add_u32 v238, s42, v238\n\t"
                     "v_xor_b32 v239, 0x80, v229\n\t" "v_add_u32 v239, s42, v239\n\t" "v_xor_b32 v226, 0xc0, v229\n\t" "v_add_u32 v226, s42, v226\n\t" "ds_read_b64_tr_b16 a[192:193], v237\n\t"
                     "ds_read_b64_tr_b16 a[194:195], v238 offset:2560\n\t" "ds_read_b64_tr_b16 a[216:217], v239\n\t" "ds_read_b64_tr_b16 a[218:219], v226 offset:2560\n\t"
                     "ds_read_b64_tr_b16 a[240:241], v237 offset:256\n\t" "ds_read_b64_tr_b16 a[242:243], v238 offset:2816\n\t" "ds_read_b64_tr_b16 v[104:105], v239 offset:256\n\t"
                     "ds_read_b64_tr_b16 v[106:107], v226 offset:2816\n\t" "ds_read_b64_tr_b16 v[128:129], v237 offset:512\n\t" "ds_read_b64_tr_b16 v[130:131], v238 offset:3072\n\t"
                     "ds_read_b64_tr_b16 v[152:153], v239 offset:512\n\t" "ds_read_b64_tr_b16 v[154:155], v226 offset:3072\n\t" "ds_read_b64_tr_b16 v[176:177], v237 offset:768\n\t"
                     "ds_read_b64_tr_b16 v[178:179], v238 offset:3328\n\t" "ds_read_b64_tr_b16 v[200:201], v239 offset:768\n\t" "ds_read_b64_tr_b16 v[202:203], v226 offset:3328\n\t"
                     "ds_read_b64_tr_b16 v[64:65], v237 offset:1024\n\t" "ds_read_b64_tr_b16 v[66:67], v238 offset:3584\n\t" "ds_read_b64_tr_b16 v[88:89], v239 offset:1024\n\t"
                     "ds_read_b64_tr_b16 v[90:91], v226 offset:3584\n\t" "s_waitcnt vmcnt(0) lgkmcnt(0)\n\t" "s_barrier\n\t" "s_add_u32 s42, %21, 0xc200\n\t" "v_xor_b32 v237, 0x0, v229\n\t" "v_add_u32 v237, s42, v237\n\t"
                     "v_xor_b32 v238, 0x40, v229\n\t" "v_add_u32 v238, s42, v238\n\t" "v_xor_b32 v239, 0x80, v229\n\t" "v_add_u32 v239, s42, v239\n\t" "v_xor_b32 v226, 0xc0, v229\n\t" "v_add_u32 v226, s42, v226\n\t"
                     "ds_read_b64_tr_b16 a[196:197], v237\n\t" "ds_read_b64_tr_b16 a[198:199], v238 offset:2560\n\t" "ds_read_b64_tr_b16 a[220:221], v239\n\t" "ds_read_b64_tr_b16 a[222:223], v226 offset:2560\n\t"
                     "ds_read_b64_tr_b16 a[244:245], v237 offset:256\n\t" "ds_read_b64_tr_b16 a[246:247], v238 offset:2816\n\t" "ds_read_b64_tr_b16 v[108:109], v239 offset:256\n\t"
                     "ds_read_b64_tr_b16 v[110:111], v226 offset:2816\n\t" "ds_read_b64_tr_b16 v[132:133], v237 offset:512\n\t" "ds_read_b64_tr_b16 v[134:135], v238 offset:3072\n\t"
                     "ds_read_b64_tr_b16 v[156:157], v239 offset:512\n\t" "ds_read_b64_tr_b16 v[158:159], v226 offset:3072\n\t" "ds_read_b64_tr_b16 v[180:181], v237 offset:768\n\t"
                     "ds_read_b64_tr_b16 v[182:183], v238 offset:3328\n\t" "ds_read_b64_tr_b16 v[204:205], v239 offset:768\n\t" "ds_read_b64_tr_b16 v[206:207], v226 offset:3328\n\t"
                     "ds_read_b64_tr_b16 v[68:69], v237 offset:1024\n\t" "ds_read_b64_tr_b16 v[70:71], v238 offset:3584\n\t" "ds_read_b64_tr_b16 v[92:93], v239 offset:1024\n\t"
                     "ds_read_b64_tr_b16 v[94:95], v226 offset:3584\n\t" "s_waitcnt lgkmcnt(0)\n\t" "ds_write_b128 v230, v[0:3]\n\t" "ds_write_b128 v230, v[4:7] offset:1024\n\t"
                     "ds_write_b128 v230, v[8:11] offset:2048\n\t" "ds_write_b128 v230, v[12:15] offset:3072\n\t" "ds_write_b128 v230, v[16:19] offset:4096\n\t" "ds_write_b128 v230, v[20:23] offset:5120\n\t"
                     "ds_write_b128 v230, v[24:27] offset:6144\n\t" "ds_write_b128 v230, v[28:31] offset:7168\n\t" "ds_write_b128 v230, v[32:35] offset:8192\n\t" "ds_write_b128 v230, v[36:39] offset:9216\n\t"
                     "ds_write_b128 v230, v[40:43] offset:10240\n\t" "ds_write_b128 v230, v[44:47] offset:11264\n\t" "ds_write_b128 v230, v[48:51] offset:12288\n\t" "ds_write_b128 v230, v[52:55] offset:13312\n\t"
                     "ds_write_b128 v230, v[56:59] offset:14336\n\t" "ds_write_b128 v230, v[60:63] offset:15360\n\t" "ds_write_b128 v230, v[64:67] offset:16384\n\t" "ds_write_b128 v230, v[68:71] offset:17408\n\t"
                     "ds_write_b128 v230, v[72:75] offset:18432\n\t" "ds_write_b128 v230, v[76:79] offset:19456\n\t" "ds_write_b128 v230, v[80:83] offset:20480\n\t" "ds_write_b128 v230, v[84:87] offset:21504\n\t"
                     "ds_write_b128 v230, v[88:91] offset:22528\n\t" "ds_write_b128 v230, v[92:95] offset:23552\n\t"
                     : "=&{a[0:31]}"(wa[0]), "=&{a[32:63]}"(wa[1]), "=&{a[64:95]}"(wa[2]), "=&{a[96:127]}"(wa[3]), "=&{a[128:159]}"(wa[4]), "=&{a[160:191]}"(wa[5]), "=&{a[192:223]}"(wa[6]), "=&{a[224:255]}"(wa[7]), "=&{v[96:127]}"(wv[0]), "=&{v[128:159]}"(wv[1]), "=&{v[160:191]}"(wv[2]), "=&{v[192:223]}"(wv[3]), "=&v"(y.r), "=&v"(y.g), "=&v"(y.e), "+{v[224:239]}"(pa)
                     : "{s[44:45]}"(p.wv + h * DH * 2 + (size_t)(16 * w) * (HD * 2)), "{s[46:47]}"(p.wo + (size_t)(h * DH + 8 * w) * 1280),
                       "s"(lds + 2 * w * GS), "s"(p.ws + OFF_LAT), "s"(lds + w * 10240), "s"(lds)
                     : "v0", "v1", "v2", "v3", "v4", "v5", "v6", "v7", "v8", "v9", "v10", "v11", "v12", "v13", "v14", "v15", "v16", "v17", "v18", "v19", "v20", "v21", "v22", "v23", "v24", "v25", "v26", "v27", "v28", "v29", "v30", "v31", "v32", "v33", "v34", "v35", "v36", "v37", "v38", "v39", "v40", "v41", "v42", "v43", "v44", "v45", "v46", "v47", "v48", "v49", "v50", "v51", "v52", "v53", "v54", "v55", "v56", "v57", "v58", "v59", "v60", "v61", "v62", "v63", "v64", "v65", "v66", "v67", "v68", "v69", "v70", "v71", "v72", "v73", "v74", "v75", "v76", "v77", "v78", "v79", "v80", "v81", "v82", "v83", "v84", "v85", "v86", "v87", "v88", "v89", "v90", "v91", "v92", "v93", "v94", "v95", "s42", "s43", "scc", "memory");
        sd = sync_end(y, p.ws, phi, w == 0);
        barrier();
    }
    const int lane = lanex(), tid = 64 * w + lane;
    const int n = lane & 15, g = lane >> 4, qq = (lane >> 2) & 3, pp = lane & 3;
    char* const fl = p.ws + OFF_ST;
    const u32 iw = lds + 65536 + ((lane * 8) ^ (lane & 32));
    const u32 qr0 = lds + 65536 + (g >> 1) * 512 + pp * 128 + 64 * (g & 1) + 8 * qq + 32 * (pp >> 1);
    const u32 uo = (u32)((2 * w + (n >> 3)) * 4096 + h * 128 + (n & 7) * 16);
    u32x16 ad = u32x16{(u32)lane * 16, (lane & 2) ? 0xEEEEu : 0x4444u, lds + (u32)((w & 1) * 24576 + (w >> 1) * 99328 + lane * 16), iw + ((3 * w + (w & 1)) >> 1) * 512,
                       iw + (1 + 3 * (w >> 1)) * 512 + 4 * (w & 1), qr0, qr0 ^ 32, uo + (2 + g) * 32768, uo + (g & 1) * 32768 + (g >> 1) * 8, uo, uo, uo, uo, uo, uo, uo};
    asm volatile("" : "+{v[224:239]}"(ad));
    const char* memp = p.ws + mslot(sd, 0);
    int budget = 30000;
    Clock clk; clk.init();
    for (int s = 0; s < NSEG; ++s) {
        int tries = 0;
        u64 bad, tp8, en = ~0ull;
        while (true) {
            const u32 tgt = clk.sched(phi, 0, cfg.G);
            char* up = p.ws + uslot(sd, s);
            asm volatile("ds_read_b128 v[64:67], v226\n\t" "ds_read_b128 v[68:71], v226 offset:1024\n\t" "ds_read_b128 v[72:75], v226 offset:2048\n\t" "ds_read_b128 v[76:79], v226 offset:3072\n\t"
                         "ds_read_b128 v[80:83], v226 offset:4096\n\t" "ds_read_b128 v[84:87], v226 offset:5120\n\t" "ds_read_b128 v[88:91], v226 offset:6144\n\t" "ds_read_b128 v[92:95], v226 offset:7168\n\t"
                         "v_mov_b64 v[40:41], 0\n\t" "v_mov_b64 v[42:43], 0\n\t" "v_mov_b64 v[44:45], 0\n\t" "v_mov_b64 v[46:47], 0\n\t" "v_mov_b64 v[48:49], 0\n\t" "v_mov_b64 v[50:51], 0\n\t" ".Lsp_u:\n\t" "s_memtime vcc\n\t"
                         "s_waitcnt lgkmcnt(0)\n\t" "s_sub_i32 s42, %3, vcc_lo\n\t" "s_cmp_gt_i32 s42, 80\n\t" "s_cbranch_scc1 .Lsp_u\n\t" "s_max_i32 s42, s42, 0\n\t" "s_sub_i32 s42, 92, s42\n\t" "s_and_b32 s42, s42, -4\n\t"
                         "s_getpc_b64 vcc\n\t" "s_add_u32 vcc_lo, vcc_lo, s42\n\t" "s_addc_u32 vcc_hi, vcc_hi, 0\n\t" "s_setpc_b64 vcc\n\t" "s_nop 0\n\t" "s_nop 0\n\t" "s_nop 0\n\t" "s_nop 0\n\t" "s_nop 0\n\t" "s_nop 0\n\t"
                         "s_nop 0\n\t" "s_nop 0\n\t" "s_nop 0\n\t" "s_nop 0\n\t" "s_nop 0\n\t" "s_nop 0\n\t" "s_nop 0\n\t" "s_nop 0\n\t" "s_nop 0\n\t" "s_nop 0\n\t" "s_nop 0\n\t" "s_nop 0\n\t" "s_nop 0\n\t" "s_nop 0\n\t"
                         "global_load_dwordx4 v[0:3], v224, %4 offset:-4096 sc1\n\t" "s_nop 10\n\t" "global_load_dwordx4 v[4:7], v224, %4 offset:-3072 sc1\n\t" "s_nop 10\n\t"
                         "global_load_dwordx4 v[8:11], v224, %4 offset:-2048 sc1\n\t" "s_nop 10\n\t" "global_load_dwordx4 v[12:15], v224, %4 offset:-1024 sc1\n\t" "s_nop 10\n\t" "global_load_dwordx4 v[16:19], v224, %4 sc1\n\t"
                         "s_nop 10\n\t" "global_load_dwordx4 v[20:23], v224, %4 offset:1024 sc1\n\t" "s_nop 10\n\t" "global_load_dwordx4 v[24:27], v224, %4 offset:2048 sc1\n\t" "s_nop 10\n\t"
                         "global_load_dwordx4 v[28:31], v224, %4 offset:3072 sc1\n\t" "s_nop 10\n\t" "global_load_dwordx4 v[32:35], v224, %5 offset:-4096 sc1\n\t" "s_nop 10\n\t"
                         "global_load_dwordx4 v[36:39], v224, %5 offset:-3072 sc1\n\t" "s_waitcnt vmcnt(9)\n\t" "v_smfmac_f32_16x16x64_bf16 v[40:43], v[0:3], a[0:7], v225\n\t" "v_cmp_eq_u32 vcc, -1, v0\n\t"
                         "v_smfmac_f32_16x16x64_bf16 v[44:47], v[0:3], v[64:71], v225\n\t" "s_and_b64 vcc, vcc, %6\n\t" "s_cbranch_scc1 .Lsk_u\n\t" "ds_read_b128 v[64:67], v226 offset:8192\n\t"
                         "v_smfmac_f32_16x16x64_bf16 v[48:51], v[0:3], a[8:15], v225\n\t" "ds_read_b128 v[68:71], v226 offset:9216\n\t" "s_waitcnt vmcnt(8)\n\t" "v_smfmac_f32_16x16x64_bf16 v[40:43], v[4:7], a[16:23], v225\n\t"
                         "v_mov_b64 v[0:1], 0\n\t" "v_smfmac_f32_16x16x64_bf16 v[44:47], v[4:7], v[72:79], v225\n\t" "ds_read_b128 v[72:75], v226 offset:10240\n\t" "v_mov_b64 v[2:3], 0\n\t"
                         "v_smfmac_f32_16x16x64_bf16 v[48:51], v[4:7], a[24:31], v225\n\t" "ds_read_b128 v[76:79], v226 offset:11264\n\t" "s_waitcnt vmcnt(7)\n\t"
                         "v_smfmac_f32_16x16x64_bf16 v[40:43], v[8:11], a[32:39], v225\n\t" "v_mov_b64 v[4:5], 0\n\t" "v_smfmac_f32_16x16x64_bf16 v[44:47], v[8:11], v[80:87], v225\n\t"
                         "ds_read_b128 v[80:83], v226 offset:12288\n\t" "v_mov_b64 v[6:7], 0\n\t" "v_smfmac_f32_16x16x64_bf16 v[48:51], v[8:11], a[40:47], v225\n\t" "ds_read_b128 v[84:87], v226 offset:13312\n\t"
                         "s_waitcnt vmcnt(6)\n\t" "v_smfmac_f32_16x16x64_bf16 v[40:43], v[12:15], a[48:55], v225\n\t" "v_mov_b64 v[8:9], 0\n\t" "v_smfmac_f32_16x16x64_bf16 v[44:47], v[12:15], v[88:95], v225\n\t"
                         "ds_read_b128 v[88:91], v226 offset:14336\n\t" "v_mov_b64 v[10:11], 0\n\t" "v_smfmac_f32_16x16x64_bf16 v[48:51], v[12:15], a[56:63], v225\n\t" "ds_read_b128 v[92:95], v226 offset:15360\n\t"
                         "s_waitcnt vmcnt(5)\n\t" "v_smfmac_f32_16x16x64_bf16 v[40:43], v[16:19], a[64:71], v225\n\t" "v_mov_b64 v[12:13], 0\n\t" "s_waitcnt lgkmcnt(6)\n\t"
                         "v_smfmac_f32_16x16x64_bf16 v[44:47], v[16:19], v[64:71], v225\n\t" "ds_read_b128 v[64:67], v226 offset:16384\n\t" "v_mov_b64 v[14:15], 0\n\t"
                         "v_smfmac_f32_16x16x64_bf16 v[48:51], v[16:19], a[72:79], v225\n\t" "ds_read_b128 v[68:71], v226 offset:17408\n\t" "s_waitcnt vmcnt(4)\n\t"
                         "v_smfmac_f32_16x16x64_bf16 v[40:43], v[20:23], a[80:87], v225\n\t" "v_mov_b64 v[16:17], 0\n\t" "s_waitcnt lgkmcnt(6)\n\t" "v_smfmac_f32_16x16x64_bf16 v[44:47], v[20:23], v[72:79], v225\n\t"
                         "ds_read_b128 v[72:75], v226 offset:18432\n\t" "v_mov_b64 v[18:19], 0\n\t" "v_smfmac_f32_16x16x64_bf16 v[48:51], v[20:23], a[88:95], v225\n\t" "ds_read_b128 v[76:79], v226 offset:19456\n\t"
                         "s_waitcnt vmcnt(3)\n\t" "v_smfmac_f32_16x16x64_bf16 v[40:43], v[24:27], a[96:103], v225\n\t" "v_mov_b64 v[20:21], 0\n\t" "s_waitcnt lgkmcnt(6)\n\t"
                         "v_smfmac_f32_16x16x64_bf16 v[44:47], v[24:27], v[80:87], v225\n\t" "ds_read_b128 v[80:83], v226 offset:20480\n\t" "v_mov_b64 v[22:23], 0\n\t"
                         "v_smfmac_f32_16x16x64_bf16 v[48:51], v[24:27], a[104:111], v225\n\t" "ds_read_b128 v[84:87], v226 offset:21504\n\t" "s_waitcnt vmcnt(2)\n\t"
                         "v_smfmac_f32_16x16x64_bf16 v[40:43], v[28:31], a[112:119], v225\n\t" "v_mov_b64 v[24:25], 0\n\t" "s_waitcnt lgkmcnt(6)\n\t" "v_smfmac_f32_16x16x64_bf16 v[44:47], v[28:31], v[88:95], v225\n\t"
                         "ds_read_b128 v[88:91], v226 offset:22528\n\t" "v_mov_b64 v[26:27], 0\n\t" "v_smfmac_f32_16x16x64_bf16 v[48:51], v[28:31], a[120:127], v225\n\t" "ds_read_b128 v[92:95], v226 offset:23552\n\t"
                         "s_waitcnt vmcnt(1)\n\t" "v_smfmac_f32_16x16x64_bf16 v[40:43], v[32:35], a[128:135], v225\n\t" "v_mov_b64 v[28:29], 0\n\t" "v_smfmac_f32_16x16x64_bf16 v[44:47], v[32:35], a[136:143], v225\n\t"
                         "v_mov_b64 v[30:31], 0\n\t" "v_smfmac_f32_16x16x64_bf16 v[48:51], v[32:35], a[144:151], v225\n\t" "s_waitcnt vmcnt(0)\n\t" "v_smfmac_f32_16x16x64_bf16 v[40:43], v[36:39], a[152:159], v225\n\t"
                         "v_mov_b64 v[32:33], 0\n\t" "v_smfmac_f32_16x16x64_bf16 v[44:47], v[36:39], a[160:167], v225\n\t" "v_mov_b64 v[34:35], 0\n\t" "v_smfmac_f32_16x16x64_bf16 v[48:51], v[36:39], a[168:175], v225\n\t"
                         "v_add_f32 v52, v40, v42\n\t" "v_add_f32 v53, v41, v43\n\t" "v_cvt_pk_bf16_f32 v58, v52, v53\n\t" "v_add_f32 v54, v44, v46\n\t" "v_add_f32 v55, v45, v47\n\t" "v_cvt_pk_bf16_f32 v59, v54, v55\n\t"
                         "ds_write_b64 v227, v[58:59]\n\t" "v_pk_add_f32 v[56:57], v[48:49], v[50:51]\n\t" "v_cvt_pk_bf16_f32 v60, v56, v57\n\t" "ds_write_b32 v228, v60\n\t" "v_cmp_u_f32 vcc, v52, v53\n\t"
                         "v_mov_b64 v[36:37], 0\n\t" "v_mov_b64 v[38:39], 0\n\t" "s_and_b64 vcc, vcc, %6\n\t" "v_mov_b32 v233, 0\n\t" "s_cbranch_scc1 .Lsk_u\n\t" "v_mov_b32 v233, 0\n\t" "v_mov_b32 v233, 0\n\t"
                         "v_mov_b32 v233, 0\n\t" "v_mov_b32 v233, 0\n\t" "v_mov_b32 v233, 0\n\t" "v_mov_b32 v233, 0\n\t" "s_waitcnt lgkmcnt(0)\n\t" "s_barrier\n\t" "ds_read_b64_tr_b16 v[40:41], v229\n\t"
                         "v_mov_b32 v233, 0\n\t" "ds_read_b64_tr_b16 v[42:43], v230\n\t" "v_mov_b32 v233, 0\n\t" "ds_read_b64_tr_b16 v[44:45], v229 offset:1024\n\t" "v_mov_b32 v233, 0\n\t"
                         "ds_read_b64_tr_b16 v[46:47], v230 offset:1024\n\t" "v_mov_b32 v233, 0\n\t" "ds_read_b64_tr_b16 v[48:49], v229 offset:2048\n\t" "v_mov_b32 v233, 0\n\t"
                         "ds_read_b64_tr_b16 v[50:51], v230 offset:2048\n\t" "v_mov_b32 v233, 0\n\t" "s_waitcnt lgkmcnt(4)\n\t" "v_smfmac_f32_16x16x64_bf16 v[0:3], v[40:43], a[176:183], v225\n\t"
                         "s_waitcnt lgkmcnt(2)\n\t" "v_smfmac_f32_16x16x64_bf16 v[0:3], v[44:47], a[184:191], v225\n\t" "s_waitcnt lgkmcnt(0)\n\t" "v_smfmac_f32_16x16x64_bf16 v[0:3], v[48:51], a[192:199], v225\n\t"
                         "v_smfmac_f32_16x16x64_bf16 v[4:7], v[40:43], a[200:207], v225\n\t" "v_smfmac_f32_16x16x64_bf16 v[4:7], v[44:47], a[208:215], v225\n\t" "v_add_f32 v52, v0, v2\n\t"
                         "v_smfmac_f32_16x16x64_bf16 v[4:7], v[48:51], a[216:223], v225\n\t" "v_add_f32 v53, v1, v3\n\t" "v_smfmac_f32_16x16x64_bf16 v[8:11], v[40:43], a[224:231], v225\n\t"
                         "v_cvt_pk_bf16_f32 v54, v53, v52\n\t" "v_smfmac_f32_16x16x64_bf16 v[8:11], v[44:47], a[232:239], v225\n\t" "v_add_f32 v52, v4, v6\n\t" "v_add_f32 v53, v5, v7\n\t" "v_cvt_pk_bf16_f32 v55, v53, v52\n\t"
                         "s_nop 1\n\t" "v_permlane16_swap_b32 v54, v55\n\t" "v_smfmac_f32_16x16x64_bf16 v[8:11], v[48:51], a[240:247], v225\n\t" "global_store_dwordx2 v232, v[54:55], %2 sc1\n\t"
                         "v_smfmac_f32_16x16x64_bf16 v[12:15], v[40:43], a[248:255], v225\n\t" "v_smfmac_f32_16x16x64_bf16 v[12:15], v[44:47], v[96:103], v225\n\t" "v_add_f32 v52, v8, v10\n\t"
                         "v_smfmac_f32_16x16x64_bf16 v[12:15], v[48:51], v[104:111], v225\n\t" "v_add_f32 v53, v9, v11\n\t" "v_smfmac_f32_16x16x64_bf16 v[16:19], v[40:43], v[112:119], v225\n\t"
                         "v_cvt_pk_bf16_f32 v56, v53, v52\n\t" "v_smfmac_f32_16x16x64_bf16 v[16:19], v[44:47], v[120:127], v225\n\t" "v_add_f32 v52, v12, v14\n\t"
                         "v_smfmac_f32_16x16x64_bf16 v[16:19], v[48:51], v[128:135], v225\n\t" "v_add_f32 v53, v13, v15\n\t" "v_smfmac_f32_16x16x64_bf16 v[20:23], v[40:43], v[136:143], v225\n\t"
                         "v_cvt_pk_bf16_f32 v57, v53, v52\n\t" "v_smfmac_f32_16x16x64_bf16 v[20:23], v[44:47], v[144:151], v225\n\t" "v_add_f32 v52, v16, v18\n\t"
                         "v_smfmac_f32_16x16x64_bf16 v[20:23], v[48:51], v[152:159], v225\n\t" "v_add_f32 v53, v17, v19\n\t" "v_smfmac_f32_16x16x64_bf16 v[24:27], v[40:43], v[160:167], v225\n\t"
                         "v_cvt_pk_bf16_f32 v58, v53, v52\n\t" "v_smfmac_f32_16x16x64_bf16 v[24:27], v[44:47], v[168:175], v225\n\t" "v_add_f32 v52, v20, v22\n\t" "v_add_f32 v53, v21, v23\n\t"
                         "v_smfmac_f32_16x16x64_bf16 v[24:27], v[48:51], v[176:183], v225\n\t" "v_cvt_pk_bf16_f32 v59, v53, v52\n\t" "v_permlane32_swap_b32 v56, v58\n\t"
                         "v_smfmac_f32_16x16x64_bf16 v[28:31], v[40:43], v[184:191], v225\n\t" "s_nop 0\n\t" "v_permlane32_swap_b32 v57, v59\n\t" "v_smfmac_f32_16x16x64_bf16 v[28:31], v[44:47], v[192:199], v225\n\t"
                         "s_nop 0\n\t" "v_permlane16_swap_b32 v56, v57\n\t" "v_permlane16_swap_b32 v58, v59\n\t" "v_smfmac_f32_16x16x64_bf16 v[28:31], v[48:51], v[200:207], v225\n\t"
                         "global_store_dwordx4 v231, v[56:59], %2 sc1\n\t" "s_add_u32 s40, s40, 0x20000\n\t" "s_addc_u32 s41, s41, 0\n\t" "v_add_f32 v52, v24, v26\n\t"
                         "v_smfmac_f32_16x16x64_bf16 v[32:35], v[40:43], v[208:215], v225\n\t" "v_add_f32 v53, v25, v27\n\t" "v_smfmac_f32_16x16x64_bf16 v[32:35], v[44:47], v[216:223], v225\n\t"
                         "v_cvt_pk_bf16_f32 v60, v53, v52\n\t" "v_add_f32 v52, v28, v30\n\t" "v_smfmac_f32_16x16x64_bf16 v[32:35], v[48:51], v[64:71], v225\n\t" "v_add_f32 v53, v29, v31\n\t"
                         "v_smfmac_f32_16x16x64_bf16 v[36:39], v[40:43], v[72:79], v225\n\t" "v_cvt_pk_bf16_f32 v61, v53, v52\n\t" "v_smfmac_f32_16x16x64_bf16 v[36:39], v[44:47], v[80:87], v225\n\t"
                         "v_add_f32 v52, v32, v34\n\t" "v_smfmac_f32_16x16x64_bf16 v[36:39], v[48:51], v[88:95], v225\n\t" "v_add_f32 v53, v33, v35\n\t" "v_cvt_pk_bf16_f32 v62, v53, v52\n\t" "v_mov_b32 v233, 0\n\t"
                         "v_mov_b32 v233, 0\n\t" "v_mov_b32 v233, 0\n\t" "v_mov_b32 v233, 0\n\t" "v_mov_b32 v233, 0\n\t" "v_pk_add_f32 v[52:53], v[36:37], v[38:39]\n\t" "v_cvt_pk_bf16_f32 v63, v53, v52\n\t"
                         "v_permlane32_swap_b32 v60, v62\n\t" "s_nop 0\n\t" "v_permlane32_swap_b32 v61, v63\n\t" "s_nop 1\n\t" "v_permlane16_swap_b32 v60, v61\n\t" "v_permlane16_swap_b32 v62, v63\n\t"
                         "global_store_dwordx4 v231, v[60:63], %2 sc1\n\t" ".Lsk_u:\n\t" "s_memrealtime %1\n\t" "s_mov_b64 %0, vcc\n\t" "s_waitcnt lgkmcnt(0)\n\t"
                         : "=&s"(bad), "=&s"(tp8), "+{s[40:41]}"(up)
                         : "s"(uni((int)tgt)), "s"(memp), "s"(memp + 16384), "s"(en), "{v[224:239]}"(ad),
                           "{a[0:31]}"(wa[0]), "{a[32:63]}"(wa[1]), "{a[64:95]}"(wa[2]), "{a[96:127]}"(wa[3]), "{a[128:159]}"(wa[4]), "{a[160:191]}"(wa[5]), "{a[192:223]}"(wa[6]), "{a[224:255]}"(wa[7]), "{v[96:127]}"(wv[0]), "{v[128:159]}"(wv[1]), "{v[160:191]}"(wv[2]), "{v[192:223]}"(wv[3])
                         : "v0", "v1", "v2", "v3", "v4", "v5", "v6", "v7", "v8", "v9", "v10", "v11", "v12", "v13", "v14", "v15", "v16", "v17", "v18", "v19", "v20", "v21", "v22", "v23", "v24", "v25", "v26", "v27", "v28", "v29", "v30", "v31", "v32", "v33", "v34", "v35", "v36", "v37", "v38", "v39", "v40", "v41", "v42", "v43", "v44", "v45", "v46", "v47", "v48", "v49", "v50", "v51", "v52", "v53", "v54", "v55", "v56", "v57", "v58", "v59", "v60", "v61", "v62", "v63", "v64", "v65", "v66", "v67", "v68", "v69", "v70", "v71", "v72", "v73", "v74", "v75", "v76", "v77", "v78", "v79", "v80", "v81", "v82", "v83", "v84", "v85", "v86", "v87", "v88", "v89", "v90", "v91", "v92", "v93", "v94", "v95", "vcc", "scc", "s42", "memory");
            if (__builtin_expect(bad == 0, 1)) break;
            waitvm<0>();
            if (--budget < 0 || ((++tries & 15) == 0 && !unwritten(memp, ad[0]))) en = 0;
            phi += (u32)cfg.G;
        }
        clk.calib();
        const u32 tt = sched_ld0(s);
        if (s >= NSEG - 2) waitvm<0>();
        barrier();
        if (s >= NSEG - 2 && w == 3) flag_add(fl, F_PROG + 8 * s + (h & 7), 1);
        if (s == NSEG - 1 && w == 2) flag_add(fl, F_UD + (h & 1), 1);
        follow(phi, (int)(tt >> 16) + 4 + cfg.T3 - VISS, (u32)tp8, cfg.G);
        phi += tt & 0xffffu;
        memp = p.ws + mslot(sd, s + 1);
    }
    asm volatile(".Lkm_lu:\n\ts_waitcnt vmcnt(0)" : : : "memory");
}

#define VALL "{v[0:3]}"(o), "{v[16:47]}"(rv[0]), "{v[48:79]}"(rv[1]), "{v[80:111]}"(rv[2]), "{v[112:143]}"(rv[3]), "{v[144:175]}"(rv[4]), "{v[176:207]}"(rv[5]), "{v[208:239]}"(rv[6]), "{a[0:31]}"(ra[0]), "{a[32:63]}"(ra[1]), "{a[64:95]}"(ra[2]), "{a[96:127]}"(ra[3])
DI void value_role(const Ptrs& p, const ChainCfg cfg, int vb, int sd, u32 phi, u32 lds, int w) {
    const int lane = lanex(), tid = 64 * w + lane, n = lane & 15, g = lane >> 4;
    const int v = 2 * (vb & 3) + ((vb >> 2) & 1) + (vb & ~7);
    const u32 moff = lane * 16;
    char* const fl = p.ws + OFF_ST;
    char* const ub = p.ws + (size_t)v * 8192;
    if (w == 3) {
        const u32x4 ff = u32x4{~0u, ~0u, ~0u, ~0u};
        barrier();
#pragma nounroll
        for (int s = 1; s < NSEG - 1; ++s) {
            nap(cfg.cR3 >> 8);
            char* const mk = ub + uslot(sd, s + 1);
#pragma unroll
            for (int i = 0; i < 8; ++i) { gst16(mk + i * 1024, moff, ff); if (HQ) asm volatile("s_nop %0" : : "n"(HQ)); }
            if (vb < 32 && s < NSEG - 2) {
                gst16(p.ws + (size_t)(s + 2) * HO_STRIDE + vb * 2048, moff, ff);
                gst16(p.ws + (size_t)(s + 2) * HO_STRIDE + vb * 2048 + 1024, moff, ff);
            }
            waitvm<0>();
            barrier();
        }
        return;
    }
    if (w) {
        const u32 dof = (u32)((n >> 3) * 16384 + g * 128 + (n & 7) * 16);
        const char* dp = p.ws + OFF_D + (size_t)v * 32768 - 4096 + w * 8192;
        flag_wait(fl, F_VD + (vb & 3) + (v < 20 ? 0 : 4), 32);
#pragma nounroll
        for (int s = 0; s < NSEG - 1; ++s) {
            u32x4 df[16];
            u32 dfl;
            if (s > 1) nap(cfg.cR3 & 255);
            sfor<0, 16>([&](auto I) { gld16c<I.v * 512 - 4096>(df[I.v], dp, dof); if (HP) asm volatile("s_nop %0" : : "n"(HP)); });
            dp += D_STRIDE;
            flag_ldm(dfl, fl, F_VD + pblk(s + 1) * 8 + (vb & 7), pblk(s + 1) != pblk(s));
            const u32 la = lds + moff + (s & 1) * 32768 + (w - 1) * 16384;
            sfor<0, 8>([&](auto I) {
                constexpr int i = I.v;
                waitv<15 - 2 * i>(df[2 * i]); waitv<15 - 2 * i>(df[2 * i + 1]);
                const u32x4 x = df[2 * i], y = df[2 * i + 1];
                *LDSP(u32x4, la + 2048 * i) = u32x4{x[0], y[0], x[2], y[2]};
                *LDSP(u32x4, la + 2048 * i + 1024) = u32x4{x[1], y[1], x[3], y[3]};
                PIN();
            });
            waitvm<0>();
            asm volatile("" : "+v"(dfl));
            barrier();
            if (uni((int)dfl) < 31) flag_wait(fl, F_VD + pblk(s + 1) * 8 + (vb & 7), 32);
        }
        return;
    }
    u32x32 rv[7], ra[4];
    u32x4 o;
    asm volatile("v_accvgpr_write_b32 a64, 0\n\t" "v_accvgpr_write_b32 a65, 0\n\t" "v_accvgpr_write_b32 a66, 0\n\t" "v_accvgpr_write_b32 a67, 0\n\t" "v_accvgpr_write_b32 a68, 0\n\t" "v_accvgpr_write_b32 a69, 0\n\t"
                 "v_accvgpr_write_b32 a70, 0\n\t" "v_accvgpr_write_b32 a71, 0\n\t" "v_accvgpr_write_b32 a72, 0\n\t" "v_accvgpr_write_b32 a73, 0\n\t" "v_accvgpr_write_b32 a74, 0\n\t" "v_accvgpr_write_b32 a75, 0\n\t"
                 "v_accvgpr_write_b32 a76, 0\n\t" "v_accvgpr_write_b32 a77, 0\n\t" "v_accvgpr_write_b32 a78, 0\n\t" "v_accvgpr_write_b32 a79, 0\n\t" "v_mov_b64 v[112:113], -1\n\t" "v_mov_b64 v[120:121], -1\n\t"
                 "v_mov_b64 v[128:129], -1\n\t" "v_mov_b64 v[136:137], -1\n\t" "v_mov_b64 v[144:145], -1\n\t" "v_mov_b64 v[152:153], -1\n\t" "v_mov_b64 v[160:161], -1\n\t" "v_mov_b64 v[168:169], -1\n\t"
                 "v_mov_b64 v[176:177], -1\n\t" "v_mov_b64 v[184:185], -1\n\t" "v_mov_b64 v[192:193], -1\n\t" "v_mov_b64 v[200:201], -1\n\t" "v_mov_b64 v[208:209], -1\n\t" "v_mov_b64 v[216:217], -1\n\t"
                 "v_mov_b64 v[224:225], -1\n\t" "v_mov_b64 v[232:233], -1\n\t"
                 : "={v[0:3]}"(o), "={v[16:47]}"(rv[0]), "={v[48:79]}"(rv[1]), "={v[80:111]}"(rv[2]), "={v[112:143]}"(rv[3]), "={v[144:175]}"(rv[4]), "={v[176:207]}"(rv[5]), "={v[208:239]}"(rv[6]), "={a[0:31]}"(ra[0]), "={a[32:63]}"(ra[1]), "={a[64:95]}"(ra[2]), "={a[96:127]}"(ra[3]));
    const u32 ho = (u32)(min(lane, 39) * 256 + (lane >> 5) * 8192);
    u32x8 ad = u32x8{(u32)((n >> 3) * 4096 + g * 128 + (n & 7) * 16), (u32)((n & 4) * 128 + (n >> 3) * 256 + g * 64 + (n & 3) * 16), (u32)((g >> 1) * 256 + (n >> 2) * 64 + (n & 1) * 16), moff,
                     (lane & 2) ? 0xEEEEu : 0x4444u, (lane & 4) ? 0xEEEEu : 0x4444u, (u32)(v * 256 + (v >> 5) * 8192 + g * 64 + (n >> 3) * 32 + (n & 1) * 16 + (n & 6) * 2),
                     (lane & 1) ? 0x07060302u : 0x01000504u};
    asm volatile("" : "+{v[4:11]}"(ad));
    auto zero = [&]() {
        asm volatile("v_mov_b64 v[0:1], 0\n\t" "v_mov_b64 v[2:3], 0\n\t" "v_mov_b64 v[48:49], 0\n\t" "v_mov_b64 v[50:51], 0\n\t" "v_mov_b64 v[52:53], 0\n\t" "v_mov_b64 v[54:55], 0\n\t" "v_mov_b64 v[56:57], 0\n\t"
                     "v_mov_b64 v[58:59], 0\n\t" "v_mov_b64 v[60:61], 0\n\t" "v_mov_b64 v[62:63], 0\n\t" "v_mov_b64 v[64:65], 0\n\t" "v_mov_b64 v[66:67], 0\n\t" "v_mov_b64 v[68:69], 0\n\t" "v_mov_b64 v[70:71], 0\n\t"
                     "v_mov_b64 v[72:73], 0\n\t" "v_mov_b64 v[74:75], 0\n\t" "v_mov_b64 v[76:77], 0\n\t" "v_mov_b64 v[78:79], 0\n\t" "v_mov_b64 v[80:81], 0\n\t" "v_mov_b64 v[82:83], 0\n\t" "v_mov_b64 v[84:85], 0\n\t"
                     "v_mov_b64 v[86:87], 0\n\t" "v_mov_b64 v[88:89], 0\n\t" "v_mov_b64 v[90:91], 0\n\t" "v_mov_b64 v[92:93], 0\n\t" "v_mov_b64 v[94:95], 0\n\t" "v_mov_b64 v[96:97], 0\n\t" "v_mov_b64 v[98:99], 0\n\t"
                     "v_mov_b64 v[100:101], 0\n\t" "v_mov_b64 v[102:103], 0\n\t" "v_mov_b64 v[104:105], 0\n\t" "v_mov_b64 v[106:107], 0\n\t" "v_mov_b64 v[108:109], 0\n\t" "v_mov_b64 v[110:111], 0\n\t"
                     : : VALL : "memory");
    };
    const char* memp = p.ws + OFF_XF + 4096;
    char* up = ub + OFF_XF;
    int budget = 30000;
    Clock clk; clk.init();
    u32 tt = 0;
    for (int s = -1; s < NSEG - 1; ++s) {
        const char* const mnx = p.ws + mslot(sd, s + 1);
        bool all = false;
        int tries = 0;
        u64 tp8;
        while (true) {
            const u32 tgt = clk.sched(phi, (int)(tt >> 16), cfg.G) - (s < 0 ? 1u << 20 : 0u);
            u64 ok;
            asm volatile("s_cmp_lg_u32 %11, 0\n\t" "s_cbranch_scc0 .Lsp_v0\n\t" ".Lsp_v3:\n\t" "s_memtime vcc\n\t" "s_waitcnt lgkmcnt(0)\n\t" "s_sub_i32 s42, %10, vcc_lo\n\t" "s_cmp_gt_i32 s42, 80\n\t"
                         "s_cbranch_scc1 .Lsp_v3\n\t" "s_max_i32 s42, s42, 0\n\t" "s_sub_i32 s42, 92, s42\n\t" "s_and_b32 s42, s42, -4\n\t" "s_getpc_b64 vcc\n\t" "s_add_u32 vcc_lo, vcc_lo, s42\n\t"
                         "s_addc_u32 vcc_hi, vcc_hi, 0\n\t" "s_setpc_b64 vcc\n\t" "s_nop 0\n\t" "s_nop 0\n\t" "s_nop 0\n\t" "s_nop 0\n\t" "s_nop 0\n\t" "s_nop 0\n\t" "s_nop 0\n\t" "s_nop 0\n\t" "s_nop 0\n\t" "s_nop 0\n\t"
                         "s_nop 0\n\t" "s_nop 0\n\t" "s_nop 0\n\t" "s_nop 0\n\t" "s_nop 0\n\t" "s_nop 0\n\t" "s_nop 0\n\t" "s_nop 0\n\t" "s_nop 0\n\t" "s_nop 0\n\t"
                         "global_load_dwordx4 a[0:3], v5, %7 offset:-4096 sc1\n\t" "global_load_dwordx4 a[4:7], v5, %7 offset:-3072 sc1\n\t" "global_load_dwordx4 a[8:11], v5, %7 offset:-2048 sc1\n\t"
                         "global_load_dwordx4 a[12:15], v5, %7 offset:-1024 sc1\n\t" "global_load_dwordx4 a[16:19], v5, %7 sc1\n\t" "global_load_dwordx4 a[20:23], v5, %7 offset:1024 sc1\n\t"
                         "global_load_dwordx4 a[24:27], v5, %7 offset:2048 sc1\n\t" "global_load_dwordx4 a[28:31], v5, %7 offset:3072 sc1\n\t" "global_load_dwordx4 a[32:35], v5, %8 offset:-4096 sc1\n\t"
                         "global_load_dwordx4 a[36:39], v5, %8 offset:-3072 sc1\n\t" "global_load_dwordx4 a[40:43], v5, %8 offset:-2048 sc1\n\t" "global_load_dwordx4 a[44:47], v5, %8 offset:-1024 sc1\n\t"
                         "global_load_dwordx4 a[48:51], v5, %8 sc1\n\t" "global_load_dwordx4 a[52:55], v5, %8 offset:1024 sc1\n\t" "global_load_dwordx4 a[56:59], v5, %8 offset:2048 sc1\n\t"
                         "global_load_dwordx4 a[60:63], v5, %8 offset:3072 sc1\n\t" "s_nop 15\n\t" "s_nop 10\n\t" "s_mov_b64 exec, %9\n\t" "global_load_dwordx4 a[64:67], v6, %6 offset:-2048 sc1\n\t"
                         "global_load_dwordx4 a[68:71], v6, %6 offset:-1536 sc1\n\t" "global_load_dwordx4 a[72:75], v6, %6 offset:-1024 sc1\n\t" "global_load_dwordx4 a[76:79], v6, %6 offset:-512 sc1\n\t"
                         "s_mov_b64 exec, -1\n\t" "global_load_dwordx4 a[80:83], v7, %6 sc1\n\t" "global_load_dwordx4 a[84:87], v7, %6 offset:1024 sc1\n\t" "global_load_dwordx4 a[88:91], v7, %6 offset:2048 sc1\n\t"
                         "global_load_dwordx4 a[92:95], v7, %6 offset:3072 sc1\n\t" "s_mov_b32 s42, %11\n\t" ".Lsp_v5:\n\t" "s_sub_u32 s42, s42, 1\n\t" "s_cmp_gt_i32 s42, 0\n\t" "s_cbranch_scc1 .Lsp_v5\n\t"
                         "global_load_dwordx4 a[96:99], v4, %3 sc1\n\t" "global_load_dwordx4 a[100:103], v4, %3 offset:512 sc1\n\t" "global_load_dwordx4 a[104:107], v4, %3 offset:1024 sc1\n\t"
                         "global_load_dwordx4 a[108:111], v4, %3 offset:1536 sc1\n\t" "global_load_dwordx4 a[112:115], v4, %3 offset:2048 sc1\n\t" "global_load_dwordx4 a[116:119], v4, %3 offset:2560 sc1\n\t"
                         "global_load_dwordx4 a[120:123], v4, %3 offset:3072 sc1\n\t" "global_load_dwordx4 a[124:127], v4, %3 offset:3584 sc1\n\t" "s_waitcnt vmcnt(31)\n\t"
                         "v_smfmac_f32_16x16x64_bf16 v[48:51], a[0:3], v[112:119], v9\n\t" "s_waitcnt vmcnt(30)\n\t" "v_smfmac_f32_16x16x64_bf16 v[52:55], a[4:7], v[120:127], v9\n\t" "s_waitcnt vmcnt(29)\n\t"
                         "v_smfmac_f32_16x16x64_bf16 v[56:59], a[8:11], v[128:135], v9\n\t" "s_waitcnt vmcnt(28)\n\t" "v_cvt_pk_bf16_f32 v16, v48, v49\n\t" "v_cvt_pk_bf16_f32 v17, v50, v51\n\t"
                         "v_smfmac_f32_16x16x64_bf16 v[60:63], a[12:15], v[136:143], v9\n\t" "s_waitcnt vmcnt(27)\n\t" "v_cvt_pk_bf16_f32 v18, v52, v53\n\t" "v_cvt_pk_bf16_f32 v19, v54, v55\n\t"
                         "v_smfmac_f32_16x16x64_bf16 v[64:67], a[16:19], v[144:151], v9\n\t" "s_waitcnt vmcnt(26)\n\t" "v_cvt_pk_bf16_f32 v20, v56, v57\n\t" "v_cvt_pk_bf16_f32 v21, v58, v59\n\t"
                         "v_smfmac_f32_16x16x64_bf16 v[68:71], a[20:23], v[152:159], v9\n\t" "s_waitcnt vmcnt(25)\n\t" "v_cvt_pk_bf16_f32 v22, v60, v61\n\t" "v_cvt_pk_bf16_f32 v23, v62, v63\n\t"
                         "v_smfmac_f32_16x16x64_bf16 v[72:75], a[24:27], v[160:167], v9\n\t" "s_waitcnt vmcnt(24)\n\t" "v_cvt_pk_bf16_f32 v24, v64, v65\n\t" "v_cvt_pk_bf16_f32 v25, v66, v67\n\t"
                         "v_smfmac_f32_16x16x64_bf16 v[76:79], a[28:31], v[168:175], v9\n\t" "s_waitcnt vmcnt(23)\n\t" "v_cvt_pk_bf16_f32 v26, v68, v69\n\t" "v_cvt_pk_bf16_f32 v27, v70, v71\n\t"
                         "v_smfmac_f32_16x16x64_bf16 v[80:83], a[32:35], v[176:183], v9\n\t" "s_waitcnt vmcnt(22)\n\t" "v_cvt_pk_bf16_f32 v28, v72, v73\n\t" "v_cvt_pk_bf16_f32 v29, v74, v75\n\t"
                         "v_smfmac_f32_16x16x64_bf16 v[84:87], a[36:39], v[184:191], v9\n\t" "s_waitcnt vmcnt(21)\n\t" "v_cvt_pk_bf16_f32 v30, v76, v77\n\t" "v_cvt_pk_bf16_f32 v31, v78, v79\n\t"
                         "v_smfmac_f32_16x16x64_bf16 v[88:91], a[40:43], v[192:199], v9\n\t" "s_waitcnt vmcnt(20)\n\t" "v_cvt_pk_bf16_f32 v32, v80, v81\n\t" "v_cvt_pk_bf16_f32 v33, v82, v83\n\t"
                         "v_smfmac_f32_16x16x64_bf16 v[92:95], a[44:47], v[200:207], v9\n\t" "s_waitcnt vmcnt(19)\n\t" "v_cvt_pk_bf16_f32 v34, v84, v85\n\t" "v_cvt_pk_bf16_f32 v35, v86, v87\n\t"
                         "v_smfmac_f32_16x16x64_bf16 v[96:99], a[48:51], v[208:215], v9\n\t" "s_waitcnt vmcnt(18)\n\t" "v_cvt_pk_bf16_f32 v36, v88, v89\n\t" "v_cvt_pk_bf16_f32 v37, v90, v91\n\t"
                         "v_smfmac_f32_16x16x64_bf16 v[100:103], a[52:55], v[216:223], v9\n\t" "s_waitcnt vmcnt(17)\n\t" "v_cvt_pk_bf16_f32 v38, v92, v93\n\t" "v_cvt_pk_bf16_f32 v39, v94, v95\n\t"
                         "v_smfmac_f32_16x16x64_bf16 v[104:107], a[56:59], v[224:231], v9\n\t" "s_waitcnt vmcnt(16)\n\t" "v_cvt_pk_bf16_f32 v40, v96, v97\n\t" "v_cvt_pk_bf16_f32 v41, v98, v99\n\t"
                         "v_smfmac_f32_16x16x64_bf16 v[108:111], a[60:63], v[232:239], v9\n\t" "s_waitcnt vmcnt(15)\n\t" "v_cvt_pk_bf16_f32 v42, v100, v101\n\t" "v_cvt_pk_bf16_f32 v43, v102, v103\n\t"
                         "v_smfmac_f32_16x16x64_bf16 v[0:3], a[64:67], v[16:23], v8\n\t" "s_waitcnt vmcnt(14)\n\t" "v_cvt_pk_bf16_f32 v44, v104, v105\n\t" "v_cvt_pk_bf16_f32 v45, v106, v107\n\t"
                         "v_smfmac_f32_16x16x64_bf16 v[0:3], a[68:71], v[24:31], v8\n\t" "s_waitcnt vmcnt(13)\n\t" "v_cvt_pk_bf16_f32 v46, v108, v109\n\t" "v_cvt_pk_bf16_f32 v47, v110, v111\n\t"
                         "v_smfmac_f32_16x16x64_bf16 v[0:3], a[72:75], v[32:39], v8\n\t" "s_waitcnt vmcnt(12)\n\t" "v_smfmac_f32_16x16x64_bf16 v[0:3], a[76:79], v[40:47], v8\n\t" "s_waitcnt vmcnt(6)\n\t"
                         "v_smfmac_f32_16x16x64_bf16 v[0:3], a[80:83], a[96:103], v8\n\t" "s_waitcnt vmcnt(4)\n\t" "v_smfmac_f32_16x16x64_bf16 v[0:3], a[84:87], a[104:111], v8\n\t" "s_waitcnt vmcnt(2)\n\t"
                         "v_smfmac_f32_16x16x64_bf16 v[0:3], a[88:91], a[112:119], v8\n\t" "s_waitcnt vmcnt(0)\n\t" "v_smfmac_f32_16x16x64_bf16 v[0:3], a[92:95], a[120:127], v8\n\t" "s_branch .Lv_end\n\t" ".Lsp_v0:\n\t"
                         "s_memtime vcc\n\t" "s_waitcnt lgkmcnt(0)\n\t" "s_sub_i32 s42, %10, vcc_lo\n\t" "s_cmp_gt_i32 s42, 80\n\t" "s_cbranch_scc1 .Lsp_v0\n\t" "s_max_i32 s42, s42, 0\n\t" "s_sub_i32 s42, 92, s42\n\t"
                         "s_and_b32 s42, s42, -4\n\t" "s_getpc_b64 vcc\n\t" "s_add_u32 vcc_lo, vcc_lo, s42\n\t" "s_addc_u32 vcc_hi, vcc_hi, 0\n\t" "s_setpc_b64 vcc\n\t" "s_nop 0\n\t" "s_nop 0\n\t" "s_nop 0\n\t" "s_nop 0\n\t"
                         "s_nop 0\n\t" "s_nop 0\n\t" "s_nop 0\n\t" "s_nop 0\n\t" "s_nop 0\n\t" "s_nop 0\n\t" "s_nop 0\n\t" "s_nop 0\n\t" "s_nop 0\n\t" "s_nop 0\n\t" "s_nop 0\n\t" "s_nop 0\n\t" "s_nop 0\n\t" "s_nop 0\n\t"
                         "s_nop 0\n\t" "s_nop 0\n\t" "global_load_dwordx4 a[96:99], v4, %3 sc1\n\t" "global_load_dwordx4 a[100:103], v4, %3 offset:512 sc1\n\t"
                         "global_load_dwordx4 a[104:107], v4, %3 offset:1024 sc1\n\t" "global_load_dwordx4 a[108:111], v4, %3 offset:1536 sc1\n\t" "global_load_dwordx4 a[112:115], v4, %3 offset:2048 sc1\n\t"
                         "global_load_dwordx4 a[116:119], v4, %3 offset:2560 sc1\n\t" "global_load_dwordx4 a[120:123], v4, %3 offset:3072 sc1\n\t" "global_load_dwordx4 a[124:127], v4, %3 offset:3584 sc1\n\t" "s_nop 15\n\t"
                         "s_nop 6\n\t" "global_load_dwordx4 a[0:3], v5, %7 offset:-4096 sc1\n\t" "global_load_dwordx4 a[4:7], v5, %7 offset:-3072 sc1\n\t" "global_load_dwordx4 a[8:11], v5, %7 offset:-2048 sc1\n\t"
                         "global_load_dwordx4 a[12:15], v5, %7 offset:-1024 sc1\n\t" "global_load_dwordx4 a[16:19], v5, %7 sc1\n\t" "global_load_dwordx4 a[20:23], v5, %7 offset:1024 sc1\n\t"
                         "global_load_dwordx4 a[24:27], v5, %7 offset:2048 sc1\n\t" "global_load_dwordx4 a[28:31], v5, %7 offset:3072 sc1\n\t" "global_load_dwordx4 a[32:35], v5, %8 offset:-4096 sc1\n\t"
                         "global_load_dwordx4 a[36:39], v5, %8 offset:-3072 sc1\n\t" "global_load_dwordx4 a[40:43], v5, %8 offset:-2048 sc1\n\t" "global_load_dwordx4 a[44:47], v5, %8 offset:-1024 sc1\n\t"
                         "global_load_dwordx4 a[48:51], v5, %8 sc1\n\t" "global_load_dwordx4 a[52:55], v5, %8 offset:1024 sc1\n\t" "global_load_dwordx4 a[56:59], v5, %8 offset:2048 sc1\n\t"
                         "global_load_dwordx4 a[60:63], v5, %8 offset:3072 sc1\n\t" "s_nop 15\n\t" "s_nop 10\n\t" "global_load_dwordx4 a[80:83], v7, %6 sc1\n\t" "global_load_dwordx4 a[84:87], v7, %6 offset:1024 sc1\n\t"
                         "global_load_dwordx4 a[88:91], v7, %6 offset:2048 sc1\n\t" "global_load_dwordx4 a[92:95], v7, %6 offset:3072 sc1\n\t" "s_mov_b64 exec, %9\n\t"
                         "global_load_dwordx4 a[64:67], v6, %6 offset:-2048 sc1\n\t" "global_load_dwordx4 a[68:71], v6, %6 offset:-1536 sc1\n\t" "global_load_dwordx4 a[72:75], v6, %6 offset:-1024 sc1\n\t"
                         "global_load_dwordx4 a[76:79], v6, %6 offset:-512 sc1\n\t" "s_mov_b64 exec, -1\n\t" "s_waitcnt vmcnt(23)\n\t" "v_smfmac_f32_16x16x64_bf16 v[48:51], a[0:3], v[112:119], v9\n\t"
                         "s_waitcnt vmcnt(22)\n\t" "v_smfmac_f32_16x16x64_bf16 v[52:55], a[4:7], v[120:127], v9\n\t" "s_waitcnt vmcnt(21)\n\t" "v_smfmac_f32_16x16x64_bf16 v[56:59], a[8:11], v[128:135], v9\n\t"
                         "s_waitcnt vmcnt(20)\n\t" "v_cvt_pk_bf16_f32 v16, v48, v49\n\t" "v_cvt_pk_bf16_f32 v17, v50, v51\n\t" "v_smfmac_f32_16x16x64_bf16 v[60:63], a[12:15], v[136:143], v9\n\t" "s_waitcnt vmcnt(19)\n\t"
                         "v_cvt_pk_bf16_f32 v18, v52, v53\n\t" "v_cvt_pk_bf16_f32 v19, v54, v55\n\t" "v_smfmac_f32_16x16x64_bf16 v[64:67], a[16:19], v[144:151], v9\n\t" "s_waitcnt vmcnt(18)\n\t"
                         "v_cvt_pk_bf16_f32 v20, v56, v57\n\t" "v_cvt_pk_bf16_f32 v21, v58, v59\n\t" "v_smfmac_f32_16x16x64_bf16 v[68:71], a[20:23], v[152:159], v9\n\t" "s_waitcnt vmcnt(17)\n\t"
                         "v_cvt_pk_bf16_f32 v22, v60, v61\n\t" "v_cvt_pk_bf16_f32 v23, v62, v63\n\t" "v_smfmac_f32_16x16x64_bf16 v[72:75], a[24:27], v[160:167], v9\n\t" "s_waitcnt vmcnt(16)\n\t"
                         "v_cvt_pk_bf16_f32 v24, v64, v65\n\t" "v_cvt_pk_bf16_f32 v25, v66, v67\n\t" "v_smfmac_f32_16x16x64_bf16 v[76:79], a[28:31], v[168:175], v9\n\t" "s_waitcnt vmcnt(15)\n\t"
                         "v_cvt_pk_bf16_f32 v26, v68, v69\n\t" "v_cvt_pk_bf16_f32 v27, v70, v71\n\t" "v_smfmac_f32_16x16x64_bf16 v[80:83], a[32:35], v[176:183], v9\n\t" "s_waitcnt vmcnt(14)\n\t"
                         "v_cvt_pk_bf16_f32 v28, v72, v73\n\t" "v_cvt_pk_bf16_f32 v29, v74, v75\n\t" "v_smfmac_f32_16x16x64_bf16 v[84:87], a[36:39], v[184:191], v9\n\t" "s_waitcnt vmcnt(13)\n\t"
                         "v_cvt_pk_bf16_f32 v30, v76, v77\n\t" "v_cvt_pk_bf16_f32 v31, v78, v79\n\t" "v_smfmac_f32_16x16x64_bf16 v[88:91], a[40:43], v[192:199], v9\n\t" "s_waitcnt vmcnt(12)\n\t"
                         "v_cvt_pk_bf16_f32 v32, v80, v81\n\t" "v_cvt_pk_bf16_f32 v33, v82, v83\n\t" "v_smfmac_f32_16x16x64_bf16 v[92:95], a[44:47], v[200:207], v9\n\t" "s_waitcnt vmcnt(11)\n\t"
                         "v_cvt_pk_bf16_f32 v34, v84, v85\n\t" "v_cvt_pk_bf16_f32 v35, v86, v87\n\t" "v_smfmac_f32_16x16x64_bf16 v[96:99], a[48:51], v[208:215], v9\n\t" "s_waitcnt vmcnt(10)\n\t"
                         "v_cvt_pk_bf16_f32 v36, v88, v89\n\t" "v_cvt_pk_bf16_f32 v37, v90, v91\n\t" "v_smfmac_f32_16x16x64_bf16 v[100:103], a[52:55], v[216:223], v9\n\t" "s_waitcnt vmcnt(9)\n\t"
                         "v_cvt_pk_bf16_f32 v38, v92, v93\n\t" "v_cvt_pk_bf16_f32 v39, v94, v95\n\t" "v_smfmac_f32_16x16x64_bf16 v[104:107], a[56:59], v[224:231], v9\n\t" "s_waitcnt vmcnt(8)\n\t"
                         "v_cvt_pk_bf16_f32 v40, v96, v97\n\t" "v_smfmac_f32_16x16x64_bf16 v[108:111], a[60:63], v[232:239], v9\n\t" "s_waitcnt vmcnt(7)\n\t" "v_cvt_pk_bf16_f32 v41, v98, v99\n\t"
                         "v_smfmac_f32_16x16x64_bf16 v[0:3], a[80:83], a[96:103], v8\n\t" "s_waitcnt vmcnt(6)\n\t" "v_cvt_pk_bf16_f32 v42, v100, v101\n\t"
                         "v_smfmac_f32_16x16x64_bf16 v[0:3], a[84:87], a[104:111], v8\n\t" "s_waitcnt vmcnt(5)\n\t" "v_cvt_pk_bf16_f32 v43, v102, v103\n\t" "v_smfmac_f32_16x16x64_bf16 v[0:3], a[88:91], a[112:119], v8\n\t"
                         "s_waitcnt vmcnt(4)\n\t" "v_cvt_pk_bf16_f32 v44, v104, v105\n\t" "v_smfmac_f32_16x16x64_bf16 v[0:3], a[92:95], a[120:127], v8\n\t" "s_waitcnt vmcnt(3)\n\t"
                         "v_cvt_pk_bf16_f32 v45, v106, v107\n\t" "v_smfmac_f32_16x16x64_bf16 v[0:3], a[64:67], v[16:23], v8\n\t" "s_waitcnt vmcnt(2)\n\t" "v_cvt_pk_bf16_f32 v46, v108, v109\n\t"
                         "v_smfmac_f32_16x16x64_bf16 v[0:3], a[68:71], v[24:31], v8\n\t" "s_waitcnt vmcnt(1)\n\t" "v_cvt_pk_bf16_f32 v47, v110, v111\n\t" "v_smfmac_f32_16x16x64_bf16 v[0:3], a[72:75], v[32:39], v8\n\t"
                         "s_waitcnt vmcnt(0)\n\t" "v_smfmac_f32_16x16x64_bf16 v[0:3], a[76:79], v[40:47], v8\n\t" ".Lv_end:\n\t" "s_nop 7\n\t" "v_pk_add_f32 v[0:1], v[0:1], v[2:3]\n\t" "v_cvt_pk_bf16_f32 v2, v0, v1\n\t"
                         "v_cmp_u_f32 vcc, v0, v1\n\t" "s_cmp_eq_u64 vcc, 0\n\t" "v_mov_b32_dpp v3, v2 quad_perm:[1,0,3,2] row_mask:0xf bank_mask:0xf\n\t" "s_cselect_b64 %0, -1, 0\n\t" "v_perm_b32 v0, v2, v3, v11\n\t"
                         "s_mov_b64 exec, %0\n\t" "global_store_dword v10, v0, %5 offset:-4096 sc1\n\t" "global_store_dword v10, v0, %4 offset:-4096 sc1\n\t" "s_memrealtime %1\n\t" "s_mov_b64 exec, -1\n\t"
                         "s_waitcnt lgkmcnt(0)\n\t"
                         : "=&s"(ok), "=&s"(tp8)
                         : "{v[4:11]}"(ad), "s"(up), "s"(mnx), "s"((u64)mnx ^ 8192), "s"(memp + 16384), "s"(memp + 32768), "s"(memp + 49152), "s"(0xc30000c3c30000c3ull),
                           "s"(uni((int)(tgt - (u32)(v < 8 ? cfg.dU : cfg.cR2)))), "s"(v < 8 ? 0 : (v < 24 ? cfg.s2 : cfg.T3)), VALL : "memory", "vcc", "scc", "s42");
            if (__builtin_expect(ok != 0, 1) || s < 0) break;
            if (all || --budget < 0) {
                asm volatile("v_min_u32 v0, -2, v0\n\tglobal_store_dword v10, v0, %0 offset:-4096 sc1\n\tglobal_store_dword v10, v0, %1 offset:-4096 sc1"
                             : : "s"(mnx), "s"((u64)mnx ^ 8192), "{v[4:11]}"(ad), VALL : "memory");
                break;
            }
            if ((++tries & 15) == 0) {
                u32 m = 0;
#pragma nounroll
                for (int i = 0; i < 32; ++i) {
                    u32x4 t;
                    gld16c<0>(t, i < 24 ? memp + 12288 : up, i < 20 ? (4 + i + ((4 + i) & 24)) * 1024 + ad[3] : (i < 24 ? i * 512 - 8192 + ad[2] : (i & 7) * 512 + ad[0])); waitv<0>(t);
                    m = max3u(max3u(m, t[0], t[1]), t[2], t[3]);
                }
                all = __builtin_amdgcn_ballot_w64(m == 0xffffffffu) == 0;
            }
            phi += (u32)cfg.G;
            zero();
        }
        asm volatile("s_barrier\n\t" "ds_read_b128 v[112:115], %0\n\t" "ds_read_b128 v[116:119], %0 offset:1024\n\t" "ds_read_b128 v[120:123], %0 offset:2048\n\t" "ds_read_b128 v[124:127], %0 offset:3072\n\t"
                     "ds_read_b128 v[128:131], %0 offset:4096\n\t" "ds_read_b128 v[132:135], %0 offset:5120\n\t" "ds_read_b128 v[136:139], %0 offset:6144\n\t" "ds_read_b128 v[140:143], %0 offset:7168\n\t"
                     "ds_read_b128 v[144:147], %0 offset:8192\n\t" "ds_read_b128 v[148:151], %0 offset:9216\n\t" "ds_read_b128 v[152:155], %0 offset:10240\n\t" "ds_read_b128 v[156:159], %0 offset:11264\n\t"
                     "ds_read_b128 v[160:163], %0 offset:12288\n\t" "ds_read_b128 v[164:167], %0 offset:13312\n\t" "ds_read_b128 v[168:171], %0 offset:14336\n\t" "ds_read_b128 v[172:175], %0 offset:15360\n\t"
                     "ds_read_b128 v[176:179], %0 offset:16384\n\t" "ds_read_b128 v[180:183], %0 offset:17408\n\t" "ds_read_b128 v[184:187], %0 offset:18432\n\t" "ds_read_b128 v[188:191], %0 offset:19456\n\t"
                     "ds_read_b128 v[192:195], %0 offset:20480\n\t" "ds_read_b128 v[196:199], %0 offset:21504\n\t" "ds_read_b128 v[200:203], %0 offset:22528\n\t" "ds_read_b128 v[204:207], %0 offset:23552\n\t"
                     "ds_read_b128 v[208:211], %0 offset:24576\n\t" "ds_read_b128 v[212:215], %0 offset:25600\n\t" "ds_read_b128 v[216:219], %0 offset:26624\n\t" "ds_read_b128 v[220:223], %0 offset:27648\n\t"
                     "ds_read_b128 v[224:227], %0 offset:28672\n\t" "ds_read_b128 v[228:231], %0 offset:29696\n\t" "ds_read_b128 v[232:235], %0 offset:30720\n\t" "ds_read_b128 v[236:239], %0 offset:31744\n\t"
                     : : "v"(lds + ad[3] + ((s + 1) & 1) * 32768), VALL : "memory");
        if (s >= 0) {
            follow(phi, (int)(tt & 0xffffu) + 5 * (v >> 2) / 2 - VISV, (u32)tp8, cfg.G);
            if (vb == 0 && lane < 8) gst4c(fl, (F_CUR + lane) * 128, s - 1);
        }
        phi += tt & 0xffffu;
        memp = mnx; up = ub + uslot(sd, s + 1);
        zero();
        tt = sched_ld0(s + 1);
        clk.calib();
        while (s >= 0) {
            u32 h0;
            spin_to(clk.at(phi + (u32)cfg.dV));
            asm volatile("global_load_dword %0, %1, %2 offset:-4096 sc1\n\ts_waitcnt vmcnt(0)" : "=&v"(h0) : "v"(ho), "s"(memp) : "memory");
            if (__builtin_amdgcn_ballot_w64(h0 == 0xffffffffu) == 0 || --budget < 0) break;
            phi += (u32)cfg.G;
        }
        asm volatile(".Lkm_lv:");
    }
}

DI void xcopy_task(const Ptrs& p, int j, int w) {
    const int tid = 64 * w + lanex();
    Sync y;
    sync_ld(y, p.ws);
    u32x4 xv[10];
    sfor<0, 10>([&](auto I) { gld16<I.v * 128>(xv[I.v], p.x + (size_t)j * 40960, (u32)((tid & 31) * 1280 + (tid >> 5) * 16)); });
    sfor<0, 10>([&](auto I) { waitv<9 - I.v>(xv[I.v]); });
    asm volatile("" : "+v"(y.r), "+v"(y.g), "+v"(y.e));
    sfor<0, 10>([&](auto I) { gst16(p.ws + OFF_XF + (size_t)j * 40960 + I.v * 4096, (u32)tid * 16, xv[I.v]); });
    waitvm<0>();
    barrier();
    const u64 t = sync_tag(y.e);
    if (tid == 0) gst8(p.ws + OFF_LAT, 1536 + (u32)j * 8, u32x2{(u32)t, (u32)(t >> 32)});
}
DI void xcopy_wait(const char* ws, u64 t) {
#pragma nounroll
    for (int it = 0; it < (1 << 16); ++it) {
        u32x2 r;
        asm volatile("s_nop 4\n\tglobal_load_dwordx2 %0, %1, %2 offset:1536 sc1\n\ts_waitcnt vmcnt(0)" : "=&v"(r) : "v"(lanex() * 8), "s"(ws + OFF_LAT) : "memory");
        if (__builtin_amdgcn_ballot_w64(r[0] != (u32)t || r[1] != (u32)(t >> 32)) == 0) break;
        __builtin_amdgcn_s_sleep(16);
    }
    acquire();
}
DI void init_task(const Ptrs& p, int j, u32 lds, int w) {
    const int lane = lanex(), tid = 64 * w + lane;
    Sync y;
    sync_ld(y, p.ws);
    const u32x4 ff = u32x4{~0u, ~0u, ~0u, ~0u};
    const int i = j * 256 + tid, r = j < 8 ? j & 1 : min(j / 2 - 2, NREG - 1);
    char* const vb = p.ws + (r < 2 ? 0 : OFF_U - (4u << 20)) + ((size_t)r << 21) + 1984 * 1024 + (j < 8 ? j & 6 : (j & 1) * 2 + (j >> 1 == 15) * 4) * 8192;
    const u32 vo = (u32)((w >> 1) * 8192 + (w & 1) * 1024);
    gst16(vb, vo + lane * 16, ff);
    if (i < F_N * 8) gst16(p.ws + OFF_ST, (u32)i * 16, u32x4{i == F_ONE * 8 ? 1000u : ~0u, ~0u, ~0u, ~0u});
    gst16(p.ws + 65536, (u32)i * 16, ff);
    if (i < 4096) {
        const int k = (i & 511) + ((i >> 1) & 0x600);
        u32x4 m;
        gld16<0>(m, p.mem0, (u32)((2 * ((k >> 2) & 3) + (k & 1)) * 1280 + min(2 * (k >> 4) + ((k >> 1) & 1), 79) * 16));
        waitv<0>(m);
        gst16(p.ws, (u32)i * 16, k < 640 ? u32x4{min(m[0], 0xfffffffeu), min(m[1], 0xfffffffeu), min(m[2], 0xfffffffeu), min(m[3], 0xfffffffeu)} : ff);
    }
#pragma unroll
    for (int c = 0; c < 6; ++c) gst16(p.ws + OFF_U + c * 131072 - (c == 5) * 122880, (u32)i * 16, ff);
    waitvm<0>();
    asm volatile("" : "+v"(y.r), "+v"(y.g), "+v"(y.e));
    barrier();
    if (w) return;
    u32 dt[2] = {0xffffffu, 0xffffffu};
#pragma unroll
    for (int c = 0; c < 2 * NSAMP; ++c) {
        u32 v;
        const u32 t0 = mt();
        asm volatile("global_load_dword %0, %1, %2 sc1\n\ts_waitcnt vmcnt(0)" : "=&v"(v) : "v"((c & 1) * 8192 + (c >> 1) * 256), "s"(vb) : "memory");
        dt[c & 1] = min(dt[c & 1], mt() - t0);
    }
    const u64 t = sync_tag(y.e);
    const u32 vote = (u32)((dt[0] > dt[1]) ^ xhalf() ^ (int)((vb - p.ws) >> 21)) & 1u;
    if (tid == 0) {
        if (j == 0) gst8(p.ws + OFF_LAT, 512, u32x2{(u32)t, rt()});
        gst8(p.ws + OFF_LAT, (u32)j * 8, u32x2{(u32)t | vote, (u32)(t >> 32)});
    }
}

DI float xmax(float v) {
    const auto a = __builtin_amdgcn_permlane32_swap(__float_as_uint(v), __float_as_uint(v), false, false);
    v = fmaxf(__uint_as_float(a[0]), __uint_as_float(a[1]));
    const auto b = __builtin_amdgcn_permlane16_swap(__float_as_uint(v), __float_as_uint(v), false, false);
    return fmaxf(__uint_as_float(b[0]), __uint_as_float(b[1]));
}
DI float xsum(float v) {
    const auto a = __builtin_amdgcn_permlane32_swap(__float_as_uint(v), __float_as_uint(v), false, false);
    v = __uint_as_float(a[0]) + __uint_as_float(a[1]);
    const auto b = __builtin_amdgcn_permlane16_swap(__float_as_uint(v), __float_as_uint(v), false, false);
    return __uint_as_float(b[0]) + __uint_as_float(b[1]);
}
DI void gst16c(char* sbase, u32 voff, u32x4 v) {
    asm volatile("global_store_dwordx4 %0, %1, %2 sc1\n\ts_nop 1" : : "v"(voff), "v"(v), "s"(sbase) : "memory");
}
DI void gst8c(char* sbase, u32 voff, u32x2 v) {
    asm volatile("global_store_dwordx2 %0, %1, %2 sc1\n\ts_nop 1" : : "v"(voff), "v"(v), "s"(sbase) : "memory");
}
#define WAIT6(N, r, o) asm volatile("s_waitcnt vmcnt(" #N ")" : "+v"(r[o]), "+v"(r[o + 1]), "+v"(r[o + 2]), "+v"(r[o + 3]), "+v"(r[o + 4]), "+v"(r[o + 5]) : : "memory")
#define WSLEEP 90
DI void wg_wait(const char* fl, int w, int i0, int t0, int i1, int t1, int i2, int t2, int i3, int t3, int dt, bool acq) {
    if (w == 0) {
        const int lane = lanex(), tg = lane == 1 ? t1 : (lane == 2 ? t2 : (lane == 3 ? t3 : t0));
        const int ix = tg <= 0 ? i0 : (lane == 1 ? i1 : (lane == 2 ? i2 : (lane == 3 ? i3 : i0)));
#pragma nounroll
        for (int it = 0; it < (1 << 14); ++it) {
            u32 v;
            flag_ld(v, fl, ix);
            asm volatile("s_waitcnt vmcnt(0)" : "+v"(v) : : "memory");
            if (__builtin_amdgcn_ballot_w64((int)(v + 1u) < tg) == 0) break;
            const u32 wake = rt() + (u32)(dt + 180 * min(max(t0 - 2 - uni((int)v), 0), 64));
            while ((int)(rt() - wake) < 0) __builtin_amdgcn_s_sleep(4);
        }
        if (acq) acquire();
    }
    barrier();
}
#define PXT 58
#define PDL 60
#define ODL 30
#define TDT 35
template <bool LATE>
DI void px_wave(const Ptrs& p, const ChainCfg& cfg, int s, int h, int slow, int w) {
    const int lane = lanex(), n = lane & 15, g = lane >> 4;
    const char* qb = p.ws + OFF_QX + (size_t)(s * NH + h) * 12288;
    const char* kb = p.ws + OFF_KX + (size_t)(s * NH + h) * 12288;
    const char* mb = p.ws + OFF_KM + (size_t)(s * NH + h) * 3072;
    const u32 ko = (u32)((n >> 3) * 6144 + (8 * ((n >> 2) & 1) + (n & 3) + 16 * g) * 16);
    const u32 mo = (u32)(g * 128 + (n & 7) * 16);
    u32x4 qf[12], kf[12], mf[6];
    sfor<0, 12>([&](auto I) { gld16c<(I.v % 3) * 1024>(qf[I.v], qb + (I.v / 3) * 3072, lane * 16); });
    pace(slow);
    sfor<0, 12>([&](auto I) { gld16c<(I.v % 3) * 1024 + (I.v / 6) * 64>(kf[I.v], kb + ((I.v / 3) & 1) * 3072, ko); });
    pace(slow);
    if constexpr (LATE) { WAIT6(0, qf, 0); WAIT6(0, qf, 6); WAIT6(0, kf, 0); }
    else {
        sfor<0, 6>([&](auto I) { gld16c<I.v * 512>(mf[I.v], mb, mo); });
        WAIT6(24, qf, 0); WAIT6(18, qf, 6); WAIT6(12, kf, 0);
    }
    f32x4 sc[2][3];
#pragma unroll
    for (int tt = 0; tt < 2; ++tt)
#pragma unroll
        for (int T = 0; T < 3; ++T) sc[tt][T] = f32x4{0.f, 0.f, 0.f, 0.f};
#pragma unroll
    for (int ks = 0; ks < 6; ++ks) { sc[0][0] = mfma16(kf[ks], qf[ks], sc[0][0]); sc[1][0] = mfma16(kf[ks], qf[6 + ks], sc[1][0]); }
    WAIT6(6, kf, 6);
#pragma unroll
    for (int ks = 0; ks < 6; ++ks) { sc[0][1] = mfma16(kf[6 + ks], qf[ks], sc[0][1]); sc[1][1] = mfma16(kf[6 + ks], qf[6 + ks], sc[1][1]); }
    if constexpr (LATE) {
        wg_wait(p.ws + OFF_ST, w, F_CUR + (h & 7), min(s + 1, NSEG - 2), F_PROG + 8 * 62 + (h & 7), s == NSEG - 2 ? 8 : 0, F_ONE, 0, F_ONE, 0, cfg.s1, false);
        bool ok = s != NSEG - 1;
        if (!ok) nap(cfg.T);
#pragma nounroll
        for (int it = 0; it < (1 << 14); ++it) {
            u32 v;
            sfor<0, 6>([&](auto I) { gld16c<I.v * 512>(mf[I.v], mb, mo); });
            flag_ld(v, p.ws + OFF_ST, F_PROG + 8 * 63 + (h & 7));
            WAIT6(0, mf, 0);
            asm volatile("" : "+v"(v));
            u32 m = 0;
#pragma unroll
            for (int i = 0; i < 24; ++i) m = max(m, mf[i >> 2][i & 3]);
            if (ok || __builtin_amdgcn_ballot_w64(m == ~0u) == 0) break;
            ok = uni((int)v) >= 7;
        }
    }
    WAIT6(0, mf, 0);
#pragma unroll
    for (int ks = 0; ks < 6; ++ks) { sc[0][2] = mfma16(mf[ks], qf[ks], sc[0][2]); sc[1][2] = mfma16(mf[ks], qf[6 + ks], sc[1][2]); }
    const float kscale = 0.07216878364870322f;
    char* pb = p.ws + OFF_PX + (size_t)s * 81920;
#pragma unroll
    for (int tt = 0; tt < 2; ++tt) {
        float v[12], mx = -3.0e38f;
#pragma unroll
        for (int i = 0; i < 12; ++i) {
            const float t = rbf(rbf(sc[tt][i >> 2][i & 3]) * kscale);
            v[i] = (i >= 8 || 8 * g + i <= 16 * tt + n) ? t : -3.0e38f;
            mx = fmaxf(mx, v[i]);
        }
        mx = xmax(mx);
        float sx = 0.f, sm = 0.f;
#pragma unroll
        for (int i = 0; i < 12; ++i) {
            v[i] = __builtin_amdgcn_exp2f((v[i] - mx) * 1.4426950408889634f);
            if (i < 8) sx += v[i]; else sm += v[i];
        }
        const float inv = __builtin_amdgcn_rcpf(xsum(sx + 0.5f * sm));
        gst16c(pb + h * 2048 + tt * 1024, lane * 16,
               u32x4{cvtpk(v[0] * inv, v[1] * inv), cvtpk(v[2] * inv, v[3] * inv), cvtpk(v[4] * inv, v[5] * inv), cvtpk(v[6] * inv, v[7] * inv)});
        gst8c(pb + 65536 + (h >> 2) * 2048 + (h & 3) * 256 + tt * 1024, (u32)(n * 16 + (g & 1) * 8),
              u32x2{cvtpk(v[9] * inv, v[8] * inv), cvtpk(v[11] * inv, v[10] * inv)});
    }
}

#define WAIT10(N, r) asm volatile("s_waitcnt vmcnt(" #N ")" : "+v"(r[0]), "+v"(r[1]), "+v"(r[2]), "+v"(r[3]), "+v"(r[4]), "+v"(r[5]), "+v"(r[6]), "+v"(r[7]), "+v"(r[8]), "+v"(r[9]) : : "memory")
DI void out_task(const Ptrs& p, int s, int cg, u32 lds, int slow, int w, int sd) {
    const int lane = lanex(), tid = 64 * w + lane, n = lane & 15, g = lane >> 4;
    const char* db = p.ws + OFF_D + s * D_STRIDE + (size_t)(16 * cg) * 16384 + w * 4096;
    const char* ub = p.ws + uslot(sd, s) + (size_t)(16 * cg) * 4096 + w * 1024;
    const char* pb = p.ws + OFF_PX + (size_t)s * 81920;
    const u32 dof = (u32)((n >> 3) * 16384 + g * 128 + (n & 7) * 16), uof = (u32)((n >> 3) * 4096 + g * 128 + (n & 7) * 16);
    u32x4 pf[2][10], af[3][10];
    auto aload = [&](u32x4 (&r)[10], int ct) {
        pace(slow);
        sfor<0, 8>([&](auto I) { gld16c<I.v * 512>(r[I.v], db + ct * 32768, dof); });
        gld16c<0>(r[8], ub + ct * 8192, uof); gld16c<512>(r[9], ub + ct * 8192, uof);
    };
    aload(af[0], 0);
    pace(slow);
    sfor<0, 8>([&](auto I) { gld16c<(I.v & 1) * 2048>(pf[0][I.v], pb + w * 16384 + (I.v >> 1) * 4096, lane * 16); });
    gld16c<0>(pf[0][8], pb + 65536 + w * 4096, lane * 16); gld16c<2048>(pf[0][9], pb + 65536 + w * 4096, lane * 16);
    pace(slow);
    sfor<0, 8>([&](auto I) { gld16c<(I.v & 1) * 2048 + 1024>(pf[1][I.v], pb + w * 16384 + (I.v >> 1) * 4096, lane * 16); });
    gld16c<1024>(pf[1][8], pb + 65536 + w * 4096, lane * 16); gld16c<3072>(pf[1][9], pb + 65536 + w * 4096, lane * 16);
    aload(af[1], 1);
    aload(af[2], 2);
    const int tok = 32 * s + (tid >> 3), col = 128 * cg + 8 * (tid & 7);
    u32x4 bb[2]; u32 gv;
    gld16c<0>(bb[0], p.ws + OFF_BOUT, (u32)(s * 40960 + col * 64 + (tid >> 3) * 16)); gld16c<0>(bb[1], p.ws + OFF_BOUT + 4096, (u32)(s * 40960 + col * 64 + (tid >> 3) * 16));
    asm volatile("global_load_dword %0, %1, %2 sc1" : "=v"(gv) : "v"(tok * 4), "s"(p.ws + OFF_GATE) : "memory");
    WAIT10(33, pf[0]); WAIT10(23, pf[1]);
    const u32 wa = lds + (u32)w * 16896 + n * 528 + g * 16;
    sfor<0, 8>([&](auto I) {
        constexpr int ct = I.v;
        u32x4 (&r)[10] = af[ct % 3];
        if constexpr (ct == 0) WAIT10(23, r);
        else if constexpr (ct < 3) WAIT10(23, r);
        else if constexpr (ct < 6) WAIT10(20, r);
        else if constexpr (ct == 6) WAIT10(10, r);
        else WAIT10(0, r);
        f32x4 c0 = f32x4{0.f, 0.f, 0.f, 0.f}, c1 = c0;
#pragma unroll
        for (int i = 0; i < 10; ++i) { c0 = mfma16(r[i], pf[0][i], c0); c1 = mfma16(r[i], pf[1][i], c1); }
        *LDSP(f32x4, wa + ct * 64) = c0;
        *LDSP(f32x4, wa + ct * 64 + 16 * 528) = c1;
        PIN();
        if constexpr (ct + 3 < 8) aload(r, ct + 3);
        PIN();
    });
    asm volatile("" : "+v"(bb[0]), "+v"(bb[1]), "+v"(gv));
    barrier();
    const u32 ra = lds + (u32)(tid >> 3) * 528 + (tid & 7) * 32;
    const float gt = __uint_as_float(gv), g1 = rbf(1.f - gt);
    u32 ov[8];
#pragma unroll
    for (int i = 0; i < 4; ++i) {
        const u32 ri = ra + 16 * (i & 1) + 256 * (i >> 1);
        const f32x4 z = (*LDSP(f32x4, ri) + *LDSP(f32x4, ri + 16896)) + (*LDSP(f32x4, ri + 2 * 16896) + *LDSP(f32x4, ri + 3 * 16896));
#pragma unroll
        for (int j = 0; j < 2; ++j) {
            const u32 b = bb[i >> 1][2 * (i & 1) + j];
            const u32 a = cvtpk(z[2 * j], z[2 * j + 1]);
            const u32 ga = cvtpk(gt * __uint_as_float(a << 16), gt * __uint_as_float(a & 0xffff0000u));
            const u32 gb = cvtpk(g1 * __uint_as_float(b << 16), g1 * __uint_as_float(b & 0xffff0000u));
            ov[2 * i + j] = cvtpk(__uint_as_float(ga << 16) + __uint_as_float(gb << 16), __uint_as_float(ga & 0xffff0000u) + __uint_as_float(gb & 0xffff0000u));
        }
    }
    {
    gst16p(p.out, (u32)(tok * 1280 + col * 2), u32x4{ov[0], ov[1], ov[2], ov[3]});
    gst16p(p.out, (u32)(tok * 1280 + col * 2 + 128), u32x4{ov[4], ov[5], ov[6], ov[7]});
    }
}

DI void out_tail(const Ptrs& p, const ChainCfg& cfg, int s, int cg, u32 lds, int w, int sd, int gr) {
    const int lane = lanex(), tid = 64 * w + lane, n = lane & 15, g = lane >> 4;
    const char* db = p.ws + OFF_D + s * D_STRIDE + (size_t)(4 * cg) * 16384 + w * 4096;
    const char* ub = p.ws + uslot(sd, s) + (size_t)(4 * cg) * 4096 + w * 1024;
    const char* pb = p.ws + OFF_PX + (size_t)s * 81920;
    const u32 dof = (u32)((n >> 3) * 16384 + g * 128 + (n & 7) * 16), uof = (u32)((n >> 3) * 4096 + g * 128 + (n & 7) * 16);
    u32x4 pf[2][10], af[2][10];
    sfor<0, 16>([&](auto I) { gld16c<(I.v & 7) * 512>(af[I.v >> 3][I.v & 7], db + (I.v >> 3) * 32768, dof); });
    const int tok = 32 * s + (tid >> 3), col = 32 * cg + 4 * (tid & 7);
    u32x2 bb; u32 gv;
    asm volatile("global_load_dwordx2 %0, %2, %3 sc1\n\tglobal_load_dword %1, %4, %5 sc1\n\ts_waitcnt vmcnt(0)" : "=&v"(bb), "=&v"(gv)
                 : "v"(s * 40960 + (col >> 3) * 512 + (tid >> 3) * 16 + (col & 7) * 2), "s"(p.ws + OFF_BOUT), "v"(tok * 4), "s"(p.ws + OFF_GATE) : "memory");
    sfor<0, 16>([&](auto I) { waitv<0>(af[I.v >> 3][I.v & 7]); });
    bool ok = s != NSEG - 1;
    wg_wait(p.ws + OFF_ST, w, F_CUR + gr, min(s + 1, NSEG - 2), ok ? F_PX + s : F_UD, ok ? 8 : 16, F_UD + 1, ok ? 0 : 16, F_ONE, 0, cfg.s1, false);
    if (!ok) nap(cfg.cR);
    const u32 wa = lds + (u32)w * 4608 + n * 144 + g * 16;
#pragma nounroll
    for (int it = 0; it < (1 << 14); ++it) {
        u32 v;
        flag_ld(v, p.ws + OFF_ST, F_PX + s);
        sfor<0, 8>([&](auto I) { gld16c<(I.v & 1) * 2048>(pf[0][I.v], pb + w * 16384 + (I.v >> 1) * 4096, lane * 16); });
        gld16c<0>(pf[0][8], pb + 65536 + w * 4096, lane * 16); gld16c<2048>(pf[0][9], pb + 65536 + w * 4096, lane * 16);
        sfor<0, 4>([&](auto I) { gld16c<(I.v & 1) * 512>(af[I.v >> 1][8 + (I.v & 1)], ub + (I.v >> 1) * 8192, uof); });
        sfor<0, 8>([&](auto I) { gld16c<(I.v & 1) * 2048 + 1024>(pf[1][I.v], pb + w * 16384 + (I.v >> 1) * 4096, lane * 16); });
        gld16c<1024>(pf[1][8], pb + 65536 + w * 4096, lane * 16); gld16c<3072>(pf[1][9], pb + 65536 + w * 4096, lane * 16);
        WAIT10(10, pf[0]); WAIT10(10, af[0]); WAIT10(10, af[1]);
        asm volatile("" : "+v"(v));
        float chk = 0.f;
#pragma unroll
        for (int tt = 0; tt < 2; ++tt) {
            if (tt) WAIT10(0, pf[1]);
            f32x4 c0 = f32x4{0.f, 0.f, 0.f, 0.f}, c1 = c0;
#pragma unroll
            for (int i = 0; i < 10; ++i) { c0 = mfma16(af[0][i], pf[tt][i], c0); c1 = mfma16(af[1][i], pf[tt][i], c1); }
            *LDSP(f32x4, wa + tt * 2304) = c0;
            *LDSP(f32x4, wa + tt * 2304 + 64) = c1;
            chk += c0[0];
            PIN();
        }
        if (ok || __builtin_amdgcn_ballot_w64((__float_as_uint(chk) & 0x7fffffffu) > 0x7f800000u) == 0) break;
        ok = uni((int)v) >= 7;
    }
    barrier();
    const u32 ra = lds + (u32)(tid >> 3) * 144 + (tid & 7) * 16;
    const float gt = __uint_as_float(gv), g1 = rbf(1.f - gt);
    const f32x4 z = (*LDSP(f32x4, ra) + *LDSP(f32x4, ra + 4608)) + (*LDSP(f32x4, ra + 2 * 4608) + *LDSP(f32x4, ra + 3 * 4608));
    u32x2 ov;
#pragma unroll
    for (int j = 0; j < 2; ++j) {
        const u32 b = bb[j], a = cvtpk(z[2 * j], z[2 * j + 1]);
        const u32 ga = cvtpk(gt * __uint_as_float(a << 16), gt * __uint_as_float(a & 0xffff0000u));
        const u32 gb = cvtpk(g1 * __uint_as_float(b << 16), g1 * __uint_as_float(b & 0xffff0000u));
        ov[j] = cvtpk(__uint_as_float(ga << 16) + __uint_as_float(gb << 16), __uint_as_float(ga & 0xffff0000u) + __uint_as_float(gb & 0xffff0000u));
    }
    *(u32x2*)(p.out + tok * 1280 + col * 2) = ov;
}

DI float dot2bf(u32 a, u32 b, float c) { return __builtin_amdgcn_fdot2_f32_bf16(__builtin_bit_cast(bf16x2, a), __builtin_bit_cast(bf16x2, b), c, false); }
DI void battn_task(const Ptrs& p, int t0, u32 lds, int w) {
    const int lane = lanex(), tid = 64 * w + lane, tl = tid & 31, t = t0 + tl;
    const char* const qkv = p.ws + OFF_QKVB + (size_t)(t0 >> 5) * 122880;
    {
        u32 so[3];
#pragma unroll
        for (int i = 0; i < 3; ++i) {
            const int e = 64 * i + lane, sl = e % 48;
            so[i] = (u32)((e / 48) * 512 + (sl < 16 ? (t0 ? 0 : 122880) + 256 + sl * 16 : 122880 - 256 + sl * 16));
        }
#pragma unroll
        for (int i = 0; i < 30; ++i) glds16c(qkv - 122880 + 40960 + (10 * w + i / 3) * 2048, so[i % 3], lds + (30 * w + i) * 1024);
    }
    u32x4 q[2][5], xv[10], gv[10];
    sfor<0, 10>([&](auto I) { gld16c<(I.v % 5) * 512>(q[I.v / 5][I.v % 5], qkv + (I.v / 5) * 20480, (u32)((tid >> 5) * 2560 + tl * 16)); });
    sfor<0, 10>([&](auto I) { gld16<I.v * 128>(xv[I.v], p.x + (size_t)t0 * 1280, (u32)((tid >> 3) * 1280 + (tid & 7) * 16)); gld16<I.v * 128>(gv[I.v], p.gw, (u32)((tid & 7) * 16)); });
    sfor<0, 10>([&](auto I) { waitv<0>(q[I.v / 5][I.v % 5]); waitv<0>(xv[I.v]); waitv<0>(gv[I.v]); });
    barrier();
    {
        float a = 0.f;
#pragma unroll
        for (int i = 0; i < 10; ++i)
#pragma unroll
            for (int j = 0; j < 4; ++j) {
                const u32 r = cvtpk(__uint_as_float(xv[i][j] << 16) * __uint_as_float(gv[i][j] << 16), __uint_as_float(xv[i][j] & 0xffff0000u) * __uint_as_float(gv[i][j] & 0xffff0000u));
                a += __uint_as_float(r << 16) + __uint_as_float(r & 0xffff0000u);
            }
        a += __shfl_xor(a, 1); a += __shfl_xor(a, 2); a += __shfl_xor(a, 4);
        const float z = rbf(a);
        if ((tid & 7) == 0) gst4c(p.ws + OFF_GATE, (u32)((t0 + (tid >> 3)) * 4), __float_as_uint(rbf(1.0f / (1.0f + __expf(-z)))));
    }
#pragma unroll
    for (int it = 0; it < 2; ++it) {
        const int m = (tid >> 5) + 8 * it;
        u32 ka = lds + (u32)(m * 3840 + (2 + tl) * 16), va = ka + 61440;
        asm volatile("" : "+v"(ka), "+v"(va));
        u32x4 k[5][8];
#pragma unroll
        for (int i = 0; i < 40; ++i) k[i >> 3][i & 7] = ldsr16(ka + (i >> 3) * 768 + (7 - (i & 7)) * 32);
        PIN();
        float lg[2][8], mx[2] = {-3.0e38f, -3.0e38f};
#pragma unroll
        for (int i = 0; i < 16; ++i) lg[i >> 3][i & 7] = 0.f;
#pragma unroll
        for (int i = 0; i < 5; ++i) {
#pragma unroll
            for (int j = 0; j < 4; ++j)
#pragma unroll
                for (int wv = 0; wv < 8; ++wv) lg[4 * i + j >= 10][wv] = dot2bf(q[it][i][j], k[i][wv][j], lg[4 * i + j >= 10][wv]);
            PIN();
#pragma unroll
            for (int wv = 0; wv < 8; ++wv) k[i][wv] = ldsr16(va + i * 768 + (7 - wv) * 32);
            PIN();
        }
#pragma unroll
        for (int i = 0; i < 16; ++i) {
            float& l = lg[i >> 3][i & 7];
            l = rbf(rbf(l) * 0.22360679774997896f);
            asm volatile("" : "+v"(l));
            l = t - 2 * (i & 7) >= 0 ? l : -3.0e38f;
            mx[i >> 3] = fmaxf(mx[i >> 3], l);
        }
        u32 pk[2][4];
#pragma unroll
        for (int u = 0; u < 2; ++u) {
            float sum = 0.f;
#pragma unroll
            for (int wv = 0; wv < 8; ++wv) { lg[u][wv] = __builtin_amdgcn_exp2f((lg[u][wv] - mx[u]) * 1.4426950408889634f); sum += lg[u][wv]; }
            const float inv = __builtin_amdgcn_rcpf(sum);
#pragma unroll
            for (int wv = 0; wv < 4; ++wv) pk[u][wv] = cvtpk(lg[u][2 * wv] * inv, lg[u][2 * wv + 1] * inv);
        }
#pragma unroll
        for (int i = 0; i < 5; ++i) {
            float o[8];
#pragma unroll
            for (int d = 0; d < 8; ++d) o[d] = 0.f;
#pragma unroll
            for (int wp = 0; wp < 4; ++wp) {
                const u32x4 v0 = k[i][2 * wp], v1 = k[i][2 * wp + 1];
#pragma unroll
                for (int j = 0; j < 4; ++j) {
                    const u32 pw = pk[4 * i + j >= 10][wp];
                    o[2 * j] = dot2bf(__builtin_amdgcn_perm(v1[j], v0[j], 0x05040100u), pw, o[2 * j]);
                    o[2 * j + 1] = dot2bf(__builtin_amdgcn_perm(v1[j], v0[j], 0x07060302u), pw, o[2 * j + 1]);
                }
            }
            gst16c(p.ws + OFF_OB + (size_t)(t0 >> 5) * 40960 + 512 * i, (u32)(m * 2560 + tl * 16), u32x4{cvtpk(o[0], o[1]), cvtpk(o[2], o[3]), cvtpk(o[4], o[5]), cvtpk(o[6], o[7])});
        }
    }
}

#define XPF 1
#define PFQ 0
#define PFN 8
constexpr int QN = 80, QF = 19;
struct QTab { u16 e[QN]; int o0, c1, r1, r2, ps0, pr, lg, un; u32 tt[64]; };
typedef const __attribute__((address_space(4))) int* KI;
constexpr int QT_OFF = (88 + 48) / 4;
DI int pick_task(char* fl, KI k, int gr, int& hint) {
    const int lane = lanex();
#pragma nounroll
    for (int it = 0; it < (1 << 14); ++it) {
        const int b0 = hint & 15, c0 = hint >> 4;
        const int ln = lane < 5 ? F_QH + gr : (lane == 5 ? F_CUR + gr : (lane < 8 ? F_QKVB + max(gr + 6 - lane, 0) : (lane == 8 ? F_BA + gr
                       : (lane < 11 ? F_Q + min(b0 + lane - 9, 8) * 8 + gr : F_BO + min(c0 + (lane & 1 ^ 1), 7)))));
        u32 v;
        asm volatile("s_nop 4\n\tglobal_load_dword %0, %1, %2 sc1\n\ts_waitcnt vmcnt(0)" : "=&v"(v) : "v"(ln * 128 + (lane < 5 ? lane * 4 : 0)), "s"(fl) : "memory");
        auto at = [&](int i) { return __builtin_amdgcn_readlane((int)v, i) + 1; };
        const int ng = at(0) + QF, na = at(1), nb = at(2), np = at(3), no = at(4), cur = at(5);
        const int pb = pblk(min(np, 63)), os = min((8 * no + 7) / 5, 63), ob = os >> 3;
        hint = pb | ob << 4;
        const bool more = ng < QN;
        auto entry = [&](int q) { const u32 e2 = (u32)k[QT_OFF + (q >> 1)]; return (int)(q & 1 ? e2 >> 16 : e2 & 0xffffu); };
        auto olim = [&](int c) { return c >= k[QT_OFF + 46] ? 99 : (min(c, k[QT_OFF + 41]) - k[QT_OFF + 40]) * k[QT_OFF + 42] + max(c - k[QT_OFF + 41], 0) * k[QT_OFF + 43] >> 3; };
        const int lg = k[QT_OFF + 46];
        auto pstep = [&](int x) { return x < PXT ? max(x + 1, k[QT_OFF + 44] + (8 * x + 8) / k[QT_OFF + 45]) : lg; };
        auto ostep = [&](int x) { int c = x < 38 ? min((8 * x + 7) / 5, 59) + 2 : lg; while (x >= olim(c)) ++c; return c; };
        const int tg = more ? entry(ng) >> 9 : 999;
        const int tp = np < NSEG && pb - b0 < 2 && at(9 + max(pb - b0, 0)) >= 4 ? pstep(np) : 999;
        const int to = no < 48 && (no < 38 ? min(os, 59) + 1 < np : np >= NSEG) && ob - c0 < 2 && at(11 + max(ob - c0, 0)) >= 4 ? ostep(no) : 999;
        int q = -1;
        if (tg <= cur) q = 0;
        else if (na < 8 && at(6) >= 5 && at(7) >= 5) q = 1;
        else if (nb < 4 && at(8) >= 8) q = 2;
        else if (to < 999 && to <= max(cur, min(tp, tg))) q = 4;
        else if (tp < 999 && tp <= max(cur, tg)) q = 3;
        else if (more && (nb >= 4 || tg <= cur + (k[QT_OFF + 47] >> 16))) q = 0;
        else if (!more && na >= 8 && nb >= 4 && np >= NSEG && no >= 48) return -1;
        if (q < 0) { nap(pb != b0 || ob != c0 ? 0 : k[QT_OFF + 47] & 0xffff); continue; }
        u32 r;
        asm volatile("s_mov_b64 exec, 1\n\ts_nop 3\n\tglobal_atomic_add %0, %1, %2, %3 sc0 sc1\n\ts_mov_b64 exec, -1\n\ts_waitcnt vmcnt(0)"
                     : "=&v"(r) : "v"((F_QH + gr) * 128 + q * 4), "v"(1), "s"(fl) : "memory");
        const int x = uni((int)r) + 1;
        if (q == 0 && x + QF < QN) { const int e = entry(x + QF); return e >> 9 > cur ? e + 512 : e & 511; }
        if (q == 1 && x < 8) return Q_BA | x << 3;
        if (q == 2 && x < 4) return Q_BO | x << 3;
        if (q == 3 && x < NSEG) return Q_PX | x << 3 | (pstep(x) + 1) << 9;
        if (q == 4 && x < 48) return Q_OUT | (x < 38 ? x : x + 2) << 3 | (ostep(x) + 1) << 9;
    }
    return -1;
}
__global__ __launch_bounds__(256, 1) void k_main(Ptrs p0, ChainCfg cfg, QTab tab) {
    extern __shared__ __attribute__((aligned(16))) char smem[];
    u32 lds = (u32)(size_t)LDSP(char, smem);
    int n = blockIdx.x, gr = n & 7;
    int w = uni(threadIdx.x >> 6);
    {
        u64 pc; int len;
        asm volatile("s_getpc_b64 %0\n.Lkm_first:\n\ts_mov_b32 %1, .Lkm_last-.Lkm_first\n\ts_max_i32 %1, %1, .Lkm_lu-.Lkm_first\n\ts_max_i32 %1, %1, .Lkm_lv-.Lkm_first\n\t"
                     "s_max_i32 %1, %1, .Lkm_g2-.Lkm_first\n\ts_max_i32 %1, %1, .Lkm_g4-.Lkm_first" : "=s"(pc), "=s"(len) : : "scc");
        if (w == 3) pfl((const char*)pc, (u32)min((int)(blockIdx.x >> 3) * 8192 + (int)(threadIdx.x & 63) * 128, max(len, 0)), lds);
    }
    karg_warm();
    if (n < NH) { score_role(p0, cfg, n, lds, w); return; }
    if (n < NS) { proj_role(p0, cfg, n - NH, lds, w); return; }
    const bool isv = n < NS + NVB;
#define qslot (lds + GRING * GBUF)
    int sdw = -1;
    int en = isv ? Q_VD + (n & 24) : tab.e[(n - NS - NVB) >> 3] & 511, hint = 0;
    u32 phi0 = 0;
    Sync y;
    if (XPF && n < 160 && w >= 2) pfl(p0.x, (u32)min(((n - NS) >> 3) * 128 + (w & 1) * 64 + lanex(), 1279) * 128, lds);
    if (n >= 160) {
        const int lane = lanex();
        if (n >= 192 && (n & 8)) {
            init_task(p0, (n & 7) + ((n - 192) >> 4) * 8, lds, w);
            barrier();
#pragma unroll
            for (int i = 0; i < 3; ++i) pfl(p0.wqkvb, (u32)min((((n - 192) >> 4) * 8 + (n & 7)) * 768 + i * 256 + w * 64 + lane, 19199 + 0) * 128, lds);
            pfl(p0.wob, (u32)min((((n - 192) >> 4) * 8 + (n & 7)) * 256 + w * 64 + lane, 6399) * 128, lds);
        } else {
            const int h = n < 192 ? n & 31 : (n & 7) + ((n - 192) >> 4) * 8;
            xcopy_task(p0, h + (n < 192 ? 0 : 32), w);
#pragma unroll
            for (int i = 0; i < PFN; ++i) {
                if (!PFQ && n < 192) break;
                const int l = 64 * min(i * 4 + w, 29) + lane;
                pfl(n < 192 ? p0.wq + h * 384 : p0.wo + (size_t)h * 245760, n < 192 ? (u32)((l / 3) * 12288 + (l % 3) * 128) : (u32)(l * 128), lds);
            }
        }
        waitvm<0>();
        if (w == 0) {
            sync_ld(y, p0.ws);
            waitvm<0>();
            *LDSP(u32, qslot) = (u32)sync_end(y, p0.ws, phi0, true);
            xcopy_wait(p0.ws, sync_tag(y.e));
        }
        barrier();
        sdw = uni((int)*LDSP(u32, qslot));
        barrier();
    }
    while (true) {
        typedef char* const __attribute__((address_space(4)))* KP;
        KP kp = (KP)__builtin_amdgcn_kernarg_segment_ptr();
        asm volatile("" : "+s"(kp), "+s"(lds), "+s"(w), "+s"(gr), "+s"(n));
        __builtin_assume(w >= 0 && w < 4 && gr >= 0 && gr < 8);
        const Ptrs p{kp[0], kp[1], kp[2], kp[3], kp[4], kp[5], kp[6], kp[7], kp[8], kp[9], kp[10]};
        const int ty = en & 7, ix = (en >> 3) & 63, gate = en >> 9;
        char* const fl = p.ws + OFF_ST;
        const bool raw = sdw < 0;
        int t0 = gate - 1, f1 = F_ONE, t1 = 0, f2 = F_ONE, t2 = 0, f3 = F_ONE, t3 = 0, dt = cfg.T2;
        const int os = ix >= 40 ? 44 + (8 * ix + gr) / 20 : (8 * ix + gr) / 5;
        if (ty == Q_BA) { f1 = F_QKVB + gr; t1 = 5; f2 = F_QKVB + max(gr - 1, 0); t2 = 5; }
        if (ty == Q_BO) { f1 = F_BA + gr; t1 = 8; }
        if (ty == Q_PX) { f1 = F_Q + pblk(ix) * 8 + gr; t1 = 4; if (ix < PXT) t0 = max(t0, ix + 1); }
        if (ty == Q_OUT) { f3 = F_BO + (os >> 3); t3 = 4; if (os < 60) { f1 = F_PX + os; t1 = 8; } else { f1 = F_VD + 64 + gr; t1 = 32; } }
        if (gate || ty >= Q_BA) wg_wait(fl, w, F_CUR + gr, t0, f1, t1, f2, t2, f3, t3, dt, ty == Q_BO);
        int sig, nrep = 1;
        if (ty <= Q_QKVB || ty == Q_BO) {
            const bool ab = ty <= Q_KQ;
            sig = ty == Q_VD ? F_VD + (ix >> 2) * 8 : (ty == Q_KQ ? F_Q + (ix >> 2) * 8 : (ty == Q_H ? (ix & 4 ? F_Q : F_K + 8 * ix) : (ty == Q_BO ? F_BO : F_QKVB))) + gr;
            nrep = ty == Q_VD ? 8 : 1;
            if (ty == Q_VD && ix < 4) { sig += n & 32 ? 4 : 0; nrep = 4; }
            run_gemm(p, ty, ab ? ix >> 2 : gr, ab ? gr + 8 * (ix & 3) : ix, (n >> 5) & 1, raw, y, lds, w);
            if (raw) sdw = sync_end(y, p.ws, phi0, w == 0);
        } else {
            if (ty == Q_BA) {
                battn_task(p, (8 * gr + ix) * 32, lds, w);
                sig = F_BA + gr;
            } else if (ty == Q_PX) {
                if (ix < PXT) px_wave<false>(p, cfg, ix, gr + 8 * w, ix - cfg.fast, w);
                else px_wave<true>(p, cfg, ix, gr + 8 * w, -1, w);
                sig = F_PX + ix;
            } else {
                if (ix >= 40) out_tail(p, cfg, os, 8 * ix + gr - 20 * (os - 44), lds, w, sdw, gr);
                else if (os < 60) out_task(p, os, 8 * ix + gr - 5 * os, lds, os + 1 - cfg.fast, w, sdw);
                sig = F_END;
            }
            waitvm<0>();
        }
        barrier();
        if (w == 0 && sig != F_END) flag_add(fl, sig, nrep);
        if (raw && !isv && w == 0) xcopy_wait(p.ws, sync_tag(y.e));
        if (isv) break;
        if (w == 0) *LDSP(int, qslot) = pick_task(fl, (KI)kp, gr, hint);
        barrier();
        en = uni(*LDSP(int, qslot));
        if (en < 0) return;
        barrier();
    }
    value_role(p0, cfg, n - NS, sdw, phi0, lds, w);
}

#define QCFG 16, 10, 30, 45, 8, 12, 4
#define LVDEF "0044220000000022220000002222000000000000220000002200000000562000"
constexpr int DX = 200, DUA = 340, DUB = 1, DUC = 2;
static ChainCfg dev_cfg(ChainCfg c) { c.T = PDL; c.cR = ODL; c.s1 = TDT; c.T2 = WSLEEP; c.cR2 = DX; c.dU = DUA; c.s2 = DUB; c.T3 = DUC; c.cR3 = 0 | 0 << 8; return c; }
static QTab set_sched(QTab t, const ChainCfg c, const char* lv) {
    for (int s = 0; s < 64; ++s) {
        const int d = lv[s] - '0';
        t.tt[s] = (u32)((s < c.s1 ? c.T : (s < c.s2 ? c.T2 : c.T3)) + 4 * d) | (u32)((s < c.s1 ? c.cR : (s < c.s2 ? c.cR2 : c.cR3)) + 5 * d / 2) << 16;
    }
    t.tt[0] = (u32)270 | (u32)165 << 16;
    t.tt[1] = (u32)270 | (u32)180 << 16;
    return t;
}
static QTab make_tab(int lv, int lk, int o0, int c1, int r1, int r2, int ps0) {
    QTab t{};
    int n = 0, dl[QN];
    auto add = [&](int ty, int ix) { t.e[n] = (u16)(ty | ix << 3); dl[n++] = ty == Q_VD || ty == Q_KQ ? 8 * (ix / 4 - 1) - (ty == Q_VD ? lv : lk) - (ix >= 32 ? 7 : 0) : 99; };
    for (int i = 1; i < 4; ++i) add(Q_VD, i);
    for (int i = 0; i < 16; ++i) add(i < 4 ? Q_H : ((i & 8) ? Q_VD : Q_KQ), i < 8 ? i : 4 + (i & 1) * 4 + ((i - 8) >> 1));
    for (int i = 0; i < 4; ++i) add(Q_KQ, 8 + i);
    for (int i = 0; i < 5; ++i) add(Q_QKVB, i);
    for (int i = 0; i < 8; ++i) add(i & 4 ? Q_KQ : Q_VD, 12 + (i & 3));
    for (int i = 4; i < 8; ++i) add(Q_H, i);
    for (int k = 0; k < 32; ++k) add(k & 8 ? Q_KQ : Q_VD, 4 * (4 + 2 * (k / 16) + (k & 1)) + ((k >> 1) & 3));
    for (int i = 0; i < 8; ++i) add(i & 4 ? Q_KQ : Q_VD, 32 + (i & 3));
    const int gap = 8;
    for (int i = QN - 1, m = 999; i >= 0; --i) { m = min(8 * dl[i], m - gap); t.e[i] |= (u16)(max(min(m / 8, 99), 0) << 9); }
    t.o0 = o0; t.c1 = c1; t.r1 = r1; t.r2 = r2; t.ps0 = ps0; t.pr = 16; t.lg = 57; t.un = 200 | 3 << 16;
    return t;
}

extern "C" void kernel_launch(void* const* d_in, const int* in_sizes, int n_in, void* d_out, int out_size, void* d_ws,
                              size_t ws_size, hipStream_t stream) {
    Ptrs p{(const char*)d_in[0], (const char*)d_in[1], (const char*)d_in[2], (const char*)d_in[3], (const char*)d_in[4], (const char*)d_in[5],
           (const char*)d_in[8], (const char*)d_in[9], (const char*)d_in[10], (char*)d_out, (char*)(((size_t)d_ws + 0x1fffff) & ~(size_t)0x1fffff)};
    (void)hipFuncSetAttribute((const void*)k_main, hipFuncAttributeMaxDynamicSharedMemorySize, S_LDS);
    k_main<<<256, 256, S_LDS, stream>>>(p, dev_cfg(ChainCfg{CFG}), set_sched(make_tab(QCFG), ChainCfg{CFG}, LVDEF));
    static_assert(sizeof(Ptrs) + sizeof(ChainCfg) + sizeof(QTab) == TT_OFF + 256, "");
}
```

```cpp
#include <hip/hip_runtime.h>
#pragma clang diagnostic ignored "-Wint-to-pointer-cast"

typedef unsigned short u16;
typedef unsigned int u32;
typedef unsigned long long u64;
typedef __bf16 bf16x8 __attribute__((ext_vector_type(8)));
typedef __bf16 bf16x16 __attribute__((ext_vector_type(16)));
typedef short s16x4 __attribute__((ext_vector_type(4)));
typedef float f32x4 __attribute__((ext_vector_type(4)));
typedef unsigned u32x4 __attribute__((ext_vector_type(4)));
typedef unsigned u32x2 __attribute__((ext_vector_type(2)));
typedef unsigned u32x8 __attribute__((ext_vector_type(8)));
typedef unsigned u32x16 __attribute__((ext_vector_type(16)));
typedef unsigned u32x32 __attribute__((ext_vector_type(32)));
#define DI __device__ __forceinline__
#define LDSP(T, a) ((__attribute__((address_space(3))) T*)(a))

constexpr int S_LEN = 2048, DM = 640, NH = 32, DH = 192, NSEG = 64, HD = NH * DH;
#define NSAMP 4
constexpr int NREG = 13;
constexpr int NV = 80, NVB = 40;
constexpr int NS = 64;
#define CFG 160, 106, 108, 30, 62, 16, 160, 106, 52, 160, 106, 6

constexpr size_t U_STRIDE = NV * 4096, D_STRIDE = NV * 16384;
constexpr size_t HO_STRIDE = 65536;
constexpr size_t OFF_ST = NSEG * HO_STRIDE;
enum { F_ACK = 0, F_ONE = 3, F_VD = 16, F_QH = 96, F_B = 104, F_CUR = 128, F_PROG = 136 - 8 * 62, F_UD = 152, F_PX = 160, F_K = 224, F_END = 512, F_Q = 520, F_N = 592 };
constexpr size_t OFF_LAT = OFF_ST + F_N * 128;
constexpr size_t OFF_KX = 5u << 20;
constexpr size_t OFF_QX = OFF_KX + (size_t)S_LEN * HD * 2;
constexpr size_t OFF_D = OFF_QX + (size_t)S_LEN * HD * 2;
constexpr size_t OFF_U = (OFF_D + NSEG * D_STRIDE + 0x1fffff) & ~(size_t)0x1fffff;
constexpr size_t OFF_KM = OFF_U + (22u << 20);
constexpr size_t OFF_PX = OFF_KM + (size_t)NSEG * NH * 3072;
constexpr size_t OFF_QKVB = OFF_PX + (size_t)NSEG * 81920;
constexpr size_t OFF_OB = OFF_QKVB + (size_t)S_LEN * 1920 * 2;
constexpr size_t OFF_BOUT = OFF_OB + (size_t)S_LEN * DM * 2;
constexpr size_t OFF_GATE = OFF_BOUT + (size_t)S_LEN * DM * 2;
constexpr size_t OFF_XF = OFF_GATE + S_LEN * 4;
constexpr size_t WS_END = OFF_XF + (size_t)S_LEN * DM * 2;
static_assert(WS_END + (2u << 20) <= (256u << 20), "workspace");

typedef __bf16 bf16x2 __attribute__((ext_vector_type(2)));
typedef float f32x2 __attribute__((ext_vector_type(2)));
DI u32 cvtpk(float lo, float hi) { return __builtin_bit_cast(u32, __builtin_convertvector(f32x2{lo, hi}, bf16x2)); }
DI float rbf(float f) { return __uint_as_float(cvtpk(f, f) << 16); }
DI f32x4 mfma16(u32x4 a, u32x4 b, f32x4 c) {
    return __builtin_amdgcn_mfma_f32_16x16x32_bf16(__builtin_bit_cast(bf16x8, a), __builtin_bit_cast(bf16x8, b), c, 0, 0, 0);
}
DI u32x2 trread(u32 a) {
    return __builtin_bit_cast(u32x2, __builtin_amdgcn_ds_read_tr16_b64_v4i16(LDSP(s16x4, a)));
}
DI void glds16c(const char* sbase, u32 voff, u32 lds) {
    u32 sv;
    asm volatile("s_mov_b32 %0, m0\n\ts_mov_b32 m0, %3\n\ts_nop 0\n\tglobal_load_lds_dwordx4 %1, %2 sc1\n\ts_mov_b32 m0, %0"
                 : "=&s"(sv) : "v"(voff), "s"(sbase), "s"(lds) : "memory");
}
template <int IMM>
DI void gld16(u32x4& r, const char* sbase, u32 voff) {
    asm volatile("s_nop 4\n\tglobal_load_dwordx4 %0, %1, %2 offset:%3" : "=v"(r) : "v"(voff), "s"(sbase), "i"(IMM) : "memory");
}
DI void gst16(char* sbase, u32 voff, u32x4 v) {
    asm volatile("global_store_dwordx4 %0, %1, %2 sc1\n\ts_nop 1" : : "v"(voff), "v"(v), "s"(sbase) : "memory");
}
#define OST " nt"
DI void gst16p(char* sbase, u32 voff, u32x4 v) {
    asm volatile("s_nop 4\n\tglobal_store_dwordx4 %0, %1, %2" OST "\n\ts_nop 1" : : "v"(voff), "v"(v), "s"(sbase) : "memory");
}
DI void gst8(char* sbase, u32 voff, u32x2 v) {
    asm volatile("global_store_dwordx2 %0, %1, %2 sc1\n\ts_nop 1" : : "v"(voff), "v"(v), "s"(sbase) : "memory");
}
template <int N>
DI void waitvm() { asm volatile("s_waitcnt vmcnt(%0)" : : "i"(N) : "memory"); }
DI void barrier() { asm volatile("s_waitcnt lgkmcnt(0)\n\ts_barrier" : : : "memory"); }
#define PIN() __builtin_amdgcn_sched_barrier(0)
DI int uni(int v) { return __builtin_amdgcn_readfirstlane(v); }
DI int lanex() {
    int t;
    asm volatile("v_mbcnt_lo_u32_b32 %0, -1, 0\n\tv_mbcnt_hi_u32_b32 %0, -1, %0" : "=v"(t));
    __builtin_assume(t >= 0 && t < 64);
    return t;
}
DI void flag_ld(u32& r, const char* fl, int idx) {
    asm volatile("s_nop 4\n\tglobal_load_dword %0, %1, %2 sc1" : "=v"(r) : "v"((idx & 0xffff) * 128 + (idx >> 14)), "s"(fl) : "memory");
}
DI void flag_ldm(u32& r, const char* fl, int idx, bool on) {
    asm volatile("v_mov_b32 %0, 1000\n\ts_mov_b32 exec_lo, %3\n\ts_mov_b32 exec_hi, 0\n\tglobal_load_dword %0, %1, %2 sc1\n\ts_mov_b64 exec, -1" : "=&v"(r) : "v"(idx * 128), "s"(fl), "s"(uni(on ? 1 : 0)) : "memory");
}
DI void flag_wait(const char* fl, int idx, int target) {
#pragma nounroll
    for (int it = 0; it < (1 << 16); ++it) {
        u32 v;
        flag_ld(v, fl, idx);
        asm volatile("s_waitcnt vmcnt(0)" : "+v"(v) : : "memory");
        if (uni((int)(v + 1u)) >= target) break;
        __builtin_amdgcn_s_sleep(6);
    }
}
DI void flag_add(char* fl, int idx, int nrep) {
    const int lane = lanex();
    if (lane < nrep) asm volatile("s_nop 4\n\tglobal_atomic_add %0, %1, %2 sc1" : : "v"(((idx & 0xffff) + lane) * 128 + (idx >> 14)), "v"(1), "s"(fl) : "memory");
}
DI void acquire() { asm volatile("buffer_inv sc1\n\ts_waitcnt vmcnt(0)" : : : "memory"); }

template <int V> struct IC { static constexpr int v = V; };
template <int B, int E, class Fn>
DI void sfor(Fn&& fn) { if constexpr (B < E) { fn(IC<B>{}); sfor<B + 1, E>(fn); } }
enum { T_QK = 0, T_VD = 1, T_BL = 3 };
constexpr int GBUF = 24576, GRING = 6;
struct GemmArgs {
    const char *w1, *w1b;
    const char* w2;
    const char* x;
    char *out, *outb;
    int ldw;
    char* sig;
    int type, np1, tn;
    int sV, sP, sQ;
    int xb;
};
DI const char* unip(const char* p) {
    const u64 v = (u64)p;
    return (const char*)(((u64)(u32)uni((int)(v >> 32)) << 32) | (u32)uni((int)v));
}
template <int Z>
DI void mf1(f32x4& c, const u32x4& a, const u32x4& b) {
    if constexpr (Z) asm volatile("v_mfma_f32_16x16x32_bf16 %0, %1, %2, 0" : "=&v"(c) : "v"(a), "a"(b));
    else asm volatile("v_mfma_f32_16x16x32_bf16 %0, %1, %2, %0" : "+v"(c) : "v"(a), "a"(b));
}
template <int Z>
DI void mf2(f32x4& c, const u32x4& a, const u32x4& b) {
    if constexpr (Z) asm volatile("v_mfma_f32_16x16x32_bf16 %0, %1, %2, 0" : "=&v"(c) : "v"(a), "v"(b));
    else asm volatile("v_mfma_f32_16x16x32_bf16 %0, %1, %2, %0" : "+v"(c) : "v"(a), "v"(b));
}
template <int IMM>
DI void gld16a(u32x4& r, const char* sbase, u32 voff) {
    asm volatile("global_load_dwordx4 %0, %1, %2 offset:%3 sc1" : "+a"(r) : "v"(voff), "s"(sbase), "i"(IMM) : "memory");
}
DI void setm0(u32 v) { asm volatile("s_mov_b32 m0, %0" : : "s"(v) : "memory"); }
template <int IMM>
DI void gldsm(const char* sbase, u32 voff) {
    asm volatile("global_load_lds_dwordx4 %0, %1 offset:%2" : : "v"(voff), "s"(sbase), "i"(IMM) : "memory");
}
DI void gst16n(char* sbase, u32 voff, u32x4 v) {
    asm volatile("global_store_dwordx4 %0, %1, %2 sc1" : : "v"(voff), "v"(v), "s"(sbase) : "memory");
}
template <int N>
DI void waitbar() { asm volatile("s_waitcnt vmcnt(%0)\n\ts_barrier" : : "i"(N) : "memory"); }
#define LGKM(n) __builtin_amdgcn_s_waitcnt(0xC07F | ((n) << 8))

DI int xhalf() { u32 x; asm volatile("s_getreg_b32 %0, hwreg(HW_REG_XCC_ID)" : "=s"(x)); return (int)((x >> 2) & 1); }
struct Sync { u32x2 r, g, e; };
DI void sync_ld(Sync& y, const char* ws) {
    asm volatile("s_nop 4\n\tglobal_load_dwordx2 %0, %3, %5 sc1\n\tglobal_load_dwordx2 %1, %4, %5 offset:512 sc1\n\tglobal_load_dwordx2 %2, %4, %5 offset:1024 sc1"
                 : "=&v"(y.r), "=&v"(y.g), "=&v"(y.e) : "v"((lanex() & 31) * 8), "v"(0), "s"(ws + OFF_LAT) : "memory");
}
DI u64 sync_tag(u32x2 e) { return (((u64)e[1] << 32 | e[0]) + 1) << 1; }
DI int sync_end(Sync& y, const char* ws, u32& phi, bool ack) {
    asm volatile("" : "+v"(y.r), "+v"(y.g), "+v"(y.e));
#pragma nounroll
    for (int it = 0; it < (1 << 16); ++it) {
        const u64 t = sync_tag(y.e);
        if (__builtin_amdgcn_ballot_w64((y.r[0] & ~1u) != (u32)t || y.r[1] != (u32)(t >> 32) || y.g[0] != (u32)t) == 0) break;
        __builtin_amdgcn_s_sleep(24);
        sync_ld(y, ws);
        asm volatile("s_waitcnt vmcnt(0)" : "+v"(y.r), "+v"(y.g), "+v"(y.e) : : "memory");
    }
    if (ack) flag_add((char*)ws + OFF_ST, F_ACK, 1);
    phi = (u32)uni((int)y.g[1]);
    const u32 b = (u32)__builtin_amdgcn_ballot_w64(y.r[0] & 1), all = __builtin_popcount(b) > 16;
    u32 q = 0;
#pragma unroll
    for (int r = 0; r < NREG; ++r) {
        const u32 c = r < 2 ? __builtin_popcount(b & (0x55u << r)) + all : 2 * __builtin_popcount((b >> (4 + 2 * r)) & 3) + all;
        q |= ((c > 2) ^ (u32)((r < 2 ? r : r + (OFF_U >> 21)) & 1)) << r;
    }
    return uni((int)(q ^ 3u * xhalf()));
}
DI void pace(int on) {
    asm volatile("s_cmp_ge_i32 %0, 0\n\ts_cbranch_scc1 .Lp%=\n\ts_waitcnt vmcnt(0)\n.Lp%=:" : : "s"(on) : "memory", "scc");
}
#define SMO 48
#define TLBT 0
#define SMB 48
#define SMP SMO
template <int C>
DI void gap(int on) {
    if constexpr (C > 0) asm volatile("s_cmp_ge_i32 %0, 0\n\ts_cbranch_scc1 .Lg%=\n\ts_nop %1\n\ts_nop %2\n\ts_nop %3\n.Lg%=:" : : "s"(on), "n"(C / 12 - 1), "n"(C / 12 - 1), "n"(C / 12 - 1) : "memory", "scc");
}
DI void early_add(char* ctr, int c) {
    asm volatile("s_cmp_lg_u32 %1, 1\n\ts_cbranch_scc1 .Le%=\n\ts_mov_b64 exec, 1\n\ts_nop 1\n\tglobal_atomic_add %2, %3, %0 sc1\n\ts_mov_b64 exec, -1\n.Le%=:"
                 : : "s"(ctr), "s"(c), "v"(0), "v"(1) : "memory", "scc");
}
#define XL 2
template <int NT>
DI void gemm_task(const GemmArgs& g, Sync& y, const char* ws, u32 lds, int w) {
    constexpr int FD = 5, NX = 2 * NT, OPS = NX + 6, BI = 24 - FD;
    const int lane = lanex();
    const int n = lane & 15, q = lane >> 4, qq = (lane >> 2) & 3, pp = lane & 3;
    const bool p2 = g.type == T_VD;
    const int rw = lane >> 3, sp = (lane >> 1) & 3;
    const u32 v2b = (u32)(rw * 1280 + 16 * (lane & 1) + 5120);
    auto v2o = [&](int i) { return v2b + (u32)(((sp ^ (rw >> 1)) << 5) + i * 9216); };
    u32 wv[6];
#pragma unroll
    for (int i = 0; i < 6; ++i)
        wv[i] = (u32)((rw + 8 * (i / 3)) * g.ldw + ((sp ^ (((rw >> 1) & 1) + 2 * (i / 3))) << 5) + 16 * (lane & 1) + (i % 3) * 128 + 5120 - 1024 * i);
    const char* const wp2 = g.w2 + (size_t)(48 * w) * 1280 - 2048;
    const char* wp = unip(g.w1 + (size_t)(16 * w) * g.ldw - 2048);
    int wstep = uni(64 * g.ldw);
    u32 wdst = lds + (u32)w * 6144 + 3072;
    const u32 wdlast = wdst + (GRING - 1) * GBUF;
    auto wring = [&]() { wdst = wdst == wdlast ? wdst - (GRING - 1) * GBUF : wdst + GBUF; };
    auto wload = [&](int i) {
        setm0(wdst);
        wring();
        asm volatile("s_nop 0");
        sfor<0, 6>([&](auto I) { gldsm<1024 * (I.v - 3)>(wp, wv[I.v]); });
        wp += wstep;
    };
    u32x4 vf[NT][6];
    u32x4 fr[FD + 1];
    u32x4 xr[5][NX];
    auto xtie = [&](u32x4 (&x)[NX]) {
#pragma unroll
        for (int i = 0; i < NX; i += 4) asm volatile("" : "+a"(x[i]), "+a"(x[i + 1]), "+a"(x[i + 2]), "+a"(x[i + 3]));
    };
#pragma unroll
    for (int i = 0; i < 5 * NX; ++i) asm volatile("" : "=a"(xr[i / NX][i % NX]));
    u32x2 flo;
    u32 c[4], slot = 0;
    int sd = GBUF;
    auto sring = [&]() { sd = slot == (GRING - 1) * GBUF ? -(GRING - 1) * GBUF : GBUF; slot += (u32)sd; };
    {
        u32 xo[NT];
#pragma unroll
        for (int tt = 0; tt < NT; ++tt) {
            const int tok = (g.type == T_VD) ? 8 * (n >> 2) + 4 * (tt & 1) + (n & 3) : 16 * (tt & 1) + n;
            xo[tt] = (u32)((NT / 2 * w + (tt >> 1)) * 40960 + (g.xb ? q * 512 + tok * 16 : q * 16 + tok * 1280));
        }
        f32x4 acc[12][NT];
        const bool rm = NT == 4 && g.type == T_BL;
        const int sw = (qq >> 1) + 2 * (q & 1);
#pragma unroll
        for (int t = 0; t < 4; ++t)
            c[t] = lds + q * 3072 + qq * 128 + (rm ? ((((t & 2) + (pp >> 1)) ^ sw) << 5) + 16 * (pp & 1) + 8 * ((pp >> 1) ^ (t & 1)) : ((t ^ sw) << 5) + 8 * pp);
        auto rlo = [&](int f) { flo = trread(c[f & 3] + (f / 12) * 12288 + ((f % 12) >> 2) * 1024); };
        auto rhi = [&](int f) {
            const u32x2 hi = trread(c[f & 3] + (f / 12) * 12288 + ((f % 12) >> 2) * 1024 + 512);
            fr[f % (FD + 1)] = u32x4{flo[0], flo[1], hi[0], hi[1]};
        };
        const int xk = uni(g.xb ? 2048 : 64);
        const char *xp = g.x, *xq = xp + xk;
        sfor<0, 4>([&](auto J) {
            if constexpr (J.v < XL) { sfor<0, NX>([&](auto I) { gld16a<0>(xr[J.v][I.v], I.v & 1 ? xq : xp, xo[I.v >> 1]); }); xp += 2 * xk; xq += 2 * xk; }
            wload(J.v);
        });
        waitbar<18 + (XL - 1) * NX>();
        xtie(xr[0]);
#pragma unroll
        for (int i = 0; i < FD; ++i) { rlo(i); rhi(i); }
        char* po = g.out;
#pragma nounroll
        for (int ph = g.np1; ph > 0; --ph) {
            sfor<0, 10>([&](auto J) {
                constexpr int j = J.v;
                u32x4 (&xc)[NX] = xr[j % 5];
                u32x4 (&xn)[NX] = xr[(j + XL) % 5];
                sfor<0, 24>([&](auto I) {
                    constexpr int idx = I.v, kk = idx / 12, T = idx % 12, z = j == 0 && kk == 0, f = (idx + FD) % 24;
                    constexpr int mo = NT == 4 ? (idx < BI && idx % 4 != 2 ? idx - (idx + 1) / 4 : -1) : (idx < BI && idx % 2 == 0 ? idx / 2 : -1);
                    auto mem = [&]() {
                        if constexpr (mo >= NX) gldsm<1024 * (mo - NX - 3)>(wp, wv[mo - NX]);
                        else if constexpr (mo >= 0) gld16a<0>(xn[mo], mo & 1 ? xq : xp, xo[mo >> 1]);
                    };
                    auto misc = [&]() {
                        if constexpr (idx == 1) wring();
                        if constexpr (idx == 13) sring();
                        if constexpr (j == 1 && idx == 22) early_add(g.sig, ph + w);
                        if constexpr (idx >= 15 && idx < BI) { c[idx - 15] += (u32)sd; asm volatile("" : "+v"(c[idx - 15])); }
                        const bool more = ph > 1;
                        if constexpr (j != 5) {
                            if constexpr (idx == 20) wp += wstep;
                        } else {
                            if constexpr (idx == 19) { wp = more ? g.w1b + (size_t)(16 * w) * g.ldw - 2048 : wp2; wstep = more ? wstep : (p2 ? 128 : 0); }
                            if constexpr (idx >= 20 && idx < 23) {
#pragma unroll
                                for (int i = 2 * (idx - 20); i < 2 * (idx - 19); ++i) { wv[i] = more ? wv[i] : (p2 ? v2o(i) : 5120u); asm volatile("" : "+v"(wv[i])); }
                            }
                        }
                        if constexpr (j != 9 - XL) {
                            if constexpr (idx == 21) xp += 2 * xk;
                            if constexpr (idx == 22) xq += 2 * xk;
                        } else if constexpr (idx == 23) {
                            xp = g.x; xq = xp + xk;
#pragma unroll
                            for (int i = 0; i < NT; ++i) { xo[i] = more ? xo[i] : 0u; asm volatile("" : "+v"(xo[i])); }
                        }
                    };
                    if constexpr (idx == BI) { waitbar<6 + (XL - 1) * OPS>(); xtie(xr[(j + 1) % 5]); }
                    if constexpr (idx % 2 == 0) LGKM(6);
                    PIN();
                    const u32x4 a = fr[idx % (FD + 1)];
                    mf1<z>(acc[T][0], a, xc[kk]); PIN();
                    rlo(f);
                    if constexpr (idx == 0) setm0(wdst);
                    if constexpr (NT == 2) rhi(f);
                    PIN();
                    mf1<z>(acc[T][1], a, xc[2 + kk]); PIN();
                    if constexpr (NT == 4) {
                        rhi(f); PIN();
                        mf1<z>(acc[T][2], a, xc[4 + kk]); PIN();
                        mem(); PIN();
                        mf1<z>(acc[T][3], a, xc[6 + kk]); PIN();
                    } else { mem(); PIN(); }
                    misc();
                    PIN();
                });
            });
            asm volatile("s_nop 7\n\ts_nop 1");
            PIN();
#pragma unroll
            for (int tt = 0; tt < NT; ++tt)
#pragma unroll
                for (int ks = 0; ks < 6; ++ks)
                    vf[tt][ks] = u32x4{cvtpk(acc[2 * ks][tt][0], acc[2 * ks][tt][1]), cvtpk(acc[2 * ks][tt][2], acc[2 * ks][tt][3]),
                                       cvtpk(acc[2 * ks + 1][tt][0], acc[2 * ks + 1][tt][1]), cvtpk(acc[2 * ks + 1][tt][2], acc[2 * ks + 1][tt][3])};
            if (g.type == T_QK) {
#pragma unroll
                for (int tt = 0; tt < NT; ++tt)
#pragma unroll
                    for (int ks = 0; ks < 6; ++ks)
                        gst16(po + (size_t)(NT / 2 * w + (tt >> 1)) * (NH * 12288) + (tt & 1) * 6144 + ks * 1024, lane * 16,
                              u32x4{vf[tt][ks][0], vf[tt][ks][2], vf[tt][ks][1], vf[tt][ks][3]});
            } else if (rm) {
                const bool sx = q >= 2;
#pragma unroll
                for (int tt = 0; tt < NT; ++tt)
#pragma unroll
                    for (int ks = 0; ks < 6; ++ks) {
                        const u32x4 v = vf[tt][ks];
                        gst16(po + (size_t)(2 * w + (tt >> 1)) * g.sV + (tt & 1) * 256 + ks * 2048, (u32)(q * 512 + n * 16),
                              u32x4{sx ? v[2] : v[0], sx ? v[3] : v[1], sx ? v[0] : v[2], sx ? v[1] : v[3]});
                    }
            }
            po = g.outb;
            PIN();
        }
    }
    if (p2) {
        constexpr int HI = 2048;
        const int r = 4 * (q & 1) + qq, sw = r >> 1;
#pragma unroll
        for (int t = 0; t < 4; ++t) c[t] = lds + slot + (q >> 1) * 1024 + r * 128 + ((t ^ sw) << 5) + 8 * pp;
        sring();
        u32 so = (u32)((n >> 3) * g.sV + q * g.sQ + (n & 7) * 16);
        char* const o0 = g.out + (size_t)(NT / 2 * w) * g.sP;
        char* const o1 = o0 + g.sP;
        const int sv2 = 2 * g.sV;
        auto rlo = [&](int f) { flo = trread(c[f / 6] + (f % 6) * 4096); };
        auto rhi = [&](int f) {
            const u32x2 hi = trread(c[f / 6] + (f % 6) * 4096 + HI);
            fr[f % (FD + 1)] = u32x4{flo[0], flo[1], hi[0], hi[1]};
        };
        f32x4 a2[2][NT];
        u32 e[NT / 2][4];
        auto cv = [&](f32x4 (&a)[NT], int i) { e[i >> 2][i & 3] = cvtpk(a[i >> 1][2 * (i & 1)], a[i >> 1][2 * (i & 1) + 1]); asm volatile("" : "+v"(e[i >> 2][i & 3])); };
        auto st = [&](int pl) { gst16n(pl ? o1 : o0, so, u32x4{e[pl][0], e[pl][1], e[pl][2], e[pl][3]}); };
        PIN();
#pragma unroll
        for (int i = 0; i < FD; ++i) { rlo(i); rhi(i); }
#pragma unroll
        for (int tt = 0; tt < NT; ++tt) a2[1][tt] = f32x4{0.f, 0.f, 0.f, 0.f};
#pragma nounroll
        for (int t = 0; t < g.tn; ++t) {
            sfor<0, 24>([&](auto I) {
                constexpr int idx = I.v, cb = idx / 6, ks = idx % 6, f = (idx + FD) % 24;
                f32x4 (&ac)[NT] = a2[cb & 1];
                f32x4 (&ap)[NT] = a2[(cb & 1) ^ 1];
                constexpr int mo = cb < 2 ? (ks == 0 ? 3 * cb : (ks >= 4 ? 3 * cb + ks - 3 : -1)) : -1;
                auto mem = [&]() {
                    if constexpr (mo >= 0) gldsm<1024 * (mo - 3)>(wp, wv[mo]);
                    if constexpr (ks == 3 || (NT == 4 && ks == 2)) { if (cb > 0 || t > 0) st(NT == 4 ? ks - 2 : 0); }
                };
                auto misc = [&]() {
                    if constexpr (idx == 12) wring();
                    if constexpr (idx == 23) sring();
                    if constexpr (idx == 16) { wp += t < g.tn - 5 ? 128 : 0; }
                    if constexpr (ks == 0) { c[cb] += (u32)sd; asm volatile("" : "+v"(c[cb])); }
                    if constexpr (ks == 4) { so += (cb > 0 || t > 0) ? (u32)sv2 : 0u; asm volatile("" : "+v"(so)); }
                };
                if constexpr (idx == BI) waitbar<NT == 4 ? 38 : 28>();
                if constexpr (idx % 2 == 0) LGKM(6);
                PIN();
                const u32x4 b = fr[idx % (FD + 1)];
                mf2<ks == 0>(ac[0], vf[0][ks], b); PIN();
                rlo(f);
                if constexpr (idx == 0) setm0(wdst);
                if constexpr (NT == 2) rhi(f);
                if constexpr (NT == 4 && ks >= 1 && ks <= 3) cv(ap, ks == 1 ? 0 : (ks == 2 ? 4 : 7));
                PIN();
                mf2<ks == 0>(ac[1], vf[1][ks], b); PIN();
                if constexpr (NT == 4) {
                    rhi(f);
                    if constexpr (ks == 1 || ks == 2) cv(ap, ks == 1 ? 1 : 5);
                    PIN();
                    mf2<ks == 0>(ac[2], vf[2][ks], b); PIN();
                    mem();
                    if constexpr (ks == 1) { cv(ap, 2); cv(ap, 3); }
                    PIN();
                    mf2<ks == 0>(ac[3], vf[3][ks], b); PIN();
                    if constexpr (ks == 2) cv(ap, 6);
                } else {
                    if constexpr (ks == 1 || ks == 2) { cv(ap, 2 * ks - 2); cv(ap, 2 * ks - 1); }
                    mem(); PIN();
                }
                misc();
                PIN();
            });
            asm volatile("s_nop 7\n\ts_nop 1");
        }
        PIN();
#pragma unroll
        for (int i = 0; i < 2 * NT; ++i) cv(a2[1], i);
#pragma unroll
        for (int pl = 0; pl < NT / 2; ++pl) st(pl);
        asm volatile("s_nop 1");
    }
    if constexpr (NT == 2) asm volatile(".Lkm_g2:"); else asm volatile(".Lkm_g4:");
    sync_ld(y, ws);
    waitvm<0>();
    asm volatile("" : "+v"(y.r), "+v"(y.g), "+v"(y.e));
    sfor<0, 5>([&](auto I) { xtie(xr[I.v]); });
}

struct Ptrs {
    const char *x, *mem0, *wq, *wk, *wv, *wo, *wqkvb, *wob, *gw;
    char* out;
    char* ws;
};

enum { Q_H = 0, Q_VD = 1, Q_KQ = 2, Q_QKVB = 3, Q_BA = 4, Q_BO = 5, Q_PX = 6, Q_OUT = 7 };
DI void run_gemm(const Ptrs& p, int ty, int b, int i, int hf, bool raw, Sync& y, u32 lds, int w) {
    GemmArgs g;
    g.sV = g.sP = g.sQ = 0; g.w2 = p.wo; g.ldw = HD * 2; g.np1 = 1; g.tn = 10;
    g.sig = p.ws + OFF_ST + F_END * 128;
    const char* xf = raw ? p.x : p.ws + OFF_XF;
    g.xb = !raw;
    int s0 = 8 * b;
    if (ty == Q_VD) {
        s0 = b < 2 ? 4 * b : s0 - 8;
        g.type = T_VD; g.w1 = g.w1b = p.wv + i * DH * 2;
        g.w2 = p.wo + (size_t)i * DH * 1280;
        g.out = g.outb = p.ws + OFF_D + (size_t)s0 * D_STRIDE + i * 512;
        g.sV = 16384; g.sP = (int)D_STRIDE; g.sQ = 128;
        if (b == 0) { g.tn = 5; g.w2 += hf * 640; g.out += hf * 40 * 16384; }
    } else if (ty == Q_H) {
        s0 = 0;
        g.type = T_QK; g.w1 = g.w1b = (b ? p.wq : p.wk) + i * DH * 2;
        g.out = g.outb = p.ws + (b ? OFF_QX : OFF_KX) + (size_t)i * 12288;
        b = 0;
    } else if (ty == Q_KQ) {
        s0 = b < 2 ? 4 * b : s0 - 8;
        g.type = T_QK; g.w1 = p.wk + i * DH * 2; g.w1b = p.wq + i * DH * 2;
        g.out = p.ws + OFF_KX + (size_t)(s0 * NH + i) * 12288;
        g.outb = g.out + (OFF_QX - OFF_KX);
        g.np1 = 2;
        g.sig = p.ws + OFF_ST + (F_K + b * 32 + i) * 128;
    } else if (ty == Q_QKVB) {
        g.type = T_BL; g.w1 = p.wqkvb + i * 768; g.w1b = g.w1 + 384; g.ldw = 3840;
        g.sV = 1920 * 64; g.np1 = 2;
        g.out = p.ws + OFF_QKVB + (size_t)s0 * g.sV + i * 48 * 512; g.outb = g.out + 24 * 512;
    } else {
        const int c0 = min(i * DH, DM - DH);
        g.type = T_BL; g.w1 = g.w1b = p.wob + c0 * 2; g.ldw = 1280;
        xf = p.ws + OFF_OB;
        g.sV = 40960;
        g.out = g.outb = p.ws + OFF_BOUT + (size_t)s0 * 40960 + c0 * 64;
    }
    g.x = xf + (size_t)s0 * 40960;
    if (ty <= Q_KQ && b < 2) gemm_task<2>(g, y, p.ws, lds, w);
    else gemm_task<4>(g, y, p.ws, lds, w);
}
DI int pblk(int s) { return s < 8 ? s >> 2 : min(s >> 3, 7) + 1; }

template <int IMM>
DI void gld16c(u32x4& r, const char* sbase, u32 voff) {
    asm volatile("global_load_dwordx4 %0, %1, %2 offset:%3 sc1" : "=v"(r) : "v"(voff), "s"(sbase), "i"(IMM) : "memory");
}
DI void gst4c(char* sbase, u32 voff, u32 v) {
    asm volatile("global_store_dword %0, %1, %2 sc1" : : "v"(voff), "v"(v), "s"(sbase) : "memory");
}
template <int N>
DI void waitv(u32x4& r) { asm volatile("s_waitcnt vmcnt(%1)" : "+v"(r) : "i"(N) : "memory"); }
DI u32 mt() { return (u32)__builtin_amdgcn_s_memtime(); }
DI u32 rt() { return (u32)__builtin_amdgcn_s_memrealtime(); }
DI void spin_to(u32 tgt) { while ((int)(mt() - tgt) < 0) {} }
DI u32 max3u(u32 a, u32 b, u32 c) { return max(max(a, b), c); }
DI u32x4 ldsr16(u32 a) { return *LDSP(u32x4, a); }
DI void clocks(u32& r, u32& m) {
    u64 a, b;
    asm volatile("s_memrealtime %0\n\ts_memtime %1\n\ts_waitcnt lgkmcnt(0)" : "=s"(a), "=s"(b) : : "memory");
    r = (u32)a; m = (u32)b;
}
#define SLATE 20
DI void edge(u32& r, u32& m) {
    u64 a, t[9];
#define RT(i) "s_memrealtime %" #i "\n\ts_nop 1\n\t"
    asm volatile("s_memtime %0\n\t" RT(1) RT(2) RT(3) RT(4) RT(5) RT(6) RT(7) RT(8) "s_memrealtime %9\n\ts_waitcnt lgkmcnt(0)"
                 : "=&s"(a), "=&s"(t[0]), "=&s"(t[1]), "=&s"(t[2]), "=&s"(t[3]), "=&s"(t[4]), "=&s"(t[5]), "=&s"(t[6]), "=&s"(t[7]), "=&s"(t[8]) : : "memory");
    m = (u32)a;
#pragma unroll
    for (int i = 1; i < 9; ++i) m += (u32)t[i] == (u32)t[0] ? 12 : 0;
    r = (u32)t[0] + 4;
}
#define SPB 90
struct Clock {
    u32 r0, m0, r1, m1; int cpt;
    DI void init() { edge(r0, m0); r1 = r0; m1 = m0; cpt = 24 * 64; }
    DI u32 sched(u32& phi, int ofs, int G) {
        edge(r1, m1);
        int rem = (int)(phi + (u32)ofs - r1);
        if (rem < -SLATE) { const int k = (-rem / G + 1) * G; phi += (u32)k; rem += k; }
        return m1 + (u32)((min(rem, 3000) * cpt) >> 6) - SPB;
    }
    DI u32 at(u32 t) { u32 r, m; clocks(r, m); return m + (u32)((min(max((int)(t - r), 0), 3000) * cpt) >> 6); }
    DI void calib() {
        const int dr = (int)(r1 - r0);
        if (dr >= 60) { cpt = uni((int)fminf(fmaxf((float)(int)(m1 - m0) * 64.f * __builtin_amdgcn_rcpf((float)dr), 256.f), 2560.f)); r0 = r1; m0 = m1; }
    }
};
#define VISS 0
#define VISV 0
DI void follow(u32& phi, int ofs, u32 t, int G) { const int late = (int)(t - phi) - ofs; if (late > 0) phi += (u32)((late / G + 1) * G); }
DI bool unwritten(const char* base, u32 off) {
#pragma nounroll
    for (int i = 0; i < 10; ++i) {
        u32x4 t;
        gld16c<-4096>(t, base + (i + (i & 8)) * 1024, off); waitv<0>(t);
        if (__builtin_amdgcn_ballot_w64(max3u(max(t[0], t[1]), t[2], t[3]) == 0xffffffffu) != 0) return true;
    }
    return false;
}

DI size_t uslot(int sd, int s) { const int r = (s * 43) >> 8; return OFF_U + (u32)uni(s * (int)U_STRIDE + r * (int)((2u << 20) - 6 * U_STRIDE) + ((sd >> (2 + r)) & 1) * 8192); }
DI size_t mslot(int sd, int s) { return (u32)uni(s * (int)HO_STRIDE + ((sd >> (s >> 5)) & 1) * 8192 + 4096); }
struct ChainCfg { int T, cR, G, dV, fast, s1, T2, cR2, s2, T3, cR3, dU; };
constexpr int TT_OFF = 88 + 48 + 360;
DI void karg_warm() {
    u32 a[13];
    asm volatile("s_load_dword %0, %13, 0\n\t" "s_load_dword %1, %13, 64\n\t" "s_load_dword %2, %13, 128\n\t" "s_load_dword %3, %13, 192\n\t" "s_load_dword %4, %13, 256\n\t" "s_load_dword %5, %13, 320\n\t" "s_load_dword %6, %13, 384\n\t" "s_load_dword %7, %13, 448\n\t" "s_load_dword %8, %13, 512\n\t" "s_load_dword %9, %13, 576\n\t" "s_load_dword %10, %13, 640\n\t" "s_load_dword %11, %13, 704\n\t" "s_load_dword %12, %13, 748\n\t" "s_waitcnt lgkmcnt(0)"
                 : "=&s"(a[0]), "=&s"(a[1]), "=&s"(a[2]), "=&s"(a[3]), "=&s"(a[4]), "=&s"(a[5]), "=&s"(a[6]), "=&s"(a[7]), "=&s"(a[8]), "=&s"(a[9]), "=&s"(a[10]), "=&s"(a[11]), "=&s"(a[12])
                 : "s"(__builtin_amdgcn_kernarg_segment_ptr()) : "memory");
}
DI u32 sched_ld0(int s) {
    u32 r;
    asm volatile("s_load_dword %0, %1, %2 offset:%3\n\ts_waitcnt lgkmcnt(0)" : "=s"(r) : "s"(__builtin_amdgcn_kernarg_segment_ptr()), "s"(4 * s), "n"(TT_OFF) : "memory");
    return r;
}
#define HP 60
#define HQ 96

constexpr int SL_W = 0;
constexpr int SL_IMG = 131072;
constexpr int SL_KX = SL_IMG + 6656;
constexpr int SL_PF = SL_KX + 12288 + 1024;
constexpr int S_LDS = SL_PF + 256;
DI void nap(int t) { const u32 wake = rt() + (u32)t; while ((int)(rt() - wake) < 0) __builtin_amdgcn_s_sleep(4); }
DI void pfl(const char* sbase, u32 voff, u32 lds) {
    asm volatile("s_mov_b32 m0, %2\n\ts_nop 3\n\tglobal_load_lds_dword %0, %1" : : "v"(voff), "s"(sbase), "s"(lds + SL_PF) : "memory");
}
DI void score_role(const Ptrs& p, const ChainCfg cfg, int h, u32 lds, int w) {
    const int lane = lanex(), tid = 64 * w + lane;
    const int n = lane & 15, g = lane >> 4, qq = (lane >> 2) & 3, pp = lane & 3;
    u32x32 wa[8], wv[3];
    const u32 wreg = lds + SL_W + (u32)w * 32768;
    const u32 lfa = wreg + lane * 16;
    const char* kxp = p.ws + OFF_KX + (size_t)h * 12288 + w * 3072;
    const char* const kxend = p.ws + OFF_QX;
    const u32 kxl = lds + SL_KX;
    char* const fl = p.ws + OFF_ST;
    const u32 kxw = kxl + w * 3072 - (w < 2 ? 16 : 0);
    auto kxdma = [&]() {
        asm volatile("s_mov_b32 m0, %2\n\ts_nop 3\n\tglobal_load_lds_dwordx4 %0, %1 sc1\n\tglobal_load_lds_dwordx4 %0, %1 offset:1024 sc1\n\tglobal_load_lds_dwordx4 %0, %1 offset:2048 sc1"
                     : : "v"(lanex() * 16), "s"(kxp), "s"(kxw) : "memory");
        kxp = kxp + NH * 12288 < kxend ? kxp + NH * 12288 : kxp;
    };
    u32 kfl, phi;
    int sd;
    {
        constexpr int GS = 3104;
        Sync y;
        if (w < 3) {
            gst16(p.ws + OFF_KM + ((NSEG - 1) * NH + h) * 3072 + w * 1024, lane * 16, u32x4{~0u, ~0u, ~0u, ~0u});
            gst16(p.ws + OFF_PX + (NSEG - 1) * 81920 + (w < 2 ? h * 2048 + w * 1024 : 65536 + h * 512), w < 2 ? lane * 16 : (lane * 16) & 511, u32x4{~0u, ~0u, ~0u, ~0u});
        }
        auto dvo = [&](int i) { const int ix = i * 64 + lane; return (u32)((ix / 24) * (HD * 2) + (ix % 24) * 16); };
        const u32 tra = lds + (2 * (g >> 1) + (qq >> 1)) * GS + (4 * (g & 1) + (qq & 1)) * 384 + 8 * pp;
        u32x8 pa = u32x8{dvo(0), dvo(1), dvo(2), tra + 96 * w + 32 * (w & 1), tra + 96 * w - 64 * (w & 1), tra + 32 * w, lfa, (u32)(lane & 31) * 8};
        asm volatile("s_nop 4\n\t" "s_add_u32 s42, s46, 0x0\n\t" "s_addc_u32 s43, s47, 0\n\t" "s_add_u32 m0, %18, 0x0\n\t" "s_nop 0\n\t" "global_load_lds_dwordx4 v224, s[42:43]\n\t" "s_add_u32 m0, %18, 0x400\n\t"
                     "s_nop 0\n\t" "global_load_lds_dwordx4 v225, s[42:43]\n\t" "s_add_u32 m0, %18, 0x800\n\t" "s_nop 0\n\t" "global_load_lds_dwordx4 v226, s[42:43]\n\t" "s_add_u32 s42, s46, 0x18000\n\t"
                     "s_addc_u32 s43, s47, 0\n\t" "s_add_u32 m0, %18, 0xc20\n\t" "s_nop 0\n\t" "global_load_lds_dwordx4 v224, s[42:43]\n\t" "s_add_u32 m0, %18, 0x1020\n\t" "s_nop 0\n\t"
                     "global_load_lds_dwordx4 v225, s[42:43]\n\t" "s_add_u32 m0, %18, 0x1420\n\t" "s_nop 0\n\t" "global_load_lds_dwordx4 v226, s[42:43]\n\t" "s_add_u32 s42, s46, 0xc0000\n\t" "s_addc_u32 s43, s47, 0\n\t"
                     "s_add_u32 m0, %18, 0x6100\n\t" "s_nop 0\n\t" "global_load_lds_dwordx4 v224, s[42:43]\n\t" "s_add_u32 m0, %18, 0x6500\n\t" "s_nop 0\n\t" "global_load_lds_dwordx4 v225, s[42:43]\n\t"
                     "s_add_u32 m0, %18, 0x6900\n\t" "s_nop 0\n\t" "global_load_lds_dwordx4 v226, s[42:43]\n\t" "s_add_u32 s42, s46, 0xd8000\n\t" "s_addc_u32 s43, s47, 0\n\t" "s_add_u32 m0, %18, 0x6d20\n\t" "s_nop 0\n\t"
                     "global_load_lds_dwordx4 v224, s[42:43]\n\t" "s_add_u32 m0, %18, 0x7120\n\t" "s_nop 0\n\t" "global_load_lds_dwordx4 v225, s[42:43]\n\t" "s_add_u32 m0, %18, 0x7520\n\t" "s_nop 0\n\t"
                     "global_load_lds_dwordx4 v226, s[42:43]\n\t" "s_add_u32 s42, s46, 0x180000\n\t" "s_addc_u32 s43, s47, 0\n\t" "s_add_u32 m0, %18, 0xc200\n\t" "s_nop 0\n\t" "global_load_lds_dwordx4 v224, s[42:43]\n\t"
                     "s_add_u32 m0, %18, 0xc600\n\t" "s_nop 0\n\t" "global_load_lds_dwordx4 v225, s[42:43]\n\t" "s_add_u32 m0, %18, 0xca00\n\t" "s_nop 0\n\t" "global_load_lds_dwordx4 v226, s[42:43]\n\t"
                     "s_add_u32 s42, s46, 0x198000\n\t" "s_addc_u32 s43, s47, 0\n\t" "s_add_u32 m0, %18, 0xce20\n\t" "s_nop 0\n\t" "global_load_lds_dwordx4 v224, s[42:43]\n\t" "s_add_u32 m0, %18, 0xd220\n\t" "s_nop 0\n\t"
                     "global_load_lds_dwordx4 v225, s[42:43]\n\t" "s_add_u32 m0, %18, 0xd620\n\t" "s_nop 0\n\t" "global_load_lds_dwordx4 v226, s[42:43]\n\t" "s_add_u32 s42, s46, 0x240000\n\t" "s_addc_u32 s43, s47, 0\n\t"
                     "s_add_u32 m0, %18, 0x12300\n\t" "s_nop 0\n\t" "global_load_lds_dwordx4 v224, s[42:43]\n\t" "s_add_u32 m0, %18, 0x12700\n\t" "s_nop 0\n\t" "global_load_lds_dwordx4 v225, s[42:43]\n\t"
                     "s_add_u32 m0, %18, 0x12b00\n\t" "s_nop 0\n\t" "global_load_lds_dwordx4 v226, s[42:43]\n\t" "s_add_u32 s42, s46, 0x258000\n\t" "s_addc_u32 s43, s47, 0\n\t" "s_add_u32 m0, %18, 0x12f20\n\t"
                     "s_nop 0\n\t" "global_load_lds_dwordx4 v224, s[42:43]\n\t" "s_add_u32 m0, %18, 0x13320\n\t" "s_nop 0\n\t" "global_load_lds_dwordx4 v225, s[42:43]\n\t" "s_add_u32 m0, %18, 0x13720\n\t" "s_nop 0\n\t"
                     "global_load_lds_dwordx4 v226, s[42:43]\n\t" "s_waitcnt vmcnt(12) lgkmcnt(0)\n\t" "s_barrier\n\t" "s_add_u32 s42, s46, 0x300000\n\t" "s_addc_u32 s43, s47, 0\n\t" "s_add_u32 m0, %18, 0x18400\n\t"
                     "s_nop 0\n\t" "global_load_lds_dwordx4 v224, s[42:43]\n\t" "s_add_u32 m0, %18, 0x18800\n\t" "s_nop 0\n\t" "global_load_lds_dwordx4 v225, s[42:43]\n\t" "s_add_u32 m0, %18, 0x18c00\n\t" "s_nop 0\n\t"
                     "global_load_lds_dwordx4 v226, s[42:43]\n\t" "s_add_u32 s42, s46, 0x318000\n\t" "s_addc_u32 s43, s47, 0\n\t" "s_add_u32 m0, %18, 0x19020\n\t" "s_nop 0\n\t" "global_load_lds_dwordx4 v224, s[42:43]\n\t"
                     "s_add_u32 m0, %18, 0x19420\n\t" "s_nop 0\n\t" "global_load_lds_dwordx4 v225, s[42:43]\n\t" "s_add_u32 m0, %18, 0x19820\n\t" "s_nop 0\n\t" "global_load_lds_dwordx4 v226, s[42:43]\n\t"
                     "s_add_u32 s42, s46, 0x3c0000\n\t" "s_addc_u32 s43, s47, 0\n\t" "s_add_u32 m0, %18, 0x1e500\n\t" "s_nop 0\n\t" "global_load_lds_dwordx4 v224, s[42:43]\n\t" "s_add_u32 m0, %18, 0x1e900\n\t"
                     "s_nop 0\n\t" "global_load_lds_dwordx4 v225, s[42:43]\n\t" "s_add_u32 m0, %18, 0x1ed00\n\t" "s_nop 0\n\t" "global_load_lds_dwordx4 v226, s[42:43]\n\t" "s_add_u32 s42, s46, 0x3d8000\n\t"
                     "s_addc_u32 s43, s47, 0\n\t" "s_add_u32 m0, %18, 0x1f120\n\t" "s_nop 0\n\t" "global_load_lds_dwordx4 v224, s[42:43]\n\t" "s_add_u32 m0, %18, 0x1f520\n\t" "s_nop 0\n\t"
                     "global_load_lds_dwordx4 v225, s[42:43]\n\t" "s_add_u32 m0, %18, 0x1f920\n\t" "s_nop 0\n\t" "global_load_lds_dwordx4 v226, s[42:43]\n\t" "v_add_u32 v232, 0x0, v229\n\t"
                     "ds_read_b64_tr_b16 v[8:9], v232\n\t" "ds_read_b64_tr_b16 v[10:11], v232 offset:768\n\t" "ds_read_b64_tr_b16 v[12:13], v232 offset:12416\n\t" "ds_read_b64_tr_b16 v[14:15], v232 offset:13184\n\t"
                     "ds_read_b64_tr_b16 a[96:97], v232 offset:128\n\t" "ds_read_b64_tr_b16 a[98:99], v232 offset:896\n\t" "ds_read_b64_tr_b16 a[100:101], v232 offset:12544\n\t"
                     "ds_read_b64_tr_b16 a[102:103], v232 offset:13312\n\t" "ds_read_b64_tr_b16 a[176:177], v232 offset:256\n\t" "ds_read_b64_tr_b16 a[178:179], v232 offset:1024\n\t"
                     "ds_read_b64_tr_b16 a[180:181], v232 offset:12672\n\t" "ds_read_b64_tr_b16 a[182:183], v232 offset:13440\n\t" "ds_read_b64_tr_b16 v[24:25], v232 offset:24832\n\t"
                     "ds_read_b64_tr_b16 v[26:27], v232 offset:25600\n\t" "ds_read_b64_tr_b16 v[28:29], v232 offset:37248\n\t" "ds_read_b64_tr_b16 v[30:31], v232 offset:38016\n\t"
                     "ds_read_b64_tr_b16 a[104:105], v232 offset:24960\n\t" "ds_read_b64_tr_b16 a[106:107], v232 offset:25728\n\t" "ds_read_b64_tr_b16 a[108:109], v232 offset:37376\n\t"
                     "ds_read_b64_tr_b16 a[110:111], v232 offset:38144\n\t" "ds_read_b64_tr_b16 a[184:185], v232 offset:25088\n\t" "ds_read_b64_tr_b16 a[186:187], v232 offset:25856\n\t"
                     "ds_read_b64_tr_b16 a[188:189], v232 offset:37504\n\t" "ds_read_b64_tr_b16 a[190:191], v232 offset:38272\n\t" "s_waitcnt vmcnt(12) lgkmcnt(0)\n\t" "s_barrier\n\t" "s_add_u32 s42, s46, 0x480000\n\t"
                     "s_addc_u32 s43, s47, 0\n\t" "s_add_u32 m0, %18, 0x0\n\t" "s_nop 0\n\t" "global_load_lds_dwordx4 v224, s[42:43]\n\t" "s_add_u32 m0, %18, 0x400\n\t" "s_nop 0\n\t"
                     "global_load_lds_dwordx4 v225, s[42:43]\n\t" "s_add_u32 m0, %18, 0x800\n\t" "s_nop 0\n\t" "global_load_lds_dwordx4 v226, s[42:43]\n\t" "s_add_u32 s42, s46, 0x498000\n\t" "s_addc_u32 s43, s47, 0\n\t"
                     "s_add_u32 m0, %18, 0xc20\n\t" "s_nop 0\n\t" "global_load_lds_dwordx4 v224, s[42:43]\n\t" "s_add_u32 m0, %18, 0x1020\n\t" "s_nop 0\n\t" "global_load_lds_dwordx4 v225, s[42:43]\n\t"
                     "s_add_u32 m0, %18, 0x1420\n\t" "s_nop 0\n\t" "global_load_lds_dwordx4 v226, s[42:43]\n\t" "s_add_u32 s42, s46, 0x540000\n\t" "s_addc_u32 s43, s47, 0\n\t" "s_add_u32 m0, %18, 0x6100\n\t" "s_nop 0\n\t"
                     "global_load_lds_dwordx4 v224, s[42:43]\n\t" "s_add_u32 m0, %18, 0x6500\n\t" "s_nop 0\n\t" "global_load_lds_dwordx4 v225, s[42:43]\n\t" "s_add_u32 m0, %18, 0x6900\n\t" "s_nop 0\n\t"
                     "global_load_lds_dwordx4 v226, s[42:43]\n\t" "s_add_u32 s42, s46, 0x558000\n\t" "s_addc_u32 s43, s47, 0\n\t" "s_add_u32 m0, %18, 0x6d20\n\t" "s_nop 0\n\t" "global_load_lds_dwordx4 v224, s[42:43]\n\t"
                     "s_add_u32 m0, %18, 0x7120\n\t" "s_nop 0\n\t" "global_load_lds_dwordx4 v225, s[42:43]\n\t" "s_add_u32 m0, %18, 0x7520\n\t" "s_nop 0\n\t" "global_load_lds_dwordx4 v226, s[42:43]\n\t"
                     "v_add_u32 v232, 0xc200, v229\n\t" "ds_read_b64_tr_b16 v[40:41], v232\n\t" "ds_read_b64_tr_b16 v[42:43], v232 offset:768\n\t" "ds_read_b64_tr_b16 v[44:45], v232 offset:12416\n\t"
                     "ds_read_b64_tr_b16 v[46:47], v232 offset:13184\n\t" "ds_read_b64_tr_b16 a[112:113], v232 offset:128\n\t" "ds_read_b64_tr_b16 a[114:115], v232 offset:896\n\t"
                     "ds_read_b64_tr_b16 a[116:117], v232 offset:12544\n\t" "ds_read_b64_tr_b16 a[118:119], v232 offset:13312\n\t" "ds_read_b64_tr_b16 a[192:193], v232 offset:256\n\t"
                     "ds_read_b64_tr_b16 a[194:195], v232 offset:1024\n\t" "ds_read_b64_tr_b16 a[196:197], v232 offset:12672\n\t" "ds_read_b64_tr_b16 a[198:199], v232 offset:13440\n\t"
                     "ds_read_b64_tr_b16 v[56:57], v232 offset:24832\n\t" "ds_read_b64_tr_b16 v[58:59], v232 offset:25600\n\t" "ds_read_b64_tr_b16 v[60:61], v232 offset:37248\n\t"
                     "ds_read_b64_tr_b16 v[62:63], v232 offset:38016\n\t" "ds_read_b64_tr_b16 a[120:121], v232 offset:24960\n\t" "ds_read_b64_tr_b16 a[122:123], v232 offset:25728\n\t"
                     "ds_read_b64_tr_b16 a[124:125], v232 offset:37376\n\t" "ds_read_b64_tr_b16 a[126:127], v232 offset:38144\n\t" "ds_read_b64_tr_b16 a[200:201], v232 offset:25088\n\t"
                     "ds_read_b64_tr_b16 a[202:203], v232 offset:25856\n\t" "ds_read_b64_tr_b16 a[204:205], v232 offset:37504\n\t" "ds_read_b64_tr_b16 a[206:207], v232 offset:38272\n\t" "s_waitcnt vmcnt(12) lgkmcnt(0)\n\t"
                     "s_barrier\n\t" "s_add_u32 s42, s46, 0x600000\n\t" "s_addc_u32 s43, s47, 0\n\t" "s_add_u32 m0, %18, 0xc200\n\t" "s_nop 0\n\t" "global_load_lds_dwordx4 v224, s[42:43]\n\t"
                     "s_add_u32 m0, %18, 0xc600\n\t" "s_nop 0\n\t" "global_load_lds_dwordx4 v225, s[42:43]\n\t" "s_add_u32 m0, %18, 0xca00\n\t" "s_nop 0\n\t" "global_load_lds_dwordx4 v226, s[42:43]\n\t"
                     "s_add_u32 s42, s46, 0x618000\n\t" "s_addc_u32 s43, s47, 0\n\t" "s_add_u32 m0, %18, 0xce20\n\t" "s_nop 0\n\t" "global_load_lds_dwordx4 v224, s[42:43]\n\t" "s_add_u32 m0, %18, 0xd220\n\t" "s_nop 0\n\t"
                     "global_load_lds_dwordx4 v225, s[42:43]\n\t" "s_add_u32 m0, %18, 0xd620\n\t" "s_nop 0\n\t" "global_load_lds_dwordx4 v226, s[42:43]\n\t" "s_add_u32 s42, s46, 0x6c0000\n\t" "s_addc_u32 s43, s47, 0\n\t"
                     "s_add_u32 m0, %18, 0x12300\n\t" "s_nop 0\n\t" "global_load_lds_dwordx4 v224, s[42:43]\n\t" "s_add_u32 m0, %18, 0x12700\n\t" "s_nop 0\n\t" "global_load_lds_dwordx4 v225, s[42:43]\n\t"
                     "s_add_u32 m0, %18, 0x12b00\n\t" "s_nop 0\n\t" "global_load_lds_dwordx4 v226, s[42:43]\n\t" "s_add_u32 s42, s46, 0x6d8000\n\t" "s_addc_u32 s43, s47, 0\n\t" "s_add_u32 m0, %18, 0x12f20\n\t"
                     "s_nop 0\n\t" "global_load_lds_dwordx4 v224, s[42:43]\n\t" "s_add_u32 m0, %18, 0x13320\n\t" "s_nop 0\n\t" "global_load_lds_dwordx4 v225, s[42:43]\n\t" "s_add_u32 m0, %18, 0x13720\n\t" "s_nop 0\n\t"
                     "global_load_lds_dwordx4 v226, s[42:43]\n\t" "v_add_u32 v232, 0x18400, v229\n\t" "ds_read_b64_tr_b16 v[72:73], v232\n\t" "ds_read_b64_tr_b16 v[74:75], v232 offset:768\n\t"
                     "ds_read_b64_tr_b16 v[76:77], v232 offset:12416\n\t" "ds_read_b64_tr_b16 v[78:79], v232 offset:13184\n\t" "ds_read_b64_tr_b16 a[128:129], v232 offset:128\n\t"
                     "ds_read_b64_tr_b16 a[130:131], v232 offset:896\n\t" "ds_read_b64_tr_b16 a[132:133], v232 offset:12544\n\t" "ds_read_b64_tr_b16 a[134:135], v232 offset:13312\n\t"
                     "ds_read_b64_tr_b16 a[208:209], v232 offset:256\n\t" "ds_read_b64_tr_b16 a[210:211], v232 offset:1024\n\t" "ds_read_b64_tr_b16 a[212:213], v232 offset:12672\n\t"
                     "ds_read_b64_tr_b16 a[214:215], v232 offset:13440\n\t" "ds_read_b64_tr_b16 v[88:89], v232 offset:24832\n\t" "ds_read_b64_tr_b16 v[90:91], v232 offset:25600\n\t"
                     "ds_read_b64_tr_b16 v[92:93], v232 offset:37248\n\t" "ds_read_b64_tr_b16 v[94:95], v232 offset:38016\n\t" "ds_read_b64_tr_b16 a[136:137], v232 offset:24960\n\t"
                     "ds_read_b64_tr_b16 a[138:139], v232 offset:25728\n\t" "ds_read_b64_tr_b16 a[140:141], v232 offset:37376\n\t" "ds_read_b64_tr_b16 a[142:143], v232 offset:38144\n\t"
                     "ds_read_b64_tr_b16 a[216:217], v232 offset:25088\n\t" "ds_read_b64_tr_b16 a[218:219], v232 offset:25856\n\t" "ds_read_b64_tr_b16 a[220:221], v232 offset:37504\n\t"
                     "ds_read_b64_tr_b16 a[222:223], v232 offset:38272\n\t" "s_waitcnt vmcnt(12) lgkmcnt(0)\n\t" "s_barrier\n\t" "s_add_u32 s42, s44, 0x0\n\t" "s_addc_u32 s43, s45, 0\n\t" "s_add_u32 m0, %18, 0x18400\n\t"
                     "s_nop 0\n\t" "global_load_lds_dwordx4 v224, s[42:43]\n\t" "s_add_u32 m0, %18, 0x18800\n\t" "s_nop 0\n\t" "global_load_lds_dwordx4 v225, s[42:43]\n\t" "s_add_u32 m0, %18, 0x18c00\n\t" "s_nop 0\n\t"
                     "global_load_lds_dwordx4 v226, s[42:43]\n\t" "s_add_u32 s42, s44, 0x18000\n\t" "s_addc_u32 s43, s45, 0\n\t" "s_add_u32 m0, %18, 0x19020\n\t" "s_nop 0\n\t" "global_load_lds_dwordx4 v224, s[42:43]\n\t"
                     "s_add_u32 m0, %18, 0x19420\n\t" "s_nop 0\n\t" "global_load_lds_dwordx4 v225, s[42:43]\n\t" "s_add_u32 m0, %18, 0x19820\n\t" "s_nop 0\n\t" "global_load_lds_dwordx4 v226, s[42:43]\n\t"
                     "s_add_u32 s42, s44, 0xc0000\n\t" "s_addc_u32 s43, s45, 0\n\t" "s_add_u32 m0, %18, 0x1e500\n\t" "s_nop 0\n\t" "global_load_lds_dwordx4 v224, s[42:43]\n\t" "s_add_u32 m0, %18, 0x1e900\n\t" "s_nop 0\n\t"
                     "global_load_lds_dwordx4 v225, s[42:43]\n\t" "s_add_u32 m0, %18, 0x1ed00\n\t" "s_nop 0\n\t" "global_load_lds_dwordx4 v226, s[42:43]\n\t" "s_add_u32 s42, s44, 0xd8000\n\t" "s_addc_u32 s43, s45, 0\n\t"
                     "s_add_u32 m0, %18, 0x1f120\n\t" "s_nop 0\n\t" "global_load_lds_dwordx4 v224, s[42:43]\n\t" "s_add_u32 m0, %18, 0x1f520\n\t" "s_nop 0\n\t" "global_load_lds_dwordx4 v225, s[42:43]\n\t"
                     "s_add_u32 m0, %18, 0x1f920\n\t" "s_nop 0\n\t" "global_load_lds_dwordx4 v226, s[42:43]\n\t" "v_add_u32 v232, 0x0, v229\n\t" "ds_read_b64_tr_b16 v[104:105], v232\n\t"
                     "ds_read_b64_tr_b16 v[106:107], v232 offset:768\n\t" "ds_read_b64_tr_b16 v[108:109], v232 offset:12416\n\t" "ds_read_b64_tr_b16 v[110:111], v232 offset:13184\n\t"
                     "ds_read_b64_tr_b16 a[144:145], v232 offset:128\n\t" "ds_read_b64_tr_b16 a[146:147], v232 offset:896\n\t" "ds_read_b64_tr_b16 a[148:149], v232 offset:12544\n\t"
                     "ds_read_b64_tr_b16 a[150:151], v232 offset:13312\n\t" "ds_read_b64_tr_b16 a[224:225], v232 offset:256\n\t" "ds_read_b64_tr_b16 a[226:227], v232 offset:1024\n\t"
                     "ds_read_b64_tr_b16 a[228:229], v232 offset:12672\n\t" "ds_read_b64_tr_b16 a[230:231], v232 offset:13440\n\t" "ds_read_b64_tr_b16 v[120:121], v232 offset:24832\n\t"
                     "ds_read_b64_tr_b16 v[122:123], v232 offset:25600\n\t" "ds_read_b64_tr_b16 v[124:125], v232 offset:37248\n\t" "ds_read_b64_tr_b16 v[126:127], v232 offset:38016\n\t"
                     "ds_read_b64_tr_b16 a[152:153], v232 offset:24960\n\t" "ds_read_b64_tr_b16 a[154:155], v232 offset:25728\n\t" "ds_read_b64_tr_b16 a[156:157], v232 offset:37376\n\t"
                     "ds_read_b64_tr_b16 a[158:159], v232 offset:38144\n\t" "ds_read_b64_tr_b16 a[232:233], v232 offset:25088\n\t" "ds_read_b64_tr_b16 a[234:235], v232 offset:25856\n\t"
                     "ds_read_b64_tr_b16 a[236:237], v232 offset:37504\n\t" "ds_read_b64_tr_b16 a[238:239], v232 offset:38272\n\t" "s_waitcnt vmcnt(12) lgkmcnt(0)\n\t" "s_barrier\n\t" "s_add_u32 s42, s44, 0x180000\n\t"
                     "s_addc_u32 s43, s45, 0\n\t" "s_add_u32 m0, %18, 0x0\n\t" "s_nop 0\n\t" "global_load_lds_dwordx4 v224, s[42:43]\n\t" "s_add_u32 m0, %18, 0x400\n\t" "s_nop 0\n\t"
                     "global_load_lds_dwordx4 v225, s[42:43]\n\t" "s_add_u32 m0, %18, 0x800\n\t" "s_nop 0\n\t" "global_load_lds_dwordx4 v226, s[42:43]\n\t" "s_add_u32 s42, s44, 0x198000\n\t" "s_addc_u32 s43, s45, 0\n\t"
                     "s_add_u32 m0, %18, 0xc20\n\t" "s_nop 0\n\t" "global_load_lds_dwordx4 v224, s[42:43]\n\t" "s_add_u32 m0, %18, 0x1020\n\t" "s_nop 0\n\t" "global_load_lds_dwordx4 v225, s[42:43]\n\t"
                     "s_add_u32 m0, %18, 0x1420\n\t" "s_nop 0\n\t" "global_load_lds_dwordx4 v226, s[42:43]\n\t" "s_add_u32 s42, s44, 0x240000\n\t" "s_addc_u32 s43, s45, 0\n\t" "s_add_u32 m0, %18, 0x6100\n\t" "s_nop 0\n\t"
                     "global_load_lds_dwordx4 v224, s[42:43]\n\t" "s_add_u32 m0, %18, 0x6500\n\t" "s_nop 0\n\t" "global_load_lds_dwordx4 v225, s[42:43]\n\t" "s_add_u32 m0, %18, 0x6900\n\t" "s_nop 0\n\t"
                     "global_load_lds_dwordx4 v226, s[42:43]\n\t" "s_add_u32 s42, s44, 0x258000\n\t" "s_addc_u32 s43, s45, 0\n\t" "s_add_u32 m0, %18, 0x6d20\n\t" "s_nop 0\n\t" "global_load_lds_dwordx4 v224, s[42:43]\n\t"
                     "s_add_u32 m0, %18, 0x7120\n\t" "s_nop 0\n\t" "global_load_lds_dwordx4 v225, s[42:43]\n\t" "s_add_u32 m0, %18, 0x7520\n\t" "s_nop 0\n\t" "global_load_lds_dwordx4 v226, s[42:43]\n\t"
                     "v_add_u32 v232, 0xc200, v229\n\t" "ds_read_b64_tr_b16 a[56:57], v232\n\t" "ds_read_b64_tr_b16 a[58:59], v232 offset:768\n\t" "ds_read_b64_tr_b16 a[60:61], v232 offset:12416\n\t"
                     "ds_read_b64_tr_b16 a[62:63], v232 offset:13184\n\t" "ds_read_b64_tr_b16 a[160:161], v232 offset:128\n\t" "ds_read_b64_tr_b16 a[162:163], v232 offset:896\n\t"
                     "ds_read_b64_tr_b16 a[164:165], v232 offset:12544\n\t" "ds_read_b64_tr_b16 a[166:167], v232 offset:13312\n\t" "ds_read_b64_tr_b16 a[240:241], v232 offset:256\n\t"
                     "ds_read_b64_tr_b16 a[242:243], v232 offset:1024\n\t" "ds_read_b64_tr_b16 a[244:245], v232 offset:12672\n\t" "ds_read_b64_tr_b16 a[246:247], v232 offset:13440\n\t"
                     "ds_read_b64_tr_b16 a[88:89], v232 offset:24832\n\t" "ds_read_b64_tr_b16 a[90:91], v232 offset:25600\n\t" "ds_read_b64_tr_b16 a[92:93], v232 offset:37248\n\t"
                     "ds_read_b64_tr_b16 a[94:95], v232 offset:38016\n\t" "ds_read_b64_tr_b16 a[168:169], v232 offset:24960\n\t" "ds_read_b64_tr_b16 a[170:171], v232 offset:25728\n\t"
                     "ds_read_b64_tr_b16 a[172:173], v232 offset:37376\n\t" "ds_read_b64_tr_b16 a[174:175], v232 offset:38144\n\t" "ds_read_b64_tr_b16 a[248:249], v232 offset:25088\n\t"
                     "ds_read_b64_tr_b16 a[250:251], v232 offset:25856\n\t" "ds_read_b64_tr_b16 a[252:253], v232 offset:37504\n\t" "ds_read_b64_tr_b16 a[254:255], v232 offset:38272\n\t" "s_waitcnt vmcnt(12) lgkmcnt(0)\n\t"
                     "s_barrier\n\t" "s_add_u32 s42, s44, 0x300000\n\t" "s_addc_u32 s43, s45, 0\n\t" "s_add_u32 m0, %18, 0xc200\n\t" "s_nop 0\n\t" "global_load_lds_dwordx4 v224, s[42:43]\n\t"
                     "s_add_u32 m0, %18, 0xc600\n\t" "s_nop 0\n\t" "global_load_lds_dwordx4 v225, s[42:43]\n\t" "s_add_u32 m0, %18, 0xca00\n\t" "s_nop 0\n\t" "global_load_lds_dwordx4 v226, s[42:43]\n\t"
                     "s_add_u32 s42, s44, 0x318000\n\t" "s_addc_u32 s43, s45, 0\n\t" "s_add_u32 m0, %18, 0xce20\n\t" "s_nop 0\n\t" "global_load_lds_dwordx4 v224, s[42:43]\n\t" "s_add_u32 m0, %18, 0xd220\n\t" "s_nop 0\n\t"
                     "global_load_lds_dwordx4 v225, s[42:43]\n\t" "s_add_u32 m0, %18, 0xd620\n\t" "s_nop 0\n\t" "global_load_lds_dwordx4 v226, s[42:43]\n\t" "s_add_u32 s42, s44, 0x3c0000\n\t" "s_addc_u32 s43, s45, 0\n\t"
                     "s_add_u32 m0, %18, 0x12300\n\t" "s_nop 0\n\t" "global_load_lds_dwordx4 v224, s[42:43]\n\t" "s_add_u32 m0, %18, 0x12700\n\t" "s_nop 0\n\t" "global_load_lds_dwordx4 v225, s[42:43]\n\t"
                     "s_add_u32 m0, %18, 0x12b00\n\t" "s_nop 0\n\t" "global_load_lds_dwordx4 v226, s[42:43]\n\t" "s_add_u32 s42, s44, 0x3d8000\n\t" "s_addc_u32 s43, s45, 0\n\t" "s_add_u32 m0, %18, 0x12f20\n\t"
                     "s_nop 0\n\t" "global_load_lds_dwordx4 v224, s[42:43]\n\t" "s_add_u32 m0, %18, 0x13320\n\t" "s_nop 0\n\t" "global_load_lds_dwordx4 v225, s[42:43]\n\t" "s_add_u32 m0, %18, 0x13720\n\t" "s_nop 0\n\t"
                     "global_load_lds_dwordx4 v226, s[42:43]\n\t" "v_add_u32 v232, 0x18400, v227\n\t" "v_add_u32 v233, 0x18400, v228\n\t" "ds_read_b64_tr_b16 v[128:129], v232\n\t"
                     "ds_read_b64_tr_b16 v[130:131], v232 offset:768\n\t" "ds_read_b64_tr_b16 v[132:133], v232 offset:12416\n\t" "ds_read_b64_tr_b16 v[134:135], v232 offset:13184\n\t"
                     "ds_read_b64_tr_b16 v[0:1], v232 offset:32\n\t" "ds_read_b64_tr_b16 v[2:3], v232 offset:800\n\t" "ds_read_b64_tr_b16 v[4:5], v232 offset:12448\n\t" "ds_read_b64_tr_b16 v[6:7], v232 offset:13216\n\t"
                     "ds_read_b64_tr_b16 v[136:137], v233 offset:64\n\t" "ds_read_b64_tr_b16 v[138:139], v233 offset:832\n\t" "ds_read_b64_tr_b16 v[140:141], v233 offset:12480\n\t"
                     "ds_read_b64_tr_b16 v[142:143], v233 offset:13248\n\t" "ds_read_b64_tr_b16 v[144:145], v232 offset:24832\n\t" "ds_read_b64_tr_b16 v[146:147], v232 offset:25600\n\t"
                     "ds_read_b64_tr_b16 v[148:149], v232 offset:37248\n\t" "ds_read_b64_tr_b16 v[150:151], v232 offset:38016\n\t" "ds_read_b64_tr_b16 v[16:17], v232 offset:24864\n\t"
                     "ds_read_b64_tr_b16 v[18:19], v232 offset:25632\n\t" "ds_read_b64_tr_b16 v[20:21], v232 offset:37280\n\t" "ds_read_b64_tr_b16 v[22:23], v232 offset:38048\n\t"
                     "ds_read_b64_tr_b16 v[152:153], v233 offset:24896\n\t" "ds_read_b64_tr_b16 v[154:155], v233 offset:25664\n\t" "ds_read_b64_tr_b16 v[156:157], v233 offset:37312\n\t"
                     "ds_read_b64_tr_b16 v[158:159], v233 offset:38080\n\t" "s_waitcnt vmcnt(12) lgkmcnt(0)\n\t" "s_barrier\n\t" "v_mov_b32 %14, 0\n\t" "global_load_dwordx2 %11, v231, %19 sc1\n\t"
                     "global_load_dwordx2 %12, %14, %19 offset:512 sc1\n\t" "global_load_dwordx2 %13, %14, %19 offset:1024 sc1\n\t" "s_add_u32 s42, s44, 0x480000\n\t" "s_addc_u32 s43, s45, 0\n\t"
                     "s_add_u32 m0, %18, 0x18400\n\t" "s_nop 0\n\t" "global_load_lds_dwordx4 v224, s[42:43]\n\t" "s_add_u32 m0, %18, 0x18800\n\t" "s_nop 0\n\t" "global_load_lds_dwordx4 v225, s[42:43]\n\t"
                     "s_add_u32 m0, %18, 0x18c00\n\t" "s_nop 0\n\t" "global_load_lds_dwordx4 v226, s[42:43]\n\t" "s_add_u32 s42, s44, 0x498000\n\t" "s_addc_u32 s43, s45, 0\n\t" "s_add_u32 m0, %18, 0x19020\n\t"
                     "s_nop 0\n\t" "global_load_lds_dwordx4 v224, s[42:43]\n\t" "s_add_u32 m0, %18, 0x19420\n\t" "s_nop 0\n\t" "global_load_lds_dwordx4 v225, s[42:43]\n\t" "s_add_u32 m0, %18, 0x19820\n\t" "s_nop 0\n\t"
                     "global_load_lds_dwordx4 v226, s[42:43]\n\t" "s_add_u32 s42, s44, 0x540000\n\t" "s_addc_u32 s43, s45, 0\n\t" "s_add_u32 m0, %18, 0x1e500\n\t" "s_nop 0\n\t" "global_load_lds_dwordx4 v224, s[42:43]\n\t"
                     "s_add_u32 m0, %18, 0x1e900\n\t" "s_nop 0\n\t" "global_load_lds_dwordx4 v225, s[42:43]\n\t" "s_add_u32 m0, %18, 0x1ed00\n\t" "s_nop 0\n\t" "global_load_lds_dwordx4 v226, s[42:43]\n\t"
                     "s_add_u32 s42, s44, 0x558000\n\t" "s_addc_u32 s43, s45, 0\n\t" "s_add_u32 m0, %18, 0x1f120\n\t" "s_nop 0\n\t" "global_load_lds_dwordx4 v224, s[42:43]\n\t" "s_add_u32 m0, %18, 0x1f520\n\t"
                     "s_nop 0\n\t" "global_load_lds_dwordx4 v225, s[42:43]\n\t" "s_add_u32 m0, %18, 0x1f920\n\t" "s_nop 0\n\t" "global_load_lds_dwordx4 v226, s[42:43]\n\t" "v_add_u32 v232, 0x0, v227\n\t"
                     "v_add_u32 v233, 0x0, v228\n\t" "ds_read_b64_tr_b16 v[160:161], v232\n\t" "ds_read_b64_tr_b16 v[162:163], v232 offset:768\n\t" "ds_read_b64_tr_b16 v[164:165], v232 offset:12416\n\t"
                     "ds_read_b64_tr_b16 v[166:167], v232 offset:13184\n\t" "ds_read_b64_tr_b16 v[32:33], v232 offset:32\n\t" "ds_read_b64_tr_b16 v[34:35], v232 offset:800\n\t"
                     "ds_read_b64_tr_b16 v[36:37], v232 offset:12448\n\t" "ds_read_b64_tr_b16 v[38:39], v232 offset:13216\n\t" "ds_read_b64_tr_b16 v[168:169], v233 offset:64\n\t"
                     "ds_read_b64_tr_b16 v[170:171], v233 offset:832\n\t" "ds_read_b64_tr_b16 v[172:173], v233 offset:12480\n\t" "ds_read_b64_tr_b16 v[174:175], v233 offset:13248\n\t"
                     "ds_read_b64_tr_b16 v[176:177], v232 offset:24832\n\t" "ds_read_b64_tr_b16 v[178:179], v232 offset:25600\n\t" "ds_read_b64_tr_b16 v[180:181], v232 offset:37248\n\t"
                     "ds_read_b64_tr_b16 v[182:183], v232 offset:38016\n\t" "ds_read_b64_tr_b16 v[48:49], v232 offset:24864\n\t" "ds_read_b64_tr_b16 v[50:51], v232 offset:25632\n\t"
                     "ds_read_b64_tr_b16 v[52:53], v232 offset:37280\n\t" "ds_read_b64_tr_b16 v[54:55], v232 offset:38048\n\t" "ds_read_b64_tr_b16 v[184:185], v233 offset:24896\n\t"
                     "ds_read_b64_tr_b16 v[186:187], v233 offset:25664\n\t" "ds_read_b64_tr_b16 v[188:189], v233 offset:37312\n\t" "ds_read_b64_tr_b16 v[190:191], v233 offset:38080\n\t" "s_waitcnt vmcnt(12) lgkmcnt(0)\n\t"
                     "s_barrier\n\t" "s_add_u32 s42, s44, 0x600000\n\t" "s_addc_u32 s43, s45, 0\n\t" "s_add_u32 m0, %18, 0x0\n\t" "s_nop 0\n\t" "global_load_lds_dwordx4 v224, s[42:43]\n\t" "s_add_u32 m0, %18, 0x400\n\t"
                     "s_nop 0\n\t" "global_load_lds_dwordx4 v225, s[42:43]\n\t" "s_add_u32 m0, %18, 0x800\n\t" "s_nop 0\n\t" "global_load_lds_dwordx4 v226, s[42:43]\n\t" "s_add_u32 s42, s44, 0x618000\n\t"
                     "s_addc_u32 s43, s45, 0\n\t" "s_add_u32 m0, %18, 0xc20\n\t" "s_nop 0\n\t" "global_load_lds_dwordx4 v224, s[42:43]\n\t" "s_add_u32 m0, %18, 0x1020\n\t" "s_nop 0\n\t"
                     "global_load_lds_dwordx4 v225, s[42:43]\n\t" "s_add_u32 m0, %18, 0x1420\n\t" "s_nop 0\n\t" "global_load_lds_dwordx4 v226, s[42:43]\n\t" "s_add_u32 s42, s44, 0x6c0000\n\t" "s_addc_u32 s43, s45, 0\n\t"
                     "s_add_u32 m0, %18, 0x6100\n\t" "s_nop 0\n\t" "global_load_lds_dwordx4 v224, s[42:43]\n\t" "s_add_u32 m0, %18, 0x6500\n\t" "s_nop 0\n\t" "global_load_lds_dwordx4 v225, s[42:43]\n\t"
                     "s_add_u32 m0, %18, 0x6900\n\t" "s_nop 0\n\t" "global_load_lds_dwordx4 v226, s[42:43]\n\t" "s_add_u32 s42, s44, 0x6d8000\n\t" "s_addc_u32 s43, s45, 0\n\t" "s_add_u32 m0, %18, 0x6d20\n\t" "s_nop 0\n\t"
                     "global_load_lds_dwordx4 v224, s[42:43]\n\t" "s_add_u32 m0, %18, 0x7120\n\t" "s_nop 0\n\t" "global_load_lds_dwordx4 v225, s[42:43]\n\t" "s_add_u32 m0, %18, 0x7520\n\t" "s_nop 0\n\t"
                     "global_load_lds_dwordx4 v226, s[42:43]\n\t" "global_load_dword %14, %14, %20 sc1\n\t" "v_add_u32 v232, 0xc200, v227\n\t" "v_add_u32 v233, 0xc200, v228\n\t" "ds_read_b64_tr_b16 v[192:193], v232\n\t"
                     "ds_read_b64_tr_b16 v[194:195], v232 offset:768\n\t" "ds_read_b64_tr_b16 v[196:197], v232 offset:12416\n\t" "ds_read_b64_tr_b16 v[198:199], v232 offset:13184\n\t"
                     "ds_read_b64_tr_b16 v[64:65], v232 offset:32\n\t" "ds_read_b64_tr_b16 v[66:67], v232 offset:800\n\t" "ds_read_b64_tr_b16 v[68:69], v232 offset:12448\n\t"
                     "ds_read_b64_tr_b16 v[70:71], v232 offset:13216\n\t" "ds_read_b64_tr_b16 v[200:201], v233 offset:64\n\t" "ds_read_b64_tr_b16 v[202:203], v233 offset:832\n\t"
                     "ds_read_b64_tr_b16 v[204:205], v233 offset:12480\n\t" "ds_read_b64_tr_b16 v[206:207], v233 offset:13248\n\t" "ds_read_b64_tr_b16 v[208:209], v232 offset:24832\n\t"
                     "ds_read_b64_tr_b16 v[210:211], v232 offset:25600\n\t" "ds_read_b64_tr_b16 v[212:213], v232 offset:37248\n\t" "ds_read_b64_tr_b16 v[214:215], v232 offset:38016\n\t"
                     "ds_read_b64_tr_b16 v[80:81], v232 offset:24864\n\t" "ds_read_b64_tr_b16 v[82:83], v232 offset:25632\n\t" "ds_read_b64_tr_b16 v[84:85], v232 offset:37280\n\t"
                     "ds_read_b64_tr_b16 v[86:87], v232 offset:38048\n\t" "ds_read_b64_tr_b16 v[216:217], v233 offset:24896\n\t" "ds_read_b64_tr_b16 v[218:219], v233 offset:25664\n\t"
                     "ds_read_b64_tr_b16 v[220:221], v233 offset:37312\n\t" "ds_read_b64_tr_b16 v[222:223], v233 offset:38080\n\t" "s_waitcnt vmcnt(13) lgkmcnt(0)\n\t" "s_barrier\n\t" "v_add_u32 v232, 0x18400, v227\n\t"
                     "v_add_u32 v233, 0x18400, v228\n\t" "ds_read_b64_tr_b16 a[0:1], v232\n\t" "ds_read_b64_tr_b16 a[2:3], v232 offset:768\n\t" "ds_read_b64_tr_b16 a[4:5], v232 offset:12416\n\t"
                     "ds_read_b64_tr_b16 a[6:7], v232 offset:13184\n\t" "ds_read_b64_tr_b16 v[96:97], v232 offset:32\n\t" "ds_read_b64_tr_b16 v[98:99], v232 offset:800\n\t"
                     "ds_read_b64_tr_b16 v[100:101], v232 offset:12448\n\t" "ds_read_b64_tr_b16 v[102:103], v232 offset:13216\n\t" "ds_read_b64_tr_b16 a[8:9], v233 offset:64\n\t"
                     "ds_read_b64_tr_b16 a[10:11], v233 offset:832\n\t" "ds_read_b64_tr_b16 a[12:13], v233 offset:12480\n\t" "ds_read_b64_tr_b16 a[14:15], v233 offset:13248\n\t"
                     "ds_read_b64_tr_b16 a[16:17], v232 offset:24832\n\t" "ds_read_b64_tr_b16 a[18:19], v232 offset:25600\n\t" "ds_read_b64_tr_b16 a[20:21], v232 offset:37248\n\t"
                     "ds_read_b64_tr_b16 a[22:23], v232 offset:38016\n\t" "ds_read_b64_tr_b16 v[112:113], v232 offset:24864\n\t" "ds_read_b64_tr_b16 v[114:115], v232 offset:25632\n\t"
                     "ds_read_b64_tr_b16 v[116:117], v232 offset:37280\n\t" "ds_read_b64_tr_b16 v[118:119], v232 offset:38048\n\t" "ds_read_b64_tr_b16 a[24:25], v233 offset:24896\n\t"
                     "ds_read_b64_tr_b16 a[26:27], v233 offset:25664\n\t" "ds_read_b64_tr_b16 a[28:29], v233 offset:37312\n\t" "ds_read_b64_tr_b16 a[30:31], v233 offset:38080\n\t" "s_waitcnt vmcnt(0) lgkmcnt(0)\n\t"
                     "s_barrier\n\t" "v_add_u32 v232, 0x0, v227\n\t" "v_add_u32 v233, 0x0, v228\n\t" "ds_read_b64_tr_b16 a[32:33], v232\n\t" "ds_read_b64_tr_b16 a[34:35], v232 offset:768\n\t"
                     "ds_read_b64_tr_b16 a[36:37], v232 offset:12416\n\t" "ds_read_b64_tr_b16 a[38:39], v232 offset:13184\n\t" "ds_read_b64_tr_b16 a[40:41], v232 offset:32\n\t"
                     "ds_read_b64_tr_b16 a[42:43], v232 offset:800\n\t" "ds_read_b64_tr_b16 a[44:45], v232 offset:12448\n\t" "ds_read_b64_tr_b16 a[46:47], v232 offset:13216\n\t"
                     "ds_read_b64_tr_b16 a[48:49], v233 offset:64\n\t" "ds_read_b64_tr_b16 a[50:51], v233 offset:832\n\t" "ds_read_b64_tr_b16 a[52:53], v233 offset:12480\n\t"
                     "ds_read_b64_tr_b16 a[54:55], v233 offset:13248\n\t" "ds_read_b64_tr_b16 a[64:65], v232 offset:24832\n\t" "ds_read_b64_tr_b16 a[66:67], v232 offset:25600\n\t"
                     "ds_read_b64_tr_b16 a[68:69], v232 offset:37248\n\t" "ds_read_b64_tr_b16 a[70:71], v232 offset:38016\n\t" "ds_read_b64_tr_b16 a[72:73], v232 offset:24864\n\t"
                     "ds_read_b64_tr_b16 a[74:75], v232 offset:25632\n\t" "ds_read_b64_tr_b16 a[76:77], v232 offset:37280\n\t" "ds_read_b64_tr_b16 a[78:79], v232 offset:38048\n\t"
                     "ds_read_b64_tr_b16 a[80:81], v233 offset:24896\n\t" "ds_read_b64_tr_b16 a[82:83], v233 offset:25664\n\t" "ds_read_b64_tr_b16 a[84:85], v233 offset:37312\n\t"
                     "ds_read_b64_tr_b16 a[86:87], v233 offset:38080\n\t" "s_waitcnt lgkmcnt(0)\n\t" "s_barrier\n\t" "ds_write_b128 v230, v[0:3]\n\t" "ds_write_b128 v230, v[4:7] offset:1024\n\t"
                     "ds_write_b128 v230, v[8:11] offset:2048\n\t" "ds_write_b128 v230, v[12:15] offset:3072\n\t" "ds_write_b128 v230, v[16:19] offset:4096\n\t" "ds_write_b128 v230, v[20:23] offset:5120\n\t"
                     "ds_write_b128 v230, v[24:27] offset:6144\n\t" "ds_write_b128 v230, v[28:31] offset:7168\n\t" "ds_write_b128 v230, v[32:35] offset:8192\n\t" "ds_write_b128 v230, v[36:39] offset:9216\n\t"
                     "ds_write_b128 v230, v[40:43] offset:10240\n\t" "ds_write_b128 v230, v[44:47] offset:11264\n\t" "ds_write_b128 v230, v[48:51] offset:12288\n\t" "ds_write_b128 v230, v[52:55] offset:13312\n\t"
                     "ds_write_b128 v230, v[56:59] offset:14336\n\t" "ds_write_b128 v230, v[60:63] offset:15360\n\t" "ds_write_b128 v230, v[64:67] offset:16384\n\t" "ds_write_b128 v230, v[68:71] offset:17408\n\t"
                     "ds_write_b128 v230, v[72:75] offset:18432\n\t" "ds_write_b128 v230, v[76:79] offset:19456\n\t" "ds_write_b128 v230, v[80:83] offset:20480\n\t" "ds_write_b128 v230, v[84:87] offset:21504\n\t"
                     "ds_write_b128 v230, v[88:91] offset:22528\n\t" "ds_write_b128 v230, v[92:95] offset:23552\n\t" "ds_write_b128 v230, v[96:99] offset:24576\n\t" "ds_write_b128 v230, v[100:103] offset:25600\n\t"
                     "ds_write_b128 v230, v[104:107] offset:26624\n\t" "ds_write_b128 v230, v[108:111] offset:27648\n\t" "ds_write_b128 v230, v[112:115] offset:28672\n\t" "ds_write_b128 v230, v[116:119] offset:29696\n\t"
                     "ds_write_b128 v230, v[120:123] offset:30720\n\t" "ds_write_b128 v230, v[124:127] offset:31744\n\t"
                     : "=&{a[0:31]}"(wa[0]), "=&{a[32:63]}"(wa[1]), "=&{a[64:95]}"(wa[2]), "=&{a[96:127]}"(wa[3]), "=&{a[128:159]}"(wa[4]), "=&{a[160:191]}"(wa[5]), "=&{a[192:223]}"(wa[6]), "=&{a[224:255]}"(wa[7]), "=&{v[128:159]}"(wv[0]), "=&{v[160:191]}"(wv[1]), "=&{v[192:223]}"(wv[2]), "=&v"(y.r), "=&v"(y.g), "=&v"(y.e), "=&v"(kfl)
                     : "{v[224:231]}"(pa), "{s[44:45]}"(p.wq + h * DH * 2 + (size_t)(16 * w) * (HD * 2)), "{s[46:47]}"(p.wk + h * DH * 2 + (size_t)(16 * w) * (HD * 2)),
                       "s"(lds + 2 * w * GS), "s"(p.ws + OFF_LAT), "s"(fl + (F_K + h) * 128)
                     : "v0", "v1", "v2", "v3", "v4", "v5", "v6", "v7", "v8", "v9", "v10", "v11", "v12", "v13", "v14", "v15", "v16", "v17", "v18", "v19", "v20", "v21", "v22", "v23", "v24", "v25", "v26", "v27", "v28", "v29", "v30", "v31", "v32", "v33", "v34", "v35", "v36", "v37", "v38", "v39", "v40", "v41", "v42", "v43", "v44", "v45", "v46", "v47", "v48", "v49", "v50", "v51", "v52", "v53", "v54", "v55", "v56", "v57", "v58", "v59", "v60", "v61", "v62", "v63", "v64", "v65", "v66", "v67", "v68", "v69", "v70", "v71", "v72", "v73", "v74", "v75", "v76", "v77", "v78", "v79", "v80", "v81", "v82", "v83", "v84", "v85", "v86", "v87", "v88", "v89", "v90", "v91", "v92", "v93", "v94", "v95", "v96", "v97", "v98", "v99", "v100", "v101", "v102", "v103", "v104", "v105", "v106", "v107", "v108", "v109", "v110", "v111", "v112", "v113", "v114", "v115", "v116", "v117", "v118", "v119", "v120", "v121", "v122", "v123", "v124", "v125", "v126", "v127", "v232", "v233", "s42", "s43", "scc", "memory");
        const u64 t = sync_tag(y.e);
        const bool rdy = __builtin_amdgcn_ballot_w64((y.r[0] & ~1u) != (u32)t || y.r[1] != (u32)(t >> 32) || y.g[0] != (u32)t) == 0 && uni((int)kfl) != -1;
        if (rdy) kxdma();
        sd = sync_end(y, p.ws, phi, w == 0);
        if (!rdy) { flag_wait(fl, F_K + h, 1); kxdma(); }
    }
    asm volatile("v_mov_b32 %0, 0" : "=v"(kfl));
    const u32 sidx = (lane & 2) ? 0xEEEEu : 0x4444u;
    const u32 moff = lane * 16;
    u32 kxa = kxl - 16 + (n >> 3) * 6160 + (16 * g + 4 * (n & 3) + ((n >> 1) & 2)) * 16;
    const u64 mb0 = 0xffff0000ffff0000ull;
    const int tp = (3 * w + (w & 1)) >> 1, ts = 1 + 3 * (w >> 1);
    u32 qw0 = lds + SL_IMG + tp * 512 + lane * 8, qw1 = lds + SL_IMG + ts * 512 + lane * 8 + 4 * (w & 1);
    const u32 kw0 = lds + SL_IMG + 3072 + w * 288 + n * 16 + 4 * g;
    const u32 qr0 = lds + SL_IMG + (g >> 1) * 512 + w * 128 + 64 * (g & 1) + 8 * qq;
    const u32 kr0 = lds + SL_IMG + 3072 + (qq >> 1) * 288 + g * 64 + (qq & 1) * 16 + 8 * (pp & 1);
    const u32 xso = 32768 + (h >> 4) * 16384 + (h & 15) * 512 + (w >> 1) * 256 + (n >> 3) * 64 + (n & 4) * 32 + (w & 1) * 32 + (g & 1) * 16 + (n & 3) * 4 + (g >> 1) * 8192;
    const u32 pso = (n < 8 ? 20480 + (h >> 1) * 256 + (n & 2) * 16 + (h & 1) * 8 + (n & 4) : 18432 + (h >> 2) * 256 + (h & 1) * 8 + (h & 2) * 2) + w * 64 + (g & 1) * 16 + (g >> 1) * 8192;
    const u64 psm = 0x01aa01aa01aa01aaull;
    const u32 kmo = (u32)(((n >> 3) * 4 + g) * 128 + (n & 7) * 16);
    u32x16 ad = u32x16{moff, sidx, lfa, kxa, qw0, qw1, kw0, kr0 + 1152 * w + (pp >> 1) * 576, qr0, kr0, xso, pso, 0x3dd53b94u, 0x3dd53b94u, kmo, kmo};
    asm volatile("" : "+{v[224:239]}"(ad));
    const char* memp = p.ws + mslot(sd, 0);
    char* kmp = p.ws + OFF_KM + h * 3072;
    const float kscale = 0.07216878364870322f;
    int budget = 30000;
    Clock clk; clk.init();
    for (int s = 0; s < NSEG; ++s) {
        int tries = 0;
        u64 bad, tp8, en = ~0ull;
        while (true) {
            const u32 tgt = clk.sched(phi, 0, cfg.G);
            asm volatile("ds_read_b128 v[64:67], v226\n\t" "ds_read_b128 v[68:71], v226 offset:1024\n\t" "ds_read_b128 v[72:75], v226 offset:2048\n\t" "ds_read_b128 v[76:79], v226 offset:3072\n\t"
                         "ds_read_b128 v[80:83], v226 offset:4096\n\t" "ds_read_b128 v[84:87], v226 offset:5120\n\t" "ds_read_b128 v[88:91], v226 offset:6144\n\t" "ds_read_b128 v[92:95], v226 offset:7168\n\t"
                         "ds_read_b128 v[96:99], v226 offset:8192\n\t" "ds_read_b128 v[100:103], v226 offset:9216\n\t" "ds_read_b128 v[104:107], v226 offset:10240\n\t" "ds_read_b128 v[108:111], v226 offset:11264\n\t"
                         "ds_read_b128 v[112:115], v226 offset:12288\n\t" "ds_read_b128 v[116:119], v226 offset:13312\n\t" "ds_read_b128 v[120:123], v226 offset:14336\n\t" "ds_read_b128 v[124:127], v226 offset:15360\n\t"
                         "v_mov_b64 v[40:41], 0\n\t" "v_mov_b64 v[42:43], 0\n\t" "v_mov_b64 v[44:45], 0\n\t" "v_mov_b64 v[46:47], 0\n\t" "v_mov_b64 v[48:49], 0\n\t" "v_mov_b64 v[50:51], 0\n\t" "v_mov_b64 v[52:53], 0\n\t"
                         "v_mov_b64 v[54:55], 0\n\t" "v_mov_b64 v[56:57], 0\n\t" "v_mov_b64 v[58:59], 0\n\t" "v_mov_b64 v[60:61], 0\n\t" "v_mov_b64 v[62:63], 0\n\t" "s_cmp_lg_u32 %10, 0\n\t" "s_cbranch_scc1 .Lsp_s\n\t"
                         "s_waitcnt vmcnt(0) lgkmcnt(0)\n\t" "s_barrier\n\t" ".Lsp_s:\n\t" "s_memtime vcc\n\t" "s_waitcnt lgkmcnt(0)\n\t" "s_sub_i32 s42, %2, vcc_lo\n\t" "s_cmp_gt_i32 s42, 80\n\t" "s_cbranch_scc1 .Lsp_s\n\t"
                         "s_max_i32 s42, s42, 0\n\t" "s_sub_i32 s42, 92, s42\n\t" "s_and_b32 s42, s42, -4\n\t" "s_getpc_b64 vcc\n\t" "s_add_u32 vcc_lo, vcc_lo, s42\n\t" "s_addc_u32 vcc_hi, vcc_hi, 0\n\t" "s_setpc_b64 vcc\n\t"
                         "s_nop 0\n\t" "s_nop 0\n\t" "s_nop 0\n\t" "s_nop 0\n\t" "s_nop 0\n\t" "s_nop 0\n\t" "s_nop 0\n\t" "s_nop 0\n\t" "s_nop 0\n\t" "s_nop 0\n\t" "s_nop 0\n\t" "s_nop 0\n\t" "s_nop 0\n\t" "s_nop 0\n\t"
                         "s_nop 0\n\t" "s_nop 0\n\t" "s_nop 0\n\t" "s_nop 0\n\t" "s_nop 0\n\t" "s_nop 0\n\t" "global_load_dwordx4 v[0:3], v224, %3 offset:-4096 sc1\n\t" "s_nop 10\n\t"
                         "global_load_dwordx4 v[4:7], v224, %3 offset:-3072 sc1\n\t" "s_nop 10\n\t" "global_load_dwordx4 v[8:11], v224, %3 offset:-2048 sc1\n\t" "s_nop 10\n\t"
                         "global_load_dwordx4 v[12:15], v224, %3 offset:-1024 sc1\n\t" "s_nop 10\n\t" "global_load_dwordx4 v[16:19], v224, %3 sc1\n\t" "s_nop 10\n\t" "global_load_dwordx4 v[20:23], v224, %3 offset:1024 sc1\n\t"
                         "s_nop 10\n\t" "global_load_dwordx4 v[24:27], v224, %3 offset:2048 sc1\n\t" "s_nop 10\n\t" "global_load_dwordx4 v[28:31], v224, %3 offset:3072 sc1\n\t" "s_nop 10\n\t"
                         "global_load_dwordx4 v[32:35], v224, %4 offset:-4096 sc1\n\t" "s_nop 10\n\t" "global_load_dwordx4 v[36:39], v224, %4 offset:-3072 sc1\n\t" "s_waitcnt vmcnt(9)\n\t"
                         "v_smfmac_f32_16x16x64_bf16 v[40:43], v[0:3], v[128:135], v225\n\t" "v_smfmac_f32_16x16x64_bf16 v[44:47], v[0:3], v[64:71], v225\n\t" "ds_read_b128 v[64:67], v226 offset:16384\n\t"
                         "v_smfmac_f32_16x16x64_bf16 v[48:51], v[0:3], v[136:143], v225\n\t" "ds_read_b128 v[68:71], v226 offset:17408\n\t" "v_smfmac_f32_16x16x64_bf16 v[52:55], v[0:3], v[72:79], v225\n\t"
                         "ds_read_b128 v[72:75], v226 offset:18432\n\t" "s_waitcnt vmcnt(8)\n\t" "v_smfmac_f32_16x16x64_bf16 v[40:43], v[4:7], v[144:151], v225\n\t" "ds_read_b128 v[76:79], v226 offset:19456\n\t"
                         "v_smfmac_f32_16x16x64_bf16 v[44:47], v[4:7], v[80:87], v225\n\t" "ds_read_b128 v[80:83], v226 offset:20480\n\t" "v_smfmac_f32_16x16x64_bf16 v[48:51], v[4:7], v[152:159], v225\n\t"
                         "ds_read_b128 v[84:87], v226 offset:21504\n\t" "v_smfmac_f32_16x16x64_bf16 v[52:55], v[4:7], v[88:95], v225\n\t" "ds_read_b128 v[88:91], v226 offset:22528\n\t" "s_waitcnt vmcnt(7)\n\t"
                         "v_smfmac_f32_16x16x64_bf16 v[40:43], v[8:11], v[160:167], v225\n\t" "ds_read_b128 v[92:95], v226 offset:23552\n\t" "v_max_u32 v238, v0, v4\n\t"
                         "v_smfmac_f32_16x16x64_bf16 v[44:47], v[8:11], v[96:103], v225\n\t" "ds_read_b128 v[96:99], v226 offset:24576\n\t" "v_cmp_eq_u32 vcc, -1, v238\n\t"
                         "v_smfmac_f32_16x16x64_bf16 v[48:51], v[8:11], v[168:175], v225\n\t" "ds_read_b128 v[100:103], v226 offset:25600\n\t" "v_smfmac_f32_16x16x64_bf16 v[52:55], v[8:11], v[104:111], v225\n\t"
                         "s_and_b64 vcc, vcc, %9\n\t" "s_cbranch_scc1 .Lsk_s\n\t" "ds_read_b128 v[104:107], v226 offset:26624\n\t" "s_waitcnt vmcnt(6)\n\t" "v_smfmac_f32_16x16x64_bf16 v[40:43], v[12:15], v[176:183], v225\n\t"
                         "ds_read_b128 v[108:111], v226 offset:27648\n\t" "v_smfmac_f32_16x16x64_bf16 v[44:47], v[12:15], v[112:119], v225\n\t" "ds_read_b128 v[112:115], v226 offset:28672\n\t"
                         "v_smfmac_f32_16x16x64_bf16 v[48:51], v[12:15], v[184:191], v225\n\t" "ds_read_b128 v[116:119], v226 offset:29696\n\t" "v_smfmac_f32_16x16x64_bf16 v[52:55], v[12:15], v[120:127], v225\n\t"
                         "ds_read_b128 v[120:123], v226 offset:30720\n\t" "s_waitcnt vmcnt(5)\n\t" "v_smfmac_f32_16x16x64_bf16 v[40:43], v[16:19], v[192:199], v225\n\t" "ds_read_b128 v[124:127], v226 offset:31744\n\t"
                         "s_waitcnt lgkmcnt(14)\n\t" "v_smfmac_f32_16x16x64_bf16 v[44:47], v[16:19], v[64:71], v225\n\t" "ds_read_b128 v[64:67], v227\n\t" "v_smfmac_f32_16x16x64_bf16 v[48:51], v[16:19], v[200:207], v225\n\t"
                         "ds_read_b128 v[68:71], v227 offset:1024\n\t" "s_waitcnt lgkmcnt(14)\n\t" "v_smfmac_f32_16x16x64_bf16 v[52:55], v[16:19], v[72:79], v225\n\t" "ds_read_b128 v[72:75], v227 offset:16\n\t"
                         "s_waitcnt vmcnt(4)\n\t" "v_smfmac_f32_16x16x64_bf16 v[40:43], v[20:23], v[208:215], v225\n\t" "ds_read_b128 v[76:79], v227 offset:1040\n\t" "s_waitcnt lgkmcnt(14)\n\t"
                         "v_smfmac_f32_16x16x64_bf16 v[44:47], v[20:23], v[80:87], v225\n\t" "ds_read_b128 v[80:83], v227 offset:2048\n\t" "v_smfmac_f32_16x16x64_bf16 v[48:51], v[20:23], v[216:223], v225\n\t"
                         "ds_read_b128 v[84:87], v227 offset:3072\n\t" "s_waitcnt lgkmcnt(14)\n\t" "v_smfmac_f32_16x16x64_bf16 v[52:55], v[20:23], v[88:95], v225\n\t" "ds_read_b128 v[88:91], v227 offset:2064\n\t"
                         "s_waitcnt vmcnt(3)\n\t" "v_smfmac_f32_16x16x64_bf16 v[40:43], v[24:27], a[0:7], v225\n\t" "ds_read_b128 v[92:95], v227 offset:3088\n\t" "s_waitcnt lgkmcnt(14)\n\t"
                         "v_smfmac_f32_16x16x64_bf16 v[44:47], v[24:27], v[96:103], v225\n\t" "ds_read_b128 v[96:99], v227 offset:4096\n\t" "v_smfmac_f32_16x16x64_bf16 v[48:51], v[24:27], a[8:15], v225\n\t"
                         "ds_read_b128 v[100:103], v227 offset:5120\n\t" "s_waitcnt lgkmcnt(14)\n\t" "v_smfmac_f32_16x16x64_bf16 v[52:55], v[24:27], v[104:111], v225\n\t" "ds_read_b128 v[104:107], v227 offset:4112\n\t"
                         "s_waitcnt vmcnt(2)\n\t" "v_smfmac_f32_16x16x64_bf16 v[40:43], v[28:31], a[16:23], v225\n\t" "ds_read_b128 v[108:111], v227 offset:5136\n\t" "s_waitcnt lgkmcnt(14)\n\t"
                         "v_smfmac_f32_16x16x64_bf16 v[44:47], v[28:31], v[112:119], v225\n\t" "v_smfmac_f32_16x16x64_bf16 v[48:51], v[28:31], a[24:31], v225\n\t" "s_waitcnt lgkmcnt(12)\n\t"
                         "v_smfmac_f32_16x16x64_bf16 v[52:55], v[28:31], v[120:127], v225\n\t" "s_waitcnt vmcnt(1)\n\t" "v_smfmac_f32_16x16x64_bf16 v[40:43], v[32:35], a[32:39], v225\n\t" "v_mov_b64 v[112:113], 0\n\t"
                         "v_smfmac_f32_16x16x64_bf16 v[44:47], v[32:35], a[40:47], v225\n\t" "v_mov_b64 v[114:115], 0\n\t" "v_smfmac_f32_16x16x64_bf16 v[48:51], v[32:35], a[48:55], v225\n\t" "v_mov_b64 v[116:117], 0\n\t"
                         "v_smfmac_f32_16x16x64_bf16 v[52:55], v[32:35], a[56:63], v225\n\t" "v_mov_b64 v[118:119], 0\n\t" "s_waitcnt vmcnt(0)\n\t" "v_smfmac_f32_16x16x64_bf16 v[40:43], v[36:39], a[64:71], v225\n\t"
                         "v_mov_b64 v[120:121], 0\n\t" "v_smfmac_f32_16x16x64_bf16 v[44:47], v[36:39], a[72:79], v225\n\t" "v_mov_b64 v[122:123], 0\n\t" "v_smfmac_f32_16x16x64_bf16 v[48:51], v[36:39], a[80:87], v225\n\t"
                         "v_mov_b64 v[124:125], 0\n\t" "v_smfmac_f32_16x16x64_bf16 v[52:55], v[36:39], a[88:95], v225\n\t" "v_add_f32 v40, v40, v42\n\t" "v_add_f32 v41, v41, v43\n\t"
                         "v_smfmac_f32_16x16x64_bf16 v[56:59], v[0:3], a[96:103], v225\n\t" "v_cvt_pk_bf16_f32 v42, v40, v41\n\t" "v_smfmac_f32_16x16x64_bf16 v[56:59], v[4:7], a[104:111], v225\n\t"
                         "v_add_f32 v44, v44, v46\n\t" "v_smfmac_f32_16x16x64_bf16 v[56:59], v[8:11], a[112:119], v225\n\t" "v_add_f32 v45, v45, v47\n\t" "v_smfmac_f32_16x16x64_bf16 v[56:59], v[12:15], a[120:127], v225\n\t"
                         "v_cvt_pk_bf16_f32 v43, v44, v45\n\t" "v_smfmac_f32_16x16x64_bf16 v[56:59], v[16:19], a[128:135], v225\n\t" "ds_write_b64 v228, v[42:43]\n\t" "v_add_f32 v48, v48, v50\n\t"
                         "v_smfmac_f32_16x16x64_bf16 v[56:59], v[20:23], a[136:143], v225\n\t" "v_add_f32 v49, v49, v51\n\t" "v_cvt_pk_bf16_f32 v46, v48, v49\n\t"
                         "v_smfmac_f32_16x16x64_bf16 v[56:59], v[24:27], a[144:151], v225\n\t" "ds_write_b32 v229, v46\n\t" "v_add_f32 v52, v52, v54\n\t" "v_smfmac_f32_16x16x64_bf16 v[56:59], v[28:31], a[152:159], v225\n\t"
                         "v_add_f32 v53, v53, v55\n\t" "v_cvt_pk_bf16_f32 v54, v52, v53\n\t" "v_smfmac_f32_16x16x64_bf16 v[56:59], v[32:35], a[160:167], v225\n\t" "ds_write_b32 v230, v54\n\t"
                         "v_smfmac_f32_16x16x64_bf16 v[56:59], v[36:39], a[168:175], v225\n\t" "v_cmp_u_f32 vcc, v40, v41\n\t" "v_smfmac_f32_16x16x64_bf16 v[60:63], v[0:3], a[176:183], v225\n\t"
                         "v_smfmac_f32_16x16x64_bf16 v[60:63], v[4:7], a[184:191], v225\n\t" "s_and_b64 vcc, vcc, %9\n\t" "s_cbranch_scc1 .Lsk_s\n\t" "v_add_f32 v56, v56, v58\n\t"
                         "v_smfmac_f32_16x16x64_bf16 v[60:63], v[8:11], a[192:199], v225\n\t" "s_waitcnt lgkmcnt(0)\n\t" "s_barrier\n\t" "v_smfmac_f32_16x16x64_bf16 v[60:63], v[12:15], a[200:207], v225\n\t"
                         "ds_read_b64_tr_b16 v[40:41], v232\n\t" "v_add_f32 v57, v57, v59\n\t" "v_smfmac_f32_16x16x64_bf16 v[60:63], v[16:19], a[208:215], v225\n\t" "ds_read_b64_tr_b16 v[42:43], v232 offset:32\n\t"
                         "v_cvt_pk_bf16_f32 v58, v56, v57\n\t" "v_smfmac_f32_16x16x64_bf16 v[60:63], v[20:23], a[216:223], v225\n\t" "ds_write_b32 v230, v58 offset:1152\n\t"
                         "v_smfmac_f32_16x16x64_bf16 v[60:63], v[24:27], a[224:231], v225\n\t" "ds_read_b64_tr_b16 v[44:45], v232 offset:1024\n\t" "ds_read_b64_tr_b16 v[46:47], v232 offset:1056\n\t"
                         "v_smfmac_f32_16x16x64_bf16 v[60:63], v[28:31], a[232:239], v225\n\t" "ds_read_b64_tr_b16 v[48:49], v232 offset:2048\n\t" "ds_read_b64_tr_b16 v[50:51], v232 offset:2080\n\t"
                         "v_smfmac_f32_16x16x64_bf16 v[60:63], v[32:35], a[240:247], v225\n\t" "ds_read_b64_tr_b16 v[0:1], v233\n\t" "ds_read_b64_tr_b16 v[2:3], v233 offset:32\n\t"
                         "v_smfmac_f32_16x16x64_bf16 v[60:63], v[36:39], a[248:255], v225\n\t" "ds_read_b64_tr_b16 v[4:5], v233 offset:576\n\t" "ds_read_b64_tr_b16 v[6:7], v233 offset:608\n\t" "s_waitcnt lgkmcnt(9)\n\t"
                         "v_smfmac_f32_16x16x64_bf16 v[112:115], v[40:43], v[64:71], v225\n\t" "s_waitcnt lgkmcnt(8)\n\t" "s_barrier\n\t" "v_smfmac_f32_16x16x64_bf16 v[116:119], v[40:43], v[72:79], v225\n\t"
                         "ds_read_b64_tr_b16 v[8:9], v233 offset:1152\n\t" "s_waitcnt lgkmcnt(7)\n\t" "v_add_f32 v60, v60, v62\n\t" "v_add_f32 v61, v61, v63\n\t"
                         "v_smfmac_f32_16x16x64_bf16 v[112:115], v[44:47], v[80:87], v225\n\t" "ds_read_b64_tr_b16 v[10:11], v233 offset:1184\n\t" "v_cvt_pk_bf16_f32 v62, v60, v61\n\t"
                         "v_smfmac_f32_16x16x64_bf16 v[116:119], v[44:47], v[88:95], v225\n\t" "ds_write_b32 v230, v62 offset:2304\n\t" "s_waitcnt lgkmcnt(7)\n\t"
                         "v_smfmac_f32_16x16x64_bf16 v[112:115], v[48:51], v[96:103], v225\n\t" "ds_read_b64_tr_b16 v[12:13], v233 offset:1728\n\t" "v_smfmac_f32_16x16x64_bf16 v[116:119], v[48:51], v[104:111], v225\n\t"
                         "ds_read_b64_tr_b16 v[14:15], v233 offset:1760\n\t" "s_waitcnt lgkmcnt(5)\n\t" "v_smfmac_f32_16x16x64_bf16 v[120:123], v[40:43], v[0:7], v225\n\t" "v_cndmask_b32 v24, v112, v113, %6\n\t"
                         "v_cndmask_b32 v25, v114, v115, %6\n\t" "v_add_f32 v24, v24, v25\n\t" "v_pk_add_f32 v[26:27], v[116:117], v[118:119]\n\t" "v_cndmask_b32 v25, v26, v27, %6\n\t" "v_max_f32 v26, v24, v25\n\t"
                         "s_waitcnt lgkmcnt(2)\n\t" "s_barrier\n\t" "ds_read_b64_tr_b16 v[16:17], v233 offset:2304\n\t" "v_max_f32_dpp v27, v26, v26 row_ror:4 row_mask:0xf bank_mask:0xf\n\t"
                         "ds_read_b64_tr_b16 v[18:19], v233 offset:2336\n\t" "v_max_f32_dpp v27, v26, v27 row_ror:8 row_mask:0xf bank_mask:0xf\n\t" "v_max_f32_dpp v27, v26, v27 row_ror:12 row_mask:0xf bank_mask:0xf\n\t"
                         "ds_read_b64_tr_b16 v[20:21], v233 offset:2880\n\t" "ds_read_b64_tr_b16 v[22:23], v233 offset:2912\n\t" "v_max_f32_dpp v30, v27, v27 row_ror:1 row_mask:0xf bank_mask:0xf\n\t"
                         "v_max_f32_dpp v30, v27, v30 row_ror:2 row_mask:0xf bank_mask:0xf\n\t" "v_max_f32_dpp v30, v27, v30 row_ror:3 row_mask:0xf bank_mask:0xf\n\t" "s_waitcnt lgkmcnt(4)\n\t"
                         "v_pk_add_f32 v[24:25], v[24:25], v[30:31] op_sel_hi:[1,0] neg_lo:[0,1] neg_hi:[0,1]\n\t" "v_pk_mul_f32 v[24:25], v[24:25], v[236:237]\n\t" "v_exp_f32 v24, v24\n\t" "v_exp_f32 v25, v25\n\t"
                         "v_mul_f32 v31, %8, v30\n\t" "v_cvt_pk_bf16_f32 v28, |v24|, |v25|\n\t" "v_smfmac_f32_16x16x64_bf16 v[120:123], v[44:47], v[8:15], v225\n\t" "global_store_dword v234, v28, %5 offset:-4096 sc1\n\t"
                         "s_waitcnt lgkmcnt(0)\n\t" "v_add_f32 v26, v24, v25\n\t" "v_smfmac_f32_16x16x64_bf16 v[120:123], v[48:51], v[16:23], v225\n\t" "s_nop 0\n\t"
                         "v_add_f32_dpp v27, v26, v26 row_ror:4 row_mask:0xf bank_mask:0xf\n\t" "v_add_f32_dpp v27, v26, v27 row_ror:8 row_mask:0xf bank_mask:0xf\n\t"
                         "v_add_f32_dpp v27, v26, v27 row_ror:12 row_mask:0xf bank_mask:0xf\n\t" "s_nop 1\n\t" "v_add_f32_dpp v29, v27, v27 row_ror:1 row_mask:0xf bank_mask:0xf\n\t"
                         "v_pk_add_f32 v[24:25], v[120:121], v[122:123]\n\t" "v_cndmask_b32 v24, v24, v25, %6\n\t" "v_max_f32 v26, v24, v30\n\t" "v_add_f32_dpp v29, v27, v29 row_ror:2 row_mask:0xf bank_mask:0xf\n\t"
                         "v_add_f32_dpp v29, v27, v29 row_ror:3 row_mask:0xf bank_mask:0xf\n\t" "v_max_f32_dpp v27, v26, v26 row_ror:1 row_mask:0xf bank_mask:0xf\n\t"
                         "v_max_f32_dpp v27, v26, v27 row_ror:2 row_mask:0xf bank_mask:0xf\n\t" "v_max_f32_dpp v27, v26, v27 row_ror:3 row_mask:0xf bank_mask:0xf\n\t" "v_mul_f32 v24, %8, v24\n\t"
                         "v_mul_f32 v29, 0x3e000000, v29\n\t" "v_max_f32_dpp v26, v27, v27 row_ror:4 row_mask:0xf bank_mask:0xf\n\t" "v_fma_f32 v24, -v26, %8, v24\n\t" "v_fma_f32 v25, -v26, %8, v31\n\t"
                         "v_exp_f32 v24, v24\n\t" "v_exp_f32 v25, v25\n\t" "s_nop 0\n\t" "v_fma_f32 v26, v25, v29, v24\n\t" "v_mov_b32_dpp v124, v24 quad_perm:[0,1,2,3] row_mask:0xf bank_mask:0x3\n\t"
                         "v_mov_b32_dpp v125, v24 quad_perm:[1,0,3,2] row_mask:0xf bank_mask:0x3\n\t" "v_add_f32_dpp v27, v26, v26 row_ror:1 row_mask:0xf bank_mask:0xf\n\t"
                         "v_add_f32_dpp v27, v26, v27 row_ror:2 row_mask:0xf bank_mask:0xf\n\t" "v_add_f32_dpp v27, v26, v27 row_ror:3 row_mask:0xf bank_mask:0xf\n\t"
                         "v_mov_b32_dpp v124, v25 quad_perm:[0,1,2,3] row_mask:0x5 bank_mask:0x4\n\t" "v_mov_b32_dpp v125, v25 quad_perm:[0,1,2,3] row_mask:0xa bank_mask:0x4\n\t"
                         "v_add_f32_dpp v26, v27, v27 row_ror:4 row_mask:0xf bank_mask:0xf\n\t" "v_rcp_f32 v26, v26\n\t" "s_mov_b64 exec, %7\n\t" "v_pk_mul_f32 v[124:125], v[124:125], v[26:27] op_sel_hi:[1,0]\n\t"
                         "v_cvt_pk_bf16_f32 v24, |v124|, |v125|\n\t" "global_store_dword v235, v24, %5 offset:-4096 sc1\n\t" "s_mov_b64 exec, -1\n\t" ".Lsk_s:\n\t" "s_memrealtime %1\n\t" "s_mov_b64 %0, vcc\n\t"
                         "s_waitcnt lgkmcnt(0)\n\t"
                         : "=&s"(bad), "=&s"(tp8)
                         : "s"(uni((int)tgt)), "s"(memp), "s"(memp + 16384), "s"((u64)memp & ~8192ull), "s"(mb0), "s"(psm), "s"(kscale * 1.4426950408889634f), "s"(en), "s"(tries), "{v[224:239]}"(ad),
                           "{a[0:31]}"(wa[0]), "{a[32:63]}"(wa[1]), "{a[64:95]}"(wa[2]), "{a[96:127]}"(wa[3]), "{a[128:159]}"(wa[4]), "{a[160:191]}"(wa[5]), "{a[192:223]}"(wa[6]), "{a[224:255]}"(wa[7]), "{v[128:159]}"(wv[0]), "{v[160:191]}"(wv[1]), "{v[192:223]}"(wv[2])
                         : "v0", "v1", "v2", "v3", "v4", "v5", "v6", "v7", "v8", "v9", "v10", "v11", "v12", "v13", "v14", "v15", "v16", "v17", "v18", "v19", "v20", "v21", "v22", "v23", "v24", "v25", "v26", "v27", "v28", "v29", "v30", "v31", "v32", "v33", "v34", "v35", "v36", "v37", "v38", "v39", "v40", "v41", "v42", "v43", "v44", "v45", "v46", "v47", "v48", "v49", "v50", "v51", "v52", "v53", "v54", "v55", "v56", "v57", "v58", "v59", "v60", "v61", "v62", "v63", "v64", "v65", "v66", "v67", "v68", "v69", "v70", "v71", "v72", "v73", "v74", "v75", "v76", "v77", "v78", "v79", "v80", "v81", "v82", "v83", "v84", "v85", "v86", "v87", "v88", "v89", "v90", "v91", "v92", "v93", "v94", "v95", "v96", "v97", "v98", "v99", "v100", "v101", "v102", "v103", "v104", "v105", "v106", "v107", "v108", "v109", "v110", "v111", "v112", "v113", "v114", "v115", "v116", "v117", "v118", "v119", "v120", "v121", "v122", "v123", "v124", "v125", "v126", "v127", "vcc", "scc", "s42", "memory");
            if (__builtin_expect(bad == 0, 1)) break;
            waitvm<0>();
            if (((++tries & 15) == 0 && !unwritten(memp, ad[0])) || --budget < 0) en = 0;
            phi += (u32)cfg.G;
        }
        const u32 tp_ = (u32)tp8;
        asm volatile("" : "+v"(kfl) : : "memory");
        if (uni((int)kfl) == -1) flag_wait(fl, F_K + pblk(s + 1) * 32 + h, 1);
        kxdma();
        flag_ldm(kfl, fl, F_K + pblk(s + 2) * 32 + h, pblk(s + 2) != pblk(s + 1));
        if (w < 3) {
            const u32x2 k0 = trread(ad[7]), k1 = trread(ad[7] + 32);
            gst16(kmp + w * 1024, ad[15], u32x4{k0[0], k0[1], k1[0], k1[1]});
        }
        clk.calib();
        const u32 tt = sched_ld0(s);
        if (s >= NSEG - 2) {
            waitvm<0>();
            barrier();
            if (w == 3) flag_add(fl, F_PROG + 8 * s + (h & 7), 1);
        }
        follow(phi, (int)(tt >> 16) - VISS, tp_, cfg.G);
        phi += tt & 0xffffu;
        memp = p.ws + mslot(sd, s + 1); kmp += NH * 3072;
    }
    if (h == 0 && w == 0) {
        flag_wait(fl, F_ACK, 256);
        Sync y;
        sync_ld(y, p.ws);
        asm volatile("s_waitcnt vmcnt(0)" : "+v"(y.r), "+v"(y.g), "+v"(y.e) : : "memory");
        const u64 t = ((u64)y.e[1] << 32 | y.e[0]) + 1;
        if (lanex() == 0) gst8(p.ws + OFF_LAT, 1024, u32x2{(u32)t, (u32)(t >> 32)});
    }
    asm volatile(".Lkm_last:\n\ts_waitcnt vmcnt(0)" : : : "memory");
}

DI void proj_role(const Ptrs& p, const ChainCfg cfg, int h, u32 lds, int w) {
    u32x32 wa[8], wv[4];
    u32 phi;
    int sd;
    {
        const int lane = lanex(), tid = 64 * w + lane;
        const int g = lane >> 4, qq = (lane >> 2) & 3, pp = lane & 3;
        constexpr int GS = 3104;
        auto dvo = [&](int i) { const int ix = i * 64 + lane; return (u32)((ix / 24) * (HD * 2) + (ix % 24) * 16); };
        auto ovo = [&](int i) { const int u = i * 64 + lane, r = u / 80; return (u32)(r * 1280 + (((u % 80) ^ (2 * (r ^ (w & 2)))) << 4)); };
        const u32 tra = lds + (2 * (g >> 1) + (qq >> 1)) * GS + (4 * (g & 1) + (qq & 1)) * 384 + 96 * w + 32 * (w & 1) + 8 * pp;
        const u32 tro = (4 * g + (qq & 1) + 16 * (qq >> 1)) * 1280 + 8 * pp + ((u32)(2 * w ^ 2 * ((4 * g + qq) & 7)) << 4);
        u32x16 pa = u32x16{dvo(0), dvo(1), dvo(2), tra, tra - 96 * (w & 1), tro, lds + (u32)((w & 1) * 24576 + (w >> 1) * 99328 + lane * 16), (u32)(lane & 31) * 8,
                           ovo(0), ovo(1), ovo(2), ovo(3), ovo(4), tro, tro, tro};
        Sync y;
        asm volatile("s_nop 4\n\t" "s_add_u32 s42, s44, 0x0\n\t" "s_addc_u32 s43, s45, 0\n\t" "s_add_u32 m0, %18, 0x0\n\t" "s_nop 0\n\t" "global_load_lds_dwordx4 v224, s[42:43]\n\t" "s_add_u32 m0, %18, 0x400\n\t"
                     "s_nop 0\n\t" "global_load_lds_dwordx4 v225, s[42:43]\n\t" "s_add_u32 m0, %18, 0x800\n\t" "s_nop 0\n\t" "global_load_lds_dwordx4 v226, s[42:43]\n\t" "s_add_u32 s42, s44, 0x18000\n\t"
                     "s_addc_u32 s43, s45, 0\n\t" "s_add_u32 m0, %18, 0xc20\n\t" "s_nop 0\n\t" "global_load_lds_dwordx4 v224, s[42:43]\n\t" "s_add_u32 m0, %18, 0x1020\n\t" "s_nop 0\n\t"
                     "global_load_lds_dwordx4 v225, s[42:43]\n\t" "s_add_u32 m0, %18, 0x1420\n\t" "s_nop 0\n\t" "global_load_lds_dwordx4 v226, s[42:43]\n\t" "s_add_u32 s42, s44, 0xc0000\n\t" "s_addc_u32 s43, s45, 0\n\t"
                     "s_add_u32 m0, %18, 0x6100\n\t" "s_nop 0\n\t" "global_load_lds_dwordx4 v224, s[42:43]\n\t" "s_add_u32 m0, %18, 0x6500\n\t" "s_nop 0\n\t" "global_load_lds_dwordx4 v225, s[42:43]\n\t"
                     "s_add_u32 m0, %18, 0x6900\n\t" "s_nop 0\n\t" "global_load_lds_dwordx4 v226, s[42:43]\n\t" "s_add_u32 s42, s44, 0xd8000\n\t" "s_addc_u32 s43, s45, 0\n\t" "s_add_u32 m0, %18, 0x6d20\n\t" "s_nop 0\n\t"
                     "global_load_lds_dwordx4 v224, s[42:43]\n\t" "s_add_u32 m0, %18, 0x7120\n\t" "s_nop 0\n\t" "global_load_lds_dwordx4 v225, s[42:43]\n\t" "s_add_u32 m0, %18, 0x7520\n\t" "s_nop 0\n\t"
                     "global_load_lds_dwordx4 v226, s[42:43]\n\t" "s_add_u32 s42, s44, 0x180000\n\t" "s_addc_u32 s43, s45, 0\n\t" "s_add_u32 m0, %18, 0xc200\n\t" "s_nop 0\n\t" "global_load_lds_dwordx4 v224, s[42:43]\n\t"
                     "s_add_u32 m0, %18, 0xc600\n\t" "s_nop 0\n\t" "global_load_lds_dwordx4 v225, s[42:43]\n\t" "s_add_u32 m0, %18, 0xca00\n\t" "s_nop 0\n\t" "global_load_lds_dwordx4 v226, s[42:43]\n\t"
                     "s_add_u32 s42, s44, 0x198000\n\t" "s_addc_u32 s43, s45, 0\n\t" "s_add_u32 m0, %18, 0xce20\n\t" "s_nop 0\n\t" "global_load_lds_dwordx4 v224, s[42:43]\n\t" "s_add_u32 m0, %18, 0xd220\n\t" "s_nop 0\n\t"
                     "global_load_lds_dwordx4 v225, s[42:43]\n\t" "s_add_u32 m0, %18, 0xd620\n\t" "s_nop 0\n\t" "global_load_lds_dwordx4 v226, s[42:43]\n\t" "s_add_u32 s42, s44, 0x240000\n\t" "s_addc_u32 s43, s45, 0\n\t"
                     "s_add_u32 m0, %18, 0x12300\n\t" "s_nop 0\n\t" "global_load_lds_dwordx4 v224, s[42:43]\n\t" "s_add_u32 m0, %18, 0x12700\n\t" "s_nop 0\n\t" "global_load_lds_dwordx4 v225, s[42:43]\n\t"
                     "s_add_u32 m0, %18, 0x12b00\n\t" "s_nop 0\n\t" "global_load_lds_dwordx4 v226, s[42:43]\n\t" "s_add_u32 s42, s44, 0x258000\n\t" "s_addc_u32 s43, s45, 0\n\t" "s_add_u32 m0, %18, 0x12f20\n\t"
                     "s_nop 0\n\t" "global_load_lds_dwordx4 v224, s[42:43]\n\t" "s_add_u32 m0, %18, 0x13320\n\t" "s_nop 0\n\t" "global_load_lds_dwordx4 v225, s[42:43]\n\t" "s_add_u32 m0, %18, 0x13720\n\t" "s_nop 0\n\t"
                     "global_load_lds_dwordx4 v226, s[42:43]\n\t" "s_waitcnt vmcnt(12) lgkmcnt(0)\n\t" "s_barrier\n\t" "s_add_u32 s42, s44, 0x300000\n\t" "s_addc_u32 s43, s45, 0\n\t" "s_add_u32 m0, %18, 0x18400\n\t"
                     "s_nop 0\n\t" "global_load_lds_dwordx4 v224, s[42:43]\n\t" "s_add_u32 m0, %18, 0x18800\n\t" "s_nop 0\n\t" "global_load_lds_dwordx4 v225, s[42:43]\n\t" "s_add_u32 m0, %18, 0x18c00\n\t" "s_nop 0\n\t"
                     "global_load_lds_dwordx4 v226, s[42:43]\n\t" "s_add_u32 s42, s44, 0x318000\n\t" "s_addc_u32 s43, s45, 0\n\t" "s_add_u32 m0, %18, 0x19020\n\t" "s_nop 0\n\t" "global_load_lds_dwordx4 v224, s[42:43]\n\t"
                     "s_add_u32 m0, %18, 0x19420\n\t" "s_nop 0\n\t" "global_load_lds_dwordx4 v225, s[42:43]\n\t" "s_add_u32 m0, %18, 0x19820\n\t" "s_nop 0\n\t" "global_load_lds_dwordx4 v226, s[42:43]\n\t"
                     "s_add_u32 s42, s44, 0x3c0000\n\t" "s_addc_u32 s43, s45, 0\n\t" "s_add_u32 m0, %18, 0x1e500\n\t" "s_nop 0\n\t" "global_load_lds_dwordx4 v224, s[42:43]\n\t" "s_add_u32 m0, %18, 0x1e900\n\t"
                     "s_nop 0\n\t" "global_load_lds_dwordx4 v225, s[42:43]\n\t" "s_add_u32 m0, %18, 0x1ed00\n\t" "s_nop 0\n\t" "global_load_lds_dwordx4 v226, s[42:43]\n\t" "s_add_u32 s42, s44, 0x3d8000\n\t"
                     "s_addc_u32 s43, s45, 0\n\t" "s_add_u32 m0, %18, 0x1f120\n\t" "s_nop 0\n\t" "global_load_lds_dwordx4 v224, s[42:43]\n\t" "s_add_u32 m0, %18, 0x1f520\n\t" "s_nop 0\n\t"
                     "global_load_lds_dwordx4 v225, s[42:43]\n\t" "s_add_u32 m0, %18, 0x1f920\n\t" "s_nop 0\n\t" "global_load_lds_dwordx4 v226, s[42:43]\n\t" "v_add_u32 v237, 0x0, v227\n\t" "v_add_u32 v238, 0x0, v228\n\t"
                     "ds_read_b64_tr_b16 a[0:1], v237\n\t" "ds_read_b64_tr_b16 a[2:3], v237 offset:768\n\t" "ds_read_b64_tr_b16 a[4:5], v237 offset:12416\n\t" "ds_read_b64_tr_b16 a[6:7], v237 offset:13184\n\t"
                     "ds_read_b64_tr_b16 v[0:1], v237 offset:32\n\t" "ds_read_b64_tr_b16 v[2:3], v237 offset:800\n\t" "ds_read_b64_tr_b16 v[4:5], v237 offset:12448\n\t" "ds_read_b64_tr_b16 v[6:7], v237 offset:13216\n\t"
                     "ds_read_b64_tr_b16 a[8:9], v238 offset:64\n\t" "ds_read_b64_tr_b16 a[10:11], v238 offset:832\n\t" "ds_read_b64_tr_b16 a[12:13], v238 offset:12480\n\t"
                     "ds_read_b64_tr_b16 a[14:15], v238 offset:13248\n\t" "ds_read_b64_tr_b16 a[16:17], v237 offset:24832\n\t" "ds_read_b64_tr_b16 a[18:19], v237 offset:25600\n\t"
                     "ds_read_b64_tr_b16 a[20:21], v237 offset:37248\n\t" "ds_read_b64_tr_b16 a[22:23], v237 offset:38016\n\t" "ds_read_b64_tr_b16 v[8:9], v237 offset:24864\n\t"
                     "ds_read_b64_tr_b16 v[10:11], v237 offset:25632\n\t" "ds_read_b64_tr_b16 v[12:13], v237 offset:37280\n\t" "ds_read_b64_tr_b16 v[14:15], v237 offset:38048\n\t"
                     "ds_read_b64_tr_b16 a[24:25], v238 offset:24896\n\t" "ds_read_b64_tr_b16 a[26:27], v238 offset:25664\n\t" "ds_read_b64_tr_b16 a[28:29], v238 offset:37312\n\t"
                     "ds_read_b64_tr_b16 a[30:31], v238 offset:38080\n\t" "s_waitcnt vmcnt(12) lgkmcnt(0)\n\t" "s_barrier\n\t" "s_add_u32 s42, s44, 0x480000\n\t" "s_addc_u32 s43, s45, 0\n\t" "s_add_u32 m0, %18, 0x0\n\t"
                     "s_nop 0\n\t" "global_load_lds_dwordx4 v224, s[42:43]\n\t" "s_add_u32 m0, %18, 0x400\n\t" "s_nop 0\n\t" "global_load_lds_dwordx4 v225, s[42:43]\n\t" "s_add_u32 m0, %18, 0x800\n\t" "s_nop 0\n\t"
                     "global_load_lds_dwordx4 v226, s[42:43]\n\t" "s_add_u32 s42, s44, 0x498000\n\t" "s_addc_u32 s43, s45, 0\n\t" "s_add_u32 m0, %18, 0xc20\n\t" "s_nop 0\n\t" "global_load_lds_dwordx4 v224, s[42:43]\n\t"
                     "s_add_u32 m0, %18, 0x1020\n\t" "s_nop 0\n\t" "global_load_lds_dwordx4 v225, s[42:43]\n\t" "s_add_u32 m0, %18, 0x1420\n\t" "s_nop 0\n\t" "global_load_lds_dwordx4 v226, s[42:43]\n\t"
                     "s_add_u32 s42, s44, 0x540000\n\t" "s_addc_u32 s43, s45, 0\n\t" "s_add_u32 m0, %18, 0x6100\n\t" "s_nop 0\n\t" "global_load_lds_dwordx4 v224, s[42:43]\n\t" "s_add_u32 m0, %18, 0x6500\n\t" "s_nop 0\n\t"
                     "global_load_lds_dwordx4 v225, s[42:43]\n\t" "s_add_u32 m0, %18, 0x6900\n\t" "s_nop 0\n\t" "global_load_lds_dwordx4 v226, s[42:43]\n\t" "s_add_u32 s42, s44, 0x558000\n\t" "s_addc_u32 s43, s45, 0\n\t"
                     "s_add_u32 m0, %18, 0x6d20\n\t" "s_nop 0\n\t" "global_load_lds_dwordx4 v224, s[42:43]\n\t" "s_add_u32 m0, %18, 0x7120\n\t" "s_nop 0\n\t" "global_load_lds_dwordx4 v225, s[42:43]\n\t"
                     "s_add_u32 m0, %18, 0x7520\n\t" "s_nop 0\n\t" "global_load_lds_dwordx4 v226, s[42:43]\n\t" "v_add_u32 v237, 0xc200, v227\n\t" "v_add_u32 v238, 0xc200, v228\n\t" "ds_read_b64_tr_b16 a[32:33], v237\n\t"
                     "ds_read_b64_tr_b16 a[34:35], v237 offset:768\n\t" "ds_read_b64_tr_b16 a[36:37], v237 offset:12416\n\t" "ds_read_b64_tr_b16 a[38:39], v237 offset:13184\n\t"
                     "ds_read_b64_tr_b16 v[16:17], v237 offset:32\n\t" "ds_read_b64_tr_b16 v[18:19], v237 offset:800\n\t" "ds_read_b64_tr_b16 v[20:21], v237 offset:12448\n\t"
                     "ds_read_b64_tr_b16 v[22:23], v237 offset:13216\n\t" "ds_read_b64_tr_b16 a[40:41], v238 offset:64\n\t" "ds_read_b64_tr_b16 a[42:43], v238 offset:832\n\t"
                     "ds_read_b64_tr_b16 a[44:45], v238 offset:12480\n\t" "ds_read_b64_tr_b16 a[46:47], v238 offset:13248\n\t" "ds_read_b64_tr_b16 a[48:49], v237 offset:24832\n\t"
                     "ds_read_b64_tr_b16 a[50:51], v237 offset:25600\n\t" "ds_read_b64_tr_b16 a[52:53], v237 offset:37248\n\t" "ds_read_b64_tr_b16 a[54:55], v237 offset:38016\n\t"
                     "ds_read_b64_tr_b16 v[24:25], v237 offset:24864\n\t" "ds_read_b64_tr_b16 v[26:27], v237 offset:25632\n\t" "ds_read_b64_tr_b16 v[28:29], v237 offset:37280\n\t"
                     "ds_read_b64_tr_b16 v[30:31], v237 offset:38048\n\t" "ds_read_b64_tr_b16 a[56:57], v238 offset:24896\n\t" "ds_read_b64_tr_b16 a[58:59], v238 offset:25664\n\t"
                     "ds_read_b64_tr_b16 a[60:61], v238 offset:37312\n\t" "ds_read_b64_tr_b16 a[62:63], v238 offset:38080\n\t" "s_waitcnt vmcnt(12) lgkmcnt(0)\n\t" "s_barrier\n\t" "s_add_u32 s42, s44, 0x600000\n\t"
                     "s_addc_u32 s43, s45, 0\n\t" "s_add_u32 m0, %18, 0xc200\n\t" "s_nop 0\n\t" "global_load_lds_dwordx4 v224, s[42:43]\n\t" "s_add_u32 m0, %18, 0xc600\n\t" "s_nop 0\n\t"
                     "global_load_lds_dwordx4 v225, s[42:43]\n\t" "s_add_u32 m0, %18, 0xca00\n\t" "s_nop 0\n\t" "global_load_lds_dwordx4 v226, s[42:43]\n\t" "s_add_u32 s42, s44, 0x618000\n\t" "s_addc_u32 s43, s45, 0\n\t"
                     "s_add_u32 m0, %18, 0xce20\n\t" "s_nop 0\n\t" "global_load_lds_dwordx4 v224, s[42:43]\n\t" "s_add_u32 m0, %18, 0xd220\n\t" "s_nop 0\n\t" "global_load_lds_dwordx4 v225, s[42:43]\n\t"
                     "s_add_u32 m0, %18, 0xd620\n\t" "s_nop 0\n\t" "global_load_lds_dwordx4 v226, s[42:43]\n\t" "s_add_u32 s42, s44, 0x6c0000\n\t" "s_addc_u32 s43, s45, 0\n\t" "s_add_u32 m0, %18, 0x12300\n\t" "s_nop 0\n\t"
                     "global_load_lds_dwordx4 v224, s[42:43]\n\t" "s_add_u32 m0, %18, 0x12700\n\t" "s_nop 0\n\t" "global_load_lds_dwordx4 v225, s[42:43]\n\t" "s_add_u32 m0, %18, 0x12b00\n\t" "s_nop 0\n\t"
                     "global_load_lds_dwordx4 v226, s[42:43]\n\t" "s_add_u32 s42, s44, 0x6d8000\n\t" "s_addc_u32 s43, s45, 0\n\t" "s_add_u32 m0, %18, 0x12f20\n\t" "s_nop 0\n\t" "global_load_lds_dwordx4 v224, s[42:43]\n\t"
                     "s_add_u32 m0, %18, 0x13320\n\t" "s_nop 0\n\t" "global_load_lds_dwordx4 v225, s[42:43]\n\t" "s_add_u32 m0, %18, 0x13720\n\t" "s_nop 0\n\t" "global_load_lds_dwordx4 v226, s[42:43]\n\t"
                     "v_add_u32 v237, 0x18400, v227\n\t" "v_add_u32 v238, 0x18400, v228\n\t" "ds_read_b64_tr_b16 a[64:65], v237\n\t" "ds_read_b64_tr_b16 a[66:67], v237 offset:768\n\t"
                     "ds_read_b64_tr_b16 a[68:69], v237 offset:12416\n\t" "ds_read_b64_tr_b16 a[70:71], v237 offset:13184\n\t" "ds_read_b64_tr_b16 v[32:33], v237 offset:32\n\t"
                     "ds_read_b64_tr_b16 v[34:35], v237 offset:800\n\t" "ds_read_b64_tr_b16 v[36:37], v237 offset:12448\n\t" "ds_read_b64_tr_b16 v[38:39], v237 offset:13216\n\t"
                     "ds_read_b64_tr_b16 a[72:73], v238 offset:64\n\t" "ds_read_b64_tr_b16 a[74:75], v238 offset:832\n\t" "ds_read_b64_tr_b16 a[76:77], v238 offset:12480\n\t"
                     "ds_read_b64_tr_b16 a[78:79], v238 offset:13248\n\t" "ds_read_b64_tr_b16 a[80:81], v237 offset:24832\n\t" "ds_read_b64_tr_b16 a[82:83], v237 offset:25600\n\t"
                     "ds_read_b64_tr_b16 a[84:85], v237 offset:37248\n\t" "ds_read_b64_tr_b16 a[86:87], v237 offset:38016\n\t" "ds_read_b64_tr_b16 v[40:41], v237 offset:24864\n\t"
                     "ds_read_b64_tr_b16 v[42:43], v237 offset:25632\n\t" "ds_read_b64_tr_b16 v[44:45], v237 offset:37280\n\t" "ds_read_b64_tr_b16 v[46:47], v237 offset:38048\n\t"
                     "ds_read_b64_tr_b16 a[88:89], v238 offset:24896\n\t" "ds_read_b64_tr_b16 a[90:91], v238 offset:25664\n\t" "ds_read_b64_tr_b16 a[92:93], v238 offset:37312\n\t"
                     "ds_read_b64_tr_b16 a[94:95], v238 offset:38080\n\t" "s_waitcnt vmcnt(12) lgkmcnt(0)\n\t" "s_barrier\n\t" "s_add_u32 s42, s46, 0x0\n\t" "s_addc_u32 s43, s47, 0\n\t" "s_add_u32 m0, %20, 0x18400\n\t"
                     "s_nop 0\n\t" "global_load_lds_dwordx4 v232, s[42:43]\n\t" "s_add_u32 m0, %20, 0x18800\n\t" "s_nop 0\n\t" "global_load_lds_dwordx4 v233, s[42:43]\n\t" "s_add_u32 m0, %20, 0x18c00\n\t" "s_nop 0\n\t"
                     "global_load_lds_dwordx4 v234, s[42:43]\n\t" "s_add_u32 m0, %20, 0x19000\n\t" "s_nop 0\n\t" "global_load_lds_dwordx4 v235, s[42:43]\n\t" "s_add_u32 m0, %20, 0x19400\n\t" "s_nop 0\n\t"
                     "global_load_lds_dwordx4 v236, s[42:43]\n\t" "s_add_u32 s42, s46, 0x1400\n\t" "s_addc_u32 s43, s47, 0\n\t" "s_add_u32 m0, %20, 0x19800\n\t" "v_xor_b32 v224, 0x80, v232\n\t"
                     "global_load_lds_dwordx4 v224, s[42:43]\n\t" "s_add_u32 m0, %20, 0x19c00\n\t" "v_xor_b32 v224, 0x80, v233\n\t" "global_load_lds_dwordx4 v224, s[42:43]\n\t" "s_add_u32 m0, %20, 0x1a000\n\t"
                     "v_xor_b32 v224, 0x80, v234\n\t" "global_load_lds_dwordx4 v224, s[42:43]\n\t" "s_add_u32 m0, %20, 0x1a400\n\t" "v_xor_b32 v224, 0x80, v235\n\t" "global_load_lds_dwordx4 v224, s[42:43]\n\t"
                     "s_add_u32 m0, %20, 0x1a800\n\t" "v_xor_b32 v224, 0x80, v236\n\t" "global_load_lds_dwordx4 v224, s[42:43]\n\t" "v_add_u32 v237, 0x0, v227\n\t" "v_add_u32 v238, 0x0, v228\n\t"
                     "ds_read_b64_tr_b16 a[96:97], v237\n\t" "ds_read_b64_tr_b16 a[98:99], v237 offset:768\n\t" "ds_read_b64_tr_b16 a[100:101], v237 offset:12416\n\t" "ds_read_b64_tr_b16 a[102:103], v237 offset:13184\n\t"
                     "ds_read_b64_tr_b16 v[48:49], v237 offset:32\n\t" "ds_read_b64_tr_b16 v[50:51], v237 offset:800\n\t" "ds_read_b64_tr_b16 v[52:53], v237 offset:12448\n\t"
                     "ds_read_b64_tr_b16 v[54:55], v237 offset:13216\n\t" "ds_read_b64_tr_b16 a[104:105], v238 offset:64\n\t" "ds_read_b64_tr_b16 a[106:107], v238 offset:832\n\t"
                     "ds_read_b64_tr_b16 a[108:109], v238 offset:12480\n\t" "ds_read_b64_tr_b16 a[110:111], v238 offset:13248\n\t" "ds_read_b64_tr_b16 a[112:113], v237 offset:24832\n\t"
                     "ds_read_b64_tr_b16 a[114:115], v237 offset:25600\n\t" "ds_read_b64_tr_b16 a[116:117], v237 offset:37248\n\t" "ds_read_b64_tr_b16 a[118:119], v237 offset:38016\n\t"
                     "ds_read_b64_tr_b16 v[56:57], v237 offset:24864\n\t" "ds_read_b64_tr_b16 v[58:59], v237 offset:25632\n\t" "ds_read_b64_tr_b16 v[60:61], v237 offset:37280\n\t"
                     "ds_read_b64_tr_b16 v[62:63], v237 offset:38048\n\t" "ds_read_b64_tr_b16 a[120:121], v238 offset:24896\n\t" "ds_read_b64_tr_b16 a[122:123], v238 offset:25664\n\t"
                     "ds_read_b64_tr_b16 a[124:125], v238 offset:37312\n\t" "ds_read_b64_tr_b16 a[126:127], v238 offset:38080\n\t" "s_waitcnt vmcnt(10) lgkmcnt(0)\n\t" "s_barrier\n\t" "s_add_u32 s42, s46, 0xa000\n\t"
                     "s_addc_u32 s43, s47, 0\n\t" "s_add_u32 m0, %20, 0x0\n\t" "s_nop 0\n\t" "global_load_lds_dwordx4 v232, s[42:43]\n\t" "s_add_u32 m0, %20, 0x400\n\t" "s_nop 0\n\t"
                     "global_load_lds_dwordx4 v233, s[42:43]\n\t" "s_add_u32 m0, %20, 0x800\n\t" "s_nop 0\n\t" "global_load_lds_dwordx4 v234, s[42:43]\n\t" "s_add_u32 m0, %20, 0xc00\n\t" "s_nop 0\n\t"
                     "global_load_lds_dwordx4 v235, s[42:43]\n\t" "s_add_u32 m0, %20, 0x1000\n\t" "s_nop 0\n\t" "global_load_lds_dwordx4 v236, s[42:43]\n\t" "s_add_u32 s42, s46, 0xb400\n\t" "s_addc_u32 s43, s47, 0\n\t"
                     "s_add_u32 m0, %20, 0x1400\n\t" "v_xor_b32 v224, 0x80, v232\n\t" "global_load_lds_dwordx4 v224, s[42:43]\n\t" "s_add_u32 m0, %20, 0x1800\n\t" "v_xor_b32 v224, 0x80, v233\n\t"
                     "global_load_lds_dwordx4 v224, s[42:43]\n\t" "s_add_u32 m0, %20, 0x1c00\n\t" "v_xor_b32 v224, 0x80, v234\n\t" "global_load_lds_dwordx4 v224, s[42:43]\n\t" "s_add_u32 m0, %20, 0x2000\n\t"
                     "v_xor_b32 v224, 0x80, v235\n\t" "global_load_lds_dwordx4 v224, s[42:43]\n\t" "s_add_u32 m0, %20, 0x2400\n\t" "v_xor_b32 v224, 0x80, v236\n\t" "global_load_lds_dwordx4 v224, s[42:43]\n\t"
                     "v_add_u32 v237, 0xc200, v227\n\t" "v_add_u32 v238, 0xc200, v228\n\t" "ds_read_b64_tr_b16 a[128:129], v237\n\t" "ds_read_b64_tr_b16 a[130:131], v237 offset:768\n\t"
                     "ds_read_b64_tr_b16 a[132:133], v237 offset:12416\n\t" "ds_read_b64_tr_b16 a[134:135], v237 offset:13184\n\t" "ds_read_b64_tr_b16 a[136:137], v237 offset:32\n\t"
                     "ds_read_b64_tr_b16 a[138:139], v237 offset:800\n\t" "ds_read_b64_tr_b16 a[140:141], v237 offset:12448\n\t" "ds_read_b64_tr_b16 a[142:143], v237 offset:13216\n\t"
                     "ds_read_b64_tr_b16 a[144:145], v238 offset:64\n\t" "ds_read_b64_tr_b16 a[146:147], v238 offset:832\n\t" "ds_read_b64_tr_b16 a[148:149], v238 offset:12480\n\t"
                     "ds_read_b64_tr_b16 a[150:151], v238 offset:13248\n\t" "ds_read_b64_tr_b16 a[152:153], v237 offset:24832\n\t" "ds_read_b64_tr_b16 a[154:155], v237 offset:25600\n\t"
                     "ds_read_b64_tr_b16 a[156:157], v237 offset:37248\n\t" "ds_read_b64_tr_b16 a[158:159], v237 offset:38016\n\t" "ds_read_b64_tr_b16 a[160:161], v237 offset:24864\n\t"
                     "ds_read_b64_tr_b16 a[162:163], v237 offset:25632\n\t" "ds_read_b64_tr_b16 a[164:165], v237 offset:37280\n\t" "ds_read_b64_tr_b16 a[166:167], v237 offset:38048\n\t"
                     "ds_read_b64_tr_b16 a[168:169], v238 offset:24896\n\t" "ds_read_b64_tr_b16 a[170:171], v238 offset:25664\n\t" "ds_read_b64_tr_b16 a[172:173], v238 offset:37312\n\t"
                     "ds_read_b64_tr_b16 a[174:175], v238 offset:38080\n\t" "s_waitcnt vmcnt(10) lgkmcnt(0)\n\t" "s_barrier\n\t" "s_add_u32 s42, s46, 0x14000\n\t" "s_addc_u32 s43, s47, 0\n\t"
                     "s_add_u32 m0, %20, 0xc200\n\t" "s_nop 0\n\t" "global_load_lds_dwordx4 v232, s[42:43]\n\t" "s_add_u32 m0, %20, 0xc600\n\t" "s_nop 0\n\t" "global_load_lds_dwordx4 v233, s[42:43]\n\t"
                     "s_add_u32 m0, %20, 0xca00\n\t" "s_nop 0\n\t" "global_load_lds_dwordx4 v234, s[42:43]\n\t" "s_add_u32 m0, %20, 0xce00\n\t" "s_nop 0\n\t" "global_load_lds_dwordx4 v235, s[42:43]\n\t"
                     "s_add_u32 m0, %20, 0xd200\n\t" "s_nop 0\n\t" "global_load_lds_dwordx4 v236, s[42:43]\n\t" "s_add_u32 s42, s46, 0x15400\n\t" "s_addc_u32 s43, s47, 0\n\t" "s_add_u32 m0, %20, 0xd600\n\t"
                     "v_xor_b32 v224, 0x80, v232\n\t" "global_load_lds_dwordx4 v224, s[42:43]\n\t" "s_add_u32 m0, %20, 0xda00\n\t" "v_xor_b32 v224, 0x80, v233\n\t" "global_load_lds_dwordx4 v224, s[42:43]\n\t"
                     "s_add_u32 m0, %20, 0xde00\n\t" "v_xor_b32 v224, 0x80, v234\n\t" "global_load_lds_dwordx4 v224, s[42:43]\n\t" "s_add_u32 m0, %20, 0xe200\n\t" "v_xor_b32 v224, 0x80, v235\n\t"
                     "global_load_lds_dwordx4 v224, s[42:43]\n\t" "s_add_u32 m0, %20, 0xe600\n\t" "v_xor_b32 v224, 0x80, v236\n\t" "global_load_lds_dwordx4 v224, s[42:43]\n\t" "s_add_u32 s42, %21, 0x18400\n\t"
                     "v_xor_b32 v237, 0x0, v229\n\t" "v_add_u32 v237, s42, v237\n\t" "v_xor_b32 v238, 0x40, v229\n\t" "v_add_u32 v238, s42, v238\n\t" "v_xor_b32 v239, 0x80, v229\n\t" "v_add_u32 v239, s42, v239\n\t"
                     "v_xor_b32 v226, 0xc0, v229\n\t" "v_add_u32 v226, s42, v226\n\t" "ds_read_b64_tr_b16 a[176:177], v237\n\t" "ds_read_b64_tr_b16 a[178:179], v238 offset:2560\n\t"
                     "ds_read_b64_tr_b16 a[200:201], v239\n\t" "ds_read_b64_tr_b16 a[202:203], v226 offset:2560\n\t" "ds_read_b64_tr_b16 a[224:225], v237 offset:256\n\t"
                     "ds_read_b64_tr_b16 a[226:227], v238 offset:2816\n\t" "ds_read_b64_tr_b16 a[248:249], v239 offset:256\n\t" "ds_read_b64_tr_b16 a[250:251], v226 offset:2816\n\t"
                     "ds_read_b64_tr_b16 v[112:113], v237 offset:512\n\t" "ds_read_b64_tr_b16 v[114:115], v238 offset:3072\n\t" "ds_read_b64_tr_b16 v[136:137], v239 offset:512\n\t"
                     "ds_read_b64_tr_b16 v[138:139], v226 offset:3072\n\t" "ds_read_b64_tr_b16 v[160:161], v237 offset:768\n\t" "ds_read_b64_tr_b16 v[162:163], v238 offset:3328\n\t"
                     "ds_read_b64_tr_b16 v[184:185], v239 offset:768\n\t" "ds_read_b64_tr_b16 v[186:187], v226 offset:3328\n\t" "ds_read_b64_tr_b16 v[208:209], v237 offset:1024\n\t"
                     "ds_read_b64_tr_b16 v[210:211], v238 offset:3584\n\t" "ds_read_b64_tr_b16 v[72:73], v239 offset:1024\n\t" "ds_read_b64_tr_b16 v[74:75], v226 offset:3584\n\t" "s_waitcnt vmcnt(10) lgkmcnt(0)\n\t"
                     "s_barrier\n\t" "s_add_u32 s42, s46, 0x1e000\n\t" "s_addc_u32 s43, s47, 0\n\t" "s_add_u32 m0, %20, 0x18400\n\t" "s_nop 0\n\t" "global_load_lds_dwordx4 v232, s[42:43]\n\t"
                     "s_add_u32 m0, %20, 0x18800\n\t" "s_nop 0\n\t" "global_load_lds_dwordx4 v233, s[42:43]\n\t" "s_add_u32 m0, %20, 0x18c00\n\t" "s_nop 0\n\t" "global_load_lds_dwordx4 v234, s[42:43]\n\t"
                     "s_add_u32 m0, %20, 0x19000\n\t" "s_nop 0\n\t" "global_load_lds_dwordx4 v235, s[42:43]\n\t" "s_add_u32 m0, %20, 0x19400\n\t" "s_nop 0\n\t" "global_load_lds_dwordx4 v236, s[42:43]\n\t"
                     "s_add_u32 s42, s46, 0x1f400\n\t" "s_addc_u32 s43, s47, 0\n\t" "s_add_u32 m0, %20, 0x19800\n\t" "v_xor_b32 v224, 0x80, v232\n\t" "global_load_lds_dwordx4 v224, s[42:43]\n\t"
                     "s_add_u32 m0, %20, 0x19c00\n\t" "v_xor_b32 v224, 0x80, v233\n\t" "global_load_lds_dwordx4 v224, s[42:43]\n\t" "s_add_u32 m0, %20, 0x1a000\n\t" "v_xor_b32 v224, 0x80, v234\n\t"
                     "global_load_lds_dwordx4 v224, s[42:43]\n\t" "s_add_u32 m0, %20, 0x1a400\n\t" "v_xor_b32 v224, 0x80, v235\n\t" "global_load_lds_dwordx4 v224, s[42:43]\n\t" "s_add_u32 m0, %20, 0x1a800\n\t"
                     "v_xor_b32 v224, 0x80, v236\n\t" "global_load_lds_dwordx4 v224, s[42:43]\n\t" "s_add_u32 s42, %21, 0x0\n\t" "v_xor_b32 v237, 0x0, v229\n\t" "v_add_u32 v237, s42, v237\n\t"
                     "v_xor_b32 v238, 0x40, v229\n\t" "v_add_u32 v238, s42, v238\n\t" "v_xor_b32 v239, 0x80, v229\n\t" "v_add_u32 v239, s42, v239\n\t" "v_xor_b32 v226, 0xc0, v229\n\t" "v_add_u32 v226, s42, v226\n\t"
                     "ds_read_b64_tr_b16 a[180:181], v237\n\t" "ds_read_b64_tr_b16 a[182:183], v238 offset:2560\n\t" "ds_read_b64_tr_b16 a[204:205], v239\n\t" "ds_read_b64_tr_b16 a[206:207], v226 offset:2560\n\t"
                     "ds_read_b64_tr_b16 a[228:229], v237 offset:256\n\t" "ds_read_b64_tr_b16 a[230:231], v238 offset:2816\n\t" "ds_read_b64_tr_b16 a[252:253], v239 offset:256\n\t"
                     "ds_read_b64_tr_b16 a[254:255], v226 offset:2816\n\t" "ds_read_b64_tr_b16 v[116:117], v237 offset:512\n\t" "ds_read_b64_tr_b16 v[118:119], v238 offset:3072\n\t"
                     "ds_read_b64_tr_b16 v[140:141], v239 offset:512\n\t" "ds_read_b64_tr_b16 v[142:143], v226 offset:3072\n\t" "ds_read_b64_tr_b16 v[164:165], v237 offset:768\n\t"
                     "ds_read_b64_tr_b16 v[166:167], v238 offset:3328\n\t" "ds_read_b64_tr_b16 v[188:189], v239 offset:768\n\t" "ds_read_b64_tr_b16 v[190:191], v226 offset:3328\n\t"
                     "ds_read_b64_tr_b16 v[212:213], v237 offset:1024\n\t" "ds_read_b64_tr_b16 v[214:215], v238 offset:3584\n\t" "ds_read_b64_tr_b16 v[76:77], v239 offset:1024\n\t"
                     "ds_read_b64_tr_b16 v[78:79], v226 offset:3584\n\t" "s_waitcnt vmcnt(10) lgkmcnt(0)\n\t" "s_barrier\n\t" "v_mov_b32 v225, 0\n\t" "global_load_dwordx2 %12, v231, %19 sc1\n\t"
                     "global_load_dwordx2 %13, v225, %19 offset:512 sc1\n\t" "global_load_dwordx2 %14, v225, %19 offset:1024 sc1\n\t" "s_add_u32 s42, s46, 0x28000\n\t" "s_addc_u32 s43, s47, 0\n\t"
                     "s_add_u32 m0, %20, 0x0\n\t" "s_nop 0\n\t" "global_load_lds_dwordx4 v232, s[42:43]\n\t" "s_add_u32 m0, %20, 0x400\n\t" "s_nop 0\n\t" "global_load_lds_dwordx4 v233, s[42:43]\n\t"
                     "s_add_u32 m0, %20, 0x800\n\t" "s_nop 0\n\t" "global_load_lds_dwordx4 v234, s[42:43]\n\t" "s_add_u32 m0, %20, 0xc00\n\t" "s_nop 0\n\t" "global_load_lds_dwordx4 v235, s[42:43]\n\t"
                     "s_add_u32 m0, %20, 0x1000\n\t" "s_nop 0\n\t" "global_load_lds_dwordx4 v236, s[42:43]\n\t" "s_add_u32 s42, s46, 0x29400\n\t" "s_addc_u32 s43, s47, 0\n\t" "s_add_u32 m0, %20, 0x1400\n\t"
                     "v_xor_b32 v224, 0x80, v232\n\t" "global_load_lds_dwordx4 v224, s[42:43]\n\t" "s_add_u32 m0, %20, 0x1800\n\t" "v_xor_b32 v224, 0x80, v233\n\t" "global_load_lds_dwordx4 v224, s[42:43]\n\t"
                     "s_add_u32 m0, %20, 0x1c00\n\t" "v_xor_b32 v224, 0x80, v234\n\t" "global_load_lds_dwordx4 v224, s[42:43]\n\t" "s_add_u32 m0, %20, 0x2000\n\t" "v_xor_b32 v224, 0x80, v235\n\t"
                     "global_load_lds_dwordx4 v224, s[42:43]\n\t" "s_add_u32 m0, %20, 0x2400\n\t" "v_xor_b32 v224, 0x80, v236\n\t" "global_load_lds_dwordx4 v224, s[42:43]\n\t" "s_add_u32 s42, %21, 0xc200\n\t"
                     "v_xor_b32 v237, 0x0, v229\n\t" "v_add_u32 v237, s42, v237\n\t" "v_xor_b32 v238, 0x40, v229\n\t" "v_add_u32 v238, s42, v238\n\t" "v_xor_b32 v239, 0x80, v229\n\t" "v_add_u32 v239, s42, v239\n\t"
                     "v_xor_b32 v226, 0xc0, v229\n\t" "v_add_u32 v226, s42, v226\n\t" "ds_read_b64_tr_b16 a[184:185], v237\n\t" "ds_read_b64_tr_b16 a[186:187], v238 offset:2560\n\t"
                     "ds_read_b64_tr_b16 a[208:209], v239\n\t" "ds_read_b64_tr_b16 a[210:211], v226 offset:2560\n\t" "ds_read_b64_tr_b16 a[232:233], v237 offset:256\n\t"
                     "ds_read_b64_tr_b16 a[234:235], v238 offset:2816\n\t" "ds_read_b64_tr_b16 v[96:97], v239 offset:256\n\t" "ds_read_b64_tr_b16 v[98:99], v226 offset:2816\n\t"
                     "ds_read_b64_tr_b16 v[120:121], v237 offset:512\n\t" "ds_read_b64_tr_b16 v[122:123], v238 offset:3072\n\t" "ds_read_b64_tr_b16 v[144:145], v239 offset:512\n\t"
                     "ds_read_b64_tr_b16 v[146:147], v226 offset:3072\n\t" "ds_read_b64_tr_b16 v[168:169], v237 offset:768\n\t" "ds_read_b64_tr_b16 v[170:171], v238 offset:3328\n\t"
                     "ds_read_b64_tr_b16 v[192:193], v239 offset:768\n\t" "ds_read_b64_tr_b16 v[194:195], v226 offset:3328\n\t" "ds_read_b64_tr_b16 v[216:217], v237 offset:1024\n\t"
                     "ds_read_b64_tr_b16 v[218:219], v238 offset:3584\n\t" "ds_read_b64_tr_b16 v[80:81], v239 offset:1024\n\t" "ds_read_b64_tr_b16 v[82:83], v226 offset:3584\n\t" "s_waitcnt vmcnt(10) lgkmcnt(0)\n\t"
                     "s_barrier\n\t" "s_add_u32 s42, s46, 0x32000\n\t" "s_addc_u32 s43, s47, 0\n\t" "s_add_u32 m0, %20, 0xc200\n\t" "s_nop 0\n\t" "global_load_lds_dwordx4 v232, s[42:43]\n\t" "s_add_u32 m0, %20, 0xc600\n\t"
                     "s_nop 0\n\t" "global_load_lds_dwordx4 v233, s[42:43]\n\t" "s_add_u32 m0, %20, 0xca00\n\t" "s_nop 0\n\t" "global_load_lds_dwordx4 v234, s[42:43]\n\t" "s_add_u32 m0, %20, 0xce00\n\t" "s_nop 0\n\t"
                     "global_load_lds_dwordx4 v235, s[42:43]\n\t" "s_add_u32 m0, %20, 0xd200\n\t" "s_nop 0\n\t" "global_load_lds_dwordx4 v236, s[42:43]\n\t" "s_add_u32 s42, s46, 0x33400\n\t" "s_addc_u32 s43, s47, 0\n\t"
                     "s_add_u32 m0, %20, 0xd600\n\t" "v_xor_b32 v224, 0x80, v232\n\t" "global_load_lds_dwordx4 v224, s[42:43]\n\t" "s_add_u32 m0, %20, 0xda00\n\t" "v_xor_b32 v224, 0x80, v233\n\t"
                     "global_load_lds_dwordx4 v224, s[42:43]\n\t" "s_add_u32 m0, %20, 0xde00\n\t" "v_xor_b32 v224, 0x80, v234\n\t" "global_load_lds_dwordx4 v224, s[42:43]\n\t" "s_add_u32 m0, %20, 0xe200\n\t"
                     "v_xor_b32 v224, 0x80, v235\n\t" "global_load_lds_dwordx4 v224, s[42:43]\n\t" "s_add_u32 m0, %20, 0xe600\n\t" "v_xor_b32 v224, 0x80, v236\n\t" "global_load_lds_dwordx4 v224, s[42:43]\n\t"
                     "s_add_u32 s42, %21, 0x18400\n\t" "v_xor_b32 v237, 0x0, v229\n\t" "v_add_u32 v237, s42, v237\n\t" "v_xor_b32 v238, 0x40, v229\n\t" "v_add_u32 v238, s42, v238\n\t" "v_xor_b32 v239, 0x80, v229\n\t"
                     "v_add_u32 v239, s42, v239\n\t" "v_xor_b32 v226, 0xc0, v229\n\t" "v_add_u32 v226, s42, v226\n\t" "ds_read_b64_tr_b16 a[188:189], v237\n\t" "ds_read_b64_tr_b16 a[190:191], v238 offset:2560\n\t"
                     "ds_read_b64_tr_b16 a[212:213], v239\n\t" "ds_read_b64_tr_b16 a[214:215], v226 offset:2560\n\t" "ds_read_b64_tr_b16 a[236:237], v237 offset:256\n\t"
                     "ds_read_b64_tr_b16 a[238:239], v238 offset:2816\n\t" "ds_read_b64_tr_b16 v[100:101], v239 offset:256\n\t" "ds_read_b64_tr_b16 v[102:103], v226 offset:2816\n\t"
                     "ds_read_b64_tr_b16 v[124:125], v237 offset:512\n\t" "ds_read_b64_tr_b16 v[126:127], v238 offset:3072\n\t" "ds_read_b64_tr_b16 v[148:149], v239 offset:512\n\t"
                     "ds_read_b64_tr_b16 v[150:151], v226 offset:3072\n\t" "ds_read_b64_tr_b16 v[172:173], v237 offset:768\n\t" "ds_read_b64_tr_b16 v[174:175], v238 offset:3328\n\t"
                     "ds_read_b64_tr_b16 v[196:197], v239 offset:768\n\t" "ds_read_b64_tr_b16 v[198:199], v226 offset:3328\n\t" "ds_read_b64_tr_b16 v[220:221], v237 offset:1024\n\t"
                     "ds_read_b64_tr_b16 v[222:223], v238 offset:3584\n\t" "ds_read_b64_tr_b16 v[84:85], v239 offset:1024\n\t" "ds_read_b64_tr_b16 v[86:87], v226 offset:3584\n\t" "s_waitcnt vmcnt(10) lgkmcnt(0)\n\t"
                     "s_barrier\n\t" "s_add_u32 s42, %21, 0x0\n\t" "v_xor_b32 v237, 0x0, v229\n\t" "v_add_u32 v237, s42, v237\n\t" "v_xor_b32 v238, 0x40, v229\n\t" "v_add_u32 v238, s42, v238\n\t"
                     "v_xor_b32 v239, 0x80, v229\n\t" "v_add_u32 v239, s42, v239\n\t" "v_xor_b32 v226, 0xc0, v229\n\t" "v_add_u32 v226, s42, v226\n\t" "ds_read_b64_tr_b16 a[192:193], v237\n\t"
                     "ds_read_b64_tr_b16 a[194:195], v238 offset:2560\n\t" "ds_read_b64_tr_b16 a[216:217], v239\n\t" "ds_read_b64_tr_b16 a[218:219], v226 offset:2560\n\t"
                     "ds_read_b64_tr_b16 a[240:241], v237 offset:256\n\t" "ds_read_b64_tr_b16 a[242:243], v238 offset:2816\n\t" "ds_read_b64_tr_b16 v[104:105], v239 offset:256\n\t"
                     "ds_read_b64_tr_b16 v[106:107], v226 offset:2816\n\t" "ds_read_b64_tr_b16 v[128:129], v237 offset:512\n\t" "ds_read_b64_tr_b16 v[130:131], v238 offset:3072\n\t"
                     "ds_read_b64_tr_b16 v[152:153], v239 offset:512\n\t" "ds_read_b64_tr_b16 v[154:155], v226 offset:3072\n\t" "ds_read_b64_tr_b16 v[176:177], v237 offset:768\n\t"
                     "ds_read_b64_tr_b16 v[178:179], v238 offset:3328\n\t" "ds_read_b64_tr_b16 v[200:201], v239 offset:768\n\t" "ds_read_b64_tr_b16 v[202:203], v226 offset:3328\n\t"
                     "ds_read_b64_tr_b16 v[64:65], v237 offset:1024\n\t" "ds_read_b64_tr_b16 v[66:67], v238 offset:3584\n\t" "ds_read_b64_tr_b16 v[88:89], v239 offset:1024\n\t"
                     "ds_read_b64_tr_b16 v[90:91], v226 offset:3584\n\t" "s_waitcnt vmcnt(0) lgkmcnt(0)\n\t" "s_barrier\n\t" "s_add_u32 s42, %21, 0xc200\n\t" "v_xor_b32 v237, 0x0, v229\n\t" "v_add_u32 v237, s42, v237\n\t"
                     "v_xor_b32 v238, 0x40, v229\n\t" "v_add_u32 v238, s42, v238\n\t" "v_xor_b32 v239, 0x80, v229\n\t" "v_add_u32 v239, s42, v239\n\t" "v_xor_b32 v226, 0xc0, v229\n\t" "v_add_u32 v226, s42, v226\n\t"
                     "ds_read_b64_tr_b16 a[196:197], v237\n\t" "ds_read_b64_tr_b16 a[198:199], v238 offset:2560\n\t" "ds_read_b64_tr_b16 a[220:221], v239\n\t" "ds_read_b64_tr_b16 a[222:223], v226 offset:2560\n\t"
                     "ds_read_b64_tr_b16 a[244:245], v237 offset:256\n\t" "ds_read_b64_tr_b16 a[246:247], v238 offset:2816\n\t" "ds_read_b64_tr_b16 v[108:109], v239 offset:256\n\t"
                     "ds_read_b64_tr_b16 v[110:111], v226 offset:2816\n\t" "ds_read_b64_tr_b16 v[132:133], v237 offset:512\n\t" "ds_read_b64_tr_b16 v[134:135], v238 offset:3072\n\t"
                     "ds_read_b64_tr_b16 v[156:157], v239 offset:512\n\t" "ds_read_b64_tr_b16 v[158:159], v226 offset:3072\n\t" "ds_read_b64_tr_b16 v[180:181], v237 offset:768\n\t"
                     "ds_read_b64_tr_b16 v[182:183], v238 offset:3328\n\t" "ds_read_b64_tr_b16 v[204:205], v239 offset:768\n\t" "ds_read_b64_tr_b16 v[206:207], v226 offset:3328\n\t"
                     "ds_read_b64_tr_b16 v[68:69], v237 offset:1024\n\t" "ds_read_b64_tr_b16 v[70:71], v238 offset:3584\n\t" "ds_read_b64_tr_b16 v[92:93], v239 offset:1024\n\t"
                     "ds_read_b64_tr_b16 v[94:95], v226 offset:3584\n\t" "s_waitcnt lgkmcnt(0)\n\t" "ds_write_b128 v230, v[0:3]\n\t" "ds_write_b128 v230, v[4:7] offset:1024\n\t"
                     "ds_write_b128 v230, v[8:11] offset:2048\n\t" "ds_write_b128 v230, v[12:15] offset:3072\n\t" "ds_write_b128 v230, v[16:19] offset:4096\n\t" "ds_write_b128 v230, v[20:23] offset:5120\n\t"
                     "ds_write_b128 v230, v[24:27] offset:6144\n\t" "ds_write_b128 v230, v[28:31] offset:7168\n\t" "ds_write_b128 v230, v[32:35] offset:8192\n\t" "ds_write_b128 v230, v[36:39] offset:9216\n\t"
                     "ds_write_b128 v230, v[40:43] offset:10240\n\t" "ds_write_b128 v230, v[44:47] offset:11264\n\t" "ds_write_b128 v230, v[48:51] offset:12288\n\t" "ds_write_b128 v230, v[52:55] offset:13312\n\t"
                     "ds_write_b128 v230, v[56:59] offset:14336\n\t" "ds_write_b128 v230, v[60:63] offset:15360\n\t" "ds_write_b128 v230, v[64:67] offset:16384\n\t" "ds_write_b128 v230, v[68:71] offset:17408\n\t"
                     "ds_write_b128 v230, v[72:75] offset:18432\n\t" "ds_write_b128 v230, v[76:79] offset:19456\n\t" "ds_write_b128 v230, v[80:83] offset:20480\n\t" "ds_write_b128 v230, v[84:87] offset:21504\n\t"
                     "ds_write_b128 v230, v[88:91] offset:22528\n\t" "ds_write_b128 v230, v[92:95] offset:23552\n\t"
                     : "=&{a[0:31]}"(wa[0]), "=&{a[32:63]}"(wa[1]), "=&{a[64:95]}"(wa[2]), "=&{a[96:127]}"(wa[3]), "=&{a[128:159]}"(wa[4]), "=&{a[160:191]}"(wa[5]), "=&{a[192:223]}"(wa[6]), "=&{a[224:255]}"(wa[7]), "=&{v[96:127]}"(wv[0]), "=&{v[128:159]}"(wv[1]), "=&{v[160:191]}"(wv[2]), "=&{v[192:223]}"(wv[3]), "=&v"(y.r), "=&v"(y.g), "=&v"(y.e), "+{v[224:239]}"(pa)
                     : "{s[44:45]}"(p.wv + h * DH * 2 + (size_t)(16 * w) * (HD * 2)), "{s[46:47]}"(p.wo + (size_t)(h * DH + 8 * w) * 1280),
                       "s"(lds + 2 * w * GS), "s"(p.ws + OFF_LAT), "s"(lds + w * 10240), "s"(lds)
                     : "v0", "v1", "v2", "v3", "v4", "v5", "v6", "v7", "v8", "v9", "v10", "v11", "v12", "v13", "v14", "v15", "v16", "v17", "v18", "v19", "v20", "v21", "v22", "v23", "v24", "v25", "v26", "v27", "v28", "v29", "v30", "v31", "v32", "v33", "v34", "v35", "v36", "v37", "v38", "v39", "v40", "v41", "v42", "v43", "v44", "v45", "v46", "v47", "v48", "v49", "v50", "v51", "v52", "v53", "v54", "v55", "v56", "v57", "v58", "v59", "v60", "v61", "v62", "v63", "v64", "v65", "v66", "v67", "v68", "v69", "v70", "v71", "v72", "v73", "v74", "v75", "v76", "v77", "v78", "v79", "v80", "v81", "v82", "v83", "v84", "v85", "v86", "v87", "v88", "v89", "v90", "v91", "v92", "v93", "v94", "v95", "s42", "s43", "scc", "memory");
        sd = sync_end(y, p.ws, phi, w == 0);
        barrier();
    }
    const int lane = lanex(), tid = 64 * w + lane;
    const int n = lane & 15, g = lane >> 4, qq = (lane >> 2) & 3, pp = lane & 3;
    char* const fl = p.ws + OFF_ST;
    const u32 iw = lds + 65536 + ((lane * 8) ^ (lane & 32));
    const u32 qr0 = lds + 65536 + (g >> 1) * 512 + pp * 128 + 64 * (g & 1) + 8 * qq + 32 * (pp >> 1);
    const u32 uo = (u32)((2 * w + (n >> 3)) * 4096 + h * 128 + (n & 7) * 16);
    u32x16 ad = u32x16{(u32)lane * 16, (lane & 2) ? 0xEEEEu : 0x4444u, lds + (u32)((w & 1) * 24576 + (w >> 1) * 99328 + lane * 16), iw + ((3 * w + (w & 1)) >> 1) * 512,
                       iw + (1 + 3 * (w >> 1)) * 512 + 4 * (w & 1), qr0, qr0 ^ 32, uo + (2 + g) * 32768, uo + (g & 1) * 32768 + (g >> 1) * 8, uo, uo, uo, uo, uo, uo, uo};
    asm volatile("" : "+{v[224:239]}"(ad));
    const char* memp = p.ws + mslot(sd, 0);
    int budget = 30000;
    Clock clk; clk.init();
    for (int s = 0; s < NSEG; ++s) {
        int tries = 0;
        u64 bad, tp8, en = ~0ull;
        while (true) {
            const u32 tgt = clk.sched(phi, 0, cfg.G);
            char* up = p.ws + uslot(sd, s);
            asm volatile("ds_read_b128 v[64:67], v226\n\t" "ds_read_b128 v[68:71], v226 offset:1024\n\t" "ds_read_b128 v[72:75], v226 offset:2048\n\t" "ds_read_b128 v[76:79], v226 offset:3072\n\t"
                         "ds_read_b128 v[80:83], v226 offset:4096\n\t" "ds_read_b128 v[84:87], v226 offset:5120\n\t" "ds_read_b128 v[88:91], v226 offset:6144\n\t" "ds_read_b128 v[92:95], v226 offset:7168\n\t"
                         "v_mov_b64 v[40:41], 0\n\t" "v_mov_b64 v[42:43], 0\n\t" "v_mov_b64 v[44:45], 0\n\t" "v_mov_b64 v[46:47], 0\n\t" "v_mov_b64 v[48:49], 0\n\t" "v_mov_b64 v[50:51], 0\n\t" ".Lsp_u:\n\t" "s_memtime vcc\n\t"
                         "s_waitcnt lgkmcnt(0)\n\t" "s_sub_i32 s42, %3, vcc_lo\n\t" "s_cmp_gt_i32 s42, 80\n\t" "s_cbranch_scc1 .Lsp_u\n\t" "s_max_i32 s42, s42, 0\n\t" "s_sub_i32 s42, 92, s42\n\t" "s_and_b32 s42, s42, -4\n\t"
                         "s_getpc_b64 vcc\n\t" "s_add_u32 vcc_lo, vcc_lo, s42\n\t" "s_addc_u32 vcc_hi, vcc_hi, 0\n\t" "s_setpc_b64 vcc\n\t" "s_nop 0\n\t" "s_nop 0\n\t" "s_nop 0\n\t" "s_nop 0\n\t" "s_nop 0\n\t" "s_nop 0\n\t"
                         "s_nop 0\n\t" "s_nop 0\n\t" "s_nop 0\n\t" "s_nop 0\n\t" "s_nop 0\n\t" "s_nop 0\n\t" "s_nop 0\n\t" "s_nop 0\n\t" "s_nop 0\n\t" "s_nop 0\n\t" "s_nop 0\n\t" "s_nop 0\n\t" "s_nop 0\n\t" "s_nop 0\n\t"
                         "global_load_dwordx4 v[0:3], v224, %4 offset:-4096 sc1\n\t" "s_nop 10\n\t" "global_load_dwordx4 v[4:7], v224, %4 offset:-3072 sc1\n\t" "s_nop 10\n\t"
                         "global_load_dwordx4 v[8:11], v224, %4 offset:-2048 sc1\n\t" "s_nop 10\n\t" "global_load_dwordx4 v[12:15], v224, %4 offset:-1024 sc1\n\t" "s_nop 10\n\t" "global_load_dwordx4 v[16:19], v224, %4 sc1\n\t"
                         "s_nop 10\n\t" "global_load_dwordx4 v[20:23], v224, %4 offset:1024 sc1\n\t" "s_nop 10\n\t" "global_load_dwordx4 v[24:27], v224, %4 offset:2048 sc1\n\t" "s_nop 10\n\t"
                         "global_load_dwordx4 v[28:31], v224, %4 offset:3072 sc1\n\t" "s_nop 10\n\t" "global_load_dwordx4 v[32:35], v224, %5 offset:-4096 sc1\n\t" "s_nop 10\n\t"
                         "global_load_dwordx4 v[36:39], v224, %5 offset:-3072 sc1\n\t" "s_waitcnt vmcnt(9)\n\t" "v_smfmac_f32_16x16x64_bf16 v[40:43], v[0:3], a[0:7], v225\n\t"
                         "v_smfmac_f32_16x16x64_bf16 v[44:47], v[0:3], v[64:71], v225\n\t" "ds_read_b128 v[64:67], v226 offset:8192\n\t" "v_smfmac_f32_16x16x64_bf16 v[48:51], v[0:3], a[8:15], v225\n\t"
                         "ds_read_b128 v[68:71], v226 offset:9216\n\t" "v_cmp_eq_u32 vcc, -1, v0\n\t" "s_waitcnt vmcnt(8)\n\t" "v_smfmac_f32_16x16x64_bf16 v[40:43], v[4:7], a[16:23], v225\n\t" "v_mov_b64 v[0:1], 0\n\t"
                         "v_smfmac_f32_16x16x64_bf16 v[44:47], v[4:7], v[72:79], v225\n\t" "s_and_b64 vcc, vcc, %6\n\t" "s_cbranch_scc1 .Lsk_u\n\t" "ds_read_b128 v[72:75], v226 offset:10240\n\t" "v_mov_b64 v[2:3], 0\n\t"
                         "v_smfmac_f32_16x16x64_bf16 v[48:51], v[4:7], a[24:31], v225\n\t" "ds_read_b128 v[76:79], v226 offset:11264\n\t" "s_waitcnt vmcnt(7)\n\t"
                         "v_smfmac_f32_16x16x64_bf16 v[40:43], v[8:11], a[32:39], v225\n\t" "v_mov_b64 v[4:5], 0\n\t" "v_smfmac_f32_16x16x64_bf16 v[44:47], v[8:11], v[80:87], v225\n\t"
                         "ds_read_b128 v[80:83], v226 offset:12288\n\t" "v_mov_b64 v[6:7], 0\n\t" "v_smfmac_f32_16x16x64_bf16 v[48:51], v[8:11], a[40:47], v225\n\t" "ds_read_b128 v[84:87], v226 offset:13312\n\t"
                         "s_waitcnt vmcnt(6)\n\t" "v_smfmac_f32_16x16x64_bf16 v[40:43], v[12:15], a[48:55], v225\n\t" "v_mov_b64 v[8:9], 0\n\t" "v_smfmac_f32_16x16x64_bf16 v[44:47], v[12:15], v[88:95], v225\n\t"
                         "ds_read_b128 v[88:91], v226 offset:14336\n\t" "v_mov_b64 v[10:11], 0\n\t" "v_smfmac_f32_16x16x64_bf16 v[48:51], v[12:15], a[56:63], v225\n\t" "ds_read_b128 v[92:95], v226 offset:15360\n\t"
                         "s_waitcnt vmcnt(5)\n\t" "v_smfmac_f32_16x16x64_bf16 v[40:43], v[16:19], a[64:71], v225\n\t" "v_mov_b64 v[12:13], 0\n\t" "s_waitcnt lgkmcnt(6)\n\t"
                         "v_smfmac_f32_16x16x64_bf16 v[44:47], v[16:19], v[64:71], v225\n\t" "ds_read_b128 v[64:67], v226 offset:16384\n\t" "v_mov_b64 v[14:15], 0\n\t"
                         "v_smfmac_f32_16x16x64_bf16 v[48:51], v[16:19], a[72:79], v225\n\t" "ds_read_b128 v[68:71], v226 offset:17408\n\t" "s_waitcnt vmcnt(4)\n\t"
                         "v_smfmac_f32_16x16x64_bf16 v[40:43], v[20:23], a[80:87], v225\n\t" "v_mov_b64 v[16:17], 0\n\t" "s_waitcnt lgkmcnt(6)\n\t" "v_smfmac_f32_16x16x64_bf16 v[44:47], v[20:23], v[72:79], v225\n\t"
                         "ds_read_b128 v[72:75], v226 offset:18432\n\t" "v_mov_b64 v[18:19], 0\n\t" "v_smfmac_f32_16x16x64_bf16 v[48:51], v[20:23], a[88:95], v225\n\t" "ds_read_b128 v[76:79], v226 offset:19456\n\t"
                         "s_waitcnt vmcnt(3)\n\t" "v_smfmac_f32_16x16x64_bf16 v[40:43], v[24:27], a[96:103], v225\n\t" "v_mov_b64 v[20:21], 0\n\t" "s_waitcnt lgkmcnt(6)\n\t"
                         "v_smfmac_f32_16x16x64_bf16 v[44:47], v[24:27], v[80:87], v225\n\t" "ds_read_b128 v[80:83], v226 offset:20480\n\t" "v_mov_b64 v[22:23], 0\n\t"
                         "v_smfmac_f32_16x16x64_bf16 v[48:51], v[24:27], a[104:111], v225\n\t" "ds_read_b128 v[84:87], v226 offset:21504\n\t" "s_waitcnt vmcnt(2)\n\t"
                         "v_smfmac_f32_16x16x64_bf16 v[40:43], v[28:31], a[112:119], v225\n\t" "v_mov_b64 v[24:25], 0\n\t" "s_waitcnt lgkmcnt(6)\n\t" "v_smfmac_f32_16x16x64_bf16 v[44:47], v[28:31], v[88:95], v225\n\t"
                         "ds_read_b128 v[88:91], v226 offset:22528\n\t" "v_mov_b64 v[26:27], 0\n\t" "v_smfmac_f32_16x16x64_bf16 v[48:51], v[28:31], a[120:127], v225\n\t" "ds_read_b128 v[92:95], v226 offset:23552\n\t"
                         "s_waitcnt vmcnt(1)\n\t" "v_smfmac_f32_16x16x64_bf16 v[40:43], v[32:35], a[128:135], v225\n\t" "v_mov_b64 v[28:29], 0\n\t" "v_smfmac_f32_16x16x64_bf16 v[44:47], v[32:35], a[136:143], v225\n\t"
                         "v_mov_b64 v[30:31], 0\n\t" "v_smfmac_f32_16x16x64_bf16 v[48:51], v[32:35], a[144:151], v225\n\t" "s_waitcnt vmcnt(0)\n\t" "v_smfmac_f32_16x16x64_bf16 v[40:43], v[36:39], a[152:159], v225\n\t"
                         "v_mov_b64 v[32:33], 0\n\t" "v_smfmac_f32_16x16x64_bf16 v[44:47], v[36:39], a[160:167], v225\n\t" "v_mov_b64 v[34:35], 0\n\t" "v_smfmac_f32_16x16x64_bf16 v[48:51], v[36:39], a[168:175], v225\n\t"
                         "v_add_f32 v52, v40, v42\n\t" "v_add_f32 v53, v41, v43\n\t" "v_cvt_pk_bf16_f32 v58, v52, v53\n\t" "v_add_f32 v54, v44, v46\n\t" "v_add_f32 v55, v45, v47\n\t" "v_cvt_pk_bf16_f32 v59, v54, v55\n\t"
                         "ds_write_b64 v227, v[58:59]\n\t" "v_pk_add_f32 v[56:57], v[48:49], v[50:51]\n\t" "v_cvt_pk_bf16_f32 v60, v56, v57\n\t" "ds_write_b32 v228, v60\n\t" "v_cmp_u_f32 vcc, v52, v53\n\t"
                         "v_mov_b64 v[36:37], 0\n\t" "v_mov_b64 v[38:39], 0\n\t" "s_and_b64 vcc, vcc, %6\n\t" "v_mov_b32 v233, 0\n\t" "s_cbranch_scc1 .Lsk_u\n\t" "v_mov_b32 v233, 0\n\t" "v_mov_b32 v233, 0\n\t"
                         "v_mov_b32 v233, 0\n\t" "v_mov_b32 v233, 0\n\t" "v_mov_b32 v233, 0\n\t" "v_mov_b32 v233, 0\n\t" "s_waitcnt lgkmcnt(0)\n\t" "s_barrier\n\t" "ds_read_b64_tr_b16 v[40:41], v229\n\t"
                         "v_mov_b32 v233, 0\n\t" "ds_read_b64_tr_b16 v[42:43], v230\n\t" "v_mov_b32 v233, 0\n\t" "ds_read_b64_tr_b16 v[44:45], v229 offset:1024\n\t" "v_mov_b32 v233, 0\n\t"
                         "ds_read_b64_tr_b16 v[46:47], v230 offset:1024\n\t" "v_mov_b32 v233, 0\n\t" "ds_read_b64_tr_b16 v[48:49], v229 offset:2048\n\t" "v_mov_b32 v233, 0\n\t"
                         "ds_read_b64_tr_b16 v[50:51], v230 offset:2048\n\t" "v_mov_b32 v233, 0\n\t" "s_waitcnt lgkmcnt(4)\n\t" "v_smfmac_f32_16x16x64_bf16 v[0:3], v[40:43], a[176:183], v225\n\t"
                         "s_waitcnt lgkmcnt(2)\n\t" "v_smfmac_f32_16x16x64_bf16 v[0:3], v[44:47], a[184:191], v225\n\t" "s_waitcnt lgkmcnt(0)\n\t" "v_smfmac_f32_16x16x64_bf16 v[0:3], v[48:51], a[192:199], v225\n\t"
                         "v_smfmac_f32_16x16x64_bf16 v[4:7], v[40:43], a[200:207], v225\n\t" "v_smfmac_f32_16x16x64_bf16 v[4:7], v[44:47], a[208:215], v225\n\t" "v_add_f32 v52, v0, v2\n\t"
                         "v_smfmac_f32_16x16x64_bf16 v[4:7], v[48:51], a[216:223], v225\n\t" "v_add_f32 v53, v1, v3\n\t" "v_smfmac_f32_16x16x64_bf16 v[8:11], v[40:43], a[224:231], v225\n\t"
                         "v_cvt_pk_bf16_f32 v54, v53, v52\n\t" "v_smfmac_f32_16x16x64_bf16 v[8:11], v[44:47], a[232:239], v225\n\t" "v_add_f32 v52, v4, v6\n\t" "v_add_f32 v53, v5, v7\n\t" "v_cvt_pk_bf16_f32 v55, v53, v52\n\t"
                         "s_nop 1\n\t" "v_permlane16_swap_b32 v54, v55\n\t" "v_smfmac_f32_16x16x64_bf16 v[8:11], v[48:51], a[240:247], v225\n\t" "global_store_dwordx2 v232, v[54:55], %2 sc1\n\t"
                         "v_smfmac_f32_16x16x64_bf16 v[12:15], v[40:43], a[248:255], v225\n\t" "v_smfmac_f32_16x16x64_bf16 v[12:15], v[44:47], v[96:103], v225\n\t" "v_add_f32 v52, v8, v10\n\t"
                         "v_smfmac_f32_16x16x64_bf16 v[12:15], v[48:51], v[104:111], v225\n\t" "v_add_f32 v53, v9, v11\n\t" "v_smfmac_f32_16x16x64_bf16 v[16:19], v[40:43], v[112:119], v225\n\t"
                         "v_cvt_pk_bf16_f32 v56, v53, v52\n\t" "v_smfmac_f32_16x16x64_bf16 v[16:19], v[44:47], v[120:127], v225\n\t" "v_add_f32 v52, v12, v14\n\t"
                         "v_smfmac_f32_16x16x64_bf16 v[16:19], v[48:51], v[128:135], v225\n\t" "v_add_f32 v53, v13, v15\n\t" "v_smfmac_f32_16x16x64_bf16 v[20:23], v[40:43], v[136:143], v225\n\t"
                         "v_cvt_pk_bf16_f32 v57, v53, v52\n\t" "v_smfmac_f32_16x16x64_bf16 v[20:23], v[44:47], v[144:151], v225\n\t" "v_add_f32 v52, v16, v18\n\t"
                         "v_smfmac_f32_16x16x64_bf16 v[20:23], v[48:51], v[152:159], v225\n\t" "v_add_f32 v53, v17, v19\n\t" "v_smfmac_f32_16x16x64_bf16 v[24:27], v[40:43], v[160:167], v225\n\t"
                         "v_cvt_pk_bf16_f32 v58, v53, v52\n\t" "v_smfmac_f32_16x16x64_bf16 v[24:27], v[44:47], v[168:175], v225\n\t" "v_add_f32 v52, v20, v22\n\t" "v_add_f32 v53, v21, v23\n\t"
                         "v_smfmac_f32_16x16x64_bf16 v[24:27], v[48:51], v[176:183], v225\n\t" "v_cvt_pk_bf16_f32 v59, v53, v52\n\t" "v_permlane32_swap_b32 v56, v58\n\t"
                         "v_smfmac_f32_16x16x64_bf16 v[28:31], v[40:43], v[184:191], v225\n\t" "s_nop 0\n\t" "v_permlane32_swap_b32 v57, v59\n\t" "v_smfmac_f32_16x16x64_bf16 v[28:31], v[44:47], v[192:199], v225\n\t"
                         "s_nop 0\n\t" "v_permlane16_swap_b32 v56, v57\n\t" "v_permlane16_swap_b32 v58, v59\n\t" "v_smfmac_f32_16x16x64_bf16 v[28:31], v[48:51], v[200:207], v225\n\t"
                         "global_store_dwordx4 v231, v[56:59], %2 sc1\n\t" "s_add_u32 s40, s40, 0x20000\n\t" "s_addc_u32 s41, s41, 0\n\t" "v_add_f32 v52, v24, v26\n\t"
                         "v_smfmac_f32_16x16x64_bf16 v[32:35], v[40:43], v[208:215], v225\n\t" "v_add_f32 v53, v25, v27\n\t" "v_smfmac_f32_16x16x64_bf16 v[32:35], v[44:47], v[216:223], v225\n\t"
                         "v_cvt_pk_bf16_f32 v60, v53, v52\n\t" "v_add_f32 v52, v28, v30\n\t" "v_smfmac_f32_16x16x64_bf16 v[32:35], v[48:51], v[64:71], v225\n\t" "v_add_f32 v53, v29, v31\n\t"
                         "v_smfmac_f32_16x16x64_bf16 v[36:39], v[40:43], v[72:79], v225\n\t" "v_cvt_pk_bf16_f32 v61, v53, v52\n\t" "v_smfmac_f32_16x16x64_bf16 v[36:39], v[44:47], v[80:87], v225\n\t"
                         "v_add_f32 v52, v32, v34\n\t" "v_smfmac_f32_16x16x64_bf16 v[36:39], v[48:51], v[88:95], v225\n\t" "v_add_f32 v53, v33, v35\n\t" "v_cvt_pk_bf16_f32 v62, v53, v52\n\t" "v_mov_b32 v233, 0\n\t"
                         "v_mov_b32 v233, 0\n\t" "v_mov_b32 v233, 0\n\t" "v_mov_b32 v233, 0\n\t" "v_mov_b32 v233, 0\n\t" "v_pk_add_f32 v[52:53], v[36:37], v[38:39]\n\t" "v_cvt_pk_bf16_f32 v63, v53, v52\n\t"
                         "v_permlane32_swap_b32 v60, v62\n\t" "s_nop 0\n\t" "v_permlane32_swap_b32 v61, v63\n\t" "s_nop 1\n\t" "v_permlane16_swap_b32 v60, v61\n\t" "v_permlane16_swap_b32 v62, v63\n\t"
                         "global_store_dwordx4 v231, v[60:63], %2 sc1\n\t" ".Lsk_u:\n\t" "s_memrealtime %1\n\t" "s_mov_b64 %0, vcc\n\t" "s_waitcnt lgkmcnt(0)\n\t"
                         : "=&s"(bad), "=&s"(tp8), "+{s[40:41]}"(up)
                         : "s"(uni((int)tgt)), "s"(memp), "s"(memp + 16384), "s"(en), "{v[224:239]}"(ad),
                           "{a[0:31]}"(wa[0]), "{a[32:63]}"(wa[1]), "{a[64:95]}"(wa[2]), "{a[96:127]}"(wa[3]), "{a[128:159]}"(wa[4]), "{a[160:191]}"(wa[5]), "{a[192:223]}"(wa[6]), "{a[224:255]}"(wa[7]), "{v[96:127]}"(wv[0]), "{v[128:159]}"(wv[1]), "{v[160:191]}"(wv[2]), "{v[192:223]}"(wv[3])
                         : "v0", "v1", "v2", "v3", "v4", "v5", "v6", "v7", "v8", "v9", "v10", "v11", "v12", "v13", "v14", "v15", "v16", "v17", "v18", "v19", "v20", "v21", "v22", "v23", "v24", "v25", "v26", "v27", "v28", "v29", "v30", "v31", "v32", "v33", "v34", "v35", "v36", "v37", "v38", "v39", "v40", "v41", "v42", "v43", "v44", "v45", "v46", "v47", "v48", "v49", "v50", "v51", "v52", "v53", "v54", "v55", "v56", "v57", "v58", "v59", "v60", "v61", "v62", "v63", "v64", "v65", "v66", "v67", "v68", "v69", "v70", "v71", "v72", "v73", "v74", "v75", "v76", "v77", "v78", "v79", "v80", "v81", "v82", "v83", "v84", "v85", "v86", "v87", "v88", "v89", "v90", "v91", "v92", "v93", "v94", "v95", "vcc", "scc", "s42", "memory");
            if (__builtin_expect(bad == 0, 1)) break;
            waitvm<0>();
            if (--budget < 0 || ((++tries & 15) == 0 && !unwritten(memp, ad[0]))) en = 0;
            phi += (u32)cfg.G;
        }
        clk.calib();
        const u32 tt = sched_ld0(s);
        if (s >= NSEG - 2) waitvm<0>();
        barrier();
        if (s >= NSEG - 2 && w == 3) flag_add(fl, F_PROG + 8 * s + (h & 7), 1);
        if (s == NSEG - 1 && w == 2) flag_add(fl, F_UD + (h & 1), 1);
        follow(phi, (int)(tt >> 16) + 4 + cfg.T3 - VISS, (u32)tp8, cfg.G);
        phi += tt & 0xffffu;
        memp = p.ws + mslot(sd, s + 1);
    }
    asm volatile(".Lkm_lu:\n\ts_waitcnt vmcnt(0)" : : : "memory");
}

#define VALL "{v[0:3]}"(o), "{v[16:47]}"(rv[0]), "{v[48:79]}"(rv[1]), "{v[80:111]}"(rv[2]), "{v[112:143]}"(rv[3]), "{v[144:175]}"(rv[4]), "{v[176:207]}"(rv[5]), "{v[208:239]}"(rv[6]), "{a[0:31]}"(ra[0]), "{a[32:63]}"(ra[1]), "{a[64:95]}"(ra[2]), "{a[96:127]}"(ra[3])
DI void value_role(const Ptrs& p, const ChainCfg cfg, int vb, int sd, u32 phi, u32 lds, int w) {
    const int lane = lanex(), tid = 64 * w + lane, n = lane & 15, g = lane >> 4;
    const int v = 2 * (vb & 3) + ((vb >> 2) & 1) + (vb & ~7);
    const u32 moff = lane * 16;
    char* const fl = p.ws + OFF_ST;
    char* const ub = p.ws + (size_t)v * 8192;
    if (w == 3) {
        const u32x4 ff = u32x4{~0u, ~0u, ~0u, ~0u};
        barrier();
#pragma nounroll
        for (int s = 1; s < NSEG - 1; ++s) {
            nap(cfg.cR3 >> 8);
            char* const mk = ub + uslot(sd, s + 1);
#pragma unroll
            for (int i = 0; i < 8; ++i) { gst16(mk + i * 1024, moff, ff); gap<HQ>(-1); }
            if (vb < 32 && s < NSEG - 2) {
                gst16(p.ws + (size_t)(s + 2) * HO_STRIDE + vb * 2048, moff, ff);
                gst16(p.ws + (size_t)(s + 2) * HO_STRIDE + vb * 2048 + 1024, moff, ff);
            }
            waitvm<0>();
            barrier();
        }
        return;
    }
    if (w) {
        const u32 dof = (u32)((n >> 3) * 16384 + g * 128 + (n & 7) * 16);
        const char* dp = p.ws + OFF_D + (size_t)v * 32768 - 4096 + w * 8192;
        flag_wait(fl, F_VD + (vb & 3) + (v < 20 ? 0 : 4), 32);
#pragma nounroll
        for (int s = 0; s < NSEG - 1; ++s) {
            u32x4 df[16];
            u32 dfl;
            if (s > 1) nap(cfg.cR3 & 255);
            sfor<0, 16>([&](auto I) { gld16c<I.v * 512 - 4096>(df[I.v], dp, dof); gap<HP>(1 - s); });
            dp += D_STRIDE;
            flag_ldm(dfl, fl, F_VD + pblk(s + 1) * 8 + (vb & 7), pblk(s + 1) != pblk(s));
            const u32 la = lds + moff + (s & 1) * 32768 + (w - 1) * 16384;
            sfor<0, 8>([&](auto I) {
                constexpr int i = I.v;
                waitv<15 - 2 * i>(df[2 * i]); waitv<15 - 2 * i>(df[2 * i + 1]);
                const u32x4 x = df[2 * i], y = df[2 * i + 1];
                *LDSP(u32x4, la + 2048 * i) = u32x4{x[0], y[0], x[2], y[2]};
                *LDSP(u32x4, la + 2048 * i + 1024) = u32x4{x[1], y[1], x[3], y[3]};
                PIN();
            });
            waitvm<0>();
            asm volatile("" : "+v"(dfl));
            barrier();
            if (uni((int)dfl) < 31) flag_wait(fl, F_VD + pblk(s + 1) * 8 + (vb & 7), 32);
        }
        return;
    }
    u32x32 rv[7], ra[4];
    u32x4 o;
    asm volatile("v_accvgpr_write_b32 a64, 0\n\t" "v_accvgpr_write_b32 a65, 0\n\t" "v_accvgpr_write_b32 a66, 0\n\t" "v_accvgpr_write_b32 a67, 0\n\t" "v_accvgpr_write_b32 a68, 0\n\t" "v_accvgpr_write_b32 a69, 0\n\t"
                 "v_accvgpr_write_b32 a70, 0\n\t" "v_accvgpr_write_b32 a71, 0\n\t" "v_accvgpr_write_b32 a72, 0\n\t" "v_accvgpr_write_b32 a73, 0\n\t" "v_accvgpr_write_b32 a74, 0\n\t" "v_accvgpr_write_b32 a75, 0\n\t"
                 "v_accvgpr_write_b32 a76, 0\n\t" "v_accvgpr_write_b32 a77, 0\n\t" "v_accvgpr_write_b32 a78, 0\n\t" "v_accvgpr_write_b32 a79, 0\n\t" "v_mov_b64 v[112:113], -1\n\t" "v_mov_b64 v[120:121], -1\n\t"
                 "v_mov_b64 v[128:129], -1\n\t" "v_mov_b64 v[136:137], -1\n\t" "v_mov_b64 v[144:145], -1\n\t" "v_mov_b64 v[152:153], -1\n\t" "v_mov_b64 v[160:161], -1\n\t" "v_mov_b64 v[168:169], -1\n\t"
                 "v_mov_b64 v[176:177], -1\n\t" "v_mov_b64 v[184:185], -1\n\t" "v_mov_b64 v[192:193], -1\n\t" "v_mov_b64 v[200:201], -1\n\t" "v_mov_b64 v[208:209], -1\n\t" "v_mov_b64 v[216:217], -1\n\t"
                 "v_mov_b64 v[224:225], -1\n\t" "v_mov_b64 v[232:233], -1\n\t"
                 : "={v[0:3]}"(o), "={v[16:47]}"(rv[0]), "={v[48:79]}"(rv[1]), "={v[80:111]}"(rv[2]), "={v[112:143]}"(rv[3]), "={v[144:175]}"(rv[4]), "={v[176:207]}"(rv[5]), "={v[208:239]}"(rv[6]), "={a[0:31]}"(ra[0]), "={a[32:63]}"(ra[1]), "={a[64:95]}"(ra[2]), "={a[96:127]}"(ra[3]));
    const u32 ho = (u32)(min(lane, 39) * 256 + (lane >> 5) * 8192);
    u32x8 ad = u32x8{(u32)((n >> 3) * 4096 + g * 128 + (n & 7) * 16), (u32)((n & 4) * 128 + (n >> 3) * 256 + g * 64 + (n & 3) * 16), (u32)((g >> 1) * 256 + (n >> 2) * 64 + (n & 1) * 16), moff,
                     (lane & 2) ? 0xEEEEu : 0x4444u, (lane & 4) ? 0xEEEEu : 0x4444u, (u32)(v * 256 + (v >> 5) * 8192 + g * 64 + (n >> 3) * 32 + (n & 1) * 16 + (n & 6) * 2),
                     (lane & 1) ? 0x07060302u : 0x01000504u};
    asm volatile("" : "+{v[4:11]}"(ad));
    auto zero = [&]() {
        asm volatile("v_mov_b64 v[0:1], 0\n\t" "v_mov_b64 v[2:3], 0\n\t" "v_mov_b64 v[48:49], 0\n\t" "v_mov_b64 v[50:51], 0\n\t" "v_mov_b64 v[52:53], 0\n\t" "v_mov_b64 v[54:55], 0\n\t" "v_mov_b64 v[56:57], 0\n\t"
                     "v_mov_b64 v[58:59], 0\n\t" "v_mov_b64 v[60:61], 0\n\t" "v_mov_b64 v[62:63], 0\n\t" "v_mov_b64 v[64:65], 0\n\t" "v_mov_b64 v[66:67], 0\n\t" "v_mov_b64 v[68:69], 0\n\t" "v_mov_b64 v[70:71], 0\n\t"
                     "v_mov_b64 v[72:73], 0\n\t" "v_mov_b64 v[74:75], 0\n\t" "v_mov_b64 v[76:77], 0\n\t" "v_mov_b64 v[78:79], 0\n\t" "v_mov_b64 v[80:81], 0\n\t" "v_mov_b64 v[82:83], 0\n\t" "v_mov_b64 v[84:85], 0\n\t"
                     "v_mov_b64 v[86:87], 0\n\t" "v_mov_b64 v[88:89], 0\n\t" "v_mov_b64 v[90:91], 0\n\t" "v_mov_b64 v[92:93], 0\n\t" "v_mov_b64 v[94:95], 0\n\t" "v_mov_b64 v[96:97], 0\n\t" "v_mov_b64 v[98:99], 0\n\t"
                     "v_mov_b64 v[100:101], 0\n\t" "v_mov_b64 v[102:103], 0\n\t" "v_mov_b64 v[104:105], 0\n\t" "v_mov_b64 v[106:107], 0\n\t" "v_mov_b64 v[108:109], 0\n\t" "v_mov_b64 v[110:111], 0\n\t"
                     : : VALL : "memory");
    };
    const char* memp = p.ws + OFF_XF + 4096;
    char* up = ub + OFF_XF;
    int budget = 30000;
    Clock clk; clk.init();
    u32 tt = 0;
    for (int s = -1; s < NSEG - 1; ++s) {
        const char* const mnx = p.ws + mslot(sd, s + 1);
        bool all = false;
        int tries = 0;
        u64 tp8;
        while (true) {
            const u32 tgt = clk.sched(phi, (int)(tt >> 16), cfg.G) - (s < 0 ? 1u << 20 : 0u);
            u64 ok;
            asm volatile("s_cmp_lg_u32 %11, 0\n\t" "s_cbranch_scc0 .Lsp_v0\n\t" ".Lsp_v3:\n\t" "s_memtime vcc\n\t" "s_waitcnt lgkmcnt(0)\n\t" "s_sub_i32 s42, %10, vcc_lo\n\t" "s_cmp_gt_i32 s42, 80\n\t"
                         "s_cbranch_scc1 .Lsp_v3\n\t" "s_max_i32 s42, s42, 0\n\t" "s_sub_i32 s42, 92, s42\n\t" "s_and_b32 s42, s42, -4\n\t" "s_getpc_b64 vcc\n\t" "s_add_u32 vcc_lo, vcc_lo, s42\n\t"
                         "s_addc_u32 vcc_hi, vcc_hi, 0\n\t" "s_setpc_b64 vcc\n\t" "s_nop 0\n\t" "s_nop 0\n\t" "s_nop 0\n\t" "s_nop 0\n\t" "s_nop 0\n\t" "s_nop 0\n\t" "s_nop 0\n\t" "s_nop 0\n\t" "s_nop 0\n\t" "s_nop 0\n\t"
                         "s_nop 0\n\t" "s_nop 0\n\t" "s_nop 0\n\t" "s_nop 0\n\t" "s_nop 0\n\t" "s_nop 0\n\t" "s_nop 0\n\t" "s_nop 0\n\t" "s_nop 0\n\t" "s_nop 0\n\t"
                         "global_load_dwordx4 a[0:3], v5, %7 offset:-4096 sc1\n\t" "global_load_dwordx4 a[4:7], v5, %7 offset:-3072 sc1\n\t" "global_load_dwordx4 a[8:11], v5, %7 offset:-2048 sc1\n\t"
                         "global_load_dwordx4 a[12:15], v5, %7 offset:-1024 sc1\n\t" "global_load_dwordx4 a[16:19], v5, %7 sc1\n\t" "global_load_dwordx4 a[20:23], v5, %7 offset:1024 sc1\n\t"
                         "global_load_dwordx4 a[24:27], v5, %7 offset:2048 sc1\n\t" "global_load_dwordx4 a[28:31], v5, %7 offset:3072 sc1\n\t" "global_load_dwordx4 a[32:35], v5, %8 offset:-4096 sc1\n\t"
                         "global_load_dwordx4 a[36:39], v5, %8 offset:-3072 sc1\n\t" "global_load_dwordx4 a[40:43], v5, %8 offset:-2048 sc1\n\t" "global_load_dwordx4 a[44:47], v5, %8 offset:-1024 sc1\n\t"
                         "global_load_dwordx4 a[48:51], v5, %8 sc1\n\t" "global_load_dwordx4 a[52:55], v5, %8 offset:1024 sc1\n\t" "global_load_dwordx4 a[56:59], v5, %8 offset:2048 sc1\n\t"
                         "global_load_dwordx4 a[60:63], v5, %8 offset:3072 sc1\n\t" "s_nop 15\n\t" "s_nop 10\n\t" "s_mov_b64 exec, %9\n\t" "global_load_dwordx4 a[64:67], v6, %6 offset:-2048 sc1\n\t"
                         "global_load_dwordx4 a[68:71], v6, %6 offset:-1536 sc1\n\t" "global_load_dwordx4 a[72:75], v6, %6 offset:-1024 sc1\n\t" "global_load_dwordx4 a[76:79], v6, %6 offset:-512 sc1\n\t"
                         "s_mov_b64 exec, -1\n\t" "global_load_dwordx4 a[80:83], v7, %6 sc1\n\t" "global_load_dwordx4 a[84:87], v7, %6 offset:1024 sc1\n\t" "global_load_dwordx4 a[88:91], v7, %6 offset:2048 sc1\n\t"
                         "global_load_dwordx4 a[92:95], v7, %6 offset:3072 sc1\n\t" "s_mov_b32 s42, %11\n\t" ".Lsp_v5:\n\t" "s_sub_u32 s42, s42, 1\n\t" "s_cmp_gt_i32 s42, 0\n\t" "s_cbranch_scc1 .Lsp_v5\n\t"
                         "global_load_dwordx4 a[96:99], v4, %3 sc1\n\t" "global_load_dwordx4 a[100:103], v4, %3 offset:512 sc1\n\t" "global_load_dwordx4 a[104:107], v4, %3 offset:1024 sc1\n\t"
                         "global_load_dwordx4 a[108:111], v4, %3 offset:1536 sc1\n\t" "global_load_dwordx4 a[112:115], v4, %3 offset:2048 sc1\n\t" "global_load_dwordx4 a[116:119], v4, %3 offset:2560 sc1\n\t"
                         "global_load_dwordx4 a[120:123], v4, %3 offset:3072 sc1\n\t" "global_load_dwordx4 a[124:127], v4, %3 offset:3584 sc1\n\t" "s_waitcnt vmcnt(31)\n\t"
                         "v_smfmac_f32_16x16x64_bf16 v[48:51], a[0:3], v[112:119], v9\n\t" "s_waitcnt vmcnt(30)\n\t" "v_smfmac_f32_16x16x64_bf16 v[52:55], a[4:7], v[120:127], v9\n\t" "s_waitcnt vmcnt(29)\n\t"
                         "v_smfmac_f32_16x16x64_bf16 v[56:59], a[8:11], v[128:135], v9\n\t" "s_waitcnt vmcnt(28)\n\t" "v_cvt_pk_bf16_f32 v16, v48, v49\n\t" "v_cvt_pk_bf16_f32 v17, v50, v51\n\t"
                         "v_smfmac_f32_16x16x64_bf16 v[60:63], a[12:15], v[136:143], v9\n\t" "s_waitcnt vmcnt(27)\n\t" "v_cvt_pk_bf16_f32 v18, v52, v53\n\t" "v_cvt_pk_bf16_f32 v19, v54, v55\n\t"
                         "v_smfmac_f32_16x16x64_bf16 v[64:67], a[16:19], v[144:151], v9\n\t" "s_waitcnt vmcnt(26)\n\t" "v_cvt_pk_bf16_f32 v20, v56, v57\n\t" "v_cvt_pk_bf16_f32 v21, v58, v59\n\t"
                         "v_smfmac_f32_16x16x64_bf16 v[68:71], a[20:23], v[152:159], v9\n\t" "s_waitcnt vmcnt(25)\n\t" "v_cvt_pk_bf16_f32 v22, v60, v61\n\t" "v_cvt_pk_bf16_f32 v23, v62, v63\n\t"
                         "v_smfmac_f32_16x16x64_bf16 v[72:75], a[24:27], v[160:167], v9\n\t" "s_waitcnt vmcnt(24)\n\t" "v_cvt_pk_bf16_f32 v24, v64, v65\n\t" "v_cvt_pk_bf16_f32 v25, v66, v67\n\t"
                         "v_smfmac_f32_16x16x64_bf16 v[76:79], a[28:31], v[168:175], v9\n\t" "s_waitcnt vmcnt(23)\n\t" "v_cvt_pk_bf16_f32 v26, v68, v69\n\t" "v_cvt_pk_bf16_f32 v27, v70, v71\n\t"
                         "v_smfmac_f32_16x16x64_bf16 v[80:83], a[32:35], v[176:183], v9\n\t" "s_waitcnt vmcnt(22)\n\t" "v_cvt_pk_bf16_f32 v28, v72, v73\n\t" "v_cvt_pk_bf16_f32 v29, v74, v75\n\t"
                         "v_smfmac_f32_16x16x64_bf16 v[84:87], a[36:39], v[184:191], v9\n\t" "s_waitcnt vmcnt(21)\n\t" "v_cvt_pk_bf16_f32 v30, v76, v77\n\t" "v_cvt_pk_bf16_f32 v31, v78, v79\n\t"
                         "v_smfmac_f32_16x16x64_bf16 v[88:91], a[40:43], v[192:199], v9\n\t" "s_waitcnt vmcnt(20)\n\t" "v_cvt_pk_bf16_f32 v32, v80, v81\n\t" "v_cvt_pk_bf16_f32 v33, v82, v83\n\t"
                         "v_smfmac_f32_16x16x64_bf16 v[92:95], a[44:47], v[200:207], v9\n\t" "s_waitcnt vmcnt(19)\n\t" "v_cvt_pk_bf16_f32 v34, v84, v85\n\t" "v_cvt_pk_bf16_f32 v35, v86, v87\n\t"
                         "v_smfmac_f32_16x16x64_bf16 v[96:99], a[48:51], v[208:215], v9\n\t" "s_waitcnt vmcnt(18)\n\t" "v_cvt_pk_bf16_f32 v36, v88, v89\n\t" "v_cvt_pk_bf16_f32 v37, v90, v91\n\t"
                         "v_smfmac_f32_16x16x64_bf16 v[100:103], a[52:55], v[216:223], v9\n\t" "s_waitcnt vmcnt(17)\n\t" "v_cvt_pk_bf16_f32 v38, v92, v93\n\t" "v_cvt_pk_bf16_f32 v39, v94, v95\n\t"
                         "v_smfmac_f32_16x16x64_bf16 v[104:107], a[56:59], v[224:231], v9\n\t" "s_waitcnt vmcnt(16)\n\t" "v_cvt_pk_bf16_f32 v40, v96, v97\n\t" "v_cvt_pk_bf16_f32 v41, v98, v99\n\t"
                         "v_smfmac_f32_16x16x64_bf16 v[108:111], a[60:63], v[232:239], v9\n\t" "s_waitcnt vmcnt(15)\n\t" "v_cvt_pk_bf16_f32 v42, v100, v101\n\t" "v_cvt_pk_bf16_f32 v43, v102, v103\n\t"
                         "v_smfmac_f32_16x16x64_bf16 v[0:3], a[64:67], v[16:23], v8\n\t" "s_waitcnt vmcnt(14)\n\t" "v_cvt_pk_bf16_f32 v44, v104, v105\n\t" "v_cvt_pk_bf16_f32 v45, v106, v107\n\t"
                         "v_smfmac_f32_16x16x64_bf16 v[0:3], a[68:71], v[24:31], v8\n\t" "s_waitcnt vmcnt(13)\n\t" "v_cvt_pk_bf16_f32 v46, v108, v109\n\t" "v_cvt_pk_bf16_f32 v47, v110, v111\n\t"
                         "v_smfmac_f32_16x16x64_bf16 v[0:3], a[72:75], v[32:39], v8\n\t" "s_waitcnt vmcnt(12)\n\t" "v_smfmac_f32_16x16x64_bf16 v[0:3], a[76:79], v[40:47], v8\n\t" "s_waitcnt vmcnt(6)\n\t"
                         "v_smfmac_f32_16x16x64_bf16 v[0:3], a[80:83], a[96:103], v8\n\t" "s_waitcnt vmcnt(4)\n\t" "v_smfmac_f32_16x16x64_bf16 v[0:3], a[84:87], a[104:111], v8\n\t" "s_waitcnt vmcnt(2)\n\t"
                         "v_smfmac_f32_16x16x64_bf16 v[0:3], a[88:91], a[112:119], v8\n\t" "s_waitcnt vmcnt(0)\n\t" "v_smfmac_f32_16x16x64_bf16 v[0:3], a[92:95], a[120:127], v8\n\t" "s_branch .Lv_end\n\t" ".Lsp_v0:\n\t"
                         "s_memtime vcc\n\t" "s_waitcnt lgkmcnt(0)\n\t" "s_sub_i32 s42, %10, vcc_lo\n\t" "s_cmp_gt_i32 s42, 80\n\t" "s_cbranch_scc1 .Lsp_v0\n\t" "s_max_i32 s42, s42, 0\n\t" "s_sub_i32 s42, 92, s42\n\t"
                         "s_and_b32 s42, s42, -4\n\t" "s_getpc_b64 vcc\n\t" "s_add_u32 vcc_lo, vcc_lo, s42\n\t" "s_addc_u32 vcc_hi, vcc_hi, 0\n\t" "s_setpc_b64 vcc\n\t" "s_nop 0\n\t" "s_nop 0\n\t" "s_nop 0\n\t" "s_nop 0\n\t"
                         "s_nop 0\n\t" "s_nop 0\n\t" "s_nop 0\n\t" "s_nop 0\n\t" "s_nop 0\n\t" "s_nop 0\n\t" "s_nop 0\n\t" "s_nop 0\n\t" "s_nop 0\n\t" "s_nop 0\n\t" "s_nop 0\n\t" "s_nop 0\n\t" "s_nop 0\n\t" "s_nop 0\n\t"
                         "s_nop 0\n\t" "s_nop 0\n\t" "global_load_dwordx4 a[96:99], v4, %3 sc1\n\t" "global_load_dwordx4 a[100:103], v4, %3 offset:512 sc1\n\t"
                         "global_load_dwordx4 a[104:107], v4, %3 offset:1024 sc1\n\t" "global_load_dwordx4 a[108:111], v4, %3 offset:1536 sc1\n\t" "global_load_dwordx4 a[112:115], v4, %3 offset:2048 sc1\n\t"
                         "global_load_dwordx4 a[116:119], v4, %3 offset:2560 sc1\n\t" "global_load_dwordx4 a[120:123], v4, %3 offset:3072 sc1\n\t" "global_load_dwordx4 a[124:127], v4, %3 offset:3584 sc1\n\t" "s_nop 15\n\t"
                         "s_nop 6\n\t" "global_load_dwordx4 a[0:3], v5, %7 offset:-4096 sc1\n\t" "global_load_dwordx4 a[4:7], v5, %7 offset:-3072 sc1\n\t" "global_load_dwordx4 a[8:11], v5, %7 offset:-2048 sc1\n\t"
                         "global_load_dwordx4 a[12:15], v5, %7 offset:-1024 sc1\n\t" "global_load_dwordx4 a[16:19], v5, %7 sc1\n\t" "global_load_dwordx4 a[20:23], v5, %7 offset:1024 sc1\n\t"
                         "global_load_dwordx4 a[24:27], v5, %7 offset:2048 sc1\n\t" "global_load_dwordx4 a[28:31], v5, %7 offset:3072 sc1\n\t" "global_load_dwordx4 a[32:35], v5, %8 offset:-4096 sc1\n\t"
                         "global_load_dwordx4 a[36:39], v5, %8 offset:-3072 sc1\n\t" "global_load_dwordx4 a[40:43], v5, %8 offset:-2048 sc1\n\t" "global_load_dwordx4 a[44:47], v5, %8 offset:-1024 sc1\n\t"
                         "global_load_dwordx4 a[48:51], v5, %8 sc1\n\t" "global_load_dwordx4 a[52:55], v5, %8 offset:1024 sc1\n\t" "global_load_dwordx4 a[56:59], v5, %8 offset:2048 sc1\n\t"
                         "global_load_dwordx4 a[60:63], v5, %8 offset:3072 sc1\n\t" "s_nop 15\n\t" "s_nop 10\n\t" "global_load_dwordx4 a[80:83], v7, %6 sc1\n\t" "global_load_dwordx4 a[84:87], v7, %6 offset:1024 sc1\n\t"
                         "global_load_dwordx4 a[88:91], v7, %6 offset:2048 sc1\n\t" "global_load_dwordx4 a[92:95], v7, %6 offset:3072 sc1\n\t" "s_mov_b64 exec, %9\n\t"
                         "global_load_dwordx4 a[64:67], v6, %6 offset:-2048 sc1\n\t" "global_load_dwordx4 a[68:71], v6, %6 offset:-1536 sc1\n\t" "global_load_dwordx4 a[72:75], v6, %6 offset:-1024 sc1\n\t"
                         "global_load_dwordx4 a[76:79], v6, %6 offset:-512 sc1\n\t" "s_mov_b64 exec, -1\n\t" "s_waitcnt vmcnt(23)\n\t" "v_smfmac_f32_16x16x64_bf16 v[48:51], a[0:3], v[112:119], v9\n\t"
                         "s_waitcnt vmcnt(22)\n\t" "v_smfmac_f32_16x16x64_bf16 v[52:55], a[4:7], v[120:127], v9\n\t" "s_waitcnt vmcnt(21)\n\t" "v_smfmac_f32_16x16x64_bf16 v[56:59], a[8:11], v[128:135], v9\n\t"
                         "s_waitcnt vmcnt(20)\n\t" "v_cvt_pk_bf16_f32 v16, v48, v49\n\t" "v_cvt_pk_bf16_f32 v17, v50, v51\n\t" "v_smfmac_f32_16x16x64_bf16 v[60:63], a[12:15], v[136:143], v9\n\t" "s_waitcnt vmcnt(19)\n\t"
                         "v_cvt_pk_bf16_f32 v18, v52, v53\n\t" "v_cvt_pk_bf16_f32 v19, v54, v55\n\t" "v_smfmac_f32_16x16x64_bf16 v[64:67], a[16:19], v[144:151], v9\n\t" "s_waitcnt vmcnt(18)\n\t"
                         "v_cvt_pk_bf16_f32 v20, v56, v57\n\t" "v_cvt_pk_bf16_f32 v21, v58, v59\n\t" "v_smfmac_f32_16x16x64_bf16 v[68:71], a[20:23], v[152:159], v9\n\t" "s_waitcnt vmcnt(17)\n\t"
                         "v_cvt_pk_bf16_f32 v22, v60, v61\n\t" "v_cvt_pk_bf16_f32 v23, v62, v63\n\t" "v_smfmac_f32_16x16x64_bf16 v[72:75], a[24:27], v[160:167], v9\n\t" "s_waitcnt vmcnt(16)\n\t"
                         "v_cvt_pk_bf16_f32 v24, v64, v65\n\t" "v_cvt_pk_bf16_f32 v25, v66, v67\n\t" "v_smfmac_f32_16x16x64_bf16 v[76:79], a[28:31], v[168:175], v9\n\t" "s_waitcnt vmcnt(15)\n\t"
                         "v_cvt_pk_bf16_f32 v26, v68, v69\n\t" "v_cvt_pk_bf16_f32 v27, v70, v71\n\t" "v_smfmac_f32_16x16x64_bf16 v[80:83], a[32:35], v[176:183], v9\n\t" "s_waitcnt vmcnt(14)\n\t"
                         "v_cvt_pk_bf16_f32 v28, v72, v73\n\t" "v_cvt_pk_bf16_f32 v29, v74, v75\n\t" "v_smfmac_f32_16x16x64_bf16 v[84:87], a[36:39], v[184:191], v9\n\t" "s_waitcnt vmcnt(13)\n\t"
                         "v_cvt_pk_bf16_f32 v30, v76, v77\n\t" "v_cvt_pk_bf16_f32 v31, v78, v79\n\t" "v_smfmac_f32_16x16x64_bf16 v[88:91], a[40:43], v[192:199], v9\n\t" "s_waitcnt vmcnt(12)\n\t"
                         "v_cvt_pk_bf16_f32 v32, v80, v81\n\t" "v_cvt_pk_bf16_f32 v33, v82, v83\n\t" "v_smfmac_f32_16x16x64_bf16 v[92:95], a[44:47], v[200:207], v9\n\t" "s_waitcnt vmcnt(11)\n\t"
                         "v_cvt_pk_bf16_f32 v34, v84, v85\n\t" "v_cvt_pk_bf16_f32 v35, v86, v87\n\t" "v_smfmac_f32_16x16x64_bf16 v[96:99], a[48:51], v[208:215], v9\n\t" "s_waitcnt vmcnt(10)\n\t"
                         "v_cvt_pk_bf16_f32 v36, v88, v89\n\t" "v_cvt_pk_bf16_f32 v37, v90, v91\n\t" "v_smfmac_f32_16x16x64_bf16 v[100:103], a[52:55], v[216:223], v9\n\t" "s_waitcnt vmcnt(9)\n\t"
                         "v_cvt_pk_bf16_f32 v38, v92, v93\n\t" "v_cvt_pk_bf16_f32 v39, v94, v95\n\t" "v_smfmac_f32_16x16x64_bf16 v[104:107], a[56:59], v[224:231], v9\n\t" "s_waitcnt vmcnt(8)\n\t"
                         "v_cvt_pk_bf16_f32 v40, v96, v97\n\t" "v_smfmac_f32_16x16x64_bf16 v[108:111], a[60:63], v[232:239], v9\n\t" "s_waitcnt vmcnt(7)\n\t" "v_cvt_pk_bf16_f32 v41, v98, v99\n\t"
                         "v_smfmac_f32_16x16x64_bf16 v[0:3], a[80:83], a[96:103], v8\n\t" "s_waitcnt vmcnt(6)\n\t" "v_cvt_pk_bf16_f32 v42, v100, v101\n\t"
                         "v_smfmac_f32_16x16x64_bf16 v[0:3], a[84:87], a[104:111], v8\n\t" "s_waitcnt vmcnt(5)\n\t" "v_cvt_pk_bf16_f32 v43, v102, v103\n\t" "v_smfmac_f32_16x16x64_bf16 v[0:3], a[88:91], a[112:119], v8\n\t"
                         "s_waitcnt vmcnt(4)\n\t" "v_cvt_pk_bf16_f32 v44, v104, v105\n\t" "v_smfmac_f32_16x16x64_bf16 v[0:3], a[92:95], a[120:127], v8\n\t" "s_waitcnt vmcnt(3)\n\t"
                         "v_cvt_pk_bf16_f32 v45, v106, v107\n\t" "v_smfmac_f32_16x16x64_bf16 v[0:3], a[64:67], v[16:23], v8\n\t" "s_waitcnt vmcnt(2)\n\t" "v_cvt_pk_bf16_f32 v46, v108, v109\n\t"
                         "v_smfmac_f32_16x16x64_bf16 v[0:3], a[68:71], v[24:31], v8\n\t" "s_waitcnt vmcnt(1)\n\t" "v_cvt_pk_bf16_f32 v47, v110, v111\n\t" "v_smfmac_f32_16x16x64_bf16 v[0:3], a[72:75], v[32:39], v8\n\t"
                         "s_waitcnt vmcnt(0)\n\t" "v_smfmac_f32_16x16x64_bf16 v[0:3], a[76:79], v[40:47], v8\n\t" ".Lv_end:\n\t" "s_nop 7\n\t" "v_pk_add_f32 v[0:1], v[0:1], v[2:3]\n\t" "v_cvt_pk_bf16_f32 v2, v0, v1\n\t"
                         "v_cmp_u_f32 vcc, v0, v1\n\t" "s_cmp_eq_u64 vcc, 0\n\t" "v_mov_b32_dpp v3, v2 quad_perm:[1,0,3,2] row_mask:0xf bank_mask:0xf\n\t" "s_cselect_b64 %0, -1, 0\n\t" "v_perm_b32 v0, v2, v3, v11\n\t"
                         "s_mov_b64 exec, %0\n\t" "global_store_dword v10, v0, %5 offset:-4096 sc1\n\t" "global_store_dword v10, v0, %4 offset:-4096 sc1\n\t" "s_memrealtime %1\n\t" "s_mov_b64 exec, -1\n\t"
                         "s_waitcnt lgkmcnt(0)\n\t"
                         : "=&s"(ok), "=&s"(tp8)
                         : "{v[4:11]}"(ad), "s"(up), "s"(mnx), "s"((u64)mnx ^ 8192), "s"(memp + 16384), "s"(memp + 32768), "s"(memp + 49152), "s"(0xc30000c3c30000c3ull),
                           "s"(uni((int)(tgt - (u32)(v < 8 ? cfg.dU : cfg.cR2)))), "s"(v < 8 ? 0 : (v < 24 ? cfg.s2 : cfg.T3)), VALL : "memory", "vcc", "scc", "s42");
            if (__builtin_expect(ok != 0, 1) || s < 0) break;
            if (all || --budget < 0) {
                asm volatile("v_min_u32 v0, -2, v0\n\tglobal_store_dword v10, v0, %0 offset:-4096 sc1\n\tglobal_store_dword v10, v0, %1 offset:-4096 sc1"
                             : : "s"(mnx), "s"((u64)mnx ^ 8192), "{v[4:11]}"(ad), VALL : "memory");
                break;
            }
            if ((++tries & 15) == 0) {
                u32 m = 0;
#pragma nounroll
                for (int i = 0; i < 32; ++i) {
                    u32x4 t;
                    gld16c<0>(t, i < 24 ? memp + 12288 : up, i < 20 ? (4 + i + ((4 + i) & 24)) * 1024 + ad[3] : (i < 24 ? i * 512 - 8192 + ad[2] : (i & 7) * 512 + ad[0])); waitv<0>(t);
                    m = max3u(max3u(m, t[0], t[1]), t[2], t[3]);
                }
                all = __builtin_amdgcn_ballot_w64(m == 0xffffffffu) == 0;
            }
            phi += (u32)cfg.G;
            zero();
        }
        asm volatile("s_barrier\n\t" "ds_read_b128 v[112:115], %0\n\t" "ds_read_b128 v[116:119], %0 offset:1024\n\t" "ds_read_b128 v[120:123], %0 offset:2048\n\t" "ds_read_b128 v[124:127], %0 offset:3072\n\t"
                     "ds_read_b128 v[128:131], %0 offset:4096\n\t" "ds_read_b128 v[132:135], %0 offset:5120\n\t" "ds_read_b128 v[136:139], %0 offset:6144\n\t" "ds_read_b128 v[140:143], %0 offset:7168\n\t"
                     "ds_read_b128 v[144:147], %0 offset:8192\n\t" "ds_read_b128 v[148:151], %0 offset:9216\n\t" "ds_read_b128 v[152:155], %0 offset:10240\n\t" "ds_read_b128 v[156:159], %0 offset:11264\n\t"
                     "ds_read_b128 v[160:163], %0 offset:12288\n\t" "ds_read_b128 v[164:167], %0 offset:13312\n\t" "ds_read_b128 v[168:171], %0 offset:14336\n\t" "ds_read_b128 v[172:175], %0 offset:15360\n\t"
                     "ds_read_b128 v[176:179], %0 offset:16384\n\t" "ds_read_b128 v[180:183], %0 offset:17408\n\t" "ds_read_b128 v[184:187], %0 offset:18432\n\t" "ds_read_b128 v[188:191], %0 offset:19456\n\t"
                     "ds_read_b128 v[192:195], %0 offset:20480\n\t" "ds_read_b128 v[196:199], %0 offset:21504\n\t" "ds_read_b128 v[200:203], %0 offset:22528\n\t" "ds_read_b128 v[204:207], %0 offset:23552\n\t"
                     "ds_read_b128 v[208:211], %0 offset:24576\n\t" "ds_read_b128 v[212:215], %0 offset:25600\n\t" "ds_read_b128 v[216:219], %0 offset:26624\n\t" "ds_read_b128 v[220:223], %0 offset:27648\n\t"
                     "ds_read_b128 v[224:227], %0 offset:28672\n\t" "ds_read_b128 v[228:231], %0 offset:29696\n\t" "ds_read_b128 v[232:235], %0 offset:30720\n\t" "ds_read_b128 v[236:239], %0 offset:31744\n\t"
                     : : "v"(lds + ad[3] + ((s + 1) & 1) * 32768), VALL : "memory");
        if (s >= 0) {
            follow(phi, (int)(tt & 0xffffu) + 5 * (v >> 2) / 2 - VISV, (u32)tp8, cfg.G);
            if (vb == 0 && lane < 8) gst4c(fl, (F_CUR + lane) * 128, s - 1);
        }
        phi += tt & 0xffffu;
        memp = mnx; up = ub + uslot(sd, s + 1);
        zero();
        tt = sched_ld0(s + 1);
        clk.calib();
        while (s >= 0) {
            u32 h0;
            spin_to(clk.at(phi + (u32)cfg.dV));
            asm volatile("global_load_dword %0, %1, %2 offset:-4096 sc1\n\ts_waitcnt vmcnt(0)" : "=&v"(h0) : "v"(ho), "s"(memp) : "memory");
            if (__builtin_amdgcn_ballot_w64(h0 == 0xffffffffu) == 0 || --budget < 0) break;
            phi += (u32)cfg.G;
        }
        asm volatile(".Lkm_lv:");
    }
}

DI void xcopy_task(const Ptrs& p, int j, int w) {
    const int tid = 64 * w + lanex();
    Sync y;
    sync_ld(y, p.ws);
    u32x4 xv[10];
    sfor<0, 10>([&](auto I) { gld16<I.v * 128>(xv[I.v], p.x + (size_t)j * 40960, (u32)((tid & 31) * 1280 + (tid >> 5) * 16)); });
    sfor<0, 10>([&](auto I) { waitv<9 - I.v>(xv[I.v]); });
    asm volatile("" : "+v"(y.r), "+v"(y.g), "+v"(y.e));
    sfor<0, 10>([&](auto I) { gst16(p.ws + OFF_XF + (size_t)j * 40960 + I.v * 4096, (u32)tid * 16, xv[I.v]); });
    waitvm<0>();
    barrier();
    const u64 t = sync_tag(y.e);
    if (tid == 0) gst8(p.ws + OFF_LAT, 1536 + (u32)j * 8, u32x2{(u32)t, (u32)(t >> 32)});
}
DI void xcopy_wait(const char* ws, u64 t) {
#pragma nounroll
    for (int it = 0; it < (1 << 16); ++it) {
        u32x2 r;
        asm volatile("s_nop 4\n\tglobal_load_dwordx2 %0, %1, %2 offset:1536 sc1\n\ts_waitcnt vmcnt(0)" : "=&v"(r) : "v"(lanex() * 8), "s"(ws + OFF_LAT) : "memory");
        if (__builtin_amdgcn_ballot_w64(r[0] != (u32)t || r[1] != (u32)(t >> 32)) == 0) break;
        __builtin_amdgcn_s_sleep(16);
    }
}
DI void init_task(const Ptrs& p, int j, u32 lds, int w) {
    const int lane = lanex(), tid = 64 * w + lane;
    Sync y;
    sync_ld(y, p.ws);
    const u32x4 ff = u32x4{~0u, ~0u, ~0u, ~0u};
    const int i = j * 256 + tid, r = j < 8 ? j & 1 : min(j / 2 - 2, NREG - 1);
    char* const vb = p.ws + (r < 2 ? 0 : OFF_U - (4u << 20)) + ((size_t)r << 21) + 1984 * 1024 + (j < 8 ? j & 6 : (j & 1) * 2 + (j >> 1 == 15) * 4) * 8192;
    const u32 vo = (u32)((w >> 1) * 8192 + (w & 1) * 1024);
    gst16(vb, vo + lane * 16, ff);
    if (i < F_N * 8) gst16(p.ws + OFF_ST, (u32)i * 16, u32x4{i == F_ONE * 8 ? 1000u : ~0u, ~0u, ~0u, ~0u});
    gst16(p.ws + 65536, (u32)i * 16, ff);
    if (i < 4096) {
        const int k = (i & 511) + ((i >> 1) & 0x600);
        u32x4 m;
        gld16<0>(m, p.mem0, (u32)((2 * ((k >> 2) & 3) + (k & 1)) * 1280 + min(2 * (k >> 4) + ((k >> 1) & 1), 79) * 16));
        waitv<0>(m);
        gst16(p.ws, (u32)i * 16, k < 640 ? u32x4{min(m[0], 0xfffffffeu), min(m[1], 0xfffffffeu), min(m[2], 0xfffffffeu), min(m[3], 0xfffffffeu)} : ff);
    }
#pragma unroll
    for (int c = 0; c < 6; ++c) gst16(p.ws + OFF_U + c * 131072 - (c == 5) * 122880, (u32)i * 16, ff);
    waitvm<0>();
    asm volatile("" : "+v"(y.r), "+v"(y.g), "+v"(y.e));
    barrier();
    if (w) return;
    u32 dt[2] = {0xffffffu, 0xffffffu};
#pragma unroll
    for (int c = 0; c < 2 * NSAMP; ++c) {
        u32 v;
        const u32 t0 = mt();
        asm volatile("global_load_dword %0, %1, %2 sc1\n\ts_waitcnt vmcnt(0)" : "=&v"(v) : "v"((c & 1) * 8192 + (c >> 1) * 256), "s"(vb) : "memory");
        dt[c & 1] = min(dt[c & 1], mt() - t0);
    }
    const u64 t = sync_tag(y.e);
    const u32 vote = (u32)((dt[0] > dt[1]) ^ xhalf() ^ (int)((vb - p.ws) >> 21)) & 1u;
    if (tid == 0) {
        if (j == 0) gst8(p.ws + OFF_LAT, 512, u32x2{(u32)t, rt()});
        gst8(p.ws + OFF_LAT, (u32)j * 8, u32x2{(u32)t | vote, (u32)(t >> 32)});
    }
}

DI float xmax(float v) {
    const auto a = __builtin_amdgcn_permlane32_swap(__float_as_uint(v), __float_as_uint(v), false, false);
    v = fmaxf(__uint_as_float(a[0]), __uint_as_float(a[1]));
    const auto b = __builtin_amdgcn_permlane16_swap(__float_as_uint(v), __float_as_uint(v), false, false);
    return fmaxf(__uint_as_float(b[0]), __uint_as_float(b[1]));
}
DI float xsum(float v) {
    const auto a = __builtin_amdgcn_permlane32_swap(__float_as_uint(v), __float_as_uint(v), false, false);
    v = __uint_as_float(a[0]) + __uint_as_float(a[1]);
    const auto b = __builtin_amdgcn_permlane16_swap(__float_as_uint(v), __float_as_uint(v), false, false);
    return __uint_as_float(b[0]) + __uint_as_float(b[1]);
}
DI void gst16c(char* sbase, u32 voff, u32x4 v) {
    asm volatile("global_store_dwordx4 %0, %1, %2 sc1\n\ts_nop 1" : : "v"(voff), "v"(v), "s"(sbase) : "memory");
}
DI void gst8c(char* sbase, u32 voff, u32x2 v) {
    asm volatile("global_store_dwordx2 %0, %1, %2 sc1\n\ts_nop 1" : : "v"(voff), "v"(v), "s"(sbase) : "memory");
}
#define WAIT6(N, r, o) asm volatile("s_waitcnt vmcnt(" #N ")" : "+v"(r[o]), "+v"(r[o + 1]), "+v"(r[o + 2]), "+v"(r[o + 3]), "+v"(r[o + 4]), "+v"(r[o + 5]) : : "memory")
#define WSLEEP 90
DI void wg_wait(const char* fl, int w, int i0, int t0, int i1, int t1, int i2, int t2, int i3, int t3, int dt, bool acq) {
    if (w == 0) {
        const int lane = lanex(), tg = lane == 1 ? t1 : (lane == 2 ? t2 : (lane == 3 ? t3 : t0));
        const int ix = tg <= 0 ? i0 : (lane == 1 ? i1 : (lane == 2 ? i2 : (lane == 3 ? i3 : i0)));
#pragma nounroll
        for (int it = 0; it < (1 << 14); ++it) {
            u32 v;
            flag_ld(v, fl, ix);
            asm volatile("s_waitcnt vmcnt(0)" : "+v"(v) : : "memory");
            if (__builtin_amdgcn_ballot_w64((int)(v + 1u) < tg) == 0) break;
            const u32 wake = rt() + (u32)(dt + 180 * min(max(t0 - 2 - uni((int)v), 0), 64));
            while ((int)(rt() - wake) < 0) __builtin_amdgcn_s_sleep(4);
        }
        if (acq) acquire();
    }
    barrier();
}
#define PXT 58
#define PDL 60
#define ODL 30
#define TDT 35
template <bool LATE>
DI void px_wave(const Ptrs& p, const ChainCfg& cfg, int s, int h, int slow, int w, u32 lds, bool keep) {
    const int lane = lanex(), n = lane & 15, g = lane >> 4;
    const char* qb = p.ws + OFF_QX + (size_t)(s * NH + h) * 12288;
    const char* kb = p.ws + OFF_KX + (size_t)(s * NH + h) * 12288;
    const char* mb = p.ws + OFF_KM + (size_t)(s * NH + h) * 3072;
    const u32 ko = (u32)((n >> 3) * 6144 + (8 * ((n >> 2) & 1) + (n & 3) + 16 * g) * 16);
    const u32 mo = (u32)(g * 128 + (n & 7) * 16);
    u32x4 qf[12], kf[12], mf[6];
    sfor<0, 12>([&](auto I) { gld16c<(I.v % 3) * 1024>(qf[I.v], qb + (I.v / 3) * 3072, lane * 16); gap<SMP>(slow); });
    if (!SMP) pace(slow);
    sfor<0, 12>([&](auto I) { gld16c<(I.v % 3) * 1024 + (I.v / 6) * 64>(kf[I.v], kb + ((I.v / 3) & 1) * 3072, ko); gap<SMP>(slow); });
    if (!SMP) pace(slow);
    if constexpr (LATE) { WAIT6(0, qf, 0); WAIT6(0, qf, 6); WAIT6(0, kf, 0); }
    else {
        sfor<0, 6>([&](auto I) { gld16c<I.v * 512>(mf[I.v], mb, mo); });
        WAIT6(24, qf, 0); WAIT6(18, qf, 6); WAIT6(12, kf, 0);
    }
    f32x4 sc[2][3];
#pragma unroll
    for (int tt = 0; tt < 2; ++tt)
#pragma unroll
        for (int T = 0; T < 3; ++T) sc[tt][T] = f32x4{0.f, 0.f, 0.f, 0.f};
#pragma unroll
    for (int ks = 0; ks < 6; ++ks) { sc[0][0] = mfma16(kf[ks], qf[ks], sc[0][0]); sc[1][0] = mfma16(kf[ks], qf[6 + ks], sc[1][0]); }
    WAIT6(6, kf, 6);
#pragma unroll
    for (int ks = 0; ks < 6; ++ks) { sc[0][1] = mfma16(kf[6 + ks], qf[ks], sc[0][1]); sc[1][1] = mfma16(kf[6 + ks], qf[6 + ks], sc[1][1]); }
    if constexpr (LATE) {
        wg_wait(p.ws + OFF_ST, w, F_CUR + (h & 7), min(s + 1, NSEG - 2), F_PROG + 8 * 62 + (h & 7), s == NSEG - 2 ? 8 : 0, F_ONE, 0, F_ONE, 0, cfg.s1, false);
        bool ok = s != NSEG - 1;
        if (!ok) nap(cfg.T);
#pragma nounroll
        for (int it = 0; it < (1 << 14); ++it) {
            u32 v;
            sfor<0, 6>([&](auto I) { gld16c<I.v * 512>(mf[I.v], mb, mo); });
            flag_ld(v, p.ws + OFF_ST, F_PROG + 8 * 63 + (h & 7));
            WAIT6(0, mf, 0);
            asm volatile("" : "+v"(v));
            u32 m = 0;
#pragma unroll
            for (int i = 0; i < 24; ++i) m = max(m, mf[i >> 2][i & 3]);
            if (ok || __builtin_amdgcn_ballot_w64(m == ~0u) == 0) break;
            ok = uni((int)v) >= 7;
        }
    }
    WAIT6(0, mf, 0);
#pragma unroll
    for (int ks = 0; ks < 6; ++ks) { sc[0][2] = mfma16(mf[ks], qf[ks], sc[0][2]); sc[1][2] = mfma16(mf[ks], qf[6 + ks], sc[1][2]); }
    const float kscale = 0.07216878364870322f;
    char* pb = p.ws + OFF_PX + (size_t)s * 81920;
#pragma unroll
    for (int tt = 0; tt < 2; ++tt) {
        float v[12], mx = -3.0e38f;
#pragma unroll
        for (int i = 0; i < 12; ++i) {
            const float t = rbf(rbf(sc[tt][i >> 2][i & 3]) * kscale);
            v[i] = (i >= 8 || 8 * g + i <= 16 * tt + n) ? t : -3.0e38f;
            mx = fmaxf(mx, v[i]);
        }
        mx = xmax(mx);
        float sx = 0.f, sm = 0.f;
#pragma unroll
        for (int i = 0; i < 12; ++i) {
            v[i] = __builtin_amdgcn_exp2f((v[i] - mx) * 1.4426950408889634f);
            if (i < 8) sx += v[i]; else sm += v[i];
        }
        const float inv = __builtin_amdgcn_rcpf(xsum(sx + 0.5f * sm));
        const u32x4 px = u32x4{cvtpk(v[0] * inv, v[1] * inv), cvtpk(v[2] * inv, v[3] * inv), cvtpk(v[4] * inv, v[5] * inv), cvtpk(v[6] * inv, v[7] * inv)};
        const u32x2 pm = u32x2{cvtpk(v[9] * inv, v[8] * inv), cvtpk(v[11] * inv, v[10] * inv)};
        const int ox = h * 2048 + tt * 1024, om = 65536 + (h >> 2) * 2048 + (h & 3) * 256 + tt * 1024;
        if (!LATE && keep) {
            *LDSP(u32x4, lds + ox + lane * 16) = px;
            *LDSP(u32x2, lds + om + n * 16 + (g & 1) * 8) = pm;
        } else {
            gst16c(pb + ox, lane * 16, px);
            gst8c(pb + om, (u32)(n * 16 + (g & 1) * 8), pm);
        }
    }
}

#define WAIT10(N, r) asm volatile("s_waitcnt vmcnt(" #N ")" : "+v"(r[0]), "+v"(r[1]), "+v"(r[2]), "+v"(r[3]), "+v"(r[4]), "+v"(r[5]), "+v"(r[6]), "+v"(r[7]), "+v"(r[8]), "+v"(r[9]) : : "memory")
DI void out_task(const Ptrs& p, int s, int cg0, int cg1, u32 lds, int slow, int w, int sd) {
    u32x4 pf[2][10];
    if (cg1 - cg0 > 1) {
        const u32 la = lds + lanex() * 16;
#pragma unroll
        for (int i = 0; i < 20; ++i) pf[i & 1][i >> 1] = ldsr16(la + (i < 16 ? w * 16384 + i * 1024 : 65536 + w * 4096 + (i - 16) * 1024));
    } else {
        const char* pb = p.ws + OFF_PX + (size_t)s * 81920;
        const u32 l16 = lanex() * 16;
        sfor<0, 20>([&](auto I) { gld16c<(I.v & 3) * 1024>(pf[I.v & 1][I.v >> 1], pb + (I.v < 16 ? w * 16384 + (I.v >> 2) * 4096 : 65536 + w * 4096), l16); gap<SMO>(slow); if (!SMO && I.v == 9) pace(slow); });
        WAIT10(0, pf[0]); WAIT10(0, pf[1]);
    }
#pragma nounroll
    for (int cg = cg0; cg < cg1; ++cg) {
    const int lane = lanex(), tid = 64 * w + lane, n = lane & 15, g = lane >> 4;
    const char* db = p.ws + OFF_D + s * D_STRIDE + (size_t)(16 * cg) * 16384 + w * 4096;
    const char* ub = p.ws + uslot(sd, s) + (size_t)(16 * cg) * 4096 + w * 1024;
    const u32 dof = (u32)((n >> 3) * 16384 + g * 128 + (n & 7) * 16), uof = (u32)((n >> 3) * 4096 + g * 128 + (n & 7) * 16);
    u32x4 af[3][10];
    auto aload = [&](u32x4 (&r)[10], int ct) {
        if (!SMO) pace(slow);
        sfor<0, 8>([&](auto I) { gld16c<I.v * 512>(r[I.v], db + ct * 32768, dof); gap<SMO>(slow); });
        gld16c<0>(r[8], ub + ct * 8192, uof); gap<SMO>(slow); gld16c<512>(r[9], ub + ct * 8192, uof); gap<SMO>(slow);
    };
    aload(af[0], 0);
    aload(af[1], 1);
    aload(af[2], 2);
    const int tok = 32 * s + (tid >> 3), col = 128 * cg + 8 * (tid & 7);
    u32x4 bb[2]; u32 gv;
    gld16c<0>(bb[0], p.ws + OFF_BOUT, (u32)(s * 40960 + col * 64 + (tid >> 3) * 16)); gld16c<0>(bb[1], p.ws + OFF_BOUT + 4096, (u32)(s * 40960 + col * 64 + (tid >> 3) * 16));
    asm volatile("global_load_dword %0, %1, %2 sc1" : "=v"(gv) : "v"(tok * 4), "s"(p.ws + OFF_GATE) : "memory");
    barrier();
    const u32 wa = lds + (u32)w * 16896 + n * 528 + g * 16;
    sfor<0, 8>([&](auto I) {
        constexpr int ct = I.v;
        u32x4 (&r)[10] = af[ct % 3];
        if constexpr (ct == 0) WAIT10(23, r);
        else if constexpr (ct < 3) WAIT10(23, r);
        else if constexpr (ct < 6) WAIT10(20, r);
        else if constexpr (ct == 6) WAIT10(10, r);
        else WAIT10(0, r);
        f32x4 c0 = f32x4{0.f, 0.f, 0.f, 0.f}, c1 = c0;
#pragma unroll
        for (int i = 0; i < 10; ++i) { c0 = mfma16(r[i], pf[0][i], c0); c1 = mfma16(r[i], pf[1][i], c1); }
        *LDSP(f32x4, wa + ct * 64) = c0;
        *LDSP(f32x4, wa + ct * 64 + 16 * 528) = c1;
        PIN();
        if constexpr (ct + 3 < 8) aload(r, ct + 3);
        PIN();
    });
    asm volatile("" : "+v"(bb[0]), "+v"(bb[1]), "+v"(gv));
    barrier();
    const u32 ra = lds + (u32)(tid >> 3) * 528 + (tid & 7) * 32;
    const float gt = __uint_as_float(gv), g1 = rbf(1.f - gt);
    u32 ov[8];
#pragma unroll
    for (int i = 0; i < 4; ++i) {
        const u32 ri = ra + 16 * (i & 1) + 256 * (i >> 1);
        const f32x4 z = (*LDSP(f32x4, ri) + *LDSP(f32x4, ri + 16896)) + (*LDSP(f32x4, ri + 2 * 16896) + *LDSP(f32x4, ri + 3 * 16896));
#pragma unroll
        for (int j = 0; j < 2; ++j) {
            const u32 b = bb[i >> 1][2 * (i & 1) + j];
            const u32 a = cvtpk(z[2 * j], z[2 * j + 1]);
            const u32 ga = cvtpk(gt * __uint_as_float(a << 16), gt * __uint_as_float(a & 0xffff0000u));
            const u32 gb = cvtpk(g1 * __uint_as_float(b << 16), g1 * __uint_as_float(b & 0xffff0000u));
            ov[2 * i + j] = cvtpk(__uint_as_float(ga << 16) + __uint_as_float(gb << 16), __uint_as_float(ga & 0xffff0000u) + __uint_as_float(gb & 0xffff0000u));
        }
    }
    gst16p(p.out, (u32)(tok * 1280 + col * 2), u32x4{ov[0], ov[1], ov[2], ov[3]});
    gst16p(p.out, (u32)(tok * 1280 + col * 2 + 128), u32x4{ov[4], ov[5], ov[6], ov[7]});
    }
}

DI void out_tail(const Ptrs& p, const ChainCfg& cfg, int s, int cg, u32 lds, int w, int sd, int gr) {
    const int lane = lanex(), tid = 64 * w + lane, n = lane & 15, g = lane >> 4;
    const char* db = p.ws + OFF_D + s * D_STRIDE + (size_t)(4 * cg) * 16384 + w * 4096;
    const char* ub = p.ws + uslot(sd, s) + (size_t)(4 * cg) * 4096 + w * 1024;
    const char* pb = p.ws + OFF_PX + (size_t)s * 81920;
    const u32 dof = (u32)((n >> 3) * 16384 + g * 128 + (n & 7) * 16), uof = (u32)((n >> 3) * 4096 + g * 128 + (n & 7) * 16);
    u32x4 pf[2][10], af[2][10];
    sfor<0, 16>([&](auto I) { gld16c<(I.v & 7) * 512>(af[I.v >> 3][I.v & 7], db + (I.v >> 3) * 32768, dof); });
    const int tok = 32 * s + (tid >> 3), col = 32 * cg + 4 * (tid & 7);
    u32x2 bb; u32 gv;
    asm volatile("global_load_dwordx2 %0, %2, %3 sc1\n\tglobal_load_dword %1, %4, %5 sc1\n\ts_waitcnt vmcnt(0)" : "=&v"(bb), "=&v"(gv)
                 : "v"(s * 40960 + (col >> 3) * 512 + (tid >> 3) * 16 + (col & 7) * 2), "s"(p.ws + OFF_BOUT), "v"(tok * 4), "s"(p.ws + OFF_GATE) : "memory");
    sfor<0, 16>([&](auto I) { waitv<0>(af[I.v >> 3][I.v & 7]); });
    bool ok = s != NSEG - 1;
    wg_wait(p.ws + OFF_ST, w, F_CUR + gr, min(s + 1, NSEG - 2), ok ? F_PX + s : F_UD, ok ? 8 : 16, F_UD + 1, ok ? 0 : 16, F_ONE, 0, cfg.s1, false);
    if (!ok) nap(cfg.cR);
    const u32 wa = lds + (u32)w * 4608 + n * 144 + g * 16;
#pragma nounroll
    for (int it = 0; it < (1 << 14); ++it) {
        u32 v;
        flag_ld(v, p.ws + OFF_ST, F_PX + s);
        sfor<0, 8>([&](auto I) { gld16c<(I.v & 1) * 2048>(pf[0][I.v], pb + w * 16384 + (I.v >> 1) * 4096, lane * 16); });
        gld16c<0>(pf[0][8], pb + 65536 + w * 4096, lane * 16); gld16c<2048>(pf[0][9], pb + 65536 + w * 4096, lane * 16);
        sfor<0, 4>([&](auto I) { gld16c<(I.v & 1) * 512>(af[I.v >> 1][8 + (I.v & 1)], ub + (I.v >> 1) * 8192, uof); });
        sfor<0, 8>([&](auto I) { gld16c<(I.v & 1) * 2048 + 1024>(pf[1][I.v], pb + w * 16384 + (I.v >> 1) * 4096, lane * 16); });
        gld16c<1024>(pf[1][8], pb + 65536 + w * 4096, lane * 16); gld16c<3072>(pf[1][9], pb + 65536 + w * 4096, lane * 16);
        WAIT10(10, pf[0]); WAIT10(10, af[0]); WAIT10(10, af[1]);
        asm volatile("" : "+v"(v));
        float chk = 0.f;
#pragma unroll
        for (int tt = 0; tt < 2; ++tt) {
            if (tt) WAIT10(0, pf[1]);
            f32x4 c0 = f32x4{0.f, 0.f, 0.f, 0.f}, c1 = c0;
#pragma unroll
            for (int i = 0; i < 10; ++i) { c0 = mfma16(af[0][i], pf[tt][i], c0); c1 = mfma16(af[1][i], pf[tt][i], c1); }
            *LDSP(f32x4, wa + tt * 2304) = c0;
            *LDSP(f32x4, wa + tt * 2304 + 64) = c1;
            chk += c0[0];
            PIN();
        }
        if (ok || __builtin_amdgcn_ballot_w64((__float_as_uint(chk) & 0x7fffffffu) > 0x7f800000u) == 0) break;
        ok = uni((int)v) >= 7;
    }
    barrier();
    const u32 ra = lds + (u32)(tid >> 3) * 144 + (tid & 7) * 16;
    const float gt = __uint_as_float(gv), g1 = rbf(1.f - gt);
    const f32x4 z = (*LDSP(f32x4, ra) + *LDSP(f32x4, ra + 4608)) + (*LDSP(f32x4, ra + 2 * 4608) + *LDSP(f32x4, ra + 3 * 4608));
    u32x2 ov;
#pragma unroll
    for (int j = 0; j < 2; ++j) {
        const u32 b = bb[j], a = cvtpk(z[2 * j], z[2 * j + 1]);
        const u32 ga = cvtpk(gt * __uint_as_float(a << 16), gt * __uint_as_float(a & 0xffff0000u));
        const u32 gb = cvtpk(g1 * __uint_as_float(b << 16), g1 * __uint_as_float(b & 0xffff0000u));
        ov[j] = cvtpk(__uint_as_float(ga << 16) + __uint_as_float(gb << 16), __uint_as_float(ga & 0xffff0000u) + __uint_as_float(gb & 0xffff0000u));
    }
    *(u32x2*)(p.out + tok * 1280 + col * 2) = ov;
}

DI float dot2bf(u32 a, u32 b, float c) { return __builtin_amdgcn_fdot2_f32_bf16(__builtin_bit_cast(bf16x2, a), __builtin_bit_cast(bf16x2, b), c, false); }
DI void battn_task(const Ptrs& p, int t0, u32 lds, int w) {
    const int lane = lanex(), tid = 64 * w + lane, tl = tid & 31, t = t0 + tl;
    const char* const qkv = p.ws + OFF_QKVB + (size_t)(t0 >> 5) * 122880;
    {
        u32 so[3];
#pragma unroll
        for (int i = 0; i < 3; ++i) {
            const int e = 64 * i + lane, sl = e % 48;
            so[i] = (u32)((e / 48) * 512 + (sl < 16 ? (t0 ? 0 : 122880) + 256 + sl * 16 : 122880 - 256 + sl * 16));
        }
#pragma unroll
        for (int i = 0; i < 30; ++i) { glds16c(qkv - 122880 + 40960 + (10 * w + i / 3) * 2048, so[i % 3], lds + (30 * w + i) * 1024); gap<SMB>(-1); }
    }
    u32x4 q[2][5], xv[10], gv[10];
    sfor<0, 10>([&](auto I) { gld16c<(I.v % 5) * 512>(q[I.v / 5][I.v % 5], qkv + (I.v / 5) * 20480, (u32)((tid >> 5) * 2560 + tl * 16)); gap<SMB>(-1); });
    sfor<0, 10>([&](auto I) { gld16<I.v * 128>(xv[I.v], p.x + (size_t)t0 * 1280, (u32)((tid >> 3) * 1280 + (tid & 7) * 16)); gld16<I.v * 128>(gv[I.v], p.gw, (u32)((tid & 7) * 16)); gap<SMB>(-1); });
    sfor<0, 10>([&](auto I) { waitv<0>(q[I.v / 5][I.v % 5]); waitv<0>(xv[I.v]); waitv<0>(gv[I.v]); });
    barrier();
    {
        float a = 0.f;
#pragma unroll
        for (int i = 0; i < 10; ++i)
#pragma unroll
            for (int j = 0; j < 4; ++j) {
                const u32 r = cvtpk(__uint_as_float(xv[i][j] << 16) * __uint_as_float(gv[i][j] << 16), __uint_as_float(xv[i][j] & 0xffff0000u) * __uint_as_float(gv[i][j] & 0xffff0000u));
                a += __uint_as_float(r << 16) + __uint_as_float(r & 0xffff0000u);
            }
        a += __shfl_xor(a, 1); a += __shfl_xor(a, 2); a += __shfl_xor(a, 4);
        const float z = rbf(a);
        if ((tid & 7) == 0) gst4c(p.ws + OFF_GATE, (u32)((t0 + (tid >> 3)) * 4), __float_as_uint(rbf(1.0f / (1.0f + __expf(-z)))));
    }
#pragma unroll
    for (int it = 0; it < 2; ++it) {
        const int m = (tid >> 5) + 8 * it;
        u32 ka = lds + (u32)(m * 3840 + (2 + tl) * 16), va = ka + 61440;
        asm volatile("" : "+v"(ka), "+v"(va));
        u32x4 k[5][8];
#pragma unroll
        for (int i = 0; i < 40; ++i) k[i >> 3][i & 7] = ldsr16(ka + (i >> 3) * 768 + (7 - (i & 7)) * 32);
        PIN();
        float lg[2][8], mx[2] = {-3.0e38f, -3.0e38f};
#pragma unroll
        for (int i = 0; i < 16; ++i) lg[i >> 3][i & 7] = 0.f;
#pragma unroll
        for (int i = 0; i < 5; ++i) {
#pragma unroll
            for (int j = 0; j < 4; ++j)
#pragma unroll
                for (int wv = 0; wv < 8; ++wv) lg[4 * i + j >= 10][wv] = dot2bf(q[it][i][j], k[i][wv][j], lg[4 * i + j >= 10][wv]);
            PIN();
#pragma unroll
            for (int wv = 0; wv < 8; ++wv) k[i][wv] = ldsr16(va + i * 768 + (7 - wv) * 32);
            PIN();
        }
#pragma unroll
        for (int i = 0; i < 16; ++i) {
            float& l = lg[i >> 3][i & 7];
            l = rbf(rbf(l) * 0.22360679774997896f);
            asm volatile("" : "+v"(l));
            l = t - 2 * (i & 7) >= 0 ? l : -3.0e38f;
            mx[i >> 3] = fmaxf(mx[i >> 3], l);
        }
        u32 pk[2][4];
#pragma unroll
        for (int u = 0; u < 2; ++u) {
            float sum = 0.f;
#pragma unroll
            for (int wv = 0; wv < 8; ++wv) { lg[u][wv] = __builtin_amdgcn_exp2f((lg[u][wv] - mx[u]) * 1.4426950408889634f); sum += lg[u][wv]; }
            const float inv = __builtin_amdgcn_rcpf(sum);
#pragma unroll
            for (int wv = 0; wv < 4; ++wv) pk[u][wv] = cvtpk(lg[u][2 * wv] * inv, lg[u][2 * wv + 1] * inv);
        }
#pragma unroll
        for (int i = 0; i < 5; ++i) {
            float o[8];
#pragma unroll
            for (int d = 0; d < 8; ++d) o[d] = 0.f;
#pragma unroll
            for (int wp = 0; wp < 4; ++wp) {
                const u32x4 v0 = k[i][2 * wp], v1 = k[i][2 * wp + 1];
#pragma unroll
                for (int j = 0; j < 4; ++j) {
                    const u32 pw = pk[4 * i + j >= 10][wp];
                    o[2 * j] = dot2bf(__builtin_amdgcn_perm(v1[j], v0[j], 0x05040100u), pw, o[2 * j]);
                    o[2 * j + 1] = dot2bf(__builtin_amdgcn_perm(v1[j], v0[j], 0x07060302u), pw, o[2 * j + 1]);
                }
            }
            gst16c(p.ws + OFF_OB + (size_t)(t0 >> 5) * 40960 + 512 * i, (u32)(m * 2560 + tl * 16), u32x4{cvtpk(o[0], o[1]), cvtpk(o[2], o[3]), cvtpk(o[4], o[5]), cvtpk(o[6], o[7])});
        }
    }
}

#define XPF 1
#define PFQ 0
#define PFN 8
#define OLAG 3
constexpr int QN = 176, QF = 19;
struct QTab { u16 e[QN]; int n, nf; u32 tt[64]; };
typedef const __attribute__((address_space(4))) int* KI;
constexpr int QT_OFF = (88 + 48) / 4;
DI int pick_task(char* fl, KI k, int gr) {
    u32 r;
    asm volatile("s_mov_b64 exec, 1\n\ts_nop 3\n\tglobal_atomic_add %0, %1, %2, %3 sc0 sc1\n\ts_mov_b64 exec, -1\n\ts_waitcnt vmcnt(0)"
                 : "=&v"(r) : "v"((F_QH + gr) * 128), "v"(1), "s"(fl) : "memory");
    const int x = uni((int)r) + 1 + QF;
    if (x >= k[QT_OFF + QN / 2]) return -1;
    const u32 e2 = (u32)k[QT_OFF + (x >> 1)];
    return (int)(x & 1 ? e2 >> 16 : e2 & 0xffffu);
}
__global__ __launch_bounds__(256, 1) void k_main(Ptrs p0, ChainCfg cfg, QTab tab) {
    extern __shared__ __attribute__((aligned(16))) char smem[];
    u32 lds = (u32)(size_t)LDSP(char, smem);
    int n = blockIdx.x, gr = n & 7;
    int w = uni(threadIdx.x >> 6);
    {
        u64 pc; int len;
        asm volatile("s_getpc_b64 %0\n.Lkm_first:\n\ts_mov_b32 %1, .Lkm_last-.Lkm_first\n\ts_max_i32 %1, %1, .Lkm_lu-.Lkm_first\n\ts_max_i32 %1, %1, .Lkm_lv-.Lkm_first\n\t"
                     "s_max_i32 %1, %1, .Lkm_g2-.Lkm_first\n\ts_max_i32 %1, %1, .Lkm_g4-.Lkm_first" : "=s"(pc), "=s"(len) : : "scc");
        if (w == 3) pfl((const char*)pc, (u32)min((int)(blockIdx.x >> 3) * 8192 + (int)(threadIdx.x & 63) * 128, max(len, 0)), lds);
    }
    karg_warm();
    if (n < NH) { score_role(p0, cfg, n, lds, w); return; }
    if (n < NS) { proj_role(p0, cfg, n - NH, lds, w); return; }
    const bool isv = n < NS + NVB;
#define qslot (lds + GRING * GBUF)
    int sdw = -1;
    int en = isv ? Q_VD + (n & 24) : tab.e[(n - NS - NVB) >> 3] & 511;
    u32 phi0 = 0;
    Sync y;
    if (XPF && n < 160 && w >= 2) pfl(p0.x, (u32)min(((n - NS) >> 3) * 128 + (w & 1) * 64 + lanex(), 1279) * 128, lds);
    if (n >= 160) {
        const int lane = lanex();
        if (TLBT && n < 192) {
            const u32 k = (u32)(((n - 160) >> 3) * 256 + w * 64 + lane);
            for (int i = 0; i < TLBT; ++i) pfl(p0.ws, min((k + 1024u * i) * (262144u / TLBT), (u32)WS_END - 128), lds);
            if (n < 168) pfl(p0.out, min(k * 16384u, (u32)(S_LEN * DM * 2 - 128)), lds);
        }
        if (n >= 192 && (n & 8)) {
            init_task(p0, (n & 7) + ((n - 192) >> 4) * 8, lds, w);
            barrier();
#pragma unroll
            for (int i = 0; i < 3; ++i) pfl(p0.wqkvb, (u32)min((((n - 192) >> 4) * 8 + (n & 7)) * 768 + i * 256 + w * 64 + lane, 19199 + 0) * 128, lds);
            pfl(p0.wob, (u32)min((((n - 192) >> 4) * 8 + (n & 7)) * 256 + w * 64 + lane, 6399) * 128, lds);
        } else {
            const int h = n < 192 ? n & 31 : (n & 7) + ((n - 192) >> 4) * 8;
            xcopy_task(p0, h + (n < 192 ? 0 : 32), w);
#pragma unroll
            for (int i = 0; i < PFN; ++i) {
                if (!PFQ && n < 192) break;
                const int l = 64 * min(i * 4 + w, 29) + lane;
                pfl(n < 192 ? p0.wq + h * 384 : p0.wo + (size_t)h * 245760, n < 192 ? (u32)((l / 3) * 12288 + (l % 3) * 128) : (u32)(l * 128), lds);
            }
        }
        waitvm<0>();
        if (w == 0) {
            sync_ld(y, p0.ws);
            waitvm<0>();
            *LDSP(u32, qslot) = (u32)sync_end(y, p0.ws, phi0, true);
            xcopy_wait(p0.ws, sync_tag(y.e));
        }
        barrier();
        sdw = uni((int)*LDSP(u32, qslot));
        barrier();
    }
    while (true) {
        typedef char* const __attribute__((address_space(4)))* KP;
        KP kp = (KP)__builtin_amdgcn_kernarg_segment_ptr();
        asm volatile("" : "+s"(kp), "+s"(lds), "+s"(w), "+s"(gr), "+s"(n));
        __builtin_assume(w >= 0 && w < 4 && gr >= 0 && gr < 8);
        const Ptrs p{kp[0], kp[1], kp[2], kp[3], kp[4], kp[5], kp[6], kp[7], kp[8], kp[9], kp[10]};
        const int ty = en & 7, gate = en >> 9;
        const bool fu = ty == Q_PX && (en >> 3 & 63) < (((KI)kp)[QT_OFF + QN / 2 + 1] & 255);
        const int ix = fu ? (en & 504) + gr : en >> 3 & 63;
        char* const fl = p.ws + OFF_ST;
        const bool raw = sdw < 0;
        int t0 = gate - 1, f1 = F_ONE, t1 = 0, f2 = F_ONE, t2 = 0, f3 = F_ONE, t3 = 0, dt = cfg.T2;
        const int os = ix >= 40 ? 44 + (8 * ix + gr) / 20 : (8 * ix + gr) / 5;
        const int bb = ty == Q_BO ? (8 * ix + gr) >> 2 : os;
        if (ty == Q_BA) { f1 = F_B + gr | ix << 16; t1 = 5; f2 = F_B + gr | max(ix - 1, 0) << 16; t2 = 5; }
        if (ty == Q_BO) { f1 = F_B + gr | (8 + bb) << 16; t1 = 8; }
        if (ty == Q_PX) { f1 = F_Q + pblk(ix) * 8 + gr | fu << 16; t1 = fu ? 32 : 4; if (fu) t0 += gr * (((KI)kp)[QT_OFF + QN / 2 + 1] >> 8) >> 3; if (ix < PXT) t0 = max(t0, ix + 1); }
        if (ty == Q_OUT) { f3 = F_B + gr | (16 + (os >> 3)) << 16; t3 = 4; if (os < 60) { f1 = F_PX + os; t1 = 8; t0 = max(t0, min(os + OLAG, NSEG - 2)); } else { f1 = F_VD + 64 + gr; t1 = 32; } }
        if (gate || ty >= Q_BA) wg_wait(fl, w, F_CUR + gr, t0, f1, t1, f2, t2, f3, t3, dt, false);
        int sig = F_END, nrep = 1;
        if (ty <= Q_QKVB || ty == Q_BO) {
            const bool ab = ty <= Q_KQ;
            sig = ty == Q_VD ? F_VD + (ix >> 2) * 8 : (ty == Q_KQ ? F_Q + (ix >> 2) * 8 : (ix & 4 ? F_Q : F_K + 8 * ix)) + gr;
            if (!ab) sig = F_B | (bb + (ty == Q_BO ? 16 : 0)) << 16;
            nrep = ty == Q_VD || !ab ? 8 : 1;
            if (ty == Q_VD && ix < 4) { sig += n & 32 ? 4 : 0; nrep = 4; }
            run_gemm(p, ty, ab ? ix >> 2 : bb, ab ? gr + 8 * (ix & 3) : 8 * ix + gr - (ty == Q_BO ? 4 : 5) * bb, (n >> 5) & 1, raw, y, lds, w);
            if (raw) sdw = sync_end(y, p.ws, phi0, w == 0);
        } else {
            if (ty == Q_BA) {
                battn_task(p, (8 * ix + gr) * 32, lds, w);
                sig = F_B | (8 + ix) << 16; nrep = 8;
            } else if (ty == Q_PX) {
                if (ix < PXT) {
#pragma nounroll
                    for (int i = fu ? 0 : gr; i < (fu ? 8 : gr + 1); ++i) px_wave<false>(p, cfg, ix, i + 8 * w, ix - cfg.fast, w, lds, fu);
                } else px_wave<true>(p, cfg, ix, gr + 8 * w, -1, w, lds, false);
                sig = F_PX + ix;
                if (fu) wg_wait(fl, w, F_ONE, 0, F_B + gr | (16 + (ix >> 3)) << 16, 4, F_ONE, 0, F_ONE, 0, dt, false);
            }
            if (ty == Q_OUT || fu) {
                const int cg = 8 * ix + gr - 5 * os;
                if (ix >= 40 && !fu) out_tail(p, cfg, os, 8 * ix + gr - 20 * (os - 44), lds, w, sdw, gr);
                else if (os < 60 || fu) out_task(p, fu ? ix : os, fu ? 0 : cg, fu ? 5 : cg + 1, lds, (fu ? ix : os) + 1 - cfg.fast, w, sdw);
                sig = F_END;
            }
            waitvm<0>();
        }
        barrier();
        if (w == 0 && sig != F_END) flag_add(fl, sig, nrep);
        if (w == 1 && sig >= F_Q && sig < F_N) flag_add(fl, sig - gr | 1 << 16, 8);
        if (raw && !isv && w == 0) xcopy_wait(p.ws, sync_tag(y.e));
        if (isv) break;
        if (w == 0) *LDSP(int, qslot) = pick_task(fl, (KI)kp, gr);
        barrier();
        en = uni(*LDSP(int, qslot));
        if (en < 0) return;
        barrier();
    }
    value_role(p0, cfg, n - NS, sdw, phi0, lds, w);
}

#define QCFG 14, 11, 3, 5, 0, 57, 5
#define LVDEF "00DDD>9:9;;;;::<<776789;::99999888:8=AA@<98:;:;=<875863538@:5226"
constexpr int DX = 200, DUA = 340, DUB = 1, DUC = 2;
static ChainCfg dev_cfg(ChainCfg c) { c.T = PDL; c.cR = ODL; c.s1 = TDT; c.T2 = WSLEEP; c.cR2 = DX; c.dU = DUA; c.s2 = DUB; c.T3 = DUC; c.cR3 = 0 | 0 << 8; return c; }
static QTab set_sched(QTab t, const ChainCfg c, const char* lv) {
    for (int s = 0; s < 64; ++s) {
        const int d = lv[s] - '0';
        t.tt[s] = (u32)((s < c.s1 ? c.T : (s < c.s2 ? c.T2 : c.T3)) + 4 * d) | (u32)((s < c.s1 ? c.cR : (s < c.s2 ? c.cR2 : c.cR3)) + 5 * d / 2) << 16;
    }
    t.tt[0] = (u32)270 | (u32)165 << 16;
    t.tt[1] = (u32)270 | (u32)180 << 16;
    return t;
}
#define QORD "bbqV3bK3m44bm55bm66m77m88"
#define GATES "11 12 13 16 26 28 35 37 19 22 33 43 12 17 21 26 33"
static QTab make_tab(int lv, int lk, int b0, int d1, int d2, int lg, int nf) {
    QTab t{};
    int n = 0, dl[QN], nq = 0;
    auto add = [&](int ty, int ix) { t.e[n] = (u16)(ty | ix << 3); dl[n++] = ty == Q_VD || ty == Q_KQ ? 8 * (ix / 4 - 1) - (ty == Q_VD ? lv : lk) - (ix >= 32 ? 0 : 0) : 99; };
    for (int i = 1; i < 4; ++i) add(Q_VD, i);
    for (int i = 0; i < 16; ++i) add(i < 4 ? Q_H : ((i & 8) ? Q_VD : Q_KQ), i < 8 ? i : 4 + (i & 1) * 4 + ((i - 8) >> 1));
    for (int i = 0; i < 4; ++i) add(Q_KQ, 8 + i);
    for (const char* c = QORD; *c; ++c) {
        if (*c == 'b') add(Q_QKVB, nq++);
        else if (*c == 'q') for (int i = 4; i < 8; ++i) add(Q_H, i);
        else if (*c == 'm') { const int b = c[1] - '0', b2 = c[2] - '0'; c += 2; for (int i = 0; i < 8; ++i) add(i & 1 ? Q_KQ : Q_VD, 4 * (i & 1 ? b2 : b) + ((i >> 1) ^ (i & 2 ? 0 : 0))); }
        else {
            const int ty = (*c | 32) == 'v' ? Q_VD : Q_KQ, two = *c >> 5 & 1, b = *++c - '0';
            for (int i = 0; i < (4 << two); ++i) add(ty, 4 * (b + (i & two)) + (i >> two));
        }
    }
    const int gap = 8;
    for (int i = n - 1, m = 999; i >= 0; --i) { m = min(8 * dl[i], m - gap); dl[i] = max(m, 0); }
    auto put = [&](int ty, int ix, int g, int pos) {
        int i = n++;
        for (; dl[i - 1] > pos; --i) { t.e[i] = t.e[i - 1]; dl[i] = dl[i - 1]; }
        t.e[i] = (u16)(ty | ix << 3 | (g + 1) << 9); dl[i] = pos;
    };
    for (int i = 0; i < n; ++i) if (dl[i] >= 8) t.e[i] |= (u16)((dl[i] / 8 + 1) << 9);
    const char* gs = GATES;
    auto gt = [&](int i) { return 10 * (gs[3 * i] - '0') + gs[3 * i + 1] - '0'; };
    for (int i = 0; i < 8; ++i) put(Q_BA, i, gt(i), 8 * gt(i));
    for (int i = 0; i < 4; ++i) put(Q_BO, i, gt(8 + i), 8 * gt(8 + i));
    for (int i = 0; i < nf; ++i) put(Q_PX, i, gt(12 + i), 8 * (max(gt(12 + i), 8 * i + 1) + b0));
    for (int s = 8 * nf; s < NSEG; ++s) put(Q_PX, s, min(s + 1, lg), s < PXT ? 8 * s + 8 : 8 * lg);
    for (int x = 5 * nf; x < 38; ++x) put(Q_OUT, x, 0, 8 * ((8 * x + 4) / 5 + OLAG));
    for (int x = 40; x < 50; ++x) put(Q_OUT, x, lg, 8 * (lg + 2) + 4 * (x - 40));
    t.n = n; t.nf = nf | d1 << 8;
    return t;
}

extern "C" void kernel_launch(void* const* d_in, const int* in_sizes, int n_in, void* d_out, int out_size, void* d_ws,
                              size_t ws_size, hipStream_t stream) {
    Ptrs p{(const char*)d_in[0], (const char*)d_in[1], (const char*)d_in[2], (const char*)d_in[3], (const char*)d_in[4], (const char*)d_in[5],
           (const char*)d_in[8], (const char*)d_in[9], (const char*)d_in[10], (char*)d_out, (char*)(((size_t)d_ws + 0x1fffff) & ~(size_t)0x1fffff)};
    (void)hipFuncSetAttribute((const void*)k_main, hipFuncAttributeMaxDynamicSharedMemorySize, S_LDS);
    k_main<<<256, 256, S_LDS, stream>>>(p, dev_cfg(ChainCfg{CFG}), set_sched(make_tab(QCFG), ChainCfg{CFG}, LVDEF));
    static_assert(sizeof(Ptrs) + sizeof(ChainCfg) + sizeof(QTab) == TT_OFF + 256, "");
}
```

```cpp
#include <hip/hip_runtime.h>
#pragma clang diagnostic ignored "-Wint-to-pointer-cast"

typedef unsigned short u16;
typedef unsigned int u32;
typedef unsigned long long u64;
typedef __bf16 bf16x8 __attribute__((ext_vector_type(8)));
typedef __bf16 bf16x16 __attribute__((ext_vector_type(16)));
typedef short s16x4 __attribute__((ext_vector_type(4)));
typedef float f32x4 __attribute__((ext_vector_type(4)));
typedef unsigned u32x4 __attribute__((ext_vector_type(4)));
typedef unsigned u32x2 __attribute__((ext_vector_type(2)));
typedef unsigned u32x8 __attribute__((ext_vector_type(8)));
typedef unsigned u32x16 __attribute__((ext_vector_type(16)));
typedef unsigned u32x32 __attribute__((ext_vector_type(32)));
#define DI __device__ __forceinline__
#define LDSP(T, a) ((__attribute__((address_space(3))) T*)(a))

constexpr int S_LEN = 2048, DM = 640, NH = 32, DH = 192, NSEG = 64, HD = NH * DH;
#define NSAMP 4
constexpr int NREG = 13;
constexpr int NV = 80, NVB = 40;
constexpr int NS = 64;
#define CFG 154, 103, 100, 20, 57, 16, 154, 103, 52, 154, 103, 6

constexpr size_t U_STRIDE = NV * 4096, D_STRIDE = NV * 16384;
constexpr size_t HO_STRIDE = 65536;
constexpr size_t OFF_ST = NSEG * HO_STRIDE;
enum { F_ACK = 0, F_ONE = 3, F_VD = 16, F_QH = 96, F_B = 104, F_CUR = 128, F_PROG = 136 - 8 * 62, F_UD = 152, F_PX = 160, F_K = 224, F_END = 512, F_Q = 520, F_N = 592 };
constexpr size_t OFF_LAT = OFF_ST + F_N * 128;
constexpr size_t OFF_KX = 5u << 20;
constexpr size_t OFF_QX = OFF_KX + (size_t)S_LEN * HD * 2;
constexpr size_t OFF_D = OFF_QX + (size_t)S_LEN * HD * 2;
constexpr size_t OFF_U = (OFF_D + NSEG * D_STRIDE + 0x1fffff) & ~(size_t)0x1fffff;
constexpr size_t OFF_KM = OFF_U + (22u << 20);
constexpr size_t OFF_PX = OFF_KM + (size_t)NSEG * NH * 3072;
constexpr size_t OFF_QKVB = OFF_PX + (size_t)NSEG * 81920;
constexpr size_t OFF_OB = OFF_QKVB + (size_t)S_LEN * 1920 * 2;
constexpr size_t OFF_BOUT = OFF_OB + (size_t)S_LEN * DM * 2;
constexpr size_t OFF_GATE = OFF_BOUT + (size_t)S_LEN * DM * 2;
constexpr size_t OFF_XF = OFF_GATE + S_LEN * 4;
constexpr size_t WS_END = OFF_XF + (size_t)S_LEN * DM * 2;
static_assert(WS_END + (2u << 20) <= (256u << 20), "workspace");

typedef __bf16 bf16x2 __attribute__((ext_vector_type(2)));
typedef float f32x2 __attribute__((ext_vector_type(2)));
DI u32 cvtpk(float lo, float hi) { return __builtin_bit_cast(u32, __builtin_convertvector(f32x2{lo, hi}, bf16x2)); }
DI float rbf(float f) { return __uint_as_float(cvtpk(f, f) << 16); }
DI f32x4 mfma16(u32x4 a, u32x4 b, f32x4 c) {
    return __builtin_amdgcn_mfma_f32_16x16x32_bf16(__builtin_bit_cast(bf16x8, a), __builtin_bit_cast(bf16x8, b), c, 0, 0, 0);
}
DI u32x2 trread(u32 a) {
    return __builtin_bit_cast(u32x2, __builtin_amdgcn_ds_read_tr16_b64_v4i16(LDSP(s16x4, a)));
}
DI void glds16c(const char* sbase, u32 voff, u32 lds) {
    u32 sv;
    asm volatile("s_mov_b32 %0, m0\n\ts_mov_b32 m0, %3\n\ts_nop 0\n\tglobal_load_lds_dwordx4 %1, %2 sc1\n\ts_mov_b32 m0, %0"
                 : "=&s"(sv) : "v"(voff), "s"(sbase), "s"(lds) : "memory");
}
template <int IMM>
DI void gld16(u32x4& r, const char* sbase, u32 voff) {
    asm volatile("s_nop 4\n\tglobal_load_dwordx4 %0, %1, %2 offset:%3" : "=v"(r) : "v"(voff), "s"(sbase), "i"(IMM) : "memory");
}
DI void gst16(char* sbase, u32 voff, u32x4 v) {
    asm volatile("global_store_dwordx4 %0, %1, %2 sc1\n\ts_nop 1" : : "v"(voff), "v"(v), "s"(sbase) : "memory");
}
#define OST " nt"
DI void gst16p(char* sbase, u32 voff, u32x4 v) {
    asm volatile("s_nop 4\n\tglobal_store_dwordx4 %0, %1, %2" OST "\n\ts_nop 1" : : "v"(voff), "v"(v), "s"(sbase) : "memory");
}
DI void gst8(char* sbase, u32 voff, u32x2 v) {
    asm volatile("global_store_dwordx2 %0, %1, %2 sc1\n\ts_nop 1" : : "v"(voff), "v"(v), "s"(sbase) : "memory");
}
template <int N>
DI void waitvm() { asm volatile("s_waitcnt vmcnt(%0)" : : "i"(N) : "memory"); }
DI void barrier() { asm volatile("s_waitcnt lgkmcnt(0)\n\ts_barrier" : : : "memory"); }
#define PIN() __builtin_amdgcn_sched_barrier(0)
DI int uni(int v) { return __builtin_amdgcn_readfirstlane(v); }
DI int lanex() {
    int t;
    asm volatile("v_mbcnt_lo_u32_b32 %0, -1, 0\n\tv_mbcnt_hi_u32_b32 %0, -1, %0" : "=v"(t));
    __builtin_assume(t >= 0 && t < 64);
    return t;
}
DI void flag_ld(u32& r, const char* fl, int idx) {
    asm volatile("s_nop 4\n\tglobal_load_dword %0, %1, %2 sc1" : "=v"(r) : "v"((idx & 0xffff) * 128 + (idx >> 14)), "s"(fl) : "memory");
}
DI void flag_ldm(u32& r, const char* fl, int idx, bool on) {
    asm volatile("v_mov_b32 %0, 1000\n\ts_mov_b32 exec_lo, %3\n\ts_mov_b32 exec_hi, 0\n\tglobal_load_dword %0, %1, %2 sc1\n\ts_mov_b64 exec, -1" : "=&v"(r) : "v"(idx * 128), "s"(fl), "s"(uni(on ? 1 : 0)) : "memory");
}
DI void flag_wait(const char* fl, int idx, int target) {
#pragma nounroll
    for (int it = 0; it < (1 << 16); ++it) {
        u32 v;
        flag_ld(v, fl, idx);
        asm volatile("s_waitcnt vmcnt(0)" : "+v"(v) : : "memory");
        if (uni((int)(v + 1u)) >= target) break;
        __builtin_amdgcn_s_sleep(6);
    }
}
DI void flag_add(char* fl, int idx, int nrep) {
    const int lane = lanex();
    if (lane < nrep) asm volatile("s_nop 4\n\tglobal_atomic_add %0, %1, %2 sc1" : : "v"(((idx & 0xffff) + lane) * 128 + (idx >> 14)), "v"(1), "s"(fl) : "memory");
}
DI void acquire() { asm volatile("buffer_inv sc1\n\ts_waitcnt vmcnt(0)" : : : "memory"); }

template <int V> struct IC { static constexpr int v = V; };
template <int B, int E, class Fn>
DI void sfor(Fn&& fn) { if constexpr (B < E) { fn(IC<B>{}); sfor<B + 1, E>(fn); } }
enum { T_QK = 0, T_VD = 1, T_BL = 3 };
constexpr int GBUF = 24576, GRING = 6;
struct GemmArgs {
    const char *w1, *w1b;
    const char* w2;
    const char* x;
    char *out, *outb;
    int ldw;
    char* sig;
    int type, np1, tn;
    int sV, sP, sQ;
    int xb;
};
DI const char* unip(const char* p) {
    const u64 v = (u64)p;
    return (const char*)(((u64)(u32)uni((int)(v >> 32)) << 32) | (u32)uni((int)v));
}
template <int Z>
DI void mf1(f32x4& c, const u32x4& a, const u32x4& b) {
    if constexpr (Z) asm volatile("v_mfma_f32_16x16x32_bf16 %0, %1, %2, 0" : "=&v"(c) : "v"(a), "a"(b));
    else asm volatile("v_mfma_f32_16x16x32_bf16 %0, %1, %2, %0" : "+v"(c) : "v"(a), "a"(b));
}
template <int Z>
DI void mf2(f32x4& c, const u32x4& a, const u32x4& b) {
    if constexpr (Z) asm volatile("v_mfma_f32_16x16x32_bf16 %0, %1, %2, 0" : "=&v"(c) : "v"(a), "v"(b));
    else asm volatile("v_mfma_f32_16x16x32_bf16 %0, %1, %2, %0" : "+v"(c) : "v"(a), "v"(b));
}
template <int IMM>
DI void gld16a(u32x4& r, const char* sbase, u32 voff) {
    asm volatile("global_load_dwordx4 %0, %1, %2 offset:%3 sc1" : "+a"(r) : "v"(voff), "s"(sbase), "i"(IMM) : "memory");
}
DI void setm0(u32 v) { asm volatile("s_mov_b32 m0, %0" : : "s"(v) : "memory"); }
template <int IMM>
DI void gldsm(const char* sbase, u32 voff) {
    asm volatile("global_load_lds_dwordx4 %0, %1 offset:%2" : : "v"(voff), "s"(sbase), "i"(IMM) : "memory");
}
DI void gst16n(char* sbase, u32 voff, u32x4 v) {
    asm volatile("global_store_dwordx4 %0, %1, %2 sc1" : : "v"(voff), "v"(v), "s"(sbase) : "memory");
}
template <int N>
DI void waitbar() { asm volatile("s_waitcnt vmcnt(%0)\n\ts_barrier" : : "i"(N) : "memory"); }
#define LGKM(n) __builtin_amdgcn_s_waitcnt(0xC07F | ((n) << 8))

DI int xhalf() { u32 x; asm volatile("s_getreg_b32 %0, hwreg(HW_REG_XCC_ID)" : "=s"(x)); return (int)((x >> 2) & 1); }
struct Sync { u32x2 r, g, e; };
DI void sync_ld(Sync& y, const char* ws) {
    asm volatile("s_nop 4\n\tglobal_load_dwordx2 %0, %3, %5 sc1\n\tglobal_load_dwordx2 %1, %4, %5 offset:512 sc1\n\tglobal_load_dwordx2 %2, %4, %5 offset:1024 sc1"
                 : "=&v"(y.r), "=&v"(y.g), "=&v"(y.e) : "v"((lanex() & 31) * 8), "v"(0), "s"(ws + OFF_LAT) : "memory");
}
DI u64 sync_tag(u32x2 e) { return (((u64)e[1] << 32 | e[0]) + 1) << 1; }
DI int sync_end(Sync& y, const char* ws, u32& phi, bool ack) {
    asm volatile("" : "+v"(y.r), "+v"(y.g), "+v"(y.e));
#pragma nounroll
    for (int it = 0; it < (1 << 16); ++it) {
        const u64 t = sync_tag(y.e);
        if (__builtin_amdgcn_ballot_w64((y.r[0] & ~1u) != (u32)t || y.r[1] != (u32)(t >> 32) || y.g[0] != (u32)t) == 0) break;
        __builtin_amdgcn_s_sleep(24);
        sync_ld(y, ws);
        asm volatile("s_waitcnt vmcnt(0)" : "+v"(y.r), "+v"(y.g), "+v"(y.e) : : "memory");
    }
    if (ack) flag_add((char*)ws + OFF_ST, F_ACK, 1);
    phi = (u32)uni((int)y.g[1]);
    const u32 b = (u32)__builtin_amdgcn_ballot_w64(y.r[0] & 1), all = __builtin_popcount(b) > 16;
    u32 q = 0;
#pragma unroll
    for (int r = 0; r < NREG; ++r) {
        const u32 c = r < 2 ? __builtin_popcount(b & (0x55u << r)) + all : 2 * __builtin_popcount((b >> (4 + 2 * r)) & 3) + all;
        q |= ((c > 2) ^ (u32)((r < 2 ? r : r + (OFF_U >> 21)) & 1)) << r;
    }
    return uni((int)(q ^ 3u * xhalf()));
}
DI void pace(int on) {
    asm volatile("s_cmp_ge_i32 %0, 0\n\ts_cbranch_scc1 .Lp%=\n\ts_waitcnt vmcnt(0)\n.Lp%=:" : : "s"(on) : "memory", "scc");
}
#define SMO 48
#define RM 7
#define BLS 1
#define EPG 1
#define PXT 58
#define PDX 40
#define SMB 48
#define SMP SMO
template <int C>
DI void gap(int on) {
    if constexpr (C > 0) asm volatile("s_cmp_ge_i32 %0, 0\n\ts_cbranch_scc1 .Lg%=\n\ts_nop %1\n\ts_nop %2\n\ts_nop %3\n.Lg%=:" : : "s"(on), "n"(C / 12 - 1), "n"(C / 12 - 1), "n"(C / 12 - 1) : "memory", "scc");
}
DI void early_add(char* ctr, int c) {
    asm volatile("s_cmp_lg_u32 %1, 1\n\ts_cbranch_scc1 .Le%=\n\ts_mov_b64 exec, 1\n\ts_nop 1\n\tglobal_atomic_add %2, %3, %0 sc1\n\ts_mov_b64 exec, -1\n.Le%=:"
                 : : "s"(ctr), "s"(c), "v"(0), "v"(1) : "memory", "scc");
}
#define XL 2
template <int NT>
DI void gemm_task(const GemmArgs& g, Sync& y, const char* ws, u32 lds, int w) {
    constexpr int FD = 5, NX = 2 * NT, OPS = NX + 6, BI = 24 - FD;
    const int lane = lanex();
    const int n = lane & 15, q = lane >> 4, qq = (lane >> 2) & 3, pp = lane & 3;
    const bool p2 = g.type == T_VD;
    const int rw = lane >> 3, sp = (lane >> 1) & 3;
    const u32 v2b = (u32)(rw * 1280 + 16 * (lane & 1) + 5120);
    auto v2o = [&](int i) { return v2b + (u32)(((sp ^ (rw >> 1)) << 5) + i * 9216); };
    u32 wv[6];
#pragma unroll
    for (int i = 0; i < 6; ++i)
        wv[i] = (u32)((rw + 8 * (i / 3)) * g.ldw + ((sp ^ (((rw >> 1) & 1) + 2 * (i / 3))) << 5) + 16 * (lane & 1) + (i % 3) * 128 + 5120 - 1024 * i);
    const char* const wp2 = g.w2 + (size_t)(48 * w) * 1280 - 2048;
    const char* wp = unip(g.w1 + (size_t)(16 * w) * g.ldw - 2048);
    int wstep = uni(64 * g.ldw);
    u32 wdst = lds + (u32)w * 6144 + 3072;
    const u32 wdlast = wdst + (GRING - 1) * GBUF;
    auto wring = [&]() { wdst = wdst == wdlast ? wdst - (GRING - 1) * GBUF : wdst + GBUF; };
    auto wload = [&](int i) {
        setm0(wdst);
        wring();
        asm volatile("s_nop 0");
        sfor<0, 6>([&](auto I) { gldsm<1024 * (I.v - 3)>(wp, wv[I.v]); });
        wp += wstep;
    };
    u32x4 vf[NT][6];
    u32x4 fr[FD + 1];
    u32x4 xr[5][NX];
    auto xtie = [&](u32x4 (&x)[NX]) {
#pragma unroll
        for (int i = 0; i < NX; i += 4) asm volatile("" : "+a"(x[i]), "+a"(x[i + 1]), "+a"(x[i + 2]), "+a"(x[i + 3]));
    };
#pragma unroll
    for (int i = 0; i < 5 * NX; ++i) asm volatile("" : "=a"(xr[i / NX][i % NX]));
    u32x2 flo;
    u32 c[4], slot = 0;
    int sd = GBUF;
    auto sring = [&]() { sd = slot == (GRING - 1) * GBUF ? -(GRING - 1) * GBUF : GBUF; slot += (u32)sd; };
    {
        u32 xo[NT];
#pragma unroll
        for (int tt = 0; tt < NT; ++tt) {
            const int tok = (g.type == T_VD) ? 8 * (n >> 2) + 4 * (tt & 1) + (n & 3) : 16 * (tt & 1) + n;
            xo[tt] = (u32)((NT / 2 * w + (tt >> 1)) * 40960 + (g.xb ? q * 512 + tok * 16 : q * 16 + tok * 1280));
        }
        f32x4 acc[12][NT];
        const bool rm = NT == 4 && g.type == T_BL;
        const int sw = (qq >> 1) + 2 * (q & 1);
#pragma unroll
        for (int t = 0; t < 4; ++t)
            c[t] = lds + q * 3072 + qq * 128 + (rm ? ((((t & 2) + (pp >> 1)) ^ sw) << 5) + 16 * (pp & 1) + 8 * ((pp >> 1) ^ (t & 1)) : ((t ^ sw) << 5) + 8 * pp);
        auto rlo = [&](int f) { flo = trread(c[f & 3] + (f / 12) * 12288 + ((f % 12) >> 2) * 1024); };
        auto rhi = [&](int f) {
            const u32x2 hi = trread(c[f & 3] + (f / 12) * 12288 + ((f % 12) >> 2) * 1024 + 512);
            fr[f % (FD + 1)] = u32x4{flo[0], flo[1], hi[0], hi[1]};
        };
        const int xk = uni(g.xb ? 2048 : 64);
        const char *xp = g.x, *xq = xp + xk;
        sfor<0, 4>([&](auto J) {
            if constexpr (J.v < XL) { sfor<0, NX>([&](auto I) { gld16a<0>(xr[J.v][I.v], I.v & 1 ? xq : xp, xo[I.v >> 1]); }); xp += 2 * xk; xq += 2 * xk; }
            wload(J.v);
        });
        waitbar<18 + (XL - 1) * NX>();
        xtie(xr[0]);
#pragma unroll
        for (int i = 0; i < FD; ++i) { rlo(i); rhi(i); }
        char* po = g.out;
#pragma nounroll
        for (int ph = g.np1; ph > 0; --ph) {
            sfor<0, 10>([&](auto J) {
                constexpr int j = J.v;
                u32x4 (&xc)[NX] = xr[j % 5];
                u32x4 (&xn)[NX] = xr[(j + XL) % 5];
                sfor<0, 24>([&](auto I) {
                    constexpr int idx = I.v, kk = idx / 12, T = idx % 12, z = j == 0 && kk == 0, f = (idx + FD) % 24;
                    constexpr int mo = NT == 4 ? (idx < BI && idx % 4 != 2 ? idx - (idx + 1) / 4 : -1) : (idx < BI && idx % 2 == 0 ? idx / 2 : -1);
                    auto mem = [&]() {
                        if constexpr (mo >= NX) gldsm<1024 * (mo - NX - 3)>(wp, wv[mo - NX]);
                        else if constexpr (mo >= 0) gld16a<0>(xn[mo], mo & 1 ? xq : xp, xo[mo >> 1]);
                    };
                    auto misc = [&]() {
                        if constexpr (idx == 1) wring();
                        if constexpr (idx == 13) sring();
                        if constexpr (j == 1 && idx == 22) early_add(g.sig, ph + w);
                        if constexpr (idx >= 15 && idx < BI) { c[idx - 15] += (u32)sd; asm volatile("" : "+v"(c[idx - 15])); }
                        const bool more = ph > 1;
                        if constexpr (j != 5) {
                            if constexpr (idx == 20) wp += wstep;
                        } else {
                            if constexpr (idx == 19) { wp = more ? g.w1b + (size_t)(16 * w) * g.ldw - 2048 : wp2; wstep = more ? wstep : (p2 ? 128 : 0); }
                            if constexpr (idx >= 20 && idx < 23) {
#pragma unroll
                                for (int i = 2 * (idx - 20); i < 2 * (idx - 19); ++i) { wv[i] = more ? wv[i] : (p2 ? v2o(i) : 5120u); asm volatile("" : "+v"(wv[i])); }
                            }
                        }
                        if constexpr (j != 9 - XL) {
                            if constexpr (idx == 21) xp += 2 * xk;
                            if constexpr (idx == 22) xq += 2 * xk;
                        } else if constexpr (idx == 23) {
                            xp = g.x; xq = xp + xk;
#pragma unroll
                            for (int i = 0; i < NT; ++i) { xo[i] = more ? xo[i] : 0u; asm volatile("" : "+v"(xo[i])); }
                        }
                    };
                    if constexpr (idx == BI) { waitbar<6 + (XL - 1) * OPS>(); xtie(xr[(j + 1) % 5]); }
                    if constexpr (idx % 2 == 0) LGKM(6);
                    PIN();
                    const u32x4 a = fr[idx % (FD + 1)];
                    mf1<z>(acc[T][0], a, xc[kk]); PIN();
                    rlo(f);
                    if constexpr (idx == 0) setm0(wdst);
                    if constexpr (NT == 2) rhi(f);
                    PIN();
                    mf1<z>(acc[T][1], a, xc[2 + kk]); PIN();
                    if constexpr (NT == 4) {
                        rhi(f); PIN();
                        mf1<z>(acc[T][2], a, xc[4 + kk]); PIN();
                        mem(); PIN();
                        mf1<z>(acc[T][3], a, xc[6 + kk]); PIN();
                    } else { mem(); PIN(); }
                    misc();
                    PIN();
                });
            });
            asm volatile("s_nop 7\n\ts_nop 1");
            PIN();
#pragma unroll
            for (int tt = 0; tt < NT; ++tt)
#pragma unroll
                for (int ks = 0; ks < 6; ++ks)
                    vf[tt][ks] = u32x4{cvtpk(acc[2 * ks][tt][0], acc[2 * ks][tt][1]), cvtpk(acc[2 * ks][tt][2], acc[2 * ks][tt][3]),
                                       cvtpk(acc[2 * ks + 1][tt][0], acc[2 * ks + 1][tt][1]), cvtpk(acc[2 * ks + 1][tt][2], acc[2 * ks + 1][tt][3])};
            if (g.type == T_QK) {
#pragma unroll
                for (int tt = 0; tt < NT; ++tt)
#pragma unroll
                    for (int ks = 0; ks < 6; ++ks) {
                        gst16(po + (size_t)(NT / 2 * w + (tt >> 1)) * (NH * 12288) + (tt & 1) * 6144 + ks * 1024, lane * 16,
                              u32x4{vf[tt][ks][0], vf[tt][ks][2], vf[tt][ks][1], vf[tt][ks][3]});
                        if (NT == 4 && EPG) asm volatile("s_sleep %0" : : "n"(EPG));
                    }
            } else if (rm) {
                const bool sx = q >= 2;
#pragma unroll
                for (int i = 0; i < 3 * NT; ++i) {
                    const int tp = i / 6, ks = i % 6;
                    u32x4 o[2];
#pragma unroll
                    for (int j = 0; j < 4; ++j) {
                        const u32x4 &v = vf[2 * tp][ks], &u = vf[2 * tp + 1][ks];
                        if (BLS) {
                            const auto r = __builtin_amdgcn_permlane32_swap(v[j], u[j], false, false);
                            o[0][j] = r[0]; o[1][j ^ 2] = r[1];
                        } else { o[0][j] = sx ? v[j ^ 2] : v[j]; o[1][j] = sx ? u[j ^ 2] : u[j]; }
                    }
#pragma unroll
                    for (int j = 0; j < 2; ++j) {
                        gst16(po + (size_t)(2 * w + tp) * g.sV + j * (BLS ? 1024 : 256) + ks * 2048, BLS ? (u32)((q & 1) * 512 + (q >> 1) * 256 + n * 16) : (u32)(q * 512 + n * 16), o[j]);
                        if (EPG) asm volatile("s_sleep %0" : : "n"(EPG));
                    }
                }
            }
            po = g.outb;
            PIN();
        }
    }
    if (p2) {
        constexpr int HI = 2048;
        const int r = 4 * (q & 1) + qq, sw = r >> 1;
#pragma unroll
        for (int t = 0; t < 4; ++t) c[t] = lds + slot + (q >> 1) * 1024 + r * 128 + ((t ^ sw) << 5) + 8 * pp;
        sring();
        u32 so = (u32)((n >> 3) * g.sV + q * g.sQ + (n & 7) * 16);
        char* const o0 = g.out + (size_t)(NT / 2 * w) * g.sP;
        char* const o1 = o0 + g.sP;
        const int sv2 = 2 * g.sV;
        auto rlo = [&](int f) { flo = trread(c[f / 6] + (f % 6) * 4096); };
        auto rhi = [&](int f) {
            const u32x2 hi = trread(c[f / 6] + (f % 6) * 4096 + HI);
            fr[f % (FD + 1)] = u32x4{flo[0], flo[1], hi[0], hi[1]};
        };
        f32x4 a2[2][NT];
        u32 e[NT / 2][4];
        auto cv = [&](f32x4 (&a)[NT], int i) { e[i >> 2][i & 3] = cvtpk(a[i >> 1][2 * (i & 1)], a[i >> 1][2 * (i & 1) + 1]); asm volatile("" : "+v"(e[i >> 2][i & 3])); };
        auto st = [&](int pl) { gst16n(pl ? o1 : o0, so, u32x4{e[pl][0], e[pl][1], e[pl][2], e[pl][3]}); };
        PIN();
#pragma unroll
        for (int i = 0; i < FD; ++i) { rlo(i); rhi(i); }
#pragma unroll
        for (int tt = 0; tt < NT; ++tt) a2[1][tt] = f32x4{0.f, 0.f, 0.f, 0.f};
#pragma nounroll
        for (int t = 0; t < g.tn; ++t) {
            sfor<0, 24>([&](auto I) {
                constexpr int idx = I.v, cb = idx / 6, ks = idx % 6, f = (idx + FD) % 24;
                f32x4 (&ac)[NT] = a2[cb & 1];
                f32x4 (&ap)[NT] = a2[(cb & 1) ^ 1];
                constexpr int mo = cb < 2 ? (ks == 0 ? 3 * cb : (ks >= 4 ? 3 * cb + ks - 3 : -1)) : -1;
                auto mem = [&]() {
                    if constexpr (mo >= 0) gldsm<1024 * (mo - 3)>(wp, wv[mo]);
                    if constexpr (ks == 3 || (NT == 4 && ks == 2)) { if (cb > 0 || t > 0) st(NT == 4 ? ks - 2 : 0); }
                };
                auto misc = [&]() {
                    if constexpr (idx == 12) wring();
                    if constexpr (idx == 23) sring();
                    if constexpr (idx == 16) { wp += t < g.tn - 5 ? 128 : 0; }
                    if constexpr (ks == 0) { c[cb] += (u32)sd; asm volatile("" : "+v"(c[cb])); }
                    if constexpr (ks == 4) { so += (cb > 0 || t > 0) ? (u32)sv2 : 0u; asm volatile("" : "+v"(so)); }
                };
                if constexpr (idx == BI) waitbar<NT == 4 ? 38 : 28>();
                if constexpr (idx % 2 == 0) LGKM(6);
                PIN();
                const u32x4 b = fr[idx % (FD + 1)];
                mf2<ks == 0>(ac[0], vf[0][ks], b); PIN();
                rlo(f);
                if constexpr (idx == 0) setm0(wdst);
                if constexpr (NT == 2) rhi(f);
                if constexpr (NT == 4 && ks >= 1 && ks <= 3) cv(ap, ks == 1 ? 0 : (ks == 2 ? 4 : 7));
                PIN();
                mf2<ks == 0>(ac[1], vf[1][ks], b); PIN();
                if constexpr (NT == 4) {
                    rhi(f);
                    if constexpr (ks == 1 || ks == 2) cv(ap, ks == 1 ? 1 : 5);
                    PIN();
                    mf2<ks == 0>(ac[2], vf[2][ks], b); PIN();
                    mem();
                    if constexpr (ks == 1) { cv(ap, 2); cv(ap, 3); }
                    PIN();
                    mf2<ks == 0>(ac[3], vf[3][ks], b); PIN();
                    if constexpr (ks == 2) cv(ap, 6);
                } else {
                    if constexpr (ks == 1 || ks == 2) { cv(ap, 2 * ks - 2); cv(ap, 2 * ks - 1); }
                    mem(); PIN();
                }
                misc();
                PIN();
            });
            asm volatile("s_nop 7\n\ts_nop 1");
        }
        PIN();
#pragma unroll
        for (int i = 0; i < 2 * NT; ++i) cv(a2[1], i);
#pragma unroll
        for (int pl = 0; pl < NT / 2; ++pl) st(pl);
        asm volatile("s_nop 1");
    }
    if constexpr (NT == 2) asm volatile(".Lkm_g2:"); else asm volatile(".Lkm_g4:");
    sync_ld(y, ws);
    waitvm<0>();
    asm volatile("" : "+v"(y.r), "+v"(y.g), "+v"(y.e));
    sfor<0, 5>([&](auto I) { xtie(xr[I.v]); });
}

struct Ptrs {
    const char *x, *mem0, *wq, *wk, *wv, *wo, *wqkvb, *wob, *gw;
    char* out;
    char* ws;
};

enum { Q_H = 0, Q_VD = 1, Q_KQ = 2, Q_QKVB = 3, Q_BA = 4, Q_BO = 5, Q_PX = 6, Q_OUT = 7 };
DI void run_gemm(const Ptrs& p, int ty, int b, int i, int hf, bool raw, Sync& y, u32 lds, int w) {
    GemmArgs g;
    g.sV = g.sP = g.sQ = 0; g.w2 = p.wo; g.ldw = HD * 2; g.np1 = 1; g.tn = 10;
    g.sig = p.ws + OFF_ST + F_END * 128;
    const char* xf = raw ? p.x : p.ws + OFF_XF;
    g.xb = !raw;
    int s0 = 8 * b;
    if (ty == Q_VD) {
        s0 = b < 2 ? 4 * b : s0 - 8;
        g.type = T_VD; g.w1 = g.w1b = p.wv + i * DH * 2;
        g.w2 = p.wo + (size_t)i * DH * 1280;
        g.out = g.outb = p.ws + OFF_D + (size_t)s0 * D_STRIDE + i * 512;
        g.sV = 16384; g.sP = (int)D_STRIDE; g.sQ = 128;
        if (b == 0) { g.tn = 5; g.w2 += hf * 640; g.out += hf * 40 * 16384; }
    } else if (ty == Q_H) {
        s0 = 0;
        g.type = T_QK; g.w1 = g.w1b = (b ? p.wq : p.wk) + i * DH * 2;
        g.out = g.outb = p.ws + (b ? OFF_QX : OFF_KX) + (size_t)i * 12288;
        b = 0;
    } else if (ty == Q_KQ) {
        s0 = b < 2 ? 4 * b : s0 - 8;
        g.type = T_QK; g.w1 = p.wk + i * DH * 2; g.w1b = p.wq + i * DH * 2;
        g.out = p.ws + OFF_KX + (size_t)(s0 * NH + i) * 12288;
        g.outb = g.out + (OFF_QX - OFF_KX);
        g.np1 = 2;
        g.sig = p.ws + OFF_ST + (F_K + b * 32 + i) * 128;
    } else if (ty == Q_QKVB) {
        g.type = T_BL; g.w1 = p.wqkvb + i * 768; g.w1b = g.w1 + 384; g.ldw = 3840;
        g.sV = 1920 * 64; g.np1 = 2;
        g.out = p.ws + OFF_QKVB + (size_t)s0 * g.sV + i * 48 * 512; g.outb = g.out + 24 * 512;
    } else {
        const int c0 = min(i * DH, DM - DH);
        g.type = T_BL; g.w1 = g.w1b = p.wob + c0 * 2; g.ldw = 1280;
        xf = p.ws + OFF_OB;
        g.sV = 40960;
        g.out = g.outb = p.ws + OFF_BOUT + (size_t)s0 * 40960 + c0 * 64;
    }
    g.x = xf + (size_t)s0 * 40960;
    if (ty <= Q_KQ && b < 2) gemm_task<2>(g, y, p.ws, lds, w);
    else gemm_task<4>(g, y, p.ws, lds, w);
}
DI int pblk(int s) { return s < 8 ? s >> 2 : min(s >> 3, 7) + 1; }

template <int IMM>
DI void gld16c(u32x4& r, const char* sbase, u32 voff) {
    asm volatile("global_load_dwordx4 %0, %1, %2 offset:%3 sc1" : "=v"(r) : "v"(voff), "s"(sbase), "i"(IMM) : "memory");
}
DI void gst4c(char* sbase, u32 voff, u32 v) {
    asm volatile("global_store_dword %0, %1, %2 sc1" : : "v"(voff), "v"(v), "s"(sbase) : "memory");
}
template <int N>
DI void waitv(u32x4& r) { asm volatile("s_waitcnt vmcnt(%1)" : "+v"(r) : "i"(N) : "memory"); }
DI u32 mt() { return (u32)__builtin_amdgcn_s_memtime(); }
DI u32 rt() { return (u32)__builtin_amdgcn_s_memrealtime(); }
DI void spin_to(u32 tgt) { while ((int)(mt() - tgt) < 0) {} }
DI u32 max3u(u32 a, u32 b, u32 c) { return max(max(a, b), c); }
DI u32x4 ldsr16(u32 a) { return *LDSP(u32x4, a); }
DI void clocks(u32& r, u32& m) {
    u64 a, b;
    asm volatile("s_memrealtime %0\n\ts_memtime %1\n\ts_waitcnt lgkmcnt(0)" : "=s"(a), "=s"(b) : : "memory");
    r = (u32)a; m = (u32)b;
}
#define SLATE 20
DI void edge(u32& r, u32& m) {
    u64 a, t[9];
    u32 n = 0;
#define RT(i) "s_memrealtime %" #i "\n\ts_nop 1\n\t"
#define RC(i) "\n\ts_cmp_eq_u64 %" #i ", %1\n\ts_addc_u32 %10, %10, 0"
    asm volatile("s_memtime %0\n\t" RT(1) RT(2) RT(3) RT(4) RT(5) RT(6) RT(7) RT(8) "s_memrealtime %9\n\ts_waitcnt lgkmcnt(0)" RC(2) RC(3) RC(4) RC(5) RC(6) RC(7) RC(8) RC(9)
                 : "=&s"(a), "=&s"(t[0]), "=&s"(t[1]), "=&s"(t[2]), "=&s"(t[3]), "=&s"(t[4]), "=&s"(t[5]), "=&s"(t[6]), "=&s"(t[7]), "=&s"(t[8]), "+s"(n) : : "scc", "memory");
    m = (u32)a + 12 * n;
    r = (u32)t[0] + 4;
}
#define SPB 90
struct Clock {
    u32 r0, m0, r1, m1; int cpt;
    DI void init() { edge(r0, m0); r1 = r0; m1 = m0; cpt = 24 * 64; }
    DI u32 sched(u32& phi, int ofs, int G, int mode) {
        if (mode == 0) edge(r1, m1);
        u32 r = r1, m = m1;
        if (mode == 2) clocks(r, m);
        int rem = (int)(phi + (u32)ofs - r);
        if (__builtin_expect(rem < -SLATE, 0)) { const int k = (-rem / G + 1) * G; phi += (u32)k; rem += k; }
        return m + (u32)((min(rem, 3000) * cpt) >> 6) - SPB;
    }
    DI u32 at(u32 t) { u32 r, m; clocks(r, m); return m + (u32)((min(max((int)(t - r), 0), 3000) * cpt) >> 6); }
    DI void calib() {
        const int dr = (int)(r1 - r0);
        if (dr >= 60) { int c = (int)fminf(fmaxf((float)(int)(m1 - m0) * 64.f * __builtin_amdgcn_rcpf((float)dr), 256.f), 2560.f); asm volatile("" : "+v"(c)); cpt = uni(c); r0 = r1; m0 = m1; }
    }
};
#define VISS 0
#define VISV 0
DI void follow(u32& phi, int ofs, u32 t, int G) { const int late = (int)(t - phi) - ofs; if (__builtin_expect(late > 0, 0)) phi += (u32)((late / G + 1) * G); }
DI bool unwritten(const char* base, u32 off) {
#pragma nounroll
    for (int i = 0; i < 10; ++i) {
        u32x4 t;
        gld16c<-4096>(t, base + (i + (i & 8)) * 1024, off); waitv<0>(t);
        if (__builtin_amdgcn_ballot_w64(max3u(max(t[0], t[1]), t[2], t[3]) == 0xffffffffu) != 0) return true;
    }
    return false;
}

DI size_t uslot(int sd, int s) { const int r = (s * 43) >> 8; return OFF_U + (u32)uni(s * (int)U_STRIDE + r * (int)((2u << 20) - 6 * U_STRIDE) + ((sd >> (2 + r)) & 1) * 8192); }
DI size_t mslot(int sd, int s) { return (u32)uni(s * (int)HO_STRIDE + ((sd >> (s >> 5)) & 1) * 8192 + 4096); }
struct ChainCfg { int T, cR, G, dV, fast, s1, T2, cR2, s2, T3, cR3, dU; };
constexpr int TT_OFF = 88 + 48 + 360;
DI void karg_warm() {
    u32 a[13];
    asm volatile("s_load_dword %0, %13, 0\n\t" "s_load_dword %1, %13, 64\n\t" "s_load_dword %2, %13, 128\n\t" "s_load_dword %3, %13, 192\n\t" "s_load_dword %4, %13, 256\n\t" "s_load_dword %5, %13, 320\n\t" "s_load_dword %6, %13, 384\n\t" "s_load_dword %7, %13, 448\n\t" "s_load_dword %8, %13, 512\n\t" "s_load_dword %9, %13, 576\n\t" "s_load_dword %10, %13, 640\n\t" "s_load_dword %11, %13, 704\n\t" "s_load_dword %12, %13, 748\n\t" "s_waitcnt lgkmcnt(0)"
                 : "=&s"(a[0]), "=&s"(a[1]), "=&s"(a[2]), "=&s"(a[3]), "=&s"(a[4]), "=&s"(a[5]), "=&s"(a[6]), "=&s"(a[7]), "=&s"(a[8]), "=&s"(a[9]), "=&s"(a[10]), "=&s"(a[11]), "=&s"(a[12])
                 : "s"(__builtin_amdgcn_kernarg_segment_ptr()) : "memory");
}
DI u32 sched_ld0(int s) {
    u32 r;
    asm volatile("s_load_dword %0, %1, %2 offset:%3\n\ts_waitcnt lgkmcnt(0)" : "=s"(r) : "s"(__builtin_amdgcn_kernarg_segment_ptr()), "s"(4 * s), "n"(TT_OFF) : "memory");
    return r;
}
#define HP 60
#define HQ 96

constexpr int SL_W = 0;
constexpr int SL_IMG = 131072;
constexpr int SL_KX = SL_IMG + 6656;
constexpr int SL_PF = SL_KX + 12288 + 1024;
constexpr int S_LDS = SL_PF + 256;
DI void nap(int t) { const u32 wake = rt() + (u32)t; while ((int)(rt() - wake) < 0) __builtin_amdgcn_s_sleep(4); }
DI void dly(int n) {
    asm volatile(".Ld%=:\n\ts_sub_u32 %0, %0, 1\n\ts_nop 0\n\ts_cbranch_scc0 .Ld%=" : "+s"(n) : : "scc", "memory");
}
DI void pfl(const char* sbase, u32 voff, u32 lds) {
    asm volatile("s_mov_b32 m0, %2\n\ts_nop 3\n\tglobal_load_lds_dword %0, %1" : : "v"(voff), "s"(sbase), "s"(lds + SL_PF) : "memory");
}
DI void score_role(const Ptrs& p, const ChainCfg cfg, int h, u32 lds, int w) {
    const int lane = lanex(), tid = 64 * w + lane;
    const int n = lane & 15, g = lane >> 4, qq = (lane >> 2) & 3, pp = lane & 3;
    u32x32 wa[8], wv[3];
    const u32 wreg = lds + SL_W + (u32)w * 32768;
    const u32 lfa = wreg + lane * 16;
    const char* kxp = p.ws + OFF_KX + (size_t)h * 12288 + w * 3072;
    const u32 kxl = lds + SL_KX;
    char* const fl = p.ws + OFF_ST;
    const u32 kxw = kxl + w * 3072 - (w < 2 ? 16 : 0);
    auto kxdma = [&]() {
        asm volatile("s_mov_b32 m0, %2\n\ts_nop 3\n\tglobal_load_lds_dwordx4 %0, %1 sc1\n\tglobal_load_lds_dwordx4 %0, %1 offset:1024 sc1\n\tglobal_load_lds_dwordx4 %0, %1 offset:2048 sc1"
                     : : "v"(lanex() * 16), "s"(kxp), "s"(kxw) : "memory");
    };
    u32 kfl, phi;
    int sd;
    {
        constexpr int GS = 3104;
        Sync y;
        if (w < 3) {
#pragma unroll
            for (int i = PXT; i < NSEG; ++i) gst16(p.ws + OFF_KM + (i * NH + h) * 3072 + w * 1024, lane * 16, u32x4{~0u, ~0u, ~0u, ~0u});
#pragma unroll
            for (int i = NSEG - 4; i < NSEG; ++i) gst16(p.ws + OFF_PX + i * 81920 + (w < 2 ? h * 2048 + w * 1024 : 65536 + h * 512), w < 2 ? lane * 16 : (lane * 16) & 511, u32x4{~0u, ~0u, ~0u, ~0u});
        }
        auto dvo = [&](int i) { const int ix = i * 64 + lane; return (u32)((ix / 24) * (HD * 2) + (ix % 24) * 16); };
        const u32 tra = lds + (2 * (g >> 1) + (qq >> 1)) * GS + (4 * (g & 1) + (qq & 1)) * 384 + 8 * pp;
        u32x8 pa = u32x8{dvo(0), dvo(1), dvo(2), tra + 96 * w + 32 * (w & 1), tra + 96 * w - 64 * (w & 1), tra + 32 * w, lfa, (u32)(lane & 31) * 8};
        asm volatile("s_nop 4\n\t" "s_add_u32 s42, s46, 0x0\n\t" "s_addc_u32 s43, s47, 0\n\t" "s_add_u32 m0, %18, 0x0\n\t" "s_nop 0\n\t" "global_load_lds_dwordx4 v224, s[42:43]\n\t" "s_add_u32 m0, %18, 0x400\n\t"
                     "s_nop 0\n\t" "global_load_lds_dwordx4 v225, s[42:43]\n\t" "s_add_u32 m0, %18, 0x800\n\t" "s_nop 0\n\t" "global_load_lds_dwordx4 v226, s[42:43]\n\t" "s_add_u32 s42, s46, 0x18000\n\t"
                     "s_addc_u32 s43, s47, 0\n\t" "s_add_u32 m0, %18, 0xc20\n\t" "s_nop 0\n\t" "global_load_lds_dwordx4 v224, s[42:43]\n\t" "s_add_u32 m0, %18, 0x1020\n\t" "s_nop 0\n\t"
                     "global_load_lds_dwordx4 v225, s[42:43]\n\t" "s_add_u32 m0, %18, 0x1420\n\t" "s_nop 0\n\t" "global_load_lds_dwordx4 v226, s[42:43]\n\t" "s_add_u32 s42, s46, 0xc0000\n\t" "s_addc_u32 s43, s47, 0\n\t"
                     "s_add_u32 m0, %18, 0x6100\n\t" "s_nop 0\n\t" "global_load_lds_dwordx4 v224, s[42:43]\n\t" "s_add_u32 m0, %18, 0x6500\n\t" "s_nop 0\n\t" "global_load_lds_dwordx4 v225, s[42:43]\n\t"
                     "s_add_u32 m0, %18, 0x6900\n\t" "s_nop 0\n\t" "global_load_lds_dwordx4 v226, s[42:43]\n\t" "s_add_u32 s42, s46, 0xd8000\n\t" "s_addc_u32 s43, s47, 0\n\t" "s_add_u32 m0, %18, 0x6d20\n\t" "s_nop 0\n\t"
                     "global_load_lds_dwordx4 v224, s[42:43]\n\t" "s_add_u32 m0, %18, 0x7120\n\t" "s_nop 0\n\t" "global_load_lds_dwordx4 v225, s[42:43]\n\t" "s_add_u32 m0, %18, 0x7520\n\t" "s_nop 0\n\t"
                     "global_load_lds_dwordx4 v226, s[42:43]\n\t" "s_add_u32 s42, s46, 0x180000\n\t" "s_addc_u32 s43, s47, 0\n\t" "s_add_u32 m0, %18, 0xc200\n\t" "s_nop 0\n\t" "global_load_lds_dwordx4 v224, s[42:43]\n\t"
                     "s_add_u32 m0, %18, 0xc600\n\t" "s_nop 0\n\t" "global_load_lds_dwordx4 v225, s[42:43]\n\t" "s_add_u32 m0, %18, 0xca00\n\t" "s_nop 0\n\t" "global_load_lds_dwordx4 v226, s[42:43]\n\t"
                     "s_add_u32 s42, s46, 0x198000\n\t" "s_addc_u32 s43, s47, 0\n\t" "s_add_u32 m0, %18, 0xce20\n\t" "s_nop 0\n\t" "global_load_lds_dwordx4 v224, s[42:43]\n\t" "s_add_u32 m0, %18, 0xd220\n\t" "s_nop 0\n\t"
                     "global_load_lds_dwordx4 v225, s[42:43]\n\t" "s_add_u32 m0, %18, 0xd620\n\t" "s_nop 0\n\t" "global_load_lds_dwordx4 v226, s[42:43]\n\t" "s_add_u32 s42, s46, 0x240000\n\t" "s_addc_u32 s43, s47, 0\n\t"
                     "s_add_u32 m0, %18, 0x12300\n\t" "s_nop 0\n\t" "global_load_lds_dwordx4 v224, s[42:43]\n\t" "s_add_u32 m0, %18, 0x12700\n\t" "s_nop 0\n\t" "global_load_lds_dwordx4 v225, s[42:43]\n\t"
                     "s_add_u32 m0, %18, 0x12b00\n\t" "s_nop 0\n\t" "global_load_lds_dwordx4 v226, s[42:43]\n\t" "s_add_u32 s42, s46, 0x258000\n\t" "s_addc_u32 s43, s47, 0\n\t" "s_add_u32 m0, %18, 0x12f20\n\t"
                     "s_nop 0\n\t" "global_load_lds_dwordx4 v224, s[42:43]\n\t" "s_add_u32 m0, %18, 0x13320\n\t" "s_nop 0\n\t" "global_load_lds_dwordx4 v225, s[42:43]\n\t" "s_add_u32 m0, %18, 0x13720\n\t" "s_nop 0\n\t"
                     "global_load_lds_dwordx4 v226, s[42:43]\n\t" "s_waitcnt vmcnt(12) lgkmcnt(0)\n\t" "s_barrier\n\t" "s_add_u32 s42, s46, 0x300000\n\t" "s_addc_u32 s43, s47, 0\n\t" "s_add_u32 m0, %18, 0x18400\n\t"
                     "s_nop 0\n\t" "global_load_lds_dwordx4 v224, s[42:43]\n\t" "s_add_u32 m0, %18, 0x18800\n\t" "s_nop 0\n\t" "global_load_lds_dwordx4 v225, s[42:43]\n\t" "s_add_u32 m0, %18, 0x18c00\n\t" "s_nop 0\n\t"
                     "global_load_lds_dwordx4 v226, s[42:43]\n\t" "s_add_u32 s42, s46, 0x318000\n\t" "s_addc_u32 s43, s47, 0\n\t" "s_add_u32 m0, %18, 0x19020\n\t" "s_nop 0\n\t" "global_load_lds_dwordx4 v224, s[42:43]\n\t"
                     "s_add_u32 m0, %18, 0x19420\n\t" "s_nop 0\n\t" "global_load_lds_dwordx4 v225, s[42:43]\n\t" "s_add_u32 m0, %18, 0x19820\n\t" "s_nop 0\n\t" "global_load_lds_dwordx4 v226, s[42:43]\n\t"
                     "s_add_u32 s42, s46, 0x3c0000\n\t" "s_addc_u32 s43, s47, 0\n\t" "s_add_u32 m0, %18, 0x1e500\n\t" "s_nop 0\n\t" "global_load_lds_dwordx4 v224, s[42:43]\n\t" "s_add_u32 m0, %18, 0x1e900\n\t"
                     "s_nop 0\n\t" "global_load_lds_dwordx4 v225, s[42:43]\n\t" "s_add_u32 m0, %18, 0x1ed00\n\t" "s_nop 0\n\t" "global_load_lds_dwordx4 v226, s[42:43]\n\t" "s_add_u32 s42, s46, 0x3d8000\n\t"
                     "s_addc_u32 s43, s47, 0\n\t" "s_add_u32 m0, %18, 0x1f120\n\t" "s_nop 0\n\t" "global_load_lds_dwordx4 v224, s[42:43]\n\t" "s_add_u32 m0, %18, 0x1f520\n\t" "s_nop 0\n\t"
                     "global_load_lds_dwordx4 v225, s[42:43]\n\t" "s_add_u32 m0, %18, 0x1f920\n\t" "s_nop 0\n\t" "global_load_lds_dwordx4 v226, s[42:43]\n\t" "v_add_u32 v232, 0x0, v229\n\t"
                     "ds_read_b64_tr_b16 v[8:9], v232\n\t" "ds_read_b64_tr_b16 v[10:11], v232 offset:768\n\t" "ds_read_b64_tr_b16 v[12:13], v232 offset:12416\n\t" "ds_read_b64_tr_b16 v[14:15], v232 offset:13184\n\t"
                     "ds_read_b64_tr_b16 a[96:97], v232 offset:128\n\t" "ds_read_b64_tr_b16 a[98:99], v232 offset:896\n\t" "ds_read_b64_tr_b16 a[100:101], v232 offset:12544\n\t"
                     "ds_read_b64_tr_b16 a[102:103], v232 offset:13312\n\t" "ds_read_b64_tr_b16 a[176:177], v232 offset:256\n\t" "ds_read_b64_tr_b16 a[178:179], v232 offset:1024\n\t"
                     "ds_read_b64_tr_b16 a[180:181], v232 offset:12672\n\t" "ds_read_b64_tr_b16 a[182:183], v232 offset:13440\n\t" "ds_read_b64_tr_b16 v[24:25], v232 offset:24832\n\t"
                     "ds_read_b64_tr_b16 v[26:27], v232 offset:25600\n\t" "ds_read_b64_tr_b16 v[28:29], v232 offset:37248\n\t" "ds_read_b64_tr_b16 v[30:31], v232 offset:38016\n\t"
                     "ds_read_b64_tr_b16 a[104:105], v232 offset:24960\n\t" "ds_read_b64_tr_b16 a[106:107], v232 offset:25728\n\t" "ds_read_b64_tr_b16 a[108:109], v232 offset:37376\n\t"
                     "ds_read_b64_tr_b16 a[110:111], v232 offset:38144\n\t" "ds_read_b64_tr_b16 a[184:185], v232 offset:25088\n\t" "ds_read_b64_tr_b16 a[186:187], v232 offset:25856\n\t"
                     "ds_read_b64_tr_b16 a[188:189], v232 offset:37504\n\t" "ds_read_b64_tr_b16 a[190:191], v232 offset:38272\n\t" "s_waitcnt vmcnt(12) lgkmcnt(0)\n\t" "s_barrier\n\t" "s_add_u32 s42, s46, 0x480000\n\t"
                     "s_addc_u32 s43, s47, 0\n\t" "s_add_u32 m0, %18, 0x0\n\t" "s_nop 0\n\t" "global_load_lds_dwordx4 v224, s[42:43]\n\t" "s_add_u32 m0, %18, 0x400\n\t" "s_nop 0\n\t"
                     "global_load_lds_dwordx4 v225, s[42:43]\n\t" "s_add_u32 m0, %18, 0x800\n\t" "s_nop 0\n\t" "global_load_lds_dwordx4 v226, s[42:43]\n\t" "s_add_u32 s42, s46, 0x498000\n\t" "s_addc_u32 s43, s47, 0\n\t"
                     "s_add_u32 m0, %18, 0xc20\n\t" "s_nop 0\n\t" "global_load_lds_dwordx4 v224, s[42:43]\n\t" "s_add_u32 m0, %18, 0x1020\n\t" "s_nop 0\n\t" "global_load_lds_dwordx4 v225, s[42:43]\n\t"
                     "s_add_u32 m0, %18, 0x1420\n\t" "s_nop 0\n\t" "global_load_lds_dwordx4 v226, s[42:43]\n\t" "s_add_u32 s42, s46, 0x540000\n\t" "s_addc_u32 s43, s47, 0\n\t" "s_add_u32 m0, %18, 0x6100\n\t" "s_nop 0\n\t"
                     "global_load_lds_dwordx4 v224, s[42:43]\n\t" "s_add_u32 m0, %18, 0x6500\n\t" "s_nop 0\n\t" "global_load_lds_dwordx4 v225, s[42:43]\n\t" "s_add_u32 m0, %18, 0x6900\n\t" "s_nop 0\n\t"
                     "global_load_lds_dwordx4 v226, s[42:43]\n\t" "s_add_u32 s42, s46, 0x558000\n\t" "s_addc_u32 s43, s47, 0\n\t" "s_add_u32 m0, %18, 0x6d20\n\t" "s_nop 0\n\t" "global_load_lds_dwordx4 v224, s[42:43]\n\t"
                     "s_add_u32 m0, %18, 0x7120\n\t" "s_nop 0\n\t" "global_load_lds_dwordx4 v225, s[42:43]\n\t" "s_add_u32 m0, %18, 0x7520\n\t" "s_nop 0\n\t" "global_load_lds_dwordx4 v226, s[42:43]\n\t"
                     "v_add_u32 v232, 0xc200, v229\n\t" "ds_read_b64_tr_b16 v[40:41], v232\n\t" "ds_read_b64_tr_b16 v[42:43], v232 offset:768\n\t" "ds_read_b64_tr_b16 v[44:45], v232 offset:12416\n\t"
                     "ds_read_b64_tr_b16 v[46:47], v232 offset:13184\n\t" "ds_read_b64_tr_b16 a[112:113], v232 offset:128\n\t" "ds_read_b64_tr_b16 a[114:115], v232 offset:896\n\t"
                     "ds_read_b64_tr_b16 a[116:117], v232 offset:12544\n\t" "ds_read_b64_tr_b16 a[118:119], v232 offset:13312\n\t" "ds_read_b64_tr_b16 a[192:193], v232 offset:256\n\t"
                     "ds_read_b64_tr_b16 a[194:195], v232 offset:1024\n\t" "ds_read_b64_tr_b16 a[196:197], v232 offset:12672\n\t" "ds_read_b64_tr_b16 a[198:199], v232 offset:13440\n\t"
                     "ds_read_b64_tr_b16 v[56:57], v232 offset:24832\n\t" "ds_read_b64_tr_b16 v[58:59], v232 offset:25600\n\t" "ds_read_b64_tr_b16 v[60:61], v232 offset:37248\n\t"
                     "ds_read_b64_tr_b16 v[62:63], v232 offset:38016\n\t" "ds_read_b64_tr_b16 a[120:121], v232 offset:24960\n\t" "ds_read_b64_tr_b16 a[122:123], v232 offset:25728\n\t"
                     "ds_read_b64_tr_b16 a[124:125], v232 offset:37376\n\t" "ds_read_b64_tr_b16 a[126:127], v232 offset:38144\n\t" "ds_read_b64_tr_b16 a[200:201], v232 offset:25088\n\t"
                     "ds_read_b64_tr_b16 a[202:203], v232 offset:25856\n\t" "ds_read_b64_tr_b16 a[204:205], v232 offset:37504\n\t" "ds_read_b64_tr_b16 a[206:207], v232 offset:38272\n\t" "s_waitcnt vmcnt(12) lgkmcnt(0)\n\t"
                     "s_barrier\n\t" "s_add_u32 s42, s46, 0x600000\n\t" "s_addc_u32 s43, s47, 0\n\t" "s_add_u32 m0, %18, 0xc200\n\t" "s_nop 0\n\t" "global_load_lds_dwordx4 v224, s[42:43]\n\t"
                     "s_add_u32 m0, %18, 0xc600\n\t" "s_nop 0\n\t" "global_load_lds_dwordx4 v225, s[42:43]\n\t" "s_add_u32 m0, %18, 0xca00\n\t" "s_nop 0\n\t" "global_load_lds_dwordx4 v226, s[42:43]\n\t"
                     "s_add_u32 s42, s46, 0x618000\n\t" "s_addc_u32 s43, s47, 0\n\t" "s_add_u32 m0, %18, 0xce20\n\t" "s_nop 0\n\t" "global_load_lds_dwordx4 v224, s[42:43]\n\t" "s_add_u32 m0, %18, 0xd220\n\t" "s_nop 0\n\t"
                     "global_load_lds_dwordx4 v225, s[42:43]\n\t" "s_add_u32 m0, %18, 0xd620\n\t" "s_nop 0\n\t" "global_load_lds_dwordx4 v226, s[42:43]\n\t" "s_add_u32 s42, s46, 0x6c0000\n\t" "s_addc_u32 s43, s47, 0\n\t"
                     "s_add_u32 m0, %18, 0x12300\n\t" "s_nop 0\n\t" "global_load_lds_dwordx4 v224, s[42:43]\n\t" "s_add_u32 m0, %18, 0x12700\n\t" "s_nop 0\n\t" "global_load_lds_dwordx4 v225, s[42:43]\n\t"
                     "s_add_u32 m0, %18, 0x12b00\n\t" "s_nop 0\n\t" "global_load_lds_dwordx4 v226, s[42:43]\n\t" "s_add_u32 s42, s46, 0x6d8000\n\t" "s_addc_u32 s43, s47, 0\n\t" "s_add_u32 m0, %18, 0x12f20\n\t"
                     "s_nop 0\n\t" "global_load_lds_dwordx4 v224, s[42:43]\n\t" "s_add_u32 m0, %18, 0x13320\n\t" "s_nop 0\n\t" "global_load_lds_dwordx4 v225, s[42:43]\n\t" "s_add_u32 m0, %18, 0x13720\n\t" "s_nop 0\n\t"
                     "global_load_lds_dwordx4 v226, s[42:43]\n\t" "v_add_u32 v232, 0x18400, v229\n\t" "ds_read_b64_tr_b16 v[72:73], v232\n\t" "ds_read_b64_tr_b16 v[74:75], v232 offset:768\n\t"
                     "ds_read_b64_tr_b16 v[76:77], v232 offset:12416\n\t" "ds_read_b64_tr_b16 v[78:79], v232 offset:13184\n\t" "ds_read_b64_tr_b16 a[128:129], v232 offset:128\n\t"
                     "ds_read_b64_tr_b16 a[130:131], v232 offset:896\n\t" "ds_read_b64_tr_b16 a[132:133], v232 offset:12544\n\t" "ds_read_b64_tr_b16 a[134:135], v232 offset:13312\n\t"
                     "ds_read_b64_tr_b16 a[208:209], v232 offset:256\n\t" "ds_read_b64_tr_b16 a[210:211], v232 offset:1024\n\t" "ds_read_b64_tr_b16 a[212:213], v232 offset:12672\n\t"
                     "ds_read_b64_tr_b16 a[214:215], v232 offset:13440\n\t" "ds_read_b64_tr_b16 v[88:89], v232 offset:24832\n\t" "ds_read_b64_tr_b16 v[90:91], v232 offset:25600\n\t"
                     "ds_read_b64_tr_b16 v[92:93], v232 offset:37248\n\t" "ds_read_b64_tr_b16 v[94:95], v232 offset:38016\n\t" "ds_read_b64_tr_b16 a[136:137], v232 offset:24960\n\t"
                     "ds_read_b64_tr_b16 a[138:139], v232 offset:25728\n\t" "ds_read_b64_tr_b16 a[140:141], v232 offset:37376\n\t" "ds_read_b64_tr_b16 a[142:143], v232 offset:38144\n\t"
                     "ds_read_b64_tr_b16 a[216:217], v232 offset:25088\n\t" "ds_read_b64_tr_b16 a[218:219], v232 offset:25856\n\t" "ds_read_b64_tr_b16 a[220:221], v232 offset:37504\n\t"
                     "ds_read_b64_tr_b16 a[222:223], v232 offset:38272\n\t" "s_waitcnt vmcnt(12) lgkmcnt(0)\n\t" "s_barrier\n\t" "s_add_u32 s42, s44, 0x0\n\t" "s_addc_u32 s43, s45, 0\n\t" "s_add_u32 m0, %18, 0x18400\n\t"
                     "s_nop 0\n\t" "global_load_lds_dwordx4 v224, s[42:43]\n\t" "s_add_u32 m0, %18, 0x18800\n\t" "s_nop 0\n\t" "global_load_lds_dwordx4 v225, s[42:43]\n\t" "s_add_u32 m0, %18, 0x18c00\n\t" "s_nop 0\n\t"
                     "global_load_lds_dwordx4 v226, s[42:43]\n\t" "s_add_u32 s42, s44, 0x18000\n\t" "s_addc_u32 s43, s45, 0\n\t" "s_add_u32 m0, %18, 0x19020\n\t" "s_nop 0\n\t" "global_load_lds_dwordx4 v224, s[42:43]\n\t"
                     "s_add_u32 m0, %18, 0x19420\n\t" "s_nop 0\n\t" "global_load_lds_dwordx4 v225, s[42:43]\n\t" "s_add_u32 m0, %18, 0x19820\n\t" "s_nop 0\n\t" "global_load_lds_dwordx4 v226, s[42:43]\n\t"
                     "s_add_u32 s42, s44, 0xc0000\n\t" "s_addc_u32 s43, s45, 0\n\t" "s_add_u32 m0, %18, 0x1e500\n\t" "s_nop 0\n\t" "global_load_lds_dwordx4 v224, s[42:43]\n\t" "s_add_u32 m0, %18, 0x1e900\n\t" "s_nop 0\n\t"
                     "global_load_lds_dwordx4 v225, s[42:43]\n\t" "s_add_u32 m0, %18, 0x1ed00\n\t" "s_nop 0\n\t" "global_load_lds_dwordx4 v226, s[42:43]\n\t" "s_add_u32 s42, s44, 0xd8000\n\t" "s_addc_u32 s43, s45, 0\n\t"
                     "s_add_u32 m0, %18, 0x1f120\n\t" "s_nop 0\n\t" "global_load_lds_dwordx4 v224, s[42:43]\n\t" "s_add_u32 m0, %18, 0x1f520\n\t" "s_nop 0\n\t" "global_load_lds_dwordx4 v225, s[42:43]\n\t"
                     "s_add_u32 m0, %18, 0x1f920\n\t" "s_nop 0\n\t" "global_load_lds_dwordx4 v226, s[42:43]\n\t" "v_add_u32 v232, 0x0, v229\n\t" "ds_read_b64_tr_b16 v[104:105], v232\n\t"
                     "ds_read_b64_tr_b16 v[106:107], v232 offset:768\n\t" "ds_read_b64_tr_b16 v[108:109], v232 offset:12416\n\t" "ds_read_b64_tr_b16 v[110:111], v232 offset:13184\n\t"
                     "ds_read_b64_tr_b16 a[144:145], v232 offset:128\n\t" "ds_read_b64_tr_b16 a[146:147], v232 offset:896\n\t" "ds_read_b64_tr_b16 a[148:149], v232 offset:12544\n\t"
                     "ds_read_b64_tr_b16 a[150:151], v232 offset:13312\n\t" "ds_read_b64_tr_b16 a[224:225], v232 offset:256\n\t" "ds_read_b64_tr_b16 a[226:227], v232 offset:1024\n\t"
                     "ds_read_b64_tr_b16 a[228:229], v232 offset:12672\n\t" "ds_read_b64_tr_b16 a[230:231], v232 offset:13440\n\t" "ds_read_b64_tr_b16 v[120:121], v232 offset:24832\n\t"
                     "ds_read_b64_tr_b16 v[122:123], v232 offset:25600\n\t" "ds_read_b64_tr_b16 v[124:125], v232 offset:37248\n\t" "ds_read_b64_tr_b16 v[126:127], v232 offset:38016\n\t"
                     "ds_read_b64_tr_b16 a[152:153], v232 offset:24960\n\t" "ds_read_b64_tr_b16 a[154:155], v232 offset:25728\n\t" "ds_read_b64_tr_b16 a[156:157], v232 offset:37376\n\t"
                     "ds_read_b64_tr_b16 a[158:159], v232 offset:38144\n\t" "ds_read_b64_tr_b16 a[232:233], v232 offset:25088\n\t" "ds_read_b64_tr_b16 a[234:235], v232 offset:25856\n\t"
                     "ds_read_b64_tr_b16 a[236:237], v232 offset:37504\n\t" "ds_read_b64_tr_b16 a[238:239], v232 offset:38272\n\t" "s_waitcnt vmcnt(12) lgkmcnt(0)\n\t" "s_barrier\n\t" "s_add_u32 s42, s44, 0x180000\n\t"
                     "s_addc_u32 s43, s45, 0\n\t" "s_add_u32 m0, %18, 0x0\n\t" "s_nop 0\n\t" "global_load_lds_dwordx4 v224, s[42:43]\n\t" "s_add_u32 m0, %18, 0x400\n\t" "s_nop 0\n\t"
                     "global_load_lds_dwordx4 v225, s[42:43]\n\t" "s_add_u32 m0, %18, 0x800\n\t" "s_nop 0\n\t" "global_load_lds_dwordx4 v226, s[42:43]\n\t" "s_add_u32 s42, s44, 0x198000\n\t" "s_addc_u32 s43, s45, 0\n\t"
                     "s_add_u32 m0, %18, 0xc20\n\t" "s_nop 0\n\t" "global_load_lds_dwordx4 v224, s[42:43]\n\t" "s_add_u32 m0, %18, 0x1020\n\t" "s_nop 0\n\t" "global_load_lds_dwordx4 v225, s[42:43]\n\t"
                     "s_add_u32 m0, %18, 0x1420\n\t" "s_nop 0\n\t" "global_load_lds_dwordx4 v226, s[42:43]\n\t" "s_add_u32 s42, s44, 0x240000\n\t" "s_addc_u32 s43, s45, 0\n\t" "s_add_u32 m0, %18, 0x6100\n\t" "s_nop 0\n\t"
                     "global_load_lds_dwordx4 v224, s[42:43]\n\t" "s_add_u32 m0, %18, 0x6500\n\t" "s_nop 0\n\t" "global_load_lds_dwordx4 v225, s[42:43]\n\t" "s_add_u32 m0, %18, 0x6900\n\t" "s_nop 0\n\t"
                     "global_load_lds_dwordx4 v226, s[42:43]\n\t" "s_add_u32 s42, s44, 0x258000\n\t" "s_addc_u32 s43, s45, 0\n\t" "s_add_u32 m0, %18, 0x6d20\n\t" "s_nop 0\n\t" "global_load_lds_dwordx4 v224, s[42:43]\n\t"
                     "s_add_u32 m0, %18, 0x7120\n\t" "s_nop 0\n\t" "global_load_lds_dwordx4 v225, s[42:43]\n\t" "s_add_u32 m0, %18, 0x7520\n\t" "s_nop 0\n\t" "global_load_lds_dwordx4 v226, s[42:43]\n\t"
                     "v_add_u32 v232, 0xc200, v229\n\t" "ds_read_b64_tr_b16 a[56:57], v232\n\t" "ds_read_b64_tr_b16 a[58:59], v232 offset:768\n\t" "ds_read_b64_tr_b16 a[60:61], v232 offset:12416\n\t"
                     "ds_read_b64_tr_b16 a[62:63], v232 offset:13184\n\t" "ds_read_b64_tr_b16 a[160:161], v232 offset:128\n\t" "ds_read_b64_tr_b16 a[162:163], v232 offset:896\n\t"
                     "ds_read_b64_tr_b16 a[164:165], v232 offset:12544\n\t" "ds_read_b64_tr_b16 a[166:167], v232 offset:13312\n\t" "ds_read_b64_tr_b16 a[240:241], v232 offset:256\n\t"
                     "ds_read_b64_tr_b16 a[242:243], v232 offset:1024\n\t" "ds_read_b64_tr_b16 a[244:245], v232 offset:12672\n\t" "ds_read_b64_tr_b16 a[246:247], v232 offset:13440\n\t"
                     "ds_read_b64_tr_b16 a[88:89], v232 offset:24832\n\t" "ds_read_b64_tr_b16 a[90:91], v232 offset:25600\n\t" "ds_read_b64_tr_b16 a[92:93], v232 offset:37248\n\t"
                     "ds_read_b64_tr_b16 a[94:95], v232 offset:38016\n\t" "ds_read_b64_tr_b16 a[168:169], v232 offset:24960\n\t" "ds_read_b64_tr_b16 a[170:171], v232 offset:25728\n\t"
                     "ds_read_b64_tr_b16 a[172:173], v232 offset:37376\n\t" "ds_read_b64_tr_b16 a[174:175], v232 offset:38144\n\t" "ds_read_b64_tr_b16 a[248:249], v232 offset:25088\n\t"
                     "ds_read_b64_tr_b16 a[250:251], v232 offset:25856\n\t" "ds_read_b64_tr_b16 a[252:253], v232 offset:37504\n\t" "ds_read_b64_tr_b16 a[254:255], v232 offset:38272\n\t" "s_waitcnt vmcnt(12) lgkmcnt(0)\n\t"
                     "s_barrier\n\t" "s_add_u32 s42, s44, 0x300000\n\t" "s_addc_u32 s43, s45, 0\n\t" "s_add_u32 m0, %18, 0xc200\n\t" "s_nop 0\n\t" "global_load_lds_dwordx4 v224, s[42:43]\n\t"
                     "s_add_u32 m0, %18, 0xc600\n\t" "s_nop 0\n\t" "global_load_lds_dwordx4 v225, s[42:43]\n\t" "s_add_u32 m0, %18, 0xca00\n\t" "s_nop 0\n\t" "global_load_lds_dwordx4 v226, s[42:43]\n\t"
                     "s_add_u32 s42, s44, 0x318000\n\t" "s_addc_u32 s43, s45, 0\n\t" "s_add_u32 m0, %18, 0xce20\n\t" "s_nop 0\n\t" "global_load_lds_dwordx4 v224, s[42:43]\n\t" "s_add_u32 m0, %18, 0xd220\n\t" "s_nop 0\n\t"
                     "global_load_lds_dwordx4 v225, s[42:43]\n\t" "s_add_u32 m0, %18, 0xd620\n\t" "s_nop 0\n\t" "global_load_lds_dwordx4 v226, s[42:43]\n\t" "s_add_u32 s42, s44, 0x3c0000\n\t" "s_addc_u32 s43, s45, 0\n\t"
                     "s_add_u32 m0, %18, 0x12300\n\t" "s_nop 0\n\t" "global_load_lds_dwordx4 v224, s[42:43]\n\t" "s_add_u32 m0, %18, 0x12700\n\t" "s_nop 0\n\t" "global_load_lds_dwordx4 v225, s[42:43]\n\t"
                     "s_add_u32 m0, %18, 0x12b00\n\t" "s_nop 0\n\t" "global_load_lds_dwordx4 v226, s[42:43]\n\t" "s_add_u32 s42, s44, 0x3d8000\n\t" "s_addc_u32 s43, s45, 0\n\t" "s_add_u32 m0, %18, 0x12f20\n\t"
                     "s_nop 0\n\t" "global_load_lds_dwordx4 v224, s[42:43]\n\t" "s_add_u32 m0, %18, 0x13320\n\t" "s_nop 0\n\t" "global_load_lds_dwordx4 v225, s[42:43]\n\t" "s_add_u32 m0, %18, 0x13720\n\t" "s_nop 0\n\t"
                     "global_load_lds_dwordx4 v226, s[42:43]\n\t" "v_add_u32 v232, 0x18400, v227\n\t" "v_add_u32 v233, 0x18400, v228\n\t" "ds_read_b64_tr_b16 v[128:129], v232\n\t"
                     "ds_read_b64_tr_b16 v[130:131], v232 offset:768\n\t" "ds_read_b64_tr_b16 v[132:133], v232 offset:12416\n\t" "ds_read_b64_tr_b16 v[134:135], v232 offset:13184\n\t"
                     "ds_read_b64_tr_b16 v[0:1], v232 offset:32\n\t" "ds_read_b64_tr_b16 v[2:3], v232 offset:800\n\t" "ds_read_b64_tr_b16 v[4:5], v232 offset:12448\n\t" "ds_read_b64_tr_b16 v[6:7], v232 offset:13216\n\t"
                     "ds_read_b64_tr_b16 v[136:137], v233 offset:64\n\t" "ds_read_b64_tr_b16 v[138:139], v233 offset:832\n\t" "ds_read_b64_tr_b16 v[140:141], v233 offset:12480\n\t"
                     "ds_read_b64_tr_b16 v[142:143], v233 offset:13248\n\t" "ds_read_b64_tr_b16 v[144:145], v232 offset:24832\n\t" "ds_read_b64_tr_b16 v[146:147], v232 offset:25600\n\t"
                     "ds_read_b64_tr_b16 v[148:149], v232 offset:37248\n\t" "ds_read_b64_tr_b16 v[150:151], v232 offset:38016\n\t" "ds_read_b64_tr_b16 v[16:17], v232 offset:24864\n\t"
                     "ds_read_b64_tr_b16 v[18:19], v232 offset:25632\n\t" "ds_read_b64_tr_b16 v[20:21], v232 offset:37280\n\t" "ds_read_b64_tr_b16 v[22:23], v232 offset:38048\n\t"
                     "ds_read_b64_tr_b16 v[152:153], v233 offset:24896\n\t" "ds_read_b64_tr_b16 v[154:155], v233 offset:25664\n\t" "ds_read_b64_tr_b16 v[156:157], v233 offset:37312\n\t"
                     "ds_read_b64_tr_b16 v[158:159], v233 offset:38080\n\t" "s_waitcnt vmcnt(12) lgkmcnt(0)\n\t" "s_barrier\n\t" "v_mov_b32 %14, 0\n\t" "global_load_dwordx2 %11, v231, %19 sc1\n\t"
                     "global_load_dwordx2 %12, %14, %19 offset:512 sc1\n\t" "global_load_dwordx2 %13, %14, %19 offset:1024 sc1\n\t" "s_add_u32 s42, s44, 0x480000\n\t" "s_addc_u32 s43, s45, 0\n\t"
                     "s_add_u32 m0, %18, 0x18400\n\t" "s_nop 0\n\t" "global_load_lds_dwordx4 v224, s[42:43]\n\t" "s_add_u32 m0, %18, 0x18800\n\t" "s_nop 0\n\t" "global_load_lds_dwordx4 v225, s[42:43]\n\t"
                     "s_add_u32 m0, %18, 0x18c00\n\t" "s_nop 0\n\t" "global_load_lds_dwordx4 v226, s[42:43]\n\t" "s_add_u32 s42, s44, 0x498000\n\t" "s_addc_u32 s43, s45, 0\n\t" "s_add_u32 m0, %18, 0x19020\n\t"
                     "s_nop 0\n\t" "global_load_lds_dwordx4 v224, s[42:43]\n\t" "s_add_u32 m0, %18, 0x19420\n\t" "s_nop 0\n\t" "global_load_lds_dwordx4 v225, s[42:43]\n\t" "s_add_u32 m0, %18, 0x19820\n\t" "s_nop 0\n\t"
                     "global_load_lds_dwordx4 v226, s[42:43]\n\t" "s_add_u32 s42, s44, 0x540000\n\t" "s_addc_u32 s43, s45, 0\n\t" "s_add_u32 m0, %18, 0x1e500\n\t" "s_nop 0\n\t" "global_load_lds_dwordx4 v224, s[42:43]\n\t"
                     "s_add_u32 m0, %18, 0x1e900\n\t" "s_nop 0\n\t" "global_load_lds_dwordx4 v225, s[42:43]\n\t" "s_add_u32 m0, %18, 0x1ed00\n\t" "s_nop 0\n\t" "global_load_lds_dwordx4 v226, s[42:43]\n\t"
                     "s_add_u32 s42, s44, 0x558000\n\t" "s_addc_u32 s43, s45, 0\n\t" "s_add_u32 m0, %18, 0x1f120\n\t" "s_nop 0\n\t" "global_load_lds_dwordx4 v224, s[42:43]\n\t" "s_add_u32 m0, %18, 0x1f520\n\t"
                     "s_nop 0\n\t" "global_load_lds_dwordx4 v225, s[42:43]\n\t" "s_add_u32 m0, %18, 0x1f920\n\t" "s_nop 0\n\t" "global_load_lds_dwordx4 v226, s[42:43]\n\t" "v_add_u32 v232, 0x0, v227\n\t"
                     "v_add_u32 v233, 0x0, v228\n\t" "ds_read_b64_tr_b16 v[160:161], v232\n\t" "ds_read_b64_tr_b16 v[162:163], v232 offset:768\n\t" "ds_read_b64_tr_b16 v[164:165], v232 offset:12416\n\t"
                     "ds_read_b64_tr_b16 v[166:167], v232 offset:13184\n\t" "ds_read_b64_tr_b16 v[32:33], v232 offset:32\n\t" "ds_read_b64_tr_b16 v[34:35], v232 offset:800\n\t"
                     "ds_read_b64_tr_b16 v[36:37], v232 offset:12448\n\t" "ds_read_b64_tr_b16 v[38:39], v232 offset:13216\n\t" "ds_read_b64_tr_b16 v[168:169], v233 offset:64\n\t"
                     "ds_read_b64_tr_b16 v[170:171], v233 offset:832\n\t" "ds_read_b64_tr_b16 v[172:173], v233 offset:12480\n\t" "ds_read_b64_tr_b16 v[174:175], v233 offset:13248\n\t"
                     "ds_read_b64_tr_b16 v[176:177], v232 offset:24832\n\t" "ds_read_b64_tr_b16 v[178:179], v232 offset:25600\n\t" "ds_read_b64_tr_b16 v[180:181], v232 offset:37248\n\t"
                     "ds_read_b64_tr_b16 v[182:183], v232 offset:38016\n\t" "ds_read_b64_tr_b16 v[48:49], v232 offset:24864\n\t" "ds_read_b64_tr_b16 v[50:51], v232 offset:25632\n\t"
                     "ds_read_b64_tr_b16 v[52:53], v232 offset:37280\n\t" "ds_read_b64_tr_b16 v[54:55], v232 offset:38048\n\t" "ds_read_b64_tr_b16 v[184:185], v233 offset:24896\n\t"
                     "ds_read_b64_tr_b16 v[186:187], v233 offset:25664\n\t" "ds_read_b64_tr_b16 v[188:189], v233 offset:37312\n\t" "ds_read_b64_tr_b16 v[190:191], v233 offset:38080\n\t" "s_waitcnt vmcnt(12) lgkmcnt(0)\n\t"
                     "s_barrier\n\t" "s_add_u32 s42, s44, 0x600000\n\t" "s_addc_u32 s43, s45, 0\n\t" "s_add_u32 m0, %18, 0x0\n\t" "s_nop 0\n\t" "global_load_lds_dwordx4 v224, s[42:43]\n\t" "s_add_u32 m0, %18, 0x400\n\t"
                     "s_nop 0\n\t" "global_load_lds_dwordx4 v225, s[42:43]\n\t" "s_add_u32 m0, %18, 0x800\n\t" "s_nop 0\n\t" "global_load_lds_dwordx4 v226, s[42:43]\n\t" "s_add_u32 s42, s44, 0x618000\n\t"
                     "s_addc_u32 s43, s45, 0\n\t" "s_add_u32 m0, %18, 0xc20\n\t" "s_nop 0\n\t" "global_load_lds_dwordx4 v224, s[42:43]\n\t" "s_add_u32 m0, %18, 0x1020\n\t" "s_nop 0\n\t"
                     "global_load_lds_dwordx4 v225, s[42:43]\n\t" "s_add_u32 m0, %18, 0x1420\n\t" "s_nop 0\n\t" "global_load_lds_dwordx4 v226, s[42:43]\n\t" "s_add_u32 s42, s44, 0x6c0000\n\t" "s_addc_u32 s43, s45, 0\n\t"
                     "s_add_u32 m0, %18, 0x6100\n\t" "s_nop 0\n\t" "global_load_lds_dwordx4 v224, s[42:43]\n\t" "s_add_u32 m0, %18, 0x6500\n\t" "s_nop 0\n\t" "global_load_lds_dwordx4 v225, s[42:43]\n\t"
                     "s_add_u32 m0, %18, 0x6900\n\t" "s_nop 0\n\t" "global_load_lds_dwordx4 v226, s[42:43]\n\t" "s_add_u32 s42, s44, 0x6d8000\n\t" "s_addc_u32 s43, s45, 0\n\t" "s_add_u32 m0, %18, 0x6d20\n\t" "s_nop 0\n\t"
                     "global_load_lds_dwordx4 v224, s[42:43]\n\t" "s_add_u32 m0, %18, 0x7120\n\t" "s_nop 0\n\t" "global_load_lds_dwordx4 v225, s[42:43]\n\t" "s_add_u32 m0, %18, 0x7520\n\t" "s_nop 0\n\t"
                     "global_load_lds_dwordx4 v226, s[42:43]\n\t" "global_load_dword %14, %14, %20 sc1\n\t" "v_add_u32 v232, 0xc200, v227\n\t" "v_add_u32 v233, 0xc200, v228\n\t" "ds_read_b64_tr_b16 v[192:193], v232\n\t"
                     "ds_read_b64_tr_b16 v[194:195], v232 offset:768\n\t" "ds_read_b64_tr_b16 v[196:197], v232 offset:12416\n\t" "ds_read_b64_tr_b16 v[198:199], v232 offset:13184\n\t"
                     "ds_read_b64_tr_b16 v[64:65], v232 offset:32\n\t" "ds_read_b64_tr_b16 v[66:67], v232 offset:800\n\t" "ds_read_b64_tr_b16 v[68:69], v232 offset:12448\n\t"
                     "ds_read_b64_tr_b16 v[70:71], v232 offset:13216\n\t" "ds_read_b64_tr_b16 v[200:201], v233 offset:64\n\t" "ds_read_b64_tr_b16 v[202:203], v233 offset:832\n\t"
                     "ds_read_b64_tr_b16 v[204:205], v233 offset:12480\n\t" "ds_read_b64_tr_b16 v[206:207], v233 offset:13248\n\t" "ds_read_b64_tr_b16 v[208:209], v232 offset:24832\n\t"
                     "ds_read_b64_tr_b16 v[210:211], v232 offset:25600\n\t" "ds_read_b64_tr_b16 v[212:213], v232 offset:37248\n\t" "ds_read_b64_tr_b16 v[214:215], v232 offset:38016\n\t"
                     "ds_read_b64_tr_b16 v[80:81], v232 offset:24864\n\t" "ds_read_b64_tr_b16 v[82:83], v232 offset:25632\n\t" "ds_read_b64_tr_b16 v[84:85], v232 offset:37280\n\t"
                     "ds_read_b64_tr_b16 v[86:87], v232 offset:38048\n\t" "ds_read_b64_tr_b16 v[216:217], v233 offset:24896\n\t" "ds_read_b64_tr_b16 v[218:219], v233 offset:25664\n\t"
                     "ds_read_b64_tr_b16 v[220:221], v233 offset:37312\n\t" "ds_read_b64_tr_b16 v[222:223], v233 offset:38080\n\t" "s_waitcnt vmcnt(13) lgkmcnt(0)\n\t" "s_barrier\n\t" "v_add_u32 v232, 0x18400, v227\n\t"
                     "v_add_u32 v233, 0x18400, v228\n\t" "ds_read_b64_tr_b16 a[0:1], v232\n\t" "ds_read_b64_tr_b16 a[2:3], v232 offset:768\n\t" "ds_read_b64_tr_b16 a[4:5], v232 offset:12416\n\t"
                     "ds_read_b64_tr_b16 a[6:7], v232 offset:13184\n\t" "ds_read_b64_tr_b16 v[96:97], v232 offset:32\n\t" "ds_read_b64_tr_b16 v[98:99], v232 offset:800\n\t"
                     "ds_read_b64_tr_b16 v[100:101], v232 offset:12448\n\t" "ds_read_b64_tr_b16 v[102:103], v232 offset:13216\n\t" "ds_read_b64_tr_b16 a[8:9], v233 offset:64\n\t"
                     "ds_read_b64_tr_b16 a[10:11], v233 offset:832\n\t" "ds_read_b64_tr_b16 a[12:13], v233 offset:12480\n\t" "ds_read_b64_tr_b16 a[14:15], v233 offset:13248\n\t"
                     "ds_read_b64_tr_b16 a[16:17], v232 offset:24832\n\t" "ds_read_b64_tr_b16 a[18:19], v232 offset:25600\n\t" "ds_read_b64_tr_b16 a[20:21], v232 offset:37248\n\t"
                     "ds_read_b64_tr_b16 a[22:23], v232 offset:38016\n\t" "ds_read_b64_tr_b16 v[112:113], v232 offset:24864\n\t" "ds_read_b64_tr_b16 v[114:115], v232 offset:25632\n\t"
                     "ds_read_b64_tr_b16 v[116:117], v232 offset:37280\n\t" "ds_read_b64_tr_b16 v[118:119], v232 offset:38048\n\t" "ds_read_b64_tr_b16 a[24:25], v233 offset:24896\n\t"
                     "ds_read_b64_tr_b16 a[26:27], v233 offset:25664\n\t" "ds_read_b64_tr_b16 a[28:29], v233 offset:37312\n\t" "ds_read_b64_tr_b16 a[30:31], v233 offset:38080\n\t" "s_waitcnt vmcnt(0) lgkmcnt(0)\n\t"
                     "s_barrier\n\t" "v_add_u32 v232, 0x0, v227\n\t" "v_add_u32 v233, 0x0, v228\n\t" "ds_read_b64_tr_b16 a[32:33], v232\n\t" "ds_read_b64_tr_b16 a[34:35], v232 offset:768\n\t"
                     "ds_read_b64_tr_b16 a[36:37], v232 offset:12416\n\t" "ds_read_b64_tr_b16 a[38:39], v232 offset:13184\n\t" "ds_read_b64_tr_b16 a[40:41], v232 offset:32\n\t"
                     "ds_read_b64_tr_b16 a[42:43], v232 offset:800\n\t" "ds_read_b64_tr_b16 a[44:45], v232 offset:12448\n\t" "ds_read_b64_tr_b16 a[46:47], v232 offset:13216\n\t"
                     "ds_read_b64_tr_b16 a[48:49], v233 offset:64\n\t" "ds_read_b64_tr_b16 a[50:51], v233 offset:832\n\t" "ds_read_b64_tr_b16 a[52:53], v233 offset:12480\n\t"
                     "ds_read_b64_tr_b16 a[54:55], v233 offset:13248\n\t" "ds_read_b64_tr_b16 a[64:65], v232 offset:24832\n\t" "ds_read_b64_tr_b16 a[66:67], v232 offset:25600\n\t"
                     "ds_read_b64_tr_b16 a[68:69], v232 offset:37248\n\t" "ds_read_b64_tr_b16 a[70:71], v232 offset:38016\n\t" "ds_read_b64_tr_b16 a[72:73], v232 offset:24864\n\t"
                     "ds_read_b64_tr_b16 a[74:75], v232 offset:25632\n\t" "ds_read_b64_tr_b16 a[76:77], v232 offset:37280\n\t" "ds_read_b64_tr_b16 a[78:79], v232 offset:38048\n\t"
                     "ds_read_b64_tr_b16 a[80:81], v233 offset:24896\n\t" "ds_read_b64_tr_b16 a[82:83], v233 offset:25664\n\t" "ds_read_b64_tr_b16 a[84:85], v233 offset:37312\n\t"
                     "ds_read_b64_tr_b16 a[86:87], v233 offset:38080\n\t" "s_waitcnt lgkmcnt(0)\n\t" "s_barrier\n\t" "ds_write_b128 v230, v[0:3]\n\t" "ds_write_b128 v230, v[4:7] offset:1024\n\t"
                     "ds_write_b128 v230, v[8:11] offset:2048\n\t" "ds_write_b128 v230, v[12:15] offset:3072\n\t" "ds_write_b128 v230, v[16:19] offset:4096\n\t" "ds_write_b128 v230, v[20:23] offset:5120\n\t"
                     "ds_write_b128 v230, v[24:27] offset:6144\n\t" "ds_write_b128 v230, v[28:31] offset:7168\n\t" "ds_write_b128 v230, v[32:35] offset:8192\n\t" "ds_write_b128 v230, v[36:39] offset:9216\n\t"
                     "ds_write_b128 v230, v[40:43] offset:10240\n\t" "ds_write_b128 v230, v[44:47] offset:11264\n\t" "ds_write_b128 v230, v[48:51] offset:12288\n\t" "ds_write_b128 v230, v[52:55] offset:13312\n\t"
                     "ds_write_b128 v230, v[56:59] offset:14336\n\t" "ds_write_b128 v230, v[60:63] offset:15360\n\t" "ds_write_b128 v230, v[64:67] offset:16384\n\t" "ds_write_b128 v230, v[68:71] offset:17408\n\t"
                     "ds_write_b128 v230, v[72:75] offset:18432\n\t" "ds_write_b128 v230, v[76:79] offset:19456\n\t" "ds_write_b128 v230, v[80:83] offset:20480\n\t" "ds_write_b128 v230, v[84:87] offset:21504\n\t"
                     "ds_write_b128 v230, v[88:91] offset:22528\n\t" "ds_write_b128 v230, v[92:95] offset:23552\n\t" "ds_write_b128 v230, v[96:99] offset:24576\n\t" "ds_write_b128 v230, v[100:103] offset:25600\n\t"
                     "ds_write_b128 v230, v[104:107] offset:26624\n\t" "ds_write_b128 v230, v[108:111] offset:27648\n\t" "ds_write_b128 v230, v[112:115] offset:28672\n\t" "ds_write_b128 v230, v[116:119] offset:29696\n\t"
                     "ds_write_b128 v230, v[120:123] offset:30720\n\t" "ds_write_b128 v230, v[124:127] offset:31744\n\t"
                     : "=&{a[0:31]}"(wa[0]), "=&{a[32:63]}"(wa[1]), "=&{a[64:95]}"(wa[2]), "=&{a[96:127]}"(wa[3]), "=&{a[128:159]}"(wa[4]), "=&{a[160:191]}"(wa[5]), "=&{a[192:223]}"(wa[6]), "=&{a[224:255]}"(wa[7]), "=&{v[128:159]}"(wv[0]), "=&{v[160:191]}"(wv[1]), "=&{v[192:223]}"(wv[2]), "=&v"(y.r), "=&v"(y.g), "=&v"(y.e), "=&v"(kfl)
                     : "{v[224:231]}"(pa), "{s[44:45]}"(p.wq + h * DH * 2 + (size_t)(16 * w) * (HD * 2)), "{s[46:47]}"(p.wk + h * DH * 2 + (size_t)(16 * w) * (HD * 2)),
                       "s"(lds + 2 * w * GS), "s"(p.ws + OFF_LAT), "s"(fl + (F_K + h) * 128)
                     : "v0", "v1", "v2", "v3", "v4", "v5", "v6", "v7", "v8", "v9", "v10", "v11", "v12", "v13", "v14", "v15", "v16", "v17", "v18", "v19", "v20", "v21", "v22", "v23", "v24", "v25", "v26", "v27", "v28", "v29", "v30", "v31", "v32", "v33", "v34", "v35", "v36", "v37", "v38", "v39", "v40", "v41", "v42", "v43", "v44", "v45", "v46", "v47", "v48", "v49", "v50", "v51", "v52", "v53", "v54", "v55", "v56", "v57", "v58", "v59", "v60", "v61", "v62", "v63", "v64", "v65", "v66", "v67", "v68", "v69", "v70", "v71", "v72", "v73", "v74", "v75", "v76", "v77", "v78", "v79", "v80", "v81", "v82", "v83", "v84", "v85", "v86", "v87", "v88", "v89", "v90", "v91", "v92", "v93", "v94", "v95", "v96", "v97", "v98", "v99", "v100", "v101", "v102", "v103", "v104", "v105", "v106", "v107", "v108", "v109", "v110", "v111", "v112", "v113", "v114", "v115", "v116", "v117", "v118", "v119", "v120", "v121", "v122", "v123", "v124", "v125", "v126", "v127", "v232", "v233", "s42", "s43", "scc", "memory");
        const u64 t = sync_tag(y.e);
        const bool rdy = __builtin_amdgcn_ballot_w64((y.r[0] & ~1u) != (u32)t || y.r[1] != (u32)(t >> 32) || y.g[0] != (u32)t) == 0 && uni((int)kfl) != -1;
        if (rdy) kxdma();
        sd = sync_end(y, p.ws, phi, w == 0);
        if (!rdy) { flag_wait(fl, F_K + h, 1); kxdma(); }
        kxp += NH * 12288;
    }
    const u32 sidx = (lane & 2) ? 0xEEEEu : 0x4444u;
    const u32 moff = lane * 16;
    u32 kxa = kxl - 16 + (n >> 3) * 6160 + (16 * g + 4 * (n & 3) + ((n >> 1) & 2)) * 16;
    const u64 mb0 = 0xffff0000ffff0000ull;
    const int tp = (3 * w + (w & 1)) >> 1, ts = 1 + 3 * (w >> 1);
    u32 qw0 = lds + SL_IMG + tp * 512 + lane * 8, qw1 = lds + SL_IMG + ts * 512 + lane * 8 + 4 * (w & 1);
    const u32 kw0 = lds + SL_IMG + 3072 + w * 288 + n * 16 + 4 * g;
    const u32 qr0 = lds + SL_IMG + (g >> 1) * 512 + w * 128 + 64 * (g & 1) + 8 * qq;
    const u32 kr0 = lds + SL_IMG + 3072 + (qq >> 1) * 288 + g * 64 + (qq & 1) * 16 + 8 * (pp & 1);
    const u32 xso = 32768 + (h >> 4) * 16384 + (h & 15) * 512 + (w >> 1) * 256 + (n >> 3) * 64 + (n & 4) * 32 + (w & 1) * 32 + (g & 1) * 16 + (n & 3) * 4 + (g >> 1) * 8192;
    const u32 pso = (n < 8 ? 20480 + (h >> 1) * 256 + (n & 2) * 16 + (h & 1) * 8 + (n & 4) : 18432 + (h >> 2) * 256 + (h & 1) * 8 + (h & 2) * 2) + w * 64 + (g & 1) * 16 + (g >> 1) * 8192;
    const u64 psm = 0x01aa01aa01aa01aaull;
    const u32 kmo = (u32)(((n >> 3) * 4 + g) * 128 + (n & 7) * 16);
    u32x16 ad = u32x16{moff, sidx, lfa, kxa, qw0, qw1, kw0, kr0 + 1152 * min(w, 2) + (pp >> 1) * 576, qr0, kr0, xso, pso, 0x3dd53b94u, kmo, kmo, kmo};
    asm volatile("" : "+{v[224:239]}"(ad));
    const char* memp = p.ws + mslot(sd, 0);
    char* kmp = p.ws + OFF_KM + h * 3072 + min(w, 2) * 1024;
    const float kscale = 0.07216878364870322f;
    int budget = 30000;
    Clock clk; clk.init();
    u32x32 wk[3];
    asm volatile("" : "={v[64:95]}"(wk[0]), "={v[96:127]}"(wk[1]), "={v[32:63]}"(wk[2]));
    const int kfo = (F_K + 32 + h) * 128;
    for (int s = 0; s < NSEG; ++s) {
        int tries = 0;
        u64 bad, tp8, en = ~0ull;
        u32 tt;
        int kf;
        while (true) {
            const u32 tgt = clk.sched(phi, 0, cfg.G, tries ? 2 : 0);
            asm volatile("s_cmp_lg_u32 %12, 0\n\t" "s_cbranch_scc1 .Lrt_s\n\t" "s_waitcnt vmcnt(0) lgkmcnt(0)\n\t" "s_barrier\n\t" "s_cmp_lg_u32 %13, 0\n\t" "s_cbranch_scc1 .Lsp_s\n\t" ".Lrt_s:\n\t"
                         "ds_read_b128 v[64:67], v226\n\t" "v_mov_b64 v[40:41], 0\n\t" "ds_read_b128 v[68:71], v226 offset:1024\n\t" "v_mov_b64 v[42:43], 0\n\t" "ds_read_b128 v[72:75], v226 offset:2048\n\t"
                         "v_mov_b64 v[44:45], 0\n\t" "ds_read_b128 v[76:79], v226 offset:3072\n\t" "v_mov_b64 v[46:47], 0\n\t" "ds_read_b128 v[80:83], v226 offset:4096\n\t" "v_mov_b64 v[48:49], 0\n\t"
                         "ds_read_b128 v[84:87], v226 offset:5120\n\t" "v_mov_b64 v[50:51], 0\n\t" "ds_read_b128 v[88:91], v226 offset:6144\n\t" "v_mov_b64 v[52:53], 0\n\t" "ds_read_b128 v[92:95], v226 offset:7168\n\t"
                         "v_mov_b64 v[54:55], 0\n\t" "ds_read_b128 v[96:99], v226 offset:8192\n\t" "v_mov_b64 v[56:57], 0\n\t" "ds_read_b128 v[100:103], v226 offset:9216\n\t" "v_mov_b64 v[58:59], 0\n\t"
                         "ds_read_b128 v[104:107], v226 offset:10240\n\t" "v_mov_b64 v[60:61], 0\n\t" "ds_read_b128 v[108:111], v226 offset:11264\n\t" "v_mov_b64 v[62:63], 0\n\t"
                         "ds_read_b128 v[112:115], v226 offset:12288\n\t" "ds_read_b128 v[116:119], v226 offset:13312\n\t" "ds_read_b128 v[120:123], v226 offset:14336\n\t" "ds_read_b128 v[124:127], v226 offset:15360\n\t"
                         ".Lsp_s:\n\t" "s_memtime vcc\n\t" "s_waitcnt lgkmcnt(0)\n\t" "s_sub_i32 s42, %4, vcc_lo\n\t" "s_cmp_gt_i32 s42, 80\n\t" "s_cbranch_scc1 .Lsp_s\n\t" "s_max_i32 s42, s42, 0\n\t"
                         "s_sub_i32 s42, 92, s42\n\t" "s_and_b32 s42, s42, -4\n\t" "s_getpc_b64 vcc\n\t" "s_add_u32 vcc_lo, vcc_lo, s42\n\t" "s_addc_u32 vcc_hi, vcc_hi, 0\n\t" "s_setpc_b64 vcc\n\t" "s_nop 0\n\t" "s_nop 0\n\t"
                         "s_nop 0\n\t" "s_nop 0\n\t" "s_nop 0\n\t" "s_nop 0\n\t" "s_nop 0\n\t" "s_nop 0\n\t" "s_nop 0\n\t" "s_nop 0\n\t" "s_nop 0\n\t" "s_nop 0\n\t" "s_nop 0\n\t" "s_nop 0\n\t" "s_nop 0\n\t" "s_nop 0\n\t"
                         "s_nop 0\n\t" "s_nop 0\n\t" "s_nop 0\n\t" "s_nop 0\n\t" "global_load_dwordx4 v[0:3], v224, %5 offset:-4096 sc1\n\t" "s_nop 10\n\t" "global_load_dwordx4 v[4:7], v224, %5 offset:-3072 sc1\n\t"
                         "s_nop 10\n\t" "global_load_dwordx4 v[8:11], v224, %5 offset:-2048 sc1\n\t" "s_nop 10\n\t" "global_load_dwordx4 v[12:15], v224, %5 offset:-1024 sc1\n\t" "s_nop 10\n\t"
                         "global_load_dwordx4 v[16:19], v224, %5 sc1\n\t" "s_nop 10\n\t" "global_load_dwordx4 v[20:23], v224, %5 offset:1024 sc1\n\t" "s_nop 10\n\t" "global_load_dwordx4 v[24:27], v224, %5 offset:2048 sc1\n\t"
                         "s_nop 10\n\t" "global_load_dwordx4 v[28:31], v224, %5 offset:3072 sc1\n\t" "s_nop 10\n\t" "global_load_dwordx4 v[32:35], v224, %6 offset:-4096 sc1\n\t" "s_nop 10\n\t"
                         "global_load_dwordx4 v[36:39], v224, %6 offset:-3072 sc1\n\t" "s_waitcnt vmcnt(9)\n\t" "v_smfmac_f32_16x16x64_bf16 v[40:43], v[0:3], v[128:135], v225\n\t"
                         "v_smfmac_f32_16x16x64_bf16 v[44:47], v[0:3], v[64:71], v225\n\t" "ds_read_b128 v[64:67], v226 offset:16384\n\t" "v_smfmac_f32_16x16x64_bf16 v[48:51], v[0:3], v[136:143], v225\n\t"
                         "ds_read_b128 v[68:71], v226 offset:17408\n\t" "v_smfmac_f32_16x16x64_bf16 v[52:55], v[0:3], v[72:79], v225\n\t" "ds_read_b128 v[72:75], v226 offset:18432\n\t" "s_waitcnt vmcnt(8)\n\t"
                         "v_smfmac_f32_16x16x64_bf16 v[40:43], v[4:7], v[144:151], v225\n\t" "ds_read_b128 v[76:79], v226 offset:19456\n\t" "v_smfmac_f32_16x16x64_bf16 v[44:47], v[4:7], v[80:87], v225\n\t"
                         "ds_read_b128 v[80:83], v226 offset:20480\n\t" "v_smfmac_f32_16x16x64_bf16 v[48:51], v[4:7], v[152:159], v225\n\t" "ds_read_b128 v[84:87], v226 offset:21504\n\t"
                         "v_smfmac_f32_16x16x64_bf16 v[52:55], v[4:7], v[88:95], v225\n\t" "ds_read_b128 v[88:91], v226 offset:22528\n\t" "s_waitcnt vmcnt(7)\n\t"
                         "v_smfmac_f32_16x16x64_bf16 v[40:43], v[8:11], v[160:167], v225\n\t" "ds_read_b128 v[92:95], v226 offset:23552\n\t" "v_max_u32 v238, v0, v4\n\t"
                         "v_smfmac_f32_16x16x64_bf16 v[44:47], v[8:11], v[96:103], v225\n\t" "ds_read_b128 v[96:99], v226 offset:24576\n\t" "v_cmp_eq_u32 vcc, -1, v238\n\t"
                         "v_smfmac_f32_16x16x64_bf16 v[48:51], v[8:11], v[168:175], v225\n\t" "ds_read_b128 v[100:103], v226 offset:25600\n\t" "v_smfmac_f32_16x16x64_bf16 v[52:55], v[8:11], v[104:111], v225\n\t"
                         "s_and_b64 vcc, vcc, %11\n\t" "s_cbranch_scc1 .Lsk_s\n\t" "ds_read_b128 v[104:107], v226 offset:26624\n\t" "s_waitcnt vmcnt(6)\n\t" "v_smfmac_f32_16x16x64_bf16 v[40:43], v[12:15], v[176:183], v225\n\t"
                         "ds_read_b128 v[108:111], v226 offset:27648\n\t" "v_smfmac_f32_16x16x64_bf16 v[44:47], v[12:15], v[112:119], v225\n\t" "ds_read_b128 v[112:115], v226 offset:28672\n\t"
                         "v_smfmac_f32_16x16x64_bf16 v[48:51], v[12:15], v[184:191], v225\n\t" "ds_read_b128 v[116:119], v226 offset:29696\n\t" "v_smfmac_f32_16x16x64_bf16 v[52:55], v[12:15], v[120:127], v225\n\t"
                         "ds_read_b128 v[120:123], v226 offset:30720\n\t" "s_waitcnt vmcnt(5)\n\t" "v_smfmac_f32_16x16x64_bf16 v[40:43], v[16:19], v[192:199], v225\n\t" "ds_read_b128 v[124:127], v226 offset:31744\n\t"
                         "s_waitcnt lgkmcnt(14)\n\t" "v_smfmac_f32_16x16x64_bf16 v[44:47], v[16:19], v[64:71], v225\n\t" "ds_read_b128 v[64:67], v227\n\t" "v_smfmac_f32_16x16x64_bf16 v[48:51], v[16:19], v[200:207], v225\n\t"
                         "ds_read_b128 v[68:71], v227 offset:1024\n\t" "s_waitcnt lgkmcnt(14)\n\t" "v_smfmac_f32_16x16x64_bf16 v[52:55], v[16:19], v[72:79], v225\n\t" "ds_read_b128 v[72:75], v227 offset:16\n\t"
                         "s_waitcnt vmcnt(4)\n\t" "v_smfmac_f32_16x16x64_bf16 v[40:43], v[20:23], v[208:215], v225\n\t" "ds_read_b128 v[76:79], v227 offset:1040\n\t" "s_waitcnt lgkmcnt(14)\n\t"
                         "v_smfmac_f32_16x16x64_bf16 v[44:47], v[20:23], v[80:87], v225\n\t" "ds_read_b128 v[80:83], v227 offset:2048\n\t" "v_smfmac_f32_16x16x64_bf16 v[48:51], v[20:23], v[216:223], v225\n\t"
                         "ds_read_b128 v[84:87], v227 offset:3072\n\t" "s_waitcnt lgkmcnt(14)\n\t" "v_smfmac_f32_16x16x64_bf16 v[52:55], v[20:23], v[88:95], v225\n\t" "ds_read_b128 v[88:91], v227 offset:2064\n\t"
                         "s_waitcnt vmcnt(3)\n\t" "v_smfmac_f32_16x16x64_bf16 v[40:43], v[24:27], a[0:7], v225\n\t" "ds_read_b128 v[92:95], v227 offset:3088\n\t" "s_waitcnt lgkmcnt(14)\n\t"
                         "v_smfmac_f32_16x16x64_bf16 v[44:47], v[24:27], v[96:103], v225\n\t" "ds_read_b128 v[96:99], v227 offset:4096\n\t" "v_smfmac_f32_16x16x64_bf16 v[48:51], v[24:27], a[8:15], v225\n\t"
                         "ds_read_b128 v[100:103], v227 offset:5120\n\t" "s_waitcnt lgkmcnt(14)\n\t" "v_smfmac_f32_16x16x64_bf16 v[52:55], v[24:27], v[104:111], v225\n\t" "ds_read_b128 v[104:107], v227 offset:4112\n\t"
                         "s_waitcnt vmcnt(2)\n\t" "v_smfmac_f32_16x16x64_bf16 v[40:43], v[28:31], a[16:23], v225\n\t" "ds_read_b128 v[108:111], v227 offset:5136\n\t" "s_waitcnt lgkmcnt(14)\n\t"
                         "v_smfmac_f32_16x16x64_bf16 v[44:47], v[28:31], v[112:119], v225\n\t" "v_smfmac_f32_16x16x64_bf16 v[48:51], v[28:31], a[24:31], v225\n\t" "s_waitcnt lgkmcnt(12)\n\t"
                         "v_smfmac_f32_16x16x64_bf16 v[52:55], v[28:31], v[120:127], v225\n\t" "s_waitcnt vmcnt(1)\n\t" "v_smfmac_f32_16x16x64_bf16 v[40:43], v[32:35], a[32:39], v225\n\t" "v_mov_b64 v[112:113], 0\n\t"
                         "v_smfmac_f32_16x16x64_bf16 v[44:47], v[32:35], a[40:47], v225\n\t" "v_mov_b64 v[114:115], 0\n\t" "v_smfmac_f32_16x16x64_bf16 v[48:51], v[32:35], a[48:55], v225\n\t" "v_mov_b64 v[116:117], 0\n\t"
                         "v_smfmac_f32_16x16x64_bf16 v[52:55], v[32:35], a[56:63], v225\n\t" "v_mov_b64 v[118:119], 0\n\t" "s_waitcnt vmcnt(0)\n\t" "v_smfmac_f32_16x16x64_bf16 v[40:43], v[36:39], a[64:71], v225\n\t"
                         "v_mov_b64 v[120:121], 0\n\t" "v_smfmac_f32_16x16x64_bf16 v[44:47], v[36:39], a[72:79], v225\n\t" "v_mov_b64 v[122:123], 0\n\t" "v_smfmac_f32_16x16x64_bf16 v[48:51], v[36:39], a[80:87], v225\n\t"
                         "v_mov_b64 v[124:125], 0\n\t" "v_smfmac_f32_16x16x64_bf16 v[52:55], v[36:39], a[88:95], v225\n\t" "v_add_f32 v40, v40, v42\n\t" "v_add_f32 v41, v41, v43\n\t"
                         "v_smfmac_f32_16x16x64_bf16 v[56:59], v[0:3], a[96:103], v225\n\t" "v_cmp_u_f32 vcc, v40, v41\n\t" "v_smfmac_f32_16x16x64_bf16 v[56:59], v[4:7], a[104:111], v225\n\t"
                         "v_cvt_pk_bf16_f32 v42, v40, v41\n\t" "v_add_f32 v44, v44, v46\n\t" "v_smfmac_f32_16x16x64_bf16 v[56:59], v[8:11], a[112:119], v225\n\t" "s_and_b64 vcc, vcc, %11\n\t" "s_cbranch_scc1 .Lsk_s\n\t"
                         "v_smfmac_f32_16x16x64_bf16 v[56:59], v[12:15], a[120:127], v225\n\t" "v_add_f32 v45, v45, v47\n\t" "v_cvt_pk_bf16_f32 v43, v44, v45\n\t"
                         "v_smfmac_f32_16x16x64_bf16 v[56:59], v[16:19], a[128:135], v225\n\t" "ds_write_b64 v228, v[42:43]\n\t" "v_add_f32 v48, v48, v50\n\t"
                         "v_smfmac_f32_16x16x64_bf16 v[56:59], v[20:23], a[136:143], v225\n\t" "v_add_f32 v49, v49, v51\n\t" "v_cvt_pk_bf16_f32 v46, v48, v49\n\t"
                         "v_smfmac_f32_16x16x64_bf16 v[56:59], v[24:27], a[144:151], v225\n\t" "ds_write_b32 v229, v46\n\t" "v_add_f32 v52, v52, v54\n\t" "v_smfmac_f32_16x16x64_bf16 v[56:59], v[28:31], a[152:159], v225\n\t"
                         "v_add_f32 v53, v53, v55\n\t" "v_cvt_pk_bf16_f32 v54, v52, v53\n\t" "v_smfmac_f32_16x16x64_bf16 v[56:59], v[32:35], a[160:167], v225\n\t" "ds_write_b32 v230, v54\n\t"
                         "v_smfmac_f32_16x16x64_bf16 v[56:59], v[36:39], a[168:175], v225\n\t" "v_smfmac_f32_16x16x64_bf16 v[60:63], v[0:3], a[176:183], v225\n\t"
                         "v_smfmac_f32_16x16x64_bf16 v[60:63], v[4:7], a[184:191], v225\n\t" "v_add_f32 v56, v56, v58\n\t" "v_smfmac_f32_16x16x64_bf16 v[60:63], v[8:11], a[192:199], v225\n\t" "s_waitcnt lgkmcnt(0)\n\t"
                         "s_barrier\n\t" "v_smfmac_f32_16x16x64_bf16 v[60:63], v[12:15], a[200:207], v225\n\t" "ds_read_b64_tr_b16 v[40:41], v232\n\t" "v_add_f32 v57, v57, v59\n\t"
                         "v_smfmac_f32_16x16x64_bf16 v[60:63], v[16:19], a[208:215], v225\n\t" "ds_read_b64_tr_b16 v[42:43], v232 offset:32\n\t" "v_cvt_pk_bf16_f32 v58, v56, v57\n\t"
                         "v_smfmac_f32_16x16x64_bf16 v[60:63], v[20:23], a[216:223], v225\n\t" "ds_write_b32 v230, v58 offset:1152\n\t" "v_smfmac_f32_16x16x64_bf16 v[60:63], v[24:27], a[224:231], v225\n\t"
                         "ds_read_b64_tr_b16 v[44:45], v232 offset:1024\n\t" "ds_read_b64_tr_b16 v[46:47], v232 offset:1056\n\t" "v_smfmac_f32_16x16x64_bf16 v[60:63], v[28:31], a[232:239], v225\n\t"
                         "ds_read_b64_tr_b16 v[48:49], v232 offset:2048\n\t" "ds_read_b64_tr_b16 v[50:51], v232 offset:2080\n\t" "v_smfmac_f32_16x16x64_bf16 v[60:63], v[32:35], a[240:247], v225\n\t"
                         "ds_read_b64_tr_b16 v[0:1], v233\n\t" "ds_read_b64_tr_b16 v[2:3], v233 offset:32\n\t" "v_smfmac_f32_16x16x64_bf16 v[60:63], v[36:39], a[248:255], v225\n\t"
                         "ds_read_b64_tr_b16 v[4:5], v233 offset:576\n\t" "ds_read_b64_tr_b16 v[6:7], v233 offset:608\n\t" "s_waitcnt lgkmcnt(9)\n\t" "v_smfmac_f32_16x16x64_bf16 v[112:115], v[40:43], v[64:71], v225\n\t"
                         "s_waitcnt lgkmcnt(8)\n\t" "s_barrier\n\t" "v_smfmac_f32_16x16x64_bf16 v[116:119], v[40:43], v[72:79], v225\n\t" "ds_read_b64_tr_b16 v[8:9], v233 offset:1152\n\t" "s_waitcnt lgkmcnt(7)\n\t"
                         "v_add_f32 v60, v60, v62\n\t" "v_add_f32 v61, v61, v63\n\t" "v_smfmac_f32_16x16x64_bf16 v[112:115], v[44:47], v[80:87], v225\n\t" "ds_read_b64_tr_b16 v[10:11], v233 offset:1184\n\t"
                         "v_cvt_pk_bf16_f32 v62, v60, v61\n\t" "v_smfmac_f32_16x16x64_bf16 v[116:119], v[44:47], v[88:95], v225\n\t" "ds_write_b32 v230, v62 offset:2304\n\t" "s_waitcnt lgkmcnt(7)\n\t"
                         "v_smfmac_f32_16x16x64_bf16 v[112:115], v[48:51], v[96:103], v225\n\t" "ds_read_b64_tr_b16 v[12:13], v233 offset:1728\n\t" "v_smfmac_f32_16x16x64_bf16 v[116:119], v[48:51], v[104:111], v225\n\t"
                         "ds_read_b64_tr_b16 v[14:15], v233 offset:1760\n\t" "s_waitcnt lgkmcnt(5)\n\t" "v_smfmac_f32_16x16x64_bf16 v[120:123], v[40:43], v[0:7], v225\n\t" "v_cndmask_b32 v24, v112, v113, %8\n\t"
                         "v_cndmask_b32 v25, v114, v115, %8\n\t" "v_add_f32 v24, v24, v25\n\t" "v_pk_add_f32 v[26:27], v[116:117], v[118:119]\n\t" "v_cndmask_b32 v25, v26, v27, %8\n\t" "v_max_f32 v26, v24, v25\n\t"
                         "s_waitcnt lgkmcnt(2)\n\t" "s_barrier\n\t" "ds_read_b64_tr_b16 v[16:17], v233 offset:2304\n\t" "v_max_f32_dpp v27, v26, v26 row_ror:4 row_mask:0xf bank_mask:0xf\n\t"
                         "ds_read_b64_tr_b16 v[18:19], v233 offset:2336\n\t" "v_max_f32_dpp v27, v26, v27 row_ror:8 row_mask:0xf bank_mask:0xf\n\t" "v_max_f32_dpp v27, v26, v27 row_ror:12 row_mask:0xf bank_mask:0xf\n\t"
                         "ds_read_b64_tr_b16 v[20:21], v233 offset:2880\n\t" "ds_read_b64_tr_b16 v[22:23], v233 offset:2912\n\t" "v_max_f32_dpp v30, v27, v27 row_ror:1 row_mask:0xf bank_mask:0xf\n\t"
                         "v_max_f32_dpp v30, v27, v30 row_ror:2 row_mask:0xf bank_mask:0xf\n\t" "v_max_f32_dpp v30, v27, v30 row_ror:3 row_mask:0xf bank_mask:0xf\n\t" "s_waitcnt lgkmcnt(4)\n\t"
                         "v_pk_add_f32 v[24:25], v[24:25], v[30:31] op_sel_hi:[1,0] neg_lo:[0,1] neg_hi:[0,1]\n\t" "v_pk_mul_f32 v[24:25], v[24:25], v[236:237] op_sel_hi:[1,0]\n\t" "v_exp_f32 v24, v24\n\t"
                         "v_exp_f32 v25, v25\n\t" "v_mul_f32 v31, %10, v30\n\t" "v_cvt_pk_bf16_f32 v28, |v24|, |v25|\n\t" "v_smfmac_f32_16x16x64_bf16 v[120:123], v[44:47], v[8:15], v225\n\t"
                         "global_store_dword v234, v28, %7 offset:-4096 sc1\n\t" "s_waitcnt lgkmcnt(0)\n\t" "v_add_f32 v26, v24, v25\n\t" "v_smfmac_f32_16x16x64_bf16 v[120:123], v[48:51], v[16:23], v225\n\t"
                         "s_nop 0\n\t" "v_add_f32_dpp v27, v26, v26 row_ror:4 row_mask:0xf bank_mask:0xf\n\t" "v_add_f32_dpp v27, v26, v27 row_ror:8 row_mask:0xf bank_mask:0xf\n\t"
                         "v_add_f32_dpp v27, v26, v27 row_ror:12 row_mask:0xf bank_mask:0xf\n\t" "s_nop 1\n\t" "v_add_f32_dpp v29, v27, v27 row_ror:1 row_mask:0xf bank_mask:0xf\n\t"
                         "v_pk_add_f32 v[24:25], v[120:121], v[122:123]\n\t" "v_cndmask_b32 v24, v24, v25, %8\n\t" "v_max_f32 v26, v24, v30\n\t" "v_add_f32_dpp v29, v27, v29 row_ror:2 row_mask:0xf bank_mask:0xf\n\t"
                         "v_add_f32_dpp v29, v27, v29 row_ror:3 row_mask:0xf bank_mask:0xf\n\t" "v_max_f32_dpp v27, v26, v26 row_ror:1 row_mask:0xf bank_mask:0xf\n\t"
                         "v_max_f32_dpp v27, v26, v27 row_ror:2 row_mask:0xf bank_mask:0xf\n\t" "v_max_f32_dpp v27, v26, v27 row_ror:3 row_mask:0xf bank_mask:0xf\n\t" "v_mul_f32 v24, %10, v24\n\t"
                         "v_mul_f32 v29, 0x3e000000, v29\n\t" "v_max_f32_dpp v26, v27, v27 row_ror:4 row_mask:0xf bank_mask:0xf\n\t" "v_fma_f32 v24, -v26, %10, v24\n\t" "v_fma_f32 v25, -v26, %10, v31\n\t"
                         "v_exp_f32 v24, v24\n\t" "v_exp_f32 v25, v25\n\t" "s_nop 0\n\t" "v_fma_f32 v26, v25, v29, v24\n\t" "v_mov_b32_dpp v124, v24 quad_perm:[0,1,2,3] row_mask:0xf bank_mask:0x3\n\t"
                         "v_mov_b32_dpp v125, v24 quad_perm:[1,0,3,2] row_mask:0xf bank_mask:0x3\n\t" "v_add_f32_dpp v27, v26, v26 row_ror:1 row_mask:0xf bank_mask:0xf\n\t"
                         "v_add_f32_dpp v27, v26, v27 row_ror:2 row_mask:0xf bank_mask:0xf\n\t" "v_add_f32_dpp v27, v26, v27 row_ror:3 row_mask:0xf bank_mask:0xf\n\t"
                         "v_mov_b32_dpp v124, v25 quad_perm:[0,1,2,3] row_mask:0x5 bank_mask:0x4\n\t" "v_mov_b32_dpp v125, v25 quad_perm:[0,1,2,3] row_mask:0xa bank_mask:0x4\n\t"
                         "v_add_f32_dpp v26, v27, v27 row_ror:4 row_mask:0xf bank_mask:0xf\n\t" "v_rcp_f32 v26, v26\n\t" "s_mov_b64 exec, %9\n\t" "v_pk_mul_f32 v[124:125], v[124:125], v[26:27] op_sel_hi:[1,0]\n\t"
                         "v_cvt_pk_bf16_f32 v24, |v124|, |v125|\n\t" "global_store_dword v235, v24, %7 offset:-4096 sc1\n\t" "s_mov_b64 exec, -1\n\t" "s_memrealtime %1\n\t" "s_load_dword %2, %14, %13 offset:%15\n\t"
                         "ds_read_b64_tr_b16 v[36:37], v231\n\t" "ds_read_b64_tr_b16 v[38:39], v231 offset:32\n\t" "v_readfirstlane_b32 %3, v237\n\t" "s_mov_b32 m0, %17\n\t" "v_mov_b64 v[40:41], 0\n\t"
                         "v_mov_b64 v[42:43], 0\n\t" "v_mov_b64 v[44:45], 0\n\t" "v_mov_b64 v[46:47], 0\n\t" "v_mov_b64 v[48:49], 0\n\t" "v_mov_b64 v[50:51], 0\n\t" "v_mov_b64 v[52:53], 0\n\t" "v_mov_b64 v[54:55], 0\n\t"
                         "v_mov_b64 v[56:57], 0\n\t" "v_mov_b64 v[58:59], 0\n\t" "v_mov_b64 v[60:61], 0\n\t" "v_mov_b64 v[62:63], 0\n\t" "v_mov_b32 v237, %18\n\t" "s_waitcnt lgkmcnt(0)\n\t" "ds_read_b128 v[64:67], v226\n\t"
                         "ds_read_b128 v[68:71], v226 offset:1024\n\t" "ds_read_b128 v[72:75], v226 offset:2048\n\t" "ds_read_b128 v[76:79], v226 offset:3072\n\t" "ds_read_b128 v[80:83], v226 offset:4096\n\t"
                         "ds_read_b128 v[84:87], v226 offset:5120\n\t" "s_cmp_eq_u32 %3, -1\n\t" "s_cbranch_scc1 .Lnk_s\n\t" "global_load_lds_dwordx4 v224, %16 sc1\n\t" "global_load_lds_dwordx4 v224, %16 offset:1024 sc1\n\t"
                         "global_load_lds_dwordx4 v224, %16 offset:2048 sc1\n\t" ".Lnk_s:\n\t" "global_store_dwordx4 v239, v[36:39], %21 sc1\n\t" "s_and_b64 exec, %19, 1\n\t" "global_load_dword v237, v237, %20 sc1\n\t"
                         "s_mov_b64 exec, -1\n\t" "ds_read_b128 v[88:91], v226 offset:6144\n\t" "ds_read_b128 v[92:95], v226 offset:7168\n\t" "ds_read_b128 v[96:99], v226 offset:8192\n\t"
                         "ds_read_b128 v[100:103], v226 offset:9216\n\t" "ds_read_b128 v[104:107], v226 offset:10240\n\t" "ds_read_b128 v[108:111], v226 offset:11264\n\t" "ds_read_b128 v[112:115], v226 offset:12288\n\t"
                         "ds_read_b128 v[116:119], v226 offset:13312\n\t" "ds_read_b128 v[120:123], v226 offset:14336\n\t" "ds_read_b128 v[124:127], v226 offset:15360\n\t" ".Lsk_s:\n\t" "s_mov_b64 %0, vcc\n\t"
                         : "=&s"(bad), "=&s"(tp8), "=&s"(tt), "=&s"(kf)
                         : "s"(uni((int)tgt)), "s"(memp), "s"(memp + 16384), "s"((u64)memp & ~8192ull), "s"(mb0), "s"(psm), "s"(kscale * 1.4426950408889634f), "s"(en), "s"(tries), "s"(4 * s),
                           "s"(__builtin_amdgcn_kernarg_segment_ptr()), "n"(TT_OFF), "s"(kxp), "s"(kxw), "s"(kfo + (s < 6 ? 0 : (s + 2) >> 3) * 4096), "s"(0x0040404040404044ull >> s), "s"(fl), "s"(kmp),
                           "{v[224:239]}"(ad), "{v[64:95]}"(wk[0]), "{v[96:127]}"(wk[1]), "{v[32:63]}"(wk[2]),
                           "{a[0:31]}"(wa[0]), "{a[32:63]}"(wa[1]), "{a[64:95]}"(wa[2]), "{a[96:127]}"(wa[3]), "{a[128:159]}"(wa[4]), "{a[160:191]}"(wa[5]), "{a[192:223]}"(wa[6]), "{a[224:255]}"(wa[7]), "{v[128:159]}"(wv[0]), "{v[160:191]}"(wv[1]), "{v[192:223]}"(wv[2])
                         : "v0", "v1", "v2", "v3", "v4", "v5", "v6", "v7", "v8", "v9", "v10", "v11", "v12", "v13", "v14", "v15", "v16", "v17", "v18", "v19", "v20", "v21", "v22", "v23", "v24", "v25", "v26", "v27", "v28", "v29", "v30", "v31", "vcc", "scc", "s42", "memory");
            if (__builtin_expect(bad == 0, 1)) break;
            waitvm<0>();
            if (((++tries & 15) == 0 && !unwritten(memp, ad[0])) || --budget < 0) en = 0;
            phi += (u32)cfg.G;
        }
        if (__builtin_expect(kf == -1, 0)) { flag_wait(fl, F_K + pblk(s + 1) * 32 + h, 1); kxdma(); }
        kxp += s < NSEG - 2 ? NH * 12288 : 0;
        clk.calib();
        if (__builtin_expect(s >= NSEG - 2, 0)) {
            waitvm<0>();
            barrier();
            if (w == 3) flag_add(fl, F_PROG + 8 * s + (h & 7), 1);
        }
        follow(phi, (int)(tt >> 16) - VISS, (u32)tp8, cfg.G);
        phi += tt & 0xffffu;
        memp = p.ws + mslot(sd, s + 1); kmp += NH * 3072;
    }
    if (h == 0 && w == 0) {
        flag_wait(fl, F_ACK, 256);
        Sync y;
        sync_ld(y, p.ws);
        asm volatile("s_waitcnt vmcnt(0)" : "+v"(y.r), "+v"(y.g), "+v"(y.e) : : "memory");
        const u64 t = ((u64)y.e[1] << 32 | y.e[0]) + 1;
        if (lanex() == 0) gst8(p.ws + OFF_LAT, 1024, u32x2{(u32)t, (u32)(t >> 32)});
    }
    asm volatile(".Lkm_last:\n\ts_waitcnt vmcnt(0)" : : : "memory");
}

DI void proj_role(const Ptrs& p, const ChainCfg cfg, int h, u32 lds, int w) {
    u32x32 wa[8], wv[4];
    u32 phi;
    int sd;
    {
        const int lane = lanex(), tid = 64 * w + lane;
        const int g = lane >> 4, qq = (lane >> 2) & 3, pp = lane & 3;
        constexpr int GS = 3104;
        auto dvo = [&](int i) { const int ix = i * 64 + lane; return (u32)((ix / 24) * (HD * 2) + (ix % 24) * 16); };
        auto ovo = [&](int i) { const int u = i * 64 + lane, r = u / 80; return (u32)(r * 1280 + (((u % 80) ^ (2 * (r ^ (w & 2)))) << 4)); };
        const u32 tra = lds + (2 * (g >> 1) + (qq >> 1)) * GS + (4 * (g & 1) + (qq & 1)) * 384 + 96 * w + 32 * (w & 1) + 8 * pp;
        const u32 tro = (4 * g + (qq & 1) + 16 * (qq >> 1)) * 1280 + 8 * pp + ((u32)(2 * w ^ 2 * ((4 * g + qq) & 7)) << 4);
        u32x16 pa = u32x16{dvo(0), dvo(1), dvo(2), tra, tra - 96 * (w & 1), tro, lds + (u32)((w & 1) * 24576 + (w >> 1) * 99328 + lane * 16), (u32)(lane & 31) * 8,
                           ovo(0), ovo(1), ovo(2), ovo(3), ovo(4), tro, tro, tro};
        Sync y;
        asm volatile("s_nop 4\n\t" "s_add_u32 s42, s44, 0x0\n\t" "s_addc_u32 s43, s45, 0\n\t" "s_add_u32 m0, %18, 0x0\n\t" "s_nop 0\n\t" "global_load_lds_dwordx4 v224, s[42:43]\n\t" "s_add_u32 m0, %18, 0x400\n\t"
                     "s_nop 0\n\t" "global_load_lds_dwordx4 v225, s[42:43]\n\t" "s_add_u32 m0, %18, 0x800\n\t" "s_nop 0\n\t" "global_load_lds_dwordx4 v226, s[42:43]\n\t" "s_add_u32 s42, s44, 0x18000\n\t"
                     "s_addc_u32 s43, s45, 0\n\t" "s_add_u32 m0, %18, 0xc20\n\t" "s_nop 0\n\t" "global_load_lds_dwordx4 v224, s[42:43]\n\t" "s_add_u32 m0, %18, 0x1020\n\t" "s_nop 0\n\t"
                     "global_load_lds_dwordx4 v225, s[42:43]\n\t" "s_add_u32 m0, %18, 0x1420\n\t" "s_nop 0\n\t" "global_load_lds_dwordx4 v226, s[42:43]\n\t" "s_add_u32 s42, s44, 0xc0000\n\t" "s_addc_u32 s43, s45, 0\n\t"
                     "s_add_u32 m0, %18, 0x6100\n\t" "s_nop 0\n\t" "global_load_lds_dwordx4 v224, s[42:43]\n\t" "s_add_u32 m0, %18, 0x6500\n\t" "s_nop 0\n\t" "global_load_lds_dwordx4 v225, s[42:43]\n\t"
                     "s_add_u32 m0, %18, 0x6900\n\t" "s_nop 0\n\t" "global_load_lds_dwordx4 v226, s[42:43]\n\t" "s_add_u32 s42, s44, 0xd8000\n\t" "s_addc_u32 s43, s45, 0\n\t" "s_add_u32 m0, %18, 0x6d20\n\t" "s_nop 0\n\t"
                     "global_load_lds_dwordx4 v224, s[42:43]\n\t" "s_add_u32 m0, %18, 0x7120\n\t" "s_nop 0\n\t" "global_load_lds_dwordx4 v225, s[42:43]\n\t" "s_add_u32 m0, %18, 0x7520\n\t" "s_nop 0\n\t"
                     "global_load_lds_dwordx4 v226, s[42:43]\n\t" "s_add_u32 s42, s44, 0x180000\n\t" "s_addc_u32 s43, s45, 0\n\t" "s_add_u32 m0, %18, 0xc200\n\t" "s_nop 0\n\t" "global_load_lds_dwordx4 v224, s[42:43]\n\t"
                     "s_add_u32 m0, %18, 0xc600\n\t" "s_nop 0\n\t" "global_load_lds_dwordx4 v225, s[42:43]\n\t" "s_add_u32 m0, %18, 0xca00\n\t" "s_nop 0\n\t" "global_load_lds_dwordx4 v226, s[42:43]\n\t"
                     "s_add_u32 s42, s44, 0x198000\n\t" "s_addc_u32 s43, s45, 0\n\t" "s_add_u32 m0, %18, 0xce20\n\t" "s_nop 0\n\t" "global_load_lds_dwordx4 v224, s[42:43]\n\t" "s_add_u32 m0, %18, 0xd220\n\t" "s_nop 0\n\t"
                     "global_load_lds_dwordx4 v225, s[42:43]\n\t" "s_add_u32 m0, %18, 0xd620\n\t" "s_nop 0\n\t" "global_load_lds_dwordx4 v226, s[42:43]\n\t" "s_add_u32 s42, s44, 0x240000\n\t" "s_addc_u32 s43, s45, 0\n\t"
                     "s_add_u32 m0, %18, 0x12300\n\t" "s_nop 0\n\t" "global_load_lds_dwordx4 v224, s[42:43]\n\t" "s_add_u32 m0, %18, 0x12700\n\t" "s_nop 0\n\t" "global_load_lds_dwordx4 v225, s[42:43]\n\t"
                     "s_add_u32 m0, %18, 0x12b00\n\t" "s_nop 0\n\t" "global_load_lds_dwordx4 v226, s[42:43]\n\t" "s_add_u32 s42, s44, 0x258000\n\t" "s_addc_u32 s43, s45, 0\n\t" "s_add_u32 m0, %18, 0x12f20\n\t"
                     "s_nop 0\n\t" "global_load_lds_dwordx4 v224, s[42:43]\n\t" "s_add_u32 m0, %18, 0x13320\n\t" "s_nop 0\n\t" "global_load_lds_dwordx4 v225, s[42:43]\n\t" "s_add_u32 m0, %18, 0x13720\n\t" "s_nop 0\n\t"
                     "global_load_lds_dwordx4 v226, s[42:43]\n\t" "s_waitcnt vmcnt(12) lgkmcnt(0)\n\t" "s_barrier\n\t" "s_add_u32 s42, s44, 0x300000\n\t" "s_addc_u32 s43, s45, 0\n\t" "s_add_u32 m0, %18, 0x18400\n\t"
                     "s_nop 0\n\t" "global_load_lds_dwordx4 v224, s[42:43]\n\t" "s_add_u32 m0, %18, 0x18800\n\t" "s_nop 0\n\t" "global_load_lds_dwordx4 v225, s[42:43]\n\t" "s_add_u32 m0, %18, 0x18c00\n\t" "s_nop 0\n\t"
                     "global_load_lds_dwordx4 v226, s[42:43]\n\t" "s_add_u32 s42, s44, 0x318000\n\t" "s_addc_u32 s43, s45, 0\n\t" "s_add_u32 m0, %18, 0x19020\n\t" "s_nop 0\n\t" "global_load_lds_dwordx4 v224, s[42:43]\n\t"
                     "s_add_u32 m0, %18, 0x19420\n\t" "s_nop 0\n\t" "global_load_lds_dwordx4 v225, s[42:43]\n\t" "s_add_u32 m0, %18, 0x19820\n\t" "s_nop 0\n\t" "global_load_lds_dwordx4 v226, s[42:43]\n\t"
                     "s_add_u32 s42, s44, 0x3c0000\n\t" "s_addc_u32 s43, s45, 0\n\t" "s_add_u32 m0, %18, 0x1e500\n\t" "s_nop 0\n\t" "global_load_lds_dwordx4 v224, s[42:43]\n\t" "s_add_u32 m0, %18, 0x1e900\n\t"
                     "s_nop 0\n\t" "global_load_lds_dwordx4 v225, s[42:43]\n\t" "s_add_u32 m0, %18, 0x1ed00\n\t" "s_nop 0\n\t" "global_load_lds_dwordx4 v226, s[42:43]\n\t" "s_add_u32 s42, s44, 0x3d8000\n\t"
                     "s_addc_u32 s43, s45, 0\n\t" "s_add_u32 m0, %18, 0x1f120\n\t" "s_nop 0\n\t" "global_load_lds_dwordx4 v224, s[42:43]\n\t" "s_add_u32 m0, %18, 0x1f520\n\t" "s_nop 0\n\t"
                     "global_load_lds_dwordx4 v225, s[42:43]\n\t" "s_add_u32 m0, %18, 0x1f920\n\t" "s_nop 0\n\t" "global_load_lds_dwordx4 v226, s[42:43]\n\t" "v_add_u32 v237, 0x0, v227\n\t" "v_add_u32 v238, 0x0, v228\n\t"
                     "ds_read_b64_tr_b16 a[0:1], v237\n\t" "ds_read_b64_tr_b16 a[2:3], v237 offset:768\n\t" "ds_read_b64_tr_b16 a[4:5], v237 offset:12416\n\t" "ds_read_b64_tr_b16 a[6:7], v237 offset:13184\n\t"
                     "ds_read_b64_tr_b16 v[0:1], v237 offset:32\n\t" "ds_read_b64_tr_b16 v[2:3], v237 offset:800\n\t" "ds_read_b64_tr_b16 v[4:5], v237 offset:12448\n\t" "ds_read_b64_tr_b16 v[6:7], v237 offset:13216\n\t"
                     "ds_read_b64_tr_b16 a[8:9], v238 offset:64\n\t" "ds_read_b64_tr_b16 a[10:11], v238 offset:832\n\t" "ds_read_b64_tr_b16 a[12:13], v238 offset:12480\n\t"
                     "ds_read_b64_tr_b16 a[14:15], v238 offset:13248\n\t" "ds_read_b64_tr_b16 a[16:17], v237 offset:24832\n\t" "ds_read_b64_tr_b16 a[18:19], v237 offset:25600\n\t"
                     "ds_read_b64_tr_b16 a[20:21], v237 offset:37248\n\t" "ds_read_b64_tr_b16 a[22:23], v237 offset:38016\n\t" "ds_read_b64_tr_b16 v[8:9], v237 offset:24864\n\t"
                     "ds_read_b64_tr_b16 v[10:11], v237 offset:25632\n\t" "ds_read_b64_tr_b16 v[12:13], v237 offset:37280\n\t" "ds_read_b64_tr_b16 v[14:15], v237 offset:38048\n\t"
                     "ds_read_b64_tr_b16 a[24:25], v238 offset:24896\n\t" "ds_read_b64_tr_b16 a[26:27], v238 offset:25664\n\t" "ds_read_b64_tr_b16 a[28:29], v238 offset:37312\n\t"
                     "ds_read_b64_tr_b16 a[30:31], v238 offset:38080\n\t" "s_waitcnt vmcnt(12) lgkmcnt(0)\n\t" "s_barrier\n\t" "s_add_u32 s42, s44, 0x480000\n\t" "s_addc_u32 s43, s45, 0\n\t" "s_add_u32 m0, %18, 0x0\n\t"
                     "s_nop 0\n\t" "global_load_lds_dwordx4 v224, s[42:43]\n\t" "s_add_u32 m0, %18, 0x400\n\t" "s_nop 0\n\t" "global_load_lds_dwordx4 v225, s[42:43]\n\t" "s_add_u32 m0, %18, 0x800\n\t" "s_nop 0\n\t"
                     "global_load_lds_dwordx4 v226, s[42:43]\n\t" "s_add_u32 s42, s44, 0x498000\n\t" "s_addc_u32 s43, s45, 0\n\t" "s_add_u32 m0, %18, 0xc20\n\t" "s_nop 0\n\t" "global_load_lds_dwordx4 v224, s[42:43]\n\t"
                     "s_add_u32 m0, %18, 0x1020\n\t" "s_nop 0\n\t" "global_load_lds_dwordx4 v225, s[42:43]\n\t" "s_add_u32 m0, %18, 0x1420\n\t" "s_nop 0\n\t" "global_load_lds_dwordx4 v226, s[42:43]\n\t"
                     "s_add_u32 s42, s44, 0x540000\n\t" "s_addc_u32 s43, s45, 0\n\t" "s_add_u32 m0, %18, 0x6100\n\t" "s_nop 0\n\t" "global_load_lds_dwordx4 v224, s[42:43]\n\t" "s_add_u32 m0, %18, 0x6500\n\t" "s_nop 0\n\t"
                     "global_load_lds_dwordx4 v225, s[42:43]\n\t" "s_add_u32 m0, %18, 0x6900\n\t" "s_nop 0\n\t" "global_load_lds_dwordx4 v226, s[42:43]\n\t" "s_add_u32 s42, s44, 0x558000\n\t" "s_addc_u32 s43, s45, 0\n\t"
                     "s_add_u32 m0, %18, 0x6d20\n\t" "s_nop 0\n\t" "global_load_lds_dwordx4 v224, s[42:43]\n\t" "s_add_u32 m0, %18, 0x7120\n\t" "s_nop 0\n\t" "global_load_lds_dwordx4 v225, s[42:43]\n\t"
                     "s_add_u32 m0, %18, 0x7520\n\t" "s_nop 0\n\t" "global_load_lds_dwordx4 v226, s[42:43]\n\t" "v_add_u32 v237, 0xc200, v227\n\t" "v_add_u32 v238, 0xc200, v228\n\t" "ds_read_b64_tr_b16 a[32:33], v237\n\t"
                     "ds_read_b64_tr_b16 a[34:35], v237 offset:768\n\t" "ds_read_b64_tr_b16 a[36:37], v237 offset:12416\n\t" "ds_read_b64_tr_b16 a[38:39], v237 offset:13184\n\t"
                     "ds_read_b64_tr_b16 v[16:17], v237 offset:32\n\t" "ds_read_b64_tr_b16 v[18:19], v237 offset:800\n\t" "ds_read_b64_tr_b16 v[20:21], v237 offset:12448\n\t"
                     "ds_read_b64_tr_b16 v[22:23], v237 offset:13216\n\t" "ds_read_b64_tr_b16 a[40:41], v238 offset:64\n\t" "ds_read_b64_tr_b16 a[42:43], v238 offset:832\n\t"
                     "ds_read_b64_tr_b16 a[44:45], v238 offset:12480\n\t" "ds_read_b64_tr_b16 a[46:47], v238 offset:13248\n\t" "ds_read_b64_tr_b16 a[48:49], v237 offset:24832\n\t"
                     "ds_read_b64_tr_b16 a[50:51], v237 offset:25600\n\t" "ds_read_b64_tr_b16 a[52:53], v237 offset:37248\n\t" "ds_read_b64_tr_b16 a[54:55], v237 offset:38016\n\t"
                     "ds_read_b64_tr_b16 v[24:25], v237 offset:24864\n\t" "ds_read_b64_tr_b16 v[26:27], v237 offset:25632\n\t" "ds_read_b64_tr_b16 v[28:29], v237 offset:37280\n\t"
                     "ds_read_b64_tr_b16 v[30:31], v237 offset:38048\n\t" "ds_read_b64_tr_b16 a[56:57], v238 offset:24896\n\t" "ds_read_b64_tr_b16 a[58:59], v238 offset:25664\n\t"
                     "ds_read_b64_tr_b16 a[60:61], v238 offset:37312\n\t" "ds_read_b64_tr_b16 a[62:63], v238 offset:38080\n\t" "s_waitcnt vmcnt(12) lgkmcnt(0)\n\t" "s_barrier\n\t" "s_add_u32 s42, s44, 0x600000\n\t"
                     "s_addc_u32 s43, s45, 0\n\t" "s_add_u32 m0, %18, 0xc200\n\t" "s_nop 0\n\t" "global_load_lds_dwordx4 v224, s[42:43]\n\t" "s_add_u32 m0, %18, 0xc600\n\t" "s_nop 0\n\t"
                     "global_load_lds_dwordx4 v225, s[42:43]\n\t" "s_add_u32 m0, %18, 0xca00\n\t" "s_nop 0\n\t" "global_load_lds_dwordx4 v226, s[42:43]\n\t" "s_add_u32 s42, s44, 0x618000\n\t" "s_addc_u32 s43, s45, 0\n\t"
                     "s_add_u32 m0, %18, 0xce20\n\t" "s_nop 0\n\t" "global_load_lds_dwordx4 v224, s[42:43]\n\t" "s_add_u32 m0, %18, 0xd220\n\t" "s_nop 0\n\t" "global_load_lds_dwordx4 v225, s[42:43]\n\t"
                     "s_add_u32 m0, %18, 0xd620\n\t" "s_nop 0\n\t" "global_load_lds_dwordx4 v226, s[42:43]\n\t" "s_add_u32 s42, s44, 0x6c0000\n\t" "s_addc_u32 s43, s45, 0\n\t" "s_add_u32 m0, %18, 0x12300\n\t" "s_nop 0\n\t"
                     "global_load_lds_dwordx4 v224, s[42:43]\n\t" "s_add_u32 m0, %18, 0x12700\n\t" "s_nop 0\n\t" "global_load_lds_dwordx4 v225, s[42:43]\n\t" "s_add_u32 m0, %18, 0x12b00\n\t" "s_nop 0\n\t"
                     "global_load_lds_dwordx4 v226, s[42:43]\n\t" "s_add_u32 s42, s44, 0x6d8000\n\t" "s_addc_u32 s43, s45, 0\n\t" "s_add_u32 m0, %18, 0x12f20\n\t" "s_nop 0\n\t" "global_load_lds_dwordx4 v224, s[42:43]\n\t"
                     "s_add_u32 m0, %18, 0x13320\n\t" "s_nop 0\n\t" "global_load_lds_dwordx4 v225, s[42:43]\n\t" "s_add_u32 m0, %18, 0x13720\n\t" "s_nop 0\n\t" "global_load_lds_dwordx4 v226, s[42:43]\n\t"
                     "v_add_u32 v237, 0x18400, v227\n\t" "v_add_u32 v238, 0x18400, v228\n\t" "ds_read_b64_tr_b16 a[64:65], v237\n\t" "ds_read_b64_tr_b16 a[66:67], v237 offset:768\n\t"
                     "ds_read_b64_tr_b16 a[68:69], v237 offset:12416\n\t" "ds_read_b64_tr_b16 a[70:71], v237 offset:13184\n\t" "ds_read_b64_tr_b16 v[32:33], v237 offset:32\n\t"
                     "ds_read_b64_tr_b16 v[34:35], v237 offset:800\n\t" "ds_read_b64_tr_b16 v[36:37], v237 offset:12448\n\t" "ds_read_b64_tr_b16 v[38:39], v237 offset:13216\n\t"
                     "ds_read_b64_tr_b16 a[72:73], v238 offset:64\n\t" "ds_read_b64_tr_b16 a[74:75], v238 offset:832\n\t" "ds_read_b64_tr_b16 a[76:77], v238 offset:12480\n\t"
                     "ds_read_b64_tr_b16 a[78:79], v238 offset:13248\n\t" "ds_read_b64_tr_b16 a[80:81], v237 offset:24832\n\t" "ds_read_b64_tr_b16 a[82:83], v237 offset:25600\n\t"
                     "ds_read_b64_tr_b16 a[84:85], v237 offset:37248\n\t" "ds_read_b64_tr_b16 a[86:87], v237 offset:38016\n\t" "ds_read_b64_tr_b16 v[40:41], v237 offset:24864\n\t"
                     "ds_read_b64_tr_b16 v[42:43], v237 offset:25632\n\t" "ds_read_b64_tr_b16 v[44:45], v237 offset:37280\n\t" "ds_read_b64_tr_b16 v[46:47], v237 offset:38048\n\t"
                     "ds_read_b64_tr_b16 a[88:89], v238 offset:24896\n\t" "ds_read_b64_tr_b16 a[90:91], v238 offset:25664\n\t" "ds_read_b64_tr_b16 a[92:93], v238 offset:37312\n\t"
                     "ds_read_b64_tr_b16 a[94:95], v238 offset:38080\n\t" "s_waitcnt vmcnt(12) lgkmcnt(0)\n\t" "s_barrier\n\t" "s_add_u32 s42, s46, 0x0\n\t" "s_addc_u32 s43, s47, 0\n\t" "s_add_u32 m0, %20, 0x18400\n\t"
                     "s_nop 0\n\t" "global_load_lds_dwordx4 v232, s[42:43]\n\t" "s_add_u32 m0, %20, 0x18800\n\t" "s_nop 0\n\t" "global_load_lds_dwordx4 v233, s[42:43]\n\t" "s_add_u32 m0, %20, 0x18c00\n\t" "s_nop 0\n\t"
                     "global_load_lds_dwordx4 v234, s[42:43]\n\t" "s_add_u32 m0, %20, 0x19000\n\t" "s_nop 0\n\t" "global_load_lds_dwordx4 v235, s[42:43]\n\t" "s_add_u32 m0, %20, 0x19400\n\t" "s_nop 0\n\t"
                     "global_load_lds_dwordx4 v236, s[42:43]\n\t" "s_add_u32 s42, s46, 0x1400\n\t" "s_addc_u32 s43, s47, 0\n\t" "s_add_u32 m0, %20, 0x19800\n\t" "v_xor_b32 v224, 0x80, v232\n\t"
                     "global_load_lds_dwordx4 v224, s[42:43]\n\t" "s_add_u32 m0, %20, 0x19c00\n\t" "v_xor_b32 v224, 0x80, v233\n\t" "global_load_lds_dwordx4 v224, s[42:43]\n\t" "s_add_u32 m0, %20, 0x1a000\n\t"
                     "v_xor_b32 v224, 0x80, v234\n\t" "global_load_lds_dwordx4 v224, s[42:43]\n\t" "s_add_u32 m0, %20, 0x1a400\n\t" "v_xor_b32 v224, 0x80, v235\n\t" "global_load_lds_dwordx4 v224, s[42:43]\n\t"
                     "s_add_u32 m0, %20, 0x1a800\n\t" "v_xor_b32 v224, 0x80, v236\n\t" "global_load_lds_dwordx4 v224, s[42:43]\n\t" "v_add_u32 v237, 0x0, v227\n\t" "v_add_u32 v238, 0x0, v228\n\t"
                     "ds_read_b64_tr_b16 a[96:97], v237\n\t" "ds_read_b64_tr_b16 a[98:99], v237 offset:768\n\t" "ds_read_b64_tr_b16 a[100:101], v237 offset:12416\n\t" "ds_read_b64_tr_b16 a[102:103], v237 offset:13184\n\t"
                     "ds_read_b64_tr_b16 v[48:49], v237 offset:32\n\t" "ds_read_b64_tr_b16 v[50:51], v237 offset:800\n\t" "ds_read_b64_tr_b16 v[52:53], v237 offset:12448\n\t"
                     "ds_read_b64_tr_b16 v[54:55], v237 offset:13216\n\t" "ds_read_b64_tr_b16 a[104:105], v238 offset:64\n\t" "ds_read_b64_tr_b16 a[106:107], v238 offset:832\n\t"
                     "ds_read_b64_tr_b16 a[108:109], v238 offset:12480\n\t" "ds_read_b64_tr_b16 a[110:111], v238 offset:13248\n\t" "ds_read_b64_tr_b16 a[112:113], v237 offset:24832\n\t"
                     "ds_read_b64_tr_b16 a[114:115], v237 offset:25600\n\t" "ds_read_b64_tr_b16 a[116:117], v237 offset:37248\n\t" "ds_read_b64_tr_b16 a[118:119], v237 offset:38016\n\t"
                     "ds_read_b64_tr_b16 v[56:57], v237 offset:24864\n\t" "ds_read_b64_tr_b16 v[58:59], v237 offset:25632\n\t" "ds_read_b64_tr_b16 v[60:61], v237 offset:37280\n\t"
                     "ds_read_b64_tr_b16 v[62:63], v237 offset:38048\n\t" "ds_read_b64_tr_b16 a[120:121], v238 offset:24896\n\t" "ds_read_b64_tr_b16 a[122:123], v238 offset:25664\n\t"
                     "ds_read_b64_tr_b16 a[124:125], v238 offset:37312\n\t" "ds_read_b64_tr_b16 a[126:127], v238 offset:38080\n\t" "s_waitcnt vmcnt(10) lgkmcnt(0)\n\t" "s_barrier\n\t" "s_add_u32 s42, s46, 0xa000\n\t"
                     "s_addc_u32 s43, s47, 0\n\t" "s_add_u32 m0, %20, 0x0\n\t" "s_nop 0\n\t" "global_load_lds_dwordx4 v232, s[42:43]\n\t" "s_add_u32 m0, %20, 0x400\n\t" "s_nop 0\n\t"
                     "global_load_lds_dwordx4 v233, s[42:43]\n\t" "s_add_u32 m0, %20, 0x800\n\t" "s_nop 0\n\t" "global_load_lds_dwordx4 v234, s[42:43]\n\t" "s_add_u32 m0, %20, 0xc00\n\t" "s_nop 0\n\t"
                     "global_load_lds_dwordx4 v235, s[42:43]\n\t" "s_add_u32 m0, %20, 0x1000\n\t" "s_nop 0\n\t" "global_load_lds_dwordx4 v236, s[42:43]\n\t" "s_add_u32 s42, s46, 0xb400\n\t" "s_addc_u32 s43, s47, 0\n\t"
                     "s_add_u32 m0, %20, 0x1400\n\t" "v_xor_b32 v224, 0x80, v232\n\t" "global_load_lds_dwordx4 v224, s[42:43]\n\t" "s_add_u32 m0, %20, 0x1800\n\t" "v_xor_b32 v224, 0x80, v233\n\t"
                     "global_load_lds_dwordx4 v224, s[42:43]\n\t" "s_add_u32 m0, %20, 0x1c00\n\t" "v_xor_b32 v224, 0x80, v234\n\t" "global_load_lds_dwordx4 v224, s[42:43]\n\t" "s_add_u32 m0, %20, 0x2000\n\t"
                     "v_xor_b32 v224, 0x80, v235\n\t" "global_load_lds_dwordx4 v224, s[42:43]\n\t" "s_add_u32 m0, %20, 0x2400\n\t" "v_xor_b32 v224, 0x80, v236\n\t" "global_load_lds_dwordx4 v224, s[42:43]\n\t"
                     "v_add_u32 v237, 0xc200, v227\n\t" "v_add_u32 v238, 0xc200, v228\n\t" "ds_read_b64_tr_b16 a[128:129], v237\n\t" "ds_read_b64_tr_b16 a[130:131], v237 offset:768\n\t"
                     "ds_read_b64_tr_b16 a[132:133], v237 offset:12416\n\t" "ds_read_b64_tr_b16 a[134:135], v237 offset:13184\n\t" "ds_read_b64_tr_b16 a[136:137], v237 offset:32\n\t"
                     "ds_read_b64_tr_b16 a[138:139], v237 offset:800\n\t" "ds_read_b64_tr_b16 a[140:141], v237 offset:12448\n\t" "ds_read_b64_tr_b16 a[142:143], v237 offset:13216\n\t"
                     "ds_read_b64_tr_b16 a[144:145], v238 offset:64\n\t" "ds_read_b64_tr_b16 a[146:147], v238 offset:832\n\t" "ds_read_b64_tr_b16 a[148:149], v238 offset:12480\n\t"
                     "ds_read_b64_tr_b16 a[150:151], v238 offset:13248\n\t" "ds_read_b64_tr_b16 a[152:153], v237 offset:24832\n\t" "ds_read_b64_tr_b16 a[154:155], v237 offset:25600\n\t"
                     "ds_read_b64_tr_b16 a[156:157], v237 offset:37248\n\t" "ds_read_b64_tr_b16 a[158:159], v237 offset:38016\n\t" "ds_read_b64_tr_b16 a[160:161], v237 offset:24864\n\t"
                     "ds_read_b64_tr_b16 a[162:163], v237 offset:25632\n\t" "ds_read_b64_tr_b16 a[164:165], v237 offset:37280\n\t" "ds_read_b64_tr_b16 a[166:167], v237 offset:38048\n\t"
                     "ds_read_b64_tr_b16 a[168:169], v238 offset:24896\n\t" "ds_read_b64_tr_b16 a[170:171], v238 offset:25664\n\t" "ds_read_b64_tr_b16 a[172:173], v238 offset:37312\n\t"
                     "ds_read_b64_tr_b16 a[174:175], v238 offset:38080\n\t" "s_waitcnt vmcnt(10) lgkmcnt(0)\n\t" "s_barrier\n\t" "s_add_u32 s42, s46, 0x14000\n\t" "s_addc_u32 s43, s47, 0\n\t"
                     "s_add_u32 m0, %20, 0xc200\n\t" "s_nop 0\n\t" "global_load_lds_dwordx4 v232, s[42:43]\n\t" "s_add_u32 m0, %20, 0xc600\n\t" "s_nop 0\n\t" "global_load_lds_dwordx4 v233, s[42:43]\n\t"
                     "s_add_u32 m0, %20, 0xca00\n\t" "s_nop 0\n\t" "global_load_lds_dwordx4 v234, s[42:43]\n\t" "s_add_u32 m0, %20, 0xce00\n\t" "s_nop 0\n\t" "global_load_lds_dwordx4 v235, s[42:43]\n\t"
                     "s_add_u32 m0, %20, 0xd200\n\t" "s_nop 0\n\t" "global_load_lds_dwordx4 v236, s[42:43]\n\t" "s_add_u32 s42, s46, 0x15400\n\t" "s_addc_u32 s43, s47, 0\n\t" "s_add_u32 m0, %20, 0xd600\n\t"
                     "v_xor_b32 v224, 0x80, v232\n\t" "global_load_lds_dwordx4 v224, s[42:43]\n\t" "s_add_u32 m0, %20, 0xda00\n\t" "v_xor_b32 v224, 0x80, v233\n\t" "global_load_lds_dwordx4 v224, s[42:43]\n\t"
                     "s_add_u32 m0, %20, 0xde00\n\t" "v_xor_b32 v224, 0x80, v234\n\t" "global_load_lds_dwordx4 v224, s[42:43]\n\t" "s_add_u32 m0, %20, 0xe200\n\t" "v_xor_b32 v224, 0x80, v235\n\t"
                     "global_load_lds_dwordx4 v224, s[42:43]\n\t" "s_add_u32 m0, %20, 0xe600\n\t" "v_xor_b32 v224, 0x80, v236\n\t" "global_load_lds_dwordx4 v224, s[42:43]\n\t" "s_add_u32 s42, %21, 0x18400\n\t"
                     "v_xor_b32 v237, 0x0, v229\n\t" "v_add_u32 v237, s42, v237\n\t" "v_xor_b32 v238, 0x40, v229\n\t" "v_add_u32 v238, s42, v238\n\t" "v_xor_b32 v239, 0x80, v229\n\t" "v_add_u32 v239, s42, v239\n\t"
                     "v_xor_b32 v226, 0xc0, v229\n\t" "v_add_u32 v226, s42, v226\n\t" "ds_read_b64_tr_b16 a[176:177], v237\n\t" "ds_read_b64_tr_b16 a[178:179], v238 offset:2560\n\t"
                     "ds_read_b64_tr_b16 a[200:201], v239\n\t" "ds_read_b64_tr_b16 a[202:203], v226 offset:2560\n\t" "ds_read_b64_tr_b16 a[224:225], v237 offset:256\n\t"
                     "ds_read_b64_tr_b16 a[226:227], v238 offset:2816\n\t" "ds_read_b64_tr_b16 a[248:249], v239 offset:256\n\t" "ds_read_b64_tr_b16 a[250:251], v226 offset:2816\n\t"
                     "ds_read_b64_tr_b16 v[112:113], v237 offset:512\n\t" "ds_read_b64_tr_b16 v[114:115], v238 offset:3072\n\t" "ds_read_b64_tr_b16 v[136:137], v239 offset:512\n\t"
                     "ds_read_b64_tr_b16 v[138:139], v226 offset:3072\n\t" "ds_read_b64_tr_b16 v[160:161], v237 offset:768\n\t" "ds_read_b64_tr_b16 v[162:163], v238 offset:3328\n\t"
                     "ds_read_b64_tr_b16 v[184:185], v239 offset:768\n\t" "ds_read_b64_tr_b16 v[186:187], v226 offset:3328\n\t" "ds_read_b64_tr_b16 v[208:209], v237 offset:1024\n\t"
                     "ds_read_b64_tr_b16 v[210:211], v238 offset:3584\n\t" "ds_read_b64_tr_b16 v[72:73], v239 offset:1024\n\t" "ds_read_b64_tr_b16 v[74:75], v226 offset:3584\n\t" "s_waitcnt vmcnt(10) lgkmcnt(0)\n\t"
                     "s_barrier\n\t" "s_add_u32 s42, s46, 0x1e000\n\t" "s_addc_u32 s43, s47, 0\n\t" "s_add_u32 m0, %20, 0x18400\n\t" "s_nop 0\n\t" "global_load_lds_dwordx4 v232, s[42:43]\n\t"
                     "s_add_u32 m0, %20, 0x18800\n\t" "s_nop 0\n\t" "global_load_lds_dwordx4 v233, s[42:43]\n\t" "s_add_u32 m0, %20, 0x18c00\n\t" "s_nop 0\n\t" "global_load_lds_dwordx4 v234, s[42:43]\n\t"
                     "s_add_u32 m0, %20, 0x19000\n\t" "s_nop 0\n\t" "global_load_lds_dwordx4 v235, s[42:43]\n\t" "s_add_u32 m0, %20, 0x19400\n\t" "s_nop 0\n\t" "global_load_lds_dwordx4 v236, s[42:43]\n\t"
                     "s_add_u32 s42, s46, 0x1f400\n\t" "s_addc_u32 s43, s47, 0\n\t" "s_add_u32 m0, %20, 0x19800\n\t" "v_xor_b32 v224, 0x80, v232\n\t" "global_load_lds_dwordx4 v224, s[42:43]\n\t"
                     "s_add_u32 m0, %20, 0x19c00\n\t" "v_xor_b32 v224, 0x80, v233\n\t" "global_load_lds_dwordx4 v224, s[42:43]\n\t" "s_add_u32 m0, %20, 0x1a000\n\t" "v_xor_b32 v224, 0x80, v234\n\t"
                     "global_load_lds_dwordx4 v224, s[42:43]\n\t" "s_add_u32 m0, %20, 0x1a400\n\t" "v_xor_b32 v224, 0x80, v235\n\t" "global_load_lds_dwordx4 v224, s[42:43]\n\t" "s_add_u32 m0, %20, 0x1a800\n\t"
                     "v_xor_b32 v224, 0x80, v236\n\t" "global_load_lds_dwordx4 v224, s[42:43]\n\t" "s_add_u32 s42, %21, 0x0\n\t" "v_xor_b32 v237, 0x0, v229\n\t" "v_add_u32 v237, s42, v237\n\t"
                     "v_xor_b32 v238, 0x40, v229\n\t" "v_add_u32 v238, s42, v238\n\t" "v_xor_b32 v239, 0x80, v229\n\t" "v_add_u32 v239, s42, v239\n\t" "v_xor_b32 v226, 0xc0, v229\n\t" "v_add_u32 v226, s42, v226\n\t"
                     "ds_read_b64_tr_b16 a[180:181], v237\n\t" "ds_read_b64_tr_b16 a[182:183], v238 offset:2560\n\t" "ds_read_b64_tr_b16 a[204:205], v239\n\t" "ds_read_b64_tr_b16 a[206:207], v226 offset:2560\n\t"
                     "ds_read_b64_tr_b16 a[228:229], v237 offset:256\n\t" "ds_read_b64_tr_b16 a[230:231], v238 offset:2816\n\t" "ds_read_b64_tr_b16 a[252:253], v239 offset:256\n\t"
                     "ds_read_b64_tr_b16 a[254:255], v226 offset:2816\n\t" "ds_read_b64_tr_b16 v[116:117], v237 offset:512\n\t" "ds_read_b64_tr_b16 v[118:119], v238 offset:3072\n\t"
                     "ds_read_b64_tr_b16 v[140:141], v239 offset:512\n\t" "ds_read_b64_tr_b16 v[142:143], v226 offset:3072\n\t" "ds_read_b64_tr_b16 v[164:165], v237 offset:768\n\t"
                     "ds_read_b64_tr_b16 v[166:167], v238 offset:3328\n\t" "ds_read_b64_tr_b16 v[188:189], v239 offset:768\n\t" "ds_read_b64_tr_b16 v[190:191], v226 offset:3328\n\t"
                     "ds_read_b64_tr_b16 v[212:213], v237 offset:1024\n\t" "ds_read_b64_tr_b16 v[214:215], v238 offset:3584\n\t" "ds_read_b64_tr_b16 v[76:77], v239 offset:1024\n\t"
                     "ds_read_b64_tr_b16 v[78:79], v226 offset:3584\n\t" "s_waitcnt vmcnt(10) lgkmcnt(0)\n\t" "s_barrier\n\t" "v_mov_b32 v225, 0\n\t" "global_load_dwordx2 %12, v231, %19 sc1\n\t"
                     "global_load_dwordx2 %13, v225, %19 offset:512 sc1\n\t" "global_load_dwordx2 %14, v225, %19 offset:1024 sc1\n\t" "s_add_u32 s42, s46, 0x28000\n\t" "s_addc_u32 s43, s47, 0\n\t"
                     "s_add_u32 m0, %20, 0x0\n\t" "s_nop 0\n\t" "global_load_lds_dwordx4 v232, s[42:43]\n\t" "s_add_u32 m0, %20, 0x400\n\t" "s_nop 0\n\t" "global_load_lds_dwordx4 v233, s[42:43]\n\t"
                     "s_add_u32 m0, %20, 0x800\n\t" "s_nop 0\n\t" "global_load_lds_dwordx4 v234, s[42:43]\n\t" "s_add_u32 m0, %20, 0xc00\n\t" "s_nop 0\n\t" "global_load_lds_dwordx4 v235, s[42:43]\n\t"
                     "s_add_u32 m0, %20, 0x1000\n\t" "s_nop 0\n\t" "global_load_lds_dwordx4 v236, s[42:43]\n\t" "s_add_u32 s42, s46, 0x29400\n\t" "s_addc_u32 s43, s47, 0\n\t" "s_add_u32 m0, %20, 0x1400\n\t"
                     "v_xor_b32 v224, 0x80, v232\n\t" "global_load_lds_dwordx4 v224, s[42:43]\n\t" "s_add_u32 m0, %20, 0x1800\n\t" "v_xor_b32 v224, 0x80, v233\n\t" "global_load_lds_dwordx4 v224, s[42:43]\n\t"
                     "s_add_u32 m0, %20, 0x1c00\n\t" "v_xor_b32 v224, 0x80, v234\n\t" "global_load_lds_dwordx4 v224, s[42:43]\n\t" "s_add_u32 m0, %20, 0x2000\n\t" "v_xor_b32 v224, 0x80, v235\n\t"
                     "global_load_lds_dwordx4 v224, s[42:43]\n\t" "s_add_u32 m0, %20, 0x2400\n\t" "v_xor_b32 v224, 0x80, v236\n\t" "global_load_lds_dwordx4 v224, s[42:43]\n\t" "s_add_u32 s42, %21, 0xc200\n\t"
                     "v_xor_b32 v237, 0x0, v229\n\t" "v_add_u32 v237, s42, v237\n\t" "v_xor_b32 v238, 0x40, v229\n\t" "v_add_u32 v238, s42, v238\n\t" "v_xor_b32 v239, 0x80, v229\n\t" "v_add_u32 v239, s42, v239\n\t"
                     "v_xor_b32 v226, 0xc0, v229\n\t" "v_add_u32 v226, s42, v226\n\t" "ds_read_b64_tr_b16 a[184:185], v237\n\t" "ds_read_b64_tr_b16 a[186:187], v238 offset:2560\n\t"
                     "ds_read_b64_tr_b16 a[208:209], v239\n\t" "ds_read_b64_tr_b16 a[210:211], v226 offset:2560\n\t" "ds_read_b64_tr_b16 a[232:233], v237 offset:256\n\t"
                     "ds_read_b64_tr_b16 a[234:235], v238 offset:2816\n\t" "ds_read_b64_tr_b16 v[96:97], v239 offset:256\n\t" "ds_read_b64_tr_b16 v[98:99], v226 offset:2816\n\t"
                     "ds_read_b64_tr_b16 v[120:121], v237 offset:512\n\t" "ds_read_b64_tr_b16 v[122:123], v238 offset:3072\n\t" "ds_read_b64_tr_b16 v[144:145], v239 offset:512\n\t"
                     "ds_read_b64_tr_b16 v[146:147], v226 offset:3072\n\t" "ds_read_b64_tr_b16 v[168:169], v237 offset:768\n\t" "ds_read_b64_tr_b16 v[170:171], v238 offset:3328\n\t"
                     "ds_read_b64_tr_b16 v[192:193], v239 offset:768\n\t" "ds_read_b64_tr_b16 v[194:195], v226 offset:3328\n\t" "ds_read_b64_tr_b16 v[216:217], v237 offset:1024\n\t"
                     "ds_read_b64_tr_b16 v[218:219], v238 offset:3584\n\t" "ds_read_b64_tr_b16 v[80:81], v239 offset:1024\n\t" "ds_read_b64_tr_b16 v[82:83], v226 offset:3584\n\t" "s_waitcnt vmcnt(10) lgkmcnt(0)\n\t"
                     "s_barrier\n\t" "s_add_u32 s42, s46, 0x32000\n\t" "s_addc_u32 s43, s47, 0\n\t" "s_add_u32 m0, %20, 0xc200\n\t" "s_nop 0\n\t" "global_load_lds_dwordx4 v232, s[42:43]\n\t" "s_add_u32 m0, %20, 0xc600\n\t"
                     "s_nop 0\n\t" "global_load_lds_dwordx4 v233, s[42:43]\n\t" "s_add_u32 m0, %20, 0xca00\n\t" "s_nop 0\n\t" "global_load_lds_dwordx4 v234, s[42:43]\n\t" "s_add_u32 m0, %20, 0xce00\n\t" "s_nop 0\n\t"
                     "global_load_lds_dwordx4 v235, s[42:43]\n\t" "s_add_u32 m0, %20, 0xd200\n\t" "s_nop 0\n\t" "global_load_lds_dwordx4 v236, s[42:43]\n\t" "s_add_u32 s42, s46, 0x33400\n\t" "s_addc_u32 s43, s47, 0\n\t"
                     "s_add_u32 m0, %20, 0xd600\n\t" "v_xor_b32 v224, 0x80, v232\n\t" "global_load_lds_dwordx4 v224, s[42:43]\n\t" "s_add_u32 m0, %20, 0xda00\n\t" "v_xor_b32 v224, 0x80, v233\n\t"
                     "global_load_lds_dwordx4 v224, s[42:43]\n\t" "s_add_u32 m0, %20, 0xde00\n\t" "v_xor_b32 v224, 0x80, v234\n\t" "global_load_lds_dwordx4 v224, s[42:43]\n\t" "s_add_u32 m0, %20, 0xe200\n\t"
                     "v_xor_b32 v224, 0x80, v235\n\t" "global_load_lds_dwordx4 v224, s[42:43]\n\t" "s_add_u32 m0, %20, 0xe600\n\t" "v_xor_b32 v224, 0x80, v236\n\t" "global_load_lds_dwordx4 v224, s[42:43]\n\t"
                     "s_add_u32 s42, %21, 0x18400\n\t" "v_xor_b32 v237, 0x0, v229\n\t" "v_add_u32 v237, s42, v237\n\t" "v_xor_b32 v238, 0x40, v229\n\t" "v_add_u32 v238, s42, v238\n\t" "v_xor_b32 v239, 0x80, v229\n\t"
                     "v_add_u32 v239, s42, v239\n\t" "v_xor_b32 v226, 0xc0, v229\n\t" "v_add_u32 v226, s42, v226\n\t" "ds_read_b64_tr_b16 a[188:189], v237\n\t" "ds_read_b64_tr_b16 a[190:191], v238 offset:2560\n\t"
                     "ds_read_b64_tr_b16 a[212:213], v239\n\t" "ds_read_b64_tr_b16 a[214:215], v226 offset:2560\n\t" "ds_read_b64_tr_b16 a[236:237], v237 offset:256\n\t"
                     "ds_read_b64_tr_b16 a[238:239], v238 offset:2816\n\t" "ds_read_b64_tr_b16 v[100:101], v239 offset:256\n\t" "ds_read_b64_tr_b16 v[102:103], v226 offset:2816\n\t"
                     "ds_read_b64_tr_b16 v[124:125], v237 offset:512\n\t" "ds_read_b64_tr_b16 v[126:127], v238 offset:3072\n\t" "ds_read_b64_tr_b16 v[148:149], v239 offset:512\n\t"
                     "ds_read_b64_tr_b16 v[150:151], v226 offset:3072\n\t" "ds_read_b64_tr_b16 v[172:173], v237 offset:768\n\t" "ds_read_b64_tr_b16 v[174:175], v238 offset:3328\n\t"
                     "ds_read_b64_tr_b16 v[196:197], v239 offset:768\n\t" "ds_read_b64_tr_b16 v[198:199], v226 offset:3328\n\t" "ds_read_b64_tr_b16 v[220:221], v237 offset:1024\n\t"
                     "ds_read_b64_tr_b16 v[222:223], v238 offset:3584\n\t" "ds_read_b64_tr_b16 v[84:85], v239 offset:1024\n\t" "ds_read_b64_tr_b16 v[86:87], v226 offset:3584\n\t" "s_waitcnt vmcnt(10) lgkmcnt(0)\n\t"
                     "s_barrier\n\t" "s_add_u32 s42, %21, 0x0\n\t" "v_xor_b32 v237, 0x0, v229\n\t" "v_add_u32 v237, s42, v237\n\t" "v_xor_b32 v238, 0x40, v229\n\t" "v_add_u32 v238, s42, v238\n\t"
                     "v_xor_b32 v239, 0x80, v229\n\t" "v_add_u32 v239, s42, v239\n\t" "v_xor_b32 v226, 0xc0, v229\n\t" "v_add_u32 v226, s42, v226\n\t" "ds_read_b64_tr_b16 a[192:193], v237\n\t"
                     "ds_read_b64_tr_b16 a[194:195], v238 offset:2560\n\t" "ds_read_b64_tr_b16 a[216:217], v239\n\t" "ds_read_b64_tr_b16 a[218:219], v226 offset:2560\n\t"
                     "ds_read_b64_tr_b16 a[240:241], v237 offset:256\n\t" "ds_read_b64_tr_b16 a[242:243], v238 offset:2816\n\t" "ds_read_b64_tr_b16 v[104:105], v239 offset:256\n\t"
                     "ds_read_b64_tr_b16 v[106:107], v226 offset:2816\n\t" "ds_read_b64_tr_b16 v[128:129], v237 offset:512\n\t" "ds_read_b64_tr_b16 v[130:131], v238 offset:3072\n\t"
                     "ds_read_b64_tr_b16 v[152:153], v239 offset:512\n\t" "ds_read_b64_tr_b16 v[154:155], v226 offset:3072\n\t" "ds_read_b64_tr_b16 v[176:177], v237 offset:768\n\t"
                     "ds_read_b64_tr_b16 v[178:179], v238 offset:3328\n\t" "ds_read_b64_tr_b16 v[200:201], v239 offset:768\n\t" "ds_read_b64_tr_b16 v[202:203], v226 offset:3328\n\t"
                     "ds_read_b64_tr_b16 v[64:65], v237 offset:1024\n\t" "ds_read_b64_tr_b16 v[66:67], v238 offset:3584\n\t" "ds_read_b64_tr_b16 v[88:89], v239 offset:1024\n\t"
                     "ds_read_b64_tr_b16 v[90:91], v226 offset:3584\n\t" "s_waitcnt vmcnt(0) lgkmcnt(0)\n\t" "s_barrier\n\t" "s_add_u32 s42, %21, 0xc200\n\t" "v_xor_b32 v237, 0x0, v229\n\t" "v_add_u32 v237, s42, v237\n\t"
                     "v_xor_b32 v238, 0x40, v229\n\t" "v_add_u32 v238, s42, v238\n\t" "v_xor_b32 v239, 0x80, v229\n\t" "v_add_u32 v239, s42, v239\n\t" "v_xor_b32 v226, 0xc0, v229\n\t" "v_add_u32 v226, s42, v226\n\t"
                     "ds_read_b64_tr_b16 a[196:197], v237\n\t" "ds_read_b64_tr_b16 a[198:199], v238 offset:2560\n\t" "ds_read_b64_tr_b16 a[220:221], v239\n\t" "ds_read_b64_tr_b16 a[222:223], v226 offset:2560\n\t"
                     "ds_read_b64_tr_b16 a[244:245], v237 offset:256\n\t" "ds_read_b64_tr_b16 a[246:247], v238 offset:2816\n\t" "ds_read_b64_tr_b16 v[108:109], v239 offset:256\n\t"
                     "ds_read_b64_tr_b16 v[110:111], v226 offset:2816\n\t" "ds_read_b64_tr_b16 v[132:133], v237 offset:512\n\t" "ds_read_b64_tr_b16 v[134:135], v238 offset:3072\n\t"
                     "ds_read_b64_tr_b16 v[156:157], v239 offset:512\n\t" "ds_read_b64_tr_b16 v[158:159], v226 offset:3072\n\t" "ds_read_b64_tr_b16 v[180:181], v237 offset:768\n\t"
                     "ds_read_b64_tr_b16 v[182:183], v238 offset:3328\n\t" "ds_read_b64_tr_b16 v[204:205], v239 offset:768\n\t" "ds_read_b64_tr_b16 v[206:207], v226 offset:3328\n\t"
                     "ds_read_b64_tr_b16 v[68:69], v237 offset:1024\n\t" "ds_read_b64_tr_b16 v[70:71], v238 offset:3584\n\t" "ds_read_b64_tr_b16 v[92:93], v239 offset:1024\n\t"
                     "ds_read_b64_tr_b16 v[94:95], v226 offset:3584\n\t" "s_waitcnt lgkmcnt(0)\n\t" "ds_write_b128 v230, v[0:3]\n\t" "ds_write_b128 v230, v[4:7] offset:1024\n\t"
                     "ds_write_b128 v230, v[8:11] offset:2048\n\t" "ds_write_b128 v230, v[12:15] offset:3072\n\t" "ds_write_b128 v230, v[16:19] offset:4096\n\t" "ds_write_b128 v230, v[20:23] offset:5120\n\t"
                     "ds_write_b128 v230, v[24:27] offset:6144\n\t" "ds_write_b128 v230, v[28:31] offset:7168\n\t" "ds_write_b128 v230, v[32:35] offset:8192\n\t" "ds_write_b128 v230, v[36:39] offset:9216\n\t"
                     "ds_write_b128 v230, v[40:43] offset:10240\n\t" "ds_write_b128 v230, v[44:47] offset:11264\n\t" "ds_write_b128 v230, v[48:51] offset:12288\n\t" "ds_write_b128 v230, v[52:55] offset:13312\n\t"
                     "ds_write_b128 v230, v[56:59] offset:14336\n\t" "ds_write_b128 v230, v[60:63] offset:15360\n\t" "ds_write_b128 v230, v[64:67] offset:16384\n\t" "ds_write_b128 v230, v[68:71] offset:17408\n\t"
                     "ds_write_b128 v230, v[72:75] offset:18432\n\t" "ds_write_b128 v230, v[76:79] offset:19456\n\t" "ds_write_b128 v230, v[80:83] offset:20480\n\t" "ds_write_b128 v230, v[84:87] offset:21504\n\t"
                     "ds_write_b128 v230, v[88:91] offset:22528\n\t" "ds_write_b128 v230, v[92:95] offset:23552\n\t"
                     : "=&{a[0:31]}"(wa[0]), "=&{a[32:63]}"(wa[1]), "=&{a[64:95]}"(wa[2]), "=&{a[96:127]}"(wa[3]), "=&{a[128:159]}"(wa[4]), "=&{a[160:191]}"(wa[5]), "=&{a[192:223]}"(wa[6]), "=&{a[224:255]}"(wa[7]), "=&{v[96:127]}"(wv[0]), "=&{v[128:159]}"(wv[1]), "=&{v[160:191]}"(wv[2]), "=&{v[192:223]}"(wv[3]), "=&v"(y.r), "=&v"(y.g), "=&v"(y.e), "+{v[224:239]}"(pa)
                     : "{s[44:45]}"(p.wv + h * DH * 2 + (size_t)(16 * w) * (HD * 2)), "{s[46:47]}"(p.wo + (size_t)(h * DH + 8 * w) * 1280),
                       "s"(lds + 2 * w * GS), "s"(p.ws + OFF_LAT), "s"(lds + w * 10240), "s"(lds)
                     : "v0", "v1", "v2", "v3", "v4", "v5", "v6", "v7", "v8", "v9", "v10", "v11", "v12", "v13", "v14", "v15", "v16", "v17", "v18", "v19", "v20", "v21", "v22", "v23", "v24", "v25", "v26", "v27", "v28", "v29", "v30", "v31", "v32", "v33", "v34", "v35", "v36", "v37", "v38", "v39", "v40", "v41", "v42", "v43", "v44", "v45", "v46", "v47", "v48", "v49", "v50", "v51", "v52", "v53", "v54", "v55", "v56", "v57", "v58", "v59", "v60", "v61", "v62", "v63", "v64", "v65", "v66", "v67", "v68", "v69", "v70", "v71", "v72", "v73", "v74", "v75", "v76", "v77", "v78", "v79", "v80", "v81", "v82", "v83", "v84", "v85", "v86", "v87", "v88", "v89", "v90", "v91", "v92", "v93", "v94", "v95", "s42", "s43", "scc", "memory");
        sd = sync_end(y, p.ws, phi, w == 0);
        barrier();
    }
    const int lane = lanex(), tid = 64 * w + lane;
    const int n = lane & 15, g = lane >> 4, qq = (lane >> 2) & 3, pp = lane & 3;
    char* const fl = p.ws + OFF_ST;
    const u32 iw = lds + 65536 + ((lane * 8) ^ (lane & 32));
    const u32 qr0 = lds + 65536 + (g >> 1) * 512 + pp * 128 + 64 * (g & 1) + 8 * qq + 32 * (pp >> 1);
    const u32 uo = (u32)((2 * w + (n >> 3)) * 4096 + h * 128 + (n & 7) * 16);
    u32x16 ad = u32x16{(u32)lane * 16, (lane & 2) ? 0xEEEEu : 0x4444u, lds + (u32)((w & 1) * 24576 + (w >> 1) * 99328 + lane * 16), iw + ((3 * w + (w & 1)) >> 1) * 512,
                       iw + (1 + 3 * (w >> 1)) * 512 + 4 * (w & 1), qr0, qr0 ^ 32, uo + (2 + g) * 32768, uo + (g & 1) * 32768 + (g >> 1) * 8, uo, uo, uo, uo, uo, uo, uo};
    asm volatile("" : "+{v[224:239]}"(ad));
    const char* memp = p.ws + mslot(sd, 0);
    int budget = 30000;
    Clock clk; clk.init();
    for (int s = 0; s < NSEG; ++s) {
        int tries = 0;
        u64 bad, tp8, en = ~0ull;
        while (true) {
            const u32 tgt = clk.sched(phi, 0, cfg.G, tries ? 2 : 0);
            char* up = p.ws + uslot(sd, s);
            asm volatile("ds_read_b128 v[64:67], v226\n\t" "ds_read_b128 v[68:71], v226 offset:1024\n\t" "ds_read_b128 v[72:75], v226 offset:2048\n\t" "ds_read_b128 v[76:79], v226 offset:3072\n\t"
                         "ds_read_b128 v[80:83], v226 offset:4096\n\t" "ds_read_b128 v[84:87], v226 offset:5120\n\t" "ds_read_b128 v[88:91], v226 offset:6144\n\t" "ds_read_b128 v[92:95], v226 offset:7168\n\t"
                         "v_mov_b64 v[40:41], 0\n\t" "v_mov_b64 v[42:43], 0\n\t" "v_mov_b64 v[44:45], 0\n\t" "v_mov_b64 v[46:47], 0\n\t" "v_mov_b64 v[48:49], 0\n\t" "v_mov_b64 v[50:51], 0\n\t" ".Lsp_u:\n\t" "s_memtime vcc\n\t"
                         "s_waitcnt lgkmcnt(0)\n\t" "s_sub_i32 s42, %3, vcc_lo\n\t" "s_cmp_gt_i32 s42, 80\n\t" "s_cbranch_scc1 .Lsp_u\n\t" "s_max_i32 s42, s42, 0\n\t" "s_sub_i32 s42, 92, s42\n\t" "s_and_b32 s42, s42, -4\n\t"
                         "s_getpc_b64 vcc\n\t" "s_add_u32 vcc_lo, vcc_lo, s42\n\t" "s_addc_u32 vcc_hi, vcc_hi, 0\n\t" "s_setpc_b64 vcc\n\t" "s_nop 0\n\t" "s_nop 0\n\t" "s_nop 0\n\t" "s_nop 0\n\t" "s_nop 0\n\t" "s_nop 0\n\t"
                         "s_nop 0\n\t" "s_nop 0\n\t" "s_nop 0\n\t" "s_nop 0\n\t" "s_nop 0\n\t" "s_nop 0\n\t" "s_nop 0\n\t" "s_nop 0\n\t" "s_nop 0\n\t" "s_nop 0\n\t" "s_nop 0\n\t" "s_nop 0\n\t" "s_nop 0\n\t" "s_nop 0\n\t"
                         "global_load_dwordx4 v[0:3], v224, %4 offset:-4096 sc1\n\t" "s_nop 10\n\t" "global_load_dwordx4 v[4:7], v224, %4 offset:-3072 sc1\n\t" "s_nop 10\n\t"
                         "global_load_dwordx4 v[8:11], v224, %4 offset:-2048 sc1\n\t" "s_nop 10\n\t" "global_load_dwordx4 v[12:15], v224, %4 offset:-1024 sc1\n\t" "s_nop 10\n\t" "global_load_dwordx4 v[16:19], v224, %4 sc1\n\t"
                         "s_nop 10\n\t" "global_load_dwordx4 v[20:23], v224, %4 offset:1024 sc1\n\t" "s_nop 10\n\t" "global_load_dwordx4 v[24:27], v224, %4 offset:2048 sc1\n\t" "s_nop 10\n\t"
                         "global_load_dwordx4 v[28:31], v224, %4 offset:3072 sc1\n\t" "s_nop 10\n\t" "global_load_dwordx4 v[32:35], v224, %5 offset:-4096 sc1\n\t" "s_nop 10\n\t"
                         "global_load_dwordx4 v[36:39], v224, %5 offset:-3072 sc1\n\t" "s_waitcnt vmcnt(9)\n\t" "v_smfmac_f32_16x16x64_bf16 v[40:43], v[0:3], a[0:7], v225\n\t"
                         "v_smfmac_f32_16x16x64_bf16 v[44:47], v[0:3], v[64:71], v225\n\t" "ds_read_b128 v[64:67], v226 offset:8192\n\t" "v_smfmac_f32_16x16x64_bf16 v[48:51], v[0:3], a[8:15], v225\n\t"
                         "ds_read_b128 v[68:71], v226 offset:9216\n\t" "v_cmp_eq_u32 vcc, -1, v0\n\t" "s_waitcnt vmcnt(8)\n\t" "v_smfmac_f32_16x16x64_bf16 v[40:43], v[4:7], a[16:23], v225\n\t" "v_mov_b64 v[0:1], 0\n\t"
                         "v_smfmac_f32_16x16x64_bf16 v[44:47], v[4:7], v[72:79], v225\n\t" "s_and_b64 vcc, vcc, %6\n\t" "s_cbranch_scc1 .Lsk_u\n\t" "ds_read_b128 v[72:75], v226 offset:10240\n\t" "v_mov_b64 v[2:3], 0\n\t"
                         "v_smfmac_f32_16x16x64_bf16 v[48:51], v[4:7], a[24:31], v225\n\t" "ds_read_b128 v[76:79], v226 offset:11264\n\t" "s_waitcnt vmcnt(7)\n\t"
                         "v_smfmac_f32_16x16x64_bf16 v[40:43], v[8:11], a[32:39], v225\n\t" "v_mov_b64 v[4:5], 0\n\t" "v_smfmac_f32_16x16x64_bf16 v[44:47], v[8:11], v[80:87], v225\n\t"
                         "ds_read_b128 v[80:83], v226 offset:12288\n\t" "v_mov_b64 v[6:7], 0\n\t" "v_smfmac_f32_16x16x64_bf16 v[48:51], v[8:11], a[40:47], v225\n\t" "ds_read_b128 v[84:87], v226 offset:13312\n\t"
                         "s_waitcnt vmcnt(6)\n\t" "v_smfmac_f32_16x16x64_bf16 v[40:43], v[12:15], a[48:55], v225\n\t" "v_mov_b64 v[8:9], 0\n\t" "v_smfmac_f32_16x16x64_bf16 v[44:47], v[12:15], v[88:95], v225\n\t"
                         "ds_read_b128 v[88:91], v226 offset:14336\n\t" "v_mov_b64 v[10:11], 0\n\t" "v_smfmac_f32_16x16x64_bf16 v[48:51], v[12:15], a[56:63], v225\n\t" "ds_read_b128 v[92:95], v226 offset:15360\n\t"
                         "s_waitcnt vmcnt(5)\n\t" "v_smfmac_f32_16x16x64_bf16 v[40:43], v[16:19], a[64:71], v225\n\t" "v_mov_b64 v[12:13], 0\n\t" "s_waitcnt lgkmcnt(6)\n\t"
                         "v_smfmac_f32_16x16x64_bf16 v[44:47], v[16:19], v[64:71], v225\n\t" "ds_read_b128 v[64:67], v226 offset:16384\n\t" "v_mov_b64 v[14:15], 0\n\t"
                         "v_smfmac_f32_16x16x64_bf16 v[48:51], v[16:19], a[72:79], v225\n\t" "ds_read_b128 v[68:71], v226 offset:17408\n\t" "s_waitcnt vmcnt(4)\n\t"
                         "v_smfmac_f32_16x16x64_bf16 v[40:43], v[20:23], a[80:87], v225\n\t" "v_mov_b64 v[16:17], 0\n\t" "s_waitcnt lgkmcnt(6)\n\t" "v_smfmac_f32_16x16x64_bf16 v[44:47], v[20:23], v[72:79], v225\n\t"
                         "ds_read_b128 v[72:75], v226 offset:18432\n\t" "v_mov_b64 v[18:19], 0\n\t" "v_smfmac_f32_16x16x64_bf16 v[48:51], v[20:23], a[88:95], v225\n\t" "ds_read_b128 v[76:79], v226 offset:19456\n\t"
                         "s_waitcnt vmcnt(3)\n\t" "v_smfmac_f32_16x16x64_bf16 v[40:43], v[24:27], a[96:103], v225\n\t" "v_mov_b64 v[20:21], 0\n\t" "s_waitcnt lgkmcnt(6)\n\t"
                         "v_smfmac_f32_16x16x64_bf16 v[44:47], v[24:27], v[80:87], v225\n\t" "ds_read_b128 v[80:83], v226 offset:20480\n\t" "v_mov_b64 v[22:23], 0\n\t"
                         "v_smfmac_f32_16x16x64_bf16 v[48:51], v[24:27], a[104:111], v225\n\t" "ds_read_b128 v[84:87], v226 offset:21504\n\t" "s_waitcnt vmcnt(2)\n\t"
                         "v_smfmac_f32_16x16x64_bf16 v[40:43], v[28:31], a[112:119], v225\n\t" "v_mov_b64 v[24:25], 0\n\t" "s_waitcnt lgkmcnt(6)\n\t" "v_smfmac_f32_16x16x64_bf16 v[44:47], v[28:31], v[88:95], v225\n\t"
                         "ds_read_b128 v[88:91], v226 offset:22528\n\t" "v_mov_b64 v[26:27], 0\n\t" "v_smfmac_f32_16x16x64_bf16 v[48:51], v[28:31], a[120:127], v225\n\t" "ds_read_b128 v[92:95], v226 offset:23552\n\t"
                         "s_waitcnt vmcnt(1)\n\t" "v_smfmac_f32_16x16x64_bf16 v[40:43], v[32:35], a[128:135], v225\n\t" "v_mov_b64 v[28:29], 0\n\t" "v_smfmac_f32_16x16x64_bf16 v[44:47], v[32:35], a[136:143], v225\n\t"
                         "v_mov_b64 v[30:31], 0\n\t" "v_smfmac_f32_16x16x64_bf16 v[48:51], v[32:35], a[144:151], v225\n\t" "s_waitcnt vmcnt(0)\n\t" "v_smfmac_f32_16x16x64_bf16 v[40:43], v[36:39], a[152:159], v225\n\t"
                         "v_mov_b64 v[32:33], 0\n\t" "v_smfmac_f32_16x16x64_bf16 v[44:47], v[36:39], a[160:167], v225\n\t" "v_mov_b64 v[34:35], 0\n\t" "v_smfmac_f32_16x16x64_bf16 v[48:51], v[36:39], a[168:175], v225\n\t"
                         "v_add_f32 v52, v40, v42\n\t" "v_add_f32 v53, v41, v43\n\t" "v_cmp_u_f32 vcc, v52, v53\n\t" "v_add_f32 v54, v44, v46\n\t" "v_add_f32 v55, v45, v47\n\t" "v_cvt_pk_bf16_f32 v58, v52, v53\n\t"
                         "v_pk_add_f32 v[56:57], v[48:49], v[50:51]\n\t" "v_cvt_pk_bf16_f32 v59, v54, v55\n\t" "v_cvt_pk_bf16_f32 v60, v56, v57\n\t" "ds_write_b64 v227, v[58:59]\n\t" "ds_write_b32 v228, v60\n\t"
                         "v_mov_b64 v[36:37], 0\n\t" "v_mov_b64 v[38:39], 0\n\t" "s_and_b64 vcc, vcc, %6\n\t" "v_mov_b32 v233, 0\n\t" "s_cbranch_scc1 .Lsk_u\n\t" "v_mov_b32 v233, 0\n\t" "v_mov_b32 v233, 0\n\t"
                         "v_mov_b32 v233, 0\n\t" "s_waitcnt lgkmcnt(0)\n\t" "s_barrier\n\t" "ds_read_b64_tr_b16 v[40:41], v229\n\t" "ds_read_b64_tr_b16 v[42:43], v230\n\t" "v_mov_b32 v233, 0\n\t"
                         "ds_read_b64_tr_b16 v[44:45], v229 offset:1024\n\t" "v_mov_b32 v233, 0\n\t" "ds_read_b64_tr_b16 v[46:47], v230 offset:1024\n\t" "v_mov_b32 v233, 0\n\t"
                         "ds_read_b64_tr_b16 v[48:49], v229 offset:2048\n\t" "v_mov_b32 v233, 0\n\t" "ds_read_b64_tr_b16 v[50:51], v230 offset:2048\n\t" "v_mov_b32 v233, 0\n\t" "s_waitcnt lgkmcnt(4)\n\t"
                         "v_smfmac_f32_16x16x64_bf16 v[0:3], v[40:43], a[176:183], v225\n\t" "v_smfmac_f32_16x16x64_bf16 v[4:7], v[40:43], a[200:207], v225\n\t" "s_waitcnt lgkmcnt(2)\n\t"
                         "v_smfmac_f32_16x16x64_bf16 v[0:3], v[44:47], a[184:191], v225\n\t" "v_smfmac_f32_16x16x64_bf16 v[4:7], v[44:47], a[208:215], v225\n\t" "s_waitcnt lgkmcnt(0)\n\t"
                         "v_smfmac_f32_16x16x64_bf16 v[0:3], v[48:51], a[192:199], v225\n\t" "v_smfmac_f32_16x16x64_bf16 v[4:7], v[48:51], a[216:223], v225\n\t"
                         "v_smfmac_f32_16x16x64_bf16 v[8:11], v[40:43], a[224:231], v225\n\t" "v_add_f32 v52, v0, v2\n\t" "v_add_f32 v53, v1, v3\n\t" "v_cvt_pk_bf16_f32 v54, v53, v52\n\t"
                         "v_smfmac_f32_16x16x64_bf16 v[8:11], v[44:47], a[232:239], v225\n\t" "v_add_f32 v52, v4, v6\n\t" "v_add_f32 v53, v5, v7\n\t" "v_cvt_pk_bf16_f32 v55, v53, v52\n\t" "s_nop 1\n\t"
                         "v_permlane16_swap_b32 v54, v55\n\t" "v_smfmac_f32_16x16x64_bf16 v[8:11], v[48:51], a[240:247], v225\n\t" "global_store_dwordx2 v232, v[54:55], %2 sc1\n\t" "s_add_u32 s40, s40, 0x10000\n\t"
                         "s_addc_u32 s41, s41, 0\n\t" "v_smfmac_f32_16x16x64_bf16 v[12:15], v[40:43], a[248:255], v225\n\t" "v_smfmac_f32_16x16x64_bf16 v[12:15], v[44:47], v[96:103], v225\n\t" "v_add_f32 v52, v8, v10\n\t"
                         "v_smfmac_f32_16x16x64_bf16 v[12:15], v[48:51], v[104:111], v225\n\t" "v_add_f32 v53, v9, v11\n\t" "v_smfmac_f32_16x16x64_bf16 v[16:19], v[40:43], v[112:119], v225\n\t"
                         "v_cvt_pk_bf16_f32 v56, v53, v52\n\t" "v_smfmac_f32_16x16x64_bf16 v[16:19], v[44:47], v[120:127], v225\n\t" "v_add_f32 v52, v12, v14\n\t" "v_add_f32 v53, v13, v15\n\t"
                         "v_cvt_pk_bf16_f32 v57, v53, v52\n\t" "s_nop 1\n\t" "v_permlane16_swap_b32 v56, v57\n\t" "v_smfmac_f32_16x16x64_bf16 v[16:19], v[48:51], v[128:135], v225\n\t"
                         "global_store_dwordx2 v232, v[56:57], %2 sc1\n\t" "s_add_u32 s40, s40, 0x10000\n\t" "s_addc_u32 s41, s41, 0\n\t" "v_smfmac_f32_16x16x64_bf16 v[20:23], v[40:43], v[136:143], v225\n\t"
                         "v_smfmac_f32_16x16x64_bf16 v[20:23], v[44:47], v[144:151], v225\n\t" "v_add_f32 v52, v16, v18\n\t" "v_smfmac_f32_16x16x64_bf16 v[20:23], v[48:51], v[152:159], v225\n\t" "v_add_f32 v53, v17, v19\n\t"
                         "v_smfmac_f32_16x16x64_bf16 v[24:27], v[40:43], v[160:167], v225\n\t" "v_cvt_pk_bf16_f32 v58, v53, v52\n\t" "v_smfmac_f32_16x16x64_bf16 v[24:27], v[44:47], v[168:175], v225\n\t"
                         "v_add_f32 v52, v20, v22\n\t" "v_add_f32 v53, v21, v23\n\t" "v_smfmac_f32_16x16x64_bf16 v[24:27], v[48:51], v[176:183], v225\n\t" "v_cvt_pk_bf16_f32 v59, v53, v52\n\t"
                         "v_smfmac_f32_16x16x64_bf16 v[28:31], v[40:43], v[184:191], v225\n\t" "s_nop 0\n\t" "v_permlane16_swap_b32 v58, v59\n\t" "v_smfmac_f32_16x16x64_bf16 v[28:31], v[44:47], v[192:199], v225\n\t"
                         "global_store_dwordx2 v232, v[58:59], %2 sc1\n\t" "v_add_f32 v52, v24, v26\n\t" "v_smfmac_f32_16x16x64_bf16 v[28:31], v[48:51], v[200:207], v225\n\t" "v_add_f32 v53, v25, v27\n\t"
                         "v_smfmac_f32_16x16x64_bf16 v[32:35], v[40:43], v[208:215], v225\n\t" "v_cvt_pk_bf16_f32 v60, v53, v52\n\t" "v_smfmac_f32_16x16x64_bf16 v[32:35], v[44:47], v[216:223], v225\n\t"
                         "v_add_f32 v52, v28, v30\n\t" "v_smfmac_f32_16x16x64_bf16 v[32:35], v[48:51], v[64:71], v225\n\t" "v_add_f32 v53, v29, v31\n\t" "v_smfmac_f32_16x16x64_bf16 v[36:39], v[40:43], v[72:79], v225\n\t"
                         "v_cvt_pk_bf16_f32 v61, v53, v52\n\t" "v_smfmac_f32_16x16x64_bf16 v[36:39], v[44:47], v[80:87], v225\n\t" "v_add_f32 v52, v32, v34\n\t"
                         "v_smfmac_f32_16x16x64_bf16 v[36:39], v[48:51], v[88:95], v225\n\t" "v_add_f32 v53, v33, v35\n\t" "v_cvt_pk_bf16_f32 v62, v53, v52\n\t" "v_mov_b32 v233, 0\n\t" "v_mov_b32 v233, 0\n\t"
                         "v_mov_b32 v233, 0\n\t" "v_mov_b32 v233, 0\n\t" "v_mov_b32 v233, 0\n\t" "v_pk_add_f32 v[52:53], v[36:37], v[38:39]\n\t" "v_cvt_pk_bf16_f32 v63, v53, v52\n\t" "v_permlane32_swap_b32 v60, v62\n\t"
                         "s_nop 0\n\t" "v_permlane32_swap_b32 v61, v63\n\t" "s_nop 1\n\t" "v_permlane16_swap_b32 v60, v61\n\t" "v_permlane16_swap_b32 v62, v63\n\t" "global_store_dwordx4 v231, v[60:63], %2 sc1\n\t"
                         ".Lsk_u:\n\t" "s_memrealtime %1\n\t" "s_mov_b64 %0, vcc\n\t" "s_waitcnt lgkmcnt(0)\n\t"
                         : "=&s"(bad), "=&s"(tp8), "+{s[40:41]}"(up)
                         : "s"(uni((int)tgt)), "s"(memp), "s"(memp + 16384), "s"(en), "{v[224:239]}"(ad),
                           "{a[0:31]}"(wa[0]), "{a[32:63]}"(wa[1]), "{a[64:95]}"(wa[2]), "{a[96:127]}"(wa[3]), "{a[128:159]}"(wa[4]), "{a[160:191]}"(wa[5]), "{a[192:223]}"(wa[6]), "{a[224:255]}"(wa[7]), "{v[96:127]}"(wv[0]), "{v[128:159]}"(wv[1]), "{v[160:191]}"(wv[2]), "{v[192:223]}"(wv[3])
                         : "v0", "v1", "v2", "v3", "v4", "v5", "v6", "v7", "v8", "v9", "v10", "v11", "v12", "v13", "v14", "v15", "v16", "v17", "v18", "v19", "v20", "v21", "v22", "v23", "v24", "v25", "v26", "v27", "v28", "v29", "v30", "v31", "v32", "v33", "v34", "v35", "v36", "v37", "v38", "v39", "v40", "v41", "v42", "v43", "v44", "v45", "v46", "v47", "v48", "v49", "v50", "v51", "v52", "v53", "v54", "v55", "v56", "v57", "v58", "v59", "v60", "v61", "v62", "v63", "v64", "v65", "v66", "v67", "v68", "v69", "v70", "v71", "v72", "v73", "v74", "v75", "v76", "v77", "v78", "v79", "v80", "v81", "v82", "v83", "v84", "v85", "v86", "v87", "v88", "v89", "v90", "v91", "v92", "v93", "v94", "v95", "vcc", "scc", "s42", "memory");
            if (__builtin_expect(bad == 0, 1)) break;
            waitvm<0>();
            if (--budget < 0 || ((++tries & 15) == 0 && !unwritten(memp, ad[0]))) en = 0;
            phi += (u32)cfg.G;
        }
        clk.calib();
        const u32 tt = sched_ld0(s);
        if (s >= NSEG - 2) waitvm<0>();
        barrier();
        if (s >= NSEG - 2 && w == 3) flag_add(fl, F_PROG + 8 * s + (h & 7), 1);
        if (s >= NSEG - 2 && w == 2) flag_add(fl, F_UD + 2 * (s & 1) + (h & 1), 1);
        follow(phi, (int)(tt >> 16) + 4 + cfg.T3 - VISS, (u32)tp8, cfg.G);
        phi += tt & 0xffffu;
        memp = p.ws + mslot(sd, s + 1);
    }
    asm volatile(".Lkm_lu:\n\ts_waitcnt vmcnt(0)" : : : "memory");
}

#define VALL "{v[0:3]}"(o), "{v[16:47]}"(rv[0]), "{v[48:79]}"(rv[1]), "{v[80:111]}"(rv[2]), "{v[112:143]}"(rv[3]), "{v[144:175]}"(rv[4]), "{v[176:207]}"(rv[5]), "{v[208:239]}"(rv[6]), "{a[0:31]}"(ra[0]), "{a[32:63]}"(ra[1]), "{a[64:95]}"(ra[2]), "{a[96:127]}"(ra[3])
DI void value_role(const Ptrs& p, const ChainCfg cfg, int vb, int sd, u32 phi, u32 lds, int w) {
    const int lane = lanex(), tid = 64 * w + lane, n = lane & 15, g = lane >> 4;
    const int v = 2 * (vb & 3) + ((vb >> 2) & 1) + (vb & ~7);
    const u32 moff = lane * 16;
    char* const fl = p.ws + OFF_ST;
    char* const ub = p.ws + (size_t)v * 8192;
    const int xh = (vb >> 2) & 1;
    if (w == 3) {
        const u32x4 ff = u32x4{~0u, ~0u, ~0u, ~0u};
#pragma nounroll
        for (int s = 0; s < NSEG - 2; ++s) {
            dly(cfg.cR3 >> 8 & 255);
            char* const mk = ub + uslot(sd, s + 2);
#pragma unroll
            for (int i = 0; i < 8; ++i) { gst16(mk + i * 1024, moff, ff); gap<HQ>(-1); }
            if (vb < 32 && s) {
                gst16(p.ws + (size_t)(s + 2) * HO_STRIDE + vb * 2048, moff, ff);
                gst16(p.ws + (size_t)(s + 2) * HO_STRIDE + vb * 2048 + 1024, moff, ff);
            }
            waitvm<0>();
            barrier();
        }
        return;
    }
    if (w) {
        const u32 dof = (u32)((n >> 3) * 16384 + g * 128 + (n & 7) * 16);
        const char* dp = p.ws + OFF_D + (size_t)v * 32768 - 4096 + w * 8192;
        flag_wait(fl, F_VD + (vb & 3) + (v < 20 ? 0 : 4), 32);
#pragma nounroll
        for (int s = 0; s < NSEG - 1; ++s) {
            u32x4 df[16];
            u32 dfl;
            dly(cfg.cR3 & 255);
            sfor<0, 16>([&](auto I) { gld16c<I.v * 512 - 4096>(df[I.v], dp, dof); gap<HP>(1 - s); });
            dp += D_STRIDE;
            flag_ldm(dfl, fl, F_VD + pblk(s + 1) * 8 + (vb & RM), pblk(s + 1) != pblk(s));
            const u32 la = lds + moff + (s & 1) * 32768 + (w - 1) * 16384;
            sfor<0, 8>([&](auto I) {
                constexpr int i = I.v;
                waitv<15 - 2 * i>(df[2 * i]); waitv<15 - 2 * i>(df[2 * i + 1]);
                const u32x4 x = df[2 * i], y = df[2 * i + 1];
                const u32 lb = i & 2 ? la - xh * 4096 : la + xh * 4096;
                *LDSP(u32x4, lb + 2048 * i) = u32x4{x[0], y[0], x[2], y[2]};
                *LDSP(u32x4, lb + 2048 * i + 1024) = u32x4{x[1], y[1], x[3], y[3]};
                PIN();
            });
            waitvm<0>();
            asm volatile("" : "+v"(dfl));
            barrier();
            if (uni((int)dfl) < 31) flag_wait(fl, F_VD + pblk(s + 1) * 8 + (vb & RM), 32);
        }
        return;
    }
    u32x32 rv[7], ra[4];
    u32x4 o;
    asm volatile("v_accvgpr_write_b32 a64, 0\n\t" "v_accvgpr_write_b32 a65, 0\n\t" "v_accvgpr_write_b32 a66, 0\n\t" "v_accvgpr_write_b32 a67, 0\n\t" "v_accvgpr_write_b32 a68, 0\n\t" "v_accvgpr_write_b32 a69, 0\n\t"
                 "v_accvgpr_write_b32 a70, 0\n\t" "v_accvgpr_write_b32 a71, 0\n\t" "v_accvgpr_write_b32 a72, 0\n\t" "v_accvgpr_write_b32 a73, 0\n\t" "v_accvgpr_write_b32 a74, 0\n\t" "v_accvgpr_write_b32 a75, 0\n\t"
                 "v_accvgpr_write_b32 a76, 0\n\t" "v_accvgpr_write_b32 a77, 0\n\t" "v_accvgpr_write_b32 a78, 0\n\t" "v_accvgpr_write_b32 a79, 0\n\t" "v_mov_b64 v[112:113], -1\n\t" "v_mov_b64 v[120:121], -1\n\t"
                 "v_mov_b64 v[128:129], -1\n\t" "v_mov_b64 v[136:137], -1\n\t" "v_mov_b64 v[144:145], -1\n\t" "v_mov_b64 v[152:153], -1\n\t" "v_mov_b64 v[160:161], -1\n\t" "v_mov_b64 v[168:169], -1\n\t"
                 "v_mov_b64 v[176:177], -1\n\t" "v_mov_b64 v[184:185], -1\n\t" "v_mov_b64 v[192:193], -1\n\t" "v_mov_b64 v[200:201], -1\n\t" "v_mov_b64 v[208:209], -1\n\t" "v_mov_b64 v[216:217], -1\n\t"
                 "v_mov_b64 v[224:225], -1\n\t" "v_mov_b64 v[232:233], -1\n\t"
                 : "={v[0:3]}"(o), "={v[16:47]}"(rv[0]), "={v[48:79]}"(rv[1]), "={v[80:111]}"(rv[2]), "={v[112:143]}"(rv[3]), "={v[144:175]}"(rv[4]), "={v[176:207]}"(rv[5]), "={v[208:239]}"(rv[6]), "={a[0:31]}"(ra[0]), "={a[32:63]}"(ra[1]), "={a[64:95]}"(ra[2]), "={a[96:127]}"(ra[3]));
    const u32 ho = (u32)(min(lane, 39) * 256 + (lane >> 5) * 8192);
    const u32 pof = (u32)((n & 4) * 128 + (n >> 3) * 256 + g * 64 + (n & 3) * 16 + 2048);
    const u32 uof = (u32)((n >> 3) * 4096 + g * 128 + (n & 7) * 16 + 512);
    u32x8 ad = u32x8{uof + xh * 512, pof + xh * 2048, (u32)(((g >> 1) ^ xh) * 256 + (n >> 2) * 64 + (n & 1) * 16), moff,
                     (lane & 2) ? 0xEEEEu : 0x4444u, (lane & 4) ? 0xEEEEu : 0x4444u, (u32)(v * 256 + (v >> 5) * 8192 + g * 64 + (n >> 3) * 32 + (n & 1) * 16 + (n & 6) * 2),
                     (lane & 1) ? 0x07060302u : 0x01000504u};
    u32 pob = pof - xh * 2048, uob = uof - xh * 512, pmo = moff ^ xh * 512;
    asm volatile("" : "+{v[4:11]}"(ad), "+v"(pob), "+v"(uob), "+v"(pmo));
    auto zero = [&]() {
        asm volatile("v_mov_b64 v[0:1], 0\n\t" "v_mov_b64 v[2:3], 0\n\t" "v_mov_b64 v[48:49], 0\n\t" "v_mov_b64 v[50:51], 0\n\t" "v_mov_b64 v[52:53], 0\n\t" "v_mov_b64 v[54:55], 0\n\t" "v_mov_b64 v[56:57], 0\n\t"
                     "v_mov_b64 v[58:59], 0\n\t" "v_mov_b64 v[60:61], 0\n\t" "v_mov_b64 v[62:63], 0\n\t" "v_mov_b64 v[64:65], 0\n\t" "v_mov_b64 v[66:67], 0\n\t" "v_mov_b64 v[68:69], 0\n\t" "v_mov_b64 v[70:71], 0\n\t"
                     "v_mov_b64 v[72:73], 0\n\t" "v_mov_b64 v[74:75], 0\n\t" "v_mov_b64 v[76:77], 0\n\t" "v_mov_b64 v[78:79], 0\n\t" "v_mov_b64 v[80:81], 0\n\t" "v_mov_b64 v[82:83], 0\n\t" "v_mov_b64 v[84:85], 0\n\t"
                     "v_mov_b64 v[86:87], 0\n\t" "v_mov_b64 v[88:89], 0\n\t" "v_mov_b64 v[90:91], 0\n\t" "v_mov_b64 v[92:93], 0\n\t" "v_mov_b64 v[94:95], 0\n\t" "v_mov_b64 v[96:97], 0\n\t" "v_mov_b64 v[98:99], 0\n\t"
                     "v_mov_b64 v[100:101], 0\n\t" "v_mov_b64 v[102:103], 0\n\t" "v_mov_b64 v[104:105], 0\n\t" "v_mov_b64 v[106:107], 0\n\t" "v_mov_b64 v[108:109], 0\n\t" "v_mov_b64 v[110:111], 0\n\t"
                     : : VALL : "memory");
    };
    const char* memp = p.ws + OFF_XF + 4096;
    char* up = ub + OFF_XF;
    int budget = 30000;
    Clock clk; clk.init();
    u32 tt = 0;
    for (int s = -1; s < NSEG - 1; ++s) {
        const char* const mnx = p.ws + mslot(sd, s + 1);
        bool all = false;
        int tries = 0;
        u64 tp8;
        while (true) {
            const u32 tgt = clk.sched(phi, (int)(tt >> 16), cfg.G, tries ? 2 : s > 0) - (s < 0 ? 1u << 20 : 0u);
            u64 ok;
            asm volatile("s_cmp_lg_u32 %11, 0\n\t" "s_cbranch_scc0 .Lsp_v0\n\t" ".Lsp_v3:\n\t" "s_memtime vcc\n\t" "s_waitcnt lgkmcnt(0)\n\t" "s_sub_i32 s42, %10, vcc_lo\n\t" "s_cmp_gt_i32 s42, 80\n\t"
                         "s_cbranch_scc1 .Lsp_v3\n\t" "s_max_i32 s42, s42, 0\n\t" "s_sub_i32 s42, 92, s42\n\t" "s_and_b32 s42, s42, -4\n\t" "s_getpc_b64 vcc\n\t" "s_add_u32 vcc_lo, vcc_lo, s42\n\t"
                         "s_addc_u32 vcc_hi, vcc_hi, 0\n\t" "s_setpc_b64 vcc\n\t" "s_nop 0\n\t" "s_nop 0\n\t" "s_nop 0\n\t" "s_nop 0\n\t" "s_nop 0\n\t" "s_nop 0\n\t" "s_nop 0\n\t" "s_nop 0\n\t" "s_nop 0\n\t" "s_nop 0\n\t"
                         "s_nop 0\n\t" "s_nop 0\n\t" "s_nop 0\n\t" "s_nop 0\n\t" "s_nop 0\n\t" "s_nop 0\n\t" "s_nop 0\n\t" "s_nop 0\n\t" "s_nop 0\n\t" "s_nop 0\n\t"
                         "global_load_dwordx4 a[0:3], v5, %7 offset:-4096 sc1\n\t" "global_load_dwordx4 a[4:7], v5, %7 offset:-3072 sc1\n\t" "global_load_dwordx4 a[16:19], v5, %7 sc1\n\t"
                         "global_load_dwordx4 a[20:23], v5, %7 offset:1024 sc1\n\t" "global_load_dwordx4 a[32:35], v5, %8 offset:-4096 sc1\n\t" "global_load_dwordx4 a[36:39], v5, %8 offset:-3072 sc1\n\t"
                         "global_load_dwordx4 a[48:51], v5, %8 sc1\n\t" "global_load_dwordx4 a[52:55], v5, %8 offset:1024 sc1\n\t" "s_nop 14\n\t" "global_load_dwordx4 a[8:11], %12, %7 offset:-2048 sc1\n\t"
                         "global_load_dwordx4 a[12:15], %12, %7 offset:-1024 sc1\n\t" "global_load_dwordx4 a[24:27], %12, %7 offset:2048 sc1\n\t" "global_load_dwordx4 a[28:31], %12, %7 offset:3072 sc1\n\t"
                         "global_load_dwordx4 a[40:43], %12, %8 offset:-2048 sc1\n\t" "global_load_dwordx4 a[44:47], %12, %8 offset:-1024 sc1\n\t" "global_load_dwordx4 a[56:59], %12, %8 offset:2048 sc1\n\t"
                         "global_load_dwordx4 a[60:63], %12, %8 offset:3072 sc1\n\t" "s_nop 15\n\t" "s_nop 5\n\t" "s_mov_b64 exec, %9\n\t" "global_load_dwordx4 a[64:67], v6, %6 offset:-2048 sc1\n\t"
                         "global_load_dwordx4 a[68:71], v6, %6 offset:-1536 sc1\n\t" "global_load_dwordx4 a[72:75], v6, %6 offset:-1024 sc1\n\t" "global_load_dwordx4 a[76:79], v6, %6 offset:-512 sc1\n\t"
                         "s_mov_b64 exec, -1\n\t" "global_load_dwordx4 a[80:83], %14, %6 sc1\n\t" "global_load_dwordx4 a[84:87], %14, %6 offset:1024 sc1\n\t" "global_load_dwordx4 a[88:91], %14, %6 offset:2048 sc1\n\t"
                         "global_load_dwordx4 a[92:95], %14, %6 offset:3072 sc1\n\t" "s_mov_b32 s42, %11\n\t" ".Lsp_v5:\n\t" "s_sub_u32 s42, s42, 1\n\t" "s_cmp_gt_i32 s42, 0\n\t" "s_cbranch_scc1 .Lsp_v5\n\t"
                         "global_load_dwordx4 a[96:99], v4, %3 offset:-512 sc1\n\t" "global_load_dwordx4 a[100:103], %13, %3 sc1\n\t" "global_load_dwordx4 a[104:107], v4, %3 offset:512 sc1\n\t"
                         "global_load_dwordx4 a[108:111], %13, %3 offset:1024 sc1\n\t" "global_load_dwordx4 a[112:115], v4, %3 offset:1536 sc1\n\t" "global_load_dwordx4 a[116:119], %13, %3 offset:2048 sc1\n\t"
                         "global_load_dwordx4 a[120:123], v4, %3 offset:2560 sc1\n\t" "global_load_dwordx4 a[124:127], %13, %3 offset:3072 sc1\n\t" "s_waitcnt vmcnt(31)\n\t"
                         "v_smfmac_f32_16x16x64_bf16 v[48:51], a[0:3], v[112:119], v9\n\t" "s_waitcnt vmcnt(30)\n\t" "v_smfmac_f32_16x16x64_bf16 v[52:55], a[4:7], v[120:127], v9\n\t" "s_waitcnt vmcnt(29)\n\t"
                         "v_smfmac_f32_16x16x64_bf16 v[64:67], a[16:19], v[144:151], v9\n\t" "s_waitcnt vmcnt(28)\n\t" "v_cvt_pk_bf16_f32 v16, v48, v49\n\t" "v_cvt_pk_bf16_f32 v17, v50, v51\n\t"
                         "v_smfmac_f32_16x16x64_bf16 v[68:71], a[20:23], v[152:159], v9\n\t" "s_waitcnt vmcnt(27)\n\t" "v_cvt_pk_bf16_f32 v18, v52, v53\n\t" "v_cvt_pk_bf16_f32 v19, v54, v55\n\t"
                         "v_smfmac_f32_16x16x64_bf16 v[80:83], a[32:35], v[176:183], v9\n\t" "s_waitcnt vmcnt(26)\n\t" "v_cvt_pk_bf16_f32 v24, v64, v65\n\t" "v_cvt_pk_bf16_f32 v25, v66, v67\n\t"
                         "v_smfmac_f32_16x16x64_bf16 v[84:87], a[36:39], v[184:191], v9\n\t" "s_waitcnt vmcnt(25)\n\t" "v_cvt_pk_bf16_f32 v26, v68, v69\n\t" "v_cvt_pk_bf16_f32 v27, v70, v71\n\t"
                         "v_smfmac_f32_16x16x64_bf16 v[96:99], a[48:51], v[208:215], v9\n\t" "s_waitcnt vmcnt(24)\n\t" "v_cvt_pk_bf16_f32 v32, v80, v81\n\t" "v_cvt_pk_bf16_f32 v33, v82, v83\n\t"
                         "v_smfmac_f32_16x16x64_bf16 v[100:103], a[52:55], v[216:223], v9\n\t" "s_waitcnt vmcnt(23)\n\t" "v_cvt_pk_bf16_f32 v34, v84, v85\n\t" "v_cvt_pk_bf16_f32 v35, v86, v87\n\t"
                         "v_smfmac_f32_16x16x64_bf16 v[56:59], a[8:11], v[128:135], v9\n\t" "s_waitcnt vmcnt(22)\n\t" "v_cvt_pk_bf16_f32 v40, v96, v97\n\t" "v_cvt_pk_bf16_f32 v41, v98, v99\n\t"
                         "v_smfmac_f32_16x16x64_bf16 v[60:63], a[12:15], v[136:143], v9\n\t" "s_waitcnt vmcnt(21)\n\t" "v_cvt_pk_bf16_f32 v42, v100, v101\n\t" "v_cvt_pk_bf16_f32 v43, v102, v103\n\t"
                         "v_smfmac_f32_16x16x64_bf16 v[72:75], a[24:27], v[160:167], v9\n\t" "s_waitcnt vmcnt(20)\n\t" "v_cvt_pk_bf16_f32 v20, v56, v57\n\t" "v_cvt_pk_bf16_f32 v21, v58, v59\n\t"
                         "v_smfmac_f32_16x16x64_bf16 v[76:79], a[28:31], v[168:175], v9\n\t" "s_waitcnt vmcnt(19)\n\t" "v_cvt_pk_bf16_f32 v22, v60, v61\n\t" "v_cvt_pk_bf16_f32 v23, v62, v63\n\t"
                         "v_smfmac_f32_16x16x64_bf16 v[88:91], a[40:43], v[192:199], v9\n\t" "s_waitcnt vmcnt(18)\n\t" "v_cvt_pk_bf16_f32 v28, v72, v73\n\t" "v_cvt_pk_bf16_f32 v29, v74, v75\n\t"
                         "v_smfmac_f32_16x16x64_bf16 v[92:95], a[44:47], v[200:207], v9\n\t" "s_waitcnt vmcnt(17)\n\t" "v_cvt_pk_bf16_f32 v30, v76, v77\n\t" "v_cvt_pk_bf16_f32 v31, v78, v79\n\t"
                         "v_smfmac_f32_16x16x64_bf16 v[104:107], a[56:59], v[224:231], v9\n\t" "s_waitcnt vmcnt(16)\n\t" "v_cvt_pk_bf16_f32 v36, v88, v89\n\t" "v_cvt_pk_bf16_f32 v37, v90, v91\n\t"
                         "v_smfmac_f32_16x16x64_bf16 v[108:111], a[60:63], v[232:239], v9\n\t" "s_waitcnt vmcnt(15)\n\t" "v_cvt_pk_bf16_f32 v38, v92, v93\n\t" "v_cvt_pk_bf16_f32 v39, v94, v95\n\t"
                         "v_smfmac_f32_16x16x64_bf16 v[0:3], a[64:67], v[16:23], v8\n\t" "s_waitcnt vmcnt(14)\n\t" "v_cvt_pk_bf16_f32 v44, v104, v105\n\t" "v_cvt_pk_bf16_f32 v45, v106, v107\n\t"
                         "v_smfmac_f32_16x16x64_bf16 v[0:3], a[68:71], v[24:31], v8\n\t" "s_waitcnt vmcnt(13)\n\t" "v_cvt_pk_bf16_f32 v46, v108, v109\n\t" "v_cvt_pk_bf16_f32 v47, v110, v111\n\t"
                         "v_smfmac_f32_16x16x64_bf16 v[0:3], a[72:75], v[32:39], v8\n\t" "s_waitcnt vmcnt(12)\n\t" "v_smfmac_f32_16x16x64_bf16 v[0:3], a[76:79], v[40:47], v8\n\t" "s_waitcnt vmcnt(6)\n\t"
                         "v_smfmac_f32_16x16x64_bf16 v[0:3], a[80:83], a[96:103], v8\n\t" "s_waitcnt vmcnt(4)\n\t" "v_smfmac_f32_16x16x64_bf16 v[0:3], a[84:87], a[104:111], v8\n\t" "s_waitcnt vmcnt(2)\n\t"
                         "v_smfmac_f32_16x16x64_bf16 v[0:3], a[88:91], a[112:119], v8\n\t" "s_waitcnt vmcnt(0)\n\t" "v_smfmac_f32_16x16x64_bf16 v[0:3], a[92:95], a[120:127], v8\n\t" "s_branch .Lv_end\n\t" ".Lsp_v0:\n\t"
                         "s_memtime vcc\n\t" "s_waitcnt lgkmcnt(0)\n\t" "s_sub_i32 s42, %10, vcc_lo\n\t" "s_cmp_gt_i32 s42, 80\n\t" "s_cbranch_scc1 .Lsp_v0\n\t" "s_max_i32 s42, s42, 0\n\t" "s_sub_i32 s42, 92, s42\n\t"
                         "s_and_b32 s42, s42, -4\n\t" "s_getpc_b64 vcc\n\t" "s_add_u32 vcc_lo, vcc_lo, s42\n\t" "s_addc_u32 vcc_hi, vcc_hi, 0\n\t" "s_setpc_b64 vcc\n\t" "s_nop 0\n\t" "s_nop 0\n\t" "s_nop 0\n\t" "s_nop 0\n\t"
                         "s_nop 0\n\t" "s_nop 0\n\t" "s_nop 0\n\t" "s_nop 0\n\t" "s_nop 0\n\t" "s_nop 0\n\t" "s_nop 0\n\t" "s_nop 0\n\t" "s_nop 0\n\t" "s_nop 0\n\t" "s_nop 0\n\t" "s_nop 0\n\t" "s_nop 0\n\t" "s_nop 0\n\t"
                         "s_nop 0\n\t" "s_nop 0\n\t" "global_load_dwordx4 a[96:99], v4, %3 offset:-512 sc1\n\t" "global_load_dwordx4 a[104:107], v4, %3 offset:512 sc1\n\t"
                         "global_load_dwordx4 a[112:115], v4, %3 offset:1536 sc1\n\t" "global_load_dwordx4 a[120:123], v4, %3 offset:2560 sc1\n\t" "global_load_dwordx4 a[100:103], %13, %3 sc1\n\t"
                         "global_load_dwordx4 a[108:111], %13, %3 offset:1024 sc1\n\t" "global_load_dwordx4 a[116:119], %13, %3 offset:2048 sc1\n\t" "global_load_dwordx4 a[124:127], %13, %3 offset:3072 sc1\n\t" "s_nop 9\n\t"
                         "global_load_dwordx4 a[0:3], v5, %7 offset:-4096 sc1\n\t" "global_load_dwordx4 a[4:7], v5, %7 offset:-3072 sc1\n\t" "global_load_dwordx4 a[16:19], v5, %7 sc1\n\t"
                         "global_load_dwordx4 a[20:23], v5, %7 offset:1024 sc1\n\t" "global_load_dwordx4 a[32:35], v5, %8 offset:-4096 sc1\n\t" "global_load_dwordx4 a[36:39], v5, %8 offset:-3072 sc1\n\t"
                         "global_load_dwordx4 a[48:51], v5, %8 sc1\n\t" "global_load_dwordx4 a[52:55], v5, %8 offset:1024 sc1\n\t" "s_nop 14\n\t" "global_load_dwordx4 a[8:11], %12, %7 offset:-2048 sc1\n\t"
                         "global_load_dwordx4 a[12:15], %12, %7 offset:-1024 sc1\n\t" "global_load_dwordx4 a[24:27], %12, %7 offset:2048 sc1\n\t" "global_load_dwordx4 a[28:31], %12, %7 offset:3072 sc1\n\t"
                         "global_load_dwordx4 a[40:43], %12, %8 offset:-2048 sc1\n\t" "global_load_dwordx4 a[44:47], %12, %8 offset:-1024 sc1\n\t" "global_load_dwordx4 a[56:59], %12, %8 offset:2048 sc1\n\t"
                         "global_load_dwordx4 a[60:63], %12, %8 offset:3072 sc1\n\t" "s_nop 15\n\t" "s_nop 5\n\t" "global_load_dwordx4 a[80:83], %14, %6 sc1\n\t"
                         "global_load_dwordx4 a[84:87], %14, %6 offset:1024 sc1\n\t" "global_load_dwordx4 a[88:91], %14, %6 offset:2048 sc1\n\t" "global_load_dwordx4 a[92:95], %14, %6 offset:3072 sc1\n\t"
                         "s_mov_b64 exec, %9\n\t" "global_load_dwordx4 a[64:67], v6, %6 offset:-2048 sc1\n\t" "global_load_dwordx4 a[68:71], v6, %6 offset:-1536 sc1\n\t"
                         "global_load_dwordx4 a[72:75], v6, %6 offset:-1024 sc1\n\t" "global_load_dwordx4 a[76:79], v6, %6 offset:-512 sc1\n\t" "s_mov_b64 exec, -1\n\t" "s_waitcnt vmcnt(23)\n\t"
                         "v_smfmac_f32_16x16x64_bf16 v[48:51], a[0:3], v[112:119], v9\n\t" "s_waitcnt vmcnt(22)\n\t" "v_smfmac_f32_16x16x64_bf16 v[52:55], a[4:7], v[120:127], v9\n\t" "s_waitcnt vmcnt(21)\n\t"
                         "v_smfmac_f32_16x16x64_bf16 v[64:67], a[16:19], v[144:151], v9\n\t" "s_waitcnt vmcnt(20)\n\t" "v_cvt_pk_bf16_f32 v16, v48, v49\n\t" "v_cvt_pk_bf16_f32 v17, v50, v51\n\t"
                         "v_smfmac_f32_16x16x64_bf16 v[68:71], a[20:23], v[152:159], v9\n\t" "s_waitcnt vmcnt(19)\n\t" "v_cvt_pk_bf16_f32 v18, v52, v53\n\t" "v_cvt_pk_bf16_f32 v19, v54, v55\n\t"
                         "v_smfmac_f32_16x16x64_bf16 v[80:83], a[32:35], v[176:183], v9\n\t" "s_waitcnt vmcnt(18)\n\t" "v_cvt_pk_bf16_f32 v24, v64, v65\n\t" "v_cvt_pk_bf16_f32 v25, v66, v67\n\t"
                         "v_smfmac_f32_16x16x64_bf16 v[84:87], a[36:39], v[184:191], v9\n\t" "s_waitcnt vmcnt(17)\n\t" "v_cvt_pk_bf16_f32 v26, v68, v69\n\t" "v_cvt_pk_bf16_f32 v27, v70, v71\n\t"
                         "v_smfmac_f32_16x16x64_bf16 v[96:99], a[48:51], v[208:215], v9\n\t" "s_waitcnt vmcnt(16)\n\t" "v_cvt_pk_bf16_f32 v32, v80, v81\n\t" "v_cvt_pk_bf16_f32 v33, v82, v83\n\t"
                         "v_smfmac_f32_16x16x64_bf16 v[100:103], a[52:55], v[216:223], v9\n\t" "s_waitcnt vmcnt(15)\n\t" "v_cvt_pk_bf16_f32 v34, v84, v85\n\t" "v_cvt_pk_bf16_f32 v35, v86, v87\n\t"
                         "v_smfmac_f32_16x16x64_bf16 v[56:59], a[8:11], v[128:135], v9\n\t" "s_waitcnt vmcnt(14)\n\t" "v_cvt_pk_bf16_f32 v40, v96, v97\n\t" "v_cvt_pk_bf16_f32 v41, v98, v99\n\t"
                         "v_smfmac_f32_16x16x64_bf16 v[60:63], a[12:15], v[136:143], v9\n\t" "s_waitcnt vmcnt(13)\n\t" "v_cvt_pk_bf16_f32 v42, v100, v101\n\t" "v_cvt_pk_bf16_f32 v43, v102, v103\n\t"
                         "v_smfmac_f32_16x16x64_bf16 v[72:75], a[24:27], v[160:167], v9\n\t" "s_waitcnt vmcnt(12)\n\t" "v_cvt_pk_bf16_f32 v20, v56, v57\n\t" "v_cvt_pk_bf16_f32 v21, v58, v59\n\t"
                         "v_smfmac_f32_16x16x64_bf16 v[76:79], a[28:31], v[168:175], v9\n\t" "s_waitcnt vmcnt(11)\n\t" "v_cvt_pk_bf16_f32 v22, v60, v61\n\t" "v_cvt_pk_bf16_f32 v23, v62, v63\n\t"
                         "v_smfmac_f32_16x16x64_bf16 v[88:91], a[40:43], v[192:199], v9\n\t" "s_waitcnt vmcnt(10)\n\t" "v_cvt_pk_bf16_f32 v28, v72, v73\n\t" "v_cvt_pk_bf16_f32 v29, v74, v75\n\t"
                         "v_smfmac_f32_16x16x64_bf16 v[92:95], a[44:47], v[200:207], v9\n\t" "s_waitcnt vmcnt(9)\n\t" "v_cvt_pk_bf16_f32 v30, v76, v77\n\t" "v_cvt_pk_bf16_f32 v31, v78, v79\n\t"
                         "v_smfmac_f32_16x16x64_bf16 v[104:107], a[56:59], v[224:231], v9\n\t" "s_waitcnt vmcnt(8)\n\t" "v_cvt_pk_bf16_f32 v36, v88, v89\n\t"
                         "v_smfmac_f32_16x16x64_bf16 v[108:111], a[60:63], v[232:239], v9\n\t" "s_waitcnt vmcnt(7)\n\t" "v_cvt_pk_bf16_f32 v37, v90, v91\n\t"
                         "v_smfmac_f32_16x16x64_bf16 v[0:3], a[80:83], a[96:103], v8\n\t" "s_waitcnt vmcnt(6)\n\t" "v_cvt_pk_bf16_f32 v38, v92, v93\n\t" "v_smfmac_f32_16x16x64_bf16 v[0:3], a[84:87], a[104:111], v8\n\t"
                         "s_waitcnt vmcnt(5)\n\t" "v_cvt_pk_bf16_f32 v39, v94, v95\n\t" "v_smfmac_f32_16x16x64_bf16 v[0:3], a[88:91], a[112:119], v8\n\t" "s_waitcnt vmcnt(4)\n\t" "v_cvt_pk_bf16_f32 v44, v104, v105\n\t"
                         "v_smfmac_f32_16x16x64_bf16 v[0:3], a[92:95], a[120:127], v8\n\t" "s_waitcnt vmcnt(3)\n\t" "v_cvt_pk_bf16_f32 v45, v106, v107\n\t" "v_smfmac_f32_16x16x64_bf16 v[0:3], a[64:67], v[16:23], v8\n\t"
                         "s_waitcnt vmcnt(2)\n\t" "v_cvt_pk_bf16_f32 v46, v108, v109\n\t" "v_smfmac_f32_16x16x64_bf16 v[0:3], a[68:71], v[24:31], v8\n\t" "s_waitcnt vmcnt(1)\n\t" "v_cvt_pk_bf16_f32 v47, v110, v111\n\t"
                         "v_smfmac_f32_16x16x64_bf16 v[0:3], a[72:75], v[32:39], v8\n\t" "s_waitcnt vmcnt(0)\n\t" "v_smfmac_f32_16x16x64_bf16 v[0:3], a[76:79], v[40:47], v8\n\t" ".Lv_end:\n\t" "s_nop 7\n\t"
                         "v_pk_add_f32 v[0:1], v[0:1], v[2:3]\n\t" "v_cvt_pk_bf16_f32 v2, v0, v1\n\t" "v_cmp_u_f32 vcc, v0, v1\n\t" "s_cmp_eq_u64 vcc, 0\n\t"
                         "v_mov_b32_dpp v3, v2 quad_perm:[1,0,3,2] row_mask:0xf bank_mask:0xf\n\t" "s_cselect_b64 %0, -1, 0\n\t" "v_perm_b32 v0, v2, v3, v11\n\t" "s_mov_b64 exec, %0\n\t"
                         "global_store_dword v10, v0, %5 offset:-4096 sc1\n\t" "global_store_dword v10, v0, %4 offset:-4096 sc1\n\t" "s_memrealtime %1\n\t" "s_mov_b64 exec, -1\n\t" "s_waitcnt lgkmcnt(0)\n\t"
                         : "=&s"(ok), "=&s"(tp8)
                         : "{v[4:11]}"(ad), "s"(up), "s"(mnx), "s"((u64)mnx ^ 8192), "s"(memp + 16384), "s"(memp + 32768 - 2048), "s"(memp + 49152 - 2048), "s"(0xc30000c3c30000c3ull),
                           "s"(uni((int)(tgt - (u32)(v < 16 ? cfg.dU - (v >> 3) * cfg.s2 : cfg.cR2)))), "s"(v < 16 ? 0 : (v < 24 ? 1 : cfg.T3)), "v"(pob), "v"(uob), "v"(pmo), VALL : "memory", "vcc", "scc", "s42");
            if (__builtin_expect(ok != 0, 1) || s < 0) break;
            if (all || --budget < 0) {
                asm volatile("v_min_u32 v0, -2, v0\n\tglobal_store_dword v10, v0, %0 offset:-4096 sc1\n\tglobal_store_dword v10, v0, %1 offset:-4096 sc1"
                             : : "s"(mnx), "s"((u64)mnx ^ 8192), "{v[4:11]}"(ad), VALL : "memory");
                break;
            }
            if ((++tries & 15) == 0) {
                u32 m = 0;
#pragma nounroll
                for (int i = 0; i < 32; ++i) {
                    u32x4 t;
                    gld16c<0>(t, i < 24 ? memp + 12288 : up, i < 20 ? (4 + i + ((4 + i) & 24)) * 1024 + ad[3] : (i < 24 ? i * 512 - 8192 + ad[2] : (i & 7) * 512 - 512 + (i & 1 ? uob : ad[0]))); waitv<0>(t);
                    m = max3u(max3u(m, t[0], t[1]), t[2], t[3]);
                }
                all = __builtin_amdgcn_ballot_w64(m == 0xffffffffu) == 0;
            }
            phi += (u32)cfg.G;
            zero();
        }
        asm volatile("s_barrier\n\t" "ds_read_b128 v[112:115], %0\n\t" "ds_read_b128 v[116:119], %0 offset:1024\n\t" "ds_read_b128 v[120:123], %0 offset:2048\n\t" "ds_read_b128 v[124:127], %0 offset:3072\n\t"
                     "ds_read_b128 v[128:131], %0 offset:4096\n\t" "ds_read_b128 v[132:135], %0 offset:5120\n\t" "ds_read_b128 v[136:139], %0 offset:6144\n\t" "ds_read_b128 v[140:143], %0 offset:7168\n\t"
                     "ds_read_b128 v[144:147], %0 offset:8192\n\t" "ds_read_b128 v[148:151], %0 offset:9216\n\t" "ds_read_b128 v[152:155], %0 offset:10240\n\t" "ds_read_b128 v[156:159], %0 offset:11264\n\t"
                     "ds_read_b128 v[160:163], %0 offset:12288\n\t" "ds_read_b128 v[164:167], %0 offset:13312\n\t" "ds_read_b128 v[168:171], %0 offset:14336\n\t" "ds_read_b128 v[172:175], %0 offset:15360\n\t"
                     "ds_read_b128 v[176:179], %0 offset:16384\n\t" "ds_read_b128 v[180:183], %0 offset:17408\n\t" "ds_read_b128 v[184:187], %0 offset:18432\n\t" "ds_read_b128 v[188:191], %0 offset:19456\n\t"
                     "ds_read_b128 v[192:195], %0 offset:20480\n\t" "ds_read_b128 v[196:199], %0 offset:21504\n\t" "ds_read_b128 v[200:203], %0 offset:22528\n\t" "ds_read_b128 v[204:207], %0 offset:23552\n\t"
                     "ds_read_b128 v[208:211], %0 offset:24576\n\t" "ds_read_b128 v[212:215], %0 offset:25600\n\t" "ds_read_b128 v[216:219], %0 offset:26624\n\t" "ds_read_b128 v[220:223], %0 offset:27648\n\t"
                     "ds_read_b128 v[224:227], %0 offset:28672\n\t" "ds_read_b128 v[228:231], %0 offset:29696\n\t" "ds_read_b128 v[232:235], %0 offset:30720\n\t" "ds_read_b128 v[236:239], %0 offset:31744\n\t"
                     : : "v"(lds + ad[3] + ((s + 1) & 1) * 32768), VALL : "memory");
        if (s >= 0) {
            follow(phi, (int)(tt & 0xffffu) + 5 * (v >> 2) / 2 - VISV, (u32)tp8, cfg.G);
            if (vb == 0 && lane < 8) gst8(fl, (F_CUR + lane) * 128, u32x2{(u32)s - 1, phi + (tt & 0xffffu)});
        }
        phi += tt & 0xffffu;
        memp = mnx; up = ub + uslot(sd, s + 1);
        zero();
        tt = sched_ld0(s + 1);
        clk.calib();
        while (s >= 0) {
            u32 h0;
            spin_to(clk.at(phi + (u32)cfg.dV));
            asm volatile("global_load_dword %0, %1, %2 offset:-4096 sc1\n\ts_sleep 5" : "=&v"(h0) : "v"(ho), "s"(memp) : "memory");
            edge(clk.r1, clk.m1);
            asm volatile("s_waitcnt vmcnt(0)" : "+v"(h0) : : "memory");
            if (__builtin_amdgcn_ballot_w64(h0 == 0xffffffffu) == 0 || --budget < 0) break;
            phi += (u32)cfg.G;
        }
        asm volatile(".Lkm_lv:");
    }
}

DI void xcopy_task(const Ptrs& p, int j, int w) {
    const int tid = 64 * w + lanex();
    Sync y;
    sync_ld(y, p.ws);
    u32x4 xv[10];
    sfor<0, 10>([&](auto I) { gld16<I.v * 128>(xv[I.v], p.x + (size_t)j * 40960, (u32)((tid & 31) * 1280 + (tid >> 5) * 16)); });
    sfor<0, 10>([&](auto I) { waitv<9 - I.v>(xv[I.v]); });
    asm volatile("" : "+v"(y.r), "+v"(y.g), "+v"(y.e));
    sfor<0, 10>([&](auto I) { gst16(p.ws + OFF_XF + (size_t)j * 40960 + I.v * 4096, (u32)tid * 16, xv[I.v]); });
    waitvm<0>();
    barrier();
    const u64 t = sync_tag(y.e);
    if (tid == 0) gst8(p.ws + OFF_LAT, 1536 + (u32)j * 8, u32x2{(u32)t, (u32)(t >> 32)});
}
DI void xcopy_wait(const char* ws, u64 t) {
#pragma nounroll
    for (int it = 0; it < (1 << 16); ++it) {
        u32x2 r;
        asm volatile("s_nop 4\n\tglobal_load_dwordx2 %0, %1, %2 offset:1536 sc1\n\ts_waitcnt vmcnt(0)" : "=&v"(r) : "v"(lanex() * 8), "s"(ws + OFF_LAT) : "memory");
        if (__builtin_amdgcn_ballot_w64(r[0] != (u32)t || r[1] != (u32)(t >> 32)) == 0) break;
        __builtin_amdgcn_s_sleep(16);
    }
}
DI void init_task(const Ptrs& p, int j, u32 lds, int w) {
    const int lane = lanex(), tid = 64 * w + lane;
    Sync y;
    sync_ld(y, p.ws);
    const u32x4 ff = u32x4{~0u, ~0u, ~0u, ~0u};
    const int i = j * 256 + tid, r = j < 8 ? j & 1 : min(j / 2 - 2, NREG - 1);
    char* const vb = p.ws + (r < 2 ? 0 : OFF_U - (4u << 20)) + ((size_t)r << 21) + 1984 * 1024 + (j < 8 ? j & 6 : (j & 1) * 2 + (j >> 1 == 15) * 4) * 8192;
    const u32 vo = (u32)((w >> 1) * 8192 + (w & 1) * 1024);
    gst16(vb, vo + lane * 16, ff);
    if (i < F_N * 8) gst16(p.ws + OFF_ST, (u32)i * 16, u32x4{i == F_ONE * 8 ? 1000u : ~0u, ~0u, ~0u, ~0u});
    gst16(p.ws + 65536, (u32)i * 16, ff);
    if (i < 4096) {
        const int k = (i & 511) + ((i >> 1) & 0x600);
        u32x4 m;
        gld16<0>(m, p.mem0, (u32)((2 * ((k >> 2) & 3) + (k & 1)) * 1280 + min(2 * (k >> 4) + ((k >> 1) & 1), 79) * 16));
        waitv<0>(m);
        gst16(p.ws, (u32)i * 16, k < 640 ? u32x4{min(m[0], 0xfffffffeu), min(m[1], 0xfffffffeu), min(m[2], 0xfffffffeu), min(m[3], 0xfffffffeu)} : ff);
    }
#pragma unroll
    for (int c = 0; c < 6; ++c) gst16(p.ws + OFF_U + c * 131072 - (c == 5) * 122880, (u32)i * 16, ff);
    waitvm<0>();
    asm volatile("" : "+v"(y.r), "+v"(y.g), "+v"(y.e));
    barrier();
    if (w) return;
    u32 dt[2] = {0xffffffu, 0xffffffu};
#pragma unroll
    for (int c = 0; c < 2 * NSAMP; ++c) {
        u32 v;
        const u32 t0 = mt();
        asm volatile("global_load_dword %0, %1, %2 sc1\n\ts_waitcnt vmcnt(0)" : "=&v"(v) : "v"((c & 1) * 8192 + (c >> 1) * 256), "s"(vb) : "memory");
        dt[c & 1] = min(dt[c & 1], mt() - t0);
    }
    const u64 t = sync_tag(y.e);
    const u32 vote = (u32)((dt[0] > dt[1]) ^ xhalf() ^ (int)((vb - p.ws) >> 21)) & 1u;
    if (tid == 0) {
        if (j == 0) gst8(p.ws + OFF_LAT, 512, u32x2{(u32)t, rt()});
        gst8(p.ws + OFF_LAT, (u32)j * 8, u32x2{(u32)t | vote, (u32)(t >> 32)});
    }
}

DI float xmax(float v) {
    const auto a = __builtin_amdgcn_permlane32_swap(__float_as_uint(v), __float_as_uint(v), false, false);
    v = fmaxf(__uint_as_float(a[0]), __uint_as_float(a[1]));
    const auto b = __builtin_amdgcn_permlane16_swap(__float_as_uint(v), __float_as_uint(v), false, false);
    return fmaxf(__uint_as_float(b[0]), __uint_as_float(b[1]));
}
DI float xsum(float v) {
    const auto a = __builtin_amdgcn_permlane32_swap(__float_as_uint(v), __float_as_uint(v), false, false);
    v = __uint_as_float(a[0]) + __uint_as_float(a[1]);
    const auto b = __builtin_amdgcn_permlane16_swap(__float_as_uint(v), __float_as_uint(v), false, false);
    return __uint_as_float(b[0]) + __uint_as_float(b[1]);
}
DI void gst16c(char* sbase, u32 voff, u32x4 v) {
    asm volatile("global_store_dwordx4 %0, %1, %2 sc1\n\ts_nop 1" : : "v"(voff), "v"(v), "s"(sbase) : "memory");
}
DI void gst8c(char* sbase, u32 voff, u32x2 v) {
    asm volatile("global_store_dwordx2 %0, %1, %2 sc1\n\ts_nop 1" : : "v"(voff), "v"(v), "s"(sbase) : "memory");
}
#define WAIT6(N, r, o) asm volatile("s_waitcnt vmcnt(" #N ")" : "+v"(r[o]), "+v"(r[o + 1]), "+v"(r[o + 2]), "+v"(r[o + 3]), "+v"(r[o + 4]), "+v"(r[o + 5]) : : "memory")
#define WSLEEP 90
DI void wg_wait(const char* fl, int w, int i0, int t0, int i1, int t1, int i2, int t2, int i3, int t3, int dt, bool acq) {
    if (w == 0) {
        const int lane = lanex(), tg = lane == 1 ? t1 : (lane == 2 ? t2 : (lane == 3 ? t3 : t0));
        const int ix = tg <= 0 ? i0 : (lane == 1 ? i1 : (lane == 2 ? i2 : (lane == 3 ? i3 : i0)));
#pragma nounroll
        for (int it = 0; it < (1 << 14); ++it) {
            u32 v;
            flag_ld(v, fl, ix);
            asm volatile("s_waitcnt vmcnt(0)" : "+v"(v) : : "memory");
            if (__builtin_amdgcn_ballot_w64((int)(v + 1u) < tg) == 0) break;
            const u32 wake = rt() + (u32)(dt + 180 * min(max(t0 - 2 - uni((int)v), 0), 64));
            while ((int)(rt() - wake) < 0) __builtin_amdgcn_s_sleep(4);
        }
        if (acq) acquire();
    }
    barrier();
}
#define TEST 172
DI void until(u32 t) { while ((int)(rt() - t) < 0) __builtin_amdgcn_s_sleep(1); }
DI u32 step_wait(const char* fl, int w, int gr, int t, u32 lds) {
    if (w == 0) {
        u32x2 v;
#pragma nounroll
        for (int it = 0; it < (1 << 14); ++it) {
            asm volatile("s_nop 4\n\tglobal_load_dwordx2 %0, %1, %2 sc1\n\ts_waitcnt vmcnt(0)" : "=&v"(v) : "v"((F_CUR + gr) * 128), "s"(fl) : "memory");
            const int d = t - 1 - uni((int)v[0]);
            if (d <= 0) { v[1] += (u32)(d * TEST); break; }
            const u32 now = rt();
            nap(min(max((int)((u32)uni((int)v[1]) - now) + d * TEST + 10, 40), 2000));
        }
        *LDSP(u32, lds + GRING * GBUF + 4) = v[1];
    }
    barrier();
    return (u32)uni((int)*LDSP(u32, lds + GRING * GBUF + 4));
}
#define PDL 135
#define ODL 280
#define TDT 35
template <bool LATE>
DI void px_wave(const Ptrs& p, const ChainCfg& cfg, int s, int h, int slow, int w, u32 lds, bool keep) {
    const int lane = lanex(), n = lane & 15, g = lane >> 4;
    const char* qb = p.ws + OFF_QX + (size_t)(s * NH + h) * 12288;
    const char* kb = p.ws + OFF_KX + (size_t)(s * NH + h) * 12288;
    const char* mb = p.ws + OFF_KM + (size_t)(s * NH + h) * 3072;
    const u32 ko = (u32)((n >> 3) * 6144 + (8 * ((n >> 2) & 1) + (n & 3) + 16 * g) * 16);
    const u32 mo = (u32)(g * 128 + (n & 7) * 16);
    u32x4 qf[12], kf[12], mf[6];
    sfor<0, 12>([&](auto I) { gld16c<(I.v % 3) * 1024>(qf[I.v], qb + (I.v / 3) * 3072, lane * 16); gap<SMP>(slow); });
    if (!SMP) pace(slow);
    sfor<0, 12>([&](auto I) { gld16c<(I.v % 3) * 1024 + (I.v / 6) * 64>(kf[I.v], kb + ((I.v / 3) & 1) * 3072, ko); gap<SMP>(slow); });
    if (!SMP) pace(slow);
    if constexpr (LATE) { WAIT6(0, qf, 0); WAIT6(0, qf, 6); WAIT6(0, kf, 0); }
    else {
        sfor<0, 6>([&](auto I) { gld16c<I.v * 512>(mf[I.v], mb, mo); });
        WAIT6(24, qf, 0); WAIT6(18, qf, 6); WAIT6(12, kf, 0);
    }
    f32x4 sc[2][3];
#pragma unroll
    for (int tt = 0; tt < 2; ++tt)
#pragma unroll
        for (int T = 0; T < 3; ++T) sc[tt][T] = f32x4{0.f, 0.f, 0.f, 0.f};
#pragma unroll
    for (int ks = 0; ks < 6; ++ks) { sc[0][0] = mfma16(kf[ks], qf[ks], sc[0][0]); sc[1][0] = mfma16(kf[ks], qf[6 + ks], sc[1][0]); }
    WAIT6(6, kf, 6);
#pragma unroll
    for (int ks = 0; ks < 6; ++ks) { sc[0][1] = mfma16(kf[6 + ks], qf[ks], sc[0][1]); sc[1][1] = mfma16(kf[6 + ks], qf[6 + ks], sc[1][1]); }
    if constexpr (LATE) {
        until(step_wait(p.ws + OFF_ST, w, h & 7, s - 1, lds) + (u32)(s == NSEG - 1 ? cfg.T : cfg.T + PDX));
        bool ok = false;
#pragma nounroll
        for (int it = 0; it < (1 << 14); ++it) {
            u32 v;
            sfor<0, 6>([&](auto I) { gld16c<I.v * 512>(mf[I.v], mb, mo); });
            flag_ldm(v, p.ws + OFF_ST, (s < NSEG - 2 ? F_CUR : F_PROG + 8 * s) + (h & 7), it > 3);
            WAIT6(0, mf, 0);
            asm volatile("" : "+v"(v));
            u32 m = 0;
#pragma unroll
            for (int i = 0; i < 24; ++i) m = max(m, mf[i >> 2][i & 3]);
            if (ok || __builtin_amdgcn_ballot_w64(m == ~0u) == 0) break;
            ok = it > 3 && uni((int)v) >= (s < NSEG - 2 ? s : 7);
            if (s < NSEG - 1) nap(60);
        }
    }
    WAIT6(0, mf, 0);
#pragma unroll
    for (int ks = 0; ks < 6; ++ks) { sc[0][2] = mfma16(mf[ks], qf[ks], sc[0][2]); sc[1][2] = mfma16(mf[ks], qf[6 + ks], sc[1][2]); }
    const float kscale = 0.07216878364870322f;
    char* pb = p.ws + OFF_PX + (size_t)s * 81920;
#pragma unroll
    for (int tt = 0; tt < 2; ++tt) {
        float v[12], mx = -3.0e38f;
#pragma unroll
        for (int i = 0; i < 12; ++i) {
            const float t = rbf(rbf(sc[tt][i >> 2][i & 3]) * kscale);
            v[i] = (i >= 8 || 8 * g + i <= 16 * tt + n) ? t : -3.0e38f;
            mx = fmaxf(mx, v[i]);
        }
        mx = xmax(mx);
        float sx = 0.f, sm = 0.f;
#pragma unroll
        for (int i = 0; i < 12; ++i) {
            v[i] = __builtin_amdgcn_exp2f((v[i] - mx) * 1.4426950408889634f);
            if (i < 8) sx += v[i]; else sm += v[i];
        }
        const float inv = __builtin_amdgcn_rcpf(xsum(sx + 0.5f * sm));
        const u32x4 px = u32x4{cvtpk(v[0] * inv, v[1] * inv), cvtpk(v[2] * inv, v[3] * inv), cvtpk(v[4] * inv, v[5] * inv), cvtpk(v[6] * inv, v[7] * inv)};
        const u32x2 pm = u32x2{cvtpk(v[9] * inv, v[8] * inv), cvtpk(v[11] * inv, v[10] * inv)};
        const int ox = h * 2048 + tt * 1024, om = 65536 + (h >> 2) * 2048 + (h & 3) * 256 + tt * 1024;
        if (!LATE && keep) {
            *LDSP(u32x4, lds + ox + lane * 16) = px;
            *LDSP(u32x2, lds + om + n * 16 + (g & 1) * 8) = pm;
        } else {
            gst16c(pb + ox, lane * 16, px);
            gst8c(pb + om, (u32)(n * 16 + (g & 1) * 8), pm);
        }
    }
}

#define WAIT10(N, r) asm volatile("s_waitcnt vmcnt(" #N ")" : "+v"(r[0]), "+v"(r[1]), "+v"(r[2]), "+v"(r[3]), "+v"(r[4]), "+v"(r[5]), "+v"(r[6]), "+v"(r[7]), "+v"(r[8]), "+v"(r[9]) : : "memory")
DI void out_task(const Ptrs& p, int s, int cg0, int cg1, u32 lds, int slow, int w, int sd) {
    u32x4 pf[2][10];
    if (cg1 - cg0 > 1) {
        const u32 la = lds + lanex() * 16;
#pragma unroll
        for (int i = 0; i < 20; ++i) pf[i & 1][i >> 1] = ldsr16(la + (i < 16 ? w * 16384 + i * 1024 : 65536 + w * 4096 + (i - 16) * 1024));
    } else {
        const char* pb = p.ws + OFF_PX + (size_t)s * 81920;
        const u32 l16 = lanex() * 16;
        sfor<0, 20>([&](auto I) { gld16c<(I.v & 3) * 1024>(pf[I.v & 1][I.v >> 1], pb + (I.v < 16 ? w * 16384 + (I.v >> 2) * 4096 : 65536 + w * 4096), l16); gap<SMO>(slow); if (!SMO && I.v == 9) pace(slow); });
        WAIT10(0, pf[0]); WAIT10(0, pf[1]);
    }
#pragma nounroll
    for (int cg = cg0; cg < cg1; ++cg) {
    const int lane = lanex(), tid = 64 * w + lane, n = lane & 15, g = lane >> 4;
    const char* db = p.ws + OFF_D + s * D_STRIDE + (size_t)(16 * cg) * 16384 + w * 4096;
    const char* ub = p.ws + uslot(sd, s) + (size_t)(16 * cg) * 4096 + w * 1024;
    const u32 dof = (u32)((n >> 3) * 16384 + g * 128 + (n & 7) * 16), uof = (u32)((n >> 3) * 4096 + g * 128 + (n & 7) * 16);
    u32x4 af[3][10];
    auto aload = [&](u32x4 (&r)[10], int ct) {
        if (!SMO) pace(slow);
        sfor<0, 8>([&](auto I) { gld16c<I.v * 512>(r[I.v], db + ct * 32768, dof); gap<SMO>(slow); });
        gld16c<0>(r[8], ub + ct * 8192, uof); gap<SMO>(slow); gld16c<512>(r[9], ub + ct * 8192, uof); gap<SMO>(slow);
    };
    aload(af[0], 0);
    aload(af[1], 1);
    aload(af[2], 2);
    const int tok = 32 * s + (tid >> 3), col = 128 * cg + 8 * (tid & 7);
    u32x4 bb[2]; u32 gv;
    gld16c<0>(bb[0], p.ws + OFF_BOUT, (u32)(s * 40960 + col * 64 + (tid >> 3) * 16)); gld16c<0>(bb[1], p.ws + OFF_BOUT + 4096, (u32)(s * 40960 + col * 64 + (tid >> 3) * 16));
    asm volatile("global_load_dword %0, %1, %2 sc1" : "=v"(gv) : "v"(tok * 4), "s"(p.ws + OFF_GATE) : "memory");
    barrier();
    const u32 wa = lds + (u32)w * 16896 + n * 528 + g * 16;
    sfor<0, 8>([&](auto I) {
        constexpr int ct = I.v;
        u32x4 (&r)[10] = af[ct % 3];
        if constexpr (ct == 0) WAIT10(23, r);
        else if constexpr (ct < 3) WAIT10(23, r);
        else if constexpr (ct < 6) WAIT10(20, r);
        else if constexpr (ct == 6) WAIT10(10, r);
        else WAIT10(0, r);
        f32x4 c0 = f32x4{0.f, 0.f, 0.f, 0.f}, c1 = c0;
#pragma unroll
        for (int i = 0; i < 10; ++i) { c0 = mfma16(r[i], pf[0][i], c0); c1 = mfma16(r[i], pf[1][i], c1); }
        *LDSP(f32x4, wa + ct * 64) = c0;
        *LDSP(f32x4, wa + ct * 64 + 16 * 528) = c1;
        PIN();
        if constexpr (ct + 3 < 8) aload(r, ct + 3);
        PIN();
    });
    asm volatile("" : "+v"(bb[0]), "+v"(bb[1]), "+v"(gv));
    barrier();
    const u32 ra = lds + (u32)(tid >> 3) * 528 + (tid & 7) * 32;
    const float gt = __uint_as_float(gv), g1 = rbf(1.f - gt);
    u32 ov[8];
#pragma unroll
    for (int i = 0; i < 4; ++i) {
        const u32 ri = ra + 16 * (i & 1) + 256 * (i >> 1);
        const f32x4 z = (*LDSP(f32x4, ri) + *LDSP(f32x4, ri + 16896)) + (*LDSP(f32x4, ri + 2 * 16896) + *LDSP(f32x4, ri + 3 * 16896));
#pragma unroll
        for (int j = 0; j < 2; ++j) {
            const u32 b = bb[i >> 1][2 * (i & 1) + j];
            const u32 a = cvtpk(z[2 * j], z[2 * j + 1]);
            const u32 ga = cvtpk(gt * __uint_as_float(a << 16), gt * __uint_as_float(a & 0xffff0000u));
            const u32 gb = cvtpk(g1 * __uint_as_float(b << 16), g1 * __uint_as_float(b & 0xffff0000u));
            ov[2 * i + j] = cvtpk(__uint_as_float(ga << 16) + __uint_as_float(gb << 16), __uint_as_float(ga & 0xffff0000u) + __uint_as_float(gb & 0xffff0000u));
        }
    }
    gst16p(p.out, (u32)(tok * 1280 + col * 2), u32x4{ov[0], ov[1], ov[2], ov[3]});
    gst16p(p.out, (u32)(tok * 1280 + col * 2 + 128), u32x4{ov[4], ov[5], ov[6], ov[7]});
    }
}

DI void out_tail(const Ptrs& p, const ChainCfg& cfg, int s, int cg, u32 lds, int w, int sd, int gr) {
    const int lane = lanex(), tid = 64 * w + lane, n = lane & 15, g = lane >> 4;
    const char* db = p.ws + OFF_D + s * D_STRIDE + (size_t)(4 * cg) * 16384 + w * 4096;
    const char* ub = p.ws + uslot(sd, s) + (size_t)(4 * cg) * 4096 + w * 1024;
    const char* pb = p.ws + OFF_PX + (size_t)s * 81920;
    const u32 dof = (u32)((n >> 3) * 16384 + g * 128 + (n & 7) * 16), uof = (u32)((n >> 3) * 4096 + g * 128 + (n & 7) * 16);
    u32x4 pf[2][10], af[2][10];
    sfor<0, 16>([&](auto I) { gld16c<(I.v & 7) * 512>(af[I.v >> 3][I.v & 7], db + (I.v >> 3) * 32768, dof); });
    const int tok = 32 * s + (tid >> 3), col = 32 * cg + 4 * (tid & 7);
    u32x2 bb; u32 gv;
    asm volatile("global_load_dwordx2 %0, %2, %3 sc1\n\tglobal_load_dword %1, %4, %5 sc1\n\ts_waitcnt vmcnt(0)" : "=&v"(bb), "=&v"(gv)
                 : "v"(s * 40960 + (col >> 3) * 512 + (tid >> 3) * 16 + (col & 7) * 2), "s"(p.ws + OFF_BOUT), "v"(tok * 4), "s"(p.ws + OFF_GATE) : "memory");
    sfor<0, 16>([&](auto I) { waitv<0>(af[I.v >> 3][I.v & 7]); });
    until(step_wait(p.ws + OFF_ST, w, gr, s - 1, lds) + (u32)(s == NSEG - 1 ? cfg.cR : cfg.cR + 2 * PDX));
    const u32 wa = lds + (u32)w * 4608 + n * 144 + g * 16;
#pragma nounroll
    for (int it = 0; it < (1 << 14); ++it) {
        if (it == 4) {
            flag_wait(p.ws + OFF_ST, F_PX + s, 8);
            flag_wait(p.ws + OFF_ST, s < NSEG - 2 ? F_CUR + gr : F_UD + 2 * (s & 1), s < NSEG - 2 ? s + 1 : 16);
            flag_wait(p.ws + OFF_ST, s < NSEG - 2 ? F_ONE : F_UD + 2 * (s & 1) + 1, 16);
        }
        sfor<0, 4>([&](auto I) { gld16c<(I.v & 1) * 512>(af[I.v >> 1][8 + (I.v & 1)], ub + (I.v >> 1) * 8192, uof); });
        sfor<0, 8>([&](auto I) { gld16c<(I.v & 1) * 2048>(pf[0][I.v], pb + w * 16384 + (I.v >> 1) * 4096, lane * 16); });
        gld16c<0>(pf[0][8], pb + 65536 + w * 4096, lane * 16); gld16c<2048>(pf[0][9], pb + 65536 + w * 4096, lane * 16);
        sfor<0, 8>([&](auto I) { gld16c<(I.v & 1) * 2048 + 1024>(pf[1][I.v], pb + w * 16384 + (I.v >> 1) * 4096, lane * 16); });
        gld16c<1024>(pf[1][8], pb + 65536 + w * 4096, lane * 16); gld16c<3072>(pf[1][9], pb + 65536 + w * 4096, lane * 16);
        WAIT10(10, pf[0]); WAIT10(10, af[0]); WAIT10(10, af[1]);
        float chk = 0.f;
#pragma unroll
        for (int tt = 0; tt < 2; ++tt) {
            if (tt) WAIT10(0, pf[1]);
            f32x4 c0 = f32x4{0.f, 0.f, 0.f, 0.f}, c1 = c0;
#pragma unroll
            for (int i = 0; i < 10; ++i) { c0 = mfma16(af[0][i], pf[tt][i], c0); c1 = mfma16(af[1][i], pf[tt][i], c1); }
            *LDSP(f32x4, wa + tt * 2304) = c0;
            *LDSP(f32x4, wa + tt * 2304 + 64) = c1;
            const f32x4 c = c0 + c1;
            chk += (c[0] + c[1]) + (c[2] + c[3]);
            PIN();
        }
        if (it >= 4 || __builtin_amdgcn_ballot_w64((__float_as_uint(chk) & 0x7fffffffu) > 0x7f800000u) == 0) break;
        if (s < NSEG - 1) nap(60);
    }
    barrier();
    const u32 ra = lds + (u32)(tid >> 3) * 144 + (tid & 7) * 16;
    const float gt = __uint_as_float(gv), g1 = rbf(1.f - gt);
    const f32x4 z = (*LDSP(f32x4, ra) + *LDSP(f32x4, ra + 4608)) + (*LDSP(f32x4, ra + 2 * 4608) + *LDSP(f32x4, ra + 3 * 4608));
    u32x2 ov;
#pragma unroll
    for (int j = 0; j < 2; ++j) {
        const u32 b = bb[j], a = cvtpk(z[2 * j], z[2 * j + 1]);
        const u32 ga = cvtpk(gt * __uint_as_float(a << 16), gt * __uint_as_float(a & 0xffff0000u));
        const u32 gb = cvtpk(g1 * __uint_as_float(b << 16), g1 * __uint_as_float(b & 0xffff0000u));
        ov[j] = cvtpk(__uint_as_float(ga << 16) + __uint_as_float(gb << 16), __uint_as_float(ga & 0xffff0000u) + __uint_as_float(gb & 0xffff0000u));
    }
    *(u32x2*)(p.out + tok * 1280 + col * 2) = ov;
}

DI float dot2bf(u32 a, u32 b, float c) { return __builtin_amdgcn_fdot2_f32_bf16(__builtin_bit_cast(bf16x2, a), __builtin_bit_cast(bf16x2, b), c, false); }
DI void battn_task(const Ptrs& p, int t0, u32 lds, int w) {
    const int lane = lanex(), tid = 64 * w + lane, tl = tid & 31, t = t0 + tl;
    const char* const qkv = p.ws + OFF_QKVB + (size_t)(t0 >> 5) * 122880;
    {
        u32 so[3];
#pragma unroll
        for (int i = 0; i < 3; ++i) {
            const int e = 64 * i + lane, sl = e % 48;
            so[i] = (u32)((e / 48) * 512 + (sl < 16 ? (t0 ? 0 : 122880) + 256 + sl * 16 : 122880 - 256 + sl * 16));
        }
#pragma unroll
        for (int i = 0; i < 30; ++i) { glds16c(qkv - 122880 + 40960 + (10 * w + i / 3) * 2048, so[i % 3], lds + (30 * w + i) * 1024); gap<SMB>(-1); }
    }
    u32x4 q[2][5], xv[10], gv[10];
    sfor<0, 10>([&](auto I) { gld16c<(I.v % 5) * 512>(q[I.v / 5][I.v % 5], qkv + (I.v / 5) * 20480, (u32)((tid >> 5) * 2560 + tl * 16)); gap<SMB>(-1); });
    sfor<0, 10>([&](auto I) { gld16<I.v * 128>(xv[I.v], p.x + (size_t)t0 * 1280, (u32)((tid >> 3) * 1280 + (tid & 7) * 16)); gld16<I.v * 128>(gv[I.v], p.gw, (u32)((tid & 7) * 16)); gap<SMB>(-1); });
    sfor<0, 10>([&](auto I) { waitv<0>(q[I.v / 5][I.v % 5]); waitv<0>(xv[I.v]); waitv<0>(gv[I.v]); });
    barrier();
    {
        float a = 0.f;
#pragma unroll
        for (int i = 0; i < 10; ++i)
#pragma unroll
            for (int j = 0; j < 4; ++j) {
                const u32 r = cvtpk(__uint_as_float(xv[i][j] << 16) * __uint_as_float(gv[i][j] << 16), __uint_as_float(xv[i][j] & 0xffff0000u) * __uint_as_float(gv[i][j] & 0xffff0000u));
                a += __uint_as_float(r << 16) + __uint_as_float(r & 0xffff0000u);
            }
        a += __shfl_xor(a, 1); a += __shfl_xor(a, 2); a += __shfl_xor(a, 4);
        const float z = rbf(a);
        if ((tid & 7) == 0) gst4c(p.ws + OFF_GATE, (u32)((t0 + (tid >> 3)) * 4), __float_as_uint(rbf(1.0f / (1.0f + __expf(-z)))));
    }
#pragma unroll
    for (int it = 0; it < 2; ++it) {
        const int m = (tid >> 5) + 8 * it;
        u32 ka = lds + (u32)(m * 3840 + (2 + tl) * 16), va = ka + 61440;
        asm volatile("" : "+v"(ka), "+v"(va));
        u32x4 k[5][8];
#pragma unroll
        for (int i = 0; i < 40; ++i) k[i >> 3][i & 7] = ldsr16(ka + (i >> 3) * 768 + (7 - (i & 7)) * 32);
        PIN();
        float lg[2][8], mx[2] = {-3.0e38f, -3.0e38f};
#pragma unroll
        for (int i = 0; i < 16; ++i) lg[i >> 3][i & 7] = 0.f;
#pragma unroll
        for (int i = 0; i < 5; ++i) {
#pragma unroll
            for (int j = 0; j < 4; ++j)
#pragma unroll
                for (int wv = 0; wv < 8; ++wv) lg[4 * i + j >= 10][wv] = dot2bf(q[it][i][j], k[i][wv][j], lg[4 * i + j >= 10][wv]);
            PIN();
#pragma unroll
            for (int wv = 0; wv < 8; ++wv) k[i][wv] = ldsr16(va + i * 768 + (7 - wv) * 32);
            PIN();
        }
#pragma unroll
        for (int i = 0; i < 16; ++i) {
            float& l = lg[i >> 3][i & 7];
            l = rbf(rbf(l) * 0.22360679774997896f);
            asm volatile("" : "+v"(l));
            l = t - 2 * (i & 7) >= 0 ? l : -3.0e38f;
            mx[i >> 3] = fmaxf(mx[i >> 3], l);
        }
        u32 pk[2][4];
#pragma unroll
        for (int u = 0; u < 2; ++u) {
            float sum = 0.f;
#pragma unroll
            for (int wv = 0; wv < 8; ++wv) { lg[u][wv] = __builtin_amdgcn_exp2f((lg[u][wv] - mx[u]) * 1.4426950408889634f); sum += lg[u][wv]; }
            const float inv = __builtin_amdgcn_rcpf(sum);
#pragma unroll
            for (int wv = 0; wv < 4; ++wv) pk[u][wv] = cvtpk(lg[u][2 * wv] * inv, lg[u][2 * wv + 1] * inv);
        }
#pragma unroll
        for (int i = 0; i < 5; ++i) {
            float o[8];
#pragma unroll
            for (int d = 0; d < 8; ++d) o[d] = 0.f;
#pragma unroll
            for (int wp = 0; wp < 4; ++wp) {
                const u32x4 v0 = k[i][2 * wp], v1 = k[i][2 * wp + 1];
#pragma unroll
                for (int j = 0; j < 4; ++j) {
                    const u32 pw = pk[4 * i + j >= 10][wp];
                    o[2 * j] = dot2bf(__builtin_amdgcn_perm(v1[j], v0[j], 0x05040100u), pw, o[2 * j]);
                    o[2 * j + 1] = dot2bf(__builtin_amdgcn_perm(v1[j], v0[j], 0x07060302u), pw, o[2 * j + 1]);
                }
            }
            gst16c(p.ws + OFF_OB + (size_t)(t0 >> 5) * 40960 + 512 * i, (u32)(m * 2560 + tl * 16), u32x4{cvtpk(o[0], o[1]), cvtpk(o[2], o[3]), cvtpk(o[4], o[5]), cvtpk(o[6], o[7])});
        }
    }
}

#define XPF 1
#define PFQ 0
#define PFN 8
#define OLAG 3
constexpr int QN = 176, QF = 19;
struct QTab { u16 e[QN]; int n, nf; u32 tt[64]; };
typedef const __attribute__((address_space(4))) int* KI;
constexpr int QT_OFF = (88 + 48) / 4;
DI int pick_task(char* fl, KI k, int gr) {
    u32 r;
    asm volatile("s_mov_b64 exec, 1\n\ts_nop 3\n\tglobal_atomic_add %0, %1, %2, %3 sc0 sc1\n\ts_mov_b64 exec, -1\n\ts_waitcnt vmcnt(0)"
                 : "=&v"(r) : "v"((F_QH + gr) * 128), "v"(1), "s"(fl) : "memory");
    const int x = uni((int)r) + 1 + QF;
    if (x >= k[QT_OFF + QN / 2]) return -1;
    const u32 e2 = (u32)k[QT_OFF + (x >> 1)];
    return (int)(x & 1 ? e2 >> 16 : e2 & 0xffffu);
}
__global__ __launch_bounds__(256, 1) void k_main(Ptrs p0, ChainCfg cfg, QTab tab) {
    extern __shared__ __attribute__((aligned(16))) char smem[];
    u32 lds = (u32)(size_t)LDSP(char, smem);
#define HALF 0
    int n = HALF ? (blockIdx.x & 4) * 32 + (int)(blockIdx.x >> 3) * 4 + (blockIdx.x & 3) : blockIdx.x, gr = n & 7;
    int w = uni(threadIdx.x >> 6);
    {
        u64 pc; int len;
        asm volatile("s_getpc_b64 %0\n.Lkm_first:\n\ts_mov_b32 %1, .Lkm_last-.Lkm_first\n\ts_max_i32 %1, %1, .Lkm_lu-.Lkm_first\n\ts_max_i32 %1, %1, .Lkm_lv-.Lkm_first\n\t"
                     "s_max_i32 %1, %1, .Lkm_g2-.Lkm_first\n\ts_max_i32 %1, %1, .Lkm_g4-.Lkm_first" : "=s"(pc), "=s"(len) : : "scc");
        if (w == 3) pfl((const char*)pc, (u32)min((int)(blockIdx.x >> 3) * 8192 + (int)(threadIdx.x & 63) * 128, max(len, 0)), lds);
    }
    karg_warm();
    if (n < NH) { score_role(p0, cfg, n, lds, w); return; }
    if (n < NS) { proj_role(p0, cfg, n - NH, lds, w); return; }
    const bool isv = n < NS + NVB;
#define qslot (lds + GRING * GBUF)
    int sdw = -1;
    int en = isv ? Q_VD + (n & 24) : tab.e[(n - NS - NVB) >> 3] & 511;
    u32 phi0 = 0;
    Sync y;
    if (XPF && n < 160 && w >= 2) pfl(p0.x, (u32)min(((n - NS) >> 3) * 128 + (w & 1) * 64 + lanex(), 1279) * 128, lds);
    if (n >= 160) {
        const int lane = lanex();
        if (n >= 192 && (n & 8)) {
            init_task(p0, (n & 7) + ((n - 192) >> 4) * 8, lds, w);
            barrier();
#pragma unroll
            for (int i = 0; i < 3; ++i) pfl(p0.wqkvb, (u32)min((((n - 192) >> 4) * 8 + (n & 7)) * 768 + i * 256 + w * 64 + lane, 19199 + 0) * 128, lds);
            pfl(p0.wob, (u32)min((((n - 192) >> 4) * 8 + (n & 7)) * 256 + w * 64 + lane, 6399) * 128, lds);
        } else {
            const int h = n < 192 ? n & 31 : (n & 7) + ((n - 192) >> 4) * 8;
            xcopy_task(p0, h + (n < 192 ? 0 : 32), w);
#pragma unroll
            for (int i = 0; i < PFN; ++i) {
                if (n >= 192) break;
                const int l = 64 * min(i * 4 + w, 29) + lane;
                pfl(p0.wo + (size_t)h * 245760, (u32)(l * 128), lds);
                if (PFQ) pfl(p0.wq + h * 384, (u32)((l / 3) * 12288 + (l % 3) * 128), lds);
            }
        }
        waitvm<0>();
        if (w == 0) {
            sync_ld(y, p0.ws);
            waitvm<0>();
            *LDSP(u32, qslot) = (u32)sync_end(y, p0.ws, phi0, true);
            xcopy_wait(p0.ws, sync_tag(y.e));
        }
        barrier();
        sdw = uni((int)*LDSP(u32, qslot));
        barrier();
    }
    while (true) {
        typedef char* const __attribute__((address_space(4)))* KP;
        KP kp = (KP)__builtin_amdgcn_kernarg_segment_ptr();
        asm volatile("" : "+s"(kp), "+s"(lds), "+s"(w), "+s"(gr), "+s"(n));
        __builtin_assume(w >= 0 && w < 4 && gr >= 0 && gr < 8);
        const Ptrs p{kp[0], kp[1], kp[2], kp[3], kp[4], kp[5], kp[6], kp[7], kp[8], kp[9], kp[10]};
        const int ty = en & 7, gate = en >> 9;
        const bool fu = ty == Q_PX && (en >> 3 & 63) < 8;
        const int ix = fu ? (en & 504) + gr : en >> 3 & 63, sf = ((KI)kp)[QT_OFF + QN / 2 + 1] & 255;
        char* const fl = p.ws + OFF_ST;
        const bool raw = sdw < 0;
        int t0 = gate - 1, f1 = F_ONE, t1 = 0, f2 = F_ONE, t2 = 0, f3 = F_ONE, t3 = 0, dt = cfg.T2;
        const int os = ix >= 40 ? 44 + (8 * ix + gr) / 20 : (8 * ix + gr) / 5;
        const int bb = ty == Q_BO ? (8 * ix + gr) >> 2 : os;
        if (ty == Q_BA) { f1 = F_B + (gr & RM) | ix << 16; t1 = 5; f2 = F_B + (gr & RM) | max(ix - 1, 0) << 16; t2 = 5; }
        if (ty == Q_BO) { f1 = F_B + (gr & RM) | (8 + bb) << 16; t1 = 8; }
        if (ty == Q_PX) { f1 = F_Q + pblk(ix) * 8 + (fu ? gr & RM : gr) | fu << 16; t1 = fu ? 32 : 4; if (fu) t0 += gr * (((KI)kp)[QT_OFF + QN / 2 + 1] >> 8) >> 3; if (ix < PXT) t0 = max(t0, ix + 1); }
        if (ty == Q_OUT) { f3 = F_B + (gr & RM) | (16 + (os >> 3)) << 16; t3 = 4; if (os < 60) { f1 = F_PX + os; t1 = 8; t0 = max(t0, os + (os < PXT ? OLAG : 1)); if (os >= PXT) dt = cfg.s1; } else { f1 = F_VD + 64 + (gr & RM); t1 = 32; } }
        const bool skip = fu ? ix >= sf : ty == Q_OUT && ix < 40 && (os < sf || os >= 60);
        if ((gate || ty >= Q_BA) && !skip) wg_wait(fl, w, F_CUR + gr, t0, f1, t1, f2, t2, f3, t3, dt, false);
        int sig = F_END, nrep = 1;
        if (skip) {}
        else if (ty <= Q_QKVB || ty == Q_BO) {
            const bool ab = ty <= Q_KQ;
            sig = ty == Q_VD ? F_VD + (ix >> 2) * 8 : (ty == Q_KQ ? F_Q + (ix >> 2) * 8 : (ix & 4 ? F_Q : F_K + 8 * ix)) + gr;
            if (!ab) sig = F_B | (bb + (ty == Q_BO ? 16 : 0)) << 16;
            nrep = ty == Q_VD || !ab ? RM + 1 : 1;
            if (ty == Q_VD && ix < 4) { sig += n & 32 ? 4 : 0; nrep = 4; }
            run_gemm(p, ty, ab ? ix >> 2 : bb, ab ? gr + 8 * (ix & 3) : 8 * ix + gr - (ty == Q_BO ? 4 : 5) * bb, (n >> 5) & 1, raw, y, lds, w);
            if (raw) sdw = sync_end(y, p.ws, phi0, w == 0);
        } else {
            if (ty == Q_BA) {
                battn_task(p, (8 * ix + gr) * 32, lds, w);
                sig = F_B | (8 + ix) << 16; nrep = RM + 1;
            } else if (ty == Q_PX) {
                if (ix < PXT) {
#pragma nounroll
                    for (int i = fu ? 0 : gr; i < (fu ? 8 : gr + 1); ++i) px_wave<false>(p, cfg, ix, i + 8 * w, ix - cfg.fast, w, lds, fu);
                } else px_wave<true>(p, cfg, ix, gr + 8 * w, -1, w, lds, false);
                sig = F_PX + ix;
                if (fu) wg_wait(fl, w, F_ONE, 0, F_B + (gr & RM) | (16 + (ix >> 3)) << 16, 4, F_ONE, 0, F_ONE, 0, dt, false);
            }
            if (ty == Q_OUT || fu) {
                const int cg = 8 * ix + gr - 5 * os;
                if (ix >= 40 && !fu) out_tail(p, cfg, os, 8 * ix + gr - 20 * (os - 44), lds, w, sdw, gr);
                else out_task(p, fu ? ix : os, fu ? 0 : cg, fu ? 5 : cg + 1, lds, (fu ? ix : os) + 1 - cfg.fast, w, sdw);
                sig = F_END;
            }
            waitvm<0>();
        }
        barrier();
        if (w == 0 && sig != F_END) flag_add(fl, sig, nrep);
        if (w == 1 && sig >= F_Q && sig < F_N) flag_add(fl, sig - gr | 1 << 16, RM + 1);
        if (raw && !isv && w == 0) xcopy_wait(p.ws, sync_tag(y.e));
        if (isv) break;
        if (w == 0) *LDSP(int, qslot) = pick_task(fl, (KI)kp, gr);
        barrier();
        en = uni(*LDSP(int, qslot));
        if (en < 0) return;
        barrier();
    }
    value_role(p0, cfg, n - NS, sdw, phi0, lds, w);
}

#define QCFG 14, 11, 3, 5, 0, 50, 36
#define LVDEF "00>==>>>;;<<<<==;988889::::::::::::::;;;;;;;;;;;:988776666668886"
constexpr int DX = 240, DUA = 330, DUB = 40, DUC = 4;
static ChainCfg dev_cfg(ChainCfg c) { c.T = PDL; c.cR = ODL; c.s1 = TDT; c.T2 = WSLEEP; c.cR2 = DX; c.dU = DUA; c.s2 = DUB; c.T3 = DUC; c.cR3 = 4 | 4 << 8 | 0 << 16; return c; }
static QTab set_sched(QTab t, const ChainCfg c, const char* lv) {
    for (int s = 0; s < 64; ++s) {
        const int d = lv[s] - '0';
        t.tt[s] = (u32)((s < c.s1 ? c.T : (s < c.s2 ? c.T2 : c.T3)) + 4 * d) | (u32)((s < c.s1 ? c.cR : (s < c.s2 ? c.cR2 : c.cR3)) + 5 * d / 2) << 16;
    }
    t.tt[0] = (u32)270 | (u32)165 << 16;
    t.tt[1] = (u32)230 | (u32)152 << 16;
    return t;
}
#define QORD "bbqV3bK3m44bm55bm66m77m88"
#define GATES "11 13 15 17 22 24 32 36 19 22 29 42 13 17 21 26 33 41"
static QTab make_tab(int lv, int lk, int b0, int d1, int d2, int lg, int nf) {
    QTab t{};
    int n = 0, dl[QN], nq = 0;
    auto add = [&](int ty, int ix) { t.e[n] = (u16)(ty | ix << 3); dl[n++] = ty == Q_VD || ty == Q_KQ ? 8 * (ix / 4 - 1) - (ty == Q_VD ? lv : lk) - (ix >= 32 ? 0 : 0) : 99; };
    for (int i = 1; i < 4; ++i) add(Q_VD, i);
    for (int i = 0; i < 16; ++i) add(i < 4 ? Q_H : ((i & 8) ? Q_VD : Q_KQ), i < 8 ? i : 4 + (i & 1) * 4 + ((i - 8) >> 1));
    for (int i = 0; i < 4; ++i) add(Q_KQ, 8 + i);
    for (const char* c = QORD; *c; ++c) {
        if (*c == 'b') add(Q_QKVB, nq++);
        else if (*c == 'q') for (int i = 4; i < 8; ++i) add(Q_H, i);
        else if (*c == '.') dl[n - 1] |= 1 << 20;
        else if (*c == 'n') { const int b = *++c - '0'; for (int i = 0; i < 8; ++i) add(i & 1 ? Q_KQ : Q_VD, 4 * b + (i >> 1) - (i & 1) * 2); }
        else if (*c == 'h' || *c == 'e') { const int b = 4 * (c[1] - '0') + (*c == 'e') * 2; ++c; add(Q_KQ, b); add(Q_KQ, b + 1); }
        else if (*c == 'm') { const int b = c[1] - '0', b2 = c[2] - '0'; c += 2; for (int i = 0; i < 8; ++i) add(i & 1 ? Q_KQ : Q_VD, 4 * (i & 1 ? b2 : b) + ((i >> 1) ^ (i & 2 ? 0 : 0))); }
        else {
            const int ty = (*c | 32) == 'v' ? Q_VD : Q_KQ, two = *c >> 5 & 1, b = *++c - '0';
            for (int i = 0; i < (4 << two); ++i) add(ty, 4 * (b + (i & two)) + (i >> two));
        }
    }
    const int gap = 8;
    for (int i = n - 1, m = 999; i >= 0; --i) { const int pz = dl[i] > 999; m = min(8 * (pz ? dl[i] - (1 << 20) : dl[i]), m - gap - 8 * pz); dl[i] = max(m, 0); }
    auto put = [&](int ty, int ix, int g, int pos) {
        int i = n++;
        for (; dl[i - 1] > pos; --i) { t.e[i] = t.e[i - 1]; dl[i] = dl[i - 1]; }
        t.e[i] = (u16)(ty | ix << 3 | (g + 1) << 9); dl[i] = pos;
    };
    for (int i = 0; i < n; ++i) if (dl[i] >= 8) t.e[i] |= (u16)((dl[i] / 8 + 1) << 9);
    const char* gs = GATES;
    auto gt = [&](int i) { return 10 * (gs[3 * i] - '0') + gs[3 * i + 1] - '0'; };
    for (int i = 0; i < 8; ++i) put(Q_BA, i, gt(i), 8 * gt(i));
    for (int i = 0; i < 4; ++i) put(Q_BO, i, gt(8 + i), 8 * gt(8 + i));
    for (int i = 0; 8 * i < nf; ++i) put(Q_PX, i, gt(12 + i), 8 * (max(gt(12 + i), 8 * i + 1) + b0));
    for (int s = nf; s < NSEG; ++s) { const int g = s < PXT ? s + 1 : max(s - 3, lg); put(Q_PX, s, g, 8 * g); }
    for (int x = 5 * nf / 8; x < 38; ++x) { const int os = (8 * x + 4) / 5; put(Q_OUT, x, 0, 8 * (os + (os < PXT ? OLAG : 1))); }
    for (int x = 40; x < 50; ++x) { const int g = max(44 + 8 * x / 20 - 3, lg); put(Q_OUT, x, g, 8 * g + 4); }
    t.n = n; t.nf = nf | d1 << 8;
    return t;
}

extern "C" void kernel_launch(void* const* d_in, const int* in_sizes, int n_in, void* d_out, int out_size, void* d_ws,
                              size_t ws_size, hipStream_t stream) {
    Ptrs p{(const char*)d_in[0], (const char*)d_in[1], (const char*)d_in[2], (const char*)d_in[3], (const char*)d_in[4], (const char*)d_in[5],
           (const char*)d_in[8], (const char*)d_in[9], (const char*)d_in[10], (char*)d_out, (char*)(((size_t)d_ws + 0x1fffff) & ~(size_t)0x1fffff)};
    (void)hipFuncSetAttribute((const void*)k_main, hipFuncAttributeMaxDynamicSharedMemorySize, S_LDS);
    k_main<<<256, 256, S_LDS, stream>>>(p, dev_cfg(ChainCfg{CFG}), set_sched(make_tab(QCFG), ChainCfg{CFG}, LVDEF));
    static_assert(sizeof(Ptrs) + sizeof(ChainCfg) + sizeof(QTab) == TT_OFF + 256, "");
}
```

```cpp
#define HPI 99
#include <hip/hip_runtime.h>
#pragma clang diagnostic ignored "-Wint-to-pointer-cast"

typedef unsigned short u16;
typedef unsigned int u32;
typedef unsigned long long u64;
typedef __bf16 bf16x8 __attribute__((ext_vector_type(8)));
typedef __bf16 bf16x16 __attribute__((ext_vector_type(16)));
typedef short s16x4 __attribute__((ext_vector_type(4)));
typedef float f32x4 __attribute__((ext_vector_type(4)));
typedef unsigned u32x4 __attribute__((ext_vector_type(4)));
typedef unsigned u32x2 __attribute__((ext_vector_type(2)));
typedef unsigned u32x8 __attribute__((ext_vector_type(8)));
typedef unsigned u32x16 __attribute__((ext_vector_type(16)));
typedef unsigned u32x32 __attribute__((ext_vector_type(32)));
#define DI __device__ __forceinline__
#define LDSP(T, a) ((__attribute__((address_space(3))) T*)(a))

constexpr int S_LEN = 2048, DM = 640, NH = 32, DH = 192, NSEG = 64, HD = NH * DH;
#define NSAMP 4
constexpr int NREG = 13;
constexpr int NV = 80, NVB = 40;
constexpr int NS = 64;
#define CFG 154, 103, 100, 20, 57, 16, 154, 103, 52, 154, 103, 6

constexpr size_t U_STRIDE = NV * 4096, D_STRIDE = NV * 16384;
constexpr size_t HO_STRIDE = 65536;
constexpr size_t OFF_ST = NSEG * HO_STRIDE;
enum { F_ACK = 0, F_ONE = 3, F_VD = 16, F_QH = 96, F_B = 104, F_CUR = 128, F_PROG = 136 - 8 * 62, F_UD = 152, F_PX = 160, F_K = 224, F_END = 512, F_Q = 520, F_N = 592 };
constexpr size_t OFF_LAT = OFF_ST + F_N * 128;
constexpr size_t OFF_KX = 5u << 20;
constexpr size_t OFF_QX = OFF_KX + (size_t)S_LEN * HD * 2;
constexpr size_t OFF_D = OFF_QX + (size_t)S_LEN * HD * 2;
constexpr size_t OFF_U = (OFF_D + NSEG * D_STRIDE + 0x1fffff) & ~(size_t)0x1fffff;
constexpr size_t OFF_KM = OFF_U + (22u << 20);
constexpr size_t OFF_PX = OFF_KM + (size_t)NSEG * NH * 3072;
constexpr size_t OFF_QKVB = OFF_PX + (size_t)NSEG * 81920;
constexpr size_t OFF_OB = OFF_QKVB + (size_t)S_LEN * 1920 * 2;
constexpr size_t OFF_BOUT = OFF_OB + (size_t)S_LEN * DM * 2;
constexpr size_t OFF_GATE = OFF_BOUT + (size_t)S_LEN * DM * 2;
constexpr size_t OFF_XF = OFF_GATE + S_LEN * 4;
constexpr size_t WS_END = OFF_XF + (size_t)S_LEN * DM * 2;
static_assert(WS_END + (2u << 20) <= (256u << 20), "workspace");

typedef __bf16 bf16x2 __attribute__((ext_vector_type(2)));
typedef float f32x2 __attribute__((ext_vector_type(2)));
DI u32 cvtpk(float lo, float hi) { return __builtin_bit_cast(u32, __builtin_convertvector(f32x2{lo, hi}, bf16x2)); }
DI float rbf(float f) { return __uint_as_float(cvtpk(f, f) << 16); }
DI f32x4 mfma16(u32x4 a, u32x4 b, f32x4 c) {
    return __builtin_amdgcn_mfma_f32_16x16x32_bf16(__builtin_bit_cast(bf16x8, a), __builtin_bit_cast(bf16x8, b), c, 0, 0, 0);
}
DI u32x2 trread(u32 a) {
    return __builtin_bit_cast(u32x2, __builtin_amdgcn_ds_read_tr16_b64_v4i16(LDSP(s16x4, a)));
}
DI void glds16c(const char* sbase, u32 voff, u32 lds) {
    u32 sv;
    asm volatile("s_mov_b32 %0, m0\n\ts_mov_b32 m0, %3\n\ts_nop 0\n\tglobal_load_lds_dwordx4 %1, %2 sc1\n\ts_mov_b32 m0, %0"
                 : "=&s"(sv) : "v"(voff), "s"(sbase), "s"(lds) : "memory");
}
template <int IMM>
DI void gld16(u32x4& r, const char* sbase, u32 voff) {
    asm volatile("s_nop 4\n\tglobal_load_dwordx4 %0, %1, %2 offset:%3" : "=v"(r) : "v"(voff), "s"(sbase), "i"(IMM) : "memory");
}
DI void gst16(char* sbase, u32 voff, u32x4 v) {
    asm volatile("global_store_dwordx4 %0, %1, %2 sc1\n\ts_nop 1" : : "v"(voff), "v"(v), "s"(sbase) : "memory");
}
#define OST " nt"
DI void gst16p(char* sbase, u32 voff, u32x4 v) {
    asm volatile("s_nop 4\n\tglobal_store_dwordx4 %0, %1, %2" OST "\n\ts_nop 1" : : "v"(voff), "v"(v), "s"(sbase) : "memory");
}
DI void gst8(char* sbase, u32 voff, u32x2 v) {
    asm volatile("global_store_dwordx2 %0, %1, %2 sc1\n\ts_nop 1" : : "v"(voff), "v"(v), "s"(sbase) : "memory");
}
template <int N>
DI void waitvm() { asm volatile("s_waitcnt vmcnt(%0)" : : "i"(N) : "memory"); }
DI void barrier() { asm volatile("s_waitcnt lgkmcnt(0)\n\ts_barrier" : : : "memory"); }
#define PIN() __builtin_amdgcn_sched_barrier(0)
DI int uni(int v) { return __builtin_amdgcn_readfirstlane(v); }
DI int lanex() {
    int t;
    asm volatile("v_mbcnt_lo_u32_b32 %0, -1, 0\n\tv_mbcnt_hi_u32_b32 %0, -1, %0" : "=v"(t));
    __builtin_assume(t >= 0 && t < 64);
    return t;
}
DI void flag_ld(u32& r, const char* fl, int idx) {
    asm volatile("s_nop 4\n\tglobal_load_dword %0, %1, %2 sc1" : "=v"(r) : "v"((idx & 0xffff) * 128 + (idx >> 14)), "s"(fl) : "memory");
}
DI void flag_ldm(u32& r, const char* fl, int idx, bool on) {
    asm volatile("v_mov_b32 %0, 1000\n\ts_mov_b32 exec_lo, %3\n\ts_mov_b32 exec_hi, 0\n\tglobal_load_dword %0, %1, %2 sc1\n\ts_mov_b64 exec, -1" : "=&v"(r) : "v"(idx * 128), "s"(fl), "s"(uni(on ? 1 : 0)) : "memory");
}
DI void flag_wait(const char* fl, int idx, int target) {
#pragma nounroll
    for (int it = 0; it < (1 << 16); ++it) {
        u32 v;
        flag_ld(v, fl, idx);
        asm volatile("s_waitcnt vmcnt(0)" : "+v"(v) : : "memory");
        if (uni((int)(v + 1u)) >= target) break;
        __builtin_amdgcn_s_sleep(6);
    }
}
DI void flag_add(char* fl, int idx, int nrep) {
    const int lane = lanex();
    if (lane < nrep) asm volatile("s_nop 4\n\tglobal_atomic_add %0, %1, %2 sc1" : : "v"(((idx & 0xffff) + lane) * 128 + (idx >> 14)), "v"(1), "s"(fl) : "memory");
}
DI void acquire() { asm volatile("buffer_inv sc1\n\ts_waitcnt vmcnt(0)" : : : "memory"); }

template <int V> struct IC { static constexpr int v = V; };
template <int B, int E, class Fn>
DI void sfor(Fn&& fn) { if constexpr (B < E) { fn(IC<B>{}); sfor<B + 1, E>(fn); } }
enum { T_QK = 0, T_VD = 1, T_BL = 3 };
constexpr int GBUF = 24576, GRING = 6;
struct GemmArgs {
    const char *w1, *w1b;
    const char* w2;
    const char* x;
    char *out, *outb;
    int ldw;
    char* sig;
    int type, np1, tn;
    int sV, sP, sQ;
    int xb;
};
DI const char* unip(const char* p) {
    const u64 v = (u64)p;
    return (const char*)(((u64)(u32)uni((int)(v >> 32)) << 32) | (u32)uni((int)v));
}
template <int Z>
DI void mf1(f32x4& c, const u32x4& a, const u32x4& b) {
    if constexpr (Z) asm volatile("v_mfma_f32_16x16x32_bf16 %0, %1, %2, 0" : "=&v"(c) : "v"(a), "a"(b));
    else asm volatile("v_mfma_f32_16x16x32_bf16 %0, %1, %2, %0" : "+v"(c) : "v"(a), "a"(b));
}
template <int Z>
DI void mf2(f32x4& c, const u32x4& a, const u32x4& b) {
    if constexpr (Z) asm volatile("v_mfma_f32_16x16x32_bf16 %0, %1, %2, 0" : "=&v"(c) : "v"(a), "v"(b));
    else asm volatile("v_mfma_f32_16x16x32_bf16 %0, %1, %2, %0" : "+v"(c) : "v"(a), "v"(b));
}
template <int IMM>
DI void gld16a(u32x4& r, const char* sbase, u32 voff) {
    asm volatile("global_load_dwordx4 %0, %1, %2 offset:%3 sc1" : "+a"(r) : "v"(voff), "s"(sbase), "i"(IMM) : "memory");
}
DI void setm0(u32 v) { asm volatile("s_mov_b32 m0, %0" : : "s"(v) : "memory"); }
template <int IMM>
DI void gldsm(const char* sbase, u32 voff) {
    asm volatile("global_load_lds_dwordx4 %0, %1 offset:%2" : : "v"(voff), "s"(sbase), "i"(IMM) : "memory");
}
DI void gst16n(char* sbase, u32 voff, u32x4 v) {
    asm volatile("global_store_dwordx4 %0, %1, %2 sc1" : : "v"(voff), "v"(v), "s"(sbase) : "memory");
}
template <int N>
DI void waitbar() { asm volatile("s_waitcnt vmcnt(%0)\n\ts_barrier" : : "i"(N) : "memory"); }
#define LGKM(n) __builtin_amdgcn_s_waitcnt(0xC07F | ((n) << 8))

DI int xhalf() { u32 x; asm volatile("s_getreg_b32 %0, hwreg(HW_REG_XCC_ID)" : "=s"(x)); return (int)((x >> 2) & 1); }
struct Sync { u32x2 r, g, e; };
DI void sync_ld(Sync& y, const char* ws) {
    asm volatile("s_nop 4\n\tglobal_load_dwordx2 %0, %3, %5 sc1\n\tglobal_load_dwordx2 %1, %4, %5 offset:512 sc1\n\tglobal_load_dwordx2 %2, %4, %5 offset:1024 sc1"
                 : "=&v"(y.r), "=&v"(y.g), "=&v"(y.e) : "v"((lanex() & 31) * 8), "v"(0), "s"(ws + OFF_LAT) : "memory");
}
DI u64 sync_tag(u32x2 e) { return (((u64)e[1] << 32 | e[0]) + 1) << 1; }
DI int sync_end(Sync& y, const char* ws, u32& phi, bool ack) {
    asm volatile("" : "+v"(y.r), "+v"(y.g), "+v"(y.e));
#pragma nounroll
    for (int it = 0; it < (1 << 16); ++it) {
        const u64 t = sync_tag(y.e);
        if (__builtin_amdgcn_ballot_w64((y.r[0] & ~1u) != (u32)t || y.r[1] != (u32)(t >> 32) || y.g[0] != (u32)t) == 0) break;
        __builtin_amdgcn_s_sleep(24);
        sync_ld(y, ws);
        asm volatile("s_waitcnt vmcnt(0)" : "+v"(y.r), "+v"(y.g), "+v"(y.e) : : "memory");
    }
    if (ack) flag_add((char*)ws + OFF_ST, F_ACK, 1);
    phi = (u32)uni((int)y.g[1]);
    const u32 b = (u32)__builtin_amdgcn_ballot_w64(y.r[0] & 1), all = __builtin_popcount(b) > 16;
    u32 q = 0;
#pragma unroll
    for (int r = 0; r < NREG; ++r) {
        const u32 c = r < 2 ? __builtin_popcount(b & (0x55u << r)) + all : 2 * __builtin_popcount((b >> (4 + 2 * r)) & 3) + all;
        q |= ((c > 2) ^ (u32)((r < 2 ? r : r + (OFF_U >> 21)) & 1)) << r;
    }
    return uni((int)(q ^ 3u * xhalf()));
}
DI void pace(int on) {
    asm volatile("s_cmp_ge_i32 %0, 0\n\ts_cbranch_scc1 .Lp%=\n\ts_waitcnt vmcnt(0)\n.Lp%=:" : : "s"(on) : "memory", "scc");
}
#define SMO 48
#define RM 7
#define BLS 1
#define EPG 1
#define PXT 58
#define PDX 40
#define SMB 48
#define SMP SMO
template <int C>
DI void gap(int on) {
    if constexpr (C > 0) asm volatile("s_cmp_ge_i32 %0, 0\n\ts_cbranch_scc1 .Lg%=\n\ts_nop %1\n\ts_nop %2\n\ts_nop %3\n.Lg%=:" : : "s"(on), "n"(C / 12 - 1), "n"(C / 12 - 1), "n"(C / 12 - 1) : "memory", "scc");
}
DI void early_add(char* ctr, int c) {
    asm volatile("s_cmp_lg_u32 %1, 1\n\ts_cbranch_scc1 .Le%=\n\ts_mov_b64 exec, 1\n\ts_nop 1\n\tglobal_atomic_add %2, %3, %0 sc1\n\ts_mov_b64 exec, -1\n.Le%=:"
                 : : "s"(ctr), "s"(c), "v"(0), "v"(1) : "memory", "scc");
}
#define XL 2
template <int NT>
DI void gemm_task(const GemmArgs& g, Sync& y, const char* ws, u32 lds, int w) {
    constexpr int FD = 5, NX = 2 * NT, OPS = NX + 6, BI = 24 - FD;
    const int lane = lanex();
    const int n = lane & 15, q = lane >> 4, qq = (lane >> 2) & 3, pp = lane & 3;
    const bool p2 = g.type == T_VD;
    const int rw = lane >> 3, sp = (lane >> 1) & 3;
    const u32 v2b = (u32)(rw * 1280 + 16 * (lane & 1) + 5120);
    auto v2o = [&](int i) { return v2b + (u32)(((sp ^ (rw >> 1)) << 5) + i * 9216); };
    u32 wv[6];
#pragma unroll
    for (int i = 0; i < 6; ++i)
        wv[i] = (u32)((rw + 8 * (i / 3)) * g.ldw + ((sp ^ (((rw >> 1) & 1) + 2 * (i / 3))) << 5) + 16 * (lane & 1) + (i % 3) * 128 + 5120 - 1024 * i);
    const char* const wp2 = g.w2 + (size_t)(48 * w) * 1280 - 2048;
    const char* wp = unip(g.w1 + (size_t)(16 * w) * g.ldw - 2048);
    int wstep = uni(64 * g.ldw);
    u32 wdst = lds + (u32)w * 6144 + 3072;
    const u32 wdlast = wdst + (GRING - 1) * GBUF;
    auto wring = [&]() { wdst = wdst == wdlast ? wdst - (GRING - 1) * GBUF : wdst + GBUF; };
    auto wload = [&](int i) {
        setm0(wdst);
        wring();
        asm volatile("s_nop 0");
        sfor<0, 6>([&](auto I) { gldsm<1024 * (I.v - 3)>(wp, wv[I.v]); });
        wp += wstep;
    };
    u32x4 vf[NT][6];
    u32x4 fr[FD + 1];
    u32x4 xr[5][NX];
    auto xtie = [&](u32x4 (&x)[NX]) {
#pragma unroll
        for (int i = 0; i < NX; i += 4) asm volatile("" : "+a"(x[i]), "+a"(x[i + 1]), "+a"(x[i + 2]), "+a"(x[i + 3]));
    };
#pragma unroll
    for (int i = 0; i < 5 * NX; ++i) asm volatile("" : "=a"(xr[i / NX][i % NX]));
    u32x2 flo;
    u32 c[4], slot = 0;
    int sd = GBUF;
    auto sring = [&]() { sd = slot == (GRING - 1) * GBUF ? -(GRING - 1) * GBUF : GBUF; slot += (u32)sd; };
    {
        u32 xo[NT];
#pragma unroll
        for (int tt = 0; tt < NT; ++tt) {
            const int tok = (g.type == T_VD) ? 8 * (n >> 2) + 4 * (tt & 1) + (n & 3) : 16 * (tt & 1) + n;
            xo[tt] = (u32)((NT / 2 * w + (tt >> 1)) * 40960 + (g.xb ? q * 512 + tok * 16 : q * 16 + tok * 1280));
        }
        f32x4 acc[12][NT];
        const bool rm = NT == 4 && g.type == T_BL;
        const int sw = (qq >> 1) + 2 * (q & 1);
#pragma unroll
        for (int t = 0; t < 4; ++t)
            c[t] = lds + q * 3072 + qq * 128 + (rm ? ((((t & 2) + (pp >> 1)) ^ sw) << 5) + 16 * (pp & 1) + 8 * ((pp >> 1) ^ (t & 1)) : ((t ^ sw) << 5) + 8 * pp);
        auto rlo = [&](int f) { flo = trread(c[f & 3] + (f / 12) * 12288 + ((f % 12) >> 2) * 1024); };
        auto rhi = [&](int f) {
            const u32x2 hi = trread(c[f & 3] + (f / 12) * 12288 + ((f % 12) >> 2) * 1024 + 512);
            fr[f % (FD + 1)] = u32x4{flo[0], flo[1], hi[0], hi[1]};
        };
        const int xk = uni(g.xb ? 2048 : 64);
        const char *xp = g.x, *xq = xp + xk;
        sfor<0, 4>([&](auto J) {
            if constexpr (J.v < XL) { sfor<0, NX>([&](auto I) { gld16a<0>(xr[J.v][I.v], I.v & 1 ? xq : xp, xo[I.v >> 1]); }); xp += 2 * xk; xq += 2 * xk; }
            wload(J.v);
        });
        waitbar<18 + (XL - 1) * NX>();
        xtie(xr[0]);
#pragma unroll
        for (int i = 0; i < FD; ++i) { rlo(i); rhi(i); }
        char* po = g.out;
#pragma nounroll
        for (int ph = g.np1; ph > 0; --ph) {
            sfor<0, 10>([&](auto J) {
                constexpr int j = J.v;
                u32x4 (&xc)[NX] = xr[j % 5];
                u32x4 (&xn)[NX] = xr[(j + XL) % 5];
                sfor<0, 24>([&](auto I) {
                    constexpr int idx = I.v, kk = idx / 12, T = idx % 12, z = j == 0 && kk == 0, f = (idx + FD) % 24;
                    constexpr int mo = NT == 4 ? (idx < BI && idx % 4 != 2 ? idx - (idx + 1) / 4 : -1) : (idx < BI && idx % 2 == 0 ? idx / 2 : -1);
                    auto mem = [&]() {
                        if constexpr (mo >= NX) gldsm<1024 * (mo - NX - 3)>(wp, wv[mo - NX]);
                        else if constexpr (mo >= 0) gld16a<0>(xn[mo], mo & 1 ? xq : xp, xo[mo >> 1]);
                    };
                    auto misc = [&]() {
                        if constexpr (idx == 1) wring();
                        if constexpr (idx == 13) sring();
                        if constexpr (j == 1 && idx == 22) early_add(g.sig, ph + w);
                        if constexpr (idx >= 15 && idx < BI) { c[idx - 15] += (u32)sd; asm volatile("" : "+v"(c[idx - 15])); }
                        const bool more = ph > 1;
                        if constexpr (j != 5) {
                            if constexpr (idx == 20) wp += wstep;
                        } else {
                            if constexpr (idx == 19) { wp = more ? g.w1b + (size_t)(16 * w) * g.ldw - 2048 : wp2; wstep = more ? wstep : (p2 ? 128 : 0); }
                            if constexpr (idx >= 20 && idx < 23) {
#pragma unroll
                                for (int i = 2 * (idx - 20); i < 2 * (idx - 19); ++i) { wv[i] = more ? wv[i] : (p2 ? v2o(i) : 5120u); asm volatile("" : "+v"(wv[i])); }
                            }
                        }
                        if constexpr (j != 9 - XL) {
                            if constexpr (idx == 21) xp += 2 * xk;
                            if constexpr (idx == 22) xq += 2 * xk;
                        } else if constexpr (idx == 23) {
                            xp = g.x; xq = xp + xk;
#pragma unroll
                            for (int i = 0; i < NT; ++i) { xo[i] = more ? xo[i] : 0u; asm volatile("" : "+v"(xo[i])); }
                        }
                    };
                    if constexpr (idx == BI) { waitbar<6 + (XL - 1) * OPS>(); xtie(xr[(j + 1) % 5]); }
                    if constexpr (idx % 2 == 0) LGKM(6);
                    PIN();
                    const u32x4 a = fr[idx % (FD + 1)];
                    mf1<z>(acc[T][0], a, xc[kk]); PIN();
                    rlo(f);
                    if constexpr (idx == 0) setm0(wdst);
                    if constexpr (NT == 2) rhi(f);
                    PIN();
                    mf1<z>(acc[T][1], a, xc[2 + kk]); PIN();
                    if constexpr (NT == 4) {
                        rhi(f); PIN();
                        mf1<z>(acc[T][2], a, xc[4 + kk]); PIN();
                        mem(); PIN();
                        mf1<z>(acc[T][3], a, xc[6 + kk]); PIN();
                    } else { mem(); PIN(); }
                    misc();
                    PIN();
                });
            });
            asm volatile("s_nop 7\n\ts_nop 1");
            PIN();
#pragma unroll
            for (int tt = 0; tt < NT; ++tt)
#pragma unroll
                for (int ks = 0; ks < 6; ++ks)
                    vf[tt][ks] = u32x4{cvtpk(acc[2 * ks][tt][0], acc[2 * ks][tt][1]), cvtpk(acc[2 * ks][tt][2], acc[2 * ks][tt][3]),
                                       cvtpk(acc[2 * ks + 1][tt][0], acc[2 * ks + 1][tt][1]), cvtpk(acc[2 * ks + 1][tt][2], acc[2 * ks + 1][tt][3])};
            if (g.type == T_QK) {
#pragma unroll
                for (int tt = 0; tt < NT; ++tt)
#pragma unroll
                    for (int ks = 0; ks < 6; ++ks) {
                        gst16(po + (size_t)(NT / 2 * w + (tt >> 1)) * (NH * 12288) + (tt & 1) * 6144 + ks * 1024, lane * 16,
                              u32x4{vf[tt][ks][0], vf[tt][ks][2], vf[tt][ks][1], vf[tt][ks][3]});
                        if (NT == 4 && EPG) asm volatile("s_sleep %0" : : "n"(EPG));
                    }
            } else if (rm) {
                const bool sx = q >= 2;
#pragma unroll
                for (int i = 0; i < 3 * NT; ++i) {
                    const int tp = i / 6, ks = i % 6;
                    u32x4 o[2];
#pragma unroll
                    for (int j = 0; j < 4; ++j) {
                        const u32x4 &v = vf[2 * tp][ks], &u = vf[2 * tp + 1][ks];
                        if (BLS) {
                            const auto r = __builtin_amdgcn_permlane32_swap(v[j], u[j], false, false);
                            o[0][j] = r[0]; o[1][j ^ 2] = r[1];
                        } else { o[0][j] = sx ? v[j ^ 2] : v[j]; o[1][j] = sx ? u[j ^ 2] : u[j]; }
                    }
#pragma unroll
                    for (int j = 0; j < 2; ++j) {
                        gst16(po + (size_t)(2 * w + tp) * g.sV + j * (BLS ? 1024 : 256) + ks * 2048, BLS ? (u32)((q & 1) * 512 + (q >> 1) * 256 + n * 16) : (u32)(q * 512 + n * 16), o[j]);
                        if (EPG) asm volatile("s_sleep %0" : : "n"(EPG));
                    }
                }
            }
            po = g.outb;
            PIN();
        }
    }
    if (p2) {
        constexpr int HI = 2048;
        const int r = 4 * (q & 1) + qq, sw = r >> 1;
#pragma unroll
        for (int t = 0; t < 4; ++t) c[t] = lds + slot + (q >> 1) * 1024 + r * 128 + ((t ^ sw) << 5) + 8 * pp;
        sring();
        u32 so = (u32)((n >> 3) * g.sV + q * g.sQ + (n & 7) * 16);
        char* const o0 = g.out + (size_t)(NT / 2 * w) * g.sP;
        char* const o1 = o0 + g.sP;
        const int sv2 = 2 * g.sV;
        auto rlo = [&](int f) { flo = trread(c[f / 6] + (f % 6) * 4096); };
        auto rhi = [&](int f) {
            const u32x2 hi = trread(c[f / 6] + (f % 6) * 4096 + HI);
            fr[f % (FD + 1)] = u32x4{flo[0], flo[1], hi[0], hi[1]};
        };
        f32x4 a2[2][NT];
        u32 e[NT / 2][4];
        auto cv = [&](f32x4 (&a)[NT], int i) { e[i >> 2][i & 3] = cvtpk(a[i >> 1][2 * (i & 1)], a[i >> 1][2 * (i & 1) + 1]); asm volatile("" : "+v"(e[i >> 2][i & 3])); };
        auto st = [&](int pl) { gst16n(pl ? o1 : o0, so, u32x4{e[pl][0], e[pl][1], e[pl][2], e[pl][3]}); };
        PIN();
#pragma unroll
        for (int i = 0; i < FD; ++i) { rlo(i); rhi(i); }
#pragma unroll
        for (int tt = 0; tt < NT; ++tt) a2[1][tt] = f32x4{0.f, 0.f, 0.f, 0.f};
#pragma nounroll
        for (int t = 0; t < g.tn; ++t) {
            sfor<0, 24>([&](auto I) {
                constexpr int idx = I.v, cb = idx / 6, ks = idx % 6, f = (idx + FD) % 24;
                f32x4 (&ac)[NT] = a2[cb & 1];
                f32x4 (&ap)[NT] = a2[(cb & 1) ^ 1];
                constexpr int mo = cb < 2 ? (ks == 0 ? 3 * cb : (ks >= 4 ? 3 * cb + ks - 3 : -1)) : -1;
                auto mem = [&]() {
                    if constexpr (mo >= 0) gldsm<1024 * (mo - 3)>(wp, wv[mo]);
                    if constexpr (ks == 3 || (NT == 4 && ks == 2)) { if (cb > 0 || t > 0) st(NT == 4 ? ks - 2 : 0); }
                };
                auto misc = [&]() {
                    if constexpr (idx == 12) wring();
                    if constexpr (idx == 23) sring();
                    if constexpr (idx == 16) { wp += t < g.tn - 5 ? 128 : 0; }
                    if constexpr (ks == 0) { c[cb] += (u32)sd; asm volatile("" : "+v"(c[cb])); }
                    if constexpr (ks == 4) { so += (cb > 0 || t > 0) ? (u32)sv2 : 0u; asm volatile("" : "+v"(so)); }
                };
                if constexpr (idx == BI) waitbar<NT == 4 ? 38 : 28>();
                if constexpr (idx % 2 == 0) LGKM(6);
                PIN();
                const u32x4 b = fr[idx % (FD + 1)];
                mf2<ks == 0>(ac[0], vf[0][ks], b); PIN();
                rlo(f);
                if constexpr (idx == 0) setm0(wdst);
                if constexpr (NT == 2) rhi(f);
                if constexpr (NT == 4 && ks >= 1 && ks <= 3) cv(ap, ks == 1 ? 0 : (ks == 2 ? 4 : 7));
                PIN();
                mf2<ks == 0>(ac[1], vf[1][ks], b); PIN();
                if constexpr (NT == 4) {
                    rhi(f);
                    if constexpr (ks == 1 || ks == 2) cv(ap, ks == 1 ? 1 : 5);
                    PIN();
                    mf2<ks == 0>(ac[2], vf[2][ks], b); PIN();
                    mem();
                    if constexpr (ks == 1) { cv(ap, 2); cv(ap, 3); }
                    PIN();
                    mf2<ks == 0>(ac[3], vf[3][ks], b); PIN();
                    if constexpr (ks == 2) cv(ap, 6);
                } else {
                    if constexpr (ks == 1 || ks == 2) { cv(ap, 2 * ks - 2); cv(ap, 2 * ks - 1); }
                    mem(); PIN();
                }
                misc();
                PIN();
            });
            asm volatile("s_nop 7\n\ts_nop 1");
        }
        PIN();
#pragma unroll
        for (int i = 0; i < 2 * NT; ++i) cv(a2[1], i);
#pragma unroll
        for (int pl = 0; pl < NT / 2; ++pl) st(pl);
        asm volatile("s_nop 1");
    }
    if constexpr (NT == 2) asm volatile(".Lkm_g2:"); else asm volatile(".Lkm_g4:");
    sync_ld(y, ws);
    waitvm<0>();
    asm volatile("" : "+v"(y.r), "+v"(y.g), "+v"(y.e));
    sfor<0, 5>([&](auto I) { xtie(xr[I.v]); });
}

struct Ptrs {
    const char *x, *mem0, *wq, *wk, *wv, *wo, *wqkvb, *wob, *gw;
    char* out;
    char* ws;
};

enum { Q_H = 0, Q_VD = 1, Q_KQ = 2, Q_QKVB = 3, Q_BA = 4, Q_BO = 5, Q_PX = 6, Q_OUT = 7 };
DI void run_gemm(const Ptrs& p, int ty, int b, int i, int hf, bool raw, Sync& y, u32 lds, int w) {
    GemmArgs g;
    g.sV = g.sP = g.sQ = 0; g.w2 = p.wo; g.ldw = HD * 2; g.np1 = 1; g.tn = 10;
    g.sig = p.ws + OFF_ST + F_END * 128;
    const char* xf = raw ? p.x : p.ws + OFF_XF;
    g.xb = !raw;
    int s0 = 8 * b;
    if (ty == Q_VD) {
        s0 = b < 2 ? 4 * b : s0 - 8;
        g.type = T_VD; g.w1 = g.w1b = p.wv + i * DH * 2;
        g.w2 = p.wo + (size_t)i * DH * 1280;
        g.out = g.outb = p.ws + OFF_D + (size_t)s0 * D_STRIDE + i * 512;
        g.sV = 16384; g.sP = (int)D_STRIDE; g.sQ = 128;
        if (b == 0) { g.tn = 5; g.w2 += hf * 640; g.out += hf * 40 * 16384; }
    } else if (ty == Q_H) {
        s0 = 0;
        g.type = T_QK; g.w1 = g.w1b = (b ? p.wq : p.wk) + i * DH * 2;
        g.out = g.outb = p.ws + (b ? OFF_QX : OFF_KX) + (size_t)i * 12288;
        b = 0;
    } else if (ty == Q_KQ) {
        s0 = b < 2 ? 4 * b : s0 - 8;
        g.type = T_QK; g.w1 = p.wk + i * DH * 2; g.w1b = p.wq + i * DH * 2;
        g.out = p.ws + OFF_KX + (size_t)(s0 * NH + i) * 12288;
        g.outb = g.out + (OFF_QX - OFF_KX);
        g.np1 = 2;
        g.sig = p.ws + OFF_ST + (F_K + b * 32 + i) * 128;
    } else if (ty == Q_QKVB) {
        g.type = T_BL; g.w1 = p.wqkvb + i * 768; g.w1b = g.w1 + 384; g.ldw = 3840;
        g.sV = 1920 * 64; g.np1 = 2;
        g.out = p.ws + OFF_QKVB + (size_t)s0 * g.sV + i * 48 * 512; g.outb = g.out + 24 * 512;
    } else {
        const int c0 = min(i * DH, DM - DH);
        g.type = T_BL; g.w1 = g.w1b = p.wob + c0 * 2; g.ldw = 1280;
        xf = p.ws + OFF_OB;
        g.sV = 40960;
        g.out = g.outb = p.ws + OFF_BOUT + (size_t)s0 * 40960 + c0 * 64;
    }
    g.x = xf + (size_t)s0 * 40960;
    if (ty <= Q_KQ && b < 2) gemm_task<2>(g, y, p.ws, lds, w);
    else gemm_task<4>(g, y, p.ws, lds, w);
}
DI int pblk(int s) { return s < 8 ? s >> 2 : min(s >> 3, 7) + 1; }

template <int IMM>
DI void gld16c(u32x4& r, const char* sbase, u32 voff) {
    asm volatile("global_load_dwordx4 %0, %1, %2 offset:%3 sc1" : "=v"(r) : "v"(voff), "s"(sbase), "i"(IMM) : "memory");
}
DI void gst4c(char* sbase, u32 voff, u32 v) {
    asm volatile("global_store_dword %0, %1, %2 sc1" : : "v"(voff), "v"(v), "s"(sbase) : "memory");
}
template <int N>
DI void waitv(u32x4& r) { asm volatile("s_waitcnt vmcnt(%1)" : "+v"(r) : "i"(N) : "memory"); }
DI u32 mt() { return (u32)__builtin_amdgcn_s_memtime(); }
DI u32 rt() { return (u32)__builtin_amdgcn_s_memrealtime(); }
DI void spin_to(u32 tgt) { while ((int)(mt() - tgt) < 0) {} }
DI u32 max3u(u32 a, u32 b, u32 c) { return max(max(a, b), c); }
DI u32x4 ldsr16(u32 a) { return *LDSP(u32x4, a); }
DI void clocks(u32& r, u32& m) {
    u64 a, b;
    asm volatile("s_memrealtime %0\n\ts_memtime %1\n\ts_waitcnt lgkmcnt(0)" : "=s"(a), "=s"(b) : : "memory");
    r = (u32)a; m = (u32)b;
}
#define SLATE 20
DI void edge(u32& r, u32& m) {
    u64 a, t[9];
    u32 n = 0;
#define RT(i) "s_memrealtime %" #i "\n\ts_nop 1\n\t"
#define RC(i) "\n\ts_cmp_eq_u64 %" #i ", %1\n\ts_addc_u32 %10, %10, 0"
    asm volatile("s_memtime %0\n\t" RT(1) RT(2) RT(3) RT(4) RT(5) RT(6) RT(7) RT(8) "s_memrealtime %9\n\ts_waitcnt lgkmcnt(0)" RC(2) RC(3) RC(4) RC(5) RC(6) RC(7) RC(8) RC(9)
                 : "=&s"(a), "=&s"(t[0]), "=&s"(t[1]), "=&s"(t[2]), "=&s"(t[3]), "=&s"(t[4]), "=&s"(t[5]), "=&s"(t[6]), "=&s"(t[7]), "=&s"(t[8]), "+s"(n) : : "scc", "memory");
    m = (u32)a + 12 * n;
    r = (u32)t[0] + 4;
}
#define SPB 90
struct Clock {
    u32 r0, m0, r1, m1; int cpt;
    DI void init() { edge(r0, m0); r1 = r0; m1 = m0; cpt = 24 * 64; }
    DI u32 sched(u32& phi, int ofs, int G, int mode) {
        if (mode == 0) edge(r1, m1);
        u32 r = r1, m = m1;
        if (mode == 2) clocks(r, m);
        int rem = (int)(phi + (u32)ofs - r);
        if (__builtin_expect(rem < -SLATE, 0)) { const int k = (-rem / G + 1) * G; phi += (u32)k; rem += k; }
        return m + (u32)((min(rem, 3000) * cpt) >> 6) - SPB;
    }
    DI u32 at(u32 t) { u32 r, m; clocks(r, m); return m + (u32)((min(max((int)(t - r), 0), 3000) * cpt) >> 6); }
    DI void calib() {
        const int dr = (int)(r1 - r0);
        if (dr >= 60) { int c = (int)fminf(fmaxf((float)(int)(m1 - m0) * 64.f * __builtin_amdgcn_rcpf((float)dr), 256.f), 2560.f); asm volatile("" : "+v"(c)); cpt = uni(c); r0 = r1; m0 = m1; }
    }
};
#define VISS 0
#define VISV 0
DI void follow(u32& phi, int ofs, u32 t, int G) { const int late = (int)(t - phi) - ofs; if (__builtin_expect(late > 0, 0)) phi += (u32)((late / G + 1) * G); }
DI bool unwritten(const char* base, u32 off) {
#pragma nounroll
    for (int i = 0; i < 10; ++i) {
        u32x4 t;
        gld16c<-4096>(t, base + (i + (i & 8)) * 1024, off); waitv<0>(t);
        if (__builtin_amdgcn_ballot_w64(max3u(max(t[0], t[1]), t[2], t[3]) == 0xffffffffu) != 0) return true;
    }
    return false;
}

DI size_t uslot(int sd, int s) { const int r = (s * 43) >> 8; return OFF_U + (u32)uni(s * (int)U_STRIDE + r * (int)((2u << 20) - 6 * U_STRIDE) + ((sd >> (2 + r)) & 1) * 8192); }
DI size_t mslot(int sd, int s) { return (u32)uni(s * (int)HO_STRIDE + ((sd >> (s >> 5)) & 1) * 8192 + 4096); }
struct ChainCfg { int T, cR, G, dV, fast, s1, T2, cR2, s2, T3, cR3, dU; };
constexpr int TT_OFF = 88 + 48 + 360;
DI void karg_warm() {
    u32 a[13];
    asm volatile("s_load_dword %0, %13, 0\n\t" "s_load_dword %1, %13, 64\n\t" "s_load_dword %2, %13, 128\n\t" "s_load_dword %3, %13, 192\n\t" "s_load_dword %4, %13, 256\n\t" "s_load_dword %5, %13, 320\n\t" "s_load_dword %6, %13, 384\n\t" "s_load_dword %7, %13, 448\n\t" "s_load_dword %8, %13, 512\n\t" "s_load_dword %9, %13, 576\n\t" "s_load_dword %10, %13, 640\n\t" "s_load_dword %11, %13, 704\n\t" "s_load_dword %12, %13, 748\n\t" "s_waitcnt lgkmcnt(0)"
                 : "=&s"(a[0]), "=&s"(a[1]), "=&s"(a[2]), "=&s"(a[3]), "=&s"(a[4]), "=&s"(a[5]), "=&s"(a[6]), "=&s"(a[7]), "=&s"(a[8]), "=&s"(a[9]), "=&s"(a[10]), "=&s"(a[11]), "=&s"(a[12])
                 : "s"(__builtin_amdgcn_kernarg_segment_ptr()) : "memory");
}
DI u32 sched_ld0(int s) {
    u32 r;
    asm volatile("s_load_dword %0, %1, %2 offset:%3\n\ts_waitcnt lgkmcnt(0)" : "=s"(r) : "s"(__builtin_amdgcn_kernarg_segment_ptr()), "s"(4 * s), "n"(TT_OFF) : "memory");
    return r;
}
#define HP 60
#define HQ 96

constexpr int SL_W = 0;
constexpr int SL_IMG = 131072;
constexpr int SL_KX = SL_IMG + 6656;
constexpr int SL_PF = SL_KX + 12288 + 1024;
constexpr int S_LDS = SL_PF + 256;
DI void nap(int t) { const u32 wake = rt() + (u32)t; while ((int)(rt() - wake) < 0) __builtin_amdgcn_s_sleep(4); }
DI void dly(int n) {
    asm volatile(".Ld%=:\n\ts_sub_u32 %0, %0, 1\n\ts_nop 0\n\ts_cbranch_scc0 .Ld%=" : "+s"(n) : : "scc", "memory");
}
DI void pfl(const char* sbase, u32 voff, u32 lds) {
    asm volatile("s_mov_b32 m0, %2\n\ts_nop 3\n\tglobal_load_lds_dword %0, %1" : : "v"(voff), "s"(sbase), "s"(lds + SL_PF) : "memory");
}
DI void score_role(const Ptrs& p, const ChainCfg cfg, int h, u32 lds, int w) {
    const u32 tk_ = rt(); u32 tv_ = tk_;
    const int lane = lanex(), tid = 64 * w + lane;
    const int n = lane & 15, g = lane >> 4, qq = (lane >> 2) & 3, pp = lane & 3;
    u32x32 wa[8], wv[3];
    const u32 wreg = lds + SL_W + (u32)w * 32768;
    const u32 lfa = wreg + lane * 16;
    const char* kxp = p.ws + OFF_KX + (size_t)h * 12288 + w * 3072;
    const u32 kxl = lds + SL_KX;
    char* const fl = p.ws + OFF_ST;
    const u32 kxw = kxl + w * 3072 - (w < 2 ? 16 : 0);
    auto kxdma = [&]() {
        asm volatile("s_mov_b32 m0, %2\n\ts_nop 3\n\tglobal_load_lds_dwordx4 %0, %1 sc1\n\tglobal_load_lds_dwordx4 %0, %1 offset:1024 sc1\n\tglobal_load_lds_dwordx4 %0, %1 offset:2048 sc1"
                     : : "v"(lanex() * 16), "s"(kxp), "s"(kxw) : "memory");
    };
    u32 kfl, phi;
    int sd;
    {
        constexpr int GS = 3104;
        Sync y;
        if (w < 3) {
#pragma unroll
            for (int i = PXT; i < NSEG; ++i) gst16(p.ws + OFF_KM + (i * NH + h) * 3072 + w * 1024, lane * 16, u32x4{~0u, ~0u, ~0u, ~0u});
#pragma unroll
            for (int i = NSEG - 4; i < NSEG; ++i) gst16(p.ws + OFF_PX + i * 81920 + (w < 2 ? h * 2048 + w * 1024 : 65536 + h * 512), w < 2 ? lane * 16 : (lane * 16) & 511, u32x4{~0u, ~0u, ~0u, ~0u});
        }
        auto dvo = [&](int i) { const int ix = i * 64 + lane; return (u32)((ix / 24) * (HD * 2) + (ix % 24) * 16); };
        const u32 tra = lds + (2 * (g >> 1) + (qq >> 1)) * GS + (4 * (g & 1) + (qq & 1)) * 384 + 8 * pp;
        u32x8 pa = u32x8{dvo(0), dvo(1), dvo(2), tra + 96 * w + 32 * (w & 1), tra + 96 * w - 64 * (w & 1), tra + 32 * w, lfa, (u32)(lane & 31) * 8};
        asm volatile("s_nop 4\n\t" "s_add_u32 s42, s46, 0x0\n\t" "s_addc_u32 s43, s47, 0\n\t" "s_add_u32 m0, %18, 0x0\n\t" "s_nop 0\n\t" "global_load_lds_dwordx4 v224, s[42:43]\n\t" "s_add_u32 m0, %18, 0x400\n\t"
                     "s_nop 0\n\t" "global_load_lds_dwordx4 v225, s[42:43]\n\t" "s_add_u32 m0, %18, 0x800\n\t" "s_nop 0\n\t" "global_load_lds_dwordx4 v226, s[42:43]\n\t" "s_add_u32 s42, s46, 0x18000\n\t"
                     "s_addc_u32 s43, s47, 0\n\t" "s_add_u32 m0, %18, 0xc20\n\t" "s_nop 0\n\t" "global_load_lds_dwordx4 v224, s[42:43]\n\t" "s_add_u32 m0, %18, 0x1020\n\t" "s_nop 0\n\t"
                     "global_load_lds_dwordx4 v225, s[42:43]\n\t" "s_add_u32 m0, %18, 0x1420\n\t" "s_nop 0\n\t" "global_load_lds_dwordx4 v226, s[42:43]\n\t" "s_add_u32 s42, s46, 0xc0000\n\t" "s_addc_u32 s43, s47, 0\n\t"
                     "s_add_u32 m0, %18, 0x6100\n\t" "s_nop 0\n\t" "global_load_lds_dwordx4 v224, s[42:43]\n\t" "s_add_u32 m0, %18, 0x6500\n\t" "s_nop 0\n\t" "global_load_lds_dwordx4 v225, s[42:43]\n\t"
                     "s_add_u32 m0, %18, 0x6900\n\t" "s_nop 0\n\t" "global_load_lds_dwordx4 v226, s[42:43]\n\t" "s_add_u32 s42, s46, 0xd8000\n\t" "s_addc_u32 s43, s47, 0\n\t" "s_add_u32 m0, %18, 0x6d20\n\t" "s_nop 0\n\t"
                     "global_load_lds_dwordx4 v224, s[42:43]\n\t" "s_add_u32 m0, %18, 0x7120\n\t" "s_nop 0\n\t" "global_load_lds_dwordx4 v225, s[42:43]\n\t" "s_add_u32 m0, %18, 0x7520\n\t" "s_nop 0\n\t"
                     "global_load_lds_dwordx4 v226, s[42:43]\n\t" "s_add_u32 s42, s46, 0x180000\n\t" "s_addc_u32 s43, s47, 0\n\t" "s_add_u32 m0, %18, 0xc200\n\t" "s_nop 0\n\t" "global_load_lds_dwordx4 v224, s[42:43]\n\t"
                     "s_add_u32 m0, %18, 0xc600\n\t" "s_nop 0\n\t" "global_load_lds_dwordx4 v225, s[42:43]\n\t" "s_add_u32 m0, %18, 0xca00\n\t" "s_nop 0\n\t" "global_load_lds_dwordx4 v226, s[42:43]\n\t"
                     "s_add_u32 s42, s46, 0x198000\n\t" "s_addc_u32 s43, s47, 0\n\t" "s_add_u32 m0, %18, 0xce20\n\t" "s_nop 0\n\t" "global_load_lds_dwordx4 v224, s[42:43]\n\t" "s_add_u32 m0, %18, 0xd220\n\t" "s_nop 0\n\t"
                     "global_load_lds_dwordx4 v225, s[42:43]\n\t" "s_add_u32 m0, %18, 0xd620\n\t" "s_nop 0\n\t" "global_load_lds_dwordx4 v226, s[42:43]\n\t" "s_add_u32 s42, s46, 0x240000\n\t" "s_addc_u32 s43, s47, 0\n\t"
                     "s_add_u32 m0, %18, 0x12300\n\t" "s_nop 0\n\t" "global_load_lds_dwordx4 v224, s[42:43]\n\t" "s_add_u32 m0, %18, 0x12700\n\t" "s_nop 0\n\t" "global_load_lds_dwordx4 v225, s[42:43]\n\t"
                     "s_add_u32 m0, %18, 0x12b00\n\t" "s_nop 0\n\t" "global_load_lds_dwordx4 v226, s[42:43]\n\t" "s_add_u32 s42, s46, 0x258000\n\t" "s_addc_u32 s43, s47, 0\n\t" "s_add_u32 m0, %18, 0x12f20\n\t"
                     "s_nop 0\n\t" "global_load_lds_dwordx4 v224, s[42:43]\n\t" "s_add_u32 m0, %18, 0x13320\n\t" "s_nop 0\n\t" "global_load_lds_dwordx4 v225, s[42:43]\n\t" "s_add_u32 m0, %18, 0x13720\n\t" "s_nop 0\n\t"
                     "global_load_lds_dwordx4 v226, s[42:43]\n\t" "s_waitcnt vmcnt(12) lgkmcnt(0)\n\t" "s_barrier\n\t" "s_add_u32 s42, s46, 0x300000\n\t" "s_addc_u32 s43, s47, 0\n\t" "s_add_u32 m0, %18, 0x18400\n\t"
                     "s_nop 0\n\t" "global_load_lds_dwordx4 v224, s[42:43]\n\t" "s_add_u32 m0, %18, 0x18800\n\t" "s_nop 0\n\t" "global_load_lds_dwordx4 v225, s[42:43]\n\t" "s_add_u32 m0, %18, 0x18c00\n\t" "s_nop 0\n\t"
                     "global_load_lds_dwordx4 v226, s[42:43]\n\t" "s_add_u32 s42, s46, 0x318000\n\t" "s_addc_u32 s43, s47, 0\n\t" "s_add_u32 m0, %18, 0x19020\n\t" "s_nop 0\n\t" "global_load_lds_dwordx4 v224, s[42:43]\n\t"
                     "s_add_u32 m0, %18, 0x19420\n\t" "s_nop 0\n\t" "global_load_lds_dwordx4 v225, s[42:43]\n\t" "s_add_u32 m0, %18, 0x19820\n\t" "s_nop 0\n\t" "global_load_lds_dwordx4 v226, s[42:43]\n\t"
                     "s_add_u32 s42, s46, 0x3c0000\n\t" "s_addc_u32 s43, s47, 0\n\t" "s_add_u32 m0, %18, 0x1e500\n\t" "s_nop 0\n\t" "global_load_lds_dwordx4 v224, s[42:43]\n\t" "s_add_u32 m0, %18, 0x1e900\n\t"
                     "s_nop 0\n\t" "global_load_lds_dwordx4 v225, s[42:43]\n\t" "s_add_u32 m0, %18, 0x1ed00\n\t" "s_nop 0\n\t" "global_load_lds_dwordx4 v226, s[42:43]\n\t" "s_add_u32 s42, s46, 0x3d8000\n\t"
                     "s_addc_u32 s43, s47, 0\n\t" "s_add_u32 m0, %18, 0x1f120\n\t" "s_nop 0\n\t" "global_load_lds_dwordx4 v224, s[42:43]\n\t" "s_add_u32 m0, %18, 0x1f520\n\t" "s_nop 0\n\t"
                     "global_load_lds_dwordx4 v225, s[42:43]\n\t" "s_add_u32 m0, %18, 0x1f920\n\t" "s_nop 0\n\t" "global_load_lds_dwordx4 v226, s[42:43]\n\t" "v_add_u32 v232, 0x0, v229\n\t"
                     "ds_read_b64_tr_b16 v[8:9], v232\n\t" "ds_read_b64_tr_b16 v[10:11], v232 offset:768\n\t" "ds_read_b64_tr_b16 v[12:13], v232 offset:12416\n\t" "ds_read_b64_tr_b16 v[14:15], v232 offset:13184\n\t"
                     "ds_read_b64_tr_b16 a[96:97], v232 offset:128\n\t" "ds_read_b64_tr_b16 a[98:99], v232 offset:896\n\t" "ds_read_b64_tr_b16 a[100:101], v232 offset:12544\n\t"
                     "ds_read_b64_tr_b16 a[102:103], v232 offset:13312\n\t" "ds_read_b64_tr_b16 a[176:177], v232 offset:256\n\t" "ds_read_b64_tr_b16 a[178:179], v232 offset:1024\n\t"
                     "ds_read_b64_tr_b16 a[180:181], v232 offset:12672\n\t" "ds_read_b64_tr_b16 a[182:183], v232 offset:13440\n\t" "ds_read_b64_tr_b16 v[24:25], v232 offset:24832\n\t"
                     "ds_read_b64_tr_b16 v[26:27], v232 offset:25600\n\t" "ds_read_b64_tr_b16 v[28:29], v232 offset:37248\n\t" "ds_read_b64_tr_b16 v[30:31], v232 offset:38016\n\t"
                     "ds_read_b64_tr_b16 a[104:105], v232 offset:24960\n\t" "ds_read_b64_tr_b16 a[106:107], v232 offset:25728\n\t" "ds_read_b64_tr_b16 a[108:109], v232 offset:37376\n\t"
                     "ds_read_b64_tr_b16 a[110:111], v232 offset:38144\n\t" "ds_read_b64_tr_b16 a[184:185], v232 offset:25088\n\t" "ds_read_b64_tr_b16 a[186:187], v232 offset:25856\n\t"
                     "ds_read_b64_tr_b16 a[188:189], v232 offset:37504\n\t" "ds_read_b64_tr_b16 a[190:191], v232 offset:38272\n\t" "s_waitcnt vmcnt(12) lgkmcnt(0)\n\t" "s_barrier\n\t" "s_add_u32 s42, s46, 0x480000\n\t"
                     "s_addc_u32 s43, s47, 0\n\t" "s_add_u32 m0, %18, 0x0\n\t" "s_nop 0\n\t" "global_load_lds_dwordx4 v224, s[42:43]\n\t" "s_add_u32 m0, %18, 0x400\n\t" "s_nop 0\n\t"
                     "global_load_lds_dwordx4 v225, s[42:43]\n\t" "s_add_u32 m0, %18, 0x800\n\t" "s_nop 0\n\t" "global_load_lds_dwordx4 v226, s[42:43]\n\t" "s_add_u32 s42, s46, 0x498000\n\t" "s_addc_u32 s43, s47, 0\n\t"
                     "s_add_u32 m0, %18, 0xc20\n\t" "s_nop 0\n\t" "global_load_lds_dwordx4 v224, s[42:43]\n\t" "s_add_u32 m0, %18, 0x1020\n\t" "s_nop 0\n\t" "global_load_lds_dwordx4 v225, s[42:43]\n\t"
                     "s_add_u32 m0, %18, 0x1420\n\t" "s_nop 0\n\t" "global_load_lds_dwordx4 v226, s[42:43]\n\t" "s_add_u32 s42, s46, 0x540000\n\t" "s_addc_u32 s43, s47, 0\n\t" "s_add_u32 m0, %18, 0x6100\n\t" "s_nop 0\n\t"
                     "global_load_lds_dwordx4 v224, s[42:43]\n\t" "s_add_u32 m0, %18, 0x6500\n\t" "s_nop 0\n\t" "global_load_lds_dwordx4 v225, s[42:43]\n\t" "s_add_u32 m0, %18, 0x6900\n\t" "s_nop 0\n\t"
                     "global_load_lds_dwordx4 v226, s[42:43]\n\t" "s_add_u32 s42, s46, 0x558000\n\t" "s_addc_u32 s43, s47, 0\n\t" "s_add_u32 m0, %18, 0x6d20\n\t" "s_nop 0\n\t" "global_load_lds_dwordx4 v224, s[42:43]\n\t"
                     "s_add_u32 m0, %18, 0x7120\n\t" "s_nop 0\n\t" "global_load_lds_dwordx4 v225, s[42:43]\n\t" "s_add_u32 m0, %18, 0x7520\n\t" "s_nop 0\n\t" "global_load_lds_dwordx4 v226, s[42:43]\n\t"
                     "v_add_u32 v232, 0xc200, v229\n\t" "ds_read_b64_tr_b16 v[40:41], v232\n\t" "ds_read_b64_tr_b16 v[42:43], v232 offset:768\n\t" "ds_read_b64_tr_b16 v[44:45], v232 offset:12416\n\t"
                     "ds_read_b64_tr_b16 v[46:47], v232 offset:13184\n\t" "ds_read_b64_tr_b16 a[112:113], v232 offset:128\n\t" "ds_read_b64_tr_b16 a[114:115], v232 offset:896\n\t"
                     "ds_read_b64_tr_b16 a[116:117], v232 offset:12544\n\t" "ds_read_b64_tr_b16 a[118:119], v232 offset:13312\n\t" "ds_read_b64_tr_b16 a[192:193], v232 offset:256\n\t"
                     "ds_read_b64_tr_b16 a[194:195], v232 offset:1024\n\t" "ds_read_b64_tr_b16 a[196:197], v232 offset:12672\n\t" "ds_read_b64_tr_b16 a[198:199], v232 offset:13440\n\t"
                     "ds_read_b64_tr_b16 v[56:57], v232 offset:24832\n\t" "ds_read_b64_tr_b16 v[58:59], v232 offset:25600\n\t" "ds_read_b64_tr_b16 v[60:61], v232 offset:37248\n\t"
                     "ds_read_b64_tr_b16 v[62:63], v232 offset:38016\n\t" "ds_read_b64_tr_b16 a[120:121], v232 offset:24960\n\t" "ds_read_b64_tr_b16 a[122:123], v232 offset:25728\n\t"
                     "ds_read_b64_tr_b16 a[124:125], v232 offset:37376\n\t" "ds_read_b64_tr_b16 a[126:127], v232 offset:38144\n\t" "ds_read_b64_tr_b16 a[200:201], v232 offset:25088\n\t"
                     "ds_read_b64_tr_b16 a[202:203], v232 offset:25856\n\t" "ds_read_b64_tr_b16 a[204:205], v232 offset:37504\n\t" "ds_read_b64_tr_b16 a[206:207], v232 offset:38272\n\t" "s_waitcnt vmcnt(12) lgkmcnt(0)\n\t"
                     "s_barrier\n\t" "s_add_u32 s42, s46, 0x600000\n\t" "s_addc_u32 s43, s47, 0\n\t" "s_add_u32 m0, %18, 0xc200\n\t" "s_nop 0\n\t" "global_load_lds_dwordx4 v224, s[42:43]\n\t"
                     "s_add_u32 m0, %18, 0xc600\n\t" "s_nop 0\n\t" "global_load_lds_dwordx4 v225, s[42:43]\n\t" "s_add_u32 m0, %18, 0xca00\n\t" "s_nop 0\n\t" "global_load_lds_dwordx4 v226, s[42:43]\n\t"
                     "s_add_u32 s42, s46, 0x618000\n\t" "s_addc_u32 s43, s47, 0\n\t" "s_add_u32 m0, %18, 0xce20\n\t" "s_nop 0\n\t" "global_load_lds_dwordx4 v224, s[42:43]\n\t" "s_add_u32 m0, %18, 0xd220\n\t" "s_nop 0\n\t"
                     "global_load_lds_dwordx4 v225, s[42:43]\n\t" "s_add_u32 m0, %18, 0xd620\n\t" "s_nop 0\n\t" "global_load_lds_dwordx4 v226, s[42:43]\n\t" "s_add_u32 s42, s46, 0x6c0000\n\t" "s_addc_u32 s43, s47, 0\n\t"
                     "s_add_u32 m0, %18, 0x12300\n\t" "s_nop 0\n\t" "global_load_lds_dwordx4 v224, s[42:43]\n\t" "s_add_u32 m0, %18, 0x12700\n\t" "s_nop 0\n\t" "global_load_lds_dwordx4 v225, s[42:43]\n\t"
                     "s_add_u32 m0, %18, 0x12b00\n\t" "s_nop 0\n\t" "global_load_lds_dwordx4 v226, s[42:43]\n\t" "s_add_u32 s42, s46, 0x6d8000\n\t" "s_addc_u32 s43, s47, 0\n\t" "s_add_u32 m0, %18, 0x12f20\n\t"
                     "s_nop 0\n\t" "global_load_lds_dwordx4 v224, s[42:43]\n\t" "s_add_u32 m0, %18, 0x13320\n\t" "s_nop 0\n\t" "global_load_lds_dwordx4 v225, s[42:43]\n\t" "s_add_u32 m0, %18, 0x13720\n\t" "s_nop 0\n\t"
                     "global_load_lds_dwordx4 v226, s[42:43]\n\t" "v_add_u32 v232, 0x18400, v229\n\t" "ds_read_b64_tr_b16 v[72:73], v232\n\t" "ds_read_b64_tr_b16 v[74:75], v232 offset:768\n\t"
                     "ds_read_b64_tr_b16 v[76:77], v232 offset:12416\n\t" "ds_read_b64_tr_b16 v[78:79], v232 offset:13184\n\t" "ds_read_b64_tr_b16 a[128:129], v232 offset:128\n\t"
                     "ds_read_b64_tr_b16 a[130:131], v232 offset:896\n\t" "ds_read_b64_tr_b16 a[132:133], v232 offset:12544\n\t" "ds_read_b64_tr_b16 a[134:135], v232 offset:13312\n\t"
                     "ds_read_b64_tr_b16 a[208:209], v232 offset:256\n\t" "ds_read_b64_tr_b16 a[210:211], v232 offset:1024\n\t" "ds_read_b64_tr_b16 a[212:213], v232 offset:12672\n\t"
                     "ds_read_b64_tr_b16 a[214:215], v232 offset:13440\n\t" "ds_read_b64_tr_b16 v[88:89], v232 offset:24832\n\t" "ds_read_b64_tr_b16 v[90:91], v232 offset:25600\n\t"
                     "ds_read_b64_tr_b16 v[92:93], v232 offset:37248\n\t" "ds_read_b64_tr_b16 v[94:95], v232 offset:38016\n\t" "ds_read_b64_tr_b16 a[136:137], v232 offset:24960\n\t"
                     "ds_read_b64_tr_b16 a[138:139], v232 offset:25728\n\t" "ds_read_b64_tr_b16 a[140:141], v232 offset:37376\n\t" "ds_read_b64_tr_b16 a[142:143], v232 offset:38144\n\t"
                     "ds_read_b64_tr_b16 a[216:217], v232 offset:25088\n\t" "ds_read_b64_tr_b16 a[218:219], v232 offset:25856\n\t" "ds_read_b64_tr_b16 a[220:221], v232 offset:37504\n\t"
                     "ds_read_b64_tr_b16 a[222:223], v232 offset:38272\n\t" "s_waitcnt vmcnt(12) lgkmcnt(0)\n\t" "s_barrier\n\t" "s_add_u32 s42, s44, 0x0\n\t" "s_addc_u32 s43, s45, 0\n\t" "s_add_u32 m0, %18, 0x18400\n\t"
                     "s_nop 0\n\t" "global_load_lds_dwordx4 v224, s[42:43]\n\t" "s_add_u32 m0, %18, 0x18800\n\t" "s_nop 0\n\t" "global_load_lds_dwordx4 v225, s[42:43]\n\t" "s_add_u32 m0, %18, 0x18c00\n\t" "s_nop 0\n\t"
                     "global_load_lds_dwordx4 v226, s[42:43]\n\t" "s_add_u32 s42, s44, 0x18000\n\t" "s_addc_u32 s43, s45, 0\n\t" "s_add_u32 m0, %18, 0x19020\n\t" "s_nop 0\n\t" "global_load_lds_dwordx4 v224, s[42:43]\n\t"
                     "s_add_u32 m0, %18, 0x19420\n\t" "s_nop 0\n\t" "global_load_lds_dwordx4 v225, s[42:43]\n\t" "s_add_u32 m0, %18, 0x19820\n\t" "s_nop 0\n\t" "global_load_lds_dwordx4 v226, s[42:43]\n\t"
                     "s_add_u32 s42, s44, 0xc0000\n\t" "s_addc_u32 s43, s45, 0\n\t" "s_add_u32 m0, %18, 0x1e500\n\t" "s_nop 0\n\t" "global_load_lds_dwordx4 v224, s[42:43]\n\t" "s_add_u32 m0, %18, 0x1e900\n\t" "s_nop 0\n\t"
                     "global_load_lds_dwordx4 v225, s[42:43]\n\t" "s_add_u32 m0, %18, 0x1ed00\n\t" "s_nop 0\n\t" "global_load_lds_dwordx4 v226, s[42:43]\n\t" "s_add_u32 s42, s44, 0xd8000\n\t" "s_addc_u32 s43, s45, 0\n\t"
                     "s_add_u32 m0, %18, 0x1f120\n\t" "s_nop 0\n\t" "global_load_lds_dwordx4 v224, s[42:43]\n\t" "s_add_u32 m0, %18, 0x1f520\n\t" "s_nop 0\n\t" "global_load_lds_dwordx4 v225, s[42:43]\n\t"
                     "s_add_u32 m0, %18, 0x1f920\n\t" "s_nop 0\n\t" "global_load_lds_dwordx4 v226, s[42:43]\n\t" "v_add_u32 v232, 0x0, v229\n\t" "ds_read_b64_tr_b16 v[104:105], v232\n\t"
                     "ds_read_b64_tr_b16 v[106:107], v232 offset:768\n\t" "ds_read_b64_tr_b16 v[108:109], v232 offset:12416\n\t" "ds_read_b64_tr_b16 v[110:111], v232 offset:13184\n\t"
                     "ds_read_b64_tr_b16 a[144:145], v232 offset:128\n\t" "ds_read_b64_tr_b16 a[146:147], v232 offset:896\n\t" "ds_read_b64_tr_b16 a[148:149], v232 offset:12544\n\t"
                     "ds_read_b64_tr_b16 a[150:151], v232 offset:13312\n\t" "ds_read_b64_tr_b16 a[224:225], v232 offset:256\n\t" "ds_read_b64_tr_b16 a[226:227], v232 offset:1024\n\t"
                     "ds_read_b64_tr_b16 a[228:229], v232 offset:12672\n\t" "ds_read_b64_tr_b16 a[230:231], v232 offset:13440\n\t" "ds_read_b64_tr_b16 v[120:121], v232 offset:24832\n\t"
                     "ds_read_b64_tr_b16 v[122:123], v232 offset:25600\n\t" "ds_read_b64_tr_b16 v[124:125], v232 offset:37248\n\t" "ds_read_b64_tr_b16 v[126:127], v232 offset:38016\n\t"
                     "ds_read_b64_tr_b16 a[152:153], v232 offset:24960\n\t" "ds_read_b64_tr_b16 a[154:155], v232 offset:25728\n\t" "ds_read_b64_tr_b16 a[156:157], v232 offset:37376\n\t"
                     "ds_read_b64_tr_b16 a[158:159], v232 offset:38144\n\t" "ds_read_b64_tr_b16 a[232:233], v232 offset:25088\n\t" "ds_read_b64_tr_b16 a[234:235], v232 offset:25856\n\t"
                     "ds_read_b64_tr_b16 a[236:237], v232 offset:37504\n\t" "ds_read_b64_tr_b16 a[238:239], v232 offset:38272\n\t" "s_waitcnt vmcnt(12) lgkmcnt(0)\n\t" "s_barrier\n\t" "s_add_u32 s42, s44, 0x180000\n\t"
                     "s_addc_u32 s43, s45, 0\n\t" "s_add_u32 m0, %18, 0x0\n\t" "s_nop 0\n\t" "global_load_lds_dwordx4 v224, s[42:43]\n\t" "s_add_u32 m0, %18, 0x400\n\t" "s_nop 0\n\t"
                     "global_load_lds_dwordx4 v225, s[42:43]\n\t" "s_add_u32 m0, %18, 0x800\n\t" "s_nop 0\n\t" "global_load_lds_dwordx4 v226, s[42:43]\n\t" "s_add_u32 s42, s44, 0x198000\n\t" "s_addc_u32 s43, s45, 0\n\t"
                     "s_add_u32 m0, %18, 0xc20\n\t" "s_nop 0\n\t" "global_load_lds_dwordx4 v224, s[42:43]\n\t" "s_add_u32 m0, %18, 0x1020\n\t" "s_nop 0\n\t" "global_load_lds_dwordx4 v225, s[42:43]\n\t"
                     "s_add_u32 m0, %18, 0x1420\n\t" "s_nop 0\n\t" "global_load_lds_dwordx4 v226, s[42:43]\n\t" "s_add_u32 s42, s44, 0x240000\n\t" "s_addc_u32 s43, s45, 0\n\t" "s_add_u32 m0, %18, 0x6100\n\t" "s_nop 0\n\t"
                     "global_load_lds_dwordx4 v224, s[42:43]\n\t" "s_add_u32 m0, %18, 0x6500\n\t" "s_nop 0\n\t" "global_load_lds_dwordx4 v225, s[42:43]\n\t" "s_add_u32 m0, %18, 0x6900\n\t" "s_nop 0\n\t"
                     "global_load_lds_dwordx4 v226, s[42:43]\n\t" "s_add_u32 s42, s44, 0x258000\n\t" "s_addc_u32 s43, s45, 0\n\t" "s_add_u32 m0, %18, 0x6d20\n\t" "s_nop 0\n\t" "global_load_lds_dwordx4 v224, s[42:43]\n\t"
                     "s_add_u32 m0, %18, 0x7120\n\t" "s_nop 0\n\t" "global_load_lds_dwordx4 v225, s[42:43]\n\t" "s_add_u32 m0, %18, 0x7520\n\t" "s_nop 0\n\t" "global_load_lds_dwordx4 v226, s[42:43]\n\t"
                     "v_add_u32 v232, 0xc200, v229\n\t" "ds_read_b64_tr_b16 a[56:57], v232\n\t" "ds_read_b64_tr_b16 a[58:59], v232 offset:768\n\t" "ds_read_b64_tr_b16 a[60:61], v232 offset:12416\n\t"
                     "ds_read_b64_tr_b16 a[62:63], v232 offset:13184\n\t" "ds_read_b64_tr_b16 a[160:161], v232 offset:128\n\t" "ds_read_b64_tr_b16 a[162:163], v232 offset:896\n\t"
                     "ds_read_b64_tr_b16 a[164:165], v232 offset:12544\n\t" "ds_read_b64_tr_b16 a[166:167], v232 offset:13312\n\t" "ds_read_b64_tr_b16 a[240:241], v232 offset:256\n\t"
                     "ds_read_b64_tr_b16 a[242:243], v232 offset:1024\n\t" "ds_read_b64_tr_b16 a[244:245], v232 offset:12672\n\t" "ds_read_b64_tr_b16 a[246:247], v232 offset:13440\n\t"
                     "ds_read_b64_tr_b16 a[88:89], v232 offset:24832\n\t" "ds_read_b64_tr_b16 a[90:91], v232 offset:25600\n\t" "ds_read_b64_tr_b16 a[92:93], v232 offset:37248\n\t"
                     "ds_read_b64_tr_b16 a[94:95], v232 offset:38016\n\t" "ds_read_b64_tr_b16 a[168:169], v232 offset:24960\n\t" "ds_read_b64_tr_b16 a[170:171], v232 offset:25728\n\t"
                     "ds_read_b64_tr_b16 a[172:173], v232 offset:37376\n\t" "ds_read_b64_tr_b16 a[174:175], v232 offset:38144\n\t" "ds_read_b64_tr_b16 a[248:249], v232 offset:25088\n\t"
                     "ds_read_b64_tr_b16 a[250:251], v232 offset:25856\n\t" "ds_read_b64_tr_b16 a[252:253], v232 offset:37504\n\t" "ds_read_b64_tr_b16 a[254:255], v232 offset:38272\n\t" "s_waitcnt vmcnt(12) lgkmcnt(0)\n\t"
                     "s_barrier\n\t" "s_add_u32 s42, s44, 0x300000\n\t" "s_addc_u32 s43, s45, 0\n\t" "s_add_u32 m0, %18, 0xc200\n\t" "s_nop 0\n\t" "global_load_lds_dwordx4 v224, s[42:43]\n\t"
                     "s_add_u32 m0, %18, 0xc600\n\t" "s_nop 0\n\t" "global_load_lds_dwordx4 v225, s[42:43]\n\t" "s_add_u32 m0, %18, 0xca00\n\t" "s_nop 0\n\t" "global_load_lds_dwordx4 v226, s[42:43]\n\t"
                     "s_add_u32 s42, s44, 0x318000\n\t" "s_addc_u32 s43, s45, 0\n\t" "s_add_u32 m0, %18, 0xce20\n\t" "s_nop 0\n\t" "global_load_lds_dwordx4 v224, s[42:43]\n\t" "s_add_u32 m0, %18, 0xd220\n\t" "s_nop 0\n\t"
                     "global_load_lds_dwordx4 v225, s[42:43]\n\t" "s_add_u32 m0, %18, 0xd620\n\t" "s_nop 0\n\t" "global_load_lds_dwordx4 v226, s[42:43]\n\t" "s_add_u32 s42, s44, 0x3c0000\n\t" "s_addc_u32 s43, s45, 0\n\t"
                     "s_add_u32 m0, %18, 0x12300\n\t" "s_nop 0\n\t" "global_load_lds_dwordx4 v224, s[42:43]\n\t" "s_add_u32 m0, %18, 0x12700\n\t" "s_nop 0\n\t" "global_load_lds_dwordx4 v225, s[42:43]\n\t"
                     "s_add_u32 m0, %18, 0x12b00\n\t" "s_nop 0\n\t" "global_load_lds_dwordx4 v226, s[42:43]\n\t" "s_add_u32 s42, s44, 0x3d8000\n\t" "s_addc_u32 s43, s45, 0\n\t" "s_add_u32 m0, %18, 0x12f20\n\t"
                     "s_nop 0\n\t" "global_load_lds_dwordx4 v224, s[42:43]\n\t" "s_add_u32 m0, %18, 0x13320\n\t" "s_nop 0\n\t" "global_load_lds_dwordx4 v225, s[42:43]\n\t" "s_add_u32 m0, %18, 0x13720\n\t" "s_nop 0\n\t"
                     "global_load_lds_dwordx4 v226, s[42:43]\n\t" "v_add_u32 v232, 0x18400, v227\n\t" "v_add_u32 v233, 0x18400, v228\n\t" "ds_read_b64_tr_b16 v[128:129], v232\n\t"
                     "ds_read_b64_tr_b16 v[130:131], v232 offset:768\n\t" "ds_read_b64_tr_b16 v[132:133], v232 offset:12416\n\t" "ds_read_b64_tr_b16 v[134:135], v232 offset:13184\n\t"
                     "ds_read_b64_tr_b16 v[0:1], v232 offset:32\n\t" "ds_read_b64_tr_b16 v[2:3], v232 offset:800\n\t" "ds_read_b64_tr_b16 v[4:5], v232 offset:12448\n\t" "ds_read_b64_tr_b16 v[6:7], v232 offset:13216\n\t"
                     "ds_read_b64_tr_b16 v[136:137], v233 offset:64\n\t" "ds_read_b64_tr_b16 v[138:139], v233 offset:832\n\t" "ds_read_b64_tr_b16 v[140:141], v233 offset:12480\n\t"
                     "ds_read_b64_tr_b16 v[142:143], v233 offset:13248\n\t" "ds_read_b64_tr_b16 v[144:145], v232 offset:24832\n\t" "ds_read_b64_tr_b16 v[146:147], v232 offset:25600\n\t"
                     "ds_read_b64_tr_b16 v[148:149], v232 offset:37248\n\t" "ds_read_b64_tr_b16 v[150:151], v232 offset:38016\n\t" "ds_read_b64_tr_b16 v[16:17], v232 offset:24864\n\t"
                     "ds_read_b64_tr_b16 v[18:19], v232 offset:25632\n\t" "ds_read_b64_tr_b16 v[20:21], v232 offset:37280\n\t" "ds_read_b64_tr_b16 v[22:23], v232 offset:38048\n\t"
                     "ds_read_b64_tr_b16 v[152:153], v233 offset:24896\n\t" "ds_read_b64_tr_b16 v[154:155], v233 offset:25664\n\t" "ds_read_b64_tr_b16 v[156:157], v233 offset:37312\n\t"
                     "ds_read_b64_tr_b16 v[158:159], v233 offset:38080\n\t" "s_waitcnt vmcnt(12) lgkmcnt(0)\n\t" "s_barrier\n\t" "v_mov_b32 %14, 0\n\t" "global_load_dwordx2 %11, v231, %19 sc1\n\t"
                     "global_load_dwordx2 %12, %14, %19 offset:512 sc1\n\t" "global_load_dwordx2 %13, %14, %19 offset:1024 sc1\n\t" "s_add_u32 s42, s44, 0x480000\n\t" "s_addc_u32 s43, s45, 0\n\t"
                     "s_add_u32 m0, %18, 0x18400\n\t" "s_nop 0\n\t" "global_load_lds_dwordx4 v224, s[42:43]\n\t" "s_add_u32 m0, %18, 0x18800\n\t" "s_nop 0\n\t" "global_load_lds_dwordx4 v225, s[42:43]\n\t"
                     "s_add_u32 m0, %18, 0x18c00\n\t" "s_nop 0\n\t" "global_load_lds_dwordx4 v226, s[42:43]\n\t" "s_add_u32 s42, s44, 0x498000\n\t" "s_addc_u32 s43, s45, 0\n\t" "s_add_u32 m0, %18, 0x19020\n\t"
                     "s_nop 0\n\t" "global_load_lds_dwordx4 v224, s[42:43]\n\t" "s_add_u32 m0, %18, 0x19420\n\t" "s_nop 0\n\t" "global_load_lds_dwordx4 v225, s[42:43]\n\t" "s_add_u32 m0, %18, 0x19820\n\t" "s_nop 0\n\t"
                     "global_load_lds_dwordx4 v226, s[42:43]\n\t" "s_add_u32 s42, s44, 0x540000\n\t" "s_addc_u32 s43, s45, 0\n\t" "s_add_u32 m0, %18, 0x1e500\n\t" "s_nop 0\n\t" "global_load_lds_dwordx4 v224, s[42:43]\n\t"
                     "s_add_u32 m0, %18, 0x1e900\n\t" "s_nop 0\n\t" "global_load_lds_dwordx4 v225, s[42:43]\n\t" "s_add_u32 m0, %18, 0x1ed00\n\t" "s_nop 0\n\t" "global_load_lds_dwordx4 v226, s[42:43]\n\t"
                     "s_add_u32 s42, s44, 0x558000\n\t" "s_addc_u32 s43, s45, 0\n\t" "s_add_u32 m0, %18, 0x1f120\n\t" "s_nop 0\n\t" "global_load_lds_dwordx4 v224, s[42:43]\n\t" "s_add_u32 m0, %18, 0x1f520\n\t"
                     "s_nop 0\n\t" "global_load_lds_dwordx4 v225, s[42:43]\n\t" "s_add_u32 m0, %18, 0x1f920\n\t" "s_nop 0\n\t" "global_load_lds_dwordx4 v226, s[42:43]\n\t" "v_add_u32 v232, 0x0, v227\n\t"
                     "v_add_u32 v233, 0x0, v228\n\t" "ds_read_b64_tr_b16 v[160:161], v232\n\t" "ds_read_b64_tr_b16 v[162:163], v232 offset:768\n\t" "ds_read_b64_tr_b16 v[164:165], v232 offset:12416\n\t"
                     "ds_read_b64_tr_b16 v[166:167], v232 offset:13184\n\t" "ds_read_b64_tr_b16 v[32:33], v232 offset:32\n\t" "ds_read_b64_tr_b16 v[34:35], v232 offset:800\n\t"
                     "ds_read_b64_tr_b16 v[36:37], v232 offset:12448\n\t" "ds_read_b64_tr_b16 v[38:39], v232 offset:13216\n\t" "ds_read_b64_tr_b16 v[168:169], v233 offset:64\n\t"
                     "ds_read_b64_tr_b16 v[170:171], v233 offset:832\n\t" "ds_read_b64_tr_b16 v[172:173], v233 offset:12480\n\t" "ds_read_b64_tr_b16 v[174:175], v233 offset:13248\n\t"
                     "ds_read_b64_tr_b16 v[176:177], v232 offset:24832\n\t" "ds_read_b64_tr_b16 v[178:179], v232 offset:25600\n\t" "ds_read_b64_tr_b16 v[180:181], v232 offset:37248\n\t"
                     "ds_read_b64_tr_b16 v[182:183], v232 offset:38016\n\t" "ds_read_b64_tr_b16 v[48:49], v232 offset:24864\n\t" "ds_read_b64_tr_b16 v[50:51], v232 offset:25632\n\t"
                     "ds_read_b64_tr_b16 v[52:53], v232 offset:37280\n\t" "ds_read_b64_tr_b16 v[54:55], v232 offset:38048\n\t" "ds_read_b64_tr_b16 v[184:185], v233 offset:24896\n\t"
                     "ds_read_b64_tr_b16 v[186:187], v233 offset:25664\n\t" "ds_read_b64_tr_b16 v[188:189], v233 offset:37312\n\t" "ds_read_b64_tr_b16 v[190:191], v233 offset:38080\n\t" "s_waitcnt vmcnt(12) lgkmcnt(0)\n\t"
                     "s_barrier\n\t" "s_add_u32 s42, s44, 0x600000\n\t" "s_addc_u32 s43, s45, 0\n\t" "s_add_u32 m0, %18, 0x0\n\t" "s_nop 0\n\t" "global_load_lds_dwordx4 v224, s[42:43]\n\t" "s_add_u32 m0, %18, 0x400\n\t"
                     "s_nop 0\n\t" "global_load_lds_dwordx4 v225, s[42:43]\n\t" "s_add_u32 m0, %18, 0x800\n\t" "s_nop 0\n\t" "global_load_lds_dwordx4 v226, s[42:43]\n\t" "s_add_u32 s42, s44, 0x618000\n\t"
                     "s_addc_u32 s43, s45, 0\n\t" "s_add_u32 m0, %18, 0xc20\n\t" "s_nop 0\n\t" "global_load_lds_dwordx4 v224, s[42:43]\n\t" "s_add_u32 m0, %18, 0x1020\n\t" "s_nop 0\n\t"
                     "global_load_lds_dwordx4 v225, s[42:43]\n\t" "s_add_u32 m0, %18, 0x1420\n\t" "s_nop 0\n\t" "global_load_lds_dwordx4 v226, s[42:43]\n\t" "s_add_u32 s42, s44, 0x6c0000\n\t" "s_addc_u32 s43, s45, 0\n\t"
                     "s_add_u32 m0, %18, 0x6100\n\t" "s_nop 0\n\t" "global_load_lds_dwordx4 v224, s[42:43]\n\t" "s_add_u32 m0, %18, 0x6500\n\t" "s_nop 0\n\t" "global_load_lds_dwordx4 v225, s[42:43]\n\t"
                     "s_add_u32 m0, %18, 0x6900\n\t" "s_nop 0\n\t" "global_load_lds_dwordx4 v226, s[42:43]\n\t" "s_add_u32 s42, s44, 0x6d8000\n\t" "s_addc_u32 s43, s45, 0\n\t" "s_add_u32 m0, %18, 0x6d20\n\t" "s_nop 0\n\t"
                     "global_load_lds_dwordx4 v224, s[42:43]\n\t" "s_add_u32 m0, %18, 0x7120\n\t" "s_nop 0\n\t" "global_load_lds_dwordx4 v225, s[42:43]\n\t" "s_add_u32 m0, %18, 0x7520\n\t" "s_nop 0\n\t"
                     "global_load_lds_dwordx4 v226, s[42:43]\n\t" "global_load_dword %14, %14, %20 sc1\n\t" "v_add_u32 v232, 0xc200, v227\n\t" "v_add_u32 v233, 0xc200, v228\n\t" "ds_read_b64_tr_b16 v[192:193], v232\n\t"
                     "ds_read_b64_tr_b16 v[194:195], v232 offset:768\n\t" "ds_read_b64_tr_b16 v[196:197], v232 offset:12416\n\t" "ds_read_b64_tr_b16 v[198:199], v232 offset:13184\n\t"
                     "ds_read_b64_tr_b16 v[64:65], v232 offset:32\n\t" "ds_read_b64_tr_b16 v[66:67], v232 offset:800\n\t" "ds_read_b64_tr_b16 v[68:69], v232 offset:12448\n\t"
                     "ds_read_b64_tr_b16 v[70:71], v232 offset:13216\n\t" "ds_read_b64_tr_b16 v[200:201], v233 offset:64\n\t" "ds_read_b64_tr_b16 v[202:203], v233 offset:832\n\t"
                     "ds_read_b64_tr_b16 v[204:205], v233 offset:12480\n\t" "ds_read_b64_tr_b16 v[206:207], v233 offset:13248\n\t" "ds_read_b64_tr_b16 v[208:209], v232 offset:24832\n\t"
                     "ds_read_b64_tr_b16 v[210:211], v232 offset:25600\n\t" "ds_read_b64_tr_b16 v[212:213], v232 offset:37248\n\t" "ds_read_b64_tr_b16 v[214:215], v232 offset:38016\n\t"
                     "ds_read_b64_tr_b16 v[80:81], v232 offset:24864\n\t" "ds_read_b64_tr_b16 v[82:83], v232 offset:25632\n\t" "ds_read_b64_tr_b16 v[84:85], v232 offset:37280\n\t"
                     "ds_read_b64_tr_b16 v[86:87], v232 offset:38048\n\t" "ds_read_b64_tr_b16 v[216:217], v233 offset:24896\n\t" "ds_read_b64_tr_b16 v[218:219], v233 offset:25664\n\t"
                     "ds_read_b64_tr_b16 v[220:221], v233 offset:37312\n\t" "ds_read_b64_tr_b16 v[222:223], v233 offset:38080\n\t" "s_waitcnt vmcnt(13) lgkmcnt(0)\n\t" "s_barrier\n\t" "v_add_u32 v232, 0x18400, v227\n\t"
                     "v_add_u32 v233, 0x18400, v228\n\t" "ds_read_b64_tr_b16 a[0:1], v232\n\t" "ds_read_b64_tr_b16 a[2:3], v232 offset:768\n\t" "ds_read_b64_tr_b16 a[4:5], v232 offset:12416\n\t"
                     "ds_read_b64_tr_b16 a[6:7], v232 offset:13184\n\t" "ds_read_b64_tr_b16 v[96:97], v232 offset:32\n\t" "ds_read_b64_tr_b16 v[98:99], v232 offset:800\n\t"
                     "ds_read_b64_tr_b16 v[100:101], v232 offset:12448\n\t" "ds_read_b64_tr_b16 v[102:103], v232 offset:13216\n\t" "ds_read_b64_tr_b16 a[8:9], v233 offset:64\n\t"
                     "ds_read_b64_tr_b16 a[10:11], v233 offset:832\n\t" "ds_read_b64_tr_b16 a[12:13], v233 offset:12480\n\t" "ds_read_b64_tr_b16 a[14:15], v233 offset:13248\n\t"
                     "ds_read_b64_tr_b16 a[16:17], v232 offset:24832\n\t" "ds_read_b64_tr_b16 a[18:19], v232 offset:25600\n\t" "ds_read_b64_tr_b16 a[20:21], v232 offset:37248\n\t"
                     "ds_read_b64_tr_b16 a[22:23], v232 offset:38016\n\t" "ds_read_b64_tr_b16 v[112:113], v232 offset:24864\n\t" "ds_read_b64_tr_b16 v[114:115], v232 offset:25632\n\t"
                     "ds_read_b64_tr_b16 v[116:117], v232 offset:37280\n\t" "ds_read_b64_tr_b16 v[118:119], v232 offset:38048\n\t" "ds_read_b64_tr_b16 a[24:25], v233 offset:24896\n\t"
                     "ds_read_b64_tr_b16 a[26:27], v233 offset:25664\n\t" "ds_read_b64_tr_b16 a[28:29], v233 offset:37312\n\t" "ds_read_b64_tr_b16 a[30:31], v233 offset:38080\n\t" "s_waitcnt vmcnt(0) lgkmcnt(0)\n\t"
                     "s_barrier\n\t" "v_add_u32 v232, 0x0, v227\n\t" "v_add_u32 v233, 0x0, v228\n\t" "ds_read_b64_tr_b16 a[32:33], v232\n\t" "ds_read_b64_tr_b16 a[34:35], v232 offset:768\n\t"
                     "ds_read_b64_tr_b16 a[36:37], v232 offset:12416\n\t" "ds_read_b64_tr_b16 a[38:39], v232 offset:13184\n\t" "ds_read_b64_tr_b16 a[40:41], v232 offset:32\n\t"
                     "ds_read_b64_tr_b16 a[42:43], v232 offset:800\n\t" "ds_read_b64_tr_b16 a[44:45], v232 offset:12448\n\t" "ds_read_b64_tr_b16 a[46:47], v232 offset:13216\n\t"
                     "ds_read_b64_tr_b16 a[48:49], v233 offset:64\n\t" "ds_read_b64_tr_b16 a[50:51], v233 offset:832\n\t" "ds_read_b64_tr_b16 a[52:53], v233 offset:12480\n\t"
                     "ds_read_b64_tr_b16 a[54:55], v233 offset:13248\n\t" "ds_read_b64_tr_b16 a[64:65], v232 offset:24832\n\t" "ds_read_b64_tr_b16 a[66:67], v232 offset:25600\n\t"
                     "ds_read_b64_tr_b16 a[68:69], v232 offset:37248\n\t" "ds_read_b64_tr_b16 a[70:71], v232 offset:38016\n\t" "ds_read_b64_tr_b16 a[72:73], v232 offset:24864\n\t"
                     "ds_read_b64_tr_b16 a[74:75], v232 offset:25632\n\t" "ds_read_b64_tr_b16 a[76:77], v232 offset:37280\n\t" "ds_read_b64_tr_b16 a[78:79], v232 offset:38048\n\t"
                     "ds_read_b64_tr_b16 a[80:81], v233 offset:24896\n\t" "ds_read_b64_tr_b16 a[82:83], v233 offset:25664\n\t" "ds_read_b64_tr_b16 a[84:85], v233 offset:37312\n\t"
                     "ds_read_b64_tr_b16 a[86:87], v233 offset:38080\n\t" "s_waitcnt lgkmcnt(0)\n\t" "s_barrier\n\t" "ds_write_b128 v230, v[0:3]\n\t" "ds_write_b128 v230, v[4:7] offset:1024\n\t"
                     "ds_write_b128 v230, v[8:11] offset:2048\n\t" "ds_write_b128 v230, v[12:15] offset:3072\n\t" "ds_write_b128 v230, v[16:19] offset:4096\n\t" "ds_write_b128 v230, v[20:23] offset:5120\n\t"
                     "ds_write_b128 v230, v[24:27] offset:6144\n\t" "ds_write_b128 v230, v[28:31] offset:7168\n\t" "ds_write_b128 v230, v[32:35] offset:8192\n\t" "ds_write_b128 v230, v[36:39] offset:9216\n\t"
                     "ds_write_b128 v230, v[40:43] offset:10240\n\t" "ds_write_b128 v230, v[44:47] offset:11264\n\t" "ds_write_b128 v230, v[48:51] offset:12288\n\t" "ds_write_b128 v230, v[52:55] offset:13312\n\t"
                     "ds_write_b128 v230, v[56:59] offset:14336\n\t" "ds_write_b128 v230, v[60:63] offset:15360\n\t" "ds_write_b128 v230, v[64:67] offset:16384\n\t" "ds_write_b128 v230, v[68:71] offset:17408\n\t"
                     "ds_write_b128 v230, v[72:75] offset:18432\n\t" "ds_write_b128 v230, v[76:79] offset:19456\n\t" "ds_write_b128 v230, v[80:83] offset:20480\n\t" "ds_write_b128 v230, v[84:87] offset:21504\n\t"
                     "ds_write_b128 v230, v[88:91] offset:22528\n\t" "ds_write_b128 v230, v[92:95] offset:23552\n\t" "ds_write_b128 v230, v[96:99] offset:24576\n\t" "ds_write_b128 v230, v[100:103] offset:25600\n\t"
                     "ds_write_b128 v230, v[104:107] offset:26624\n\t" "ds_write_b128 v230, v[108:111] offset:27648\n\t" "ds_write_b128 v230, v[112:115] offset:28672\n\t" "ds_write_b128 v230, v[116:119] offset:29696\n\t"
                     "ds_write_b128 v230, v[120:123] offset:30720\n\t" "ds_write_b128 v230, v[124:127] offset:31744\n\t"
                     : "=&{a[0:31]}"(wa[0]), "=&{a[32:63]}"(wa[1]), "=&{a[64:95]}"(wa[2]), "=&{a[96:127]}"(wa[3]), "=&{a[128:159]}"(wa[4]), "=&{a[160:191]}"(wa[5]), "=&{a[192:223]}"(wa[6]), "=&{a[224:255]}"(wa[7]), "=&{v[128:159]}"(wv[0]), "=&{v[160:191]}"(wv[1]), "=&{v[192:223]}"(wv[2]), "=&v"(y.r), "=&v"(y.g), "=&v"(y.e), "=&v"(kfl)
                     : "{v[224:231]}"(pa), "{s[44:45]}"(p.wq + h * DH * 2 + (size_t)(16 * w) * (HD * 2)), "{s[46:47]}"(p.wk + h * DH * 2 + (size_t)(16 * w) * (HD * 2)),
                       "s"(lds + 2 * w * GS), "s"(p.ws + OFF_LAT), "s"(fl + (F_K + h) * 128)
                     : "v0", "v1", "v2", "v3", "v4", "v5", "v6", "v7", "v8", "v9", "v10", "v11", "v12", "v13", "v14", "v15", "v16", "v17", "v18", "v19", "v20", "v21", "v22", "v23", "v24", "v25", "v26", "v27", "v28", "v29", "v30", "v31", "v32", "v33", "v34", "v35", "v36", "v37", "v38", "v39", "v40", "v41", "v42", "v43", "v44", "v45", "v46", "v47", "v48", "v49", "v50", "v51", "v52", "v53", "v54", "v55", "v56", "v57", "v58", "v59", "v60", "v61", "v62", "v63", "v64", "v65", "v66", "v67", "v68", "v69", "v70", "v71", "v72", "v73", "v74", "v75", "v76", "v77", "v78", "v79", "v80", "v81", "v82", "v83", "v84", "v85", "v86", "v87", "v88", "v89", "v90", "v91", "v92", "v93", "v94", "v95", "v96", "v97", "v98", "v99", "v100", "v101", "v102", "v103", "v104", "v105", "v106", "v107", "v108", "v109", "v110", "v111", "v112", "v113", "v114", "v115", "v116", "v117", "v118", "v119", "v120", "v121", "v122", "v123", "v124", "v125", "v126", "v127", "v232", "v233", "s42", "s43", "scc", "memory");
        const u64 t = sync_tag(y.e);
        const bool rdy = __builtin_amdgcn_ballot_w64((y.r[0] & ~1u) != (u32)t || y.r[1] != (u32)(t >> 32) || y.g[0] != (u32)t) == 0 && uni((int)kfl) != -1;
        if (rdy) kxdma();
        sd = sync_end(y, p.ws, phi, w == 0);
        if (!rdy) { flag_wait(fl, F_K + h, 1); kxdma(); }
        kxp += NH * 12288;
    }
    const u32 sidx = (lane & 2) ? 0xEEEEu : 0x4444u;
    const u32 moff = lane * 16;
    u32 kxa = kxl - 16 + (n >> 3) * 6160 + (16 * g + 4 * (n & 3) + ((n >> 1) & 2)) * 16;
    const u64 mb0 = 0xffff0000ffff0000ull;
    const int tp = (3 * w + (w & 1)) >> 1, ts = 1 + 3 * (w >> 1);
    u32 qw0 = lds + SL_IMG + tp * 512 + lane * 8, qw1 = lds + SL_IMG + ts * 512 + lane * 8 + 4 * (w & 1);
    const u32 kw0 = lds + SL_IMG + 3072 + w * 288 + n * 16 + 4 * g;
    const u32 qr0 = lds + SL_IMG + (g >> 1) * 512 + w * 128 + 64 * (g & 1) + 8 * qq;
    const u32 kr0 = lds + SL_IMG + 3072 + (qq >> 1) * 288 + g * 64 + (qq & 1) * 16 + 8 * (pp & 1);
    const u32 xso = 32768 + (h >> 4) * 16384 + (h & 15) * 512 + (w >> 1) * 256 + (n >> 3) * 64 + (n & 4) * 32 + (w & 1) * 32 + (g & 1) * 16 + (n & 3) * 4 + (g >> 1) * 8192;
    const u32 pso = (n < 8 ? 20480 + (h >> 1) * 256 + (n & 2) * 16 + (h & 1) * 8 + (n & 4) : 18432 + (h >> 2) * 256 + (h & 1) * 8 + (h & 2) * 2) + w * 64 + (g & 1) * 16 + (g >> 1) * 8192;
    const u64 psm = 0x01aa01aa01aa01aaull;
    const u32 kmo = (u32)(((n >> 3) * 4 + g) * 128 + (n & 7) * 16);
    u32x16 ad = u32x16{moff, sidx, lfa, kxa, qw0, qw1, kw0, kr0 + 1152 * min(w, 2) + (pp >> 1) * 576, qr0, kr0, xso, pso, 0x3dd53b94u, kmo, kmo, kmo};
    asm volatile("" : "+{v[224:239]}"(ad));
    const char* memp = p.ws + mslot(sd, 0);
    char* kmp = p.ws + OFF_KM + h * 3072 + min(w, 2) * 1024;
    const float kscale = 0.07216878364870322f;
    int budget = 30000;
    Clock clk; clk.init();
    u32x32 wk[3];
    asm volatile("" : "={v[64:95]}"(wk[0]), "={v[96:127]}"(wk[1]), "={v[32:63]}"(wk[2]));
    const int kfo = (F_K + 32 + h) * 128;
    for (int s = 0; s < NSEG; ++s) {
        int tries = 0;
        u64 bad, tp8, en = ~0ull;
        u32 tt;
        int kf;
        while (true) {
            const u32 tgt = clk.sched(phi, 0, cfg.G, tries ? 2 : 0);
            asm volatile("s_cmp_lg_u32 %12, 0\n\t" "s_cbranch_scc1 .Lrt_s\n\t" "s_waitcnt vmcnt(0) lgkmcnt(0)\n\t" "s_barrier\n\t" "s_cmp_lg_u32 %13, 0\n\t" "s_cbranch_scc1 .Lsp_s\n\t" ".Lrt_s:\n\t"
                         "ds_read_b128 v[64:67], v226\n\t" "v_mov_b64 v[40:41], 0\n\t" "ds_read_b128 v[68:71], v226 offset:1024\n\t" "v_mov_b64 v[42:43], 0\n\t" "ds_read_b128 v[72:75], v226 offset:2048\n\t"
                         "v_mov_b64 v[44:45], 0\n\t" "ds_read_b128 v[76:79], v226 offset:3072\n\t" "v_mov_b64 v[46:47], 0\n\t" "ds_read_b128 v[80:83], v226 offset:4096\n\t" "v_mov_b64 v[48:49], 0\n\t"
                         "ds_read_b128 v[84:87], v226 offset:5120\n\t" "v_mov_b64 v[50:51], 0\n\t" "ds_read_b128 v[88:91], v226 offset:6144\n\t" "v_mov_b64 v[52:53], 0\n\t" "ds_read_b128 v[92:95], v226 offset:7168\n\t"
                         "v_mov_b64 v[54:55], 0\n\t" "ds_read_b128 v[96:99], v226 offset:8192\n\t" "v_mov_b64 v[56:57], 0\n\t" "ds_read_b128 v[100:103], v226 offset:9216\n\t" "v_mov_b64 v[58:59], 0\n\t"
                         "ds_read_b128 v[104:107], v226 offset:10240\n\t" "v_mov_b64 v[60:61], 0\n\t" "ds_read_b128 v[108:111], v226 offset:11264\n\t" "v_mov_b64 v[62:63], 0\n\t"
                         "ds_read_b128 v[112:115], v226 offset:12288\n\t" "ds_read_b128 v[116:119], v226 offset:13312\n\t" "ds_read_b128 v[120:123], v226 offset:14336\n\t" "ds_read_b128 v[124:127], v226 offset:15360\n\t"
                         ".Lsp_s:\n\t" "s_memtime vcc\n\t" "s_waitcnt lgkmcnt(0)\n\t" "s_sub_i32 s42, %4, vcc_lo\n\t" "s_cmp_gt_i32 s42, 80\n\t" "s_cbranch_scc1 .Lsp_s\n\t" "s_max_i32 s42, s42, 0\n\t"
                         "s_sub_i32 s42, 92, s42\n\t" "s_and_b32 s42, s42, -4\n\t" "s_getpc_b64 vcc\n\t" "s_add_u32 vcc_lo, vcc_lo, s42\n\t" "s_addc_u32 vcc_hi, vcc_hi, 0\n\t" "s_setpc_b64 vcc\n\t" "s_nop 0\n\t" "s_nop 0\n\t"
                         "s_nop 0\n\t" "s_nop 0\n\t" "s_nop 0\n\t" "s_nop 0\n\t" "s_nop 0\n\t" "s_nop 0\n\t" "s_nop 0\n\t" "s_nop 0\n\t" "s_nop 0\n\t" "s_nop 0\n\t" "s_nop 0\n\t" "s_nop 0\n\t" "s_nop 0\n\t" "s_nop 0\n\t"
                         "s_nop 0\n\t" "s_nop 0\n\t" "s_nop 0\n\t" "s_nop 0\n\t" "global_load_dwordx4 v[0:3], v224, %5 offset:-4096 sc1\n\t" "s_nop 10\n\t" "global_load_dwordx4 v[4:7], v224, %5 offset:-3072 sc1\n\t"
                         "s_nop 10\n\t" "global_load_dwordx4 v[8:11], v224, %5 offset:-2048 sc1\n\t" "s_nop 10\n\t" "global_load_dwordx4 v[12:15], v224, %5 offset:-1024 sc1\n\t" "s_nop 10\n\t"
                         "global_load_dwordx4 v[16:19], v224, %5 sc1\n\t" "s_nop 10\n\t" "global_load_dwordx4 v[20:23], v224, %5 offset:1024 sc1\n\t" "s_nop 10\n\t" "global_load_dwordx4 v[24:27], v224, %5 offset:2048 sc1\n\t"
                         "s_nop 10\n\t" "global_load_dwordx4 v[28:31], v224, %5 offset:3072 sc1\n\t" "s_nop 10\n\t" "global_load_dwordx4 v[32:35], v224, %6 offset:-4096 sc1\n\t" "s_nop 10\n\t"
                         "global_load_dwordx4 v[36:39], v224, %6 offset:-3072 sc1\n\t" "s_waitcnt vmcnt(9)\n\t" "v_smfmac_f32_16x16x64_bf16 v[40:43], v[0:3], v[128:135], v225\n\t"
                         "v_smfmac_f32_16x16x64_bf16 v[44:47], v[0:3], v[64:71], v225\n\t" "ds_read_b128 v[64:67], v226 offset:16384\n\t" "v_smfmac_f32_16x16x64_bf16 v[48:51], v[0:3], v[136:143], v225\n\t"
                         "ds_read_b128 v[68:71], v226 offset:17408\n\t" "v_smfmac_f32_16x16x64_bf16 v[52:55], v[0:3], v[72:79], v225\n\t" "ds_read_b128 v[72:75], v226 offset:18432\n\t" "s_waitcnt vmcnt(8)\n\t"
                         "v_smfmac_f32_16x16x64_bf16 v[40:43], v[4:7], v[144:151], v225\n\t" "ds_read_b128 v[76:79], v226 offset:19456\n\t" "v_smfmac_f32_16x16x64_bf16 v[44:47], v[4:7], v[80:87], v225\n\t"
                         "ds_read_b128 v[80:83], v226 offset:20480\n\t" "v_smfmac_f32_16x16x64_bf16 v[48:51], v[4:7], v[152:159], v225\n\t" "ds_read_b128 v[84:87], v226 offset:21504\n\t"
                         "v_smfmac_f32_16x16x64_bf16 v[52:55], v[4:7], v[88:95], v225\n\t" "ds_read_b128 v[88:91], v226 offset:22528\n\t" "s_waitcnt vmcnt(7)\n\t"
                         "v_smfmac_f32_16x16x64_bf16 v[40:43], v[8:11], v[160:167], v225\n\t" "ds_read_b128 v[92:95], v226 offset:23552\n\t" "v_max_u32 v238, v0, v4\n\t"
                         "v_smfmac_f32_16x16x64_bf16 v[44:47], v[8:11], v[96:103], v225\n\t" "ds_read_b128 v[96:99], v226 offset:24576\n\t" "v_cmp_eq_u32 vcc, -1, v238\n\t"
                         "v_smfmac_f32_16x16x64_bf16 v[48:51], v[8:11], v[168:175], v225\n\t" "ds_read_b128 v[100:103], v226 offset:25600\n\t" "v_smfmac_f32_16x16x64_bf16 v[52:55], v[8:11], v[104:111], v225\n\t"
                         "s_and_b64 vcc, vcc, %11\n\t" "s_cbranch_scc1 .Lsk_s\n\t" "ds_read_b128 v[104:107], v226 offset:26624\n\t" "s_waitcnt vmcnt(6)\n\t" "v_smfmac_f32_16x16x64_bf16 v[40:43], v[12:15], v[176:183], v225\n\t"
                         "ds_read_b128 v[108:111], v226 offset:27648\n\t" "v_smfmac_f32_16x16x64_bf16 v[44:47], v[12:15], v[112:119], v225\n\t" "ds_read_b128 v[112:115], v226 offset:28672\n\t"
                         "v_smfmac_f32_16x16x64_bf16 v[48:51], v[12:15], v[184:191], v225\n\t" "ds_read_b128 v[116:119], v226 offset:29696\n\t" "v_smfmac_f32_16x16x64_bf16 v[52:55], v[12:15], v[120:127], v225\n\t"
                         "ds_read_b128 v[120:123], v226 offset:30720\n\t" "s_waitcnt vmcnt(5)\n\t" "v_smfmac_f32_16x16x64_bf16 v[40:43], v[16:19], v[192:199], v225\n\t" "ds_read_b128 v[124:127], v226 offset:31744\n\t"
                         "s_waitcnt lgkmcnt(14)\n\t" "v_smfmac_f32_16x16x64_bf16 v[44:47], v[16:19], v[64:71], v225\n\t" "ds_read_b128 v[64:67], v227\n\t" "v_smfmac_f32_16x16x64_bf16 v[48:51], v[16:19], v[200:207], v225\n\t"
                         "ds_read_b128 v[68:71], v227 offset:1024\n\t" "s_waitcnt lgkmcnt(14)\n\t" "v_smfmac_f32_16x16x64_bf16 v[52:55], v[16:19], v[72:79], v225\n\t" "ds_read_b128 v[72:75], v227 offset:16\n\t"
                         "s_waitcnt vmcnt(4)\n\t" "v_smfmac_f32_16x16x64_bf16 v[40:43], v[20:23], v[208:215], v225\n\t" "ds_read_b128 v[76:79], v227 offset:1040\n\t" "s_waitcnt lgkmcnt(14)\n\t"
                         "v_smfmac_f32_16x16x64_bf16 v[44:47], v[20:23], v[80:87], v225\n\t" "ds_read_b128 v[80:83], v227 offset:2048\n\t" "v_smfmac_f32_16x16x64_bf16 v[48:51], v[20:23], v[216:223], v225\n\t"
                         "ds_read_b128 v[84:87], v227 offset:3072\n\t" "s_waitcnt lgkmcnt(14)\n\t" "v_smfmac_f32_16x16x64_bf16 v[52:55], v[20:23], v[88:95], v225\n\t" "ds_read_b128 v[88:91], v227 offset:2064\n\t"
                         "s_waitcnt vmcnt(3)\n\t" "v_smfmac_f32_16x16x64_bf16 v[40:43], v[24:27], a[0:7], v225\n\t" "ds_read_b128 v[92:95], v227 offset:3088\n\t" "s_waitcnt lgkmcnt(14)\n\t"
                         "v_smfmac_f32_16x16x64_bf16 v[44:47], v[24:27], v[96:103], v225\n\t" "ds_read_b128 v[96:99], v227 offset:4096\n\t" "v_smfmac_f32_16x16x64_bf16 v[48:51], v[24:27], a[8:15], v225\n\t"
                         "ds_read_b128 v[100:103], v227 offset:5120\n\t" "s_waitcnt lgkmcnt(14)\n\t" "v_smfmac_f32_16x16x64_bf16 v[52:55], v[24:27], v[104:111], v225\n\t" "ds_read_b128 v[104:107], v227 offset:4112\n\t"
                         "s_waitcnt vmcnt(2)\n\t" "v_smfmac_f32_16x16x64_bf16 v[40:43], v[28:31], a[16:23], v225\n\t" "ds_read_b128 v[108:111], v227 offset:5136\n\t" "s_waitcnt lgkmcnt(14)\n\t"
                         "v_smfmac_f32_16x16x64_bf16 v[44:47], v[28:31], v[112:119], v225\n\t" "v_smfmac_f32_16x16x64_bf16 v[48:51], v[28:31], a[24:31], v225\n\t" "s_waitcnt lgkmcnt(12)\n\t"
                         "v_smfmac_f32_16x16x64_bf16 v[52:55], v[28:31], v[120:127], v225\n\t" "s_waitcnt vmcnt(1)\n\t" "v_smfmac_f32_16x16x64_bf16 v[40:43], v[32:35], a[32:39], v225\n\t" "v_mov_b64 v[112:113], 0\n\t"
                         "v_smfmac_f32_16x16x64_bf16 v[44:47], v[32:35], a[40:47], v225\n\t" "v_mov_b64 v[114:115], 0\n\t" "v_smfmac_f32_16x16x64_bf16 v[48:51], v[32:35], a[48:55], v225\n\t" "v_mov_b64 v[116:117], 0\n\t"
                         "v_smfmac_f32_16x16x64_bf16 v[52:55], v[32:35], a[56:63], v225\n\t" "v_mov_b64 v[118:119], 0\n\t" "s_waitcnt vmcnt(0)\n\t" "v_smfmac_f32_16x16x64_bf16 v[40:43], v[36:39], a[64:71], v225\n\t"
                         "v_mov_b64 v[120:121], 0\n\t" "v_smfmac_f32_16x16x64_bf16 v[44:47], v[36:39], a[72:79], v225\n\t" "v_mov_b64 v[122:123], 0\n\t" "v_smfmac_f32_16x16x64_bf16 v[48:51], v[36:39], a[80:87], v225\n\t"
                         "v_mov_b64 v[124:125], 0\n\t" "v_smfmac_f32_16x16x64_bf16 v[52:55], v[36:39], a[88:95], v225\n\t" "v_add_f32 v40, v40, v42\n\t" "v_add_f32 v41, v41, v43\n\t"
                         "v_smfmac_f32_16x16x64_bf16 v[56:59], v[0:3], a[96:103], v225\n\t" "v_cmp_u_f32 vcc, v40, v41\n\t" "v_smfmac_f32_16x16x64_bf16 v[56:59], v[4:7], a[104:111], v225\n\t"
                         "v_cvt_pk_bf16_f32 v42, v40, v41\n\t" "v_add_f32 v44, v44, v46\n\t" "v_smfmac_f32_16x16x64_bf16 v[56:59], v[8:11], a[112:119], v225\n\t" "s_and_b64 vcc, vcc, %11\n\t" "s_cbranch_scc1 .Lsk_s\n\t"
                         "v_smfmac_f32_16x16x64_bf16 v[56:59], v[12:15], a[120:127], v225\n\t" "v_add_f32 v45, v45, v47\n\t" "v_cvt_pk_bf16_f32 v43, v44, v45\n\t"
                         "v_smfmac_f32_16x16x64_bf16 v[56:59], v[16:19], a[128:135], v225\n\t" "ds_write_b64 v228, v[42:43]\n\t" "v_add_f32 v48, v48, v50\n\t"
                         "v_smfmac_f32_16x16x64_bf16 v[56:59], v[20:23], a[136:143], v225\n\t" "v_add_f32 v49, v49, v51\n\t" "v_cvt_pk_bf16_f32 v46, v48, v49\n\t"
                         "v_smfmac_f32_16x16x64_bf16 v[56:59], v[24:27], a[144:151], v225\n\t" "ds_write_b32 v229, v46\n\t" "v_add_f32 v52, v52, v54\n\t" "v_smfmac_f32_16x16x64_bf16 v[56:59], v[28:31], a[152:159], v225\n\t"
                         "v_add_f32 v53, v53, v55\n\t" "v_cvt_pk_bf16_f32 v54, v52, v53\n\t" "v_smfmac_f32_16x16x64_bf16 v[56:59], v[32:35], a[160:167], v225\n\t" "ds_write_b32 v230, v54\n\t"
                         "v_smfmac_f32_16x16x64_bf16 v[56:59], v[36:39], a[168:175], v225\n\t" "v_smfmac_f32_16x16x64_bf16 v[60:63], v[0:3], a[176:183], v225\n\t"
                         "v_smfmac_f32_16x16x64_bf16 v[60:63], v[4:7], a[184:191], v225\n\t" "v_add_f32 v56, v56, v58\n\t" "v_smfmac_f32_16x16x64_bf16 v[60:63], v[8:11], a[192:199], v225\n\t" "s_waitcnt lgkmcnt(0)\n\t"
                         "s_barrier\n\t" "v_smfmac_f32_16x16x64_bf16 v[60:63], v[12:15], a[200:207], v225\n\t" "ds_read_b64_tr_b16 v[40:41], v232\n\t" "v_add_f32 v57, v57, v59\n\t"
                         "v_smfmac_f32_16x16x64_bf16 v[60:63], v[16:19], a[208:215], v225\n\t" "ds_read_b64_tr_b16 v[42:43], v232 offset:32\n\t" "v_cvt_pk_bf16_f32 v58, v56, v57\n\t"
                         "v_smfmac_f32_16x16x64_bf16 v[60:63], v[20:23], a[216:223], v225\n\t" "ds_write_b32 v230, v58 offset:1152\n\t" "v_smfmac_f32_16x16x64_bf16 v[60:63], v[24:27], a[224:231], v225\n\t"
                         "ds_read_b64_tr_b16 v[44:45], v232 offset:1024\n\t" "ds_read_b64_tr_b16 v[46:47], v232 offset:1056\n\t" "v_smfmac_f32_16x16x64_bf16 v[60:63], v[28:31], a[232:239], v225\n\t"
                         "ds_read_b64_tr_b16 v[48:49], v232 offset:2048\n\t" "ds_read_b64_tr_b16 v[50:51], v232 offset:2080\n\t" "v_smfmac_f32_16x16x64_bf16 v[60:63], v[32:35], a[240:247], v225\n\t"
                         "ds_read_b64_tr_b16 v[0:1], v233\n\t" "ds_read_b64_tr_b16 v[2:3], v233 offset:32\n\t" "v_smfmac_f32_16x16x64_bf16 v[60:63], v[36:39], a[248:255], v225\n\t"
                         "ds_read_b64_tr_b16 v[4:5], v233 offset:576\n\t" "ds_read_b64_tr_b16 v[6:7], v233 offset:608\n\t" "s_waitcnt lgkmcnt(9)\n\t" "v_smfmac_f32_16x16x64_bf16 v[112:115], v[40:43], v[64:71], v225\n\t"
                         "s_waitcnt lgkmcnt(8)\n\t" "s_barrier\n\t" "v_smfmac_f32_16x16x64_bf16 v[116:119], v[40:43], v[72:79], v225\n\t" "ds_read_b64_tr_b16 v[8:9], v233 offset:1152\n\t" "s_waitcnt lgkmcnt(7)\n\t"
                         "v_add_f32 v60, v60, v62\n\t" "v_add_f32 v61, v61, v63\n\t" "v_smfmac_f32_16x16x64_bf16 v[112:115], v[44:47], v[80:87], v225\n\t" "ds_read_b64_tr_b16 v[10:11], v233 offset:1184\n\t"
                         "v_cvt_pk_bf16_f32 v62, v60, v61\n\t" "v_smfmac_f32_16x16x64_bf16 v[116:119], v[44:47], v[88:95], v225\n\t" "ds_write_b32 v230, v62 offset:2304\n\t" "s_waitcnt lgkmcnt(7)\n\t"
                         "v_smfmac_f32_16x16x64_bf16 v[112:115], v[48:51], v[96:103], v225\n\t" "ds_read_b64_tr_b16 v[12:13], v233 offset:1728\n\t" "v_smfmac_f32_16x16x64_bf16 v[116:119], v[48:51], v[104:111], v225\n\t"
                         "ds_read_b64_tr_b16 v[14:15], v233 offset:1760\n\t" "s_waitcnt lgkmcnt(5)\n\t" "v_smfmac_f32_16x16x64_bf16 v[120:123], v[40:43], v[0:7], v225\n\t" "v_cndmask_b32 v24, v112, v113, %8\n\t"
                         "v_cndmask_b32 v25, v114, v115, %8\n\t" "v_add_f32 v24, v24, v25\n\t" "v_pk_add_f32 v[26:27], v[116:117], v[118:119]\n\t" "v_cndmask_b32 v25, v26, v27, %8\n\t" "v_max_f32 v26, v24, v25\n\t"
                         "s_waitcnt lgkmcnt(2)\n\t" "s_barrier\n\t" "ds_read_b64_tr_b16 v[16:17], v233 offset:2304\n\t" "v_max_f32_dpp v27, v26, v26 row_ror:4 row_mask:0xf bank_mask:0xf\n\t"
                         "ds_read_b64_tr_b16 v[18:19], v233 offset:2336\n\t" "v_max_f32_dpp v27, v26, v27 row_ror:8 row_mask:0xf bank_mask:0xf\n\t" "v_max_f32_dpp v27, v26, v27 row_ror:12 row_mask:0xf bank_mask:0xf\n\t"
                         "ds_read_b64_tr_b16 v[20:21], v233 offset:2880\n\t" "ds_read_b64_tr_b16 v[22:23], v233 offset:2912\n\t" "v_max_f32_dpp v30, v27, v27 row_ror:1 row_mask:0xf bank_mask:0xf\n\t"
                         "v_max_f32_dpp v30, v27, v30 row_ror:2 row_mask:0xf bank_mask:0xf\n\t" "v_max_f32_dpp v30, v27, v30 row_ror:3 row_mask:0xf bank_mask:0xf\n\t" "s_waitcnt lgkmcnt(4)\n\t"
                         "v_pk_add_f32 v[24:25], v[24:25], v[30:31] op_sel_hi:[1,0] neg_lo:[0,1] neg_hi:[0,1]\n\t" "v_pk_mul_f32 v[24:25], v[24:25], v[236:237] op_sel_hi:[1,0]\n\t" "v_exp_f32 v24, v24\n\t"
                         "v_exp_f32 v25, v25\n\t" "v_mul_f32 v31, %10, v30\n\t" "v_cvt_pk_bf16_f32 v28, |v24|, |v25|\n\t" "v_smfmac_f32_16x16x64_bf16 v[120:123], v[44:47], v[8:15], v225\n\t"
                         "global_store_dword v234, v28, %7 offset:-4096 sc1\n\t" "s_waitcnt lgkmcnt(0)\n\t" "v_add_f32 v26, v24, v25\n\t" "v_smfmac_f32_16x16x64_bf16 v[120:123], v[48:51], v[16:23], v225\n\t"
                         "s_nop 0\n\t" "v_add_f32_dpp v27, v26, v26 row_ror:4 row_mask:0xf bank_mask:0xf\n\t" "v_add_f32_dpp v27, v26, v27 row_ror:8 row_mask:0xf bank_mask:0xf\n\t"
                         "v_add_f32_dpp v27, v26, v27 row_ror:12 row_mask:0xf bank_mask:0xf\n\t" "s_nop 1\n\t" "v_add_f32_dpp v29, v27, v27 row_ror:1 row_mask:0xf bank_mask:0xf\n\t"
                         "v_pk_add_f32 v[24:25], v[120:121], v[122:123]\n\t" "v_cndmask_b32 v24, v24, v25, %8\n\t" "v_max_f32 v26, v24, v30\n\t" "v_add_f32_dpp v29, v27, v29 row_ror:2 row_mask:0xf bank_mask:0xf\n\t"
                         "v_add_f32_dpp v29, v27, v29 row_ror:3 row_mask:0xf bank_mask:0xf\n\t" "v_max_f32_dpp v27, v26, v26 row_ror:1 row_mask:0xf bank_mask:0xf\n\t"
                         "v_max_f32_dpp v27, v26, v27 row_ror:2 row_mask:0xf bank_mask:0xf\n\t" "v_max_f32_dpp v27, v26, v27 row_ror:3 row_mask:0xf bank_mask:0xf\n\t" "v_mul_f32 v24, %10, v24\n\t"
                         "v_mul_f32 v29, 0x3e000000, v29\n\t" "v_max_f32_dpp v26, v27, v27 row_ror:4 row_mask:0xf bank_mask:0xf\n\t" "v_fma_f32 v24, -v26, %10, v24\n\t" "v_fma_f32 v25, -v26, %10, v31\n\t"
                         "v_exp_f32 v24, v24\n\t" "v_exp_f32 v25, v25\n\t" "s_nop 0\n\t" "v_fma_f32 v26, v25, v29, v24\n\t" "v_mov_b32_dpp v124, v24 quad_perm:[0,1,2,3] row_mask:0xf bank_mask:0x3\n\t"
                         "v_mov_b32_dpp v125, v24 quad_perm:[1,0,3,2] row_mask:0xf bank_mask:0x3\n\t" "v_add_f32_dpp v27, v26, v26 row_ror:1 row_mask:0xf bank_mask:0xf\n\t"
                         "v_add_f32_dpp v27, v26, v27 row_ror:2 row_mask:0xf bank_mask:0xf\n\t" "v_add_f32_dpp v27, v26, v27 row_ror:3 row_mask:0xf bank_mask:0xf\n\t"
                         "v_mov_b32_dpp v124, v25 quad_perm:[0,1,2,3] row_mask:0x5 bank_mask:0x4\n\t" "v_mov_b32_dpp v125, v25 quad_perm:[0,1,2,3] row_mask:0xa bank_mask:0x4\n\t"
                         "v_add_f32_dpp v26, v27, v27 row_ror:4 row_mask:0xf bank_mask:0xf\n\t" "v_rcp_f32 v26, v26\n\t" "s_mov_b64 exec, %9\n\t" "v_pk_mul_f32 v[124:125], v[124:125], v[26:27] op_sel_hi:[1,0]\n\t"
                         "v_cvt_pk_bf16_f32 v24, |v124|, |v125|\n\t" "global_store_dword v235, v24, %7 offset:-4096 sc1\n\t" "s_mov_b64 exec, -1\n\t" "s_memrealtime %1\n\t" "s_load_dword %2, %14, %13 offset:%15\n\t"
                         "ds_read_b64_tr_b16 v[36:37], v231\n\t" "ds_read_b64_tr_b16 v[38:39], v231 offset:32\n\t" "v_readfirstlane_b32 %3, v237\n\t" "s_mov_b32 m0, %17\n\t" "v_mov_b64 v[40:41], 0\n\t"
                         "v_mov_b64 v[42:43], 0\n\t" "v_mov_b64 v[44:45], 0\n\t" "v_mov_b64 v[46:47], 0\n\t" "v_mov_b64 v[48:49], 0\n\t" "v_mov_b64 v[50:51], 0\n\t" "v_mov_b64 v[52:53], 0\n\t" "v_mov_b64 v[54:55], 0\n\t"
                         "v_mov_b64 v[56:57], 0\n\t" "v_mov_b64 v[58:59], 0\n\t" "v_mov_b64 v[60:61], 0\n\t" "v_mov_b64 v[62:63], 0\n\t" "v_mov_b32 v237, %18\n\t" "s_waitcnt lgkmcnt(0)\n\t" "ds_read_b128 v[64:67], v226\n\t"
                         "ds_read_b128 v[68:71], v226 offset:1024\n\t" "ds_read_b128 v[72:75], v226 offset:2048\n\t" "ds_read_b128 v[76:79], v226 offset:3072\n\t" "ds_read_b128 v[80:83], v226 offset:4096\n\t"
                         "ds_read_b128 v[84:87], v226 offset:5120\n\t" "s_cmp_eq_u32 %3, -1\n\t" "s_cbranch_scc1 .Lnk_s\n\t" "global_load_lds_dwordx4 v224, %16 sc1\n\t" "global_load_lds_dwordx4 v224, %16 offset:1024 sc1\n\t"
                         "global_load_lds_dwordx4 v224, %16 offset:2048 sc1\n\t" ".Lnk_s:\n\t" "global_store_dwordx4 v239, v[36:39], %21 sc1\n\t" "s_and_b64 exec, %19, 1\n\t" "global_load_dword v237, v237, %20 sc1\n\t"
                         "s_mov_b64 exec, -1\n\t" "ds_read_b128 v[88:91], v226 offset:6144\n\t" "ds_read_b128 v[92:95], v226 offset:7168\n\t" "ds_read_b128 v[96:99], v226 offset:8192\n\t"
                         "ds_read_b128 v[100:103], v226 offset:9216\n\t" "ds_read_b128 v[104:107], v226 offset:10240\n\t" "ds_read_b128 v[108:111], v226 offset:11264\n\t" "ds_read_b128 v[112:115], v226 offset:12288\n\t"
                         "ds_read_b128 v[116:119], v226 offset:13312\n\t" "ds_read_b128 v[120:123], v226 offset:14336\n\t" "ds_read_b128 v[124:127], v226 offset:15360\n\t" ".Lsk_s:\n\t" "s_mov_b64 %0, vcc\n\t"
                         : "=&s"(bad), "=&s"(tp8), "=&s"(tt), "=&s"(kf)
                         : "s"(uni((int)tgt)), "s"(memp), "s"(memp + 16384), "s"((u64)memp & ~8192ull), "s"(mb0), "s"(psm), "s"(kscale * 1.4426950408889634f), "s"(en), "s"(tries), "s"(4 * s),
                           "s"(__builtin_amdgcn_kernarg_segment_ptr()), "n"(TT_OFF), "s"(kxp), "s"(kxw), "s"(kfo + (s < 6 ? 0 : (s + 2) >> 3) * 4096), "s"(0x0040404040404044ull >> s), "s"(fl), "s"(kmp),
                           "{v[224:239]}"(ad), "{v[64:95]}"(wk[0]), "{v[96:127]}"(wk[1]), "{v[32:63]}"(wk[2]),
                           "{a[0:31]}"(wa[0]), "{a[32:63]}"(wa[1]), "{a[64:95]}"(wa[2]), "{a[96:127]}"(wa[3]), "{a[128:159]}"(wa[4]), "{a[160:191]}"(wa[5]), "{a[192:223]}"(wa[6]), "{a[224:255]}"(wa[7]), "{v[128:159]}"(wv[0]), "{v[160:191]}"(wv[1]), "{v[192:223]}"(wv[2])
                         : "v0", "v1", "v2", "v3", "v4", "v5", "v6", "v7", "v8", "v9", "v10", "v11", "v12", "v13", "v14", "v15", "v16", "v17", "v18", "v19", "v20", "v21", "v22", "v23", "v24", "v25", "v26", "v27", "v28", "v29", "v30", "v31", "vcc", "scc", "s42", "memory");
            if (__builtin_expect(bad == 0, 1)) break;
            waitvm<0>();
            if (((++tries & 15) == 0 && !unwritten(memp, ad[0])) || --budget < 0) en = 0;
            phi += (u32)cfg.G;
        }
        if (__builtin_expect(kf == -1, 0)) { flag_wait(fl, F_K + pblk(s + 1) * 32 + h, 1); kxdma(); }
        kxp += s < NSEG - 2 ? NH * 12288 : 0;
        clk.calib();
        if (__builtin_expect(s >= NSEG - 2, 0)) {
            waitvm<0>();
            barrier();
            if (w == 3) flag_add(fl, F_PROG + 8 * s + (h & 7), 1);
        }
        if (s == HPI) tv_ = phi;
        follow(phi, (int)(tt >> 16) - VISS, (u32)tp8, cfg.G);
        phi += tt & 0xffffu;
        memp = p.ws + mslot(sd, s + 1); kmp += NH * 3072;
    }
    if (h == 0 && w == 0) {
        flag_wait(fl, F_ACK, 256);
        Sync y;
        sync_ld(y, p.ws);
        asm volatile("s_waitcnt vmcnt(0)" : "+v"(y.r), "+v"(y.g), "+v"(y.e) : : "memory");
        const u64 t = ((u64)y.e[1] << 32 | y.e[0]) + 1;
        if (lanex() == 0) gst8(p.ws + OFF_LAT, 1024, u32x2{(u32)t, (u32)(t >> 32)});
    }
    if (h == 0 && w == 0) { const u32 wake = tk_ + 20000 + 4 * (tv_ - tk_); while ((int)(rt() - wake) < 0) __builtin_amdgcn_s_sleep(4); }
    asm volatile(".Lkm_last:\n\ts_waitcnt vmcnt(0)" : : : "memory");
}

DI void proj_role(const Ptrs& p, const ChainCfg cfg, int h, u32 lds, int w) {
    u32x32 wa[8], wv[4];
    u32 phi;
    int sd;
    {
        const int lane = lanex(), tid = 64 * w + lane;
        const int g = lane >> 4, qq = (lane >> 2) & 3, pp = lane & 3;
        constexpr int GS = 3104;
        auto dvo = [&](int i) { const int ix = i * 64 + lane; return (u32)((ix / 24) * (HD * 2) + (ix % 24) * 16); };
        auto ovo = [&](int i) { const int u = i * 64 + lane, r = u / 80; return (u32)(r * 1280 + (((u % 80) ^ (2 * (r ^ (w & 2)))) << 4)); };
        const u32 tra = lds + (2 * (g >> 1) + (qq >> 1)) * GS + (4 * (g & 1) + (qq & 1)) * 384 + 96 * w + 32 * (w & 1) + 8 * pp;
        const u32 tro = (4 * g + (qq & 1) + 16 * (qq >> 1)) * 1280 + 8 * pp + ((u32)(2 * w ^ 2 * ((4 * g + qq) & 7)) << 4);
        u32x16 pa = u32x16{dvo(0), dvo(1), dvo(2), tra, tra - 96 * (w & 1), tro, lds + (u32)((w & 1) * 24576 + (w >> 1) * 99328 + lane * 16), (u32)(lane & 31) * 8,
                           ovo(0), ovo(1), ovo(2), ovo(3), ovo(4), tro, tro, tro};
        Sync y;
        asm volatile("s_nop 4\n\t" "s_add_u32 s42, s44, 0x0\n\t" "s_addc_u32 s43, s45, 0\n\t" "s_add_u32 m0, %18, 0x0\n\t" "s_nop 0\n\t" "global_load_lds_dwordx4 v224, s[42:43]\n\t" "s_add_u32 m0, %18, 0x400\n\t"
                     "s_nop 0\n\t" "global_load_lds_dwordx4 v225, s[42:43]\n\t" "s_add_u32 m0, %18, 0x800\n\t" "s_nop 0\n\t" "global_load_lds_dwordx4 v226, s[42:43]\n\t" "s_add_u32 s42, s44, 0x18000\n\t"
                     "s_addc_u32 s43, s45, 0\n\t" "s_add_u32 m0, %18, 0xc20\n\t" "s_nop 0\n\t" "global_load_lds_dwordx4 v224, s[42:43]\n\t" "s_add_u32 m0, %18, 0x1020\n\t" "s_nop 0\n\t"
                     "global_load_lds_dwordx4 v225, s[42:43]\n\t" "s_add_u32 m0, %18, 0x1420\n\t" "s_nop 0\n\t" "global_load_lds_dwordx4 v226, s[42:43]\n\t" "s_add_u32 s42, s44, 0xc0000\n\t" "s_addc_u32 s43, s45, 0\n\t"
                     "s_add_u32 m0, %18, 0x6100\n\t" "s_nop 0\n\t" "global_load_lds_dwordx4 v224, s[42:43]\n\t" "s_add_u32 m0, %18, 0x6500\n\t" "s_nop 0\n\t" "global_load_lds_dwordx4 v225, s[42:43]\n\t"
                     "s_add_u32 m0, %18, 0x6900\n\t" "s_nop 0\n\t" "global_load_lds_dwordx4 v226, s[42:43]\n\t" "s_add_u32 s42, s44, 0xd8000\n\t" "s_addc_u32 s43, s45, 0\n\t" "s_add_u32 m0, %18, 0x6d20\n\t" "s_nop 0\n\t"
                     "global_load_lds_dwordx4 v224, s[42:43]\n\t" "s_add_u32 m0, %18, 0x7120\n\t" "s_nop 0\n\t" "global_load_lds_dwordx4 v225, s[42:43]\n\t" "s_add_u32 m0, %18, 0x7520\n\t" "s_nop 0\n\t"
                     "global_load_lds_dwordx4 v226, s[42:43]\n\t" "s_add_u32 s42, s44, 0x180000\n\t" "s_addc_u32 s43, s45, 0\n\t" "s_add_u32 m0, %18, 0xc200\n\t" "s_nop 0\n\t" "global_load_lds_dwordx4 v224, s[42:43]\n\t"
                     "s_add_u32 m0, %18, 0xc600\n\t" "s_nop 0\n\t" "global_load_lds_dwordx4 v225, s[42:43]\n\t" "s_add_u32 m0, %18, 0xca00\n\t" "s_nop 0\n\t" "global_load_lds_dwordx4 v226, s[42:43]\n\t"
                     "s_add_u32 s42, s44, 0x198000\n\t" "s_addc_u32 s43, s45, 0\n\t" "s_add_u32 m0, %18, 0xce20\n\t" "s_nop 0\n\t" "global_load_lds_dwordx4 v224, s[42:43]\n\t" "s_add_u32 m0, %18, 0xd220\n\t" "s_nop 0\n\t"
                     "global_load_lds_dwordx4 v225, s[42:43]\n\t" "s_add_u32 m0, %18, 0xd620\n\t" "s_nop 0\n\t" "global_load_lds_dwordx4 v226, s[42:43]\n\t" "s_add_u32 s42, s44, 0x240000\n\t" "s_addc_u32 s43, s45, 0\n\t"
                     "s_add_u32 m0, %18, 0x12300\n\t" "s_nop 0\n\t" "global_load_lds_dwordx4 v224, s[42:43]\n\t" "s_add_u32 m0, %18, 0x12700\n\t" "s_nop 0\n\t" "global_load_lds_dwordx4 v225, s[42:43]\n\t"
                     "s_add_u32 m0, %18, 0x12b00\n\t" "s_nop 0\n\t" "global_load_lds_dwordx4 v226, s[42:43]\n\t" "s_add_u32 s42, s44, 0x258000\n\t" "s_addc_u32 s43, s45, 0\n\t" "s_add_u32 m0, %18, 0x12f20\n\t"
                     "s_nop 0\n\t" "global_load_lds_dwordx4 v224, s[42:43]\n\t" "s_add_u32 m0, %18, 0x13320\n\t" "s_nop 0\n\t" "global_load_lds_dwordx4 v225, s[42:43]\n\t" "s_add_u32 m0, %18, 0x13720\n\t" "s_nop 0\n\t"
                     "global_load_lds_dwordx4 v226, s[42:43]\n\t" "s_waitcnt vmcnt(12) lgkmcnt(0)\n\t" "s_barrier\n\t" "s_add_u32 s42, s44, 0x300000\n\t" "s_addc_u32 s43, s45, 0\n\t" "s_add_u32 m0, %18, 0x18400\n\t"
                     "s_nop 0\n\t" "global_load_lds_dwordx4 v224, s[42:43]\n\t" "s_add_u32 m0, %18, 0x18800\n\t" "s_nop 0\n\t" "global_load_lds_dwordx4 v225, s[42:43]\n\t" "s_add_u32 m0, %18, 0x18c00\n\t" "s_nop 0\n\t"
                     "global_load_lds_dwordx4 v226, s[42:43]\n\t" "s_add_u32 s42, s44, 0x318000\n\t" "s_addc_u32 s43, s45, 0\n\t" "s_add_u32 m0, %18, 0x19020\n\t" "s_nop 0\n\t" "global_load_lds_dwordx4 v224, s[42:43]\n\t"
                     "s_add_u32 m0, %18, 0x19420\n\t" "s_nop 0\n\t" "global_load_lds_dwordx4 v225, s[42:43]\n\t" "s_add_u32 m0, %18, 0x19820\n\t" "s_nop 0\n\t" "global_load_lds_dwordx4 v226, s[42:43]\n\t"
                     "s_add_u32 s42, s44, 0x3c0000\n\t" "s_addc_u32 s43, s45, 0\n\t" "s_add_u32 m0, %18, 0x1e500\n\t" "s_nop 0\n\t" "global_load_lds_dwordx4 v224, s[42:43]\n\t" "s_add_u32 m0, %18, 0x1e900\n\t"
                     "s_nop 0\n\t" "global_load_lds_dwordx4 v225, s[42:43]\n\t" "s_add_u32 m0, %18, 0x1ed00\n\t" "s_nop 0\n\t" "global_load_lds_dwordx4 v226, s[42:43]\n\t" "s_add_u32 s42, s44, 0x3d8000\n\t"
                     "s_addc_u32 s43, s45, 0\n\t" "s_add_u32 m0, %18, 0x1f120\n\t" "s_nop 0\n\t" "global_load_lds_dwordx4 v224, s[42:43]\n\t" "s_add_u32 m0, %18, 0x1f520\n\t" "s_nop 0\n\t"
                     "global_load_lds_dwordx4 v225, s[42:43]\n\t" "s_add_u32 m0, %18, 0x1f920\n\t" "s_nop 0\n\t" "global_load_lds_dwordx4 v226, s[42:43]\n\t" "v_add_u32 v237, 0x0, v227\n\t" "v_add_u32 v238, 0x0, v228\n\t"
                     "ds_read_b64_tr_b16 a[0:1], v237\n\t" "ds_read_b64_tr_b16 a[2:3], v237 offset:768\n\t" "ds_read_b64_tr_b16 a[4:5], v237 offset:12416\n\t" "ds_read_b64_tr_b16 a[6:7], v237 offset:13184\n\t"
                     "ds_read_b64_tr_b16 v[0:1], v237 offset:32\n\t" "ds_read_b64_tr_b16 v[2:3], v237 offset:800\n\t" "ds_read_b64_tr_b16 v[4:5], v237 offset:12448\n\t" "ds_read_b64_tr_b16 v[6:7], v237 offset:13216\n\t"
                     "ds_read_b64_tr_b16 a[8:9], v238 offset:64\n\t" "ds_read_b64_tr_b16 a[10:11], v238 offset:832\n\t" "ds_read_b64_tr_b16 a[12:13], v238 offset:12480\n\t"
                     "ds_read_b64_tr_b16 a[14:15], v238 offset:13248\n\t" "ds_read_b64_tr_b16 a[16:17], v237 offset:24832\n\t" "ds_read_b64_tr_b16 a[18:19], v237 offset:25600\n\t"
                     "ds_read_b64_tr_b16 a[20:21], v237 offset:37248\n\t" "ds_read_b64_tr_b16 a[22:23], v237 offset:38016\n\t" "ds_read_b64_tr_b16 v[8:9], v237 offset:24864\n\t"
                     "ds_read_b64_tr_b16 v[10:11], v237 offset:25632\n\t" "ds_read_b64_tr_b16 v[12:13], v237 offset:37280\n\t" "ds_read_b64_tr_b16 v[14:15], v237 offset:38048\n\t"
                     "ds_read_b64_tr_b16 a[24:25], v238 offset:24896\n\t" "ds_read_b64_tr_b16 a[26:27], v238 offset:25664\n\t" "ds_read_b64_tr_b16 a[28:29], v238 offset:37312\n\t"
                     "ds_read_b64_tr_b16 a[30:31], v238 offset:38080\n\t" "s_waitcnt vmcnt(12) lgkmcnt(0)\n\t" "s_barrier\n\t" "s_add_u32 s42, s44, 0x480000\n\t" "s_addc_u32 s43, s45, 0\n\t" "s_add_u32 m0, %18, 0x0\n\t"
                     "s_nop 0\n\t" "global_load_lds_dwordx4 v224, s[42:43]\n\t" "s_add_u32 m0, %18, 0x400\n\t" "s_nop 0\n\t" "global_load_lds_dwordx4 v225, s[42:43]\n\t" "s_add_u32 m0, %18, 0x800\n\t" "s_nop 0\n\t"
                     "global_load_lds_dwordx4 v226, s[42:43]\n\t" "s_add_u32 s42, s44, 0x498000\n\t" "s_addc_u32 s43, s45, 0\n\t" "s_add_u32 m0, %18, 0xc20\n\t" "s_nop 0\n\t" "global_load_lds_dwordx4 v224, s[42:43]\n\t"
                     "s_add_u32 m0, %18, 0x1020\n\t" "s_nop 0\n\t" "global_load_lds_dwordx4 v225, s[42:43]\n\t" "s_add_u32 m0, %18, 0x1420\n\t" "s_nop 0\n\t" "global_load_lds_dwordx4 v226, s[42:43]\n\t"
                     "s_add_u32 s42, s44, 0x540000\n\t" "s_addc_u32 s43, s45, 0\n\t" "s_add_u32 m0, %18, 0x6100\n\t" "s_nop 0\n\t" "global_load_lds_dwordx4 v224, s[42:43]\n\t" "s_add_u32 m0, %18, 0x6500\n\t" "s_nop 0\n\t"
                     "global_load_lds_dwordx4 v225, s[42:43]\n\t" "s_add_u32 m0, %18, 0x6900\n\t" "s_nop 0\n\t" "global_load_lds_dwordx4 v226, s[42:43]\n\t" "s_add_u32 s42, s44, 0x558000\n\t" "s_addc_u32 s43, s45, 0\n\t"
                     "s_add_u32 m0, %18, 0x6d20\n\t" "s_nop 0\n\t" "global_load_lds_dwordx4 v224, s[42:43]\n\t" "s_add_u32 m0, %18, 0x7120\n\t" "s_nop 0\n\t" "global_load_lds_dwordx4 v225, s[42:43]\n\t"
                     "s_add_u32 m0, %18, 0x7520\n\t" "s_nop 0\n\t" "global_load_lds_dwordx4 v226, s[42:43]\n\t" "v_add_u32 v237, 0xc200, v227\n\t" "v_add_u32 v238, 0xc200, v228\n\t" "ds_read_b64_tr_b16 a[32:33], v237\n\t"
                     "ds_read_b64_tr_b16 a[34:35], v237 offset:768\n\t" "ds_read_b64_tr_b16 a[36:37], v237 offset:12416\n\t" "ds_read_b64_tr_b16 a[38:39], v237 offset:13184\n\t"
                     "ds_read_b64_tr_b16 v[16:17], v237 offset:32\n\t" "ds_read_b64_tr_b16 v[18:19], v237 offset:800\n\t" "ds_read_b64_tr_b16 v[20:21], v237 offset:12448\n\t"
                     "ds_read_b64_tr_b16 v[22:23], v237 offset:13216\n\t" "ds_read_b64_tr_b16 a[40:41], v238 offset:64\n\t" "ds_read_b64_tr_b16 a[42:43], v238 offset:832\n\t"
                     "ds_read_b64_tr_b16 a[44:45], v238 offset:12480\n\t" "ds_read_b64_tr_b16 a[46:47], v238 offset:13248\n\t" "ds_read_b64_tr_b16 a[48:49], v237 offset:24832\n\t"
                     "ds_read_b64_tr_b16 a[50:51], v237 offset:25600\n\t" "ds_read_b64_tr_b16 a[52:53], v237 offset:37248\n\t" "ds_read_b64_tr_b16 a[54:55], v237 offset:38016\n\t"
                     "ds_read_b64_tr_b16 v[24:25], v237 offset:24864\n\t" "ds_read_b64_tr_b16 v[26:27], v237 offset:25632\n\t" "ds_read_b64_tr_b16 v[28:29], v237 offset:37280\n\t"
                     "ds_read_b64_tr_b16 v[30:31], v237 offset:38048\n\t" "ds_read_b64_tr_b16 a[56:57], v238 offset:24896\n\t" "ds_read_b64_tr_b16 a[58:59], v238 offset:25664\n\t"
                     "ds_read_b64_tr_b16 a[60:61], v238 offset:37312\n\t" "ds_read_b64_tr_b16 a[62:63], v238 offset:38080\n\t" "s_waitcnt vmcnt(12) lgkmcnt(0)\n\t" "s_barrier\n\t" "s_add_u32 s42, s44, 0x600000\n\t"
                     "s_addc_u32 s43, s45, 0\n\t" "s_add_u32 m0, %18, 0xc200\n\t" "s_nop 0\n\t" "global_load_lds_dwordx4 v224, s[42:43]\n\t" "s_add_u32 m0, %18, 0xc600\n\t" "s_nop 0\n\t"
                     "global_load_lds_dwordx4 v225, s[42:43]\n\t" "s_add_u32 m0, %18, 0xca00\n\t" "s_nop 0\n\t" "global_load_lds_dwordx4 v226, s[42:43]\n\t" "s_add_u32 s42, s44, 0x618000\n\t" "s_addc_u32 s43, s45, 0\n\t"
                     "s_add_u32 m0, %18, 0xce20\n\t" "s_nop 0\n\t" "global_load_lds_dwordx4 v224, s[42:43]\n\t" "s_add_u32 m0, %18, 0xd220\n\t" "s_nop 0\n\t" "global_load_lds_dwordx4 v225, s[42:43]\n\t"
                     "s_add_u32 m0, %18, 0xd620\n\t" "s_nop 0\n\t" "global_load_lds_dwordx4 v226, s[42:43]\n\t" "s_add_u32 s42, s44, 0x6c0000\n\t" "s_addc_u32 s43, s45, 0\n\t" "s_add_u32 m0, %18, 0x12300\n\t" "s_nop 0\n\t"
                     "global_load_lds_dwordx4 v224, s[42:43]\n\t" "s_add_u32 m0, %18, 0x12700\n\t" "s_nop 0\n\t" "global_load_lds_dwordx4 v225, s[42:43]\n\t" "s_add_u32 m0, %18, 0x12b00\n\t" "s_nop 0\n\t"
                     "global_load_lds_dwordx4 v226, s[42:43]\n\t" "s_add_u32 s42, s44, 0x6d8000\n\t" "s_addc_u32 s43, s45, 0\n\t" "s_add_u32 m0, %18, 0x12f20\n\t" "s_nop 0\n\t" "global_load_lds_dwordx4 v224, s[42:43]\n\t"
                     "s_add_u32 m0, %18, 0x13320\n\t" "s_nop 0\n\t" "global_load_lds_dwordx4 v225, s[42:43]\n\t" "s_add_u32 m0, %18, 0x13720\n\t" "s_nop 0\n\t" "global_load_lds_dwordx4 v226, s[42:43]\n\t"
                     "v_add_u32 v237, 0x18400, v227\n\t" "v_add_u32 v238, 0x18400, v228\n\t" "ds_read_b64_tr_b16 a[64:65], v237\n\t" "ds_read_b64_tr_b16 a[66:67], v237 offset:768\n\t"
                     "ds_read_b64_tr_b16 a[68:69], v237 offset:12416\n\t" "ds_read_b64_tr_b16 a[70:71], v237 offset:13184\n\t" "ds_read_b64_tr_b16 v[32:33], v237 offset:32\n\t"
                     "ds_read_b64_tr_b16 v[34:35], v237 offset:800\n\t" "ds_read_b64_tr_b16 v[36:37], v237 offset:12448\n\t" "ds_read_b64_tr_b16 v[38:39], v237 offset:13216\n\t"
                     "ds_read_b64_tr_b16 a[72:73], v238 offset:64\n\t" "ds_read_b64_tr_b16 a[74:75], v238 offset:832\n\t" "ds_read_b64_tr_b16 a[76:77], v238 offset:12480\n\t"
                     "ds_read_b64_tr_b16 a[78:79], v238 offset:13248\n\t" "ds_read_b64_tr_b16 a[80:81], v237 offset:24832\n\t" "ds_read_b64_tr_b16 a[82:83], v237 offset:25600\n\t"
                     "ds_read_b64_tr_b16 a[84:85], v237 offset:37248\n\t" "ds_read_b64_tr_b16 a[86:87], v237 offset:38016\n\t" "ds_read_b64_tr_b16 v[40:41], v237 offset:24864\n\t"
                     "ds_read_b64_tr_b16 v[42:43], v237 offset:25632\n\t" "ds_read_b64_tr_b16 v[44:45], v237 offset:37280\n\t" "ds_read_b64_tr_b16 v[46:47], v237 offset:38048\n\t"
                     "ds_read_b64_tr_b16 a[88:89], v238 offset:24896\n\t" "ds_read_b64_tr_b16 a[90:91], v238 offset:25664\n\t" "ds_read_b64_tr_b16 a[92:93], v238 offset:37312\n\t"
                     "ds_read_b64_tr_b16 a[94:95], v238 offset:38080\n\t" "s_waitcnt vmcnt(12) lgkmcnt(0)\n\t" "s_barrier\n\t" "s_add_u32 s42, s46, 0x0\n\t" "s_addc_u32 s43, s47, 0\n\t" "s_add_u32 m0, %20, 0x18400\n\t"
                     "s_nop 0\n\t" "global_load_lds_dwordx4 v232, s[42:43]\n\t" "s_add_u32 m0, %20, 0x18800\n\t" "s_nop 0\n\t" "global_load_lds_dwordx4 v233, s[42:43]\n\t" "s_add_u32 m0, %20, 0x18c00\n\t" "s_nop 0\n\t"
                     "global_load_lds_dwordx4 v234, s[42:43]\n\t" "s_add_u32 m0, %20, 0x19000\n\t" "s_nop 0\n\t" "global_load_lds_dwordx4 v235, s[42:43]\n\t" "s_add_u32 m0, %20, 0x19400\n\t" "s_nop 0\n\t"
                     "global_load_lds_dwordx4 v236, s[42:43]\n\t" "s_add_u32 s42, s46, 0x1400\n\t" "s_addc_u32 s43, s47, 0\n\t" "s_add_u32 m0, %20, 0x19800\n\t" "v_xor_b32 v224, 0x80, v232\n\t"
                     "global_load_lds_dwordx4 v224, s[42:43]\n\t" "s_add_u32 m0, %20, 0x19c00\n\t" "v_xor_b32 v224, 0x80, v233\n\t" "global_load_lds_dwordx4 v224, s[42:43]\n\t" "s_add_u32 m0, %20, 0x1a000\n\t"
                     "v_xor_b32 v224, 0x80, v234\n\t" "global_load_lds_dwordx4 v224, s[42:43]\n\t" "s_add_u32 m0, %20, 0x1a400\n\t" "v_xor_b32 v224, 0x80, v235\n\t" "global_load_lds_dwordx4 v224, s[42:43]\n\t"
                     "s_add_u32 m0, %20, 0x1a800\n\t" "v_xor_b32 v224, 0x80, v236\n\t" "global_load_lds_dwordx4 v224, s[42:43]\n\t" "v_add_u32 v237, 0x0, v227\n\t" "v_add_u32 v238, 0x0, v228\n\t"
                     "ds_read_b64_tr_b16 a[96:97], v237\n\t" "ds_read_b64_tr_b16 a[98:99], v237 offset:768\n\t" "ds_read_b64_tr_b16 a[100:101], v237 offset:12416\n\t" "ds_read_b64_tr_b16 a[102:103], v237 offset:13184\n\t"
                     "ds_read_b64_tr_b16 v[48:49], v237 offset:32\n\t" "ds_read_b64_tr_b16 v[50:51], v237 offset:800\n\t" "ds_read_b64_tr_b16 v[52:53], v237 offset:12448\n\t"
                     "ds_read_b64_tr_b16 v[54:55], v237 offset:13216\n\t" "ds_read_b64_tr_b16 a[104:105], v238 offset:64\n\t" "ds_read_b64_tr_b16 a[106:107], v238 offset:832\n\t"
                     "ds_read_b64_tr_b16 a[108:109], v238 offset:12480\n\t" "ds_read_b64_tr_b16 a[110:111], v238 offset:13248\n\t" "ds_read_b64_tr_b16 a[112:113], v237 offset:24832\n\t"
                     "ds_read_b64_tr_b16 a[114:115], v237 offset:25600\n\t" "ds_read_b64_tr_b16 a[116:117], v237 offset:37248\n\t" "ds_read_b64_tr_b16 a[118:119], v237 offset:38016\n\t"
                     "ds_read_b64_tr_b16 v[56:57], v237 offset:24864\n\t" "ds_read_b64_tr_b16 v[58:59], v237 offset:25632\n\t" "ds_read_b64_tr_b16 v[60:61], v237 offset:37280\n\t"
                     "ds_read_b64_tr_b16 v[62:63], v237 offset:38048\n\t" "ds_read_b64_tr_b16 a[120:121], v238 offset:24896\n\t" "ds_read_b64_tr_b16 a[122:123], v238 offset:25664\n\t"
                     "ds_read_b64_tr_b16 a[124:125], v238 offset:37312\n\t" "ds_read_b64_tr_b16 a[126:127], v238 offset:38080\n\t" "s_waitcnt vmcnt(10) lgkmcnt(0)\n\t" "s_barrier\n\t" "s_add_u32 s42, s46, 0xa000\n\t"
                     "s_addc_u32 s43, s47, 0\n\t" "s_add_u32 m0, %20, 0x0\n\t" "s_nop 0\n\t" "global_load_lds_dwordx4 v232, s[42:43]\n\t" "s_add_u32 m0, %20, 0x400\n\t" "s_nop 0\n\t"
                     "global_load_lds_dwordx4 v233, s[42:43]\n\t" "s_add_u32 m0, %20, 0x800\n\t" "s_nop 0\n\t" "global_load_lds_dwordx4 v234, s[42:43]\n\t" "s_add_u32 m0, %20, 0xc00\n\t" "s_nop 0\n\t"
                     "global_load_lds_dwordx4 v235, s[42:43]\n\t" "s_add_u32 m0, %20, 0x1000\n\t" "s_nop 0\n\t" "global_load_lds_dwordx4 v236, s[42:43]\n\t" "s_add_u32 s42, s46, 0xb400\n\t" "s_addc_u32 s43, s47, 0\n\t"
                     "s_add_u32 m0, %20, 0x1400\n\t" "v_xor_b32 v224, 0x80, v232\n\t" "global_load_lds_dwordx4 v224, s[42:43]\n\t" "s_add_u32 m0, %20, 0x1800\n\t" "v_xor_b32 v224, 0x80, v233\n\t"
                     "global_load_lds_dwordx4 v224, s[42:43]\n\t" "s_add_u32 m0, %20, 0x1c00\n\t" "v_xor_b32 v224, 0x80, v234\n\t" "global_load_lds_dwordx4 v224, s[42:43]\n\t" "s_add_u32 m0, %20, 0x2000\n\t"
                     "v_xor_b32 v224, 0x80, v235\n\t" "global_load_lds_dwordx4 v224, s[42:43]\n\t" "s_add_u32 m0, %20, 0x2400\n\t" "v_xor_b32 v224, 0x80, v236\n\t" "global_load_lds_dwordx4 v224, s[42:43]\n\t"
                     "v_add_u32 v237, 0xc200, v227\n\t" "v_add_u32 v238, 0xc200, v228\n\t" "ds_read_b64_tr_b16 a[128:129], v237\n\t" "ds_read_b64_tr_b16 a[130:131], v237 offset:768\n\t"
                     "ds_read_b64_tr_b16 a[132:133], v237 offset:12416\n\t" "ds_read_b64_tr_b16 a[134:135], v237 offset:13184\n\t" "ds_read_b64_tr_b16 a[136:137], v237 offset:32\n\t"
                     "ds_read_b64_tr_b16 a[138:139], v237 offset:800\n\t" "ds_read_b64_tr_b16 a[140:141], v237 offset:12448\n\t" "ds_read_b64_tr_b16 a[142:143], v237 offset:13216\n\t"
                     "ds_read_b64_tr_b16 a[144:145], v238 offset:64\n\t" "ds_read_b64_tr_b16 a[146:147], v238 offset:832\n\t" "ds_read_b64_tr_b16 a[148:149], v238 offset:12480\n\t"
                     "ds_read_b64_tr_b16 a[150:151], v238 offset:13248\n\t" "ds_read_b64_tr_b16 a[152:153], v237 offset:24832\n\t" "ds_read_b64_tr_b16 a[154:155], v237 offset:25600\n\t"
                     "ds_read_b64_tr_b16 a[156:157], v237 offset:37248\n\t" "ds_read_b64_tr_b16 a[158:159], v237 offset:38016\n\t" "ds_read_b64_tr_b16 a[160:161], v237 offset:24864\n\t"
                     "ds_read_b64_tr_b16 a[162:163], v237 offset:25632\n\t" "ds_read_b64_tr_b16 a[164:165], v237 offset:37280\n\t" "ds_read_b64_tr_b16 a[166:167], v237 offset:38048\n\t"
                     "ds_read_b64_tr_b16 a[168:169], v238 offset:24896\n\t" "ds_read_b64_tr_b16 a[170:171], v238 offset:25664\n\t" "ds_read_b64_tr_b16 a[172:173], v238 offset:37312\n\t"
                     "ds_read_b64_tr_b16 a[174:175], v238 offset:38080\n\t" "s_waitcnt vmcnt(10) lgkmcnt(0)\n\t" "s_barrier\n\t" "s_add_u32 s42, s46, 0x14000\n\t" "s_addc_u32 s43, s47, 0\n\t"
                     "s_add_u32 m0, %20, 0xc200\n\t" "s_nop 0\n\t" "global_load_lds_dwordx4 v232, s[42:43]\n\t" "s_add_u32 m0, %20, 0xc600\n\t" "s_nop 0\n\t" "global_load_lds_dwordx4 v233, s[42:43]\n\t"
                     "s_add_u32 m0, %20, 0xca00\n\t" "s_nop 0\n\t" "global_load_lds_dwordx4 v234, s[42:43]\n\t" "s_add_u32 m0, %20, 0xce00\n\t" "s_nop 0\n\t" "global_load_lds_dwordx4 v235, s[42:43]\n\t"
                     "s_add_u32 m0, %20, 0xd200\n\t" "s_nop 0\n\t" "global_load_lds_dwordx4 v236, s[42:43]\n\t" "s_add_u32 s42, s46, 0x15400\n\t" "s_addc_u32 s43, s47, 0\n\t" "s_add_u32 m0, %20, 0xd600\n\t"
                     "v_xor_b32 v224, 0x80, v232\n\t" "global_load_lds_dwordx4 v224, s[42:43]\n\t" "s_add_u32 m0, %20, 0xda00\n\t" "v_xor_b32 v224, 0x80, v233\n\t" "global_load_lds_dwordx4 v224, s[42:43]\n\t"
                     "s_add_u32 m0, %20, 0xde00\n\t" "v_xor_b32 v224, 0x80, v234\n\t" "global_load_lds_dwordx4 v224, s[42:43]\n\t" "s_add_u32 m0, %20, 0xe200\n\t" "v_xor_b32 v224, 0x80, v235\n\t"
                     "global_load_lds_dwordx4 v224, s[42:43]\n\t" "s_add_u32 m0, %20, 0xe600\n\t" "v_xor_b32 v224, 0x80, v236\n\t" "global_load_lds_dwordx4 v224, s[42:43]\n\t" "s_add_u32 s42, %21, 0x18400\n\t"
                     "v_xor_b32 v237, 0x0, v229\n\t" "v_add_u32 v237, s42, v237\n\t" "v_xor_b32 v238, 0x40, v229\n\t" "v_add_u32 v238, s42, v238\n\t" "v_xor_b32 v239, 0x80, v229\n\t" "v_add_u32 v239, s42, v239\n\t"
                     "v_xor_b32 v226, 0xc0, v229\n\t" "v_add_u32 v226, s42, v226\n\t" "ds_read_b64_tr_b16 a[176:177], v237\n\t" "ds_read_b64_tr_b16 a[178:179], v238 offset:2560\n\t"
                     "ds_read_b64_tr_b16 a[200:201], v239\n\t" "ds_read_b64_tr_b16 a[202:203], v226 offset:2560\n\t" "ds_read_b64_tr_b16 a[224:225], v237 offset:256\n\t"
                     "ds_read_b64_tr_b16 a[226:227], v238 offset:2816\n\t" "ds_read_b64_tr_b16 a[248:249], v239 offset:256\n\t" "ds_read_b64_tr_b16 a[250:251], v226 offset:2816\n\t"
                     "ds_read_b64_tr_b16 v[112:113], v237 offset:512\n\t" "ds_read_b64_tr_b16 v[114:115], v238 offset:3072\n\t" "ds_read_b64_tr_b16 v[136:137], v239 offset:512\n\t"
                     "ds_read_b64_tr_b16 v[138:139], v226 offset:3072\n\t" "ds_read_b64_tr_b16 v[160:161], v237 offset:768\n\t" "ds_read_b64_tr_b16 v[162:163], v238 offset:3328\n\t"
                     "ds_read_b64_tr_b16 v[184:185], v239 offset:768\n\t" "ds_read_b64_tr_b16 v[186:187], v226 offset:3328\n\t" "ds_read_b64_tr_b16 v[208:209], v237 offset:1024\n\t"
                     "ds_read_b64_tr_b16 v[210:211], v238 offset:3584\n\t" "ds_read_b64_tr_b16 v[72:73], v239 offset:1024\n\t" "ds_read_b64_tr_b16 v[74:75], v226 offset:3584\n\t" "s_waitcnt vmcnt(10) lgkmcnt(0)\n\t"
                     "s_barrier\n\t" "s_add_u32 s42, s46, 0x1e000\n\t" "s_addc_u32 s43, s47, 0\n\t" "s_add_u32 m0, %20, 0x18400\n\t" "s_nop 0\n\t" "global_load_lds_dwordx4 v232, s[42:43]\n\t"
                     "s_add_u32 m0, %20, 0x18800\n\t" "s_nop 0\n\t" "global_load_lds_dwordx4 v233, s[42:43]\n\t" "s_add_u32 m0, %20, 0x18c00\n\t" "s_nop 0\n\t" "global_load_lds_dwordx4 v234, s[42:43]\n\t"
                     "s_add_u32 m0, %20, 0x19000\n\t" "s_nop 0\n\t" "global_load_lds_dwordx4 v235, s[42:43]\n\t" "s_add_u32 m0, %20, 0x19400\n\t" "s_nop 0\n\t" "global_load_lds_dwordx4 v236, s[42:43]\n\t"
                     "s_add_u32 s42, s46, 0x1f400\n\t" "s_addc_u32 s43, s47, 0\n\t" "s_add_u32 m0, %20, 0x19800\n\t" "v_xor_b32 v224, 0x80, v232\n\t" "global_load_lds_dwordx4 v224, s[42:43]\n\t"
                     "s_add_u32 m0, %20, 0x19c00\n\t" "v_xor_b32 v224, 0x80, v233\n\t" "global_load_lds_dwordx4 v224, s[42:43]\n\t" "s_add_u32 m0, %20, 0x1a000\n\t" "v_xor_b32 v224, 0x80, v234\n\t"
                     "global_load_lds_dwordx4 v224, s[42:43]\n\t" "s_add_u32 m0, %20, 0x1a400\n\t" "v_xor_b32 v224, 0x80, v235\n\t" "global_load_lds_dwordx4 v224, s[42:43]\n\t" "s_add_u32 m0, %20, 0x1a800\n\t"
                     "v_xor_b32 v224, 0x80, v236\n\t" "global_load_lds_dwordx4 v224, s[42:43]\n\t" "s_add_u32 s42, %21, 0x0\n\t" "v_xor_b32 v237, 0x0, v229\n\t" "v_add_u32 v237, s42, v237\n\t"
                     "v_xor_b32 v238, 0x40, v229\n\t" "v_add_u32 v238, s42, v238\n\t" "v_xor_b32 v239, 0x80, v229\n\t" "v_add_u32 v239, s42, v239\n\t" "v_xor_b32 v226, 0xc0, v229\n\t" "v_add_u32 v226, s42, v226\n\t"
                     "ds_read_b64_tr_b16 a[180:181], v237\n\t" "ds_read_b64_tr_b16 a[182:183], v238 offset:2560\n\t" "ds_read_b64_tr_b16 a[204:205], v239\n\t" "ds_read_b64_tr_b16 a[206:207], v226 offset:2560\n\t"
                     "ds_read_b64_tr_b16 a[228:229], v237 offset:256\n\t" "ds_read_b64_tr_b16 a[230:231], v238 offset:2816\n\t" "ds_read_b64_tr_b16 a[252:253], v239 offset:256\n\t"
                     "ds_read_b64_tr_b16 a[254:255], v226 offset:2816\n\t" "ds_read_b64_tr_b16 v[116:117], v237 offset:512\n\t" "ds_read_b64_tr_b16 v[118:119], v238 offset:3072\n\t"
                     "ds_read_b64_tr_b16 v[140:141], v239 offset:512\n\t" "ds_read_b64_tr_b16 v[142:143], v226 offset:3072\n\t" "ds_read_b64_tr_b16 v[164:165], v237 offset:768\n\t"
                     "ds_read_b64_tr_b16 v[166:167], v238 offset:3328\n\t" "ds_read_b64_tr_b16 v[188:189], v239 offset:768\n\t" "ds_read_b64_tr_b16 v[190:191], v226 offset:3328\n\t"
                     "ds_read_b64_tr_b16 v[212:213], v237 offset:1024\n\t" "ds_read_b64_tr_b16 v[214:215], v238 offset:3584\n\t" "ds_read_b64_tr_b16 v[76:77], v239 offset:1024\n\t"
                     "ds_read_b64_tr_b16 v[78:79], v226 offset:3584\n\t" "s_waitcnt vmcnt(10) lgkmcnt(0)\n\t" "s_barrier\n\t" "v_mov_b32 v225, 0\n\t" "global_load_dwordx2 %12, v231, %19 sc1\n\t"
                     "global_load_dwordx2 %13, v225, %19 offset:512 sc1\n\t" "global_load_dwordx2 %14, v225, %19 offset:1024 sc1\n\t" "s_add_u32 s42, s46, 0x28000\n\t" "s_addc_u32 s43, s47, 0\n\t"
                     "s_add_u32 m0, %20, 0x0\n\t" "s_nop 0\n\t" "global_load_lds_dwordx4 v232, s[42:43]\n\t" "s_add_u32 m0, %20, 0x400\n\t" "s_nop 0\n\t" "global_load_lds_dwordx4 v233, s[42:43]\n\t"
                     "s_add_u32 m0, %20, 0x800\n\t" "s_nop 0\n\t" "global_load_lds_dwordx4 v234, s[42:43]\n\t" "s_add_u32 m0, %20, 0xc00\n\t" "s_nop 0\n\t" "global_load_lds_dwordx4 v235, s[42:43]\n\t"
                     "s_add_u32 m0, %20, 0x1000\n\t" "s_nop 0\n\t" "global_load_lds_dwordx4 v236, s[42:43]\n\t" "s_add_u32 s42, s46, 0x29400\n\t" "s_addc_u32 s43, s47, 0\n\t" "s_add_u32 m0, %20, 0x1400\n\t"
                     "v_xor_b32 v224, 0x80, v232\n\t" "global_load_lds_dwordx4 v224, s[42:43]\n\t" "s_add_u32 m0, %20, 0x1800\n\t" "v_xor_b32 v224, 0x80, v233\n\t" "global_load_lds_dwordx4 v224, s[42:43]\n\t"
                     "s_add_u32 m0, %20, 0x1c00\n\t" "v_xor_b32 v224, 0x80, v234\n\t" "global_load_lds_dwordx4 v224, s[42:43]\n\t" "s_add_u32 m0, %20, 0x2000\n\t" "v_xor_b32 v224, 0x80, v235\n\t"
                     "global_load_lds_dwordx4 v224, s[42:43]\n\t" "s_add_u32 m0, %20, 0x2400\n\t" "v_xor_b32 v224, 0x80, v236\n\t" "global_load_lds_dwordx4 v224, s[42:43]\n\t" "s_add_u32 s42, %21, 0xc200\n\t"
                     "v_xor_b32 v237, 0x0, v229\n\t" "v_add_u32 v237, s42, v237\n\t" "v_xor_b32 v238, 0x40, v229\n\t" "v_add_u32 v238, s42, v238\n\t" "v_xor_b32 v239, 0x80, v229\n\t" "v_add_u32 v239, s42, v239\n\t"
                     "v_xor_b32 v226, 0xc0, v229\n\t" "v_add_u32 v226, s42, v226\n\t" "ds_read_b64_tr_b16 a[184:185], v237\n\t" "ds_read_b64_tr_b16 a[186:187], v238 offset:2560\n\t"
                     "ds_read_b64_tr_b16 a[208:209], v239\n\t" "ds_read_b64_tr_b16 a[210:211], v226 offset:2560\n\t" "ds_read_b64_tr_b16 a[232:233], v237 offset:256\n\t"
                     "ds_read_b64_tr_b16 a[234:235], v238 offset:2816\n\t" "ds_read_b64_tr_b16 v[96:97], v239 offset:256\n\t" "ds_read_b64_tr_b16 v[98:99], v226 offset:2816\n\t"
                     "ds_read_b64_tr_b16 v[120:121], v237 offset:512\n\t" "ds_read_b64_tr_b16 v[122:123], v238 offset:3072\n\t" "ds_read_b64_tr_b16 v[144:145], v239 offset:512\n\t"
                     "ds_read_b64_tr_b16 v[146:147], v226 offset:3072\n\t" "ds_read_b64_tr_b16 v[168:169], v237 offset:768\n\t" "ds_read_b64_tr_b16 v[170:171], v238 offset:3328\n\t"
                     "ds_read_b64_tr_b16 v[192:193], v239 offset:768\n\t" "ds_read_b64_tr_b16 v[194:195], v226 offset:3328\n\t" "ds_read_b64_tr_b16 v[216:217], v237 offset:1024\n\t"
                     "ds_read_b64_tr_b16 v[218:219], v238 offset:3584\n\t" "ds_read_b64_tr_b16 v[80:81], v239 offset:1024\n\t" "ds_read_b64_tr_b16 v[82:83], v226 offset:3584\n\t" "s_waitcnt vmcnt(10) lgkmcnt(0)\n\t"
                     "s_barrier\n\t" "s_add_u32 s42, s46, 0x32000\n\t" "s_addc_u32 s43, s47, 0\n\t" "s_add_u32 m0, %20, 0xc200\n\t" "s_nop 0\n\t" "global_load_lds_dwordx4 v232, s[42:43]\n\t" "s_add_u32 m0, %20, 0xc600\n\t"
                     "s_nop 0\n\t" "global_load_lds_dwordx4 v233, s[42:43]\n\t" "s_add_u32 m0, %20, 0xca00\n\t" "s_nop 0\n\t" "global_load_lds_dwordx4 v234, s[42:43]\n\t" "s_add_u32 m0, %20, 0xce00\n\t" "s_nop 0\n\t"
                     "global_load_lds_dwordx4 v235, s[42:43]\n\t" "s_add_u32 m0, %20, 0xd200\n\t" "s_nop 0\n\t" "global_load_lds_dwordx4 v236, s[42:43]\n\t" "s_add_u32 s42, s46, 0x33400\n\t" "s_addc_u32 s43, s47, 0\n\t"
                     "s_add_u32 m0, %20, 0xd600\n\t" "v_xor_b32 v224, 0x80, v232\n\t" "global_load_lds_dwordx4 v224, s[42:43]\n\t" "s_add_u32 m0, %20, 0xda00\n\t" "v_xor_b32 v224, 0x80, v233\n\t"
                     "global_load_lds_dwordx4 v224, s[42:43]\n\t" "s_add_u32 m0, %20, 0xde00\n\t" "v_xor_b32 v224, 0x80, v234\n\t" "global_load_lds_dwordx4 v224, s[42:43]\n\t" "s_add_u32 m0, %20, 0xe200\n\t"
                     "v_xor_b32 v224, 0x80, v235\n\t" "global_load_lds_dwordx4 v224, s[42:43]\n\t" "s_add_u32 m0, %20, 0xe600\n\t" "v_xor_b32 v224, 0x80, v236\n\t" "global_load_lds_dwordx4 v224, s[42:43]\n\t"
                     "s_add_u32 s42, %21, 0x18400\n\t" "v_xor_b32 v237, 0x0, v229\n\t" "v_add_u32 v237, s42, v237\n\t" "v_xor_b32 v238, 0x40, v229\n\t" "v_add_u32 v238, s42, v238\n\t" "v_xor_b32 v239, 0x80, v229\n\t"
                     "v_add_u32 v239, s42, v239\n\t" "v_xor_b32 v226, 0xc0, v229\n\t" "v_add_u32 v226, s42, v226\n\t" "ds_read_b64_tr_b16 a[188:189], v237\n\t" "ds_read_b64_tr_b16 a[190:191], v238 offset:2560\n\t"
                     "ds_read_b64_tr_b16 a[212:213], v239\n\t" "ds_read_b64_tr_b16 a[214:215], v226 offset:2560\n\t" "ds_read_b64_tr_b16 a[236:237], v237 offset:256\n\t"
                     "ds_read_b64_tr_b16 a[238:239], v238 offset:2816\n\t" "ds_read_b64_tr_b16 v[100:101], v239 offset:256\n\t" "ds_read_b64_tr_b16 v[102:103], v226 offset:2816\n\t"
                     "ds_read_b64_tr_b16 v[124:125], v237 offset:512\n\t" "ds_read_b64_tr_b16 v[126:127], v238 offset:3072\n\t" "ds_read_b64_tr_b16 v[148:149], v239 offset:512\n\t"
                     "ds_read_b64_tr_b16 v[150:151], v226 offset:3072\n\t" "ds_read_b64_tr_b16 v[172:173], v237 offset:768\n\t" "ds_read_b64_tr_b16 v[174:175], v238 offset:3328\n\t"
                     "ds_read_b64_tr_b16 v[196:197], v239 offset:768\n\t" "ds_read_b64_tr_b16 v[198:199], v226 offset:3328\n\t" "ds_read_b64_tr_b16 v[220:221], v237 offset:1024\n\t"
                     "ds_read_b64_tr_b16 v[222:223], v238 offset:3584\n\t" "ds_read_b64_tr_b16 v[84:85], v239 offset:1024\n\t" "ds_read_b64_tr_b16 v[86:87], v226 offset:3584\n\t" "s_waitcnt vmcnt(10) lgkmcnt(0)\n\t"
                     "s_barrier\n\t" "s_add_u32 s42, %21, 0x0\n\t" "v_xor_b32 v237, 0x0, v229\n\t" "v_add_u32 v237, s42, v237\n\t" "v_xor_b32 v238, 0x40, v229\n\t" "v_add_u32 v238, s42, v238\n\t"
                     "v_xor_b32 v239, 0x80, v229\n\t" "v_add_u32 v239, s42, v239\n\t" "v_xor_b32 v226, 0xc0, v229\n\t" "v_add_u32 v226, s42, v226\n\t" "ds_read_b64_tr_b16 a[192:193], v237\n\t"
                     "ds_read_b64_tr_b16 a[194:195], v238 offset:2560\n\t" "ds_read_b64_tr_b16 a[216:217], v239\n\t" "ds_read_b64_tr_b16 a[218:219], v226 offset:2560\n\t"
                     "ds_read_b64_tr_b16 a[240:241], v237 offset:256\n\t" "ds_read_b64_tr_b16 a[242:243], v238 offset:2816\n\t" "ds_read_b64_tr_b16 v[104:105], v239 offset:256\n\t"
                     "ds_read_b64_tr_b16 v[106:107], v226 offset:2816\n\t" "ds_read_b64_tr_b16 v[128:129], v237 offset:512\n\t" "ds_read_b64_tr_b16 v[130:131], v238 offset:3072\n\t"
                     "ds_read_b64_tr_b16 v[152:153], v239 offset:512\n\t" "ds_read_b64_tr_b16 v[154:155], v226 offset:3072\n\t" "ds_read_b64_tr_b16 v[176:177], v237 offset:768\n\t"
                     "ds_read_b64_tr_b16 v[178:179], v238 offset:3328\n\t" "ds_read_b64_tr_b16 v[200:201], v239 offset:768\n\t" "ds_read_b64_tr_b16 v[202:203], v226 offset:3328\n\t"
                     "ds_read_b64_tr_b16 v[64:65], v237 offset:1024\n\t" "ds_read_b64_tr_b16 v[66:67], v238 offset:3584\n\t" "ds_read_b64_tr_b16 v[88:89], v239 offset:1024\n\t"
                     "ds_read_b64_tr_b16 v[90:91], v226 offset:3584\n\t" "s_waitcnt vmcnt(0) lgkmcnt(0)\n\t" "s_barrier\n\t" "s_add_u32 s42, %21, 0xc200\n\t" "v_xor_b32 v237, 0x0, v229\n\t" "v_add_u32 v237, s42, v237\n\t"
                     "v_xor_b32 v238, 0x40, v229\n\t" "v_add_u32 v238, s42, v238\n\t" "v_xor_b32 v239, 0x80, v229\n\t" "v_add_u32 v239, s42, v239\n\t" "v_xor_b32 v226, 0xc0, v229\n\t" "v_add_u32 v226, s42, v226\n\t"
                     "ds_read_b64_tr_b16 a[196:197], v237\n\t" "ds_read_b64_tr_b16 a[198:199], v238 offset:2560\n\t" "ds_read_b64_tr_b16 a[220:221], v239\n\t" "ds_read_b64_tr_b16 a[222:223], v226 offset:2560\n\t"
                     "ds_read_b64_tr_b16 a[244:245], v237 offset:256\n\t" "ds_read_b64_tr_b16 a[246:247], v238 offset:2816\n\t" "ds_read_b64_tr_b16 v[108:109], v239 offset:256\n\t"
                     "ds_read_b64_tr_b16 v[110:111], v226 offset:2816\n\t" "ds_read_b64_tr_b16 v[132:133], v237 offset:512\n\t" "ds_read_b64_tr_b16 v[134:135], v238 offset:3072\n\t"
                     "ds_read_b64_tr_b16 v[156:157], v239 offset:512\n\t" "ds_read_b64_tr_b16 v[158:159], v226 offset:3072\n\t" "ds_read_b64_tr_b16 v[180:181], v237 offset:768\n\t"
                     "ds_read_b64_tr_b16 v[182:183], v238 offset:3328\n\t" "ds_read_b64_tr_b16 v[204:205], v239 offset:768\n\t" "ds_read_b64_tr_b16 v[206:207], v226 offset:3328\n\t"
                     "ds_read_b64_tr_b16 v[68:69], v237 offset:1024\n\t" "ds_read_b64_tr_b16 v[70:71], v238 offset:3584\n\t" "ds_read_b64_tr_b16 v[92:93], v239 offset:1024\n\t"
                     "ds_read_b64_tr_b16 v[94:95], v226 offset:3584\n\t" "s_waitcnt lgkmcnt(0)\n\t" "ds_write_b128 v230, v[0:3]\n\t" "ds_write_b128 v230, v[4:7] offset:1024\n\t"
                     "ds_write_b128 v230, v[8:11] offset:2048\n\t" "ds_write_b128 v230, v[12:15] offset:3072\n\t" "ds_write_b128 v230, v[16:19] offset:4096\n\t" "ds_write_b128 v230, v[20:23] offset:5120\n\t"
                     "ds_write_b128 v230, v[24:27] offset:6144\n\t" "ds_write_b128 v230, v[28:31] offset:7168\n\t" "ds_write_b128 v230, v[32:35] offset:8192\n\t" "ds_write_b128 v230, v[36:39] offset:9216\n\t"
                     "ds_write_b128 v230, v[40:43] offset:10240\n\t" "ds_write_b128 v230, v[44:47] offset:11264\n\t" "ds_write_b128 v230, v[48:51] offset:12288\n\t" "ds_write_b128 v230, v[52:55] offset:13312\n\t"
                     "ds_write_b128 v230, v[56:59] offset:14336\n\t" "ds_write_b128 v230, v[60:63] offset:15360\n\t" "ds_write_b128 v230, v[64:67] offset:16384\n\t" "ds_write_b128 v230, v[68:71] offset:17408\n\t"
                     "ds_write_b128 v230, v[72:75] offset:18432\n\t" "ds_write_b128 v230, v[76:79] offset:19456\n\t" "ds_write_b128 v230, v[80:83] offset:20480\n\t" "ds_write_b128 v230, v[84:87] offset:21504\n\t"
                     "ds_write_b128 v230, v[88:91] offset:22528\n\t" "ds_write_b128 v230, v[92:95] offset:23552\n\t"
                     : "=&{a[0:31]}"(wa[0]), "=&{a[32:63]}"(wa[1]), "=&{a[64:95]}"(wa[2]), "=&{a[96:127]}"(wa[3]), "=&{a[128:159]}"(wa[4]), "=&{a[160:191]}"(wa[5]), "=&{a[192:223]}"(wa[6]), "=&{a[224:255]}"(wa[7]), "=&{v[96:127]}"(wv[0]), "=&{v[128:159]}"(wv[1]), "=&{v[160:191]}"(wv[2]), "=&{v[192:223]}"(wv[3]), "=&v"(y.r), "=&v"(y.g), "=&v"(y.e), "+{v[224:239]}"(pa)
                     : "{s[44:45]}"(p.wv + h * DH * 2 + (size_t)(16 * w) * (HD * 2)), "{s[46:47]}"(p.wo + (size_t)(h * DH + 8 * w) * 1280),
                       "s"(lds + 2 * w * GS), "s"(p.ws + OFF_LAT), "s"(lds + w * 10240), "s"(lds)
                     : "v0", "v1", "v2", "v3", "v4", "v5", "v6", "v7", "v8", "v9", "v10", "v11", "v12", "v13", "v14", "v15", "v16", "v17", "v18", "v19", "v20", "v21", "v22", "v23", "v24", "v25", "v26", "v27", "v28", "v29", "v30", "v31", "v32", "v33", "v34", "v35", "v36", "v37", "v38", "v39", "v40", "v41", "v42", "v43", "v44", "v45", "v46", "v47", "v48", "v49", "v50", "v51", "v52", "v53", "v54", "v55", "v56", "v57", "v58", "v59", "v60", "v61", "v62", "v63", "v64", "v65", "v66", "v67", "v68", "v69", "v70", "v71", "v72", "v73", "v74", "v75", "v76", "v77", "v78", "v79", "v80", "v81", "v82", "v83", "v84", "v85", "v86", "v87", "v88", "v89", "v90", "v91", "v92", "v93", "v94", "v95", "s42", "s43", "scc", "memory");
        sd = sync_end(y, p.ws, phi, w == 0);
        barrier();
    }
    const int lane = lanex(), tid = 64 * w + lane;
    const int n = lane & 15, g = lane >> 4, qq = (lane >> 2) & 3, pp = lane & 3;
    char* const fl = p.ws + OFF_ST;
    const u32 iw = lds + 65536 + ((lane * 8) ^ (lane & 32));
    const u32 qr0 = lds + 65536 + (g >> 1) * 512 + pp * 128 + 64 * (g & 1) + 8 * qq + 32 * (pp >> 1);
    const u32 uo = (u32)((2 * w + (n >> 3)) * 4096 + h * 128 + (n & 7) * 16);
    u32x16 ad = u32x16{(u32)lane * 16, (lane & 2) ? 0xEEEEu : 0x4444u, lds + (u32)((w & 1) * 24576 + (w >> 1) * 99328 + lane * 16), iw + ((3 * w + (w & 1)) >> 1) * 512,
                       iw + (1 + 3 * (w >> 1)) * 512 + 4 * (w & 1), qr0, qr0 ^ 32, uo + (2 + g) * 32768, uo + (g & 1) * 32768 + (g >> 1) * 8, uo, uo, uo, uo, uo, uo, uo};
    asm volatile("" : "+{v[224:239]}"(ad));
    const char* memp = p.ws + mslot(sd, 0);
    int budget = 30000;
    Clock clk; clk.init();
    for (int s = 0; s < NSEG; ++s) {
        int tries = 0;
        u64 bad, tp8, en = ~0ull;
        while (true) {
            const u32 tgt = clk.sched(phi, 0, cfg.G, tries ? 2 : 0);
            char* up = p.ws + uslot(sd, s);
            asm volatile("ds_read_b128 v[64:67], v226\n\t" "ds_read_b128 v[68:71], v226 offset:1024\n\t" "ds_read_b128 v[72:75], v226 offset:2048\n\t" "ds_read_b128 v[76:79], v226 offset:3072\n\t"
                         "ds_read_b128 v[80:83], v226 offset:4096\n\t" "ds_read_b128 v[84:87], v226 offset:5120\n\t" "ds_read_b128 v[88:91], v226 offset:6144\n\t" "ds_read_b128 v[92:95], v226 offset:7168\n\t"
                         "v_mov_b64 v[40:41], 0\n\t" "v_mov_b64 v[42:43], 0\n\t" "v_mov_b64 v[44:45], 0\n\t" "v_mov_b64 v[46:47], 0\n\t" "v_mov_b64 v[48:49], 0\n\t" "v_mov_b64 v[50:51], 0\n\t" ".Lsp_u:\n\t" "s_memtime vcc\n\t"
                         "s_waitcnt lgkmcnt(0)\n\t" "s_sub_i32 s42, %3, vcc_lo\n\t" "s_cmp_gt_i32 s42, 80\n\t" "s_cbranch_scc1 .Lsp_u\n\t" "s_max_i32 s42, s42, 0\n\t" "s_sub_i32 s42, 92, s42\n\t" "s_and_b32 s42, s42, -4\n\t"
                         "s_getpc_b64 vcc\n\t" "s_add_u32 vcc_lo, vcc_lo, s42\n\t" "s_addc_u32 vcc_hi, vcc_hi, 0\n\t" "s_setpc_b64 vcc\n\t" "s_nop 0\n\t" "s_nop 0\n\t" "s_nop 0\n\t" "s_nop 0\n\t" "s_nop 0\n\t" "s_nop 0\n\t"
                         "s_nop 0\n\t" "s_nop 0\n\t" "s_nop 0\n\t" "s_nop 0\n\t" "s_nop 0\n\t" "s_nop 0\n\t" "s_nop 0\n\t" "s_nop 0\n\t" "s_nop 0\n\t" "s_nop 0\n\t" "s_nop 0\n\t" "s_nop 0\n\t" "s_nop 0\n\t" "s_nop 0\n\t"
                         "global_load_dwordx4 v[0:3], v224, %4 offset:-4096 sc1\n\t" "s_nop 10\n\t" "global_load_dwordx4 v[4:7], v224, %4 offset:-3072 sc1\n\t" "s_nop 10\n\t"
                         "global_load_dwordx4 v[8:11], v224, %4 offset:-2048 sc1\n\t" "s_nop 10\n\t" "global_load_dwordx4 v[12:15], v224, %4 offset:-1024 sc1\n\t" "s_nop 10\n\t" "global_load_dwordx4 v[16:19], v224, %4 sc1\n\t"
                         "s_nop 10\n\t" "global_load_dwordx4 v[20:23], v224, %4 offset:1024 sc1\n\t" "s_nop 10\n\t" "global_load_dwordx4 v[24:27], v224, %4 offset:2048 sc1\n\t" "s_nop 10\n\t"
                         "global_load_dwordx4 v[28:31], v224, %4 offset:3072 sc1\n\t" "s_nop 10\n\t" "global_load_dwordx4 v[32:35], v224, %5 offset:-4096 sc1\n\t" "s_nop 10\n\t"
                         "global_load_dwordx4 v[36:39], v224, %5 offset:-3072 sc1\n\t" "s_waitcnt vmcnt(9)\n\t" "v_smfmac_f32_16x16x64_bf16 v[40:43], v[0:3], a[0:7], v225\n\t"
                         "v_smfmac_f32_16x16x64_bf16 v[44:47], v[0:3], v[64:71], v225\n\t" "ds_read_b128 v[64:67], v226 offset:8192\n\t" "v_smfmac_f32_16x16x64_bf16 v[48:51], v[0:3], a[8:15], v225\n\t"
                         "ds_read_b128 v[68:71], v226 offset:9216\n\t" "v_cmp_eq_u32 vcc, -1, v0\n\t" "s_waitcnt vmcnt(8)\n\t" "v_smfmac_f32_16x16x64_bf16 v[40:43], v[4:7], a[16:23], v225\n\t" "v_mov_b64 v[0:1], 0\n\t"
                         "v_smfmac_f32_16x16x64_bf16 v[44:47], v[4:7], v[72:79], v225\n\t" "s_and_b64 vcc, vcc, %6\n\t" "s_cbranch_scc1 .Lsk_u\n\t" "ds_read_b128 v[72:75], v226 offset:10240\n\t" "v_mov_b64 v[2:3], 0\n\t"
                         "v_smfmac_f32_16x16x64_bf16 v[48:51], v[4:7], a[24:31], v225\n\t" "ds_read_b128 v[76:79], v226 offset:11264\n\t" "s_waitcnt vmcnt(7)\n\t"
                         "v_smfmac_f32_16x16x64_bf16 v[40:43], v[8:11], a[32:39], v225\n\t" "v_mov_b64 v[4:5], 0\n\t" "v_smfmac_f32_16x16x64_bf16 v[44:47], v[8:11], v[80:87], v225\n\t"
                         "ds_read_b128 v[80:83], v226 offset:12288\n\t" "v_mov_b64 v[6:7], 0\n\t" "v_smfmac_f32_16x16x64_bf16 v[48:51], v[8:11], a[40:47], v225\n\t" "ds_read_b128 v[84:87], v226 offset:13312\n\t"
                         "s_waitcnt vmcnt(6)\n\t" "v_smfmac_f32_16x16x64_bf16 v[40:43], v[12:15], a[48:55], v225\n\t" "v_mov_b64 v[8:9], 0\n\t" "v_smfmac_f32_16x16x64_bf16 v[44:47], v[12:15], v[88:95], v225\n\t"
                         "ds_read_b128 v[88:91], v226 offset:14336\n\t" "v_mov_b64 v[10:11], 0\n\t" "v_smfmac_f32_16x16x64_bf16 v[48:51], v[12:15], a[56:63], v225\n\t" "ds_read_b128 v[92:95], v226 offset:15360\n\t"
                         "s_waitcnt vmcnt(5)\n\t" "v_smfmac_f32_16x16x64_bf16 v[40:43], v[16:19], a[64:71], v225\n\t" "v_mov_b64 v[12:13], 0\n\t" "s_waitcnt lgkmcnt(6)\n\t"
                         "v_smfmac_f32_16x16x64_bf16 v[44:47], v[16:19], v[64:71], v225\n\t" "ds_read_b128 v[64:67], v226 offset:16384\n\t" "v_mov_b64 v[14:15], 0\n\t"
                         "v_smfmac_f32_16x16x64_bf16 v[48:51], v[16:19], a[72:79], v225\n\t" "ds_read_b128 v[68:71], v226 offset:17408\n\t" "s_waitcnt vmcnt(4)\n\t"
                         "v_smfmac_f32_16x16x64_bf16 v[40:43], v[20:23], a[80:87], v225\n\t" "v_mov_b64 v[16:17], 0\n\t" "s_waitcnt lgkmcnt(6)\n\t" "v_smfmac_f32_16x16x64_bf16 v[44:47], v[20:23], v[72:79], v225\n\t"
                         "ds_read_b128 v[72:75], v226 offset:18432\n\t" "v_mov_b64 v[18:19], 0\n\t" "v_smfmac_f32_16x16x64_bf16 v[48:51], v[20:23], a[88:95], v225\n\t" "ds_read_b128 v[76:79], v226 offset:19456\n\t"
                         "s_waitcnt vmcnt(3)\n\t" "v_smfmac_f32_16x16x64_bf16 v[40:43], v[24:27], a[96:103], v225\n\t" "v_mov_b64 v[20:21], 0\n\t" "s_waitcnt lgkmcnt(6)\n\t"
                         "v_smfmac_f32_16x16x64_bf16 v[44:47], v[24:27], v[80:87], v225\n\t" "ds_read_b128 v[80:83], v226 offset:20480\n\t" "v_mov_b64 v[22:23], 0\n\t"
                         "v_smfmac_f32_16x16x64_bf16 v[48:51], v[24:27], a[104:111], v225\n\t" "ds_read_b128 v[84:87], v226 offset:21504\n\t" "s_waitcnt vmcnt(2)\n\t"
                         "v_smfmac_f32_16x16x64_bf16 v[40:43], v[28:31], a[112:119], v225\n\t" "v_mov_b64 v[24:25], 0\n\t" "s_waitcnt lgkmcnt(6)\n\t" "v_smfmac_f32_16x16x64_bf16 v[44:47], v[28:31], v[88:95], v225\n\t"
                         "ds_read_b128 v[88:91], v226 offset:22528\n\t" "v_mov_b64 v[26:27], 0\n\t" "v_smfmac_f32_16x16x64_bf16 v[48:51], v[28:31], a[120:127], v225\n\t" "ds_read_b128 v[92:95], v226 offset:23552\n\t"
                         "s_waitcnt vmcnt(1)\n\t" "v_smfmac_f32_16x16x64_bf16 v[40:43], v[32:35], a[128:135], v225\n\t" "v_mov_b64 v[28:29], 0\n\t" "v_smfmac_f32_16x16x64_bf16 v[44:47], v[32:35], a[136:143], v225\n\t"
                         "v_mov_b64 v[30:31], 0\n\t" "v_smfmac_f32_16x16x64_bf16 v[48:51], v[32:35], a[144:151], v225\n\t" "s_waitcnt vmcnt(0)\n\t" "v_smfmac_f32_16x16x64_bf16 v[40:43], v[36:39], a[152:159], v225\n\t"
                         "v_mov_b64 v[32:33], 0\n\t" "v_smfmac_f32_16x16x64_bf16 v[44:47], v[36:39], a[160:167], v225\n\t" "v_mov_b64 v[34:35], 0\n\t" "v_smfmac_f32_16x16x64_bf16 v[48:51], v[36:39], a[168:175], v225\n\t"
                         "v_add_f32 v52, v40, v42\n\t" "v_add_f32 v53, v41, v43\n\t" "v_cmp_u_f32 vcc, v52, v53\n\t" "v_add_f32 v54, v44, v46\n\t" "v_add_f32 v55, v45, v47\n\t" "v_cvt_pk_bf16_f32 v58, v52, v53\n\t"
                         "v_pk_add_f32 v[56:57], v[48:49], v[50:51]\n\t" "v_cvt_pk_bf16_f32 v59, v54, v55\n\t" "v_cvt_pk_bf16_f32 v60, v56, v57\n\t" "ds_write_b64 v227, v[58:59]\n\t" "ds_write_b32 v228, v60\n\t"
                         "v_mov_b64 v[36:37], 0\n\t" "v_mov_b64 v[38:39], 0\n\t" "s_and_b64 vcc, vcc, %6\n\t" "v_mov_b32 v233, 0\n\t" "s_cbranch_scc1 .Lsk_u\n\t" "v_mov_b32 v233, 0\n\t" "v_mov_b32 v233, 0\n\t"
                         "v_mov_b32 v233, 0\n\t" "s_waitcnt lgkmcnt(0)\n\t" "s_barrier\n\t" "ds_read_b64_tr_b16 v[40:41], v229\n\t" "ds_read_b64_tr_b16 v[42:43], v230\n\t" "v_mov_b32 v233, 0\n\t"
                         "ds_read_b64_tr_b16 v[44:45], v229 offset:1024\n\t" "v_mov_b32 v233, 0\n\t" "ds_read_b64_tr_b16 v[46:47], v230 offset:1024\n\t" "v_mov_b32 v233, 0\n\t"
                         "ds_read_b64_tr_b16 v[48:49], v229 offset:2048\n\t" "v_mov_b32 v233, 0\n\t" "ds_read_b64_tr_b16 v[50:51], v230 offset:2048\n\t" "v_mov_b32 v233, 0\n\t" "s_waitcnt lgkmcnt(4)\n\t"
                         "v_smfmac_f32_16x16x64_bf16 v[0:3], v[40:43], a[176:183], v225\n\t" "v_smfmac_f32_16x16x64_bf16 v[4:7], v[40:43], a[200:207], v225\n\t" "s_waitcnt lgkmcnt(2)\n\t"
                         "v_smfmac_f32_16x16x64_bf16 v[0:3], v[44:47], a[184:191], v225\n\t" "v_smfmac_f32_16x16x64_bf16 v[4:7], v[44:47], a[208:215], v225\n\t" "s_waitcnt lgkmcnt(0)\n\t"
                         "v_smfmac_f32_16x16x64_bf16 v[0:3], v[48:51], a[192:199], v225\n\t" "v_smfmac_f32_16x16x64_bf16 v[4:7], v[48:51], a[216:223], v225\n\t"
                         "v_smfmac_f32_16x16x64_bf16 v[8:11], v[40:43], a[224:231], v225\n\t" "v_add_f32 v52, v0, v2\n\t" "v_add_f32 v53, v1, v3\n\t" "v_cvt_pk_bf16_f32 v54, v53, v52\n\t"
                         "v_smfmac_f32_16x16x64_bf16 v[8:11], v[44:47], a[232:239], v225\n\t" "v_add_f32 v52, v4, v6\n\t" "v_add_f32 v53, v5, v7\n\t" "v_cvt_pk_bf16_f32 v55, v53, v52\n\t" "s_nop 1\n\t"
                         "v_permlane16_swap_b32 v54, v55\n\t" "v_smfmac_f32_16x16x64_bf16 v[8:11], v[48:51], a[240:247], v225\n\t" "global_store_dwordx2 v232, v[54:55], %2 sc1\n\t" "s_add_u32 s40, s40, 0x10000\n\t"
                         "s_addc_u32 s41, s41, 0\n\t" "v_smfmac_f32_16x16x64_bf16 v[12:15], v[40:43], a[248:255], v225\n\t" "v_smfmac_f32_16x16x64_bf16 v[12:15], v[44:47], v[96:103], v225\n\t" "v_add_f32 v52, v8, v10\n\t"
                         "v_smfmac_f32_16x16x64_bf16 v[12:15], v[48:51], v[104:111], v225\n\t" "v_add_f32 v53, v9, v11\n\t" "v_smfmac_f32_16x16x64_bf16 v[16:19], v[40:43], v[112:119], v225\n\t"
                         "v_cvt_pk_bf16_f32 v56, v53, v52\n\t" "v_smfmac_f32_16x16x64_bf16 v[16:19], v[44:47], v[120:127], v225\n\t" "v_add_f32 v52, v12, v14\n\t" "v_add_f32 v53, v13, v15\n\t"
                         "v_cvt_pk_bf16_f32 v57, v53, v52\n\t" "s_nop 1\n\t" "v_permlane16_swap_b32 v56, v57\n\t" "v_smfmac_f32_16x16x64_bf16 v[16:19], v[48:51], v[128:135], v225\n\t"
                         "global_store_dwordx2 v232, v[56:57], %2 sc1\n\t" "s_add_u32 s40, s40, 0x10000\n\t" "s_addc_u32 s41, s41, 0\n\t" "v_smfmac_f32_16x16x64_bf16 v[20:23], v[40:43], v[136:143], v225\n\t"
                         "v_smfmac_f32_16x16x64_bf16 v[20:23], v[44:47], v[144:151], v225\n\t" "v_add_f32 v52, v16, v18\n\t" "v_smfmac_f32_16x16x64_bf16 v[20:23], v[48:51], v[152:159], v225\n\t" "v_add_f32 v53, v17, v19\n\t"
                         "v_smfmac_f32_16x16x64_bf16 v[24:27], v[40:43], v[160:167], v225\n\t" "v_cvt_pk_bf16_f32 v58, v53, v52\n\t" "v_smfmac_f32_16x16x64_bf16 v[24:27], v[44:47], v[168:175], v225\n\t"
                         "v_add_f32 v52, v20, v22\n\t" "v_add_f32 v53, v21, v23\n\t" "v_smfmac_f32_16x16x64_bf16 v[24:27], v[48:51], v[176:183], v225\n\t" "v_cvt_pk_bf16_f32 v59, v53, v52\n\t"
                         "v_smfmac_f32_16x16x64_bf16 v[28:31], v[40:43], v[184:191], v225\n\t" "s_nop 0\n\t" "v_permlane16_swap_b32 v58, v59\n\t" "v_smfmac_f32_16x16x64_bf16 v[28:31], v[44:47], v[192:199], v225\n\t"
                         "global_store_dwordx2 v232, v[58:59], %2 sc1\n\t" "v_add_f32 v52, v24, v26\n\t" "v_smfmac_f32_16x16x64_bf16 v[28:31], v[48:51], v[200:207], v225\n\t" "v_add_f32 v53, v25, v27\n\t"
                         "v_smfmac_f32_16x16x64_bf16 v[32:35], v[40:43], v[208:215], v225\n\t" "v_cvt_pk_bf16_f32 v60, v53, v52\n\t" "v_smfmac_f32_16x16x64_bf16 v[32:35], v[44:47], v[216:223], v225\n\t"
                         "v_add_f32 v52, v28, v30\n\t" "v_smfmac_f32_16x16x64_bf16 v[32:35], v[48:51], v[64:71], v225\n\t" "v_add_f32 v53, v29, v31\n\t" "v_smfmac_f32_16x16x64_bf16 v[36:39], v[40:43], v[72:79], v225\n\t"
                         "v_cvt_pk_bf16_f32 v61, v53, v52\n\t" "v_smfmac_f32_16x16x64_bf16 v[36:39], v[44:47], v[80:87], v225\n\t" "v_add_f32 v52, v32, v34\n\t"
                         "v_smfmac_f32_16x16x64_bf16 v[36:39], v[48:51], v[88:95], v225\n\t" "v_add_f32 v53, v33, v35\n\t" "v_cvt_pk_bf16_f32 v62, v53, v52\n\t" "v_mov_b32 v233, 0\n\t" "v_mov_b32 v233, 0\n\t"
                         "v_mov_b32 v233, 0\n\t" "v_mov_b32 v233, 0\n\t" "v_mov_b32 v233, 0\n\t" "v_pk_add_f32 v[52:53], v[36:37], v[38:39]\n\t" "v_cvt_pk_bf16_f32 v63, v53, v52\n\t" "v_permlane32_swap_b32 v60, v62\n\t"
                         "s_nop 0\n\t" "v_permlane32_swap_b32 v61, v63\n\t" "s_nop 1\n\t" "v_permlane16_swap_b32 v60, v61\n\t" "v_permlane16_swap_b32 v62, v63\n\t" "global_store_dwordx4 v231, v[60:63], %2 sc1\n\t"
                         ".Lsk_u:\n\t" "s_memrealtime %1\n\t" "s_mov_b64 %0, vcc\n\t" "s_waitcnt lgkmcnt(0)\n\t"
                         : "=&s"(bad), "=&s"(tp8), "+{s[40:41]}"(up)
                         : "s"(uni((int)tgt)), "s"(memp), "s"(memp + 16384), "s"(en), "{v[224:239]}"(ad),
                           "{a[0:31]}"(wa[0]), "{a[32:63]}"(wa[1]), "{a[64:95]}"(wa[2]), "{a[96:127]}"(wa[3]), "{a[128:159]}"(wa[4]), "{a[160:191]}"(wa[5]), "{a[192:223]}"(wa[6]), "{a[224:255]}"(wa[7]), "{v[96:127]}"(wv[0]), "{v[128:159]}"(wv[1]), "{v[160:191]}"(wv[2]), "{v[192:223]}"(wv[3])
                         : "v0", "v1", "v2", "v3", "v4", "v5", "v6", "v7", "v8", "v9", "v10", "v11", "v12", "v13", "v14", "v15", "v16", "v17", "v18", "v19", "v20", "v21", "v22", "v23", "v24", "v25", "v26", "v27", "v28", "v29", "v30", "v31", "v32", "v33", "v34", "v35", "v36", "v37", "v38", "v39", "v40", "v41", "v42", "v43", "v44", "v45", "v46", "v47", "v48", "v49", "v50", "v51", "v52", "v53", "v54", "v55", "v56", "v57", "v58", "v59", "v60", "v61", "v62", "v63", "v64", "v65", "v66", "v67", "v68", "v69", "v70", "v71", "v72", "v73", "v74", "v75", "v76", "v77", "v78", "v79", "v80", "v81", "v82", "v83", "v84", "v85", "v86", "v87", "v88", "v89", "v90", "v91", "v92", "v93", "v94", "v95", "vcc", "scc", "s42", "memory");
            if (__builtin_expect(bad == 0, 1)) break;
            waitvm<0>();
            if (--budget < 0 || ((++tries & 15) == 0 && !unwritten(memp, ad[0]))) en = 0;
            phi += (u32)cfg.G;
        }
        clk.calib();
        const u32 tt = sched_ld0(s);
        if (s >= NSEG - 2) waitvm<0>();
        barrier();
        if (s >= NSEG - 2 && w == 3) flag_add(fl, F_PROG + 8 * s + (h & 7), 1);
        if (s >= NSEG - 2 && w == 2) flag_add(fl, F_UD + 2 * (s & 1) + (h & 1), 1);
        follow(phi, (int)(tt >> 16) + 4 + cfg.T3 - VISS, (u32)tp8, cfg.G);
        phi += tt & 0xffffu;
        memp = p.ws + mslot(sd, s + 1);
    }
    asm volatile(".Lkm_lu:\n\ts_waitcnt vmcnt(0)" : : : "memory");
}

#define VALL "{v[0:3]}"(o), "{v[16:47]}"(rv[0]), "{v[48:79]}"(rv[1]), "{v[80:111]}"(rv[2]), "{v[112:143]}"(rv[3]), "{v[144:175]}"(rv[4]), "{v[176:207]}"(rv[5]), "{v[208:239]}"(rv[6]), "{a[0:31]}"(ra[0]), "{a[32:63]}"(ra[1]), "{a[64:95]}"(ra[2]), "{a[96:127]}"(ra[3])
DI void value_role(const Ptrs& p, const ChainCfg cfg, int vb, int sd, u32 phi, u32 lds, int w) {
    const int lane = lanex(), tid = 64 * w + lane, n = lane & 15, g = lane >> 4;
    const int v = 2 * (vb & 3) + ((vb >> 2) & 1) + (vb & ~7);
    const u32 moff = lane * 16;
    char* const fl = p.ws + OFF_ST;
    char* const ub = p.ws + (size_t)v * 8192;
    const int xh = (vb >> 2) & 1;
    if (w == 3) {
        const u32x4 ff = u32x4{~0u, ~0u, ~0u, ~0u};
#pragma nounroll
        for (int s = 0; s < NSEG - 2; ++s) {
            dly(cfg.cR3 >> 8 & 255);
            char* const mk = ub + uslot(sd, s + 2);
#pragma unroll
            for (int i = 0; i < 8; ++i) { gst16(mk + i * 1024, moff, ff); gap<HQ>(-1); }
            if (vb < 32 && s) {
                gst16(p.ws + (size_t)(s + 2) * HO_STRIDE + vb * 2048, moff, ff);
                gst16(p.ws + (size_t)(s + 2) * HO_STRIDE + vb * 2048 + 1024, moff, ff);
            }
            waitvm<0>();
            barrier();
        }
        return;
    }
    if (w) {
        const u32 dof = (u32)((n >> 3) * 16384 + g * 128 + (n & 7) * 16);
        const char* dp = p.ws + OFF_D + (size_t)v * 32768 - 4096 + w * 8192;
        flag_wait(fl, F_VD + (vb & 3) + (v < 20 ? 0 : 4), 32);
#pragma nounroll
        for (int s = 0; s < NSEG - 1; ++s) {
            u32x4 df[16];
            u32 dfl;
            dly(cfg.cR3 & 255);
            sfor<0, 16>([&](auto I) { gld16c<I.v * 512 - 4096>(df[I.v], dp, dof); gap<HP>(1 - s); });
            dp += D_STRIDE;
            flag_ldm(dfl, fl, F_VD + pblk(s + 1) * 8 + (vb & RM), pblk(s + 1) != pblk(s));
            const u32 la = lds + moff + (s & 1) * 32768 + (w - 1) * 16384;
            sfor<0, 8>([&](auto I) {
                constexpr int i = I.v;
                waitv<15 - 2 * i>(df[2 * i]); waitv<15 - 2 * i>(df[2 * i + 1]);
                const u32x4 x = df[2 * i], y = df[2 * i + 1];
                const u32 lb = i & 2 ? la - xh * 4096 : la + xh * 4096;
                *LDSP(u32x4, lb + 2048 * i) = u32x4{x[0], y[0], x[2], y[2]};
                *LDSP(u32x4, lb + 2048 * i + 1024) = u32x4{x[1], y[1], x[3], y[3]};
                PIN();
            });
            waitvm<0>();
            asm volatile("" : "+v"(dfl));
            barrier();
            if (uni((int)dfl) < 31) flag_wait(fl, F_VD + pblk(s + 1) * 8 + (vb & RM), 32);
        }
        return;
    }
    u32x32 rv[7], ra[4];
    u32x4 o;
    asm volatile("v_accvgpr_write_b32 a64, 0\n\t" "v_accvgpr_write_b32 a65, 0\n\t" "v_accvgpr_write_b32 a66, 0\n\t" "v_accvgpr_write_b32 a67, 0\n\t" "v_accvgpr_write_b32 a68, 0\n\t" "v_accvgpr_write_b32 a69, 0\n\t"
                 "v_accvgpr_write_b32 a70, 0\n\t" "v_accvgpr_write_b32 a71, 0\n\t" "v_accvgpr_write_b32 a72, 0\n\t" "v_accvgpr_write_b32 a73, 0\n\t" "v_accvgpr_write_b32 a74, 0\n\t" "v_accvgpr_write_b32 a75, 0\n\t"
                 "v_accvgpr_write_b32 a76, 0\n\t" "v_accvgpr_write_b32 a77, 0\n\t" "v_accvgpr_write_b32 a78, 0\n\t" "v_accvgpr_write_b32 a79, 0\n\t" "v_mov_b64 v[112:113], -1\n\t" "v_mov_b64 v[120:121], -1\n\t"
                 "v_mov_b64 v[128:129], -1\n\t" "v_mov_b64 v[136:137], -1\n\t" "v_mov_b64 v[144:145], -1\n\t" "v_mov_b64 v[152:153], -1\n\t" "v_mov_b64 v[160:161], -1\n\t" "v_mov_b64 v[168:169], -1\n\t"
                 "v_mov_b64 v[176:177], -1\n\t" "v_mov_b64 v[184:185], -1\n\t" "v_mov_b64 v[192:193], -1\n\t" "v_mov_b64 v[200:201], -1\n\t" "v_mov_b64 v[208:209], -1\n\t" "v_mov_b64 v[216:217], -1\n\t"
                 "v_mov_b64 v[224:225], -1\n\t" "v_mov_b64 v[232:233], -1\n\t"
                 : "={v[0:3]}"(o), "={v[16:47]}"(rv[0]), "={v[48:79]}"(rv[1]), "={v[80:111]}"(rv[2]), "={v[112:143]}"(rv[3]), "={v[144:175]}"(rv[4]), "={v[176:207]}"(rv[5]), "={v[208:239]}"(rv[6]), "={a[0:31]}"(ra[0]), "={a[32:63]}"(ra[1]), "={a[64:95]}"(ra[2]), "={a[96:127]}"(ra[3]));
    const u32 ho = (u32)(min(lane, 39) * 256 + (lane >> 5) * 8192);
    const u32 pof = (u32)((n & 4) * 128 + (n >> 3) * 256 + g * 64 + (n & 3) * 16 + 2048);
    const u32 uof = (u32)((n >> 3) * 4096 + g * 128 + (n & 7) * 16 + 512);
    u32x8 ad = u32x8{uof + xh * 512, pof + xh * 2048, (u32)(((g >> 1) ^ xh) * 256 + (n >> 2) * 64 + (n & 1) * 16), moff,
                     (lane & 2) ? 0xEEEEu : 0x4444u, (lane & 4) ? 0xEEEEu : 0x4444u, (u32)(v * 256 + (v >> 5) * 8192 + g * 64 + (n >> 3) * 32 + (n & 1) * 16 + (n & 6) * 2),
                     (lane & 1) ? 0x07060302u : 0x01000504u};
    u32 pob = pof - xh * 2048, uob = uof - xh * 512, pmo = moff ^ xh * 512;
    asm volatile("" : "+{v[4:11]}"(ad), "+v"(pob), "+v"(uob), "+v"(pmo));
    auto zero = [&]() {
        asm volatile("v_mov_b64 v[0:1], 0\n\t" "v_mov_b64 v[2:3], 0\n\t" "v_mov_b64 v[48:49], 0\n\t" "v_mov_b64 v[50:51], 0\n\t" "v_mov_b64 v[52:53], 0\n\t" "v_mov_b64 v[54:55], 0\n\t" "v_mov_b64 v[56:57], 0\n\t"
                     "v_mov_b64 v[58:59], 0\n\t" "v_mov_b64 v[60:61], 0\n\t" "v_mov_b64 v[62:63], 0\n\t" "v_mov_b64 v[64:65], 0\n\t" "v_mov_b64 v[66:67], 0\n\t" "v_mov_b64 v[68:69], 0\n\t" "v_mov_b64 v[70:71], 0\n\t"
                     "v_mov_b64 v[72:73], 0\n\t" "v_mov_b64 v[74:75], 0\n\t" "v_mov_b64 v[76:77], 0\n\t" "v_mov_b64 v[78:79], 0\n\t" "v_mov_b64 v[80:81], 0\n\t" "v_mov_b64 v[82:83], 0\n\t" "v_mov_b64 v[84:85], 0\n\t"
                     "v_mov_b64 v[86:87], 0\n\t" "v_mov_b64 v[88:89], 0\n\t" "v_mov_b64 v[90:91], 0\n\t" "v_mov_b64 v[92:93], 0\n\t" "v_mov_b64 v[94:95], 0\n\t" "v_mov_b64 v[96:97], 0\n\t" "v_mov_b64 v[98:99], 0\n\t"
                     "v_mov_b64 v[100:101], 0\n\t" "v_mov_b64 v[102:103], 0\n\t" "v_mov_b64 v[104:105], 0\n\t" "v_mov_b64 v[106:107], 0\n\t" "v_mov_b64 v[108:109], 0\n\t" "v_mov_b64 v[110:111], 0\n\t"
                     : : VALL : "memory");
    };
    const char* memp = p.ws + OFF_XF + 4096;
    char* up = ub + OFF_XF;
    int budget = 30000;
    Clock clk; clk.init();
    u32 tt = 0;
    for (int s = -1; s < NSEG - 1; ++s) {
        const char* const mnx = p.ws + mslot(sd, s + 1);
        bool all = false;
        int tries = 0;
        u64 tp8;
        while (true) {
            const u32 tgt = clk.sched(phi, (int)(tt >> 16), cfg.G, tries ? 2 : s > 0) - (s < 0 ? 1u << 20 : 0u);
            u64 ok;
            asm volatile("s_cmp_lg_u32 %11, 0\n\t" "s_cbranch_scc0 .Lsp_v0\n\t" ".Lsp_v3:\n\t" "s_memtime vcc\n\t" "s_waitcnt lgkmcnt(0)\n\t" "s_sub_i32 s42, %10, vcc_lo\n\t" "s_cmp_gt_i32 s42, 80\n\t"
                         "s_cbranch_scc1 .Lsp_v3\n\t" "s_max_i32 s42, s42, 0\n\t" "s_sub_i32 s42, 92, s42\n\t" "s_and_b32 s42, s42, -4\n\t" "s_getpc_b64 vcc\n\t" "s_add_u32 vcc_lo, vcc_lo, s42\n\t"
                         "s_addc_u32 vcc_hi, vcc_hi, 0\n\t" "s_setpc_b64 vcc\n\t" "s_nop 0\n\t" "s_nop 0\n\t" "s_nop 0\n\t" "s_nop 0\n\t" "s_nop 0\n\t" "s_nop 0\n\t" "s_nop 0\n\t" "s_nop 0\n\t" "s_nop 0\n\t" "s_nop 0\n\t"
                         "s_nop 0\n\t" "s_nop 0\n\t" "s_nop 0\n\t" "s_nop 0\n\t" "s_nop 0\n\t" "s_nop 0\n\t" "s_nop 0\n\t" "s_nop 0\n\t" "s_nop 0\n\t" "s_nop 0\n\t"
                         "global_load_dwordx4 a[0:3], v5, %7 offset:-4096 sc1\n\t" "global_load_dwordx4 a[4:7], v5, %7 offset:-3072 sc1\n\t" "global_load_dwordx4 a[16:19], v5, %7 sc1\n\t"
                         "global_load_dwordx4 a[20:23], v5, %7 offset:1024 sc1\n\t" "global_load_dwordx4 a[32:35], v5, %8 offset:-4096 sc1\n\t" "global_load_dwordx4 a[36:39], v5, %8 offset:-3072 sc1\n\t"
                         "global_load_dwordx4 a[48:51], v5, %8 sc1\n\t" "global_load_dwordx4 a[52:55], v5, %8 offset:1024 sc1\n\t" "s_nop 14\n\t" "global_load_dwordx4 a[8:11], %12, %7 offset:-2048 sc1\n\t"
                         "global_load_dwordx4 a[12:15], %12, %7 offset:-1024 sc1\n\t" "global_load_dwordx4 a[24:27], %12, %7 offset:2048 sc1\n\t" "global_load_dwordx4 a[28:31], %12, %7 offset:3072 sc1\n\t"
                         "global_load_dwordx4 a[40:43], %12, %8 offset:-2048 sc1\n\t" "global_load_dwordx4 a[44:47], %12, %8 offset:-1024 sc1\n\t" "global_load_dwordx4 a[56:59], %12, %8 offset:2048 sc1\n\t"
                         "global_load_dwordx4 a[60:63], %12, %8 offset:3072 sc1\n\t" "s_nop 15\n\t" "s_nop 5\n\t" "s_mov_b64 exec, %9\n\t" "global_load_dwordx4 a[64:67], v6, %6 offset:-2048 sc1\n\t"
                         "global_load_dwordx4 a[68:71], v6, %6 offset:-1536 sc1\n\t" "global_load_dwordx4 a[72:75], v6, %6 offset:-1024 sc1\n\t" "global_load_dwordx4 a[76:79], v6, %6 offset:-512 sc1\n\t"
                         "s_mov_b64 exec, -1\n\t" "global_load_dwordx4 a[80:83], %14, %6 sc1\n\t" "global_load_dwordx4 a[84:87], %14, %6 offset:1024 sc1\n\t" "global_load_dwordx4 a[88:91], %14, %6 offset:2048 sc1\n\t"
                         "global_load_dwordx4 a[92:95], %14, %6 offset:3072 sc1\n\t" "s_mov_b32 s42, %11\n\t" ".Lsp_v5:\n\t" "s_sub_u32 s42, s42, 1\n\t" "s_cmp_gt_i32 s42, 0\n\t" "s_cbranch_scc1 .Lsp_v5\n\t"
                         "global_load_dwordx4 a[96:99], v4, %3 offset:-512 sc1\n\t" "global_load_dwordx4 a[100:103], %13, %3 sc1\n\t" "global_load_dwordx4 a[104:107], v4, %3 offset:512 sc1\n\t"
                         "global_load_dwordx4 a[108:111], %13, %3 offset:1024 sc1\n\t" "global_load_dwordx4 a[112:115], v4, %3 offset:1536 sc1\n\t" "global_load_dwordx4 a[116:119], %13, %3 offset:2048 sc1\n\t"
                         "global_load_dwordx4 a[120:123], v4, %3 offset:2560 sc1\n\t" "global_load_dwordx4 a[124:127], %13, %3 offset:3072 sc1\n\t" "s_waitcnt vmcnt(31)\n\t"
                         "v_smfmac_f32_16x16x64_bf16 v[48:51], a[0:3], v[112:119], v9\n\t" "s_waitcnt vmcnt(30)\n\t" "v_smfmac_f32_16x16x64_bf16 v[52:55], a[4:7], v[120:127], v9\n\t" "s_waitcnt vmcnt(29)\n\t"
                         "v_smfmac_f32_16x16x64_bf16 v[64:67], a[16:19], v[144:151], v9\n\t" "s_waitcnt vmcnt(28)\n\t" "v_cvt_pk_bf16_f32 v16, v48, v49\n\t" "v_cvt_pk_bf16_f32 v17, v50, v51\n\t"
                         "v_smfmac_f32_16x16x64_bf16 v[68:71], a[20:23], v[152:159], v9\n\t" "s_waitcnt vmcnt(27)\n\t" "v_cvt_pk_bf16_f32 v18, v52, v53\n\t" "v_cvt_pk_bf16_f32 v19, v54, v55\n\t"
                         "v_smfmac_f32_16x16x64_bf16 v[80:83], a[32:35], v[176:183], v9\n\t" "s_waitcnt vmcnt(26)\n\t" "v_cvt_pk_bf16_f32 v24, v64, v65\n\t" "v_cvt_pk_bf16_f32 v25, v66, v67\n\t"
                         "v_smfmac_f32_16x16x64_bf16 v[84:87], a[36:39], v[184:191], v9\n\t" "s_waitcnt vmcnt(25)\n\t" "v_cvt_pk_bf16_f32 v26, v68, v69\n\t" "v_cvt_pk_bf16_f32 v27, v70, v71\n\t"
                         "v_smfmac_f32_16x16x64_bf16 v[96:99], a[48:51], v[208:215], v9\n\t" "s_waitcnt vmcnt(24)\n\t" "v_cvt_pk_bf16_f32 v32, v80, v81\n\t" "v_cvt_pk_bf16_f32 v33, v82, v83\n\t"
                         "v_smfmac_f32_16x16x64_bf16 v[100:103], a[52:55], v[216:223], v9\n\t" "s_waitcnt vmcnt(23)\n\t" "v_cvt_pk_bf16_f32 v34, v84, v85\n\t" "v_cvt_pk_bf16_f32 v35, v86, v87\n\t"
                         "v_smfmac_f32_16x16x64_bf16 v[56:59], a[8:11], v[128:135], v9\n\t" "s_waitcnt vmcnt(22)\n\t" "v_cvt_pk_bf16_f32 v40, v96, v97\n\t" "v_cvt_pk_bf16_f32 v41, v98, v99\n\t"
                         "v_smfmac_f32_16x16x64_bf16 v[60:63], a[12:15], v[136:143], v9\n\t" "s_waitcnt vmcnt(21)\n\t" "v_cvt_pk_bf16_f32 v42, v100, v101\n\t" "v_cvt_pk_bf16_f32 v43, v102, v103\n\t"
                         "v_smfmac_f32_16x16x64_bf16 v[72:75], a[24:27], v[160:167], v9\n\t" "s_waitcnt vmcnt(20)\n\t" "v_cvt_pk_bf16_f32 v20, v56, v57\n\t" "v_cvt_pk_bf16_f32 v21, v58, v59\n\t"
                         "v_smfmac_f32_16x16x64_bf16 v[76:79], a[28:31], v[168:175], v9\n\t" "s_waitcnt vmcnt(19)\n\t" "v_cvt_pk_bf16_f32 v22, v60, v61\n\t" "v_cvt_pk_bf16_f32 v23, v62, v63\n\t"
                         "v_smfmac_f32_16x16x64_bf16 v[88:91], a[40:43], v[192:199], v9\n\t" "s_waitcnt vmcnt(18)\n\t" "v_cvt_pk_bf16_f32 v28, v72, v73\n\t" "v_cvt_pk_bf16_f32 v29, v74, v75\n\t"
                         "v_smfmac_f32_16x16x64_bf16 v[92:95], a[44:47], v[200:207], v9\n\t" "s_waitcnt vmcnt(17)\n\t" "v_cvt_pk_bf16_f32 v30, v76, v77\n\t" "v_cvt_pk_bf16_f32 v31, v78, v79\n\t"
                         "v_smfmac_f32_16x16x64_bf16 v[104:107], a[56:59], v[224:231], v9\n\t" "s_waitcnt vmcnt(16)\n\t" "v_cvt_pk_bf16_f32 v36, v88, v89\n\t" "v_cvt_pk_bf16_f32 v37, v90, v91\n\t"
                         "v_smfmac_f32_16x16x64_bf16 v[108:111], a[60:63], v[232:239], v9\n\t" "s_waitcnt vmcnt(15)\n\t" "v_cvt_pk_bf16_f32 v38, v92, v93\n\t" "v_cvt_pk_bf16_f32 v39, v94, v95\n\t"
                         "v_smfmac_f32_16x16x64_bf16 v[0:3], a[64:67], v[16:23], v8\n\t" "s_waitcnt vmcnt(14)\n\t" "v_cvt_pk_bf16_f32 v44, v104, v105\n\t" "v_cvt_pk_bf16_f32 v45, v106, v107\n\t"
                         "v_smfmac_f32_16x16x64_bf16 v[0:3], a[68:71], v[24:31], v8\n\t" "s_waitcnt vmcnt(13)\n\t" "v_cvt_pk_bf16_f32 v46, v108, v109\n\t" "v_cvt_pk_bf16_f32 v47, v110, v111\n\t"
                         "v_smfmac_f32_16x16x64_bf16 v[0:3], a[72:75], v[32:39], v8\n\t" "s_waitcnt vmcnt(12)\n\t" "v_smfmac_f32_16x16x64_bf16 v[0:3], a[76:79], v[40:47], v8\n\t" "s_waitcnt vmcnt(6)\n\t"
                         "v_smfmac_f32_16x16x64_bf16 v[0:3], a[80:83], a[96:103], v8\n\t" "s_waitcnt vmcnt(4)\n\t" "v_smfmac_f32_16x16x64_bf16 v[0:3], a[84:87], a[104:111], v8\n\t" "s_waitcnt vmcnt(2)\n\t"
                         "v_smfmac_f32_16x16x64_bf16 v[0:3], a[88:91], a[112:119], v8\n\t" "s_waitcnt vmcnt(0)\n\t" "v_smfmac_f32_16x16x64_bf16 v[0:3], a[92:95], a[120:127], v8\n\t" "s_branch .Lv_end\n\t" ".Lsp_v0:\n\t"
                         "s_memtime vcc\n\t" "s_waitcnt lgkmcnt(0)\n\t" "s_sub_i32 s42, %10, vcc_lo\n\t" "s_cmp_gt_i32 s42, 80\n\t" "s_cbranch_scc1 .Lsp_v0\n\t" "s_max_i32 s42, s42, 0\n\t" "s_sub_i32 s42, 92, s42\n\t"
                         "s_and_b32 s42, s42, -4\n\t" "s_getpc_b64 vcc\n\t" "s_add_u32 vcc_lo, vcc_lo, s42\n\t" "s_addc_u32 vcc_hi, vcc_hi, 0\n\t" "s_setpc_b64 vcc\n\t" "s_nop 0\n\t" "s_nop 0\n\t" "s_nop 0\n\t" "s_nop 0\n\t"
                         "s_nop 0\n\t" "s_nop 0\n\t" "s_nop 0\n\t" "s_nop 0\n\t" "s_nop 0\n\t" "s_nop 0\n\t" "s_nop 0\n\t" "s_nop 0\n\t" "s_nop 0\n\t" "s_nop 0\n\t" "s_nop 0\n\t" "s_nop 0\n\t" "s_nop 0\n\t" "s_nop 0\n\t"
                         "s_nop 0\n\t" "s_nop 0\n\t" "global_load_dwordx4 a[96:99], v4, %3 offset:-512 sc1\n\t" "global_load_dwordx4 a[104:107], v4, %3 offset:512 sc1\n\t"
                         "global_load_dwordx4 a[112:115], v4, %3 offset:1536 sc1\n\t" "global_load_dwordx4 a[120:123], v4, %3 offset:2560 sc1\n\t" "global_load_dwordx4 a[100:103], %13, %3 sc1\n\t"
                         "global_load_dwordx4 a[108:111], %13, %3 offset:1024 sc1\n\t" "global_load_dwordx4 a[116:119], %13, %3 offset:2048 sc1\n\t" "global_load_dwordx4 a[124:127], %13, %3 offset:3072 sc1\n\t" "s_nop 9\n\t"
                         "global_load_dwordx4 a[0:3], v5, %7 offset:-4096 sc1\n\t" "global_load_dwordx4 a[4:7], v5, %7 offset:-3072 sc1\n\t" "global_load_dwordx4 a[16:19], v5, %7 sc1\n\t"
                         "global_load_dwordx4 a[20:23], v5, %7 offset:1024 sc1\n\t" "global_load_dwordx4 a[32:35], v5, %8 offset:-4096 sc1\n\t" "global_load_dwordx4 a[36:39], v5, %8 offset:-3072 sc1\n\t"
                         "global_load_dwordx4 a[48:51], v5, %8 sc1\n\t" "global_load_dwordx4 a[52:55], v5, %8 offset:1024 sc1\n\t" "s_nop 14\n\t" "global_load_dwordx4 a[8:11], %12, %7 offset:-2048 sc1\n\t"
                         "global_load_dwordx4 a[12:15], %12, %7 offset:-1024 sc1\n\t" "global_load_dwordx4 a[24:27], %12, %7 offset:2048 sc1\n\t" "global_load_dwordx4 a[28:31], %12, %7 offset:3072 sc1\n\t"
                         "global_load_dwordx4 a[40:43], %12, %8 offset:-2048 sc1\n\t" "global_load_dwordx4 a[44:47], %12, %8 offset:-1024 sc1\n\t" "global_load_dwordx4 a[56:59], %12, %8 offset:2048 sc1\n\t"
                         "global_load_dwordx4 a[60:63], %12, %8 offset:3072 sc1\n\t" "s_nop 15\n\t" "s_nop 5\n\t" "global_load_dwordx4 a[80:83], %14, %6 sc1\n\t"
                         "global_load_dwordx4 a[84:87], %14, %6 offset:1024 sc1\n\t" "global_load_dwordx4 a[88:91], %14, %6 offset:2048 sc1\n\t" "global_load_dwordx4 a[92:95], %14, %6 offset:3072 sc1\n\t"
                         "s_mov_b64 exec, %9\n\t" "global_load_dwordx4 a[64:67], v6, %6 offset:-2048 sc1\n\t" "global_load_dwordx4 a[68:71], v6, %6 offset:-1536 sc1\n\t"
                         "global_load_dwordx4 a[72:75], v6, %6 offset:-1024 sc1\n\t" "global_load_dwordx4 a[76:79], v6, %6 offset:-512 sc1\n\t" "s_mov_b64 exec, -1\n\t" "s_waitcnt vmcnt(23)\n\t"
                         "v_smfmac_f32_16x16x64_bf16 v[48:51], a[0:3], v[112:119], v9\n\t" "s_waitcnt vmcnt(22)\n\t" "v_smfmac_f32_16x16x64_bf16 v[52:55], a[4:7], v[120:127], v9\n\t" "s_waitcnt vmcnt(21)\n\t"
                         "v_smfmac_f32_16x16x64_bf16 v[64:67], a[16:19], v[144:151], v9\n\t" "s_waitcnt vmcnt(20)\n\t" "v_cvt_pk_bf16_f32 v16, v48, v49\n\t" "v_cvt_pk_bf16_f32 v17, v50, v51\n\t"
                         "v_smfmac_f32_16x16x64_bf16 v[68:71], a[20:23], v[152:159], v9\n\t" "s_waitcnt vmcnt(19)\n\t" "v_cvt_pk_bf16_f32 v18, v52, v53\n\t" "v_cvt_pk_bf16_f32 v19, v54, v55\n\t"
                         "v_smfmac_f32_16x16x64_bf16 v[80:83], a[32:35], v[176:183], v9\n\t" "s_waitcnt vmcnt(18)\n\t" "v_cvt_pk_bf16_f32 v24, v64, v65\n\t" "v_cvt_pk_bf16_f32 v25, v66, v67\n\t"
                         "v_smfmac_f32_16x16x64_bf16 v[84:87], a[36:39], v[184:191], v9\n\t" "s_waitcnt vmcnt(17)\n\t" "v_cvt_pk_bf16_f32 v26, v68, v69\n\t" "v_cvt_pk_bf16_f32 v27, v70, v71\n\t"
                         "v_smfmac_f32_16x16x64_bf16 v[96:99], a[48:51], v[208:215], v9\n\t" "s_waitcnt vmcnt(16)\n\t" "v_cvt_pk_bf16_f32 v32, v80, v81\n\t" "v_cvt_pk_bf16_f32 v33, v82, v83\n\t"
                         "v_smfmac_f32_16x16x64_bf16 v[100:103], a[52:55], v[216:223], v9\n\t" "s_waitcnt vmcnt(15)\n\t" "v_cvt_pk_bf16_f32 v34, v84, v85\n\t" "v_cvt_pk_bf16_f32 v35, v86, v87\n\t"
                         "v_smfmac_f32_16x16x64_bf16 v[56:59], a[8:11], v[128:135], v9\n\t" "s_waitcnt vmcnt(14)\n\t" "v_cvt_pk_bf16_f32 v40, v96, v97\n\t" "v_cvt_pk_bf16_f32 v41, v98, v99\n\t"
                         "v_smfmac_f32_16x16x64_bf16 v[60:63], a[12:15], v[136:143], v9\n\t" "s_waitcnt vmcnt(13)\n\t" "v_cvt_pk_bf16_f32 v42, v100, v101\n\t" "v_cvt_pk_bf16_f32 v43, v102, v103\n\t"
                         "v_smfmac_f32_16x16x64_bf16 v[72:75], a[24:27], v[160:167], v9\n\t" "s_waitcnt vmcnt(12)\n\t" "v_cvt_pk_bf16_f32 v20, v56, v57\n\t" "v_cvt_pk_bf16_f32 v21, v58, v59\n\t"
                         "v_smfmac_f32_16x16x64_bf16 v[76:79], a[28:31], v[168:175], v9\n\t" "s_waitcnt vmcnt(11)\n\t" "v_cvt_pk_bf16_f32 v22, v60, v61\n\t" "v_cvt_pk_bf16_f32 v23, v62, v63\n\t"
                         "v_smfmac_f32_16x16x64_bf16 v[88:91], a[40:43], v[192:199], v9\n\t" "s_waitcnt vmcnt(10)\n\t" "v_cvt_pk_bf16_f32 v28, v72, v73\n\t" "v_cvt_pk_bf16_f32 v29, v74, v75\n\t"
                         "v_smfmac_f32_16x16x64_bf16 v[92:95], a[44:47], v[200:207], v9\n\t" "s_waitcnt vmcnt(9)\n\t" "v_cvt_pk_bf16_f32 v30, v76, v77\n\t" "v_cvt_pk_bf16_f32 v31, v78, v79\n\t"
                         "v_smfmac_f32_16x16x64_bf16 v[104:107], a[56:59], v[224:231], v9\n\t" "s_waitcnt vmcnt(8)\n\t" "v_cvt_pk_bf16_f32 v36, v88, v89\n\t"
                         "v_smfmac_f32_16x16x64_bf16 v[108:111], a[60:63], v[232:239], v9\n\t" "s_waitcnt vmcnt(7)\n\t" "v_cvt_pk_bf16_f32 v37, v90, v91\n\t"
                         "v_smfmac_f32_16x16x64_bf16 v[0:3], a[80:83], a[96:103], v8\n\t" "s_waitcnt vmcnt(6)\n\t" "v_cvt_pk_bf16_f32 v38, v92, v93\n\t" "v_smfmac_f32_16x16x64_bf16 v[0:3], a[84:87], a[104:111], v8\n\t"
                         "s_waitcnt vmcnt(5)\n\t" "v_cvt_pk_bf16_f32 v39, v94, v95\n\t" "v_smfmac_f32_16x16x64_bf16 v[0:3], a[88:91], a[112:119], v8\n\t" "s_waitcnt vmcnt(4)\n\t" "v_cvt_pk_bf16_f32 v44, v104, v105\n\t"
                         "v_smfmac_f32_16x16x64_bf16 v[0:3], a[92:95], a[120:127], v8\n\t" "s_waitcnt vmcnt(3)\n\t" "v_cvt_pk_bf16_f32 v45, v106, v107\n\t" "v_smfmac_f32_16x16x64_bf16 v[0:3], a[64:67], v[16:23], v8\n\t"
                         "s_waitcnt vmcnt(2)\n\t" "v_cvt_pk_bf16_f32 v46, v108, v109\n\t" "v_smfmac_f32_16x16x64_bf16 v[0:3], a[68:71], v[24:31], v8\n\t" "s_waitcnt vmcnt(1)\n\t" "v_cvt_pk_bf16_f32 v47, v110, v111\n\t"
                         "v_smfmac_f32_16x16x64_bf16 v[0:3], a[72:75], v[32:39], v8\n\t" "s_waitcnt vmcnt(0)\n\t" "v_smfmac_f32_16x16x64_bf16 v[0:3], a[76:79], v[40:47], v8\n\t" ".Lv_end:\n\t" "s_nop 7\n\t"
                         "v_pk_add_f32 v[0:1], v[0:1], v[2:3]\n\t" "v_cvt_pk_bf16_f32 v2, v0, v1\n\t" "v_cmp_u_f32 vcc, v0, v1\n\t" "s_cmp_eq_u64 vcc, 0\n\t"
                         "v_mov_b32_dpp v3, v2 quad_perm:[1,0,3,2] row_mask:0xf bank_mask:0xf\n\t" "s_cselect_b64 %0, -1, 0\n\t" "v_perm_b32 v0, v2, v3, v11\n\t" "s_mov_b64 exec, %0\n\t"
                         "global_store_dword v10, v0, %5 offset:-4096 sc1\n\t" "global_store_dword v10, v0, %4 offset:-4096 sc1\n\t" "s_memrealtime %1\n\t" "s_mov_b64 exec, -1\n\t" "s_waitcnt lgkmcnt(0)\n\t"
                         : "=&s"(ok), "=&s"(tp8)
                         : "{v[4:11]}"(ad), "s"(up), "s"(mnx), "s"((u64)mnx ^ 8192), "s"(memp + 16384), "s"(memp + 32768 - 2048), "s"(memp + 49152 - 2048), "s"(0xc30000c3c30000c3ull),
                           "s"(uni((int)(tgt - (u32)(v < 16 ? cfg.dU - (v >> 3) * cfg.s2 : cfg.cR2)))), "s"(v < 16 ? 0 : (v < 24 ? 1 : cfg.T3)), "v"(pob), "v"(uob), "v"(pmo), VALL : "memory", "vcc", "scc", "s42");
            if (__builtin_expect(ok != 0, 1) || s < 0) break;
            if (all || --budget < 0) {
                asm volatile("v_min_u32 v0, -2, v0\n\tglobal_store_dword v10, v0, %0 offset:-4096 sc1\n\tglobal_store_dword v10, v0, %1 offset:-4096 sc1"
                             : : "s"(mnx), "s"((u64)mnx ^ 8192), "{v[4:11]}"(ad), VALL : "memory");
                break;
            }
            if ((++tries & 15) == 0) {
                u32 m = 0;
#pragma nounroll
                for (int i = 0; i < 32; ++i) {
                    u32x4 t;
                    gld16c<0>(t, i < 24 ? memp + 12288 : up, i < 20 ? (4 + i + ((4 + i) & 24)) * 1024 + ad[3] : (i < 24 ? i * 512 - 8192 + ad[2] : (i & 7) * 512 - 512 + (i & 1 ? uob : ad[0]))); waitv<0>(t);
                    m = max3u(max3u(m, t[0], t[1]), t[2], t[3]);
                }
                all = __builtin_amdgcn_ballot_w64(m == 0xffffffffu) == 0;
            }
            phi += (u32)cfg.G;
            zero();
        }
        asm volatile("s_barrier\n\t" "ds_read_b128 v[112:115], %0\n\t" "ds_read_b128 v[116:119], %0 offset:1024\n\t" "ds_read_b128 v[120:123], %0 offset:2048\n\t" "ds_read_b128 v[124:127], %0 offset:3072\n\t"
                     "ds_read_b128 v[128:131], %0 offset:4096\n\t" "ds_read_b128 v[132:135], %0 offset:5120\n\t" "ds_read_b128 v[136:139], %0 offset:6144\n\t" "ds_read_b128 v[140:143], %0 offset:7168\n\t"
                     "ds_read_b128 v[144:147], %0 offset:8192\n\t" "ds_read_b128 v[148:151], %0 offset:9216\n\t" "ds_read_b128 v[152:155], %0 offset:10240\n\t" "ds_read_b128 v[156:159], %0 offset:11264\n\t"
                     "ds_read_b128 v[160:163], %0 offset:12288\n\t" "ds_read_b128 v[164:167], %0 offset:13312\n\t" "ds_read_b128 v[168:171], %0 offset:14336\n\t" "ds_read_b128 v[172:175], %0 offset:15360\n\t"
                     "ds_read_b128 v[176:179], %0 offset:16384\n\t" "ds_read_b128 v[180:183], %0 offset:17408\n\t" "ds_read_b128 v[184:187], %0 offset:18432\n\t" "ds_read_b128 v[188:191], %0 offset:19456\n\t"
                     "ds_read_b128 v[192:195], %0 offset:20480\n\t" "ds_read_b128 v[196:199], %0 offset:21504\n\t" "ds_read_b128 v[200:203], %0 offset:22528\n\t" "ds_read_b128 v[204:207], %0 offset:23552\n\t"
                     "ds_read_b128 v[208:211], %0 offset:24576\n\t" "ds_read_b128 v[212:215], %0 offset:25600\n\t" "ds_read_b128 v[216:219], %0 offset:26624\n\t" "ds_read_b128 v[220:223], %0 offset:27648\n\t"
                     "ds_read_b128 v[224:227], %0 offset:28672\n\t" "ds_read_b128 v[228:231], %0 offset:29696\n\t" "ds_read_b128 v[232:235], %0 offset:30720\n\t" "ds_read_b128 v[236:239], %0 offset:31744\n\t"
                     : : "v"(lds + ad[3] + ((s + 1) & 1) * 32768), VALL : "memory");
        if (s >= 0) {
            follow(phi, (int)(tt & 0xffffu) + 5 * (v >> 2) / 2 - VISV, (u32)tp8, cfg.G);
            if (vb == 0 && lane < 8) gst8(fl, (F_CUR + lane) * 128, u32x2{(u32)s - 1, phi + (tt & 0xffffu)});
        }
        phi += tt & 0xffffu;
        memp = mnx; up = ub + uslot(sd, s + 1);
        zero();
        tt = sched_ld0(s + 1);
        clk.calib();
        while (s >= 0) {
            u32 h0;
            spin_to(clk.at(phi + (u32)cfg.dV));
            asm volatile("global_load_dword %0, %1, %2 offset:-4096 sc1\n\ts_sleep 5" : "=&v"(h0) : "v"(ho), "s"(memp) : "memory");
            edge(clk.r1, clk.m1);
            asm volatile("s_waitcnt vmcnt(0)" : "+v"(h0) : : "memory");
            if (__builtin_amdgcn_ballot_w64(h0 == 0xffffffffu) == 0 || --budget < 0) break;
            phi += (u32)cfg.G;
        }
        asm volatile(".Lkm_lv:");
    }
}

DI void xcopy_task(const Ptrs& p, int j, int w) {
    const int tid = 64 * w + lanex();
    Sync y;
    sync_ld(y, p.ws);
    u32x4 xv[10];
    sfor<0, 10>([&](auto I) { gld16<I.v * 128>(xv[I.v], p.x + (size_t)j * 40960, (u32)((tid & 31) * 1280 + (tid >> 5) * 16)); });
    sfor<0, 10>([&](auto I) { waitv<9 - I.v>(xv[I.v]); });
    asm volatile("" : "+v"(y.r), "+v"(y.g), "+v"(y.e));
    sfor<0, 10>([&](auto I) { gst16(p.ws + OFF_XF + (size_t)j * 40960 + I.v * 4096, (u32)tid * 16, xv[I.v]); });
    waitvm<0>();
    barrier();
    const u64 t = sync_tag(y.e);
    if (tid == 0) gst8(p.ws + OFF_LAT, 1536 + (u32)j * 8, u32x2{(u32)t, (u32)(t >> 32)});
}
DI void xcopy_wait(const char* ws, u64 t) {
#pragma nounroll
    for (int it = 0; it < (1 << 16); ++it) {
        u32x2 r;
        asm volatile("s_nop 4\n\tglobal_load_dwordx2 %0, %1, %2 offset:1536 sc1\n\ts_waitcnt vmcnt(0)" : "=&v"(r) : "v"(lanex() * 8), "s"(ws + OFF_LAT) : "memory");
        if (__builtin_amdgcn_ballot_w64(r[0] != (u32)t || r[1] != (u32)(t >> 32)) == 0) break;
        __builtin_amdgcn_s_sleep(16);
    }
}
DI void init_task(const Ptrs& p, int j, u32 lds, int w) {
    const int lane = lanex(), tid = 64 * w + lane;
    Sync y;
    sync_ld(y, p.ws);
    const u32x4 ff = u32x4{~0u, ~0u, ~0u, ~0u};
    const int i = j * 256 + tid, r = j < 8 ? j & 1 : min(j / 2 - 2, NREG - 1);
    char* const vb = p.ws + (r < 2 ? 0 : OFF_U - (4u << 20)) + ((size_t)r << 21) + 1984 * 1024 + (j < 8 ? j & 6 : (j & 1) * 2 + (j >> 1 == 15) * 4) * 8192;
    const u32 vo = (u32)((w >> 1) * 8192 + (w & 1) * 1024);
    gst16(vb, vo + lane * 16, ff);
    if (i < F_N * 8) gst16(p.ws + OFF_ST, (u32)i * 16, u32x4{i == F_ONE * 8 ? 1000u : ~0u, ~0u, ~0u, ~0u});
    gst16(p.ws + 65536, (u32)i * 16, ff);
    if (i < 4096) {
        const int k = (i & 511) + ((i >> 1) & 0x600);
        u32x4 m;
        gld16<0>(m, p.mem0, (u32)((2 * ((k >> 2) & 3) + (k & 1)) * 1280 + min(2 * (k >> 4) + ((k >> 1) & 1), 79) * 16));
        waitv<0>(m);
        gst16(p.ws, (u32)i * 16, k < 640 ? u32x4{min(m[0], 0xfffffffeu), min(m[1], 0xfffffffeu), min(m[2], 0xfffffffeu), min(m[3], 0xfffffffeu)} : ff);
    }
#pragma unroll
    for (int c = 0; c < 6; ++c) gst16(p.ws + OFF_U + c * 131072 - (c == 5) * 122880, (u32)i * 16, ff);
    waitvm<0>();
    asm volatile("" : "+v"(y.r), "+v"(y.g), "+v"(y.e));
    barrier();
    if (w) return;
    u32 dt[2] = {0xffffffu, 0xffffffu};
#pragma unroll
    for (int c = 0; c < 2 * NSAMP; ++c) {
        u32 v;
        const u32 t0 = mt();
        asm volatile("global_load_dword %0, %1, %2 sc1\n\ts_waitcnt vmcnt(0)" : "=&v"(v) : "v"((c & 1) * 8192 + (c >> 1) * 256), "s"(vb) : "memory");
        dt[c & 1] = min(dt[c & 1], mt() - t0);
    }
    const u64 t = sync_tag(y.e);
    const u32 vote = (u32)((dt[0] > dt[1]) ^ xhalf() ^ (int)((vb - p.ws) >> 21)) & 1u;
    if (tid == 0) {
        if (j == 0) gst8(p.ws + OFF_LAT, 512, u32x2{(u32)t, rt()});
        gst8(p.ws + OFF_LAT, (u32)j * 8, u32x2{(u32)t | vote, (u32)(t >> 32)});
    }
}

DI float xmax(float v) {
    const auto a = __builtin_amdgcn_permlane32_swap(__float_as_uint(v), __float_as_uint(v), false, false);
    v = fmaxf(__uint_as_float(a[0]), __uint_as_float(a[1]));
    const auto b = __builtin_amdgcn_permlane16_swap(__float_as_uint(v), __float_as_uint(v), false, false);
    return fmaxf(__uint_as_float(b[0]), __uint_as_float(b[1]));
}
DI float xsum(float v) {
    const auto a = __builtin_amdgcn_permlane32_swap(__float_as_uint(v), __float_as_uint(v), false, false);
    v = __uint_as_float(a[0]) + __uint_as_float(a[1]);
    const auto b = __builtin_amdgcn_permlane16_swap(__float_as_uint(v), __float_as_uint(v), false, false);
    return __uint_as_float(b[0]) + __uint_as_float(b[1]);
}
DI void gst16c(char* sbase, u32 voff, u32x4 v) {
    asm volatile("global_store_dwordx4 %0, %1, %2 sc1\n\ts_nop 1" : : "v"(voff), "v"(v), "s"(sbase) : "memory");
}
DI void gst8c(char* sbase, u32 voff, u32x2 v) {
    asm volatile("global_store_dwordx2 %0, %1, %2 sc1\n\ts_nop 1" : : "v"(voff), "v"(v), "s"(sbase) : "memory");
}
#define WAIT6(N, r, o) asm volatile("s_waitcnt vmcnt(" #N ")" : "+v"(r[o]), "+v"(r[o + 1]), "+v"(r[o + 2]), "+v"(r[o + 3]), "+v"(r[o + 4]), "+v"(r[o + 5]) : : "memory")
#define WSLEEP 90
DI void wg_wait(const char* fl, int w, int i0, int t0, int i1, int t1, int i2, int t2, int i3, int t3, int dt, bool acq) {
    if (w == 0) {
        const int lane = lanex(), tg = lane == 1 ? t1 : (lane == 2 ? t2 : (lane == 3 ? t3 : t0));
        const int ix = tg <= 0 ? i0 : (lane == 1 ? i1 : (lane == 2 ? i2 : (lane == 3 ? i3 : i0)));
#pragma nounroll
        for (int it = 0; it < (1 << 14); ++it) {
            u32 v;
            flag_ld(v, fl, ix);
            asm volatile("s_waitcnt vmcnt(0)" : "+v"(v) : : "memory");
            if (__builtin_amdgcn_ballot_w64((int)(v + 1u) < tg) == 0) break;
            const u32 wake = rt() + (u32)(dt + 180 * min(max(t0 - 2 - uni((int)v), 0), 64));
            while ((int)(rt() - wake) < 0) __builtin_amdgcn_s_sleep(4);
        }
        if (acq) acquire();
    }
    barrier();
}
#define TEST 172
DI void until(u32 t) { while ((int)(rt() - t) < 0) __builtin_amdgcn_s_sleep(1); }
DI u32 step_wait(const char* fl, int w, int gr, int t, u32 lds) {
    if (w == 0) {
        u32x2 v;
#pragma nounroll
        for (int it = 0; it < (1 << 14); ++it) {
            asm volatile("s_nop 4\n\tglobal_load_dwordx2 %0, %1, %2 sc1\n\ts_waitcnt vmcnt(0)" : "=&v"(v) : "v"((F_CUR + gr) * 128), "s"(fl) : "memory");
            const int d = t - 1 - uni((int)v[0]);
            if (d <= 0) { v[1] += (u32)(d * TEST); break; }
            const u32 now = rt();
            nap(min(max((int)((u32)uni((int)v[1]) - now) + d * TEST + 10, 40), 2000));
        }
        *LDSP(u32, lds + GRING * GBUF + 4) = v[1];
    }
    barrier();
    return (u32)uni((int)*LDSP(u32, lds + GRING * GBUF + 4));
}
#define PDL 135
#define ODL 280
#define TDT 35
template <bool LATE>
DI void px_wave(const Ptrs& p, const ChainCfg& cfg, int s, int h, int slow, int w, u32 lds, bool keep) {
    const int lane = lanex(), n = lane & 15, g = lane >> 4;
    const char* qb = p.ws + OFF_QX + (size_t)(s * NH + h) * 12288;
    const char* kb = p.ws + OFF_KX + (size_t)(s * NH + h) * 12288;
    const char* mb = p.ws + OFF_KM + (size_t)(s * NH + h) * 3072;
    const u32 ko = (u32)((n >> 3) * 6144 + (8 * ((n >> 2) & 1) + (n & 3) + 16 * g) * 16);
    const u32 mo = (u32)(g * 128 + (n & 7) * 16);
    u32x4 qf[12], kf[12], mf[6];
    sfor<0, 12>([&](auto I) { gld16c<(I.v % 3) * 1024>(qf[I.v], qb + (I.v / 3) * 3072, lane * 16); gap<SMP>(slow); });
    if (!SMP) pace(slow);
    sfor<0, 12>([&](auto I) { gld16c<(I.v % 3) * 1024 + (I.v / 6) * 64>(kf[I.v], kb + ((I.v / 3) & 1) * 3072, ko); gap<SMP>(slow); });
    if (!SMP) pace(slow);
    if constexpr (LATE) { WAIT6(0, qf, 0); WAIT6(0, qf, 6); WAIT6(0, kf, 0); }
    else {
        sfor<0, 6>([&](auto I) { gld16c<I.v * 512>(mf[I.v], mb, mo); });
        WAIT6(24, qf, 0); WAIT6(18, qf, 6); WAIT6(12, kf, 0);
    }
    f32x4 sc[2][3];
#pragma unroll
    for (int tt = 0; tt < 2; ++tt)
#pragma unroll
        for (int T = 0; T < 3; ++T) sc[tt][T] = f32x4{0.f, 0.f, 0.f, 0.f};
#pragma unroll
    for (int ks = 0; ks < 6; ++ks) { sc[0][0] = mfma16(kf[ks], qf[ks], sc[0][0]); sc[1][0] = mfma16(kf[ks], qf[6 + ks], sc[1][0]); }
    WAIT6(6, kf, 6);
#pragma unroll
    for (int ks = 0; ks < 6; ++ks) { sc[0][1] = mfma16(kf[6 + ks], qf[ks], sc[0][1]); sc[1][1] = mfma16(kf[6 + ks], qf[6 + ks], sc[1][1]); }
    if constexpr (LATE) {
        until(step_wait(p.ws + OFF_ST, w, h & 7, s - 1, lds) + (u32)(s == NSEG - 1 ? cfg.T : cfg.T + PDX));
        bool ok = false;
#pragma nounroll
        for (int it = 0; it < (1 << 14); ++it) {
            u32 v;
            sfor<0, 6>([&](auto I) { gld16c<I.v * 512>(mf[I.v], mb, mo); });
            flag_ldm(v, p.ws + OFF_ST, (s < NSEG - 2 ? F_CUR : F_PROG + 8 * s) + (h & 7), it > 3);
            WAIT6(0, mf, 0);
            asm volatile("" : "+v"(v));
            u32 m = 0;
#pragma unroll
            for (int i = 0; i < 24; ++i) m = max(m, mf[i >> 2][i & 3]);
            if (ok || __builtin_amdgcn_ballot_w64(m == ~0u) == 0) break;
            ok = it > 3 && uni((int)v) >= (s < NSEG - 2 ? s : 7);
            if (s < NSEG - 1) nap(60);
        }
    }
    WAIT6(0, mf, 0);
#pragma unroll
    for (int ks = 0; ks < 6; ++ks) { sc[0][2] = mfma16(mf[ks], qf[ks], sc[0][2]); sc[1][2] = mfma16(mf[ks], qf[6 + ks], sc[1][2]); }
    const float kscale = 0.07216878364870322f;
    char* pb = p.ws + OFF_PX + (size_t)s * 81920;
#pragma unroll
    for (int tt = 0; tt < 2; ++tt) {
        float v[12], mx = -3.0e38f;
#pragma unroll
        for (int i = 0; i < 12; ++i) {
            const float t = rbf(rbf(sc[tt][i >> 2][i & 3]) * kscale);
            v[i] = (i >= 8 || 8 * g + i <= 16 * tt + n) ? t : -3.0e38f;
            mx = fmaxf(mx, v[i]);
        }
        mx = xmax(mx);
        float sx = 0.f, sm = 0.f;
#pragma unroll
        for (int i = 0; i < 12; ++i) {
            v[i] = __builtin_amdgcn_exp2f((v[i] - mx) * 1.4426950408889634f);
            if (i < 8) sx += v[i]; else sm += v[i];
        }
        const float inv = __builtin_amdgcn_rcpf(xsum(sx + 0.5f * sm));
        const u32x4 px = u32x4{cvtpk(v[0] * inv, v[1] * inv), cvtpk(v[2] * inv, v[3] * inv), cvtpk(v[4] * inv, v[5] * inv), cvtpk(v[6] * inv, v[7] * inv)};
        const u32x2 pm = u32x2{cvtpk(v[9] * inv, v[8] * inv), cvtpk(v[11] * inv, v[10] * inv)};
        const int ox = h * 2048 + tt * 1024, om = 65536 + (h >> 2) * 2048 + (h & 3) * 256 + tt * 1024;
        if (!LATE && keep) {
            *LDSP(u32x4, lds + ox + lane * 16) = px;
            *LDSP(u32x2, lds + om + n * 16 + (g & 1) * 8) = pm;
        } else {
            gst16c(pb + ox, lane * 16, px);
            gst8c(pb + om, (u32)(n * 16 + (g & 1) * 8), pm);
        }
    }
}

#define WAIT10(N, r) asm volatile("s_waitcnt vmcnt(" #N ")" : "+v"(r[0]), "+v"(r[1]), "+v"(r[2]), "+v"(r[3]), "+v"(r[4]), "+v"(r[5]), "+v"(r[6]), "+v"(r[7]), "+v"(r[8]), "+v"(r[9]) : : "memory")
DI void out_task(const Ptrs& p, int s, int cg0, int cg1, u32 lds, int slow, int w, int sd) {
    u32x4 pf[2][10];
    if (cg1 - cg0 > 1) {
        const u32 la = lds + lanex() * 16;
#pragma unroll
        for (int i = 0; i < 20; ++i) pf[i & 1][i >> 1] = ldsr16(la + (i < 16 ? w * 16384 + i * 1024 : 65536 + w * 4096 + (i - 16) * 1024));
    } else {
        const char* pb = p.ws + OFF_PX + (size_t)s * 81920;
        const u32 l16 = lanex() * 16;
        sfor<0, 20>([&](auto I) { gld16c<(I.v & 3) * 1024>(pf[I.v & 1][I.v >> 1], pb + (I.v < 16 ? w * 16384 + (I.v >> 2) * 4096 : 65536 + w * 4096), l16); gap<SMO>(slow); if (!SMO && I.v == 9) pace(slow); });
        WAIT10(0, pf[0]); WAIT10(0, pf[1]);
    }
#pragma nounroll
    for (int cg = cg0; cg < cg1; ++cg) {
    const int lane = lanex(), tid = 64 * w + lane, n = lane & 15, g = lane >> 4;
    const char* db = p.ws + OFF_D + s * D_STRIDE + (size_t)(16 * cg) * 16384 + w * 4096;
    const char* ub = p.ws + uslot(sd, s) + (size_t)(16 * cg) * 4096 + w * 1024;
    const u32 dof = (u32)((n >> 3) * 16384 + g * 128 + (n & 7) * 16), uof = (u32)((n >> 3) * 4096 + g * 128 + (n & 7) * 16);
    u32x4 af[3][10];
    auto aload = [&](u32x4 (&r)[10], int ct) {
        if (!SMO) pace(slow);
        sfor<0, 8>([&](auto I) { gld16c<I.v * 512>(r[I.v], db + ct * 32768, dof); gap<SMO>(slow); });
        gld16c<0>(r[8], ub + ct * 8192, uof); gap<SMO>(slow); gld16c<512>(r[9], ub + ct * 8192, uof); gap<SMO>(slow);
    };
    aload(af[0], 0);
    aload(af[1], 1);
    aload(af[2], 2);
    const int tok = 32 * s + (tid >> 3), col = 128 * cg + 8 * (tid & 7);
    u32x4 bb[2]; u32 gv;
    gld16c<0>(bb[0], p.ws + OFF_BOUT, (u32)(s * 40960 + col * 64 + (tid >> 3) * 16)); gld16c<0>(bb[1], p.ws + OFF_BOUT + 4096, (u32)(s * 40960 + col * 64 + (tid >> 3) * 16));
    asm volatile("global_load_dword %0, %1, %2 sc1" : "=v"(gv) : "v"(tok * 4), "s"(p.ws + OFF_GATE) : "memory");
    barrier();
    const u32 wa = lds + (u32)w * 16896 + n * 528 + g * 16;
    sfor<0, 8>([&](auto I) {
        constexpr int ct = I.v;
        u32x4 (&r)[10] = af[ct % 3];
        if constexpr (ct == 0) WAIT10(23, r);
        else if constexpr (ct < 3) WAIT10(23, r);
        else if constexpr (ct < 6) WAIT10(20, r);
        else if constexpr (ct == 6) WAIT10(10, r);
        else WAIT10(0, r);
        f32x4 c0 = f32x4{0.f, 0.f, 0.f, 0.f}, c1 = c0;
#pragma unroll
        for (int i = 0; i < 10; ++i) { c0 = mfma16(r[i], pf[0][i], c0); c1 = mfma16(r[i], pf[1][i], c1); }
        *LDSP(f32x4, wa + ct * 64) = c0;
        *LDSP(f32x4, wa + ct * 64 + 16 * 528) = c1;
        PIN();
        if constexpr (ct + 3 < 8) aload(r, ct + 3);
        PIN();
    });
    asm volatile("" : "+v"(bb[0]), "+v"(bb[1]), "+v"(gv));
    barrier();
    const u32 ra = lds + (u32)(tid >> 3) * 528 + (tid & 7) * 32;
    const float gt = __uint_as_float(gv), g1 = rbf(1.f - gt);
    u32 ov[8];
#pragma unroll
    for (int i = 0; i < 4; ++i) {
        const u32 ri = ra + 16 * (i & 1) + 256 * (i >> 1);
        const f32x4 z = (*LDSP(f32x4, ri) + *LDSP(f32x4, ri + 16896)) + (*LDSP(f32x4, ri + 2 * 16896) + *LDSP(f32x4, ri + 3 * 16896));
#pragma unroll
        for (int j = 0; j < 2; ++j) {
            const u32 b = bb[i >> 1][2 * (i & 1) + j];
            const u32 a = cvtpk(z[2 * j], z[2 * j + 1]);
            const u32 ga = cvtpk(gt * __uint_as_float(a << 16), gt * __uint_as_float(a & 0xffff0000u));
            const u32 gb = cvtpk(g1 * __uint_as_float(b << 16), g1 * __uint_as_float(b & 0xffff0000u));
            ov[2 * i + j] = cvtpk(__uint_as_float(ga << 16) + __uint_as_float(gb << 16), __uint_as_float(ga & 0xffff0000u) + __uint_as_float(gb & 0xffff0000u));
        }
    }
    gst16p(p.out, (u32)(tok * 1280 + col * 2), u32x4{ov[0], ov[1], ov[2], ov[3]});
    gst16p(p.out, (u32)(tok * 1280 + col * 2 + 128), u32x4{ov[4], ov[5], ov[6], ov[7]});
    }
}

DI void out_tail(const Ptrs& p, const ChainCfg& cfg, int s, int cg, u32 lds, int w, int sd, int gr) {
    const int lane = lanex(), tid = 64 * w + lane, n = lane & 15, g = lane >> 4;
    const char* db = p.ws + OFF_D + s * D_STRIDE + (size_t)(4 * cg) * 16384 + w * 4096;
    const char* ub = p.ws + uslot(sd, s) + (size_t)(4 * cg) * 4096 + w * 1024;
    const char* pb = p.ws + OFF_PX + (size_t)s * 81920;
    const u32 dof = (u32)((n >> 3) * 16384 + g * 128 + (n & 7) * 16), uof = (u32)((n >> 3) * 4096 + g * 128 + (n & 7) * 16);
    u32x4 pf[2][10], af[2][10];
    sfor<0, 16>([&](auto I) { gld16c<(I.v & 7) * 512>(af[I.v >> 3][I.v & 7], db + (I.v >> 3) * 32768, dof); });
    const int tok = 32 * s + (tid >> 3), col = 32 * cg + 4 * (tid & 7);
    u32x2 bb; u32 gv;
    asm volatile("global_load_dwordx2 %0, %2, %3 sc1\n\tglobal_load_dword %1, %4, %5 sc1\n\ts_waitcnt vmcnt(0)" : "=&v"(bb), "=&v"(gv)
                 : "v"(s * 40960 + (col >> 3) * 512 + (tid >> 3) * 16 + (col & 7) * 2), "s"(p.ws + OFF_BOUT), "v"(tok * 4), "s"(p.ws + OFF_GATE) : "memory");
    sfor<0, 16>([&](auto I) { waitv<0>(af[I.v >> 3][I.v & 7]); });
    until(step_wait(p.ws + OFF_ST, w, gr, s - 1, lds) + (u32)(s == NSEG - 1 ? cfg.cR : cfg.cR + 2 * PDX));
    const u32 wa = lds + (u32)w * 4608 + n * 144 + g * 16;
#pragma nounroll
    for (int it = 0; it < (1 << 14); ++it) {
        if (it == 4) {
            flag_wait(p.ws + OFF_ST, F_PX + s, 8);
            flag_wait(p.ws + OFF_ST, s < NSEG - 2 ? F_CUR + gr : F_UD + 2 * (s & 1), s < NSEG - 2 ? s + 1 : 16);
            flag_wait(p.ws + OFF_ST, s < NSEG - 2 ? F_ONE : F_UD + 2 * (s & 1) + 1, 16);
        }
        sfor<0, 4>([&](auto I) { gld16c<(I.v & 1) * 512>(af[I.v >> 1][8 + (I.v & 1)], ub + (I.v >> 1) * 8192, uof); });
        sfor<0, 8>([&](auto I) { gld16c<(I.v & 1) * 2048>(pf[0][I.v], pb + w * 16384 + (I.v >> 1) * 4096, lane * 16); });
        gld16c<0>(pf[0][8], pb + 65536 + w * 4096, lane * 16); gld16c<2048>(pf[0][9], pb + 65536 + w * 4096, lane * 16);
        sfor<0, 8>([&](auto I) { gld16c<(I.v & 1) * 2048 + 1024>(pf[1][I.v], pb + w * 16384 + (I.v >> 1) * 4096, lane * 16); });
        gld16c<1024>(pf[1][8], pb + 65536 + w * 4096, lane * 16); gld16c<3072>(pf[1][9], pb + 65536 + w * 4096, lane * 16);
        WAIT10(10, pf[0]); WAIT10(10, af[0]); WAIT10(10, af[1]);
        float chk = 0.f;
#pragma unroll
        for (int tt = 0; tt < 2; ++tt) {
            if (tt) WAIT10(0, pf[1]);
            f32x4 c0 = f32x4{0.f, 0.f, 0.f, 0.f}, c1 = c0;
#pragma unroll
            for (int i = 0; i < 10; ++i) { c0 = mfma16(af[0][i], pf[tt][i], c0); c1 = mfma16(af[1][i], pf[tt][i], c1); }
            *LDSP(f32x4, wa + tt * 2304) = c0;
            *LDSP(f32x4, wa + tt * 2304 + 64) = c1;
            const f32x4 c = c0 + c1;
            chk += (c[0] + c[1]) + (c[2] + c[3]);
            PIN();
        }
        if (it >= 4 || __builtin_amdgcn_ballot_w64((__float_as_uint(chk) & 0x7fffffffu) > 0x7f800000u) == 0) break;
        if (s < NSEG - 1) nap(60);
    }
    barrier();
    const u32 ra = lds + (u32)(tid >> 3) * 144 + (tid & 7) * 16;
    const float gt = __uint_as_float(gv), g1 = rbf(1.f - gt);
    const f32x4 z = (*LDSP(f32x4, ra) + *LDSP(f32x4, ra + 4608)) + (*LDSP(f32x4, ra + 2 * 4608) + *LDSP(f32x4, ra + 3 * 4608));
    u32x2 ov;
#pragma unroll
    for (int j = 0; j < 2; ++j) {
        const u32 b = bb[j], a = cvtpk(z[2 * j], z[2 * j + 1]);
        const u32 ga = cvtpk(gt * __uint_as_float(a << 16), gt * __uint_as_float(a & 0xffff0000u));
        const u32 gb = cvtpk(g1 * __uint_as_float(b << 16), g1 * __uint_as_float(b & 0xffff0000u));
        ov[j] = cvtpk(__uint_as_float(ga << 16) + __uint_as_float(gb << 16), __uint_as_float(ga & 0xffff0000u) + __uint_as_float(gb & 0xffff0000u));
    }
    *(u32x2*)(p.out + tok * 1280 + col * 2) = ov;
}

DI float dot2bf(u32 a, u32 b, float c) { return __builtin_amdgcn_fdot2_f32_bf16(__builtin_bit_cast(bf16x2, a), __builtin_bit_cast(bf16x2, b), c, false); }
DI void battn_task(const Ptrs& p, int t0, u32 lds, int w) {
    const int lane = lanex(), tid = 64 * w + lane, tl = tid & 31, t = t0 + tl;
    const char* const qkv = p.ws + OFF_QKVB + (size_t)(t0 >> 5) * 122880;
    {
        u32 so[3];
#pragma unroll
        for (int i = 0; i < 3; ++i) {
            const int e = 64 * i + lane, sl = e % 48;
            so[i] = (u32)((e / 48) * 512 + (sl < 16 ? (t0 ? 0 : 122880) + 256 + sl * 16 : 122880 - 256 + sl * 16));
        }
#pragma unroll
        for (int i = 0; i < 30; ++i) { glds16c(qkv - 122880 + 40960 + (10 * w + i / 3) * 2048, so[i % 3], lds + (30 * w + i) * 1024); gap<SMB>(-1); }
    }
    u32x4 q[2][5], xv[10], gv[10];
    sfor<0, 10>([&](auto I) { gld16c<(I.v % 5) * 512>(q[I.v / 5][I.v % 5], qkv + (I.v / 5) * 20480, (u32)((tid >> 5) * 2560 + tl * 16)); gap<SMB>(-1); });
    sfor<0, 10>([&](auto I) { gld16<I.v * 128>(xv[I.v], p.x + (size_t)t0 * 1280, (u32)((tid >> 3) * 1280 + (tid & 7) * 16)); gld16<I.v * 128>(gv[I.v], p.gw, (u32)((tid & 7) * 16)); gap<SMB>(-1); });
    sfor<0, 10>([&](auto I) { waitv<0>(q[I.v / 5][I.v % 5]); waitv<0>(xv[I.v]); waitv<0>(gv[I.v]); });
    barrier();
    {
        float a = 0.f;
#pragma unroll
        for (int i = 0; i < 10; ++i)
#pragma unroll
            for (int j = 0; j < 4; ++j) {
                const u32 r = cvtpk(__uint_as_float(xv[i][j] << 16) * __uint_as_float(gv[i][j] << 16), __uint_as_float(xv[i][j] & 0xffff0000u) * __uint_as_float(gv[i][j] & 0xffff0000u));
                a += __uint_as_float(r << 16) + __uint_as_float(r & 0xffff0000u);
            }
        a += __shfl_xor(a, 1); a += __shfl_xor(a, 2); a += __shfl_xor(a, 4);
        const float z = rbf(a);
        if ((tid & 7) == 0) gst4c(p.ws + OFF_GATE, (u32)((t0 + (tid >> 3)) * 4), __float_as_uint(rbf(1.0f / (1.0f + __expf(-z)))));
    }
#pragma unroll
    for (int it = 0; it < 2; ++it) {
        const int m = (tid >> 5) + 8 * it;
        u32 ka = lds + (u32)(m * 3840 + (2 + tl) * 16), va = ka + 61440;
        asm volatile("" : "+v"(ka), "+v"(va));
        u32x4 k[5][8];
#pragma unroll
        for (int i = 0; i < 40; ++i) k[i >> 3][i & 7] = ldsr16(ka + (i >> 3) * 768 + (7 - (i & 7)) * 32);
        PIN();
        float lg[2][8], mx[2] = {-3.0e38f, -3.0e38f};
#pragma unroll
        for (int i = 0; i < 16; ++i) lg[i >> 3][i & 7] = 0.f;
#pragma unroll
        for (int i = 0; i < 5; ++i) {
#pragma unroll
            for (int j = 0; j < 4; ++j)
#pragma unroll
                for (int wv = 0; wv < 8; ++wv) lg[4 * i + j >= 10][wv] = dot2bf(q[it][i][j], k[i][wv][j], lg[4 * i + j >= 10][wv]);
            PIN();
#pragma unroll
            for (int wv = 0; wv < 8; ++wv) k[i][wv] = ldsr16(va + i * 768 + (7 - wv) * 32);
            PIN();
        }
#pragma unroll
        for (int i = 0; i < 16; ++i) {
            float& l = lg[i >> 3][i & 7];
            l = rbf(rbf(l) * 0.22360679774997896f);
            asm volatile("" : "+v"(l));
            l = t - 2 * (i & 7) >= 0 ? l : -3.0e38f;
            mx[i >> 3] = fmaxf(mx[i >> 3], l);
        }
        u32 pk[2][4];
#pragma unroll
        for (int u = 0; u < 2; ++u) {
            float sum = 0.f;
#pragma unroll
            for (int wv = 0; wv < 8; ++wv) { lg[u][wv] = __builtin_amdgcn_exp2f((lg[u][wv] - mx[u]) * 1.4426950408889634f); sum += lg[u][wv]; }
            const float inv = __builtin_amdgcn_rcpf(sum);
#pragma unroll
            for (int wv = 0; wv < 4; ++wv) pk[u][wv] = cvtpk(lg[u][2 * wv] * inv, lg[u][2 * wv + 1] * inv);
        }
#pragma unroll
        for (int i = 0; i < 5; ++i) {
            float o[8];
#pragma unroll
            for (int d = 0; d < 8; ++d) o[d] = 0.f;
#pragma unroll
            for (int wp = 0; wp < 4; ++wp) {
                const u32x4 v0 = k[i][2 * wp], v1 = k[i][2 * wp + 1];
#pragma unroll
                for (int j = 0; j < 4; ++j) {
                    const u32 pw = pk[4 * i + j >= 10][wp];
                    o[2 * j] = dot2bf(__builtin_amdgcn_perm(v1[j], v0[j], 0x05040100u), pw, o[2 * j]);
                    o[2 * j + 1] = dot2bf(__builtin_amdgcn_perm(v1[j], v0[j], 0x07060302u), pw, o[2 * j + 1]);
                }
            }
            gst16c(p.ws + OFF_OB + (size_t)(t0 >> 5) * 40960 + 512 * i, (u32)(m * 2560 + tl * 16), u32x4{cvtpk(o[0], o[1]), cvtpk(o[2], o[3]), cvtpk(o[4], o[5]), cvtpk(o[6], o[7])});
        }
    }
}

#define XPF 1
#define PFQ 0
#define PFN 8
#define OLAG 3
constexpr int QN = 176, QF = 19;
struct QTab { u16 e[QN]; int n, nf; u32 tt[64]; };
typedef const __attribute__((address_space(4))) int* KI;
constexpr int QT_OFF = (88 + 48) / 4;
DI int pick_task(char* fl, KI k, int gr) {
    u32 r;
    asm volatile("s_mov_b64 exec, 1\n\ts_nop 3\n\tglobal_atomic_add %0, %1, %2, %3 sc0 sc1\n\ts_mov_b64 exec, -1\n\ts_waitcnt vmcnt(0)"
                 : "=&v"(r) : "v"((F_QH + gr) * 128), "v"(1), "s"(fl) : "memory");
    const int x = uni((int)r) + 1 + QF;
    if (x >= k[QT_OFF + QN / 2]) return -1;
    const u32 e2 = (u32)k[QT_OFF + (x >> 1)];
    return (int)(x & 1 ? e2 >> 16 : e2 & 0xffffu);
}
__global__ __launch_bounds__(256, 1) void k_main(Ptrs p0, ChainCfg cfg, QTab tab) {
    extern __shared__ __attribute__((aligned(16))) char smem[];
    u32 lds = (u32)(size_t)LDSP(char, smem);
#define HALF 0
    int n = HALF ? (blockIdx.x & 4) * 32 + (int)(blockIdx.x >> 3) * 4 + (blockIdx.x & 3) : blockIdx.x, gr = n & 7;
    int w = uni(threadIdx.x >> 6);
    {
        u64 pc; int len;
        asm volatile("s_getpc_b64 %0\n.Lkm_first:\n\ts_mov_b32 %1, .Lkm_last-.Lkm_first\n\ts_max_i32 %1, %1, .Lkm_lu-.Lkm_first\n\ts_max_i32 %1, %1, .Lkm_lv-.Lkm_first\n\t"
                     "s_max_i32 %1, %1, .Lkm_g2-.Lkm_first\n\ts_max_i32 %1, %1, .Lkm_g4-.Lkm_first" : "=s"(pc), "=s"(len) : : "scc");
        if (w == 3) pfl((const char*)pc, (u32)min((int)(blockIdx.x >> 3) * 8192 + (int)(threadIdx.x & 63) * 128, max(len, 0)), lds);
    }
    karg_warm();
    if (n < NH) { score_role(p0, cfg, n, lds, w); return; }
    if (n < NS) { proj_role(p0, cfg, n - NH, lds, w); return; }
    const bool isv = n < NS + NVB;
#define qslot (lds + GRING * GBUF)
    int sdw = -1;
    int en = isv ? Q_VD + (n & 24) : tab.e[(n - NS - NVB) >> 3] & 511;
    u32 phi0 = 0;
    Sync y;
    if (XPF && n < 160 && w >= 2) pfl(p0.x, (u32)min(((n - NS) >> 3) * 128 + (w & 1) * 64 + lanex(), 1279) * 128, lds);
    if (n >= 160) {
        const int lane = lanex();
        if (n >= 192 && (n & 8)) {
            init_task(p0, (n & 7) + ((n - 192) >> 4) * 8, lds, w);
            barrier();
#pragma unroll
            for (int i = 0; i < 3; ++i) pfl(p0.wqkvb, (u32)min((((n - 192) >> 4) * 8 + (n & 7)) * 768 + i * 256 + w * 64 + lane, 19199 + 0) * 128, lds);
            pfl(p0.wob, (u32)min((((n - 192) >> 4) * 8 + (n & 7)) * 256 + w * 64 + lane, 6399) * 128, lds);
        } else {
            const int h = n < 192 ? n & 31 : (n & 7) + ((n - 192) >> 4) * 8;
            xcopy_task(p0, h + (n < 192 ? 0 : 32), w);
#pragma unroll
            for (int i = 0; i < PFN; ++i) {
                if (n >= 192) break;
                const int l = 64 * min(i * 4 + w, 29) + lane;
                pfl(p0.wo + (size_t)h * 245760, (u32)(l * 128), lds);
                if (PFQ) pfl(p0.wq + h * 384, (u32)((l / 3) * 12288 + (l % 3) * 128), lds);
            }
        }
        waitvm<0>();
        if (w == 0) {
            sync_ld(y, p0.ws);
            waitvm<0>();
            *LDSP(u32, qslot) = (u32)sync_end(y, p0.ws, phi0, true);
            xcopy_wait(p0.ws, sync_tag(y.e));
        }
        barrier();
        sdw = uni((int)*LDSP(u32, qslot));
        barrier();
    }
    while (true) {
        typedef char* const __attribute__((address_space(4)))* KP;
        KP kp = (KP)__builtin_amdgcn_kernarg_segment_ptr();
        asm volatile("" : "+s"(kp), "+s"(lds), "+s"(w), "+s"(gr), "+s"(n));
        __builtin_assume(w >= 0 && w < 4 && gr >= 0 && gr < 8);
        const Ptrs p{kp[0], kp[1], kp[2], kp[3], kp[4], kp[5], kp[6], kp[7], kp[8], kp[9], kp[10]};
        const int ty = en & 7, gate = en >> 9;
        const bool fu = ty == Q_PX && (en >> 3 & 63) < 8;
        const int ix = fu ? (en & 504) + gr : en >> 3 & 63, sf = ((KI)kp)[QT_OFF + QN / 2 + 1] & 255;
        char* const fl = p.ws + OFF_ST;
        const bool raw = sdw < 0;
        int t0 = gate - 1, f1 = F_ONE, t1 = 0, f2 = F_ONE, t2 = 0, f3 = F_ONE, t3 = 0, dt = cfg.T2;
        const int os = ix >= 40 ? 44 + (8 * ix + gr) / 20 : (8 * ix + gr) / 5;
        const int bb = ty == Q_BO ? (8 * ix + gr) >> 2 : os;
        if (ty == Q_BA) { f1 = F_B + (gr & RM) | ix << 16; t1 = 5; f2 = F_B + (gr & RM) | max(ix - 1, 0) << 16; t2 = 5; }
        if (ty == Q_BO) { f1 = F_B + (gr & RM) | (8 + bb) << 16; t1 = 8; }
        if (ty == Q_PX) { f1 = F_Q + pblk(ix) * 8 + (fu ? gr & RM : gr) | fu << 16; t1 = fu ? 32 : 4; if (fu) t0 += gr * (((KI)kp)[QT_OFF + QN / 2 + 1] >> 8) >> 3; if (ix < PXT) t0 = max(t0, ix + 1); }
        if (ty == Q_OUT) { f3 = F_B + (gr & RM) | (16 + (os >> 3)) << 16; t3 = 4; if (os < 60) { f1 = F_PX + os; t1 = 8; t0 = max(t0, os + (os < PXT ? OLAG : 1)); if (os >= PXT) dt = cfg.s1; } else { f1 = F_VD + 64 + (gr & RM); t1 = 32; } }
        const bool skip = fu ? ix >= sf : ty == Q_OUT && ix < 40 && (os < sf || os >= 60);
        if ((gate || ty >= Q_BA) && !skip) wg_wait(fl, w, F_CUR + gr, t0, f1, t1, f2, t2, f3, t3, dt, false);
        int sig = F_END, nrep = 1;
        if (skip) {}
        else if (ty <= Q_QKVB || ty == Q_BO) {
            const bool ab = ty <= Q_KQ;
            sig = ty == Q_VD ? F_VD + (ix >> 2) * 8 : (ty == Q_KQ ? F_Q + (ix >> 2) * 8 : (ix & 4 ? F_Q : F_K + 8 * ix)) + gr;
            if (!ab) sig = F_B | (bb + (ty == Q_BO ? 16 : 0)) << 16;
            nrep = ty == Q_VD || !ab ? RM + 1 : 1;
            if (ty == Q_VD && ix < 4) { sig += n & 32 ? 4 : 0; nrep = 4; }
            run_gemm(p, ty, ab ? ix >> 2 : bb, ab ? gr + 8 * (ix & 3) : 8 * ix + gr - (ty == Q_BO ? 4 : 5) * bb, (n >> 5) & 1, raw, y, lds, w);
            if (raw) sdw = sync_end(y, p.ws, phi0, w == 0);
        } else {
            if (ty == Q_BA) {
                battn_task(p, (8 * ix + gr) * 32, lds, w);
                sig = F_B | (8 + ix) << 16; nrep = RM + 1;
            } else if (ty == Q_PX) {
                if (ix < PXT) {
#pragma nounroll
                    for (int i = fu ? 0 : gr; i < (fu ? 8 : gr + 1); ++i) px_wave<false>(p, cfg, ix, i + 8 * w, ix - cfg.fast, w, lds, fu);
                } else px_wave<true>(p, cfg, ix, gr + 8 * w, -1, w, lds, false);
                sig = F_PX + ix;
                if (fu) wg_wait(fl, w, F_ONE, 0, F_B + (gr & RM) | (16 + (ix >> 3)) << 16, 4, F_ONE, 0, F_ONE, 0, dt, false);
            }
            if (ty == Q_OUT || fu) {
                const int cg = 8 * ix + gr - 5 * os;
                if (ix >= 40 && !fu) out_tail(p, cfg, os, 8 * ix + gr - 20 * (os - 44), lds, w, sdw, gr);
                else out_task(p, fu ? ix : os, fu ? 0 : cg, fu ? 5 : cg + 1, lds, (fu ? ix : os) + 1 - cfg.fast, w, sdw);
                sig = F_END;
            }
            waitvm<0>();
        }
        barrier();
        if (w == 0 && sig != F_END) flag_add(fl, sig, nrep);
        if (w == 1 && sig >= F_Q && sig < F_N) flag_add(fl, sig - gr | 1 << 16, RM + 1);
        if (raw && !isv && w == 0) xcopy_wait(p.ws, sync_tag(y.e));
        if (isv) break;
        if (w == 0) *LDSP(int, qslot) = pick_task(fl, (KI)kp, gr);
        barrier();
        en = uni(*LDSP(int, qslot));
        if (en < 0) return;
        barrier();
    }
    value_role(p0, cfg, n - NS, sdw, phi0, lds, w);
}

#define QCFG 14, 11, 3, 5, 0, 50, 36
#define LVDEF "00>==>>>;;<<<<==;988889::::::::::::::;;;;;;;;;;;:988776666668886"
constexpr int DX = 240, DUA = 330, DUB = 40, DUC = 4;
static ChainCfg dev_cfg(ChainCfg c) { c.T = PDL; c.cR = ODL; c.s1 = TDT; c.T2 = WSLEEP; c.cR2 = DX; c.dU = DUA; c.s2 = DUB; c.T3 = DUC; c.cR3 = 4 | 4 << 8 | 0 << 16; return c; }
static QTab set_sched(QTab t, const ChainCfg c, const char* lv) {
    for (int s = 0; s < 64; ++s) {
        const int d = lv[s] - '0';
        t.tt[s] = (u32)((s < c.s1 ? c.T : (s < c.s2 ? c.T2 : c.T3)) + 4 * d) | (u32)((s < c.s1 ? c.cR : (s < c.s2 ? c.cR2 : c.cR3)) + 5 * d / 2) << 16;
    }
    t.tt[0] = (u32)270 | (u32)165 << 16;
    t.tt[1] = (u32)230 | (u32)152 << 16;
    return t;
}
#define QORD "bbqV3bK3m44bm55bm66m77m88"
#define GATES "11 13 15 17 22 24 32 36 19 22 29 42 13 17 21 26 33 41"
static QTab make_tab(int lv, int lk, int b0, int d1, int d2, int lg, int nf) {
    QTab t{};
    int n = 0, dl[QN], nq = 0;
    auto add = [&](int ty, int ix) { t.e[n] = (u16)(ty | ix << 3); dl[n++] = ty == Q_VD || ty == Q_KQ ? 8 * (ix / 4 - 1) - (ty == Q_VD ? lv : lk) - (ix >= 32 ? 0 : 0) : 99; };
    for (int i = 1; i < 4; ++i) add(Q_VD, i);
    for (int i = 0; i < 16; ++i) add(i < 4 ? Q_H : ((i & 8) ? Q_VD : Q_KQ), i < 8 ? i : 4 + (i & 1) * 4 + ((i - 8) >> 1));
    for (int i = 0; i < 4; ++i) add(Q_KQ, 8 + i);
    for (const char* c = QORD; *c; ++c) {
        if (*c == 'b') add(Q_QKVB, nq++);
        else if (*c == 'q') for (int i = 4; i < 8; ++i) add(Q_H, i);
        else if (*c == '.') dl[n - 1] |= 1 << 20;
        else if (*c == 'n') { const int b = *++c - '0'; for (int i = 0; i < 8; ++i) add(i & 1 ? Q_KQ : Q_VD, 4 * b + (i >> 1) - (i & 1) * 2); }
        else if (*c == 'h' || *c == 'e') { const int b = 4 * (c[1] - '0') + (*c == 'e') * 2; ++c; add(Q_KQ, b); add(Q_KQ, b + 1); }
        else if (*c == 'm') { const int b = c[1] - '0', b2 = c[2] - '0'; c += 2; for (int i = 0; i < 8; ++i) add(i & 1 ? Q_KQ : Q_VD, 4 * (i & 1 ? b2 : b) + ((i >> 1) ^ (i & 2 ? 0 : 0))); }
        else {
            const int ty = (*c | 32) == 'v' ? Q_VD : Q_KQ, two = *c >> 5 & 1, b = *++c - '0';
            for (int i = 0; i < (4 << two); ++i) add(ty, 4 * (b + (i & two)) + (i >> two));
        }
    }
    const int gap = 8;
    for (int i = n - 1, m = 999; i >= 0; --i) { const int pz = dl[i] > 999; m = min(8 * (pz ? dl[i] - (1 << 20) : dl[i]), m - gap - 8 * pz); dl[i] = max(m, 0); }
    auto put = [&](int ty, int ix, int g, int pos) {
        int i = n++;
        for (; dl[i - 1] > pos; --i) { t.e[i] = t.e[i - 1]; dl[i] = dl[i - 1]; }
        t.e[i] = (u16)(ty | ix << 3 | (g + 1) << 9); dl[i] = pos;
    };
    for (int i = 0; i < n; ++i) if (dl[i] >= 8) t.e[i] |= (u16)((dl[i] / 8 + 1) << 9);
    const char* gs = GATES;
    auto gt = [&](int i) { return 10 * (gs[3 * i] - '0') + gs[3 * i + 1] - '0'; };
    for (int i = 0; i < 8; ++i) put(Q_BA, i, gt(i), 8 * gt(i));
    for (int i = 0; i < 4; ++i) put(Q_BO, i, gt(8 + i), 8 * gt(8 + i));
    for (int i = 0; 8 * i < nf; ++i) put(Q_PX, i, gt(12 + i), 8 * (max(gt(12 + i), 8 * i + 1) + b0));
    for (int s = nf; s < NSEG; ++s) { const int g = s < PXT ? s + 1 : max(s - 3, lg); put(Q_PX, s, g, 8 * g); }
    for (int x = 5 * nf / 8; x < 38; ++x) { const int os = (8 * x + 4) / 5; put(Q_OUT, x, 0, 8 * (os + (os < PXT ? OLAG : 1))); }
    for (int x = 40; x < 50; ++x) { const int g = max(44 + 8 * x / 20 - 3, lg); put(Q_OUT, x, g, 8 * g + 4); }
    t.n = n; t.nf = nf | d1 << 8;
    return t;
}

extern "C" void kernel_launch(void* const* d_in, const int* in_sizes, int n_in, void* d_out, int out_size, void* d_ws,
                              size_t ws_size, hipStream_t stream) {
    Ptrs p{(const char*)d_in[0], (const char*)d_in[1], (const char*)d_in[2], (const char*)d_in[3], (const char*)d_in[4], (const char*)d_in[5],
           (const char*)d_in[8], (const char*)d_in[9], (const char*)d_in[10], (char*)d_out, (char*)(((size_t)d_ws + 0x1fffff) & ~(size_t)0x1fffff)};
    (void)hipFuncSetAttribute((const void*)k_main, hipFuncAttributeMaxDynamicSharedMemorySize, S_LDS);
    k_main<<<256, 256, S_LDS, stream>>>(p, dev_cfg(ChainCfg{CFG}), set_sched(make_tab(QCFG), ChainCfg{CFG}, LVDEF));
    static_assert(sizeof(Ptrs) + sizeof(ChainCfg) + sizeof(QTab) == TT_OFF + 256, "");
}
```

```cpp
#include <hip/hip_runtime.h>
#pragma clang diagnostic ignored "-Wint-to-pointer-cast"

typedef unsigned short u16;
typedef unsigned int u32;
typedef unsigned long long u64;
typedef __bf16 bf16x8 __attribute__((ext_vector_type(8)));
typedef __bf16 bf16x16 __attribute__((ext_vector_type(16)));
typedef short s16x4 __attribute__((ext_vector_type(4)));
typedef float f32x4 __attribute__((ext_vector_type(4)));
typedef unsigned u32x4 __attribute__((ext_vector_type(4)));
typedef unsigned u32x2 __attribute__((ext_vector_type(2)));
typedef unsigned u32x8 __attribute__((ext_vector_type(8)));
typedef unsigned u32x16 __attribute__((ext_vector_type(16)));
typedef unsigned u32x32 __attribute__((ext_vector_type(32)));
#define DI __device__ __forceinline__
#define LDSP(T, a) ((__attribute__((address_space(3))) T*)(a))

constexpr int S_LEN = 2048, DM = 640, NH = 32, DH = 192, NSEG = 64, HD = NH * DH;
#define NSAMP 4
constexpr int NREG = 13;
constexpr int NV = 80, NVB = 40;
constexpr int NS = 64;
#define CFG 154, 103, 100, 20, 57, 16, 154, 103, 52, 154, 103, 6

constexpr size_t U_STRIDE = NV * 4096, D_STRIDE = NV * 16384;
constexpr size_t HO_STRIDE = 65536;
constexpr size_t OFF_ST = NSEG * HO_STRIDE;
enum { F_ACK = 0, F_ONE = 3, F_VD = 16, F_QH = 96, F_B = 104, F_CUR = 128, F_PROG = 136 - 8 * 62, F_UD = 152, F_PX = 160, F_K = 224, F_END = 512, F_Q = 520, F_N = 592 };
constexpr size_t OFF_LAT = OFF_ST + F_N * 128;
constexpr size_t OFF_KX = 5u << 20;
constexpr size_t OFF_QX = OFF_KX + (size_t)S_LEN * HD * 2;
constexpr size_t OFF_D = OFF_QX + (size_t)S_LEN * HD * 2;
constexpr size_t OFF_U = (OFF_D + NSEG * D_STRIDE + 0x1fffff) & ~(size_t)0x1fffff;
constexpr size_t OFF_KM = OFF_U + (22u << 20);
constexpr size_t OFF_PX = OFF_KM + (size_t)NSEG * NH * 3072;
constexpr size_t OFF_QKVB = OFF_PX + (size_t)NSEG * 81920;
constexpr size_t OFF_OB = OFF_QKVB + (size_t)S_LEN * 1920 * 2;
constexpr size_t OFF_BOUT = OFF_OB + (size_t)S_LEN * DM * 2;
constexpr size_t OFF_GATE = OFF_BOUT + (size_t)S_LEN * DM * 2;
constexpr size_t OFF_XF = OFF_GATE + S_LEN * 4;
constexpr size_t WS_END = OFF_XF + (size_t)S_LEN * DM * 2;
static_assert(WS_END + (2u << 20) <= (256u << 20), "workspace");

typedef __bf16 bf16x2 __attribute__((ext_vector_type(2)));
typedef float f32x2 __attribute__((ext_vector_type(2)));
DI u32 cvtpk(float lo, float hi) { return __builtin_bit_cast(u32, __builtin_convertvector(f32x2{lo, hi}, bf16x2)); }
DI float rbf(float f) { return __uint_as_float(cvtpk(f, f) << 16); }
DI f32x4 mfma16(u32x4 a, u32x4 b, f32x4 c) {
    return __builtin_amdgcn_mfma_f32_16x16x32_bf16(__builtin_bit_cast(bf16x8, a), __builtin_bit_cast(bf16x8, b), c, 0, 0, 0);
}
DI u32x2 trread(u32 a) {
    return __builtin_bit_cast(u32x2, __builtin_amdgcn_ds_read_tr16_b64_v4i16(LDSP(s16x4, a)));
}
DI void glds16c(const char* sbase, u32 voff, u32 lds) {
    u32 sv;
    asm volatile("s_mov_b32 %0, m0\n\ts_mov_b32 m0, %3\n\ts_nop 0\n\tglobal_load_lds_dwordx4 %1, %2 sc1\n\ts_mov_b32 m0, %0"
                 : "=&s"(sv) : "v"(voff), "s"(sbase), "s"(lds) : "memory");
}
template <int IMM>
DI void gld16(u32x4& r, const char* sbase, u32 voff) {
    asm volatile("s_nop 4\n\tglobal_load_dwordx4 %0, %1, %2 offset:%3" : "=v"(r) : "v"(voff), "s"(sbase), "i"(IMM) : "memory");
}
DI void gst16(char* sbase, u32 voff, u32x4 v) {
    asm volatile("global_store_dwordx4 %0, %1, %2 sc1\n\ts_nop 1" : : "v"(voff), "v"(v), "s"(sbase) : "memory");
}
#define OST " nt"
DI void gst16p(char* sbase, u32 voff, u32x4 v) {
    asm volatile("s_nop 4\n\tglobal_store_dwordx4 %0, %1, %2" OST "\n\ts_nop 1" : : "v"(voff), "v"(v), "s"(sbase) : "memory");
}
DI void gst8(char* sbase, u32 voff, u32x2 v) {
    asm volatile("global_store_dwordx2 %0, %1, %2 sc1\n\ts_nop 1" : : "v"(voff), "v"(v), "s"(sbase) : "memory");
}
template <int N>
DI void waitvm() { asm volatile("s_waitcnt vmcnt(%0)" : : "i"(N) : "memory"); }
DI void barrier() { asm volatile("s_waitcnt lgkmcnt(0)\n\ts_barrier" : : : "memory"); }
#define PIN() __builtin_amdgcn_sched_barrier(0)
DI int uni(int v) { return __builtin_amdgcn_readfirstlane(v); }
DI int lanex() {
    int t;
    asm volatile("v_mbcnt_lo_u32_b32 %0, -1, 0\n\tv_mbcnt_hi_u32_b32 %0, -1, %0" : "=v"(t));
    __builtin_assume(t >= 0 && t < 64);
    return t;
}
DI void flag_ld(u32& r, const char* fl, int idx) {
    asm volatile("s_nop 4\n\tglobal_load_dword %0, %1, %2 sc1" : "=v"(r) : "v"((idx & 0xffff) * 128 + (idx >> 14)), "s"(fl) : "memory");
}
DI void flag_ldm(u32& r, const char* fl, int idx, bool on) {
    asm volatile("v_mov_b32 %0, 1000\n\ts_mov_b32 exec_lo, %3\n\ts_mov_b32 exec_hi, 0\n\tglobal_load_dword %0, %1, %2 sc1\n\ts_mov_b64 exec, -1" : "=&v"(r) : "v"(idx * 128), "s"(fl), "s"(uni(on ? 1 : 0)) : "memory");
}
DI void flag_wait(const char* fl, int idx, int target) {
#pragma nounroll
    for (int it = 0; it < (1 << 16); ++it) {
        u32 v;
        flag_ld(v, fl, idx);
        asm volatile("s_waitcnt vmcnt(0)" : "+v"(v) : : "memory");
        if (uni((int)(v + 1u)) >= target) break;
        __builtin_amdgcn_s_sleep(6);
    }
}
DI void flag_add(char* fl, int idx, int nrep) {
    const int lane = lanex();
    if (lane < nrep) asm volatile("s_nop 4\n\tglobal_atomic_add %0, %1, %2 sc1" : : "v"(((idx & 0xffff) + lane) * 128 + (idx >> 14)), "v"(1), "s"(fl) : "memory");
}
DI void acquire() { asm volatile("buffer_inv sc1\n\ts_waitcnt vmcnt(0)" : : : "memory"); }

template <int V> struct IC { static constexpr int v = V; };
template <int B, int E, class Fn>
DI void sfor(Fn&& fn) { if constexpr (B < E) { fn(IC<B>{}); sfor<B + 1, E>(fn); } }
enum { T_QK = 0, T_VD = 1, T_BL = 3 };
constexpr int GBUF = 24576, GRING = 6;
struct GemmArgs {
    const char *w1, *w1b;
    const char* w2;
    const char* x;
    char *out, *outb;
    int ldw;
    char* sig;
    int type, np1, tn;
    int sV, sP, sQ;
    int xb;
};
DI const char* unip(const char* p) {
    const u64 v = (u64)p;
    return (const char*)(((u64)(u32)uni((int)(v >> 32)) << 32) | (u32)uni((int)v));
}
template <int Z>
DI void mf1(f32x4& c, const u32x4& a, const u32x4& b) {
    if constexpr (Z) asm volatile("v_mfma_f32_16x16x32_bf16 %0, %1, %2, 0" : "=&v"(c) : "v"(a), "a"(b));
    else asm volatile("v_mfma_f32_16x16x32_bf16 %0, %1, %2, %0" : "+v"(c) : "v"(a), "a"(b));
}
template <int Z>
DI void mf2(f32x4& c, const u32x4& a, const u32x4& b) {
    if constexpr (Z) asm volatile("v_mfma_f32_16x16x32_bf16 %0, %1, %2, 0" : "=&v"(c) : "v"(a), "v"(b));
    else asm volatile("v_mfma_f32_16x16x32_bf16 %0, %1, %2, %0" : "+v"(c) : "v"(a), "v"(b));
}
template <int IMM>
DI void gld16a(u32x4& r, const char* sbase, u32 voff) {
    asm volatile("global_load_dwordx4 %0, %1, %2 offset:%3 sc1" : "+a"(r) : "v"(voff), "s"(sbase), "i"(IMM) : "memory");
}
DI void setm0(u32 v) { asm volatile("s_mov_b32 m0, %0" : : "s"(v) : "memory"); }
template <int IMM>
DI void gldsm(const char* sbase, u32 voff) {
    asm volatile("global_load_lds_dwordx4 %0, %1 offset:%2" : : "v"(voff), "s"(sbase), "i"(IMM) : "memory");
}
DI void gst16n(char* sbase, u32 voff, u32x4 v) {
    asm volatile("global_store_dwordx4 %0, %1, %2 sc1" : : "v"(voff), "v"(v), "s"(sbase) : "memory");
}
template <int N>
DI void waitbar() { asm volatile("s_waitcnt vmcnt(%0)\n\ts_barrier" : : "i"(N) : "memory"); }
#define LGKM(n) __builtin_amdgcn_s_waitcnt(0xC07F | ((n) << 8))

DI int xhalf() { u32 x; asm volatile("s_getreg_b32 %0, hwreg(HW_REG_XCC_ID)" : "=s"(x)); return (int)((x >> 2) & 1); }
struct Sync { u32x2 r, g, e; };
DI void sync_ld(Sync& y, const char* ws) {
    asm volatile("s_nop 4\n\tglobal_load_dwordx2 %0, %3, %5 sc1\n\tglobal_load_dwordx2 %1, %4, %5 offset:512 sc1\n\tglobal_load_dwordx2 %2, %4, %5 offset:1024 sc1"
                 : "=&v"(y.r), "=&v"(y.g), "=&v"(y.e) : "v"((lanex() & 31) * 8), "v"(0), "s"(ws + OFF_LAT) : "memory");
}
DI u64 sync_tag(u32x2 e) { return (((u64)e[1] << 32 | e[0]) + 1) << 1; }
DI int sync_end(Sync& y, const char* ws, u32& phi, bool ack) {
    asm volatile("" : "+v"(y.r), "+v"(y.g), "+v"(y.e));
#pragma nounroll
    for (int it = 0; it < (1 << 16); ++it) {
        const u64 t = sync_tag(y.e);
        if (__builtin_amdgcn_ballot_w64((y.r[0] & ~1u) != (u32)t || y.r[1] != (u32)(t >> 32) || y.g[0] != (u32)t) == 0) break;
        __builtin_amdgcn_s_sleep(24);
        sync_ld(y, ws);
        asm volatile("s_waitcnt vmcnt(0)" : "+v"(y.r), "+v"(y.g), "+v"(y.e) : : "memory");
    }
    if (ack) flag_add((char*)ws + OFF_ST, F_ACK, 1);
    phi = (u32)uni((int)y.g[1]);
    const u32 b = (u32)__builtin_amdgcn_ballot_w64(y.r[0] & 1), all = __builtin_popcount(b) > 16;
    u32 q = 0;
#pragma unroll
    for (int r = 0; r < NREG; ++r) {
        const u32 c = r < 2 ? __builtin_popcount(b & (0x55u << r)) + all : 2 * __builtin_popcount((b >> (4 + 2 * r)) & 3) + all;
        q |= ((c > 2) ^ (u32)((r < 2 ? r : r + (OFF_U >> 21)) & 1)) << r;
    }
    return uni((int)(q ^ 3u * xhalf()));
}
DI void pace(int on) {
    asm volatile("s_cmp_ge_i32 %0, 0\n\ts_cbranch_scc1 .Lp%=\n\ts_waitcnt vmcnt(0)\n.Lp%=:" : : "s"(on) : "memory", "scc");
}
#define SMO 48
#define RM 7
#define BLS 1
#define EPG 1
#define PXT 58
#define PDX 40
#define SMB 48
#define SMP SMO
template <int C>
DI void gap(int on) {
    if constexpr (C > 0) asm volatile("s_cmp_ge_i32 %0, 0\n\ts_cbranch_scc1 .Lg%=\n\ts_nop %1\n\ts_nop %2\n\ts_nop %3\n.Lg%=:" : : "s"(on), "n"(C / 12 - 1), "n"(C / 12 - 1), "n"(C / 12 - 1) : "memory", "scc");
}
DI void early_add(char* ctr, int c) {
    asm volatile("s_cmp_lg_u32 %1, 1\n\ts_cbranch_scc1 .Le%=\n\ts_mov_b64 exec, 1\n\ts_nop 1\n\tglobal_atomic_add %2, %3, %0 sc1\n\ts_mov_b64 exec, -1\n.Le%=:"
                 : : "s"(ctr), "s"(c), "v"(0), "v"(1) : "memory", "scc");
}
#define XL 2
template <int NT>
DI void gemm_task(const GemmArgs& g, Sync& y, const char* ws, u32 lds, int w) {
    constexpr int FD = 5, NX = 2 * NT, OPS = NX + 6, BI = 24 - FD;
    const int lane = lanex();
    const int n = lane & 15, q = lane >> 4, qq = (lane >> 2) & 3, pp = lane & 3;
    const bool p2 = g.type == T_VD;
    const int rw = lane >> 3, sp = (lane >> 1) & 3;
    const u32 v2b = (u32)(rw * 1280 + 16 * (lane & 1) + 5120);
    auto v2o = [&](int i) { return v2b + (u32)(((sp ^ (rw >> 1)) << 5) + i * 9216); };
    u32 wv[6];
#pragma unroll
    for (int i = 0; i < 6; ++i)
        wv[i] = (u32)((rw + 8 * (i / 3)) * g.ldw + ((sp ^ (((rw >> 1) & 1) + 2 * (i / 3))) << 5) + 16 * (lane & 1) + (i % 3) * 128 + 5120 - 1024 * i);
    const char* const wp2 = g.w2 + (size_t)(48 * w) * 1280 - 2048;
    const char* wp = unip(g.w1 + (size_t)(16 * w) * g.ldw - 2048);
    int wstep = uni(64 * g.ldw);
    u32 wdst = lds + (u32)w * 6144 + 3072;
    const u32 wdlast = wdst + (GRING - 1) * GBUF;
    auto wring = [&]() { wdst = wdst == wdlast ? wdst - (GRING - 1) * GBUF : wdst + GBUF; };
    auto wload = [&](int i) {
        setm0(wdst);
        wring();
        asm volatile("s_nop 0");
        sfor<0, 6>([&](auto I) { gldsm<1024 * (I.v - 3)>(wp, wv[I.v]); });
        wp += wstep;
    };
    u32x4 vf[NT][6];
    u32x4 fr[FD + 1];
    u32x4 xr[5][NX];
    auto xtie = [&](u32x4 (&x)[NX]) {
#pragma unroll
        for (int i = 0; i < NX; i += 4) asm volatile("" : "+a"(x[i]), "+a"(x[i + 1]), "+a"(x[i + 2]), "+a"(x[i + 3]));
    };
#pragma unroll
    for (int i = 0; i < 5 * NX; ++i) asm volatile("" : "=a"(xr[i / NX][i % NX]));
    u32x2 flo;
    u32 c[4], slot = 0;
    int sd = GBUF;
    auto sring = [&]() { sd = slot == (GRING - 1) * GBUF ? -(GRING - 1) * GBUF : GBUF; slot += (u32)sd; };
    {
        u32 xo[NT];
#pragma unroll
        for (int tt = 0; tt < NT; ++tt) {
            const int tok = (g.type == T_VD) ? 8 * (n >> 2) + 4 * (tt & 1) + (n & 3) : 16 * (tt & 1) + n;
            xo[tt] = (u32)((NT / 2 * w + (tt >> 1)) * 40960 + (g.xb ? q * 512 + tok * 16 : q * 16 + tok * 1280));
        }
        f32x4 acc[12][NT];
        const bool rm = NT == 4 && g.type == T_BL;
        const int sw = (qq >> 1) + 2 * (q & 1);
#pragma unroll
        for (int t = 0; t < 4; ++t)
            c[t] = lds + q * 3072 + qq * 128 + (rm ? ((((t & 2) + (pp >> 1)) ^ sw) << 5) + 16 * (pp & 1) + 8 * ((pp >> 1) ^ (t & 1)) : ((t ^ sw) << 5) + 8 * pp);
        auto rlo = [&](int f) { flo = trread(c[f & 3] + (f / 12) * 12288 + ((f % 12) >> 2) * 1024); };
        auto rhi = [&](int f) {
            const u32x2 hi = trread(c[f & 3] + (f / 12) * 12288 + ((f % 12) >> 2) * 1024 + 512);
            fr[f % (FD + 1)] = u32x4{flo[0], flo[1], hi[0], hi[1]};
        };
        const int xk = uni(g.xb ? 2048 : 64);
        const char *xp = g.x, *xq = xp + xk;
        sfor<0, 4>([&](auto J) {
            if constexpr (J.v < XL) { sfor<0, NX>([&](auto I) { gld16a<0>(xr[J.v][I.v], I.v & 1 ? xq : xp, xo[I.v >> 1]); }); xp += 2 * xk; xq += 2 * xk; }
            wload(J.v);
        });
        waitbar<18 + (XL - 1) * NX>();
        xtie(xr[0]);
#pragma unroll
        for (int i = 0; i < FD; ++i) { rlo(i); rhi(i); }
        char* po = g.out;
#pragma nounroll
        for (int ph = g.np1; ph > 0; --ph) {
            sfor<0, 10>([&](auto J) {
                constexpr int j = J.v;
                u32x4 (&xc)[NX] = xr[j % 5];
                u32x4 (&xn)[NX] = xr[(j + XL) % 5];
                sfor<0, 24>([&](auto I) {
                    constexpr int idx = I.v, kk = idx / 12, T = idx % 12, z = j == 0 && kk == 0, f = (idx + FD) % 24;
                    constexpr int mo = NT == 4 ? (idx < BI && idx % 4 != 2 ? idx - (idx + 1) / 4 : -1) : (idx < BI && idx % 2 == 0 ? idx / 2 : -1);
                    auto mem = [&]() {
                        if constexpr (mo >= NX) gldsm<1024 * (mo - NX - 3)>(wp, wv[mo - NX]);
                        else if constexpr (mo >= 0) gld16a<0>(xn[mo], mo & 1 ? xq : xp, xo[mo >> 1]);
                    };
                    auto misc = [&]() {
                        if constexpr (idx == 1) wring();
                        if constexpr (idx == 13) sring();
                        if constexpr (j == 1 && idx == 22) early_add(g.sig, ph + w);
                        if constexpr (idx >= 15 && idx < BI) { c[idx - 15] += (u32)sd; asm volatile("" : "+v"(c[idx - 15])); }
                        const bool more = ph > 1;
                        if constexpr (j != 5) {
                            if constexpr (idx == 20) wp += wstep;
                        } else {
                            if constexpr (idx == 19) { wp = more ? g.w1b + (size_t)(16 * w) * g.ldw - 2048 : wp2; wstep = more ? wstep : (p2 ? 128 : 0); }
                            if constexpr (idx >= 20 && idx < 23) {
#pragma unroll
                                for (int i = 2 * (idx - 20); i < 2 * (idx - 19); ++i) { wv[i] = more ? wv[i] : (p2 ? v2o(i) : 5120u); asm volatile("" : "+v"(wv[i])); }
                            }
                        }
                        if constexpr (j != 9 - XL) {
                            if constexpr (idx == 21) xp += 2 * xk;
                            if constexpr (idx == 22) xq += 2 * xk;
                        } else if constexpr (idx == 23) {
                            xp = g.x; xq = xp + xk;
#pragma unroll
                            for (int i = 0; i < NT; ++i) { xo[i] = more ? xo[i] : 0u; asm volatile("" : "+v"(xo[i])); }
                        }
                    };
                    if constexpr (idx == BI) { waitbar<6 + (XL - 1) * OPS>(); xtie(xr[(j + 1) % 5]); }
                    if constexpr (idx % 2 == 0) LGKM(6);
                    PIN();
                    const u32x4 a = fr[idx % (FD + 1)];
                    mf1<z>(acc[T][0], a, xc[kk]); PIN();
                    rlo(f);
                    if constexpr (idx == 0) setm0(wdst);
                    if constexpr (NT == 2) rhi(f);
                    PIN();
                    mf1<z>(acc[T][1], a, xc[2 + kk]); PIN();
                    if constexpr (NT == 4) {
                        rhi(f); PIN();
                        mf1<z>(acc[T][2], a, xc[4 + kk]); PIN();
                        mem(); PIN();
                        mf1<z>(acc[T][3], a, xc[6 + kk]); PIN();
                    } else { mem(); PIN(); }
                    misc();
                    PIN();
                });
            });
            asm volatile("s_nop 7\n\ts_nop 1");
            PIN();
#pragma unroll
            for (int tt = 0; tt < NT; ++tt)
#pragma unroll
                for (int ks = 0; ks < 6; ++ks)
                    vf[tt][ks] = u32x4{cvtpk(acc[2 * ks][tt][0], acc[2 * ks][tt][1]), cvtpk(acc[2 * ks][tt][2], acc[2 * ks][tt][3]),
                                       cvtpk(acc[2 * ks + 1][tt][0], acc[2 * ks + 1][tt][1]), cvtpk(acc[2 * ks + 1][tt][2], acc[2 * ks + 1][tt][3])};
            if (g.type == T_QK) {
#pragma unroll
                for (int tt = 0; tt < NT; ++tt)
#pragma unroll
                    for (int ks = 0; ks < 6; ++ks) {
                        gst16(po + (size_t)(NT / 2 * w + (tt >> 1)) * (NH * 12288) + (tt & 1) * 6144 + ks * 1024, lane * 16,
                              u32x4{vf[tt][ks][0], vf[tt][ks][2], vf[tt][ks][1], vf[tt][ks][3]});
                        if (NT == 4 && EPG) asm volatile("s_sleep %0" : : "n"(EPG));
                    }
            } else if (rm) {
                const bool sx = q >= 2;
#pragma unroll
                for (int i = 0; i < 3 * NT; ++i) {
                    const int tp = i / 6, ks = i % 6;
                    u32x4 o[2];
#pragma unroll
                    for (int j = 0; j < 4; ++j) {
                        const u32x4 &v = vf[2 * tp][ks], &u = vf[2 * tp + 1][ks];
                        if (BLS) {
                            const auto r = __builtin_amdgcn_permlane32_swap(v[j], u[j], false, false);
                            o[0][j] = r[0]; o[1][j ^ 2] = r[1];
                        } else { o[0][j] = sx ? v[j ^ 2] : v[j]; o[1][j] = sx ? u[j ^ 2] : u[j]; }
                    }
#pragma unroll
                    for (int j = 0; j < 2; ++j) {
                        gst16(po + (size_t)(2 * w + tp) * g.sV + j * (BLS ? 1024 : 256) + ks * 2048, BLS ? (u32)((q & 1) * 512 + (q >> 1) * 256 + n * 16) : (u32)(q * 512 + n * 16), o[j]);
                        if (EPG) asm volatile("s_sleep %0" : : "n"(EPG));
                    }
                }
            }
            po = g.outb;
            PIN();
        }
    }
    if (p2) {
        constexpr int HI = 2048;
        const int r = 4 * (q & 1) + qq, sw = r >> 1;
#pragma unroll
        for (int t = 0; t < 4; ++t) c[t] = lds + slot + (q >> 1) * 1024 + r * 128 + ((t ^ sw) << 5) + 8 * pp;
        sring();
        u32 so = (u32)((n >> 3) * g.sV + q * g.sQ + (n & 7) * 16);
        char* const o0 = g.out + (size_t)(NT / 2 * w) * g.sP;
        char* const o1 = o0 + g.sP;
        const int sv2 = 2 * g.sV;
        auto rlo = [&](int f) { flo = trread(c[f / 6] + (f % 6) * 4096); };
        auto rhi = [&](int f) {
            const u32x2 hi = trread(c[f / 6] + (f % 6) * 4096 + HI);
            fr[f % (FD + 1)] = u32x4{flo[0], flo[1], hi[0], hi[1]};
        };
        f32x4 a2[2][NT];
        u32 e[NT / 2][4];
        auto cv = [&](f32x4 (&a)[NT], int i) { e[i >> 2][i & 3] = cvtpk(a[i >> 1][2 * (i & 1)], a[i >> 1][2 * (i & 1) + 1]); asm volatile("" : "+v"(e[i >> 2][i & 3])); };
        auto st = [&](int pl) { gst16n(pl ? o1 : o0, so, u32x4{e[pl][0], e[pl][1], e[pl][2], e[pl][3]}); };
        PIN();
#pragma unroll
        for (int i = 0; i < FD; ++i) { rlo(i); rhi(i); }
#pragma unroll
        for (int tt = 0; tt < NT; ++tt) a2[1][tt] = f32x4{0.f, 0.f, 0.f, 0.f};
#pragma nounroll
        for (int t = 0; t < g.tn; ++t) {
            sfor<0, 24>([&](auto I) {
                constexpr int idx = I.v, cb = idx / 6, ks = idx % 6, f = (idx + FD) % 24;
                f32x4 (&ac)[NT] = a2[cb & 1];
                f32x4 (&ap)[NT] = a2[(cb & 1) ^ 1];
                constexpr int mo = cb < 2 ? (ks == 0 ? 3 * cb : (ks >= 4 ? 3 * cb + ks - 3 : -1)) : -1;
                auto mem = [&]() {
                    if constexpr (mo >= 0) gldsm<1024 * (mo - 3)>(wp, wv[mo]);
                    if constexpr (ks == 3 || (NT == 4 && ks == 2)) { if (cb > 0 || t > 0) st(NT == 4 ? ks - 2 : 0); }
                };
                auto misc = [&]() {
                    if constexpr (idx == 12) wring();
                    if constexpr (idx == 23) sring();
                    if constexpr (idx == 16) { wp += t < g.tn - 5 ? 128 : 0; }
                    if constexpr (ks == 0) { c[cb] += (u32)sd; asm volatile("" : "+v"(c[cb])); }
                    if constexpr (ks == 4) { so += (cb > 0 || t > 0) ? (u32)sv2 : 0u; asm volatile("" : "+v"(so)); }
                };
                if constexpr (idx == BI) waitbar<NT == 4 ? 38 : 28>();
                if constexpr (idx % 2 == 0) LGKM(6);
                PIN();
                const u32x4 b = fr[idx % (FD + 1)];
                mf2<ks == 0>(ac[0], vf[0][ks], b); PIN();
                rlo(f);
                if constexpr (idx == 0) setm0(wdst);
                if constexpr (NT == 2) rhi(f);
                if constexpr (NT == 4 && ks >= 1 && ks <= 3) cv(ap, ks == 1 ? 0 : (ks == 2 ? 4 : 7));
                PIN();
                mf2<ks == 0>(ac[1], vf[1][ks], b); PIN();
                if constexpr (NT == 4) {
                    rhi(f);
                    if constexpr (ks == 1 || ks == 2) cv(ap, ks == 1 ? 1 : 5);
                    PIN();
                    mf2<ks == 0>(ac[2], vf[2][ks], b); PIN();
                    mem();
                    if constexpr (ks == 1) { cv(ap, 2); cv(ap, 3); }
                    PIN();
                    mf2<ks == 0>(ac[3], vf[3][ks], b); PIN();
                    if constexpr (ks == 2) cv(ap, 6);
                } else {
                    if constexpr (ks == 1 || ks == 2) { cv(ap, 2 * ks - 2); cv(ap, 2 * ks - 1); }
                    mem(); PIN();
                }
                misc();
                PIN();
            });
            asm volatile("s_nop 7\n\ts_nop 1");
        }
        PIN();
#pragma unroll
        for (int i = 0; i < 2 * NT; ++i) cv(a2[1], i);
#pragma unroll
        for (int pl = 0; pl < NT / 2; ++pl) st(pl);
        asm volatile("s_nop 1");
    }
    if constexpr (NT == 2) asm volatile(".Lkm_g2:"); else asm volatile(".Lkm_g4:");
    sync_ld(y, ws);
    waitvm<0>();
    asm volatile("" : "+v"(y.r), "+v"(y.g), "+v"(y.e));
    sfor<0, 5>([&](auto I) { xtie(xr[I.v]); });
}

struct Ptrs {
    const char *x, *mem0, *wq, *wk, *wv, *wo, *wqkvb, *wob, *gw;
    char* out;
    char* ws;
};

enum { Q_H = 0, Q_VD = 1, Q_KQ = 2, Q_QKVB = 3, Q_BA = 4, Q_BO = 5, Q_PX = 6, Q_OUT = 7 };
DI void run_gemm(const Ptrs& p, int ty, int b, int i, int hf, bool raw, Sync& y, u32 lds, int w) {
    GemmArgs g;
    g.sV = g.sP = g.sQ = 0; g.w2 = p.wo; g.ldw = HD * 2; g.np1 = 1; g.tn = 10;
    g.sig = p.ws + OFF_ST + F_END * 128;
    const char* xf = raw ? p.x : p.ws + OFF_XF;
    g.xb = !raw;
    int s0 = 8 * b;
    if (ty == Q_VD) {
        s0 = b < 2 ? 4 * b : s0 - 8;
        g.type = T_VD; g.w1 = g.w1b = p.wv + i * DH * 2;
        g.w2 = p.wo + (size_t)i * DH * 1280;
        g.out = g.outb = p.ws + OFF_D + (size_t)s0 * D_STRIDE + i * 512;
        g.sV = 16384; g.sP = (int)D_STRIDE; g.sQ = 128;
        if (b == 0) { g.tn = 5; g.w2 += hf * 640; g.out += hf * 40 * 16384; }
    } else if (ty == Q_H) {
        s0 = 0;
        g.type = T_QK; g.w1 = g.w1b = (b ? p.wq : p.wk) + i * DH * 2;
        g.out = g.outb = p.ws + (b ? OFF_QX : OFF_KX) + (size_t)i * 12288;
        b = 0;
    } else if (ty == Q_KQ) {
        s0 = b < 2 ? 4 * b : s0 - 8;
        g.type = T_QK; g.w1 = p.wk + i * DH * 2; g.w1b = p.wq + i * DH * 2;
        g.out = p.ws + OFF_KX + (size_t)(s0 * NH + i) * 12288;
        g.outb = g.out + (OFF_QX - OFF_KX);
        g.np1 = 2;
        g.sig = p.ws + OFF_ST + (F_K + b * 32 + i) * 128;
    } else if (ty == Q_QKVB) {
        g.type = T_BL; g.w1 = p.wqkvb + i * 768; g.w1b = g.w1 + 384; g.ldw = 3840;
        g.sV = 1920 * 64; g.np1 = 2;
        g.out = p.ws + OFF_QKVB + (size_t)s0 * g.sV + i * 48 * 512; g.outb = g.out + 24 * 512;
    } else {
        const int c0 = min(i * DH, DM - DH);
        g.type = T_BL; g.w1 = g.w1b = p.wob + c0 * 2; g.ldw = 1280;
        xf = p.ws + OFF_OB;
        g.sV = 40960;
        g.out = g.outb = p.ws + OFF_BOUT + (size_t)s0 * 40960 + c0 * 64;
    }
    g.x = xf + (size_t)s0 * 40960;
    if (ty <= Q_KQ && b < 2) gemm_task<2>(g, y, p.ws, lds, w);
    else gemm_task<4>(g, y, p.ws, lds, w);
}
DI int pblk(int s) { return s < 8 ? s >> 2 : min(s >> 3, 7) + 1; }

template <int IMM>
DI void gld16c(u32x4& r, const char* sbase, u32 voff) {
    asm volatile("global_load_dwordx4 %0, %1, %2 offset:%3 sc1" : "=v"(r) : "v"(voff), "s"(sbase), "i"(IMM) : "memory");
}
DI void gst4c(char* sbase, u32 voff, u32 v) {
    asm volatile("global_store_dword %0, %1, %2 sc1" : : "v"(voff), "v"(v), "s"(sbase) : "memory");
}
template <int N>
DI void waitv(u32x4& r) { asm volatile("s_waitcnt vmcnt(%1)" : "+v"(r) : "i"(N) : "memory"); }
DI u32 mt() { return (u32)__builtin_amdgcn_s_memtime(); }
DI u32 rt() { return (u32)__builtin_amdgcn_s_memrealtime(); }
DI void spin_to(u32 tgt) { while ((int)(mt() - tgt) < 0) {} }
DI u32 max3u(u32 a, u32 b, u32 c) { return max(max(a, b), c); }
DI u32x4 ldsr16(u32 a) { return *LDSP(u32x4, a); }
DI void clocks(u32& r, u32& m) {
    u64 a, b;
    asm volatile("s_memrealtime %0\n\ts_memtime %1\n\ts_waitcnt lgkmcnt(0)" : "=s"(a), "=s"(b) : : "memory");
    r = (u32)a; m = (u32)b;
}
#define SLATE 20
DI void edge(u32& r, u32& m) {
    u64 a, t[9];
    u32 n = 0;
#define RT(i) "s_memrealtime %" #i "\n\ts_nop 1\n\t"
#define RC(i) "\n\ts_cmp_eq_u64 %" #i ", %1\n\ts_addc_u32 %10, %10, 0"
    asm volatile("s_memtime %0\n\t" RT(1) RT(2) RT(3) RT(4) RT(5) RT(6) RT(7) RT(8) "s_memrealtime %9\n\ts_waitcnt lgkmcnt(0)" RC(2) RC(3) RC(4) RC(5) RC(6) RC(7) RC(8) RC(9)
                 : "=&s"(a), "=&s"(t[0]), "=&s"(t[1]), "=&s"(t[2]), "=&s"(t[3]), "=&s"(t[4]), "=&s"(t[5]), "=&s"(t[6]), "=&s"(t[7]), "=&s"(t[8]), "+s"(n) : : "scc", "memory");
    m = (u32)a + 12 * n;
    r = (u32)t[0] + 4;
}
#define SPB 90
struct Clock {
    u32 r0, m0, r1, m1; int cpt;
    DI void init() { edge(r0, m0); r1 = r0; m1 = m0; cpt = 24 * 64; }
    DI u32 sched(u32& phi, int ofs, int G, int mode) {
        if (mode == 0) edge(r1, m1);
        u32 r = r1, m = m1;
        if (mode == 2) clocks(r, m);
        int rem = (int)(phi + (u32)ofs - r);
        if (__builtin_expect(rem < -SLATE, 0)) { const int k = (-rem / G + 1) * G; phi += (u32)k; rem += k; }
        return m + (u32)((min(rem, 3000) * cpt) >> 6) - SPB;
    }
    DI u32 at(u32 t) { u32 r, m; clocks(r, m); return m + (u32)((min(max((int)(t - r), 0), 3000) * cpt) >> 6); }
    DI void calib() {
        const int dr = (int)(r1 - r0);
        if (dr >= 60) { int c = (int)fminf(fmaxf((float)(int)(m1 - m0) * 64.f * __builtin_amdgcn_rcpf((float)dr), 256.f), 2560.f); asm volatile("" : "+v"(c)); cpt = uni(c); r0 = r1; m0 = m1; }
    }
};
#define VISS 0
#define VISV 0
DI void follow(u32& phi, int ofs, u32 t, int G) { const int late = (int)(t - phi) - ofs; if (__builtin_expect(late > 0, 0)) phi += (u32)((late / G + 1) * G); }
DI bool unwritten(const char* base, u32 off) {
#pragma nounroll
    for (int i = 0; i < 10; ++i) {
        u32x4 t;
        gld16c<-4096>(t, base + (i + (i & 8)) * 1024, off); waitv<0>(t);
        if (__builtin_amdgcn_ballot_w64(max3u(max(t[0], t[1]), t[2], t[3]) == 0xffffffffu) != 0) return true;
    }
    return false;
}

DI size_t uslot(int sd, int s) { const int r = (s * 43) >> 8; return OFF_U + (u32)uni(s * (int)U_STRIDE + r * (int)((2u << 20) - 6 * U_STRIDE) + ((sd >> (2 + r)) & 1) * 8192); }
DI size_t mslot(int sd, int s) { return (u32)uni(s * (int)HO_STRIDE + ((sd >> (s >> 5)) & 1) * 8192 + 4096); }
struct ChainCfg { int T, cR, G, dV, fast, s1, T2, cR2, s2, T3, cR3, dU; };
constexpr int TT_OFF = 88 + 48 + 360;
DI void karg_warm() {
    u32 a[13];
    asm volatile("s_load_dword %0, %13, 0\n\t" "s_load_dword %1, %13, 64\n\t" "s_load_dword %2, %13, 128\n\t" "s_load_dword %3, %13, 192\n\t" "s_load_dword %4, %13, 256\n\t" "s_load_dword %5, %13, 320\n\t" "s_load_dword %6, %13, 384\n\t" "s_load_dword %7, %13, 448\n\t" "s_load_dword %8, %13, 512\n\t" "s_load_dword %9, %13, 576\n\t" "s_load_dword %10, %13, 640\n\t" "s_load_dword %11, %13, 704\n\t" "s_load_dword %12, %13, 748\n\t" "s_waitcnt lgkmcnt(0)"
                 : "=&s"(a[0]), "=&s"(a[1]), "=&s"(a[2]), "=&s"(a[3]), "=&s"(a[4]), "=&s"(a[5]), "=&s"(a[6]), "=&s"(a[7]), "=&s"(a[8]), "=&s"(a[9]), "=&s"(a[10]), "=&s"(a[11]), "=&s"(a[12])
                 : "s"(__builtin_amdgcn_kernarg_segment_ptr()) : "memory");
}
DI u32 sched_ld0(int s) {
    u32 r;
    asm volatile("s_load_dword %0, %1, %2 offset:%3\n\ts_waitcnt lgkmcnt(0)" : "=s"(r) : "s"(__builtin_amdgcn_kernarg_segment_ptr()), "s"(4 * s), "n"(TT_OFF) : "memory");
    return r;
}
#define HP 60
#define HQ 96

constexpr int SL_W = 0;
constexpr int SL_IMG = 131072;
constexpr int SL_KX = SL_IMG + 6656;
constexpr int SL_PF = SL_KX + 12288 + 1024;
constexpr int S_LDS = SL_PF + 256;
DI void nap(int t) { const u32 wake = rt() + (u32)t; while ((int)(rt() - wake) < 0) __builtin_amdgcn_s_sleep(4); }
DI void dly(int n) {
    asm volatile(".Ld%=:\n\ts_sub_u32 %0, %0, 1\n\ts_nop 0\n\ts_cbranch_scc0 .Ld%=" : "+s"(n) : : "scc", "memory");
}
DI void pfl(const char* sbase, u32 voff, u32 lds) {
    asm volatile("s_mov_b32 m0, %2\n\ts_nop 3\n\tglobal_load_lds_dword %0, %1" : : "v"(voff), "s"(sbase), "s"(lds + SL_PF) : "memory");
}
DI void score_role(const Ptrs& p, const ChainCfg cfg, int h, u32 lds, int w, u32 tk_) {
    const int lane = lanex(), tid = 64 * w + lane;
    const int n = lane & 15, g = lane >> 4, qq = (lane >> 2) & 3, pp = lane & 3;
    u32x32 wa[8], wv[3];
    const u32 wreg = lds + SL_W + (u32)w * 32768;
    const u32 lfa = wreg + lane * 16;
    const char* kxp = p.ws + OFF_KX + (size_t)h * 12288 + w * 3072;
    const u32 kxl = lds + SL_KX;
    char* const fl = p.ws + OFF_ST;
    const u32 kxw = kxl + w * 3072 - (w < 2 ? 16 : 0);
    auto kxdma = [&]() {
        asm volatile("s_mov_b32 m0, %2\n\ts_nop 3\n\tglobal_load_lds_dwordx4 %0, %1 sc1\n\tglobal_load_lds_dwordx4 %0, %1 offset:1024 sc1\n\tglobal_load_lds_dwordx4 %0, %1 offset:2048 sc1"
                     : : "v"(lanex() * 16), "s"(kxp), "s"(kxw) : "memory");
    };
    u32 kfl, phi;
    int sd;
    {
        constexpr int GS = 3104;
        Sync y;
        if (w < 3) {
#pragma unroll
            for (int i = PXT; i < NSEG; ++i) gst16(p.ws + OFF_KM + (i * NH + h) * 3072 + w * 1024, lane * 16, u32x4{~0u, ~0u, ~0u, ~0u});
#pragma unroll
            for (int i = NSEG - 4; i < NSEG; ++i) gst16(p.ws + OFF_PX + i * 81920 + (w < 2 ? h * 2048 + w * 1024 : 65536 + h * 512), w < 2 ? lane * 16 : (lane * 16) & 511, u32x4{~0u, ~0u, ~0u, ~0u});
        }
        auto dvo = [&](int i) { const int ix = i * 64 + lane; return (u32)((ix / 24) * (HD * 2) + (ix % 24) * 16); };
        const u32 tra = lds + (2 * (g >> 1) + (qq >> 1)) * GS + (4 * (g & 1) + (qq & 1)) * 384 + 8 * pp;
        u32x8 pa = u32x8{dvo(0), dvo(1), dvo(2), tra + 96 * w + 32 * (w & 1), tra + 96 * w - 64 * (w & 1), tra + 32 * w, lfa, (u32)(lane & 31) * 8};
        asm volatile("s_nop 4\n\t" "s_add_u32 s42, s46, 0x0\n\t" "s_addc_u32 s43, s47, 0\n\t" "s_add_u32 m0, %18, 0x0\n\t" "s_nop 0\n\t" "global_load_lds_dwordx4 v224, s[42:43]\n\t" "s_add_u32 m0, %18, 0x400\n\t"
                     "s_nop 0\n\t" "global_load_lds_dwordx4 v225, s[42:43]\n\t" "s_add_u32 m0, %18, 0x800\n\t" "s_nop 0\n\t" "global_load_lds_dwordx4 v226, s[42:43]\n\t" "s_add_u32 s42, s46, 0x18000\n\t"
                     "s_addc_u32 s43, s47, 0\n\t" "s_add_u32 m0, %18, 0xc20\n\t" "s_nop 0\n\t" "global_load_lds_dwordx4 v224, s[42:43]\n\t" "s_add_u32 m0, %18, 0x1020\n\t" "s_nop 0\n\t"
                     "global_load_lds_dwordx4 v225, s[42:43]\n\t" "s_add_u32 m0, %18, 0x1420\n\t" "s_nop 0\n\t" "global_load_lds_dwordx4 v226, s[42:43]\n\t" "s_add_u32 s42, s46, 0xc0000\n\t" "s_addc_u32 s43, s47, 0\n\t"
                     "s_add_u32 m0, %18, 0x6100\n\t" "s_nop 0\n\t" "global_load_lds_dwordx4 v224, s[42:43]\n\t" "s_add_u32 m0, %18, 0x6500\n\t" "s_nop 0\n\t" "global_load_lds_dwordx4 v225, s[42:43]\n\t"
                     "s_add_u32 m0, %18, 0x6900\n\t" "s_nop 0\n\t" "global_load_lds_dwordx4 v226, s[42:43]\n\t" "s_add_u32 s42, s46, 0xd8000\n\t" "s_addc_u32 s43, s47, 0\n\t" "s_add_u32 m0, %18, 0x6d20\n\t" "s_nop 0\n\t"
                     "global_load_lds_dwordx4 v224, s[42:43]\n\t" "s_add_u32 m0, %18, 0x7120\n\t" "s_nop 0\n\t" "global_load_lds_dwordx4 v225, s[42:43]\n\t" "s_add_u32 m0, %18, 0x7520\n\t" "s_nop 0\n\t"
                     "global_load_lds_dwordx4 v226, s[42:43]\n\t" "s_add_u32 s42, s46, 0x180000\n\t" "s_addc_u32 s43, s47, 0\n\t" "s_add_u32 m0, %18, 0xc200\n\t" "s_nop 0\n\t" "global_load_lds_dwordx4 v224, s[42:43]\n\t"
                     "s_add_u32 m0, %18, 0xc600\n\t" "s_nop 0\n\t" "global_load_lds_dwordx4 v225, s[42:43]\n\t" "s_add_u32 m0, %18, 0xca00\n\t" "s_nop 0\n\t" "global_load_lds_dwordx4 v226, s[42:43]\n\t"
                     "s_add_u32 s42, s46, 0x198000\n\t" "s_addc_u32 s43, s47, 0\n\t" "s_add_u32 m0, %18, 0xce20\n\t" "s_nop 0\n\t" "global_load_lds_dwordx4 v224, s[42:43]\n\t" "s_add_u32 m0, %18, 0xd220\n\t" "s_nop 0\n\t"
                     "global_load_lds_dwordx4 v225, s[42:43]\n\t" "s_add_u32 m0, %18, 0xd620\n\t" "s_nop 0\n\t" "global_load_lds_dwordx4 v226, s[42:43]\n\t" "s_add_u32 s42, s46, 0x240000\n\t" "s_addc_u32 s43, s47, 0\n\t"
                     "s_add_u32 m0, %18, 0x12300\n\t" "s_nop 0\n\t" "global_load_lds_dwordx4 v224, s[42:43]\n\t" "s_add_u32 m0, %18, 0x12700\n\t" "s_nop 0\n\t" "global_load_lds_dwordx4 v225, s[42:43]\n\t"
                     "s_add_u32 m0, %18, 0x12b00\n\t" "s_nop 0\n\t" "global_load_lds_dwordx4 v226, s[42:43]\n\t" "s_add_u32 s42, s46, 0x258000\n\t" "s_addc_u32 s43, s47, 0\n\t" "s_add_u32 m0, %18, 0x12f20\n\t"
                     "s_nop 0\n\t" "global_load_lds_dwordx4 v224, s[42:43]\n\t" "s_add_u32 m0, %18, 0x13320\n\t" "s_nop 0\n\t" "global_load_lds_dwordx4 v225, s[42:43]\n\t" "s_add_u32 m0, %18, 0x13720\n\t" "s_nop 0\n\t"
                     "global_load_lds_dwordx4 v226, s[42:43]\n\t" "s_waitcnt vmcnt(12) lgkmcnt(0)\n\t" "s_barrier\n\t" "s_add_u32 s42, s46, 0x300000\n\t" "s_addc_u32 s43, s47, 0\n\t" "s_add_u32 m0, %18, 0x18400\n\t"
                     "s_nop 0\n\t" "global_load_lds_dwordx4 v224, s[42:43]\n\t" "s_add_u32 m0, %18, 0x18800\n\t" "s_nop 0\n\t" "global_load_lds_dwordx4 v225, s[42:43]\n\t" "s_add_u32 m0, %18, 0x18c00\n\t" "s_nop 0\n\t"
                     "global_load_lds_dwordx4 v226, s[42:43]\n\t" "s_add_u32 s42, s46, 0x318000\n\t" "s_addc_u32 s43, s47, 0\n\t" "s_add_u32 m0, %18, 0x19020\n\t" "s_nop 0\n\t" "global_load_lds_dwordx4 v224, s[42:43]\n\t"
                     "s_add_u32 m0, %18, 0x19420\n\t" "s_nop 0\n\t" "global_load_lds_dwordx4 v225, s[42:43]\n\t" "s_add_u32 m0, %18, 0x19820\n\t" "s_nop 0\n\t" "global_load_lds_dwordx4 v226, s[42:43]\n\t"
                     "s_add_u32 s42, s46, 0x3c0000\n\t" "s_addc_u32 s43, s47, 0\n\t" "s_add_u32 m0, %18, 0x1e500\n\t" "s_nop 0\n\t" "global_load_lds_dwordx4 v224, s[42:43]\n\t" "s_add_u32 m0, %18, 0x1e900\n\t"
                     "s_nop 0\n\t" "global_load_lds_dwordx4 v225, s[42:43]\n\t" "s_add_u32 m0, %18, 0x1ed00\n\t" "s_nop 0\n\t" "global_load_lds_dwordx4 v226, s[42:43]\n\t" "s_add_u32 s42, s46, 0x3d8000\n\t"
                     "s_addc_u32 s43, s47, 0\n\t" "s_add_u32 m0, %18, 0x1f120\n\t" "s_nop 0\n\t" "global_load_lds_dwordx4 v224, s[42:43]\n\t" "s_add_u32 m0, %18, 0x1f520\n\t" "s_nop 0\n\t"
                     "global_load_lds_dwordx4 v225, s[42:43]\n\t" "s_add_u32 m0, %18, 0x1f920\n\t" "s_nop 0\n\t" "global_load_lds_dwordx4 v226, s[42:43]\n\t" "v_add_u32 v232, 0x0, v229\n\t"
                     "ds_read_b64_tr_b16 v[8:9], v232\n\t" "ds_read_b64_tr_b16 v[10:11], v232 offset:768\n\t" "ds_read_b64_tr_b16 v[12:13], v232 offset:12416\n\t" "ds_read_b64_tr_b16 v[14:15], v232 offset:13184\n\t"
                     "ds_read_b64_tr_b16 a[96:97], v232 offset:128\n\t" "ds_read_b64_tr_b16 a[98:99], v232 offset:896\n\t" "ds_read_b64_tr_b16 a[100:101], v232 offset:12544\n\t"
                     "ds_read_b64_tr_b16 a[102:103], v232 offset:13312\n\t" "ds_read_b64_tr_b16 a[176:177], v232 offset:256\n\t" "ds_read_b64_tr_b16 a[178:179], v232 offset:1024\n\t"
                     "ds_read_b64_tr_b16 a[180:181], v232 offset:12672\n\t" "ds_read_b64_tr_b16 a[182:183], v232 offset:13440\n\t" "ds_read_b64_tr_b16 v[24:25], v232 offset:24832\n\t"
                     "ds_read_b64_tr_b16 v[26:27], v232 offset:25600\n\t" "ds_read_b64_tr_b16 v[28:29], v232 offset:37248\n\t" "ds_read_b64_tr_b16 v[30:31], v232 offset:38016\n\t"
                     "ds_read_b64_tr_b16 a[104:105], v232 offset:24960\n\t" "ds_read_b64_tr_b16 a[106:107], v232 offset:25728\n\t" "ds_read_b64_tr_b16 a[108:109], v232 offset:37376\n\t"
                     "ds_read_b64_tr_b16 a[110:111], v232 offset:38144\n\t" "ds_read_b64_tr_b16 a[184:185], v232 offset:25088\n\t" "ds_read_b64_tr_b16 a[186:187], v232 offset:25856\n\t"
                     "ds_read_b64_tr_b16 a[188:189], v232 offset:37504\n\t" "ds_read_b64_tr_b16 a[190:191], v232 offset:38272\n\t" "s_waitcnt vmcnt(12) lgkmcnt(0)\n\t" "s_barrier\n\t" "s_add_u32 s42, s46, 0x480000\n\t"
                     "s_addc_u32 s43, s47, 0\n\t" "s_add_u32 m0, %18, 0x0\n\t" "s_nop 0\n\t" "global_load_lds_dwordx4 v224, s[42:43]\n\t" "s_add_u32 m0, %18, 0x400\n\t" "s_nop 0\n\t"
                     "global_load_lds_dwordx4 v225, s[42:43]\n\t" "s_add_u32 m0, %18, 0x800\n\t" "s_nop 0\n\t" "global_load_lds_dwordx4 v226, s[42:43]\n\t" "s_add_u32 s42, s46, 0x498000\n\t" "s_addc_u32 s43, s47, 0\n\t"
                     "s_add_u32 m0, %18, 0xc20\n\t" "s_nop 0\n\t" "global_load_lds_dwordx4 v224, s[42:43]\n\t" "s_add_u32 m0, %18, 0x1020\n\t" "s_nop 0\n\t" "global_load_lds_dwordx4 v225, s[42:43]\n\t"
                     "s_add_u32 m0, %18, 0x1420\n\t" "s_nop 0\n\t" "global_load_lds_dwordx4 v226, s[42:43]\n\t" "s_add_u32 s42, s46, 0x540000\n\t" "s_addc_u32 s43, s47, 0\n\t" "s_add_u32 m0, %18, 0x6100\n\t" "s_nop 0\n\t"
                     "global_load_lds_dwordx4 v224, s[42:43]\n\t" "s_add_u32 m0, %18, 0x6500\n\t" "s_nop 0\n\t" "global_load_lds_dwordx4 v225, s[42:43]\n\t" "s_add_u32 m0, %18, 0x6900\n\t" "s_nop 0\n\t"
                     "global_load_lds_dwordx4 v226, s[42:43]\n\t" "s_add_u32 s42, s46, 0x558000\n\t" "s_addc_u32 s43, s47, 0\n\t" "s_add_u32 m0, %18, 0x6d20\n\t" "s_nop 0\n\t" "global_load_lds_dwordx4 v224, s[42:43]\n\t"
                     "s_add_u32 m0, %18, 0x7120\n\t" "s_nop 0\n\t" "global_load_lds_dwordx4 v225, s[42:43]\n\t" "s_add_u32 m0, %18, 0x7520\n\t" "s_nop 0\n\t" "global_load_lds_dwordx4 v226, s[42:43]\n\t"
                     "v_add_u32 v232, 0xc200, v229\n\t" "ds_read_b64_tr_b16 v[40:41], v232\n\t" "ds_read_b64_tr_b16 v[42:43], v232 offset:768\n\t" "ds_read_b64_tr_b16 v[44:45], v232 offset:12416\n\t"
                     "ds_read_b64_tr_b16 v[46:47], v232 offset:13184\n\t" "ds_read_b64_tr_b16 a[112:113], v232 offset:128\n\t" "ds_read_b64_tr_b16 a[114:115], v232 offset:896\n\t"
                     "ds_read_b64_tr_b16 a[116:117], v232 offset:12544\n\t" "ds_read_b64_tr_b16 a[118:119], v232 offset:13312\n\t" "ds_read_b64_tr_b16 a[192:193], v232 offset:256\n\t"
                     "ds_read_b64_tr_b16 a[194:195], v232 offset:1024\n\t" "ds_read_b64_tr_b16 a[196:197], v232 offset:12672\n\t" "ds_read_b64_tr_b16 a[198:199], v232 offset:13440\n\t"
                     "ds_read_b64_tr_b16 v[56:57], v232 offset:24832\n\t" "ds_read_b64_tr_b16 v[58:59], v232 offset:25600\n\t" "ds_read_b64_tr_b16 v[60:61], v232 offset:37248\n\t"
                     "ds_read_b64_tr_b16 v[62:63], v232 offset:38016\n\t" "ds_read_b64_tr_b16 a[120:121], v232 offset:24960\n\t" "ds_read_b64_tr_b16 a[122:123], v232 offset:25728\n\t"
                     "ds_read_b64_tr_b16 a[124:125], v232 offset:37376\n\t" "ds_read_b64_tr_b16 a[126:127], v232 offset:38144\n\t" "ds_read_b64_tr_b16 a[200:201], v232 offset:25088\n\t"
                     "ds_read_b64_tr_b16 a[202:203], v232 offset:25856\n\t" "ds_read_b64_tr_b16 a[204:205], v232 offset:37504\n\t" "ds_read_b64_tr_b16 a[206:207], v232 offset:38272\n\t" "s_waitcnt vmcnt(12) lgkmcnt(0)\n\t"
                     "s_barrier\n\t" "s_add_u32 s42, s46, 0x600000\n\t" "s_addc_u32 s43, s47, 0\n\t" "s_add_u32 m0, %18, 0xc200\n\t" "s_nop 0\n\t" "global_load_lds_dwordx4 v224, s[42:43]\n\t"
                     "s_add_u32 m0, %18, 0xc600\n\t" "s_nop 0\n\t" "global_load_lds_dwordx4 v225, s[42:43]\n\t" "s_add_u32 m0, %18, 0xca00\n\t" "s_nop 0\n\t" "global_load_lds_dwordx4 v226, s[42:43]\n\t"
                     "s_add_u32 s42, s46, 0x618000\n\t" "s_addc_u32 s43, s47, 0\n\t" "s_add_u32 m0, %18, 0xce20\n\t" "s_nop 0\n\t" "global_load_lds_dwordx4 v224, s[42:43]\n\t" "s_add_u32 m0, %18, 0xd220\n\t" "s_nop 0\n\t"
                     "global_load_lds_dwordx4 v225, s[42:43]\n\t" "s_add_u32 m0, %18, 0xd620\n\t" "s_nop 0\n\t" "global_load_lds_dwordx4 v226, s[42:43]\n\t" "s_add_u32 s42, s46, 0x6c0000\n\t" "s_addc_u32 s43, s47, 0\n\t"
                     "s_add_u32 m0, %18, 0x12300\n\t" "s_nop 0\n\t" "global_load_lds_dwordx4 v224, s[42:43]\n\t" "s_add_u32 m0, %18, 0x12700\n\t" "s_nop 0\n\t" "global_load_lds_dwordx4 v225, s[42:43]\n\t"
                     "s_add_u32 m0, %18, 0x12b00\n\t" "s_nop 0\n\t" "global_load_lds_dwordx4 v226, s[42:43]\n\t" "s_add_u32 s42, s46, 0x6d8000\n\t" "s_addc_u32 s43, s47, 0\n\t" "s_add_u32 m0, %18, 0x12f20\n\t"
                     "s_nop 0\n\t" "global_load_lds_dwordx4 v224, s[42:43]\n\t" "s_add_u32 m0, %18, 0x13320\n\t" "s_nop 0\n\t" "global_load_lds_dwordx4 v225, s[42:43]\n\t" "s_add_u32 m0, %18, 0x13720\n\t" "s_nop 0\n\t"
                     "global_load_lds_dwordx4 v226, s[42:43]\n\t" "v_add_u32 v232, 0x18400, v229\n\t" "ds_read_b64_tr_b16 v[72:73], v232\n\t" "ds_read_b64_tr_b16 v[74:75], v232 offset:768\n\t"
                     "ds_read_b64_tr_b16 v[76:77], v232 offset:12416\n\t" "ds_read_b64_tr_b16 v[78:79], v232 offset:13184\n\t" "ds_read_b64_tr_b16 a[128:129], v232 offset:128\n\t"
                     "ds_read_b64_tr_b16 a[130:131], v232 offset:896\n\t" "ds_read_b64_tr_b16 a[132:133], v232 offset:12544\n\t" "ds_read_b64_tr_b16 a[134:135], v232 offset:13312\n\t"
                     "ds_read_b64_tr_b16 a[208:209], v232 offset:256\n\t" "ds_read_b64_tr_b16 a[210:211], v232 offset:1024\n\t" "ds_read_b64_tr_b16 a[212:213], v232 offset:12672\n\t"
                     "ds_read_b64_tr_b16 a[214:215], v232 offset:13440\n\t" "ds_read_b64_tr_b16 v[88:89], v232 offset:24832\n\t" "ds_read_b64_tr_b16 v[90:91], v232 offset:25600\n\t"
                     "ds_read_b64_tr_b16 v[92:93], v232 offset:37248\n\t" "ds_read_b64_tr_b16 v[94:95], v232 offset:38016\n\t" "ds_read_b64_tr_b16 a[136:137], v232 offset:24960\n\t"
                     "ds_read_b64_tr_b16 a[138:139], v232 offset:25728\n\t" "ds_read_b64_tr_b16 a[140:141], v232 offset:37376\n\t" "ds_read_b64_tr_b16 a[142:143], v232 offset:38144\n\t"
                     "ds_read_b64_tr_b16 a[216:217], v232 offset:25088\n\t" "ds_read_b64_tr_b16 a[218:219], v232 offset:25856\n\t" "ds_read_b64_tr_b16 a[220:221], v232 offset:37504\n\t"
                     "ds_read_b64_tr_b16 a[222:223], v232 offset:38272\n\t" "s_waitcnt vmcnt(12) lgkmcnt(0)\n\t" "s_barrier\n\t" "s_add_u32 s42, s44, 0x0\n\t" "s_addc_u32 s43, s45, 0\n\t" "s_add_u32 m0, %18, 0x18400\n\t"
                     "s_nop 0\n\t" "global_load_lds_dwordx4 v224, s[42:43]\n\t" "s_add_u32 m0, %18, 0x18800\n\t" "s_nop 0\n\t" "global_load_lds_dwordx4 v225, s[42:43]\n\t" "s_add_u32 m0, %18, 0x18c00\n\t" "s_nop 0\n\t"
                     "global_load_lds_dwordx4 v226, s[42:43]\n\t" "s_add_u32 s42, s44, 0x18000\n\t" "s_addc_u32 s43, s45, 0\n\t" "s_add_u32 m0, %18, 0x19020\n\t" "s_nop 0\n\t" "global_load_lds_dwordx4 v224, s[42:43]\n\t"
                     "s_add_u32 m0, %18, 0x19420\n\t" "s_nop 0\n\t" "global_load_lds_dwordx4 v225, s[42:43]\n\t" "s_add_u32 m0, %18, 0x19820\n\t" "s_nop 0\n\t" "global_load_lds_dwordx4 v226, s[42:43]\n\t"
                     "s_add_u32 s42, s44, 0xc0000\n\t" "s_addc_u32 s43, s45, 0\n\t" "s_add_u32 m0, %18, 0x1e500\n\t" "s_nop 0\n\t" "global_load_lds_dwordx4 v224, s[42:43]\n\t" "s_add_u32 m0, %18, 0x1e900\n\t" "s_nop 0\n\t"
                     "global_load_lds_dwordx4 v225, s[42:43]\n\t" "s_add_u32 m0, %18, 0x1ed00\n\t" "s_nop 0\n\t" "global_load_lds_dwordx4 v226, s[42:43]\n\t" "s_add_u32 s42, s44, 0xd8000\n\t" "s_addc_u32 s43, s45, 0\n\t"
                     "s_add_u32 m0, %18, 0x1f120\n\t" "s_nop 0\n\t" "global_load_lds_dwordx4 v224, s[42:43]\n\t" "s_add_u32 m0, %18, 0x1f520\n\t" "s_nop 0\n\t" "global_load_lds_dwordx4 v225, s[42:43]\n\t"
                     "s_add_u32 m0, %18, 0x1f920\n\t" "s_nop 0\n\t" "global_load_lds_dwordx4 v226, s[42:43]\n\t" "v_add_u32 v232, 0x0, v229\n\t" "ds_read_b64_tr_b16 v[104:105], v232\n\t"
                     "ds_read_b64_tr_b16 v[106:107], v232 offset:768\n\t" "ds_read_b64_tr_b16 v[108:109], v232 offset:12416\n\t" "ds_read_b64_tr_b16 v[110:111], v232 offset:13184\n\t"
                     "ds_read_b64_tr_b16 a[144:145], v232 offset:128\n\t" "ds_read_b64_tr_b16 a[146:147], v232 offset:896\n\t" "ds_read_b64_tr_b16 a[148:149], v232 offset:12544\n\t"
                     "ds_read_b64_tr_b16 a[150:151], v232 offset:13312\n\t" "ds_read_b64_tr_b16 a[224:225], v232 offset:256\n\t" "ds_read_b64_tr_b16 a[226:227], v232 offset:1024\n\t"
                     "ds_read_b64_tr_b16 a[228:229], v232 offset:12672\n\t" "ds_read_b64_tr_b16 a[230:231], v232 offset:13440\n\t" "ds_read_b64_tr_b16 v[120:121], v232 offset:24832\n\t"
                     "ds_read_b64_tr_b16 v[122:123], v232 offset:25600\n\t" "ds_read_b64_tr_b16 v[124:125], v232 offset:37248\n\t" "ds_read_b64_tr_b16 v[126:127], v232 offset:38016\n\t"
                     "ds_read_b64_tr_b16 a[152:153], v232 offset:24960\n\t" "ds_read_b64_tr_b16 a[154:155], v232 offset:25728\n\t" "ds_read_b64_tr_b16 a[156:157], v232 offset:37376\n\t"
                     "ds_read_b64_tr_b16 a[158:159], v232 offset:38144\n\t" "ds_read_b64_tr_b16 a[232:233], v232 offset:25088\n\t" "ds_read_b64_tr_b16 a[234:235], v232 offset:25856\n\t"
                     "ds_read_b64_tr_b16 a[236:237], v232 offset:37504\n\t" "ds_read_b64_tr_b16 a[238:239], v232 offset:38272\n\t" "s_waitcnt vmcnt(12) lgkmcnt(0)\n\t" "s_barrier\n\t" "s_add_u32 s42, s44, 0x180000\n\t"
                     "s_addc_u32 s43, s45, 0\n\t" "s_add_u32 m0, %18, 0x0\n\t" "s_nop 0\n\t" "global_load_lds_dwordx4 v224, s[42:43]\n\t" "s_add_u32 m0, %18, 0x400\n\t" "s_nop 0\n\t"
                     "global_load_lds_dwordx4 v225, s[42:43]\n\t" "s_add_u32 m0, %18, 0x800\n\t" "s_nop 0\n\t" "global_load_lds_dwordx4 v226, s[42:43]\n\t" "s_add_u32 s42, s44, 0x198000\n\t" "s_addc_u32 s43, s45, 0\n\t"
                     "s_add_u32 m0, %18, 0xc20\n\t" "s_nop 0\n\t" "global_load_lds_dwordx4 v224, s[42:43]\n\t" "s_add_u32 m0, %18, 0x1020\n\t" "s_nop 0\n\t" "global_load_lds_dwordx4 v225, s[42:43]\n\t"
                     "s_add_u32 m0, %18, 0x1420\n\t" "s_nop 0\n\t" "global_load_lds_dwordx4 v226, s[42:43]\n\t" "s_add_u32 s42, s44, 0x240000\n\t" "s_addc_u32 s43, s45, 0\n\t" "s_add_u32 m0, %18, 0x6100\n\t" "s_nop 0\n\t"
                     "global_load_lds_dwordx4 v224, s[42:43]\n\t" "s_add_u32 m0, %18, 0x6500\n\t" "s_nop 0\n\t" "global_load_lds_dwordx4 v225, s[42:43]\n\t" "s_add_u32 m0, %18, 0x6900\n\t" "s_nop 0\n\t"
                     "global_load_lds_dwordx4 v226, s[42:43]\n\t" "s_add_u32 s42, s44, 0x258000\n\t" "s_addc_u32 s43, s45, 0\n\t" "s_add_u32 m0, %18, 0x6d20\n\t" "s_nop 0\n\t" "global_load_lds_dwordx4 v224, s[42:43]\n\t"
                     "s_add_u32 m0, %18, 0x7120\n\t" "s_nop 0\n\t" "global_load_lds_dwordx4 v225, s[42:43]\n\t" "s_add_u32 m0, %18, 0x7520\n\t" "s_nop 0\n\t" "global_load_lds_dwordx4 v226, s[42:43]\n\t"
                     "v_add_u32 v232, 0xc200, v229\n\t" "ds_read_b64_tr_b16 a[56:57], v232\n\t" "ds_read_b64_tr_b16 a[58:59], v232 offset:768\n\t" "ds_read_b64_tr_b16 a[60:61], v232 offset:12416\n\t"
                     "ds_read_b64_tr_b16 a[62:63], v232 offset:13184\n\t" "ds_read_b64_tr_b16 a[160:161], v232 offset:128\n\t" "ds_read_b64_tr_b16 a[162:163], v232 offset:896\n\t"
                     "ds_read_b64_tr_b16 a[164:165], v232 offset:12544\n\t" "ds_read_b64_tr_b16 a[166:167], v232 offset:13312\n\t" "ds_read_b64_tr_b16 a[240:241], v232 offset:256\n\t"
                     "ds_read_b64_tr_b16 a[242:243], v232 offset:1024\n\t" "ds_read_b64_tr_b16 a[244:245], v232 offset:12672\n\t" "ds_read_b64_tr_b16 a[246:247], v232 offset:13440\n\t"
                     "ds_read_b64_tr_b16 a[88:89], v232 offset:24832\n\t" "ds_read_b64_tr_b16 a[90:91], v232 offset:25600\n\t" "ds_read_b64_tr_b16 a[92:93], v232 offset:37248\n\t"
                     "ds_read_b64_tr_b16 a[94:95], v232 offset:38016\n\t" "ds_read_b64_tr_b16 a[168:169], v232 offset:24960\n\t" "ds_read_b64_tr_b16 a[170:171], v232 offset:25728\n\t"
                     "ds_read_b64_tr_b16 a[172:173], v232 offset:37376\n\t" "ds_read_b64_tr_b16 a[174:175], v232 offset:38144\n\t" "ds_read_b64_tr_b16 a[248:249], v232 offset:25088\n\t"
                     "ds_read_b64_tr_b16 a[250:251], v232 offset:25856\n\t" "ds_read_b64_tr_b16 a[252:253], v232 offset:37504\n\t" "ds_read_b64_tr_b16 a[254:255], v232 offset:38272\n\t" "s_waitcnt vmcnt(12) lgkmcnt(0)\n\t"
                     "s_barrier\n\t" "s_add_u32 s42, s44, 0x300000\n\t" "s_addc_u32 s43, s45, 0\n\t" "s_add_u32 m0, %18, 0xc200\n\t" "s_nop 0\n\t" "global_load_lds_dwordx4 v224, s[42:43]\n\t"
                     "s_add_u32 m0, %18, 0xc600\n\t" "s_nop 0\n\t" "global_load_lds_dwordx4 v225, s[42:43]\n\t" "s_add_u32 m0, %18, 0xca00\n\t" "s_nop 0\n\t" "global_load_lds_dwordx4 v226, s[42:43]\n\t"
                     "s_add_u32 s42, s44, 0x318000\n\t" "s_addc_u32 s43, s45, 0\n\t" "s_add_u32 m0, %18, 0xce20\n\t" "s_nop 0\n\t" "global_load_lds_dwordx4 v224, s[42:43]\n\t" "s_add_u32 m0, %18, 0xd220\n\t" "s_nop 0\n\t"
                     "global_load_lds_dwordx4 v225, s[42:43]\n\t" "s_add_u32 m0, %18, 0xd620\n\t" "s_nop 0\n\t" "global_load_lds_dwordx4 v226, s[42:43]\n\t" "s_add_u32 s42, s44, 0x3c0000\n\t" "s_addc_u32 s43, s45, 0\n\t"
                     "s_add_u32 m0, %18, 0x12300\n\t" "s_nop 0\n\t" "global_load_lds_dwordx4 v224, s[42:43]\n\t" "s_add_u32 m0, %18, 0x12700\n\t" "s_nop 0\n\t" "global_load_lds_dwordx4 v225, s[42:43]\n\t"
                     "s_add_u32 m0, %18, 0x12b00\n\t" "s_nop 0\n\t" "global_load_lds_dwordx4 v226, s[42:43]\n\t" "s_add_u32 s42, s44, 0x3d8000\n\t" "s_addc_u32 s43, s45, 0\n\t" "s_add_u32 m0, %18, 0x12f20\n\t"
                     "s_nop 0\n\t" "global_load_lds_dwordx4 v224, s[42:43]\n\t" "s_add_u32 m0, %18, 0x13320\n\t" "s_nop 0\n\t" "global_load_lds_dwordx4 v225, s[42:43]\n\t" "s_add_u32 m0, %18, 0x13720\n\t" "s_nop 0\n\t"
                     "global_load_lds_dwordx4 v226, s[42:43]\n\t" "v_add_u32 v232, 0x18400, v227\n\t" "v_add_u32 v233, 0x18400, v228\n\t" "ds_read_b64_tr_b16 v[128:129], v232\n\t"
                     "ds_read_b64_tr_b16 v[130:131], v232 offset:768\n\t" "ds_read_b64_tr_b16 v[132:133], v232 offset:12416\n\t" "ds_read_b64_tr_b16 v[134:135], v232 offset:13184\n\t"
                     "ds_read_b64_tr_b16 v[0:1], v232 offset:32\n\t" "ds_read_b64_tr_b16 v[2:3], v232 offset:800\n\t" "ds_read_b64_tr_b16 v[4:5], v232 offset:12448\n\t" "ds_read_b64_tr_b16 v[6:7], v232 offset:13216\n\t"
                     "ds_read_b64_tr_b16 v[136:137], v233 offset:64\n\t" "ds_read_b64_tr_b16 v[138:139], v233 offset:832\n\t" "ds_read_b64_tr_b16 v[140:141], v233 offset:12480\n\t"
                     "ds_read_b64_tr_b16 v[142:143], v233 offset:13248\n\t" "ds_read_b64_tr_b16 v[144:145], v232 offset:24832\n\t" "ds_read_b64_tr_b16 v[146:147], v232 offset:25600\n\t"
                     "ds_read_b64_tr_b16 v[148:149], v232 offset:37248\n\t" "ds_read_b64_tr_b16 v[150:151], v232 offset:38016\n\t" "ds_read_b64_tr_b16 v[16:17], v232 offset:24864\n\t"
                     "ds_read_b64_tr_b16 v[18:19], v232 offset:25632\n\t" "ds_read_b64_tr_b16 v[20:21], v232 offset:37280\n\t" "ds_read_b64_tr_b16 v[22:23], v232 offset:38048\n\t"
                     "ds_read_b64_tr_b16 v[152:153], v233 offset:24896\n\t" "ds_read_b64_tr_b16 v[154:155], v233 offset:25664\n\t" "ds_read_b64_tr_b16 v[156:157], v233 offset:37312\n\t"
                     "ds_read_b64_tr_b16 v[158:159], v233 offset:38080\n\t" "s_waitcnt vmcnt(12) lgkmcnt(0)\n\t" "s_barrier\n\t" "v_mov_b32 %14, 0\n\t" "global_load_dwordx2 %11, v231, %19 sc1\n\t"
                     "global_load_dwordx2 %12, %14, %19 offset:512 sc1\n\t" "global_load_dwordx2 %13, %14, %19 offset:1024 sc1\n\t" "s_add_u32 s42, s44, 0x480000\n\t" "s_addc_u32 s43, s45, 0\n\t"
                     "s_add_u32 m0, %18, 0x18400\n\t" "s_nop 0\n\t" "global_load_lds_dwordx4 v224, s[42:43]\n\t" "s_add_u32 m0, %18, 0x18800\n\t" "s_nop 0\n\t" "global_load_lds_dwordx4 v225, s[42:43]\n\t"
                     "s_add_u32 m0, %18, 0x18c00\n\t" "s_nop 0\n\t" "global_load_lds_dwordx4 v226, s[42:43]\n\t" "s_add_u32 s42, s44, 0x498000\n\t" "s_addc_u32 s43, s45, 0\n\t" "s_add_u32 m0, %18, 0x19020\n\t"
                     "s_nop 0\n\t" "global_load_lds_dwordx4 v224, s[42:43]\n\t" "s_add_u32 m0, %18, 0x19420\n\t" "s_nop 0\n\t" "global_load_lds_dwordx4 v225, s[42:43]\n\t" "s_add_u32 m0, %18, 0x19820\n\t" "s_nop 0\n\t"
                     "global_load_lds_dwordx4 v226, s[42:43]\n\t" "s_add_u32 s42, s44, 0x540000\n\t" "s_addc_u32 s43, s45, 0\n\t" "s_add_u32 m0, %18, 0x1e500\n\t" "s_nop 0\n\t" "global_load_lds_dwordx4 v224, s[42:43]\n\t"
                     "s_add_u32 m0, %18, 0x1e900\n\t" "s_nop 0\n\t" "global_load_lds_dwordx4 v225, s[42:43]\n\t" "s_add_u32 m0, %18, 0x1ed00\n\t" "s_nop 0\n\t" "global_load_lds_dwordx4 v226, s[42:43]\n\t"
                     "s_add_u32 s42, s44, 0x558000\n\t" "s_addc_u32 s43, s45, 0\n\t" "s_add_u32 m0, %18, 0x1f120\n\t" "s_nop 0\n\t" "global_load_lds_dwordx4 v224, s[42:43]\n\t" "s_add_u32 m0, %18, 0x1f520\n\t"
                     "s_nop 0\n\t" "global_load_lds_dwordx4 v225, s[42:43]\n\t" "s_add_u32 m0, %18, 0x1f920\n\t" "s_nop 0\n\t" "global_load_lds_dwordx4 v226, s[42:43]\n\t" "v_add_u32 v232, 0x0, v227\n\t"
                     "v_add_u32 v233, 0x0, v228\n\t" "ds_read_b64_tr_b16 v[160:161], v232\n\t" "ds_read_b64_tr_b16 v[162:163], v232 offset:768\n\t" "ds_read_b64_tr_b16 v[164:165], v232 offset:12416\n\t"
                     "ds_read_b64_tr_b16 v[166:167], v232 offset:13184\n\t" "ds_read_b64_tr_b16 v[32:33], v232 offset:32\n\t" "ds_read_b64_tr_b16 v[34:35], v232 offset:800\n\t"
                     "ds_read_b64_tr_b16 v[36:37], v232 offset:12448\n\t" "ds_read_b64_tr_b16 v[38:39], v232 offset:13216\n\t" "ds_read_b64_tr_b16 v[168:169], v233 offset:64\n\t"
                     "ds_read_b64_tr_b16 v[170:171], v233 offset:832\n\t" "ds_read_b64_tr_b16 v[172:173], v233 offset:12480\n\t" "ds_read_b64_tr_b16 v[174:175], v233 offset:13248\n\t"
                     "ds_read_b64_tr_b16 v[176:177], v232 offset:24832\n\t" "ds_read_b64_tr_b16 v[178:179], v232 offset:25600\n\t" "ds_read_b64_tr_b16 v[180:181], v232 offset:37248\n\t"
                     "ds_read_b64_tr_b16 v[182:183], v232 offset:38016\n\t" "ds_read_b64_tr_b16 v[48:49], v232 offset:24864\n\t" "ds_read_b64_tr_b16 v[50:51], v232 offset:25632\n\t"
                     "ds_read_b64_tr_b16 v[52:53], v232 offset:37280\n\t" "ds_read_b64_tr_b16 v[54:55], v232 offset:38048\n\t" "ds_read_b64_tr_b16 v[184:185], v233 offset:24896\n\t"
                     "ds_read_b64_tr_b16 v[186:187], v233 offset:25664\n\t" "ds_read_b64_tr_b16 v[188:189], v233 offset:37312\n\t" "ds_read_b64_tr_b16 v[190:191], v233 offset:38080\n\t" "s_waitcnt vmcnt(12) lgkmcnt(0)\n\t"
                     "s_barrier\n\t" "s_add_u32 s42, s44, 0x600000\n\t" "s_addc_u32 s43, s45, 0\n\t" "s_add_u32 m0, %18, 0x0\n\t" "s_nop 0\n\t" "global_load_lds_dwordx4 v224, s[42:43]\n\t" "s_add_u32 m0, %18, 0x400\n\t"
                     "s_nop 0\n\t" "global_load_lds_dwordx4 v225, s[42:43]\n\t" "s_add_u32 m0, %18, 0x800\n\t" "s_nop 0\n\t" "global_load_lds_dwordx4 v226, s[42:43]\n\t" "s_add_u32 s42, s44, 0x618000\n\t"
                     "s_addc_u32 s43, s45, 0\n\t" "s_add_u32 m0, %18, 0xc20\n\t" "s_nop 0\n\t" "global_load_lds_dwordx4 v224, s[42:43]\n\t" "s_add_u32 m0, %18, 0x1020\n\t" "s_nop 0\n\t"
                     "global_load_lds_dwordx4 v225, s[42:43]\n\t" "s_add_u32 m0, %18, 0x1420\n\t" "s_nop 0\n\t" "global_load_lds_dwordx4 v226, s[42:43]\n\t" "s_add_u32 s42, s44, 0x6c0000\n\t" "s_addc_u32 s43, s45, 0\n\t"
                     "s_add_u32 m0, %18, 0x6100\n\t" "s_nop 0\n\t" "global_load_lds_dwordx4 v224, s[42:43]\n\t" "s_add_u32 m0, %18, 0x6500\n\t" "s_nop 0\n\t" "global_load_lds_dwordx4 v225, s[42:43]\n\t"
                     "s_add_u32 m0, %18, 0x6900\n\t" "s_nop 0\n\t" "global_load_lds_dwordx4 v226, s[42:43]\n\t" "s_add_u32 s42, s44, 0x6d8000\n\t" "s_addc_u32 s43, s45, 0\n\t" "s_add_u32 m0, %18, 0x6d20\n\t" "s_nop 0\n\t"
                     "global_load_lds_dwordx4 v224, s[42:43]\n\t" "s_add_u32 m0, %18, 0x7120\n\t" "s_nop 0\n\t" "global_load_lds_dwordx4 v225, s[42:43]\n\t" "s_add_u32 m0, %18, 0x7520\n\t" "s_nop 0\n\t"
                     "global_load_lds_dwordx4 v226, s[42:43]\n\t" "global_load_dword %14, %14, %20 sc1\n\t" "v_add_u32 v232, 0xc200, v227\n\t" "v_add_u32 v233, 0xc200, v228\n\t" "ds_read_b64_tr_b16 v[192:193], v232\n\t"
                     "ds_read_b64_tr_b16 v[194:195], v232 offset:768\n\t" "ds_read_b64_tr_b16 v[196:197], v232 offset:12416\n\t" "ds_read_b64_tr_b16 v[198:199], v232 offset:13184\n\t"
                     "ds_read_b64_tr_b16 v[64:65], v232 offset:32\n\t" "ds_read_b64_tr_b16 v[66:67], v232 offset:800\n\t" "ds_read_b64_tr_b16 v[68:69], v232 offset:12448\n\t"
                     "ds_read_b64_tr_b16 v[70:71], v232 offset:13216\n\t" "ds_read_b64_tr_b16 v[200:201], v233 offset:64\n\t" "ds_read_b64_tr_b16 v[202:203], v233 offset:832\n\t"
                     "ds_read_b64_tr_b16 v[204:205], v233 offset:12480\n\t" "ds_read_b64_tr_b16 v[206:207], v233 offset:13248\n\t" "ds_read_b64_tr_b16 v[208:209], v232 offset:24832\n\t"
                     "ds_read_b64_tr_b16 v[210:211], v232 offset:25600\n\t" "ds_read_b64_tr_b16 v[212:213], v232 offset:37248\n\t" "ds_read_b64_tr_b16 v[214:215], v232 offset:38016\n\t"
                     "ds_read_b64_tr_b16 v[80:81], v232 offset:24864\n\t" "ds_read_b64_tr_b16 v[82:83], v232 offset:25632\n\t" "ds_read_b64_tr_b16 v[84:85], v232 offset:37280\n\t"
                     "ds_read_b64_tr_b16 v[86:87], v232 offset:38048\n\t" "ds_read_b64_tr_b16 v[216:217], v233 offset:24896\n\t" "ds_read_b64_tr_b16 v[218:219], v233 offset:25664\n\t"
                     "ds_read_b64_tr_b16 v[220:221], v233 offset:37312\n\t" "ds_read_b64_tr_b16 v[222:223], v233 offset:38080\n\t" "s_waitcnt vmcnt(13) lgkmcnt(0)\n\t" "s_barrier\n\t" "v_add_u32 v232, 0x18400, v227\n\t"
                     "v_add_u32 v233, 0x18400, v228\n\t" "ds_read_b64_tr_b16 a[0:1], v232\n\t" "ds_read_b64_tr_b16 a[2:3], v232 offset:768\n\t" "ds_read_b64_tr_b16 a[4:5], v232 offset:12416\n\t"
                     "ds_read_b64_tr_b16 a[6:7], v232 offset:13184\n\t" "ds_read_b64_tr_b16 v[96:97], v232 offset:32\n\t" "ds_read_b64_tr_b16 v[98:99], v232 offset:800\n\t"
                     "ds_read_b64_tr_b16 v[100:101], v232 offset:12448\n\t" "ds_read_b64_tr_b16 v[102:103], v232 offset:13216\n\t" "ds_read_b64_tr_b16 a[8:9], v233 offset:64\n\t"
                     "ds_read_b64_tr_b16 a[10:11], v233 offset:832\n\t" "ds_read_b64_tr_b16 a[12:13], v233 offset:12480\n\t" "ds_read_b64_tr_b16 a[14:15], v233 offset:13248\n\t"
                     "ds_read_b64_tr_b16 a[16:17], v232 offset:24832\n\t" "ds_read_b64_tr_b16 a[18:19], v232 offset:25600\n\t" "ds_read_b64_tr_b16 a[20:21], v232 offset:37248\n\t"
                     "ds_read_b64_tr_b16 a[22:23], v232 offset:38016\n\t" "ds_read_b64_tr_b16 v[112:113], v232 offset:24864\n\t" "ds_read_b64_tr_b16 v[114:115], v232 offset:25632\n\t"
                     "ds_read_b64_tr_b16 v[116:117], v232 offset:37280\n\t" "ds_read_b64_tr_b16 v[118:119], v232 offset:38048\n\t" "ds_read_b64_tr_b16 a[24:25], v233 offset:24896\n\t"
                     "ds_read_b64_tr_b16 a[26:27], v233 offset:25664\n\t" "ds_read_b64_tr_b16 a[28:29], v233 offset:37312\n\t" "ds_read_b64_tr_b16 a[30:31], v233 offset:38080\n\t" "s_waitcnt vmcnt(0) lgkmcnt(0)\n\t"
                     "s_barrier\n\t" "v_add_u32 v232, 0x0, v227\n\t" "v_add_u32 v233, 0x0, v228\n\t" "ds_read_b64_tr_b16 a[32:33], v232\n\t" "ds_read_b64_tr_b16 a[34:35], v232 offset:768\n\t"
                     "ds_read_b64_tr_b16 a[36:37], v232 offset:12416\n\t" "ds_read_b64_tr_b16 a[38:39], v232 offset:13184\n\t" "ds_read_b64_tr_b16 a[40:41], v232 offset:32\n\t"
                     "ds_read_b64_tr_b16 a[42:43], v232 offset:800\n\t" "ds_read_b64_tr_b16 a[44:45], v232 offset:12448\n\t" "ds_read_b64_tr_b16 a[46:47], v232 offset:13216\n\t"
                     "ds_read_b64_tr_b16 a[48:49], v233 offset:64\n\t" "ds_read_b64_tr_b16 a[50:51], v233 offset:832\n\t" "ds_read_b64_tr_b16 a[52:53], v233 offset:12480\n\t"
                     "ds_read_b64_tr_b16 a[54:55], v233 offset:13248\n\t" "ds_read_b64_tr_b16 a[64:65], v232 offset:24832\n\t" "ds_read_b64_tr_b16 a[66:67], v232 offset:25600\n\t"
                     "ds_read_b64_tr_b16 a[68:69], v232 offset:37248\n\t" "ds_read_b64_tr_b16 a[70:71], v232 offset:38016\n\t" "ds_read_b64_tr_b16 a[72:73], v232 offset:24864\n\t"
                     "ds_read_b64_tr_b16 a[74:75], v232 offset:25632\n\t" "ds_read_b64_tr_b16 a[76:77], v232 offset:37280\n\t" "ds_read_b64_tr_b16 a[78:79], v232 offset:38048\n\t"
                     "ds_read_b64_tr_b16 a[80:81], v233 offset:24896\n\t" "ds_read_b64_tr_b16 a[82:83], v233 offset:25664\n\t" "ds_read_b64_tr_b16 a[84:85], v233 offset:37312\n\t"
                     "ds_read_b64_tr_b16 a[86:87], v233 offset:38080\n\t" "s_waitcnt lgkmcnt(0)\n\t" "s_barrier\n\t" "ds_write_b128 v230, v[0:3]\n\t" "ds_write_b128 v230, v[4:7] offset:1024\n\t"
                     "ds_write_b128 v230, v[8:11] offset:2048\n\t" "ds_write_b128 v230, v[12:15] offset:3072\n\t" "ds_write_b128 v230, v[16:19] offset:4096\n\t" "ds_write_b128 v230, v[20:23] offset:5120\n\t"
                     "ds_write_b128 v230, v[24:27] offset:6144\n\t" "ds_write_b128 v230, v[28:31] offset:7168\n\t" "ds_write_b128 v230, v[32:35] offset:8192\n\t" "ds_write_b128 v230, v[36:39] offset:9216\n\t"
                     "ds_write_b128 v230, v[40:43] offset:10240\n\t" "ds_write_b128 v230, v[44:47] offset:11264\n\t" "ds_write_b128 v230, v[48:51] offset:12288\n\t" "ds_write_b128 v230, v[52:55] offset:13312\n\t"
                     "ds_write_b128 v230, v[56:59] offset:14336\n\t" "ds_write_b128 v230, v[60:63] offset:15360\n\t" "ds_write_b128 v230, v[64:67] offset:16384\n\t" "ds_write_b128 v230, v[68:71] offset:17408\n\t"
                     "ds_write_b128 v230, v[72:75] offset:18432\n\t" "ds_write_b128 v230, v[76:79] offset:19456\n\t" "ds_write_b128 v230, v[80:83] offset:20480\n\t" "ds_write_b128 v230, v[84:87] offset:21504\n\t"
                     "ds_write_b128 v230, v[88:91] offset:22528\n\t" "ds_write_b128 v230, v[92:95] offset:23552\n\t" "ds_write_b128 v230, v[96:99] offset:24576\n\t" "ds_write_b128 v230, v[100:103] offset:25600\n\t"
                     "ds_write_b128 v230, v[104:107] offset:26624\n\t" "ds_write_b128 v230, v[108:111] offset:27648\n\t" "ds_write_b128 v230, v[112:115] offset:28672\n\t" "ds_write_b128 v230, v[116:119] offset:29696\n\t"
                     "ds_write_b128 v230, v[120:123] offset:30720\n\t" "ds_write_b128 v230, v[124:127] offset:31744\n\t"
                     : "=&{a[0:31]}"(wa[0]), "=&{a[32:63]}"(wa[1]), "=&{a[64:95]}"(wa[2]), "=&{a[96:127]}"(wa[3]), "=&{a[128:159]}"(wa[4]), "=&{a[160:191]}"(wa[5]), "=&{a[192:223]}"(wa[6]), "=&{a[224:255]}"(wa[7]), "=&{v[128:159]}"(wv[0]), "=&{v[160:191]}"(wv[1]), "=&{v[192:223]}"(wv[2]), "=&v"(y.r), "=&v"(y.g), "=&v"(y.e), "=&v"(kfl)
                     : "{v[224:231]}"(pa), "{s[44:45]}"(p.wq + h * DH * 2 + (size_t)(16 * w) * (HD * 2)), "{s[46:47]}"(p.wk + h * DH * 2 + (size_t)(16 * w) * (HD * 2)),
                       "s"(lds + 2 * w * GS), "s"(p.ws + OFF_LAT), "s"(fl + (F_K + h) * 128)
                     : "v0", "v1", "v2", "v3", "v4", "v5", "v6", "v7", "v8", "v9", "v10", "v11", "v12", "v13", "v14", "v15", "v16", "v17", "v18", "v19", "v20", "v21", "v22", "v23", "v24", "v25", "v26", "v27", "v28", "v29", "v30", "v31", "v32", "v33", "v34", "v35", "v36", "v37", "v38", "v39", "v40", "v41", "v42", "v43", "v44", "v45", "v46", "v47", "v48", "v49", "v50", "v51", "v52", "v53", "v54", "v55", "v56", "v57", "v58", "v59", "v60", "v61", "v62", "v63", "v64", "v65", "v66", "v67", "v68", "v69", "v70", "v71", "v72", "v73", "v74", "v75", "v76", "v77", "v78", "v79", "v80", "v81", "v82", "v83", "v84", "v85", "v86", "v87", "v88", "v89", "v90", "v91", "v92", "v93", "v94", "v95", "v96", "v97", "v98", "v99", "v100", "v101", "v102", "v103", "v104", "v105", "v106", "v107", "v108", "v109", "v110", "v111", "v112", "v113", "v114", "v115", "v116", "v117", "v118", "v119", "v120", "v121", "v122", "v123", "v124", "v125", "v126", "v127", "v232", "v233", "s42", "s43", "scc", "memory");
        const u64 t = sync_tag(y.e);
        const bool rdy = __builtin_amdgcn_ballot_w64((y.r[0] & ~1u) != (u32)t || y.r[1] != (u32)(t >> 32) || y.g[0] != (u32)t) == 0 && uni((int)kfl) != -1;
        if (rdy) kxdma();
        sd = sync_end(y, p.ws, phi, w == 0);
        if (!rdy) { flag_wait(fl, F_K + h, 1); kxdma(); }
        kxp += NH * 12288;
    }
    const u32 sidx = (lane & 2) ? 0xEEEEu : 0x4444u;
    const u32 moff = lane * 16;
    u32 kxa = kxl - 16 + (n >> 3) * 6160 + (16 * g + 4 * (n & 3) + ((n >> 1) & 2)) * 16;
    const u64 mb0 = 0xffff0000ffff0000ull;
    const int tp = (3 * w + (w & 1)) >> 1, ts = 1 + 3 * (w >> 1);
    u32 qw0 = lds + SL_IMG + tp * 512 + lane * 8, qw1 = lds + SL_IMG + ts * 512 + lane * 8 + 4 * (w & 1);
    const u32 kw0 = lds + SL_IMG + 3072 + w * 288 + n * 16 + 4 * g;
    const u32 qr0 = lds + SL_IMG + (g >> 1) * 512 + w * 128 + 64 * (g & 1) + 8 * qq;
    const u32 kr0 = lds + SL_IMG + 3072 + (qq >> 1) * 288 + g * 64 + (qq & 1) * 16 + 8 * (pp & 1);
    const u32 xso = 32768 + (h >> 4) * 16384 + (h & 15) * 512 + (w >> 1) * 256 + (n >> 3) * 64 + (n & 4) * 32 + (w & 1) * 32 + (g & 1) * 16 + (n & 3) * 4 + (g >> 1) * 8192;
    const u32 pso = (n < 8 ? 20480 + (h >> 1) * 256 + (n & 2) * 16 + (h & 1) * 8 + (n & 4) : 18432 + (h >> 2) * 256 + (h & 1) * 8 + (h & 2) * 2) + w * 64 + (g & 1) * 16 + (g >> 1) * 8192;
    const u64 psm = 0x01aa01aa01aa01aaull;
    const u32 kmo = (u32)(((n >> 3) * 4 + g) * 128 + (n & 7) * 16);
    u32x16 ad = u32x16{moff, sidx, lfa, kxa, qw0, qw1, kw0, kr0 + 1152 * min(w, 2) + (pp >> 1) * 576, qr0, kr0, xso, pso, 0x3dd53b94u, kmo, kmo, kmo};
    asm volatile("" : "+{v[224:239]}"(ad));
    const char* memp = p.ws + mslot(sd, 0);
    char* kmp = p.ws + OFF_KM + h * 3072 + min(w, 2) * 1024;
    const float kscale = 0.07216878364870322f;
    int budget = 30000;
    Clock clk; clk.init();
    u32x32 wk[3];
    asm volatile("" : "={v[64:95]}"(wk[0]), "={v[96:127]}"(wk[1]), "={v[32:63]}"(wk[2]));
    const int kfo = (F_K + 32 + h) * 128;
    for (int s = 0; s < NSEG; ++s) {
        int tries = 0;
        u64 bad, tp8, en = ~0ull;
        u32 tt;
        int kf;
        while (true) {
            const u32 tgt = clk.sched(phi, 0, cfg.G, tries ? 2 : 0);
            asm volatile("s_cmp_lg_u32 %12, 0\n\t" "s_cbranch_scc1 .Lrt_s\n\t" "s_waitcnt vmcnt(0) lgkmcnt(0)\n\t" "s_barrier\n\t" "s_cmp_lg_u32 %13, 0\n\t" "s_cbranch_scc1 .Lsp_s\n\t" ".Lrt_s:\n\t"
                         "ds_read_b128 v[64:67], v226\n\t" "v_mov_b64 v[40:41], 0\n\t" "ds_read_b128 v[68:71], v226 offset:1024\n\t" "v_mov_b64 v[42:43], 0\n\t" "ds_read_b128 v[72:75], v226 offset:2048\n\t"
                         "v_mov_b64 v[44:45], 0\n\t" "ds_read_b128 v[76:79], v226 offset:3072\n\t" "v_mov_b64 v[46:47], 0\n\t" "ds_read_b128 v[80:83], v226 offset:4096\n\t" "v_mov_b64 v[48:49], 0\n\t"
                         "ds_read_b128 v[84:87], v226 offset:5120\n\t" "v_mov_b64 v[50:51], 0\n\t" "ds_read_b128 v[88:91], v226 offset:6144\n\t" "v_mov_b64 v[52:53], 0\n\t" "ds_read_b128 v[92:95], v226 offset:7168\n\t"
                         "v_mov_b64 v[54:55], 0\n\t" "ds_read_b128 v[96:99], v226 offset:8192\n\t" "v_mov_b64 v[56:57], 0\n\t" "ds_read_b128 v[100:103], v226 offset:9216\n\t" "v_mov_b64 v[58:59], 0\n\t"
                         "ds_read_b128 v[104:107], v226 offset:10240\n\t" "v_mov_b64 v[60:61], 0\n\t" "ds_read_b128 v[108:111], v226 offset:11264\n\t" "v_mov_b64 v[62:63], 0\n\t"
                         "ds_read_b128 v[112:115], v226 offset:12288\n\t" "ds_read_b128 v[116:119], v226 offset:13312\n\t" "ds_read_b128 v[120:123], v226 offset:14336\n\t" "ds_read_b128 v[124:127], v226 offset:15360\n\t"
                         ".Lsp_s:\n\t" "s_memtime vcc\n\t" "s_waitcnt lgkmcnt(0)\n\t" "s_sub_i32 s42, %4, vcc_lo\n\t" "s_cmp_gt_i32 s42, 80\n\t" "s_cbranch_scc1 .Lsp_s\n\t" "s_max_i32 s42, s42, 0\n\t"
                         "s_sub_i32 s42, 92, s42\n\t" "s_and_b32 s42, s42, -4\n\t" "s_getpc_b64 vcc\n\t" "s_add_u32 vcc_lo, vcc_lo, s42\n\t" "s_addc_u32 vcc_hi, vcc_hi, 0\n\t" "s_setpc_b64 vcc\n\t" "s_nop 0\n\t" "s_nop 0\n\t"
                         "s_nop 0\n\t" "s_nop 0\n\t" "s_nop 0\n\t" "s_nop 0\n\t" "s_nop 0\n\t" "s_nop 0\n\t" "s_nop 0\n\t" "s_nop 0\n\t" "s_nop 0\n\t" "s_nop 0\n\t" "s_nop 0\n\t" "s_nop 0\n\t" "s_nop 0\n\t" "s_nop 0\n\t"
                         "s_nop 0\n\t" "s_nop 0\n\t" "s_nop 0\n\t" "s_nop 0\n\t" "global_load_dwordx4 v[0:3], v224, %5 offset:-4096 sc1\n\t" "s_nop 10\n\t" "global_load_dwordx4 v[4:7], v224, %5 offset:-3072 sc1\n\t"
                         "s_nop 10\n\t" "global_load_dwordx4 v[8:11], v224, %5 offset:-2048 sc1\n\t" "s_nop 10\n\t" "global_load_dwordx4 v[12:15], v224, %5 offset:-1024 sc1\n\t" "s_nop 10\n\t"
                         "global_load_dwordx4 v[16:19], v224, %5 sc1\n\t" "s_nop 10\n\t" "global_load_dwordx4 v[20:23], v224, %5 offset:1024 sc1\n\t" "s_nop 10\n\t" "global_load_dwordx4 v[24:27], v224, %5 offset:2048 sc1\n\t"
                         "s_nop 10\n\t" "global_load_dwordx4 v[28:31], v224, %5 offset:3072 sc1\n\t" "s_nop 10\n\t" "global_load_dwordx4 v[32:35], v224, %6 offset:-4096 sc1\n\t" "s_nop 10\n\t"
                         "global_load_dwordx4 v[36:39], v224, %6 offset:-3072 sc1\n\t" "s_waitcnt vmcnt(9)\n\t" "v_smfmac_f32_16x16x64_bf16 v[40:43], v[0:3], v[128:135], v225\n\t"
                         "v_smfmac_f32_16x16x64_bf16 v[44:47], v[0:3], v[64:71], v225\n\t" "ds_read_b128 v[64:67], v226 offset:16384\n\t" "v_smfmac_f32_16x16x64_bf16 v[48:51], v[0:3], v[136:143], v225\n\t"
                         "ds_read_b128 v[68:71], v226 offset:17408\n\t" "v_smfmac_f32_16x16x64_bf16 v[52:55], v[0:3], v[72:79], v225\n\t" "ds_read_b128 v[72:75], v226 offset:18432\n\t" "s_waitcnt vmcnt(8)\n\t"
                         "v_smfmac_f32_16x16x64_bf16 v[40:43], v[4:7], v[144:151], v225\n\t" "ds_read_b128 v[76:79], v226 offset:19456\n\t" "v_smfmac_f32_16x16x64_bf16 v[44:47], v[4:7], v[80:87], v225\n\t"
                         "ds_read_b128 v[80:83], v226 offset:20480\n\t" "v_smfmac_f32_16x16x64_bf16 v[48:51], v[4:7], v[152:159], v225\n\t" "ds_read_b128 v[84:87], v226 offset:21504\n\t"
                         "v_smfmac_f32_16x16x64_bf16 v[52:55], v[4:7], v[88:95], v225\n\t" "ds_read_b128 v[88:91], v226 offset:22528\n\t" "s_waitcnt vmcnt(7)\n\t"
                         "v_smfmac_f32_16x16x64_bf16 v[40:43], v[8:11], v[160:167], v225\n\t" "ds_read_b128 v[92:95], v226 offset:23552\n\t" "v_max_u32 v238, v0, v4\n\t"
                         "v_smfmac_f32_16x16x64_bf16 v[44:47], v[8:11], v[96:103], v225\n\t" "ds_read_b128 v[96:99], v226 offset:24576\n\t" "v_cmp_eq_u32 vcc, -1, v238\n\t"
                         "v_smfmac_f32_16x16x64_bf16 v[48:51], v[8:11], v[168:175], v225\n\t" "ds_read_b128 v[100:103], v226 offset:25600\n\t" "v_smfmac_f32_16x16x64_bf16 v[52:55], v[8:11], v[104:111], v225\n\t"
                         "s_and_b64 vcc, vcc, %11\n\t" "s_cbranch_scc1 .Lsk_s\n\t" "ds_read_b128 v[104:107], v226 offset:26624\n\t" "s_waitcnt vmcnt(6)\n\t" "v_smfmac_f32_16x16x64_bf16 v[40:43], v[12:15], v[176:183], v225\n\t"
                         "ds_read_b128 v[108:111], v226 offset:27648\n\t" "v_smfmac_f32_16x16x64_bf16 v[44:47], v[12:15], v[112:119], v225\n\t" "ds_read_b128 v[112:115], v226 offset:28672\n\t"
                         "v_smfmac_f32_16x16x64_bf16 v[48:51], v[12:15], v[184:191], v225\n\t" "ds_read_b128 v[116:119], v226 offset:29696\n\t" "v_smfmac_f32_16x16x64_bf16 v[52:55], v[12:15], v[120:127], v225\n\t"
                         "ds_read_b128 v[120:123], v226 offset:30720\n\t" "s_waitcnt vmcnt(5)\n\t" "v_smfmac_f32_16x16x64_bf16 v[40:43], v[16:19], v[192:199], v225\n\t" "ds_read_b128 v[124:127], v226 offset:31744\n\t"
                         "s_waitcnt lgkmcnt(14)\n\t" "v_smfmac_f32_16x16x64_bf16 v[44:47], v[16:19], v[64:71], v225\n\t" "ds_read_b128 v[64:67], v227\n\t" "v_smfmac_f32_16x16x64_bf16 v[48:51], v[16:19], v[200:207], v225\n\t"
                         "ds_read_b128 v[68:71], v227 offset:1024\n\t" "s_waitcnt lgkmcnt(14)\n\t" "v_smfmac_f32_16x16x64_bf16 v[52:55], v[16:19], v[72:79], v225\n\t" "ds_read_b128 v[72:75], v227 offset:16\n\t"
                         "s_waitcnt vmcnt(4)\n\t" "v_smfmac_f32_16x16x64_bf16 v[40:43], v[20:23], v[208:215], v225\n\t" "ds_read_b128 v[76:79], v227 offset:1040\n\t" "s_waitcnt lgkmcnt(14)\n\t"
                         "v_smfmac_f32_16x16x64_bf16 v[44:47], v[20:23], v[80:87], v225\n\t" "ds_read_b128 v[80:83], v227 offset:2048\n\t" "v_smfmac_f32_16x16x64_bf16 v[48:51], v[20:23], v[216:223], v225\n\t"
                         "ds_read_b128 v[84:87], v227 offset:3072\n\t" "s_waitcnt lgkmcnt(14)\n\t" "v_smfmac_f32_16x16x64_bf16 v[52:55], v[20:23], v[88:95], v225\n\t" "ds_read_b128 v[88:91], v227 offset:2064\n\t"
                         "s_waitcnt vmcnt(3)\n\t" "v_smfmac_f32_16x16x64_bf16 v[40:43], v[24:27], a[0:7], v225\n\t" "ds_read_b128 v[92:95], v227 offset:3088\n\t" "s_waitcnt lgkmcnt(14)\n\t"
                         "v_smfmac_f32_16x16x64_bf16 v[44:47], v[24:27], v[96:103], v225\n\t" "ds_read_b128 v[96:99], v227 offset:4096\n\t" "v_smfmac_f32_16x16x64_bf16 v[48:51], v[24:27], a[8:15], v225\n\t"
                         "ds_read_b128 v[100:103], v227 offset:5120\n\t" "s_waitcnt lgkmcnt(14)\n\t" "v_smfmac_f32_16x16x64_bf16 v[52:55], v[24:27], v[104:111], v225\n\t" "ds_read_b128 v[104:107], v227 offset:4112\n\t"
                         "s_waitcnt vmcnt(2)\n\t" "v_smfmac_f32_16x16x64_bf16 v[40:43], v[28:31], a[16:23], v225\n\t" "ds_read_b128 v[108:111], v227 offset:5136\n\t" "s_waitcnt lgkmcnt(14)\n\t"
                         "v_smfmac_f32_16x16x64_bf16 v[44:47], v[28:31], v[112:119], v225\n\t" "v_smfmac_f32_16x16x64_bf16 v[48:51], v[28:31], a[24:31], v225\n\t" "s_waitcnt lgkmcnt(12)\n\t"
                         "v_smfmac_f32_16x16x64_bf16 v[52:55], v[28:31], v[120:127], v225\n\t" "s_waitcnt vmcnt(1)\n\t" "v_smfmac_f32_16x16x64_bf16 v[40:43], v[32:35], a[32:39], v225\n\t" "v_mov_b64 v[112:113], 0\n\t"
                         "v_smfmac_f32_16x16x64_bf16 v[44:47], v[32:35], a[40:47], v225\n\t" "v_mov_b64 v[114:115], 0\n\t" "v_smfmac_f32_16x16x64_bf16 v[48:51], v[32:35], a[48:55], v225\n\t" "v_mov_b64 v[116:117], 0\n\t"
                         "v_smfmac_f32_16x16x64_bf16 v[52:55], v[32:35], a[56:63], v225\n\t" "v_mov_b64 v[118:119], 0\n\t" "s_waitcnt vmcnt(0)\n\t" "v_smfmac_f32_16x16x64_bf16 v[40:43], v[36:39], a[64:71], v225\n\t"
                         "v_mov_b64 v[120:121], 0\n\t" "v_smfmac_f32_16x16x64_bf16 v[44:47], v[36:39], a[72:79], v225\n\t" "v_mov_b64 v[122:123], 0\n\t" "v_smfmac_f32_16x16x64_bf16 v[48:51], v[36:39], a[80:87], v225\n\t"
                         "v_mov_b64 v[124:125], 0\n\t" "v_smfmac_f32_16x16x64_bf16 v[52:55], v[36:39], a[88:95], v225\n\t" "v_add_f32 v40, v40, v42\n\t" "v_add_f32 v41, v41, v43\n\t"
                         "v_smfmac_f32_16x16x64_bf16 v[56:59], v[0:3], a[96:103], v225\n\t" "v_cmp_u_f32 vcc, v40, v41\n\t" "v_smfmac_f32_16x16x64_bf16 v[56:59], v[4:7], a[104:111], v225\n\t"
                         "v_cvt_pk_bf16_f32 v42, v40, v41\n\t" "v_add_f32 v44, v44, v46\n\t" "v_smfmac_f32_16x16x64_bf16 v[56:59], v[8:11], a[112:119], v225\n\t" "s_and_b64 vcc, vcc, %11\n\t" "s_cbranch_scc1 .Lsk_s\n\t"
                         "v_smfmac_f32_16x16x64_bf16 v[56:59], v[12:15], a[120:127], v225\n\t" "v_add_f32 v45, v45, v47\n\t" "v_cvt_pk_bf16_f32 v43, v44, v45\n\t"
                         "v_smfmac_f32_16x16x64_bf16 v[56:59], v[16:19], a[128:135], v225\n\t" "ds_write_b64 v228, v[42:43]\n\t" "v_add_f32 v48, v48, v50\n\t"
                         "v_smfmac_f32_16x16x64_bf16 v[56:59], v[20:23], a[136:143], v225\n\t" "v_add_f32 v49, v49, v51\n\t" "v_cvt_pk_bf16_f32 v46, v48, v49\n\t"
                         "v_smfmac_f32_16x16x64_bf16 v[56:59], v[24:27], a[144:151], v225\n\t" "ds_write_b32 v229, v46\n\t" "v_add_f32 v52, v52, v54\n\t" "v_smfmac_f32_16x16x64_bf16 v[56:59], v[28:31], a[152:159], v225\n\t"
                         "v_add_f32 v53, v53, v55\n\t" "v_cvt_pk_bf16_f32 v54, v52, v53\n\t" "v_smfmac_f32_16x16x64_bf16 v[56:59], v[32:35], a[160:167], v225\n\t" "ds_write_b32 v230, v54\n\t"
                         "v_smfmac_f32_16x16x64_bf16 v[56:59], v[36:39], a[168:175], v225\n\t" "v_smfmac_f32_16x16x64_bf16 v[60:63], v[0:3], a[176:183], v225\n\t"
                         "v_smfmac_f32_16x16x64_bf16 v[60:63], v[4:7], a[184:191], v225\n\t" "v_add_f32 v56, v56, v58\n\t" "v_smfmac_f32_16x16x64_bf16 v[60:63], v[8:11], a[192:199], v225\n\t" "s_waitcnt lgkmcnt(0)\n\t"
                         "s_barrier\n\t" "v_smfmac_f32_16x16x64_bf16 v[60:63], v[12:15], a[200:207], v225\n\t" "ds_read_b64_tr_b16 v[40:41], v232\n\t" "v_add_f32 v57, v57, v59\n\t"
                         "v_smfmac_f32_16x16x64_bf16 v[60:63], v[16:19], a[208:215], v225\n\t" "ds_read_b64_tr_b16 v[42:43], v232 offset:32\n\t" "v_cvt_pk_bf16_f32 v58, v56, v57\n\t"
                         "v_smfmac_f32_16x16x64_bf16 v[60:63], v[20:23], a[216:223], v225\n\t" "ds_write_b32 v230, v58 offset:1152\n\t" "v_smfmac_f32_16x16x64_bf16 v[60:63], v[24:27], a[224:231], v225\n\t"
                         "ds_read_b64_tr_b16 v[44:45], v232 offset:1024\n\t" "ds_read_b64_tr_b16 v[46:47], v232 offset:1056\n\t" "v_smfmac_f32_16x16x64_bf16 v[60:63], v[28:31], a[232:239], v225\n\t"
                         "ds_read_b64_tr_b16 v[48:49], v232 offset:2048\n\t" "ds_read_b64_tr_b16 v[50:51], v232 offset:2080\n\t" "v_smfmac_f32_16x16x64_bf16 v[60:63], v[32:35], a[240:247], v225\n\t"
                         "ds_read_b64_tr_b16 v[0:1], v233\n\t" "ds_read_b64_tr_b16 v[2:3], v233 offset:32\n\t" "v_smfmac_f32_16x16x64_bf16 v[60:63], v[36:39], a[248:255], v225\n\t"
                         "ds_read_b64_tr_b16 v[4:5], v233 offset:576\n\t" "ds_read_b64_tr_b16 v[6:7], v233 offset:608\n\t" "s_waitcnt lgkmcnt(9)\n\t" "v_smfmac_f32_16x16x64_bf16 v[112:115], v[40:43], v[64:71], v225\n\t"
                         "s_waitcnt lgkmcnt(8)\n\t" "s_barrier\n\t" "v_smfmac_f32_16x16x64_bf16 v[116:119], v[40:43], v[72:79], v225\n\t" "ds_read_b64_tr_b16 v[8:9], v233 offset:1152\n\t" "s_waitcnt lgkmcnt(7)\n\t"
                         "v_add_f32 v60, v60, v62\n\t" "v_add_f32 v61, v61, v63\n\t" "v_smfmac_f32_16x16x64_bf16 v[112:115], v[44:47], v[80:87], v225\n\t" "ds_read_b64_tr_b16 v[10:11], v233 offset:1184\n\t"
                         "v_cvt_pk_bf16_f32 v62, v60, v61\n\t" "v_smfmac_f32_16x16x64_bf16 v[116:119], v[44:47], v[88:95], v225\n\t" "ds_write_b32 v230, v62 offset:2304\n\t" "s_waitcnt lgkmcnt(7)\n\t"
                         "v_smfmac_f32_16x16x64_bf16 v[112:115], v[48:51], v[96:103], v225\n\t" "ds_read_b64_tr_b16 v[12:13], v233 offset:1728\n\t" "v_smfmac_f32_16x16x64_bf16 v[116:119], v[48:51], v[104:111], v225\n\t"
                         "ds_read_b64_tr_b16 v[14:15], v233 offset:1760\n\t" "s_waitcnt lgkmcnt(5)\n\t" "v_smfmac_f32_16x16x64_bf16 v[120:123], v[40:43], v[0:7], v225\n\t" "v_cndmask_b32 v24, v112, v113, %8\n\t"
                         "v_cndmask_b32 v25, v114, v115, %8\n\t" "v_add_f32 v24, v24, v25\n\t" "v_pk_add_f32 v[26:27], v[116:117], v[118:119]\n\t" "v_cndmask_b32 v25, v26, v27, %8\n\t" "v_max_f32 v26, v24, v25\n\t"
                         "s_waitcnt lgkmcnt(2)\n\t" "s_barrier\n\t" "ds_read_b64_tr_b16 v[16:17], v233 offset:2304\n\t" "v_max_f32_dpp v27, v26, v26 row_ror:4 row_mask:0xf bank_mask:0xf\n\t"
                         "ds_read_b64_tr_b16 v[18:19], v233 offset:2336\n\t" "v_max_f32_dpp v27, v26, v27 row_ror:8 row_mask:0xf bank_mask:0xf\n\t" "v_max_f32_dpp v27, v26, v27 row_ror:12 row_mask:0xf bank_mask:0xf\n\t"
                         "ds_read_b64_tr_b16 v[20:21], v233 offset:2880\n\t" "ds_read_b64_tr_b16 v[22:23], v233 offset:2912\n\t" "v_max_f32_dpp v30, v27, v27 row_ror:1 row_mask:0xf bank_mask:0xf\n\t"
                         "v_max_f32_dpp v30, v27, v30 row_ror:2 row_mask:0xf bank_mask:0xf\n\t" "v_max_f32_dpp v30, v27, v30 row_ror:3 row_mask:0xf bank_mask:0xf\n\t" "s_waitcnt lgkmcnt(4)\n\t"
                         "v_pk_add_f32 v[24:25], v[24:25], v[30:31] op_sel_hi:[1,0] neg_lo:[0,1] neg_hi:[0,1]\n\t" "v_pk_mul_f32 v[24:25], v[24:25], v[236:237] op_sel_hi:[1,0]\n\t" "v_exp_f32 v24, v24\n\t"
                         "v_exp_f32 v25, v25\n\t" "v_mul_f32 v31, %10, v30\n\t" "v_cvt_pk_bf16_f32 v28, |v24|, |v25|\n\t" "v_smfmac_f32_16x16x64_bf16 v[120:123], v[44:47], v[8:15], v225\n\t"
                         "global_store_dword v234, v28, %7 offset:-4096 sc1\n\t" "s_waitcnt lgkmcnt(0)\n\t" "v_add_f32 v26, v24, v25\n\t" "v_smfmac_f32_16x16x64_bf16 v[120:123], v[48:51], v[16:23], v225\n\t"
                         "s_nop 0\n\t" "v_add_f32_dpp v27, v26, v26 row_ror:4 row_mask:0xf bank_mask:0xf\n\t" "v_add_f32_dpp v27, v26, v27 row_ror:8 row_mask:0xf bank_mask:0xf\n\t"
                         "v_add_f32_dpp v27, v26, v27 row_ror:12 row_mask:0xf bank_mask:0xf\n\t" "s_nop 1\n\t" "v_add_f32_dpp v29, v27, v27 row_ror:1 row_mask:0xf bank_mask:0xf\n\t"
                         "v_pk_add_f32 v[24:25], v[120:121], v[122:123]\n\t" "v_cndmask_b32 v24, v24, v25, %8\n\t" "v_max_f32 v26, v24, v30\n\t" "v_add_f32_dpp v29, v27, v29 row_ror:2 row_mask:0xf bank_mask:0xf\n\t"
                         "v_add_f32_dpp v29, v27, v29 row_ror:3 row_mask:0xf bank_mask:0xf\n\t" "v_max_f32_dpp v27, v26, v26 row_ror:1 row_mask:0xf bank_mask:0xf\n\t"
                         "v_max_f32_dpp v27, v26, v27 row_ror:2 row_mask:0xf bank_mask:0xf\n\t" "v_max_f32_dpp v27, v26, v27 row_ror:3 row_mask:0xf bank_mask:0xf\n\t" "v_mul_f32 v24, %10, v24\n\t"
                         "v_mul_f32 v29, 0x3e000000, v29\n\t" "v_max_f32_dpp v26, v27, v27 row_ror:4 row_mask:0xf bank_mask:0xf\n\t" "v_fma_f32 v24, -v26, %10, v24\n\t" "v_fma_f32 v25, -v26, %10, v31\n\t"
                         "v_exp_f32 v24, v24\n\t" "v_exp_f32 v25, v25\n\t" "s_nop 0\n\t" "v_fma_f32 v26, v25, v29, v24\n\t" "v_mov_b32_dpp v124, v24 quad_perm:[0,1,2,3] row_mask:0xf bank_mask:0x3\n\t"
                         "v_mov_b32_dpp v125, v24 quad_perm:[1,0,3,2] row_mask:0xf bank_mask:0x3\n\t" "v_add_f32_dpp v27, v26, v26 row_ror:1 row_mask:0xf bank_mask:0xf\n\t"
                         "v_add_f32_dpp v27, v26, v27 row_ror:2 row_mask:0xf bank_mask:0xf\n\t" "v_add_f32_dpp v27, v26, v27 row_ror:3 row_mask:0xf bank_mask:0xf\n\t"
                         "v_mov_b32_dpp v124, v25 quad_perm:[0,1,2,3] row_mask:0x5 bank_mask:0x4\n\t" "v_mov_b32_dpp v125, v25 quad_perm:[0,1,2,3] row_mask:0xa bank_mask:0x4\n\t"
                         "v_add_f32_dpp v26, v27, v27 row_ror:4 row_mask:0xf bank_mask:0xf\n\t" "v_rcp_f32 v26, v26\n\t" "s_mov_b64 exec, %9\n\t" "v_pk_mul_f32 v[124:125], v[124:125], v[26:27] op_sel_hi:[1,0]\n\t"
                         "v_cvt_pk_bf16_f32 v24, |v124|, |v125|\n\t" "global_store_dword v235, v24, %7 offset:-4096 sc1\n\t" "s_mov_b64 exec, -1\n\t" "s_memrealtime %1\n\t" "s_load_dword %2, %14, %13 offset:%15\n\t"
                         "ds_read_b64_tr_b16 v[36:37], v231\n\t" "ds_read_b64_tr_b16 v[38:39], v231 offset:32\n\t" "v_readfirstlane_b32 %3, v237\n\t" "s_mov_b32 m0, %17\n\t" "v_mov_b64 v[40:41], 0\n\t"
                         "v_mov_b64 v[42:43], 0\n\t" "v_mov_b64 v[44:45], 0\n\t" "v_mov_b64 v[46:47], 0\n\t" "v_mov_b64 v[48:49], 0\n\t" "v_mov_b64 v[50:51], 0\n\t" "v_mov_b64 v[52:53], 0\n\t" "v_mov_b64 v[54:55], 0\n\t"
                         "v_mov_b64 v[56:57], 0\n\t" "v_mov_b64 v[58:59], 0\n\t" "v_mov_b64 v[60:61], 0\n\t" "v_mov_b64 v[62:63], 0\n\t" "v_mov_b32 v237, %18\n\t" "s_waitcnt lgkmcnt(0)\n\t" "ds_read_b128 v[64:67], v226\n\t"
                         "ds_read_b128 v[68:71], v226 offset:1024\n\t" "ds_read_b128 v[72:75], v226 offset:2048\n\t" "ds_read_b128 v[76:79], v226 offset:3072\n\t" "ds_read_b128 v[80:83], v226 offset:4096\n\t"
                         "ds_read_b128 v[84:87], v226 offset:5120\n\t" "s_cmp_eq_u32 %3, -1\n\t" "s_cbranch_scc1 .Lnk_s\n\t" "global_load_lds_dwordx4 v224, %16 sc1\n\t" "global_load_lds_dwordx4 v224, %16 offset:1024 sc1\n\t"
                         "global_load_lds_dwordx4 v224, %16 offset:2048 sc1\n\t" ".Lnk_s:\n\t" "global_store_dwordx4 v239, v[36:39], %21 sc1\n\t" "s_and_b64 exec, %19, 1\n\t" "global_load_dword v237, v237, %20 sc1\n\t"
                         "s_mov_b64 exec, -1\n\t" "ds_read_b128 v[88:91], v226 offset:6144\n\t" "ds_read_b128 v[92:95], v226 offset:7168\n\t" "ds_read_b128 v[96:99], v226 offset:8192\n\t"
                         "ds_read_b128 v[100:103], v226 offset:9216\n\t" "ds_read_b128 v[104:107], v226 offset:10240\n\t" "ds_read_b128 v[108:111], v226 offset:11264\n\t" "ds_read_b128 v[112:115], v226 offset:12288\n\t"
                         "ds_read_b128 v[116:119], v226 offset:13312\n\t" "ds_read_b128 v[120:123], v226 offset:14336\n\t" "ds_read_b128 v[124:127], v226 offset:15360\n\t" ".Lsk_s:\n\t" "s_mov_b64 %0, vcc\n\t"
                         : "=&s"(bad), "=&s"(tp8), "=&s"(tt), "=&s"(kf)
                         : "s"(uni((int)tgt)), "s"(memp), "s"(memp + 16384), "s"((u64)memp & ~8192ull), "s"(mb0), "s"(psm), "s"(kscale * 1.4426950408889634f), "s"(en), "s"(tries), "s"(4 * s),
                           "s"(__builtin_amdgcn_kernarg_segment_ptr()), "n"(TT_OFF), "s"(kxp), "s"(kxw), "s"(kfo + (s < 6 ? 0 : (s + 2) >> 3) * 4096), "s"(0x0040404040404044ull >> s), "s"(fl), "s"(kmp),
                           "{v[224:239]}"(ad), "{v[64:95]}"(wk[0]), "{v[96:127]}"(wk[1]), "{v[32:63]}"(wk[2]),
                           "{a[0:31]}"(wa[0]), "{a[32:63]}"(wa[1]), "{a[64:95]}"(wa[2]), "{a[96:127]}"(wa[3]), "{a[128:159]}"(wa[4]), "{a[160:191]}"(wa[5]), "{a[192:223]}"(wa[6]), "{a[224:255]}"(wa[7]), "{v[128:159]}"(wv[0]), "{v[160:191]}"(wv[1]), "{v[192:223]}"(wv[2])
                         : "v0", "v1", "v2", "v3", "v4", "v5", "v6", "v7", "v8", "v9", "v10", "v11", "v12", "v13", "v14", "v15", "v16", "v17", "v18", "v19", "v20", "v21", "v22", "v23", "v24", "v25", "v26", "v27", "v28", "v29", "v30", "v31", "vcc", "scc", "s42", "memory");
            if (__builtin_expect(bad == 0, 1)) break;
            waitvm<0>();
            if (((++tries & 15) == 0 && !unwritten(memp, ad[0])) || --budget < 0) en = 0;
            phi += (u32)cfg.G;
        }
        if (__builtin_expect(kf == -1, 0)) { flag_wait(fl, F_K + pblk(s + 1) * 32 + h, 1); kxdma(); }
        kxp += s < NSEG - 2 ? NH * 12288 : 0;
        clk.calib();
        if (__builtin_expect(s >= NSEG - 2, 0)) {
            waitvm<0>();
            barrier();
            if (w == 3) flag_add(fl, F_PROG + 8 * s + (h & 7), 1);
        }
        follow(phi, (int)(tt >> 16) - VISS, (u32)tp8, cfg.G);
        phi += tt & 0xffffu;
        memp = p.ws + mslot(sd, s + 1); kmp += NH * 3072;
    }
    if (h == 0 && w == 0) {
        flag_wait(fl, F_ACK, 256);
        Sync y;
        sync_ld(y, p.ws);
        asm volatile("s_waitcnt vmcnt(0)" : "+v"(y.r), "+v"(y.g), "+v"(y.e) : : "memory");
        const u64 t = ((u64)y.e[1] << 32 | y.e[0]) + 1;
        if (lanex() == 0) gst8(p.ws + OFF_LAT, 1024, u32x2{(u32)t, (u32)(t >> 32)});
    }
    if (h == 0 && w == 0) { const u32 wake = tk_ + 20000; while ((int)(rt() - wake) < 0) __builtin_amdgcn_s_sleep(4); }
    asm volatile(".Lkm_last:\n\ts_waitcnt vmcnt(0)" : : : "memory");
}

DI void proj_role(const Ptrs& p, const ChainCfg cfg, int h, u32 lds, int w) {
    u32x32 wa[8], wv[4];
    u32 phi;
    int sd;
    {
        const int lane = lanex(), tid = 64 * w + lane;
        const int g = lane >> 4, qq = (lane >> 2) & 3, pp = lane & 3;
        constexpr int GS = 3104;
        auto dvo = [&](int i) { const int ix = i * 64 + lane; return (u32)((ix / 24) * (HD * 2) + (ix % 24) * 16); };
        auto ovo = [&](int i) { const int u = i * 64 + lane, r = u / 80; return (u32)(r * 1280 + (((u % 80) ^ (2 * (r ^ (w & 2)))) << 4)); };
        const u32 tra = lds + (2 * (g >> 1) + (qq >> 1)) * GS + (4 * (g & 1) + (qq & 1)) * 384 + 96 * w + 32 * (w & 1) + 8 * pp;
        const u32 tro = (4 * g + (qq & 1) + 16 * (qq >> 1)) * 1280 + 8 * pp + ((u32)(2 * w ^ 2 * ((4 * g + qq) & 7)) << 4);
        u32x16 pa = u32x16{dvo(0), dvo(1), dvo(2), tra, tra - 96 * (w & 1), tro, lds + (u32)((w & 1) * 24576 + (w >> 1) * 99328 + lane * 16), (u32)(lane & 31) * 8,
                           ovo(0), ovo(1), ovo(2), ovo(3), ovo(4), tro, tro, tro};
        Sync y;
        asm volatile("s_nop 4\n\t" "s_add_u32 s42, s44, 0x0\n\t" "s_addc_u32 s43, s45, 0\n\t" "s_add_u32 m0, %18, 0x0\n\t" "s_nop 0\n\t" "global_load_lds_dwordx4 v224, s[42:43]\n\t" "s_add_u32 m0, %18, 0x400\n\t"
                     "s_nop 0\n\t" "global_load_lds_dwordx4 v225, s[42:43]\n\t" "s_add_u32 m0, %18, 0x800\n\t" "s_nop 0\n\t" "global_load_lds_dwordx4 v226, s[42:43]\n\t" "s_add_u32 s42, s44, 0x18000\n\t"
                     "s_addc_u32 s43, s45, 0\n\t" "s_add_u32 m0, %18, 0xc20\n\t" "s_nop 0\n\t" "global_load_lds_dwordx4 v224, s[42:43]\n\t" "s_add_u32 m0, %18, 0x1020\n\t" "s_nop 0\n\t"
                     "global_load_lds_dwordx4 v225, s[42:43]\n\t" "s_add_u32 m0, %18, 0x1420\n\t" "s_nop 0\n\t" "global_load_lds_dwordx4 v226, s[42:43]\n\t" "s_add_u32 s42, s44, 0xc0000\n\t" "s_addc_u32 s43, s45, 0\n\t"
                     "s_add_u32 m0, %18, 0x6100\n\t" "s_nop 0\n\t" "global_load_lds_dwordx4 v224, s[42:43]\n\t" "s_add_u32 m0, %18, 0x6500\n\t" "s_nop 0\n\t" "global_load_lds_dwordx4 v225, s[42:43]\n\t"
                     "s_add_u32 m0, %18, 0x6900\n\t" "s_nop 0\n\t" "global_load_lds_dwordx4 v226, s[42:43]\n\t" "s_add_u32 s42, s44, 0xd8000\n\t" "s_addc_u32 s43, s45, 0\n\t" "s_add_u32 m0, %18, 0x6d20\n\t" "s_nop 0\n\t"
                     "global_load_lds_dwordx4 v224, s[42:43]\n\t" "s_add_u32 m0, %18, 0x7120\n\t" "s_nop 0\n\t" "global_load_lds_dwordx4 v225, s[42:43]\n\t" "s_add_u32 m0, %18, 0x7520\n\t" "s_nop 0\n\t"
                     "global_load_lds_dwordx4 v226, s[42:43]\n\t" "s_add_u32 s42, s44, 0x180000\n\t" "s_addc_u32 s43, s45, 0\n\t" "s_add_u32 m0, %18, 0xc200\n\t" "s_nop 0\n\t" "global_load_lds_dwordx4 v224, s[42:43]\n\t"
                     "s_add_u32 m0, %18, 0xc600\n\t" "s_nop 0\n\t" "global_load_lds_dwordx4 v225, s[42:43]\n\t" "s_add_u32 m0, %18, 0xca00\n\t" "s_nop 0\n\t" "global_load_lds_dwordx4 v226, s[42:43]\n\t"
                     "s_add_u32 s42, s44, 0x198000\n\t" "s_addc_u32 s43, s45, 0\n\t" "s_add_u32 m0, %18, 0xce20\n\t" "s_nop 0\n\t" "global_load_lds_dwordx4 v224, s[42:43]\n\t" "s_add_u32 m0, %18, 0xd220\n\t" "s_nop 0\n\t"
                     "global_load_lds_dwordx4 v225, s[42:43]\n\t" "s_add_u32 m0, %18, 0xd620\n\t" "s_nop 0\n\t" "global_load_lds_dwordx4 v226, s[42:43]\n\t" "s_add_u32 s42, s44, 0x240000\n\t" "s_addc_u32 s43, s45, 0\n\t"
                     "s_add_u32 m0, %18, 0x12300\n\t" "s_nop 0\n\t" "global_load_lds_dwordx4 v224, s[42:43]\n\t" "s_add_u32 m0, %18, 0x12700\n\t" "s_nop 0\n\t" "global_load_lds_dwordx4 v225, s[42:43]\n\t"
                     "s_add_u32 m0, %18, 0x12b00\n\t" "s_nop 0\n\t" "global_load_lds_dwordx4 v226, s[42:43]\n\t" "s_add_u32 s42, s44, 0x258000\n\t" "s_addc_u32 s43, s45, 0\n\t" "s_add_u32 m0, %18, 0x12f20\n\t"
                     "s_nop 0\n\t" "global_load_lds_dwordx4 v224, s[42:43]\n\t" "s_add_u32 m0, %18, 0x13320\n\t" "s_nop 0\n\t" "global_load_lds_dwordx4 v225, s[42:43]\n\t" "s_add_u32 m0, %18, 0x13720\n\t" "s_nop 0\n\t"
                     "global_load_lds_dwordx4 v226, s[42:43]\n\t" "s_waitcnt vmcnt(12) lgkmcnt(0)\n\t" "s_barrier\n\t" "s_add_u32 s42, s44, 0x300000\n\t" "s_addc_u32 s43, s45, 0\n\t" "s_add_u32 m0, %18, 0x18400\n\t"
                     "s_nop 0\n\t" "global_load_lds_dwordx4 v224, s[42:43]\n\t" "s_add_u32 m0, %18, 0x18800\n\t" "s_nop 0\n\t" "global_load_lds_dwordx4 v225, s[42:43]\n\t" "s_add_u32 m0, %18, 0x18c00\n\t" "s_nop 0\n\t"
                     "global_load_lds_dwordx4 v226, s[42:43]\n\t" "s_add_u32 s42, s44, 0x318000\n\t" "s_addc_u32 s43, s45, 0\n\t" "s_add_u32 m0, %18, 0x19020\n\t" "s_nop 0\n\t" "global_load_lds_dwordx4 v224, s[42:43]\n\t"
                     "s_add_u32 m0, %18, 0x19420\n\t" "s_nop 0\n\t" "global_load_lds_dwordx4 v225, s[42:43]\n\t" "s_add_u32 m0, %18, 0x19820\n\t" "s_nop 0\n\t" "global_load_lds_dwordx4 v226, s[42:43]\n\t"
                     "s_add_u32 s42, s44, 0x3c0000\n\t" "s_addc_u32 s43, s45, 0\n\t" "s_add_u32 m0, %18, 0x1e500\n\t" "s_nop 0\n\t" "global_load_lds_dwordx4 v224, s[42:43]\n\t" "s_add_u32 m0, %18, 0x1e900\n\t"
                     "s_nop 0\n\t" "global_load_lds_dwordx4 v225, s[42:43]\n\t" "s_add_u32 m0, %18, 0x1ed00\n\t" "s_nop 0\n\t" "global_load_lds_dwordx4 v226, s[42:43]\n\t" "s_add_u32 s42, s44, 0x3d8000\n\t"
                     "s_addc_u32 s43, s45, 0\n\t" "s_add_u32 m0, %18, 0x1f120\n\t" "s_nop 0\n\t" "global_load_lds_dwordx4 v224, s[42:43]\n\t" "s_add_u32 m0, %18, 0x1f520\n\t" "s_nop 0\n\t"
                     "global_load_lds_dwordx4 v225, s[42:43]\n\t" "s_add_u32 m0, %18, 0x1f920\n\t" "s_nop 0\n\t" "global_load_lds_dwordx4 v226, s[42:43]\n\t" "v_add_u32 v237, 0x0, v227\n\t" "v_add_u32 v238, 0x0, v228\n\t"
                     "ds_read_b64_tr_b16 a[0:1], v237\n\t" "ds_read_b64_tr_b16 a[2:3], v237 offset:768\n\t" "ds_read_b64_tr_b16 a[4:5], v237 offset:12416\n\t" "ds_read_b64_tr_b16 a[6:7], v237 offset:13184\n\t"
                     "ds_read_b64_tr_b16 v[0:1], v237 offset:32\n\t" "ds_read_b64_tr_b16 v[2:3], v237 offset:800\n\t" "ds_read_b64_tr_b16 v[4:5], v237 offset:12448\n\t" "ds_read_b64_tr_b16 v[6:7], v237 offset:13216\n\t"
                     "ds_read_b64_tr_b16 a[8:9], v238 offset:64\n\t" "ds_read_b64_tr_b16 a[10:11], v238 offset:832\n\t" "ds_read_b64_tr_b16 a[12:13], v238 offset:12480\n\t"
                     "ds_read_b64_tr_b16 a[14:15], v238 offset:13248\n\t" "ds_read_b64_tr_b16 a[16:17], v237 offset:24832\n\t" "ds_read_b64_tr_b16 a[18:19], v237 offset:25600\n\t"
                     "ds_read_b64_tr_b16 a[20:21], v237 offset:37248\n\t" "ds_read_b64_tr_b16 a[22:23], v237 offset:38016\n\t" "ds_read_b64_tr_b16 v[8:9], v237 offset:24864\n\t"
                     "ds_read_b64_tr_b16 v[10:11], v237 offset:25632\n\t" "ds_read_b64_tr_b16 v[12:13], v237 offset:37280\n\t" "ds_read_b64_tr_b16 v[14:15], v237 offset:38048\n\t"
                     "ds_read_b64_tr_b16 a[24:25], v238 offset:24896\n\t" "ds_read_b64_tr_b16 a[26:27], v238 offset:25664\n\t" "ds_read_b64_tr_b16 a[28:29], v238 offset:37312\n\t"
                     "ds_read_b64_tr_b16 a[30:31], v238 offset:38080\n\t" "s_waitcnt vmcnt(12) lgkmcnt(0)\n\t" "s_barrier\n\t" "s_add_u32 s42, s44, 0x480000\n\t" "s_addc_u32 s43, s45, 0\n\t" "s_add_u32 m0, %18, 0x0\n\t"
                     "s_nop 0\n\t" "global_load_lds_dwordx4 v224, s[42:43]\n\t" "s_add_u32 m0, %18, 0x400\n\t" "s_nop 0\n\t" "global_load_lds_dwordx4 v225, s[42:43]\n\t" "s_add_u32 m0, %18, 0x800\n\t" "s_nop 0\n\t"
                     "global_load_lds_dwordx4 v226, s[42:43]\n\t" "s_add_u32 s42, s44, 0x498000\n\t" "s_addc_u32 s43, s45, 0\n\t" "s_add_u32 m0, %18, 0xc20\n\t" "s_nop 0\n\t" "global_load_lds_dwordx4 v224, s[42:43]\n\t"
                     "s_add_u32 m0, %18, 0x1020\n\t" "s_nop 0\n\t" "global_load_lds_dwordx4 v225, s[42:43]\n\t" "s_add_u32 m0, %18, 0x1420\n\t" "s_nop 0\n\t" "global_load_lds_dwordx4 v226, s[42:43]\n\t"
                     "s_add_u32 s42, s44, 0x540000\n\t" "s_addc_u32 s43, s45, 0\n\t" "s_add_u32 m0, %18, 0x6100\n\t" "s_nop 0\n\t" "global_load_lds_dwordx4 v224, s[42:43]\n\t" "s_add_u32 m0, %18, 0x6500\n\t" "s_nop 0\n\t"
                     "global_load_lds_dwordx4 v225, s[42:43]\n\t" "s_add_u32 m0, %18, 0x6900\n\t" "s_nop 0\n\t" "global_load_lds_dwordx4 v226, s[42:43]\n\t" "s_add_u32 s42, s44, 0x558000\n\t" "s_addc_u32 s43, s45, 0\n\t"
                     "s_add_u32 m0, %18, 0x6d20\n\t" "s_nop 0\n\t" "global_load_lds_dwordx4 v224, s[42:43]\n\t" "s_add_u32 m0, %18, 0x7120\n\t" "s_nop 0\n\t" "global_load_lds_dwordx4 v225, s[42:43]\n\t"
                     "s_add_u32 m0, %18, 0x7520\n\t" "s_nop 0\n\t" "global_load_lds_dwordx4 v226, s[42:43]\n\t" "v_add_u32 v237, 0xc200, v227\n\t" "v_add_u32 v238, 0xc200, v228\n\t" "ds_read_b64_tr_b16 a[32:33], v237\n\t"
                     "ds_read_b64_tr_b16 a[34:35], v237 offset:768\n\t" "ds_read_b64_tr_b16 a[36:37], v237 offset:12416\n\t" "ds_read_b64_tr_b16 a[38:39], v237 offset:13184\n\t"
                     "ds_read_b64_tr_b16 v[16:17], v237 offset:32\n\t" "ds_read_b64_tr_b16 v[18:19], v237 offset:800\n\t" "ds_read_b64_tr_b16 v[20:21], v237 offset:12448\n\t"
                     "ds_read_b64_tr_b16 v[22:23], v237 offset:13216\n\t" "ds_read_b64_tr_b16 a[40:41], v238 offset:64\n\t" "ds_read_b64_tr_b16 a[42:43], v238 offset:832\n\t"
                     "ds_read_b64_tr_b16 a[44:45], v238 offset:12480\n\t" "ds_read_b64_tr_b16 a[46:47], v238 offset:13248\n\t" "ds_read_b64_tr_b16 a[48:49], v237 offset:24832\n\t"
                     "ds_read_b64_tr_b16 a[50:51], v237 offset:25600\n\t" "ds_read_b64_tr_b16 a[52:53], v237 offset:37248\n\t" "ds_read_b64_tr_b16 a[54:55], v237 offset:38016\n\t"
                     "ds_read_b64_tr_b16 v[24:25], v237 offset:24864\n\t" "ds_read_b64_tr_b16 v[26:27], v237 offset:25632\n\t" "ds_read_b64_tr_b16 v[28:29], v237 offset:37280\n\t"
                     "ds_read_b64_tr_b16 v[30:31], v237 offset:38048\n\t" "ds_read_b64_tr_b16 a[56:57], v238 offset:24896\n\t" "ds_read_b64_tr_b16 a[58:59], v238 offset:25664\n\t"
                     "ds_read_b64_tr_b16 a[60:61], v238 offset:37312\n\t" "ds_read_b64_tr_b16 a[62:63], v238 offset:38080\n\t" "s_waitcnt vmcnt(12) lgkmcnt(0)\n\t" "s_barrier\n\t" "s_add_u32 s42, s44, 0x600000\n\t"
                     "s_addc_u32 s43, s45, 0\n\t" "s_add_u32 m0, %18, 0xc200\n\t" "s_nop 0\n\t" "global_load_lds_dwordx4 v224, s[42:43]\n\t" "s_add_u32 m0, %18, 0xc600\n\t" "s_nop 0\n\t"
                     "global_load_lds_dwordx4 v225, s[42:43]\n\t" "s_add_u32 m0, %18, 0xca00\n\t" "s_nop 0\n\t" "global_load_lds_dwordx4 v226, s[42:43]\n\t" "s_add_u32 s42, s44, 0x618000\n\t" "s_addc_u32 s43, s45, 0\n\t"
                     "s_add_u32 m0, %18, 0xce20\n\t" "s_nop 0\n\t" "global_load_lds_dwordx4 v224, s[42:43]\n\t" "s_add_u32 m0, %18, 0xd220\n\t" "s_nop 0\n\t" "global_load_lds_dwordx4 v225, s[42:43]\n\t"
                     "s_add_u32 m0, %18, 0xd620\n\t" "s_nop 0\n\t" "global_load_lds_dwordx4 v226, s[42:43]\n\t" "s_add_u32 s42, s44, 0x6c0000\n\t" "s_addc_u32 s43, s45, 0\n\t" "s_add_u32 m0, %18, 0x12300\n\t" "s_nop 0\n\t"
                     "global_load_lds_dwordx4 v224, s[42:43]\n\t" "s_add_u32 m0, %18, 0x12700\n\t" "s_nop 0\n\t" "global_load_lds_dwordx4 v225, s[42:43]\n\t" "s_add_u32 m0, %18, 0x12b00\n\t" "s_nop 0\n\t"
                     "global_load_lds_dwordx4 v226, s[42:43]\n\t" "s_add_u32 s42, s44, 0x6d8000\n\t" "s_addc_u32 s43, s45, 0\n\t" "s_add_u32 m0, %18, 0x12f20\n\t" "s_nop 0\n\t" "global_load_lds_dwordx4 v224, s[42:43]\n\t"
                     "s_add_u32 m0, %18, 0x13320\n\t" "s_nop 0\n\t" "global_load_lds_dwordx4 v225, s[42:43]\n\t" "s_add_u32 m0, %18, 0x13720\n\t" "s_nop 0\n\t" "global_load_lds_dwordx4 v226, s[42:43]\n\t"
                     "v_add_u32 v237, 0x18400, v227\n\t" "v_add_u32 v238, 0x18400, v228\n\t" "ds_read_b64_tr_b16 a[64:65], v237\n\t" "ds_read_b64_tr_b16 a[66:67], v237 offset:768\n\t"
                     "ds_read_b64_tr_b16 a[68:69], v237 offset:12416\n\t" "ds_read_b64_tr_b16 a[70:71], v237 offset:13184\n\t" "ds_read_b64_tr_b16 v[32:33], v237 offset:32\n\t"
                     "ds_read_b64_tr_b16 v[34:35], v237 offset:800\n\t" "ds_read_b64_tr_b16 v[36:37], v237 offset:12448\n\t" "ds_read_b64_tr_b16 v[38:39], v237 offset:13216\n\t"
                     "ds_read_b64_tr_b16 a[72:73], v238 offset:64\n\t" "ds_read_b64_tr_b16 a[74:75], v238 offset:832\n\t" "ds_read_b64_tr_b16 a[76:77], v238 offset:12480\n\t"
                     "ds_read_b64_tr_b16 a[78:79], v238 offset:13248\n\t" "ds_read_b64_tr_b16 a[80:81], v237 offset:24832\n\t" "ds_read_b64_tr_b16 a[82:83], v237 offset:25600\n\t"
                     "ds_read_b64_tr_b16 a[84:85], v237 offset:37248\n\t" "ds_read_b64_tr_b16 a[86:87], v237 offset:38016\n\t" "ds_read_b64_tr_b16 v[40:41], v237 offset:24864\n\t"
                     "ds_read_b64_tr_b16 v[42:43], v237 offset:25632\n\t" "ds_read_b64_tr_b16 v[44:45], v237 offset:37280\n\t" "ds_read_b64_tr_b16 v[46:47], v237 offset:38048\n\t"
                     "ds_read_b64_tr_b16 a[88:89], v238 offset:24896\n\t" "ds_read_b64_tr_b16 a[90:91], v238 offset:25664\n\t" "ds_read_b64_tr_b16 a[92:93], v238 offset:37312\n\t"
                     "ds_read_b64_tr_b16 a[94:95], v238 offset:38080\n\t" "s_waitcnt vmcnt(12) lgkmcnt(0)\n\t" "s_barrier\n\t" "s_add_u32 s42, s46, 0x0\n\t" "s_addc_u32 s43, s47, 0\n\t" "s_add_u32 m0, %20, 0x18400\n\t"
                     "s_nop 0\n\t" "global_load_lds_dwordx4 v232, s[42:43]\n\t" "s_add_u32 m0, %20, 0x18800\n\t" "s_nop 0\n\t" "global_load_lds_dwordx4 v233, s[42:43]\n\t" "s_add_u32 m0, %20, 0x18c00\n\t" "s_nop 0\n\t"
                     "global_load_lds_dwordx4 v234, s[42:43]\n\t" "s_add_u32 m0, %20, 0x19000\n\t" "s_nop 0\n\t" "global_load_lds_dwordx4 v235, s[42:43]\n\t" "s_add_u32 m0, %20, 0x19400\n\t" "s_nop 0\n\t"
                     "global_load_lds_dwordx4 v236, s[42:43]\n\t" "s_add_u32 s42, s46, 0x1400\n\t" "s_addc_u32 s43, s47, 0\n\t" "s_add_u32 m0, %20, 0x19800\n\t" "v_xor_b32 v224, 0x80, v232\n\t"
                     "global_load_lds_dwordx4 v224, s[42:43]\n\t" "s_add_u32 m0, %20, 0x19c00\n\t" "v_xor_b32 v224, 0x80, v233\n\t" "global_load_lds_dwordx4 v224, s[42:43]\n\t" "s_add_u32 m0, %20, 0x1a000\n\t"
                     "v_xor_b32 v224, 0x80, v234\n\t" "global_load_lds_dwordx4 v224, s[42:43]\n\t" "s_add_u32 m0, %20, 0x1a400\n\t" "v_xor_b32 v224, 0x80, v235\n\t" "global_load_lds_dwordx4 v224, s[42:43]\n\t"
                     "s_add_u32 m0, %20, 0x1a800\n\t" "v_xor_b32 v224, 0x80, v236\n\t" "global_load_lds_dwordx4 v224, s[42:43]\n\t" "v_add_u32 v237, 0x0, v227\n\t" "v_add_u32 v238, 0x0, v228\n\t"
                     "ds_read_b64_tr_b16 a[96:97], v237\n\t" "ds_read_b64_tr_b16 a[98:99], v237 offset:768\n\t" "ds_read_b64_tr_b16 a[100:101], v237 offset:12416\n\t" "ds_read_b64_tr_b16 a[102:103], v237 offset:13184\n\t"
                     "ds_read_b64_tr_b16 v[48:49], v237 offset:32\n\t" "ds_read_b64_tr_b16 v[50:51], v237 offset:800\n\t" "ds_read_b64_tr_b16 v[52:53], v237 offset:12448\n\t"
                     "ds_read_b64_tr_b16 v[54:55], v237 offset:13216\n\t" "ds_read_b64_tr_b16 a[104:105], v238 offset:64\n\t" "ds_read_b64_tr_b16 a[106:107], v238 offset:832\n\t"
                     "ds_read_b64_tr_b16 a[108:109], v238 offset:12480\n\t" "ds_read_b64_tr_b16 a[110:111], v238 offset:13248\n\t" "ds_read_b64_tr_b16 a[112:113], v237 offset:24832\n\t"
                     "ds_read_b64_tr_b16 a[114:115], v237 offset:25600\n\t" "ds_read_b64_tr_b16 a[116:117], v237 offset:37248\n\t" "ds_read_b64_tr_b16 a[118:119], v237 offset:38016\n\t"
                     "ds_read_b64_tr_b16 v[56:57], v237 offset:24864\n\t" "ds_read_b64_tr_b16 v[58:59], v237 offset:25632\n\t" "ds_read_b64_tr_b16 v[60:61], v237 offset:37280\n\t"
                     "ds_read_b64_tr_b16 v[62:63], v237 offset:38048\n\t" "ds_read_b64_tr_b16 a[120:121], v238 offset:24896\n\t" "ds_read_b64_tr_b16 a[122:123], v238 offset:25664\n\t"
                     "ds_read_b64_tr_b16 a[124:125], v238 offset:37312\n\t" "ds_read_b64_tr_b16 a[126:127], v238 offset:38080\n\t" "s_waitcnt vmcnt(10) lgkmcnt(0)\n\t" "s_barrier\n\t" "s_add_u32 s42, s46, 0xa000\n\t"
                     "s_addc_u32 s43, s47, 0\n\t" "s_add_u32 m0, %20, 0x0\n\t" "s_nop 0\n\t" "global_load_lds_dwordx4 v232, s[42:43]\n\t" "s_add_u32 m0, %20, 0x400\n\t" "s_nop 0\n\t"
                     "global_load_lds_dwordx4 v233, s[42:43]\n\t" "s_add_u32 m0, %20, 0x800\n\t" "s_nop 0\n\t" "global_load_lds_dwordx4 v234, s[42:43]\n\t" "s_add_u32 m0, %20, 0xc00\n\t" "s_nop 0\n\t"
                     "global_load_lds_dwordx4 v235, s[42:43]\n\t" "s_add_u32 m0, %20, 0x1000\n\t" "s_nop 0\n\t" "global_load_lds_dwordx4 v236, s[42:43]\n\t" "s_add_u32 s42, s46, 0xb400\n\t" "s_addc_u32 s43, s47, 0\n\t"
                     "s_add_u32 m0, %20, 0x1400\n\t" "v_xor_b32 v224, 0x80, v232\n\t" "global_load_lds_dwordx4 v224, s[42:43]\n\t" "s_add_u32 m0, %20, 0x1800\n\t" "v_xor_b32 v224, 0x80, v233\n\t"
                     "global_load_lds_dwordx4 v224, s[42:43]\n\t" "s_add_u32 m0, %20, 0x1c00\n\t" "v_xor_b32 v224, 0x80, v234\n\t" "global_load_lds_dwordx4 v224, s[42:43]\n\t" "s_add_u32 m0, %20, 0x2000\n\t"
                     "v_xor_b32 v224, 0x80, v235\n\t" "global_load_lds_dwordx4 v224, s[42:43]\n\t" "s_add_u32 m0, %20, 0x2400\n\t" "v_xor_b32 v224, 0x80, v236\n\t" "global_load_lds_dwordx4 v224, s[42:43]\n\t"
                     "v_add_u32 v237, 0xc200, v227\n\t" "v_add_u32 v238, 0xc200, v228\n\t" "ds_read_b64_tr_b16 a[128:129], v237\n\t" "ds_read_b64_tr_b16 a[130:131], v237 offset:768\n\t"
                     "ds_read_b64_tr_b16 a[132:133], v237 offset:12416\n\t" "ds_read_b64_tr_b16 a[134:135], v237 offset:13184\n\t" "ds_read_b64_tr_b16 a[136:137], v237 offset:32\n\t"
                     "ds_read_b64_tr_b16 a[138:139], v237 offset:800\n\t" "ds_read_b64_tr_b16 a[140:141], v237 offset:12448\n\t" "ds_read_b64_tr_b16 a[142:143], v237 offset:13216\n\t"
                     "ds_read_b64_tr_b16 a[144:145], v238 offset:64\n\t" "ds_read_b64_tr_b16 a[146:147], v238 offset:832\n\t" "ds_read_b64_tr_b16 a[148:149], v238 offset:12480\n\t"
                     "ds_read_b64_tr_b16 a[150:151], v238 offset:13248\n\t" "ds_read_b64_tr_b16 a[152:153], v237 offset:24832\n\t" "ds_read_b64_tr_b16 a[154:155], v237 offset:25600\n\t"
                     "ds_read_b64_tr_b16 a[156:157], v237 offset:37248\n\t" "ds_read_b64_tr_b16 a[158:159], v237 offset:38016\n\t" "ds_read_b64_tr_b16 a[160:161], v237 offset:24864\n\t"
                     "ds_read_b64_tr_b16 a[162:163], v237 offset:25632\n\t" "ds_read_b64_tr_b16 a[164:165], v237 offset:37280\n\t" "ds_read_b64_tr_b16 a[166:167], v237 offset:38048\n\t"
                     "ds_read_b64_tr_b16 a[168:169], v238 offset:24896\n\t" "ds_read_b64_tr_b16 a[170:171], v238 offset:25664\n\t" "ds_read_b64_tr_b16 a[172:173], v238 offset:37312\n\t"
                     "ds_read_b64_tr_b16 a[174:175], v238 offset:38080\n\t" "s_waitcnt vmcnt(10) lgkmcnt(0)\n\t" "s_barrier\n\t" "s_add_u32 s42, s46, 0x14000\n\t" "s_addc_u32 s43, s47, 0\n\t"
                     "s_add_u32 m0, %20, 0xc200\n\t" "s_nop 0\n\t" "global_load_lds_dwordx4 v232, s[42:43]\n\t" "s_add_u32 m0, %20, 0xc600\n\t" "s_nop 0\n\t" "global_load_lds_dwordx4 v233, s[42:43]\n\t"
                     "s_add_u32 m0, %20, 0xca00\n\t" "s_nop 0\n\t" "global_load_lds_dwordx4 v234, s[42:43]\n\t" "s_add_u32 m0, %20, 0xce00\n\t" "s_nop 0\n\t" "global_load_lds_dwordx4 v235, s[42:43]\n\t"
                     "s_add_u32 m0, %20, 0xd200\n\t" "s_nop 0\n\t" "global_load_lds_dwordx4 v236, s[42:43]\n\t" "s_add_u32 s42, s46, 0x15400\n\t" "s_addc_u32 s43, s47, 0\n\t" "s_add_u32 m0, %20, 0xd600\n\t"
                     "v_xor_b32 v224, 0x80, v232\n\t" "global_load_lds_dwordx4 v224, s[42:43]\n\t" "s_add_u32 m0, %20, 0xda00\n\t" "v_xor_b32 v224, 0x80, v233\n\t" "global_load_lds_dwordx4 v224, s[42:43]\n\t"
                     "s_add_u32 m0, %20, 0xde00\n\t" "v_xor_b32 v224, 0x80, v234\n\t" "global_load_lds_dwordx4 v224, s[42:43]\n\t" "s_add_u32 m0, %20, 0xe200\n\t" "v_xor_b32 v224, 0x80, v235\n\t"
                     "global_load_lds_dwordx4 v224, s[42:43]\n\t" "s_add_u32 m0, %20, 0xe600\n\t" "v_xor_b32 v224, 0x80, v236\n\t" "global_load_lds_dwordx4 v224, s[42:43]\n\t" "s_add_u32 s42, %21, 0x18400\n\t"
                     "v_xor_b32 v237, 0x0, v229\n\t" "v_add_u32 v237, s42, v237\n\t" "v_xor_b32 v238, 0x40, v229\n\t" "v_add_u32 v238, s42, v238\n\t" "v_xor_b32 v239, 0x80, v229\n\t" "v_add_u32 v239, s42, v239\n\t"
                     "v_xor_b32 v226, 0xc0, v229\n\t" "v_add_u32 v226, s42, v226\n\t" "ds_read_b64_tr_b16 a[176:177], v237\n\t" "ds_read_b64_tr_b16 a[178:179], v238 offset:2560\n\t"
                     "ds_read_b64_tr_b16 a[200:201], v239\n\t" "ds_read_b64_tr_b16 a[202:203], v226 offset:2560\n\t" "ds_read_b64_tr_b16 a[224:225], v237 offset:256\n\t"
                     "ds_read_b64_tr_b16 a[226:227], v238 offset:2816\n\t" "ds_read_b64_tr_b16 a[248:249], v239 offset:256\n\t" "ds_read_b64_tr_b16 a[250:251], v226 offset:2816\n\t"
                     "ds_read_b64_tr_b16 v[112:113], v237 offset:512\n\t" "ds_read_b64_tr_b16 v[114:115], v238 offset:3072\n\t" "ds_read_b64_tr_b16 v[136:137], v239 offset:512\n\t"
                     "ds_read_b64_tr_b16 v[138:139], v226 offset:3072\n\t" "ds_read_b64_tr_b16 v[160:161], v237 offset:768\n\t" "ds_read_b64_tr_b16 v[162:163], v238 offset:3328\n\t"
                     "ds_read_b64_tr_b16 v[184:185], v239 offset:768\n\t" "ds_read_b64_tr_b16 v[186:187], v226 offset:3328\n\t" "ds_read_b64_tr_b16 v[208:209], v237 offset:1024\n\t"
                     "ds_read_b64_tr_b16 v[210:211], v238 offset:3584\n\t" "ds_read_b64_tr_b16 v[72:73], v239 offset:1024\n\t" "ds_read_b64_tr_b16 v[74:75], v226 offset:3584\n\t" "s_waitcnt vmcnt(10) lgkmcnt(0)\n\t"
                     "s_barrier\n\t" "s_add_u32 s42, s46, 0x1e000\n\t" "s_addc_u32 s43, s47, 0\n\t" "s_add_u32 m0, %20, 0x18400\n\t" "s_nop 0\n\t" "global_load_lds_dwordx4 v232, s[42:43]\n\t"
                     "s_add_u32 m0, %20, 0x18800\n\t" "s_nop 0\n\t" "global_load_lds_dwordx4 v233, s[42:43]\n\t" "s_add_u32 m0, %20, 0x18c00\n\t" "s_nop 0\n\t" "global_load_lds_dwordx4 v234, s[42:43]\n\t"
                     "s_add_u32 m0, %20, 0x19000\n\t" "s_nop 0\n\t" "global_load_lds_dwordx4 v235, s[42:43]\n\t" "s_add_u32 m0, %20, 0x19400\n\t" "s_nop 0\n\t" "global_load_lds_dwordx4 v236, s[42:43]\n\t"
                     "s_add_u32 s42, s46, 0x1f400\n\t" "s_addc_u32 s43, s47, 0\n\t" "s_add_u32 m0, %20, 0x19800\n\t" "v_xor_b32 v224, 0x80, v232\n\t" "global_load_lds_dwordx4 v224, s[42:43]\n\t"
                     "s_add_u32 m0, %20, 0x19c00\n\t" "v_xor_b32 v224, 0x80, v233\n\t" "global_load_lds_dwordx4 v224, s[42:43]\n\t" "s_add_u32 m0, %20, 0x1a000\n\t" "v_xor_b32 v224, 0x80, v234\n\t"
                     "global_load_lds_dwordx4 v224, s[42:43]\n\t" "s_add_u32 m0, %20, 0x1a400\n\t" "v_xor_b32 v224, 0x80, v235\n\t" "global_load_lds_dwordx4 v224, s[42:43]\n\t" "s_add_u32 m0, %20, 0x1a800\n\t"
                     "v_xor_b32 v224, 0x80, v236\n\t" "global_load_lds_dwordx4 v224, s[42:43]\n\t" "s_add_u32 s42, %21, 0x0\n\t" "v_xor_b32 v237, 0x0, v229\n\t" "v_add_u32 v237, s42, v237\n\t"
                     "v_xor_b32 v238, 0x40, v229\n\t" "v_add_u32 v238, s42, v238\n\t" "v_xor_b32 v239, 0x80, v229\n\t" "v_add_u32 v239, s42, v239\n\t" "v_xor_b32 v226, 0xc0, v229\n\t" "v_add_u32 v226, s42, v226\n\t"
                     "ds_read_b64_tr_b16 a[180:181], v237\n\t" "ds_read_b64_tr_b16 a[182:183], v238 offset:2560\n\t" "ds_read_b64_tr_b16 a[204:205], v239\n\t" "ds_read_b64_tr_b16 a[206:207], v226 offset:2560\n\t"
                     "ds_read_b64_tr_b16 a[228:229], v237 offset:256\n\t" "ds_read_b64_tr_b16 a[230:231], v238 offset:2816\n\t" "ds_read_b64_tr_b16 a[252:253], v239 offset:256\n\t"
                     "ds_read_b64_tr_b16 a[254:255], v226 offset:2816\n\t" "ds_read_b64_tr_b16 v[116:117], v237 offset:512\n\t" "ds_read_b64_tr_b16 v[118:119], v238 offset:3072\n\t"
                     "ds_read_b64_tr_b16 v[140:141], v239 offset:512\n\t" "ds_read_b64_tr_b16 v[142:143], v226 offset:3072\n\t" "ds_read_b64_tr_b16 v[164:165], v237 offset:768\n\t"
                     "ds_read_b64_tr_b16 v[166:167], v238 offset:3328\n\t" "ds_read_b64_tr_b16 v[188:189], v239 offset:768\n\t" "ds_read_b64_tr_b16 v[190:191], v226 offset:3328\n\t"
                     "ds_read_b64_tr_b16 v[212:213], v237 offset:1024\n\t" "ds_read_b64_tr_b16 v[214:215], v238 offset:3584\n\t" "ds_read_b64_tr_b16 v[76:77], v239 offset:1024\n\t"
                     "ds_read_b64_tr_b16 v[78:79], v226 offset:3584\n\t" "s_waitcnt vmcnt(10) lgkmcnt(0)\n\t" "s_barrier\n\t" "v_mov_b32 v225, 0\n\t" "global_load_dwordx2 %12, v231, %19 sc1\n\t"
                     "global_load_dwordx2 %13, v225, %19 offset:512 sc1\n\t" "global_load_dwordx2 %14, v225, %19 offset:1024 sc1\n\t" "s_add_u32 s42, s46, 0x28000\n\t" "s_addc_u32 s43, s47, 0\n\t"
                     "s_add_u32 m0, %20, 0x0\n\t" "s_nop 0\n\t" "global_load_lds_dwordx4 v232, s[42:43]\n\t" "s_add_u32 m0, %20, 0x400\n\t" "s_nop 0\n\t" "global_load_lds_dwordx4 v233, s[42:43]\n\t"
                     "s_add_u32 m0, %20, 0x800\n\t" "s_nop 0\n\t" "global_load_lds_dwordx4 v234, s[42:43]\n\t" "s_add_u32 m0, %20, 0xc00\n\t" "s_nop 0\n\t" "global_load_lds_dwordx4 v235, s[42:43]\n\t"
                     "s_add_u32 m0, %20, 0x1000\n\t" "s_nop 0\n\t" "global_load_lds_dwordx4 v236, s[42:43]\n\t" "s_add_u32 s42, s46, 0x29400\n\t" "s_addc_u32 s43, s47, 0\n\t" "s_add_u32 m0, %20, 0x1400\n\t"
                     "v_xor_b32 v224, 0x80, v232\n\t" "global_load_lds_dwordx4 v224, s[42:43]\n\t" "s_add_u32 m0, %20, 0x1800\n\t" "v_xor_b32 v224, 0x80, v233\n\t" "global_load_lds_dwordx4 v224, s[42:43]\n\t"
                     "s_add_u32 m0, %20, 0x1c00\n\t" "v_xor_b32 v224, 0x80, v234\n\t" "global_load_lds_dwordx4 v224, s[42:43]\n\t" "s_add_u32 m0, %20, 0x2000\n\t" "v_xor_b32 v224, 0x80, v235\n\t"
                     "global_load_lds_dwordx4 v224, s[42:43]\n\t" "s_add_u32 m0, %20, 0x2400\n\t" "v_xor_b32 v224, 0x80, v236\n\t" "global_load_lds_dwordx4 v224, s[42:43]\n\t" "s_add_u32 s42, %21, 0xc200\n\t"
                     "v_xor_b32 v237, 0x0, v229\n\t" "v_add_u32 v237, s42, v237\n\t" "v_xor_b32 v238, 0x40, v229\n\t" "v_add_u32 v238, s42, v238\n\t" "v_xor_b32 v239, 0x80, v229\n\t" "v_add_u32 v239, s42, v239\n\t"
                     "v_xor_b32 v226, 0xc0, v229\n\t" "v_add_u32 v226, s42, v226\n\t" "ds_read_b64_tr_b16 a[184:185], v237\n\t" "ds_read_b64_tr_b16 a[186:187], v238 offset:2560\n\t"
                     "ds_read_b64_tr_b16 a[208:209], v239\n\t" "ds_read_b64_tr_b16 a[210:211], v226 offset:2560\n\t" "ds_read_b64_tr_b16 a[232:233], v237 offset:256\n\t"
                     "ds_read_b64_tr_b16 a[234:235], v238 offset:2816\n\t" "ds_read_b64_tr_b16 v[96:97], v239 offset:256\n\t" "ds_read_b64_tr_b16 v[98:99], v226 offset:2816\n\t"
                     "ds_read_b64_tr_b16 v[120:121], v237 offset:512\n\t" "ds_read_b64_tr_b16 v[122:123], v238 offset:3072\n\t" "ds_read_b64_tr_b16 v[144:145], v239 offset:512\n\t"
                     "ds_read_b64_tr_b16 v[146:147], v226 offset:3072\n\t" "ds_read_b64_tr_b16 v[168:169], v237 offset:768\n\t" "ds_read_b64_tr_b16 v[170:171], v238 offset:3328\n\t"
                     "ds_read_b64_tr_b16 v[192:193], v239 offset:768\n\t" "ds_read_b64_tr_b16 v[194:195], v226 offset:3328\n\t" "ds_read_b64_tr_b16 v[216:217], v237 offset:1024\n\t"
                     "ds_read_b64_tr_b16 v[218:219], v238 offset:3584\n\t" "ds_read_b64_tr_b16 v[80:81], v239 offset:1024\n\t" "ds_read_b64_tr_b16 v[82:83], v226 offset:3584\n\t" "s_waitcnt vmcnt(10) lgkmcnt(0)\n\t"
                     "s_barrier\n\t" "s_add_u32 s42, s46, 0x32000\n\t" "s_addc_u32 s43, s47, 0\n\t" "s_add_u32 m0, %20, 0xc200\n\t" "s_nop 0\n\t" "global_load_lds_dwordx4 v232, s[42:43]\n\t" "s_add_u32 m0, %20, 0xc600\n\t"
                     "s_nop 0\n\t" "global_load_lds_dwordx4 v233, s[42:43]\n\t" "s_add_u32 m0, %20, 0xca00\n\t" "s_nop 0\n\t" "global_load_lds_dwordx4 v234, s[42:43]\n\t" "s_add_u32 m0, %20, 0xce00\n\t" "s_nop 0\n\t"
                     "global_load_lds_dwordx4 v235, s[42:43]\n\t" "s_add_u32 m0, %20, 0xd200\n\t" "s_nop 0\n\t" "global_load_lds_dwordx4 v236, s[42:43]\n\t" "s_add_u32 s42, s46, 0x33400\n\t" "s_addc_u32 s43, s47, 0\n\t"
                     "s_add_u32 m0, %20, 0xd600\n\t" "v_xor_b32 v224, 0x80, v232\n\t" "global_load_lds_dwordx4 v224, s[42:43]\n\t" "s_add_u32 m0, %20, 0xda00\n\t" "v_xor_b32 v224, 0x80, v233\n\t"
                     "global_load_lds_dwordx4 v224, s[42:43]\n\t" "s_add_u32 m0, %20, 0xde00\n\t" "v_xor_b32 v224, 0x80, v234\n\t" "global_load_lds_dwordx4 v224, s[42:43]\n\t" "s_add_u32 m0, %20, 0xe200\n\t"
                     "v_xor_b32 v224, 0x80, v235\n\t" "global_load_lds_dwordx4 v224, s[42:43]\n\t" "s_add_u32 m0, %20, 0xe600\n\t" "v_xor_b32 v224, 0x80, v236\n\t" "global_load_lds_dwordx4 v224, s[42:43]\n\t"
                     "s_add_u32 s42, %21, 0x18400\n\t" "v_xor_b32 v237, 0x0, v229\n\t" "v_add_u32 v237, s42, v237\n\t" "v_xor_b32 v238, 0x40, v229\n\t" "v_add_u32 v238, s42, v238\n\t" "v_xor_b32 v239, 0x80, v229\n\t"
                     "v_add_u32 v239, s42, v239\n\t" "v_xor_b32 v226, 0xc0, v229\n\t" "v_add_u32 v226, s42, v226\n\t" "ds_read_b64_tr_b16 a[188:189], v237\n\t" "ds_read_b64_tr_b16 a[190:191], v238 offset:2560\n\t"
                     "ds_read_b64_tr_b16 a[212:213], v239\n\t" "ds_read_b64_tr_b16 a[214:215], v226 offset:2560\n\t" "ds_read_b64_tr_b16 a[236:237], v237 offset:256\n\t"
                     "ds_read_b64_tr_b16 a[238:239], v238 offset:2816\n\t" "ds_read_b64_tr_b16 v[100:101], v239 offset:256\n\t" "ds_read_b64_tr_b16 v[102:103], v226 offset:2816\n\t"
                     "ds_read_b64_tr_b16 v[124:125], v237 offset:512\n\t" "ds_read_b64_tr_b16 v[126:127], v238 offset:3072\n\t" "ds_read_b64_tr_b16 v[148:149], v239 offset:512\n\t"
                     "ds_read_b64_tr_b16 v[150:151], v226 offset:3072\n\t" "ds_read_b64_tr_b16 v[172:173], v237 offset:768\n\t" "ds_read_b64_tr_b16 v[174:175], v238 offset:3328\n\t"
                     "ds_read_b64_tr_b16 v[196:197], v239 offset:768\n\t" "ds_read_b64_tr_b16 v[198:199], v226 offset:3328\n\t" "ds_read_b64_tr_b16 v[220:221], v237 offset:1024\n\t"
                     "ds_read_b64_tr_b16 v[222:223], v238 offset:3584\n\t" "ds_read_b64_tr_b16 v[84:85], v239 offset:1024\n\t" "ds_read_b64_tr_b16 v[86:87], v226 offset:3584\n\t" "s_waitcnt vmcnt(10) lgkmcnt(0)\n\t"
                     "s_barrier\n\t" "s_add_u32 s42, %21, 0x0\n\t" "v_xor_b32 v237, 0x0, v229\n\t" "v_add_u32 v237, s42, v237\n\t" "v_xor_b32 v238, 0x40, v229\n\t" "v_add_u32 v238, s42, v238\n\t"
                     "v_xor_b32 v239, 0x80, v229\n\t" "v_add_u32 v239, s42, v239\n\t" "v_xor_b32 v226, 0xc0, v229\n\t" "v_add_u32 v226, s42, v226\n\t" "ds_read_b64_tr_b16 a[192:193], v237\n\t"
                     "ds_read_b64_tr_b16 a[194:195], v238 offset:2560\n\t" "ds_read_b64_tr_b16 a[216:217], v239\n\t" "ds_read_b64_tr_b16 a[218:219], v226 offset:2560\n\t"
                     "ds_read_b64_tr_b16 a[240:241], v237 offset:256\n\t" "ds_read_b64_tr_b16 a[242:243], v238 offset:2816\n\t" "ds_read_b64_tr_b16 v[104:105], v239 offset:256\n\t"
                     "ds_read_b64_tr_b16 v[106:107], v226 offset:2816\n\t" "ds_read_b64_tr_b16 v[128:129], v237 offset:512\n\t" "ds_read_b64_tr_b16 v[130:131], v238 offset:3072\n\t"
                     "ds_read_b64_tr_b16 v[152:153], v239 offset:512\n\t" "ds_read_b64_tr_b16 v[154:155], v226 offset:3072\n\t" "ds_read_b64_tr_b16 v[176:177], v237 offset:768\n\t"
                     "ds_read_b64_tr_b16 v[178:179], v238 offset:3328\n\t" "ds_read_b64_tr_b16 v[200:201], v239 offset:768\n\t" "ds_read_b64_tr_b16 v[202:203], v226 offset:3328\n\t"
                     "ds_read_b64_tr_b16 v[64:65], v237 offset:1024\n\t" "ds_read_b64_tr_b16 v[66:67], v238 offset:3584\n\t" "ds_read_b64_tr_b16 v[88:89], v239 offset:1024\n\t"
                     "ds_read_b64_tr_b16 v[90:91], v226 offset:3584\n\t" "s_waitcnt vmcnt(0) lgkmcnt(0)\n\t" "s_barrier\n\t" "s_add_u32 s42, %21, 0xc200\n\t" "v_xor_b32 v237, 0x0, v229\n\t" "v_add_u32 v237, s42, v237\n\t"
                     "v_xor_b32 v238, 0x40, v229\n\t" "v_add_u32 v238, s42, v238\n\t" "v_xor_b32 v239, 0x80, v229\n\t" "v_add_u32 v239, s42, v239\n\t" "v_xor_b32 v226, 0xc0, v229\n\t" "v_add_u32 v226, s42, v226\n\t"
                     "ds_read_b64_tr_b16 a[196:197], v237\n\t" "ds_read_b64_tr_b16 a[198:199], v238 offset:2560\n\t" "ds_read_b64_tr_b16 a[220:221], v239\n\t" "ds_read_b64_tr_b16 a[222:223], v226 offset:2560\n\t"
                     "ds_read_b64_tr_b16 a[244:245], v237 offset:256\n\t" "ds_read_b64_tr_b16 a[246:247], v238 offset:2816\n\t" "ds_read_b64_tr_b16 v[108:109], v239 offset:256\n\t"
                     "ds_read_b64_tr_b16 v[110:111], v226 offset:2816\n\t" "ds_read_b64_tr_b16 v[132:133], v237 offset:512\n\t" "ds_read_b64_tr_b16 v[134:135], v238 offset:3072\n\t"
                     "ds_read_b64_tr_b16 v[156:157], v239 offset:512\n\t" "ds_read_b64_tr_b16 v[158:159], v226 offset:3072\n\t" "ds_read_b64_tr_b16 v[180:181], v237 offset:768\n\t"
                     "ds_read_b64_tr_b16 v[182:183], v238 offset:3328\n\t" "ds_read_b64_tr_b16 v[204:205], v239 offset:768\n\t" "ds_read_b64_tr_b16 v[206:207], v226 offset:3328\n\t"
                     "ds_read_b64_tr_b16 v[68:69], v237 offset:1024\n\t" "ds_read_b64_tr_b16 v[70:71], v238 offset:3584\n\t" "ds_read_b64_tr_b16 v[92:93], v239 offset:1024\n\t"
                     "ds_read_b64_tr_b16 v[94:95], v226 offset:3584\n\t" "s_waitcnt lgkmcnt(0)\n\t" "ds_write_b128 v230, v[0:3]\n\t" "ds_write_b128 v230, v[4:7] offset:1024\n\t"
                     "ds_write_b128 v230, v[8:11] offset:2048\n\t" "ds_write_b128 v230, v[12:15] offset:3072\n\t" "ds_write_b128 v230, v[16:19] offset:4096\n\t" "ds_write_b128 v230, v[20:23] offset:5120\n\t"
                     "ds_write_b128 v230, v[24:27] offset:6144\n\t" "ds_write_b128 v230, v[28:31] offset:7168\n\t" "ds_write_b128 v230, v[32:35] offset:8192\n\t" "ds_write_b128 v230, v[36:39] offset:9216\n\t"
                     "ds_write_b128 v230, v[40:43] offset:10240\n\t" "ds_write_b128 v230, v[44:47] offset:11264\n\t" "ds_write_b128 v230, v[48:51] offset:12288\n\t" "ds_write_b128 v230, v[52:55] offset:13312\n\t"
                     "ds_write_b128 v230, v[56:59] offset:14336\n\t" "ds_write_b128 v230, v[60:63] offset:15360\n\t" "ds_write_b128 v230, v[64:67] offset:16384\n\t" "ds_write_b128 v230, v[68:71] offset:17408\n\t"
                     "ds_write_b128 v230, v[72:75] offset:18432\n\t" "ds_write_b128 v230, v[76:79] offset:19456\n\t" "ds_write_b128 v230, v[80:83] offset:20480\n\t" "ds_write_b128 v230, v[84:87] offset:21504\n\t"
                     "ds_write_b128 v230, v[88:91] offset:22528\n\t" "ds_write_b128 v230, v[92:95] offset:23552\n\t"
                     : "=&{a[0:31]}"(wa[0]), "=&{a[32:63]}"(wa[1]), "=&{a[64:95]}"(wa[2]), "=&{a[96:127]}"(wa[3]), "=&{a[128:159]}"(wa[4]), "=&{a[160:191]}"(wa[5]), "=&{a[192:223]}"(wa[6]), "=&{a[224:255]}"(wa[7]), "=&{v[96:127]}"(wv[0]), "=&{v[128:159]}"(wv[1]), "=&{v[160:191]}"(wv[2]), "=&{v[192:223]}"(wv[3]), "=&v"(y.r), "=&v"(y.g), "=&v"(y.e), "+{v[224:239]}"(pa)
                     : "{s[44:45]}"(p.wv + h * DH * 2 + (size_t)(16 * w) * (HD * 2)), "{s[46:47]}"(p.wo + (size_t)(h * DH + 8 * w) * 1280),
                       "s"(lds + 2 * w * GS), "s"(p.ws + OFF_LAT), "s"(lds + w * 10240), "s"(lds)
                     : "v0", "v1", "v2", "v3", "v4", "v5", "v6", "v7", "v8", "v9", "v10", "v11", "v12", "v13", "v14", "v15", "v16", "v17", "v18", "v19", "v20", "v21", "v22", "v23", "v24", "v25", "v26", "v27", "v28", "v29", "v30", "v31", "v32", "v33", "v34", "v35", "v36", "v37", "v38", "v39", "v40", "v41", "v42", "v43", "v44", "v45", "v46", "v47", "v48", "v49", "v50", "v51", "v52", "v53", "v54", "v55", "v56", "v57", "v58", "v59", "v60", "v61", "v62", "v63", "v64", "v65", "v66", "v67", "v68", "v69", "v70", "v71", "v72", "v73", "v74", "v75", "v76", "v77", "v78", "v79", "v80", "v81", "v82", "v83", "v84", "v85", "v86", "v87", "v88", "v89", "v90", "v91", "v92", "v93", "v94", "v95", "s42", "s43", "scc", "memory");
        sd = sync_end(y, p.ws, phi, w == 0);
        barrier();
    }
    const int lane = lanex(), tid = 64 * w + lane;
    const int n = lane & 15, g = lane >> 4, qq = (lane >> 2) & 3, pp = lane & 3;
    char* const fl = p.ws + OFF_ST;
    const u32 iw = lds + 65536 + ((lane * 8) ^ (lane & 32));
    const u32 qr0 = lds + 65536 + (g >> 1) * 512 + pp * 128 + 64 * (g & 1) + 8 * qq + 32 * (pp >> 1);
    const u32 uo = (u32)((2 * w + (n >> 3)) * 4096 + h * 128 + (n & 7) * 16);
    u32x16 ad = u32x16{(u32)lane * 16, (lane & 2) ? 0xEEEEu : 0x4444u, lds + (u32)((w & 1) * 24576 + (w >> 1) * 99328 + lane * 16), iw + ((3 * w + (w & 1)) >> 1) * 512,
                       iw + (1 + 3 * (w >> 1)) * 512 + 4 * (w & 1), qr0, qr0 ^ 32, uo + (2 + g) * 32768, uo + (g & 1) * 32768 + (g >> 1) * 8, uo, uo, uo, uo, uo, uo, uo};
    asm volatile("" : "+{v[224:239]}"(ad));
    const char* memp = p.ws + mslot(sd, 0);
    int budget = 30000;
    Clock clk; clk.init();
    for (int s = 0; s < NSEG; ++s) {
        int tries = 0;
        u64 bad, tp8, en = ~0ull;
        while (true) {
            const u32 tgt = clk.sched(phi, 0, cfg.G, tries ? 2 : 0);
            char* up = p.ws + uslot(sd, s);
            asm volatile("ds_read_b128 v[64:67], v226\n\t" "ds_read_b128 v[68:71], v226 offset:1024\n\t" "ds_read_b128 v[72:75], v226 offset:2048\n\t" "ds_read_b128 v[76:79], v226 offset:3072\n\t"
                         "ds_read_b128 v[80:83], v226 offset:4096\n\t" "ds_read_b128 v[84:87], v226 offset:5120\n\t" "ds_read_b128 v[88:91], v226 offset:6144\n\t" "ds_read_b128 v[92:95], v226 offset:7168\n\t"
                         "v_mov_b64 v[40:41], 0\n\t" "v_mov_b64 v[42:43], 0\n\t" "v_mov_b64 v[44:45], 0\n\t" "v_mov_b64 v[46:47], 0\n\t" "v_mov_b64 v[48:49], 0\n\t" "v_mov_b64 v[50:51], 0\n\t" ".Lsp_u:\n\t" "s_memtime vcc\n\t"
                         "s_waitcnt lgkmcnt(0)\n\t" "s_sub_i32 s42, %3, vcc_lo\n\t" "s_cmp_gt_i32 s42, 80\n\t" "s_cbranch_scc1 .Lsp_u\n\t" "s_max_i32 s42, s42, 0\n\t" "s_sub_i32 s42, 92, s42\n\t" "s_and_b32 s42, s42, -4\n\t"
                         "s_getpc_b64 vcc\n\t" "s_add_u32 vcc_lo, vcc_lo, s42\n\t" "s_addc_u32 vcc_hi, vcc_hi, 0\n\t" "s_setpc_b64 vcc\n\t" "s_nop 0\n\t" "s_nop 0\n\t" "s_nop 0\n\t" "s_nop 0\n\t" "s_nop 0\n\t" "s_nop 0\n\t"
                         "s_nop 0\n\t" "s_nop 0\n\t" "s_nop 0\n\t" "s_nop 0\n\t" "s_nop 0\n\t" "s_nop 0\n\t" "s_nop 0\n\t" "s_nop 0\n\t" "s_nop 0\n\t" "s_nop 0\n\t" "s_nop 0\n\t" "s_nop 0\n\t" "s_nop 0\n\t" "s_nop 0\n\t"
                         "global_load_dwordx4 v[0:3], v224, %4 offset:-4096 sc1\n\t" "s_nop 10\n\t" "global_load_dwordx4 v[4:7], v224, %4 offset:-3072 sc1\n\t" "s_nop 10\n\t"
                         "global_load_dwordx4 v[8:11], v224, %4 offset:-2048 sc1\n\t" "s_nop 10\n\t" "global_load_dwordx4 v[12:15], v224, %4 offset:-1024 sc1\n\t" "s_nop 10\n\t" "global_load_dwordx4 v[16:19], v224, %4 sc1\n\t"
                         "s_nop 10\n\t" "global_load_dwordx4 v[20:23], v224, %4 offset:1024 sc1\n\t" "s_nop 10\n\t" "global_load_dwordx4 v[24:27], v224, %4 offset:2048 sc1\n\t" "s_nop 10\n\t"
                         "global_load_dwordx4 v[28:31], v224, %4 offset:3072 sc1\n\t" "s_nop 10\n\t" "global_load_dwordx4 v[32:35], v224, %5 offset:-4096 sc1\n\t" "s_nop 10\n\t"
                         "global_load_dwordx4 v[36:39], v224, %5 offset:-3072 sc1\n\t" "s_waitcnt vmcnt(9)\n\t" "v_smfmac_f32_16x16x64_bf16 v[40:43], v[0:3], a[0:7], v225\n\t"
                         "v_smfmac_f32_16x16x64_bf16 v[44:47], v[0:3], v[64:71], v225\n\t" "ds_read_b128 v[64:67], v226 offset:8192\n\t" "v_smfmac_f32_16x16x64_bf16 v[48:51], v[0:3], a[8:15], v225\n\t"
                         "ds_read_b128 v[68:71], v226 offset:9216\n\t" "v_cmp_eq_u32 vcc, -1, v0\n\t" "s_waitcnt vmcnt(8)\n\t" "v_smfmac_f32_16x16x64_bf16 v[40:43], v[4:7], a[16:23], v225\n\t" "v_mov_b64 v[0:1], 0\n\t"
                         "v_smfmac_f32_16x16x64_bf16 v[44:47], v[4:7], v[72:79], v225\n\t" "s_and_b64 vcc, vcc, %6\n\t" "s_cbranch_scc1 .Lsk_u\n\t" "ds_read_b128 v[72:75], v226 offset:10240\n\t" "v_mov_b64 v[2:3], 0\n\t"
                         "v_smfmac_f32_16x16x64_bf16 v[48:51], v[4:7], a[24:31], v225\n\t" "ds_read_b128 v[76:79], v226 offset:11264\n\t" "s_waitcnt vmcnt(7)\n\t"
                         "v_smfmac_f32_16x16x64_bf16 v[40:43], v[8:11], a[32:39], v225\n\t" "v_mov_b64 v[4:5], 0\n\t" "v_smfmac_f32_16x16x64_bf16 v[44:47], v[8:11], v[80:87], v225\n\t"
                         "ds_read_b128 v[80:83], v226 offset:12288\n\t" "v_mov_b64 v[6:7], 0\n\t" "v_smfmac_f32_16x16x64_bf16 v[48:51], v[8:11], a[40:47], v225\n\t" "ds_read_b128 v[84:87], v226 offset:13312\n\t"
                         "s_waitcnt vmcnt(6)\n\t" "v_smfmac_f32_16x16x64_bf16 v[40:43], v[12:15], a[48:55], v225\n\t" "v_mov_b64 v[8:9], 0\n\t" "v_smfmac_f32_16x16x64_bf16 v[44:47], v[12:15], v[88:95], v225\n\t"
                         "ds_read_b128 v[88:91], v226 offset:14336\n\t" "v_mov_b64 v[10:11], 0\n\t" "v_smfmac_f32_16x16x64_bf16 v[48:51], v[12:15], a[56:63], v225\n\t" "ds_read_b128 v[92:95], v226 offset:15360\n\t"
                         "s_waitcnt vmcnt(5)\n\t" "v_smfmac_f32_16x16x64_bf16 v[40:43], v[16:19], a[64:71], v225\n\t" "v_mov_b64 v[12:13], 0\n\t" "s_waitcnt lgkmcnt(6)\n\t"
                         "v_smfmac_f32_16x16x64_bf16 v[44:47], v[16:19], v[64:71], v225\n\t" "ds_read_b128 v[64:67], v226 offset:16384\n\t" "v_mov_b64 v[14:15], 0\n\t"
                         "v_smfmac_f32_16x16x64_bf16 v[48:51], v[16:19], a[72:79], v225\n\t" "ds_read_b128 v[68:71], v226 offset:17408\n\t" "s_waitcnt vmcnt(4)\n\t"
                         "v_smfmac_f32_16x16x64_bf16 v[40:43], v[20:23], a[80:87], v225\n\t" "v_mov_b64 v[16:17], 0\n\t" "s_waitcnt lgkmcnt(6)\n\t" "v_smfmac_f32_16x16x64_bf16 v[44:47], v[20:23], v[72:79], v225\n\t"
                         "ds_read_b128 v[72:75], v226 offset:18432\n\t" "v_mov_b64 v[18:19], 0\n\t" "v_smfmac_f32_16x16x64_bf16 v[48:51], v[20:23], a[88:95], v225\n\t" "ds_read_b128 v[76:79], v226 offset:19456\n\t"
                         "s_waitcnt vmcnt(3)\n\t" "v_smfmac_f32_16x16x64_bf16 v[40:43], v[24:27], a[96:103], v225\n\t" "v_mov_b64 v[20:21], 0\n\t" "s_waitcnt lgkmcnt(6)\n\t"
                         "v_smfmac_f32_16x16x64_bf16 v[44:47], v[24:27], v[80:87], v225\n\t" "ds_read_b128 v[80:83], v226 offset:20480\n\t" "v_mov_b64 v[22:23], 0\n\t"
                         "v_smfmac_f32_16x16x64_bf16 v[48:51], v[24:27], a[104:111], v225\n\t" "ds_read_b128 v[84:87], v226 offset:21504\n\t" "s_waitcnt vmcnt(2)\n\t"
                         "v_smfmac_f32_16x16x64_bf16 v[40:43], v[28:31], a[112:119], v225\n\t" "v_mov_b64 v[24:25], 0\n\t" "s_waitcnt lgkmcnt(6)\n\t" "v_smfmac_f32_16x16x64_bf16 v[44:47], v[28:31], v[88:95], v225\n\t"
                         "ds_read_b128 v[88:91], v226 offset:22528\n\t" "v_mov_b64 v[26:27], 0\n\t" "v_smfmac_f32_16x16x64_bf16 v[48:51], v[28:31], a[120:127], v225\n\t" "ds_read_b128 v[92:95], v226 offset:23552\n\t"
                         "s_waitcnt vmcnt(1)\n\t" "v_smfmac_f32_16x16x64_bf16 v[40:43], v[32:35], a[128:135], v225\n\t" "v_mov_b64 v[28:29], 0\n\t" "v_smfmac_f32_16x16x64_bf16 v[44:47], v[32:35], a[136:143], v225\n\t"
                         "v_mov_b64 v[30:31], 0\n\t" "v_smfmac_f32_16x16x64_bf16 v[48:51], v[32:35], a[144:151], v225\n\t" "s_waitcnt vmcnt(0)\n\t" "v_smfmac_f32_16x16x64_bf16 v[40:43], v[36:39], a[152:159], v225\n\t"
                         "v_mov_b64 v[32:33], 0\n\t" "v_smfmac_f32_16x16x64_bf16 v[44:47], v[36:39], a[160:167], v225\n\t" "v_mov_b64 v[34:35], 0\n\t" "v_smfmac_f32_16x16x64_bf16 v[48:51], v[36:39], a[168:175], v225\n\t"
                         "v_add_f32 v52, v40, v42\n\t" "v_add_f32 v53, v41, v43\n\t" "v_cmp_u_f32 vcc, v52, v53\n\t" "v_add_f32 v54, v44, v46\n\t" "v_add_f32 v55, v45, v47\n\t" "v_cvt_pk_bf16_f32 v58, v52, v53\n\t"
                         "v_pk_add_f32 v[56:57], v[48:49], v[50:51]\n\t" "v_cvt_pk_bf16_f32 v59, v54, v55\n\t" "v_cvt_pk_bf16_f32 v60, v56, v57\n\t" "ds_write_b64 v227, v[58:59]\n\t" "ds_write_b32 v228, v60\n\t"
                         "v_mov_b64 v[36:37], 0\n\t" "v_mov_b64 v[38:39], 0\n\t" "s_and_b64 vcc, vcc, %6\n\t" "v_mov_b32 v233, 0\n\t" "s_cbranch_scc1 .Lsk_u\n\t" "v_mov_b32 v233, 0\n\t" "v_mov_b32 v233, 0\n\t"
                         "v_mov_b32 v233, 0\n\t" "s_waitcnt lgkmcnt(0)\n\t" "s_barrier\n\t" "ds_read_b64_tr_b16 v[40:41], v229\n\t" "ds_read_b64_tr_b16 v[42:43], v230\n\t" "v_mov_b32 v233, 0\n\t"
                         "ds_read_b64_tr_b16 v[44:45], v229 offset:1024\n\t" "v_mov_b32 v233, 0\n\t" "ds_read_b64_tr_b16 v[46:47], v230 offset:1024\n\t" "v_mov_b32 v233, 0\n\t"
                         "ds_read_b64_tr_b16 v[48:49], v229 offset:2048\n\t" "v_mov_b32 v233, 0\n\t" "ds_read_b64_tr_b16 v[50:51], v230 offset:2048\n\t" "v_mov_b32 v233, 0\n\t" "s_waitcnt lgkmcnt(4)\n\t"
                         "v_smfmac_f32_16x16x64_bf16 v[0:3], v[40:43], a[176:183], v225\n\t" "v_smfmac_f32_16x16x64_bf16 v[4:7], v[40:43], a[200:207], v225\n\t" "s_waitcnt lgkmcnt(2)\n\t"
                         "v_smfmac_f32_16x16x64_bf16 v[0:3], v[44:47], a[184:191], v225\n\t" "v_smfmac_f32_16x16x64_bf16 v[4:7], v[44:47], a[208:215], v225\n\t" "s_waitcnt lgkmcnt(0)\n\t"
                         "v_smfmac_f32_16x16x64_bf16 v[0:3], v[48:51], a[192:199], v225\n\t" "v_smfmac_f32_16x16x64_bf16 v[4:7], v[48:51], a[216:223], v225\n\t"
                         "v_smfmac_f32_16x16x64_bf16 v[8:11], v[40:43], a[224:231], v225\n\t" "v_add_f32 v52, v0, v2\n\t" "v_add_f32 v53, v1, v3\n\t" "v_cvt_pk_bf16_f32 v54, v53, v52\n\t"
                         "v_smfmac_f32_16x16x64_bf16 v[8:11], v[44:47], a[232:239], v225\n\t" "v_add_f32 v52, v4, v6\n\t" "v_add_f32 v53, v5, v7\n\t" "v_cvt_pk_bf16_f32 v55, v53, v52\n\t" "s_nop 1\n\t"
                         "v_permlane16_swap_b32 v54, v55\n\t" "v_smfmac_f32_16x16x64_bf16 v[8:11], v[48:51], a[240:247], v225\n\t" "global_store_dwordx2 v232, v[54:55], %2 sc1\n\t" "s_add_u32 s40, s40, 0x10000\n\t"
                         "s_addc_u32 s41, s41, 0\n\t" "v_smfmac_f32_16x16x64_bf16 v[12:15], v[40:43], a[248:255], v225\n\t" "v_smfmac_f32_16x16x64_bf16 v[12:15], v[44:47], v[96:103], v225\n\t" "v_add_f32 v52, v8, v10\n\t"
                         "v_smfmac_f32_16x16x64_bf16 v[12:15], v[48:51], v[104:111], v225\n\t" "v_add_f32 v53, v9, v11\n\t" "v_smfmac_f32_16x16x64_bf16 v[16:19], v[40:43], v[112:119], v225\n\t"
                         "v_cvt_pk_bf16_f32 v56, v53, v52\n\t" "v_smfmac_f32_16x16x64_bf16 v[16:19], v[44:47], v[120:127], v225\n\t" "v_add_f32 v52, v12, v14\n\t" "v_add_f32 v53, v13, v15\n\t"
                         "v_cvt_pk_bf16_f32 v57, v53, v52\n\t" "s_nop 1\n\t" "v_permlane16_swap_b32 v56, v57\n\t" "v_smfmac_f32_16x16x64_bf16 v[16:19], v[48:51], v[128:135], v225\n\t"
                         "global_store_dwordx2 v232, v[56:57], %2 sc1\n\t" "s_add_u32 s40, s40, 0x10000\n\t" "s_addc_u32 s41, s41, 0\n\t" "v_smfmac_f32_16x16x64_bf16 v[20:23], v[40:43], v[136:143], v225\n\t"
                         "v_smfmac_f32_16x16x64_bf16 v[20:23], v[44:47], v[144:151], v225\n\t" "v_add_f32 v52, v16, v18\n\t" "v_smfmac_f32_16x16x64_bf16 v[20:23], v[48:51], v[152:159], v225\n\t" "v_add_f32 v53, v17, v19\n\t"
                         "v_smfmac_f32_16x16x64_bf16 v[24:27], v[40:43], v[160:167], v225\n\t" "v_cvt_pk_bf16_f32 v58, v53, v52\n\t" "v_smfmac_f32_16x16x64_bf16 v[24:27], v[44:47], v[168:175], v225\n\t"
                         "v_add_f32 v52, v20, v22\n\t" "v_add_f32 v53, v21, v23\n\t" "v_smfmac_f32_16x16x64_bf16 v[24:27], v[48:51], v[176:183], v225\n\t" "v_cvt_pk_bf16_f32 v59, v53, v52\n\t"
                         "v_smfmac_f32_16x16x64_bf16 v[28:31], v[40:43], v[184:191], v225\n\t" "s_nop 0\n\t" "v_permlane16_swap_b32 v58, v59\n\t" "v_smfmac_f32_16x16x64_bf16 v[28:31], v[44:47], v[192:199], v225\n\t"
                         "global_store_dwordx2 v232, v[58:59], %2 sc1\n\t" "v_add_f32 v52, v24, v26\n\t" "v_smfmac_f32_16x16x64_bf16 v[28:31], v[48:51], v[200:207], v225\n\t" "v_add_f32 v53, v25, v27\n\t"
                         "v_smfmac_f32_16x16x64_bf16 v[32:35], v[40:43], v[208:215], v225\n\t" "v_cvt_pk_bf16_f32 v60, v53, v52\n\t" "v_smfmac_f32_16x16x64_bf16 v[32:35], v[44:47], v[216:223], v225\n\t"
                         "v_add_f32 v52, v28, v30\n\t" "v_smfmac_f32_16x16x64_bf16 v[32:35], v[48:51], v[64:71], v225\n\t" "v_add_f32 v53, v29, v31\n\t" "v_smfmac_f32_16x16x64_bf16 v[36:39], v[40:43], v[72:79], v225\n\t"
                         "v_cvt_pk_bf16_f32 v61, v53, v52\n\t" "v_smfmac_f32_16x16x64_bf16 v[36:39], v[44:47], v[80:87], v225\n\t" "v_add_f32 v52, v32, v34\n\t"
                         "v_smfmac_f32_16x16x64_bf16 v[36:39], v[48:51], v[88:95], v225\n\t" "v_add_f32 v53, v33, v35\n\t" "v_cvt_pk_bf16_f32 v62, v53, v52\n\t" "v_mov_b32 v233, 0\n\t" "v_mov_b32 v233, 0\n\t"
                         "v_mov_b32 v233, 0\n\t" "v_mov_b32 v233, 0\n\t" "v_mov_b32 v233, 0\n\t" "v_pk_add_f32 v[52:53], v[36:37], v[38:39]\n\t" "v_cvt_pk_bf16_f32 v63, v53, v52\n\t" "v_permlane32_swap_b32 v60, v62\n\t"
                         "s_nop 0\n\t" "v_permlane32_swap_b32 v61, v63\n\t" "s_nop 1\n\t" "v_permlane16_swap_b32 v60, v61\n\t" "v_permlane16_swap_b32 v62, v63\n\t" "global_store_dwordx4 v231, v[60:63], %2 sc1\n\t"
                         ".Lsk_u:\n\t" "s_memrealtime %1\n\t" "s_mov_b64 %0, vcc\n\t" "s_waitcnt lgkmcnt(0)\n\t"
                         : "=&s"(bad), "=&s"(tp8), "+{s[40:41]}"(up)
                         : "s"(uni((int)tgt)), "s"(memp), "s"(memp + 16384), "s"(en), "{v[224:239]}"(ad),
                           "{a[0:31]}"(wa[0]), "{a[32:63]}"(wa[1]), "{a[64:95]}"(wa[2]), "{a[96:127]}"(wa[3]), "{a[128:159]}"(wa[4]), "{a[160:191]}"(wa[5]), "{a[192:223]}"(wa[6]), "{a[224:255]}"(wa[7]), "{v[96:127]}"(wv[0]), "{v[128:159]}"(wv[1]), "{v[160:191]}"(wv[2]), "{v[192:223]}"(wv[3])
                         : "v0", "v1", "v2", "v3", "v4", "v5", "v6", "v7", "v8", "v9", "v10", "v11", "v12", "v13", "v14", "v15", "v16", "v17", "v18", "v19", "v20", "v21", "v22", "v23", "v24", "v25", "v26", "v27", "v28", "v29", "v30", "v31", "v32", "v33", "v34", "v35", "v36", "v37", "v38", "v39", "v40", "v41", "v42", "v43", "v44", "v45", "v46", "v47", "v48", "v49", "v50", "v51", "v52", "v53", "v54", "v55", "v56", "v57", "v58", "v59", "v60", "v61", "v62", "v63", "v64", "v65", "v66", "v67", "v68", "v69", "v70", "v71", "v72", "v73", "v74", "v75", "v76", "v77", "v78", "v79", "v80", "v81", "v82", "v83", "v84", "v85", "v86", "v87", "v88", "v89", "v90", "v91", "v92", "v93", "v94", "v95", "vcc", "scc", "s42", "memory");
            if (__builtin_expect(bad == 0, 1)) break;
            waitvm<0>();
            if (--budget < 0 || ((++tries & 15) == 0 && !unwritten(memp, ad[0]))) en = 0;
            phi += (u32)cfg.G;
        }
        clk.calib();
        const u32 tt = sched_ld0(s);
        if (s >= NSEG - 2) waitvm<0>();
        barrier();
        if (s >= NSEG - 2 && w == 3) flag_add(fl, F_PROG + 8 * s + (h & 7), 1);
        if (s >= NSEG - 2 && w == 2) flag_add(fl, F_UD + 2 * (s & 1) + (h & 1), 1);
        follow(phi, (int)(tt >> 16) + 4 + cfg.T3 - VISS, (u32)tp8, cfg.G);
        phi += tt & 0xffffu;
        memp = p.ws + mslot(sd, s + 1);
    }
    asm volatile(".Lkm_lu:\n\ts_waitcnt vmcnt(0)" : : : "memory");
}

#define VALL "{v[0:3]}"(o), "{v[16:47]}"(rv[0]), "{v[48:79]}"(rv[1]), "{v[80:111]}"(rv[2]), "{v[112:143]}"(rv[3]), "{v[144:175]}"(rv[4]), "{v[176:207]}"(rv[5]), "{v[208:239]}"(rv[6]), "{a[0:31]}"(ra[0]), "{a[32:63]}"(ra[1]), "{a[64:95]}"(ra[2]), "{a[96:127]}"(ra[3])
DI void value_role(const Ptrs& p, const ChainCfg cfg, int vb, int sd, u32 phi, u32 lds, int w) {
    const int lane = lanex(), tid = 64 * w + lane, n = lane & 15, g = lane >> 4;
    const int v = 2 * (vb & 3) + ((vb >> 2) & 1) + (vb & ~7);
    const u32 moff = lane * 16;
    char* const fl = p.ws + OFF_ST;
    char* const ub = p.ws + (size_t)v * 8192;
    const int xh = (vb >> 2) & 1;
    if (w == 3) {
        const u32x4 ff = u32x4{~0u, ~0u, ~0u, ~0u};
#pragma nounroll
        for (int s = 0; s < NSEG - 2; ++s) {
            dly(cfg.cR3 >> 8 & 255);
            char* const mk = ub + uslot(sd, s + 2);
#pragma unroll
            for (int i = 0; i < 8; ++i) { gst16(mk + i * 1024, moff, ff); gap<HQ>(-1); }
            if (vb < 32 && s) {
                gst16(p.ws + (size_t)(s + 2) * HO_STRIDE + vb * 2048, moff, ff);
                gst16(p.ws + (size_t)(s + 2) * HO_STRIDE + vb * 2048 + 1024, moff, ff);
            }
            waitvm<0>();
            barrier();
        }
        return;
    }
    if (w) {
        const u32 dof = (u32)((n >> 3) * 16384 + g * 128 + (n & 7) * 16);
        const char* dp = p.ws + OFF_D + (size_t)v * 32768 - 4096 + w * 8192;
        flag_wait(fl, F_VD + (vb & 3) + (v < 20 ? 0 : 4), 32);
#pragma nounroll
        for (int s = 0; s < NSEG - 1; ++s) {
            u32x4 df[16];
            u32 dfl;
            dly(cfg.cR3 & 255);
            sfor<0, 16>([&](auto I) { gld16c<I.v * 512 - 4096>(df[I.v], dp, dof); gap<HP>(1 - s); });
            dp += D_STRIDE;
            flag_ldm(dfl, fl, F_VD + pblk(s + 1) * 8 + (vb & RM), pblk(s + 1) != pblk(s));
            const u32 la = lds + moff + (s & 1) * 32768 + (w - 1) * 16384;
            sfor<0, 8>([&](auto I) {
                constexpr int i = I.v;
                waitv<15 - 2 * i>(df[2 * i]); waitv<15 - 2 * i>(df[2 * i + 1]);
                const u32x4 x = df[2 * i], y = df[2 * i + 1];
                const u32 lb = i & 2 ? la - xh * 4096 : la + xh * 4096;
                *LDSP(u32x4, lb + 2048 * i) = u32x4{x[0], y[0], x[2], y[2]};
                *LDSP(u32x4, lb + 2048 * i + 1024) = u32x4{x[1], y[1], x[3], y[3]};
                PIN();
            });
            waitvm<0>();
            asm volatile("" : "+v"(dfl));
            barrier();
            if (uni((int)dfl) < 31) flag_wait(fl, F_VD + pblk(s + 1) * 8 + (vb & RM), 32);
        }
        return;
    }
    u32x32 rv[7], ra[4];
    u32x4 o;
    asm volatile("v_accvgpr_write_b32 a64, 0\n\t" "v_accvgpr_write_b32 a65, 0\n\t" "v_accvgpr_write_b32 a66, 0\n\t" "v_accvgpr_write_b32 a67, 0\n\t" "v_accvgpr_write_b32 a68, 0\n\t" "v_accvgpr_write_b32 a69, 0\n\t"
                 "v_accvgpr_write_b32 a70, 0\n\t" "v_accvgpr_write_b32 a71, 0\n\t" "v_accvgpr_write_b32 a72, 0\n\t" "v_accvgpr_write_b32 a73, 0\n\t" "v_accvgpr_write_b32 a74, 0\n\t" "v_accvgpr_write_b32 a75, 0\n\t"
                 "v_accvgpr_write_b32 a76, 0\n\t" "v_accvgpr_write_b32 a77, 0\n\t" "v_accvgpr_write_b32 a78, 0\n\t" "v_accvgpr_write_b32 a79, 0\n\t" "v_mov_b64 v[112:113], -1\n\t" "v_mov_b64 v[120:121], -1\n\t"
                 "v_mov_b64 v[128:129], -1\n\t" "v_mov_b64 v[136:137], -1\n\t" "v_mov_b64 v[144:145], -1\n\t" "v_mov_b64 v[152:153], -1\n\t" "v_mov_b64 v[160:161], -1\n\t" "v_mov_b64 v[168:169], -1\n\t"
                 "v_mov_b64 v[176:177], -1\n\t" "v_mov_b64 v[184:185], -1\n\t" "v_mov_b64 v[192:193], -1\n\t" "v_mov_b64 v[200:201], -1\n\t" "v_mov_b64 v[208:209], -1\n\t" "v_mov_b64 v[216:217], -1\n\t"
                 "v_mov_b64 v[224:225], -1\n\t" "v_mov_b64 v[232:233], -1\n\t"
                 : "={v[0:3]}"(o), "={v[16:47]}"(rv[0]), "={v[48:79]}"(rv[1]), "={v[80:111]}"(rv[2]), "={v[112:143]}"(rv[3]), "={v[144:175]}"(rv[4]), "={v[176:207]}"(rv[5]), "={v[208:239]}"(rv[6]), "={a[0:31]}"(ra[0]), "={a[32:63]}"(ra[1]), "={a[64:95]}"(ra[2]), "={a[96:127]}"(ra[3]));
    const u32 ho = (u32)(min(lane, 39) * 256 + (lane >> 5) * 8192);
    const u32 pof = (u32)((n & 4) * 128 + (n >> 3) * 256 + g * 64 + (n & 3) * 16 + 2048);
    const u32 uof = (u32)((n >> 3) * 4096 + g * 128 + (n & 7) * 16 + 512);
    u32x8 ad = u32x8{uof + xh * 512, pof + xh * 2048, (u32)(((g >> 1) ^ xh) * 256 + (n >> 2) * 64 + (n & 1) * 16), moff,
                     (lane & 2) ? 0xEEEEu : 0x4444u, (lane & 4) ? 0xEEEEu : 0x4444u, (u32)(v * 256 + (v >> 5) * 8192 + g * 64 + (n >> 3) * 32 + (n & 1) * 16 + (n & 6) * 2),
                     (lane & 1) ? 0x07060302u : 0x01000504u};
    u32 pob = pof - xh * 2048, uob = uof - xh * 512, pmo = moff ^ xh * 512;
    asm volatile("" : "+{v[4:11]}"(ad), "+v"(pob), "+v"(uob), "+v"(pmo));
    auto zero = [&]() {
        asm volatile("v_mov_b64 v[0:1], 0\n\t" "v_mov_b64 v[2:3], 0\n\t" "v_mov_b64 v[48:49], 0\n\t" "v_mov_b64 v[50:51], 0\n\t" "v_mov_b64 v[52:53], 0\n\t" "v_mov_b64 v[54:55], 0\n\t" "v_mov_b64 v[56:57], 0\n\t"
                     "v_mov_b64 v[58:59], 0\n\t" "v_mov_b64 v[60:61], 0\n\t" "v_mov_b64 v[62:63], 0\n\t" "v_mov_b64 v[64:65], 0\n\t" "v_mov_b64 v[66:67], 0\n\t" "v_mov_b64 v[68:69], 0\n\t" "v_mov_b64 v[70:71], 0\n\t"
                     "v_mov_b64 v[72:73], 0\n\t" "v_mov_b64 v[74:75], 0\n\t" "v_mov_b64 v[76:77], 0\n\t" "v_mov_b64 v[78:79], 0\n\t" "v_mov_b64 v[80:81], 0\n\t" "v_mov_b64 v[82:83], 0\n\t" "v_mov_b64 v[84:85], 0\n\t"
                     "v_mov_b64 v[86:87], 0\n\t" "v_mov_b64 v[88:89], 0\n\t" "v_mov_b64 v[90:91], 0\n\t" "v_mov_b64 v[92:93], 0\n\t" "v_mov_b64 v[94:95], 0\n\t" "v_mov_b64 v[96:97], 0\n\t" "v_mov_b64 v[98:99], 0\n\t"
                     "v_mov_b64 v[100:101], 0\n\t" "v_mov_b64 v[102:103], 0\n\t" "v_mov_b64 v[104:105], 0\n\t" "v_mov_b64 v[106:107], 0\n\t" "v_mov_b64 v[108:109], 0\n\t" "v_mov_b64 v[110:111], 0\n\t"
                     : : VALL : "memory");
    };
    const char* memp = p.ws + OFF_XF + 4096;
    char* up = ub + OFF_XF;
    int budget = 30000;
    Clock clk; clk.init();
    u32 tt = 0;
    for (int s = -1; s < NSEG - 1; ++s) {
        const char* const mnx = p.ws + mslot(sd, s + 1);
        bool all = false;
        int tries = 0;
        u64 tp8;
        while (true) {
            const u32 tgt = clk.sched(phi, (int)(tt >> 16), cfg.G, tries ? 2 : s > 0) - (s < 0 ? 1u << 20 : 0u);
            u64 ok;
            asm volatile("s_cmp_lg_u32 %11, 0\n\t" "s_cbranch_scc0 .Lsp_v0\n\t" ".Lsp_v3:\n\t" "s_memtime vcc\n\t" "s_waitcnt lgkmcnt(0)\n\t" "s_sub_i32 s42, %10, vcc_lo\n\t" "s_cmp_gt_i32 s42, 80\n\t"
                         "s_cbranch_scc1 .Lsp_v3\n\t" "s_max_i32 s42, s42, 0\n\t" "s_sub_i32 s42, 92, s42\n\t" "s_and_b32 s42, s42, -4\n\t" "s_getpc_b64 vcc\n\t" "s_add_u32 vcc_lo, vcc_lo, s42\n\t"
                         "s_addc_u32 vcc_hi, vcc_hi, 0\n\t" "s_setpc_b64 vcc\n\t" "s_nop 0\n\t" "s_nop 0\n\t" "s_nop 0\n\t" "s_nop 0\n\t" "s_nop 0\n\t" "s_nop 0\n\t" "s_nop 0\n\t" "s_nop 0\n\t" "s_nop 0\n\t" "s_nop 0\n\t"
                         "s_nop 0\n\t" "s_nop 0\n\t" "s_nop 0\n\t" "s_nop 0\n\t" "s_nop 0\n\t" "s_nop 0\n\t" "s_nop 0\n\t" "s_nop 0\n\t" "s_nop 0\n\t" "s_nop 0\n\t"
                         "global_load_dwordx4 a[0:3], v5, %7 offset:-4096 sc1\n\t" "global_load_dwordx4 a[4:7], v5, %7 offset:-3072 sc1\n\t" "global_load_dwordx4 a[16:19], v5, %7 sc1\n\t"
                         "global_load_dwordx4 a[20:23], v5, %7 offset:1024 sc1\n\t" "global_load_dwordx4 a[32:35], v5, %8 offset:-4096 sc1\n\t" "global_load_dwordx4 a[36:39], v5, %8 offset:-3072 sc1\n\t"
                         "global_load_dwordx4 a[48:51], v5, %8 sc1\n\t" "global_load_dwordx4 a[52:55], v5, %8 offset:1024 sc1\n\t" "s_nop 14\n\t" "global_load_dwordx4 a[8:11], %12, %7 offset:-2048 sc1\n\t"
                         "global_load_dwordx4 a[12:15], %12, %7 offset:-1024 sc1\n\t" "global_load_dwordx4 a[24:27], %12, %7 offset:2048 sc1\n\t" "global_load_dwordx4 a[28:31], %12, %7 offset:3072 sc1\n\t"
                         "global_load_dwordx4 a[40:43], %12, %8 offset:-2048 sc1\n\t" "global_load_dwordx4 a[44:47], %12, %8 offset:-1024 sc1\n\t" "global_load_dwordx4 a[56:59], %12, %8 offset:2048 sc1\n\t"
                         "global_load_dwordx4 a[60:63], %12, %8 offset:3072 sc1\n\t" "s_nop 15\n\t" "s_nop 5\n\t" "s_mov_b64 exec, %9\n\t" "global_load_dwordx4 a[64:67], v6, %6 offset:-2048 sc1\n\t"
                         "global_load_dwordx4 a[68:71], v6, %6 offset:-1536 sc1\n\t" "global_load_dwordx4 a[72:75], v6, %6 offset:-1024 sc1\n\t" "global_load_dwordx4 a[76:79], v6, %6 offset:-512 sc1\n\t"
                         "s_mov_b64 exec, -1\n\t" "global_load_dwordx4 a[80:83], %14, %6 sc1\n\t" "global_load_dwordx4 a[84:87], %14, %6 offset:1024 sc1\n\t" "global_load_dwordx4 a[88:91], %14, %6 offset:2048 sc1\n\t"
                         "global_load_dwordx4 a[92:95], %14, %6 offset:3072 sc1\n\t" "s_mov_b32 s42, %11\n\t" ".Lsp_v5:\n\t" "s_sub_u32 s42, s42, 1\n\t" "s_cmp_gt_i32 s42, 0\n\t" "s_cbranch_scc1 .Lsp_v5\n\t"
                         "global_load_dwordx4 a[96:99], v4, %3 offset:-512 sc1\n\t" "global_load_dwordx4 a[100:103], %13, %3 sc1\n\t" "global_load_dwordx4 a[104:107], v4, %3 offset:512 sc1\n\t"
                         "global_load_dwordx4 a[108:111], %13, %3 offset:1024 sc1\n\t" "global_load_dwordx4 a[112:115], v4, %3 offset:1536 sc1\n\t" "global_load_dwordx4 a[116:119], %13, %3 offset:2048 sc1\n\t"
                         "global_load_dwordx4 a[120:123], v4, %3 offset:2560 sc1\n\t" "global_load_dwordx4 a[124:127], %13, %3 offset:3072 sc1\n\t" "s_waitcnt vmcnt(31)\n\t"
                         "v_smfmac_f32_16x16x64_bf16 v[48:51], a[0:3], v[112:119], v9\n\t" "s_waitcnt vmcnt(30)\n\t" "v_smfmac_f32_16x16x64_bf16 v[52:55], a[4:7], v[120:127], v9\n\t" "s_waitcnt vmcnt(29)\n\t"
                         "v_smfmac_f32_16x16x64_bf16 v[64:67], a[16:19], v[144:151], v9\n\t" "s_waitcnt vmcnt(28)\n\t" "v_cvt_pk_bf16_f32 v16, v48, v49\n\t" "v_cvt_pk_bf16_f32 v17, v50, v51\n\t"
                         "v_smfmac_f32_16x16x64_bf16 v[68:71], a[20:23], v[152:159], v9\n\t" "s_waitcnt vmcnt(27)\n\t" "v_cvt_pk_bf16_f32 v18, v52, v53\n\t" "v_cvt_pk_bf16_f32 v19, v54, v55\n\t"
                         "v_smfmac_f32_16x16x64_bf16 v[80:83], a[32:35], v[176:183], v9\n\t" "s_waitcnt vmcnt(26)\n\t" "v_cvt_pk_bf16_f32 v24, v64, v65\n\t" "v_cvt_pk_bf16_f32 v25, v66, v67\n\t"
                         "v_smfmac_f32_16x16x64_bf16 v[84:87], a[36:39], v[184:191], v9\n\t" "s_waitcnt vmcnt(25)\n\t" "v_cvt_pk_bf16_f32 v26, v68, v69\n\t" "v_cvt_pk_bf16_f32 v27, v70, v71\n\t"
                         "v_smfmac_f32_16x16x64_bf16 v[96:99], a[48:51], v[208:215], v9\n\t" "s_waitcnt vmcnt(24)\n\t" "v_cvt_pk_bf16_f32 v32, v80, v81\n\t" "v_cvt_pk_bf16_f32 v33, v82, v83\n\t"
                         "v_smfmac_f32_16x16x64_bf16 v[100:103], a[52:55], v[216:223], v9\n\t" "s_waitcnt vmcnt(23)\n\t" "v_cvt_pk_bf16_f32 v34, v84, v85\n\t" "v_cvt_pk_bf16_f32 v35, v86, v87\n\t"
                         "v_smfmac_f32_16x16x64_bf16 v[56:59], a[8:11], v[128:135], v9\n\t" "s_waitcnt vmcnt(22)\n\t" "v_cvt_pk_bf16_f32 v40, v96, v97\n\t" "v_cvt_pk_bf16_f32 v41, v98, v99\n\t"
                         "v_smfmac_f32_16x16x64_bf16 v[60:63], a[12:15], v[136:143], v9\n\t" "s_waitcnt vmcnt(21)\n\t" "v_cvt_pk_bf16_f32 v42, v100, v101\n\t" "v_cvt_pk_bf16_f32 v43, v102, v103\n\t"
                         "v_smfmac_f32_16x16x64_bf16 v[72:75], a[24:27], v[160:167], v9\n\t" "s_waitcnt vmcnt(20)\n\t" "v_cvt_pk_bf16_f32 v20, v56, v57\n\t" "v_cvt_pk_bf16_f32 v21, v58, v59\n\t"
                         "v_smfmac_f32_16x16x64_bf16 v[76:79], a[28:31], v[168:175], v9\n\t" "s_waitcnt vmcnt(19)\n\t" "v_cvt_pk_bf16_f32 v22, v60, v61\n\t" "v_cvt_pk_bf16_f32 v23, v62, v63\n\t"
                         "v_smfmac_f32_16x16x64_bf16 v[88:91], a[40:43], v[192:199], v9\n\t" "s_waitcnt vmcnt(18)\n\t" "v_cvt_pk_bf16_f32 v28, v72, v73\n\t" "v_cvt_pk_bf16_f32 v29, v74, v75\n\t"
                         "v_smfmac_f32_16x16x64_bf16 v[92:95], a[44:47], v[200:207], v9\n\t" "s_waitcnt vmcnt(17)\n\t" "v_cvt_pk_bf16_f32 v30, v76, v77\n\t" "v_cvt_pk_bf16_f32 v31, v78, v79\n\t"
                         "v_smfmac_f32_16x16x64_bf16 v[104:107], a[56:59], v[224:231], v9\n\t" "s_waitcnt vmcnt(16)\n\t" "v_cvt_pk_bf16_f32 v36, v88, v89\n\t" "v_cvt_pk_bf16_f32 v37, v90, v91\n\t"
                         "v_smfmac_f32_16x16x64_bf16 v[108:111], a[60:63], v[232:239], v9\n\t" "s_waitcnt vmcnt(15)\n\t" "v_cvt_pk_bf16_f32 v38, v92, v93\n\t" "v_cvt_pk_bf16_f32 v39, v94, v95\n\t"
                         "v_smfmac_f32_16x16x64_bf16 v[0:3], a[64:67], v[16:23], v8\n\t" "s_waitcnt vmcnt(14)\n\t" "v_cvt_pk_bf16_f32 v44, v104, v105\n\t" "v_cvt_pk_bf16_f32 v45, v106, v107\n\t"
                         "v_smfmac_f32_16x16x64_bf16 v[0:3], a[68:71], v[24:31], v8\n\t" "s_waitcnt vmcnt(13)\n\t" "v_cvt_pk_bf16_f32 v46, v108, v109\n\t" "v_cvt_pk_bf16_f32 v47, v110, v111\n\t"
                         "v_smfmac_f32_16x16x64_bf16 v[0:3], a[72:75], v[32:39], v8\n\t" "s_waitcnt vmcnt(12)\n\t" "v_smfmac_f32_16x16x64_bf16 v[0:3], a[76:79], v[40:47], v8\n\t" "s_waitcnt vmcnt(6)\n\t"
                         "v_smfmac_f32_16x16x64_bf16 v[0:3], a[80:83], a[96:103], v8\n\t" "s_waitcnt vmcnt(4)\n\t" "v_smfmac_f32_16x16x64_bf16 v[0:3], a[84:87], a[104:111], v8\n\t" "s_waitcnt vmcnt(2)\n\t"
                         "v_smfmac_f32_16x16x64_bf16 v[0:3], a[88:91], a[112:119], v8\n\t" "s_waitcnt vmcnt(0)\n\t" "v_smfmac_f32_16x16x64_bf16 v[0:3], a[92:95], a[120:127], v8\n\t" "s_branch .Lv_end\n\t" ".Lsp_v0:\n\t"
                         "s_memtime vcc\n\t" "s_waitcnt lgkmcnt(0)\n\t" "s_sub_i32 s42, %10, vcc_lo\n\t" "s_cmp_gt_i32 s42, 80\n\t" "s_cbranch_scc1 .Lsp_v0\n\t" "s_max_i32 s42, s42, 0\n\t" "s_sub_i32 s42, 92, s42\n\t"
                         "s_and_b32 s42, s42, -4\n\t" "s_getpc_b64 vcc\n\t" "s_add_u32 vcc_lo, vcc_lo, s42\n\t" "s_addc_u32 vcc_hi, vcc_hi, 0\n\t" "s_setpc_b64 vcc\n\t" "s_nop 0\n\t" "s_nop 0\n\t" "s_nop 0\n\t" "s_nop 0\n\t"
                         "s_nop 0\n\t" "s_nop 0\n\t" "s_nop 0\n\t" "s_nop 0\n\t" "s_nop 0\n\t" "s_nop 0\n\t" "s_nop 0\n\t" "s_nop 0\n\t" "s_nop 0\n\t" "s_nop 0\n\t" "s_nop 0\n\t" "s_nop 0\n\t" "s_nop 0\n\t" "s_nop 0\n\t"
                         "s_nop 0\n\t" "s_nop 0\n\t" "global_load_dwordx4 a[96:99], v4, %3 offset:-512 sc1\n\t" "global_load_dwordx4 a[104:107], v4, %3 offset:512 sc1\n\t"
                         "global_load_dwordx4 a[112:115], v4, %3 offset:1536 sc1\n\t" "global_load_dwordx4 a[120:123], v4, %3 offset:2560 sc1\n\t" "global_load_dwordx4 a[100:103], %13, %3 sc1\n\t"
                         "global_load_dwordx4 a[108:111], %13, %3 offset:1024 sc1\n\t" "global_load_dwordx4 a[116:119], %13, %3 offset:2048 sc1\n\t" "global_load_dwordx4 a[124:127], %13, %3 offset:3072 sc1\n\t" "s_nop 9\n\t"
                         "global_load_dwordx4 a[0:3], v5, %7 offset:-4096 sc1\n\t" "global_load_dwordx4 a[4:7], v5, %7 offset:-3072 sc1\n\t" "global_load_dwordx4 a[16:19], v5, %7 sc1\n\t"
                         "global_load_dwordx4 a[20:23], v5, %7 offset:1024 sc1\n\t" "global_load_dwordx4 a[32:35], v5, %8 offset:-4096 sc1\n\t" "global_load_dwordx4 a[36:39], v5, %8 offset:-3072 sc1\n\t"
                         "global_load_dwordx4 a[48:51], v5, %8 sc1\n\t" "global_load_dwordx4 a[52:55], v5, %8 offset:1024 sc1\n\t" "s_nop 14\n\t" "global_load_dwordx4 a[8:11], %12, %7 offset:-2048 sc1\n\t"
                         "global_load_dwordx4 a[12:15], %12, %7 offset:-1024 sc1\n\t" "global_load_dwordx4 a[24:27], %12, %7 offset:2048 sc1\n\t" "global_load_dwordx4 a[28:31], %12, %7 offset:3072 sc1\n\t"
                         "global_load_dwordx4 a[40:43], %12, %8 offset:-2048 sc1\n\t" "global_load_dwordx4 a[44:47], %12, %8 offset:-1024 sc1\n\t" "global_load_dwordx4 a[56:59], %12, %8 offset:2048 sc1\n\t"
                         "global_load_dwordx4 a[60:63], %12, %8 offset:3072 sc1\n\t" "s_nop 15\n\t" "s_nop 5\n\t" "global_load_dwordx4 a[80:83], %14, %6 sc1\n\t"
                         "global_load_dwordx4 a[84:87], %14, %6 offset:1024 sc1\n\t" "global_load_dwordx4 a[88:91], %14, %6 offset:2048 sc1\n\t" "global_load_dwordx4 a[92:95], %14, %6 offset:3072 sc1\n\t"
                         "s_mov_b64 exec, %9\n\t" "global_load_dwordx4 a[64:67], v6, %6 offset:-2048 sc1\n\t" "global_load_dwordx4 a[68:71], v6, %6 offset:-1536 sc1\n\t"
                         "global_load_dwordx4 a[72:75], v6, %6 offset:-1024 sc1\n\t" "global_load_dwordx4 a[76:79], v6, %6 offset:-512 sc1\n\t" "s_mov_b64 exec, -1\n\t" "s_waitcnt vmcnt(23)\n\t"
                         "v_smfmac_f32_16x16x64_bf16 v[48:51], a[0:3], v[112:119], v9\n\t" "s_waitcnt vmcnt(22)\n\t" "v_smfmac_f32_16x16x64_bf16 v[52:55], a[4:7], v[120:127], v9\n\t" "s_waitcnt vmcnt(21)\n\t"
                         "v_smfmac_f32_16x16x64_bf16 v[64:67], a[16:19], v[144:151], v9\n\t" "s_waitcnt vmcnt(20)\n\t" "v_cvt_pk_bf16_f32 v16, v48, v49\n\t" "v_cvt_pk_bf16_f32 v17, v50, v51\n\t"
                         "v_smfmac_f32_16x16x64_bf16 v[68:71], a[20:23], v[152:159], v9\n\t" "s_waitcnt vmcnt(19)\n\t" "v_cvt_pk_bf16_f32 v18, v52, v53\n\t" "v_cvt_pk_bf16_f32 v19, v54, v55\n\t"
                         "v_smfmac_f32_16x16x64_bf16 v[80:83], a[32:35], v[176:183], v9\n\t" "s_waitcnt vmcnt(18)\n\t" "v_cvt_pk_bf16_f32 v24, v64, v65\n\t" "v_cvt_pk_bf16_f32 v25, v66, v67\n\t"
                         "v_smfmac_f32_16x16x64_bf16 v[84:87], a[36:39], v[184:191], v9\n\t" "s_waitcnt vmcnt(17)\n\t" "v_cvt_pk_bf16_f32 v26, v68, v69\n\t" "v_cvt_pk_bf16_f32 v27, v70, v71\n\t"
                         "v_smfmac_f32_16x16x64_bf16 v[96:99], a[48:51], v[208:215], v9\n\t" "s_waitcnt vmcnt(16)\n\t" "v_cvt_pk_bf16_f32 v32, v80, v81\n\t" "v_cvt_pk_bf16_f32 v33, v82, v83\n\t"
                         "v_smfmac_f32_16x16x64_bf16 v[100:103], a[52:55], v[216:223], v9\n\t" "s_waitcnt vmcnt(15)\n\t" "v_cvt_pk_bf16_f32 v34, v84, v85\n\t" "v_cvt_pk_bf16_f32 v35, v86, v87\n\t"
                         "v_smfmac_f32_16x16x64_bf16 v[56:59], a[8:11], v[128:135], v9\n\t" "s_waitcnt vmcnt(14)\n\t" "v_cvt_pk_bf16_f32 v40, v96, v97\n\t" "v_cvt_pk_bf16_f32 v41, v98, v99\n\t"
                         "v_smfmac_f32_16x16x64_bf16 v[60:63], a[12:15], v[136:143], v9\n\t" "s_waitcnt vmcnt(13)\n\t" "v_cvt_pk_bf16_f32 v42, v100, v101\n\t" "v_cvt_pk_bf16_f32 v43, v102, v103\n\t"
                         "v_smfmac_f32_16x16x64_bf16 v[72:75], a[24:27], v[160:167], v9\n\t" "s_waitcnt vmcnt(12)\n\t" "v_cvt_pk_bf16_f32 v20, v56, v57\n\t" "v_cvt_pk_bf16_f32 v21, v58, v59\n\t"
                         "v_smfmac_f32_16x16x64_bf16 v[76:79], a[28:31], v[168:175], v9\n\t" "s_waitcnt vmcnt(11)\n\t" "v_cvt_pk_bf16_f32 v22, v60, v61\n\t" "v_cvt_pk_bf16_f32 v23, v62, v63\n\t"
                         "v_smfmac_f32_16x16x64_bf16 v[88:91], a[40:43], v[192:199], v9\n\t" "s_waitcnt vmcnt(10)\n\t" "v_cvt_pk_bf16_f32 v28, v72, v73\n\t" "v_cvt_pk_bf16_f32 v29, v74, v75\n\t"
                         "v_smfmac_f32_16x16x64_bf16 v[92:95], a[44:47], v[200:207], v9\n\t" "s_waitcnt vmcnt(9)\n\t" "v_cvt_pk_bf16_f32 v30, v76, v77\n\t" "v_cvt_pk_bf16_f32 v31, v78, v79\n\t"
                         "v_smfmac_f32_16x16x64_bf16 v[104:107], a[56:59], v[224:231], v9\n\t" "s_waitcnt vmcnt(8)\n\t" "v_cvt_pk_bf16_f32 v36, v88, v89\n\t"
                         "v_smfmac_f32_16x16x64_bf16 v[108:111], a[60:63], v[232:239], v9\n\t" "s_waitcnt vmcnt(7)\n\t" "v_cvt_pk_bf16_f32 v37, v90, v91\n\t"
                         "v_smfmac_f32_16x16x64_bf16 v[0:3], a[80:83], a[96:103], v8\n\t" "s_waitcnt vmcnt(6)\n\t" "v_cvt_pk_bf16_f32 v38, v92, v93\n\t" "v_smfmac_f32_16x16x64_bf16 v[0:3], a[84:87], a[104:111], v8\n\t"
                         "s_waitcnt vmcnt(5)\n\t" "v_cvt_pk_bf16_f32 v39, v94, v95\n\t" "v_smfmac_f32_16x16x64_bf16 v[0:3], a[88:91], a[112:119], v8\n\t" "s_waitcnt vmcnt(4)\n\t" "v_cvt_pk_bf16_f32 v44, v104, v105\n\t"
                         "v_smfmac_f32_16x16x64_bf16 v[0:3], a[92:95], a[120:127], v8\n\t" "s_waitcnt vmcnt(3)\n\t" "v_cvt_pk_bf16_f32 v45, v106, v107\n\t" "v_smfmac_f32_16x16x64_bf16 v[0:3], a[64:67], v[16:23], v8\n\t"
                         "s_waitcnt vmcnt(2)\n\t" "v_cvt_pk_bf16_f32 v46, v108, v109\n\t" "v_smfmac_f32_16x16x64_bf16 v[0:3], a[68:71], v[24:31], v8\n\t" "s_waitcnt vmcnt(1)\n\t" "v_cvt_pk_bf16_f32 v47, v110, v111\n\t"
                         "v_smfmac_f32_16x16x64_bf16 v[0:3], a[72:75], v[32:39], v8\n\t" "s_waitcnt vmcnt(0)\n\t" "v_smfmac_f32_16x16x64_bf16 v[0:3], a[76:79], v[40:47], v8\n\t" ".Lv_end:\n\t" "s_nop 7\n\t"
                         "v_pk_add_f32 v[0:1], v[0:1], v[2:3]\n\t" "v_cvt_pk_bf16_f32 v2, v0, v1\n\t" "v_cmp_u_f32 vcc, v0, v1\n\t" "s_cmp_eq_u64 vcc, 0\n\t"
                         "v_mov_b32_dpp v3, v2 quad_perm:[1,0,3,2] row_mask:0xf bank_mask:0xf\n\t" "s_cselect_b64 %0, -1, 0\n\t" "v_perm_b32 v0, v2, v3, v11\n\t" "s_mov_b64 exec, %0\n\t"
                         "global_store_dword v10, v0, %5 offset:-4096 sc1\n\t" "global_store_dword v10, v0, %4 offset:-4096 sc1\n\t" "s_memrealtime %1\n\t" "s_mov_b64 exec, -1\n\t" "s_waitcnt lgkmcnt(0)\n\t"
                         : "=&s"(ok), "=&s"(tp8)
                         : "{v[4:11]}"(ad), "s"(up), "s"(mnx), "s"((u64)mnx ^ 8192), "s"(memp + 16384), "s"(memp + 32768 - 2048), "s"(memp + 49152 - 2048), "s"(0xc30000c3c30000c3ull),
                           "s"(uni((int)(tgt - (u32)(v < 16 ? cfg.dU - (v >> 3) * cfg.s2 : cfg.cR2)))), "s"(v < 16 ? 0 : (v < 24 ? 1 : cfg.T3)), "v"(pob), "v"(uob), "v"(pmo), VALL : "memory", "vcc", "scc", "s42");
            if (__builtin_expect(ok != 0, 1) || s < 0) break;
            if (all || --budget < 0) {
                asm volatile("v_min_u32 v0, -2, v0\n\tglobal_store_dword v10, v0, %0 offset:-4096 sc1\n\tglobal_store_dword v10, v0, %1 offset:-4096 sc1"
                             : : "s"(mnx), "s"((u64)mnx ^ 8192), "{v[4:11]}"(ad), VALL : "memory");
                break;
            }
            if ((++tries & 15) == 0) {
                u32 m = 0;
#pragma nounroll
                for (int i = 0; i < 32; ++i) {
                    u32x4 t;
                    gld16c<0>(t, i < 24 ? memp + 12288 : up, i < 20 ? (4 + i + ((4 + i) & 24)) * 1024 + ad[3] : (i < 24 ? i * 512 - 8192 + ad[2] : (i & 7) * 512 - 512 + (i & 1 ? uob : ad[0]))); waitv<0>(t);
                    m = max3u(max3u(m, t[0], t[1]), t[2], t[3]);
                }
                all = __builtin_amdgcn_ballot_w64(m == 0xffffffffu) == 0;
            }
            phi += (u32)cfg.G;
            zero();
        }
        asm volatile("s_barrier\n\t" "ds_read_b128 v[112:115], %0\n\t" "ds_read_b128 v[116:119], %0 offset:1024\n\t" "ds_read_b128 v[120:123], %0 offset:2048\n\t" "ds_read_b128 v[124:127], %0 offset:3072\n\t"
                     "ds_read_b128 v[128:131], %0 offset:4096\n\t" "ds_read_b128 v[132:135], %0 offset:5120\n\t" "ds_read_b128 v[136:139], %0 offset:6144\n\t" "ds_read_b128 v[140:143], %0 offset:7168\n\t"
                     "ds_read_b128 v[144:147], %0 offset:8192\n\t" "ds_read_b128 v[148:151], %0 offset:9216\n\t" "ds_read_b128 v[152:155], %0 offset:10240\n\t" "ds_read_b128 v[156:159], %0 offset:11264\n\t"
                     "ds_read_b128 v[160:163], %0 offset:12288\n\t" "ds_read_b128 v[164:167], %0 offset:13312\n\t" "ds_read_b128 v[168:171], %0 offset:14336\n\t" "ds_read_b128 v[172:175], %0 offset:15360\n\t"
                     "ds_read_b128 v[176:179], %0 offset:16384\n\t" "ds_read_b128 v[180:183], %0 offset:17408\n\t" "ds_read_b128 v[184:187], %0 offset:18432\n\t" "ds_read_b128 v[188:191], %0 offset:19456\n\t"
                     "ds_read_b128 v[192:195], %0 offset:20480\n\t" "ds_read_b128 v[196:199], %0 offset:21504\n\t" "ds_read_b128 v[200:203], %0 offset:22528\n\t" "ds_read_b128 v[204:207], %0 offset:23552\n\t"
                     "ds_read_b128 v[208:211], %0 offset:24576\n\t" "ds_read_b128 v[212:215], %0 offset:25600\n\t" "ds_read_b128 v[216:219], %0 offset:26624\n\t" "ds_read_b128 v[220:223], %0 offset:27648\n\t"
                     "ds_read_b128 v[224:227], %0 offset:28672\n\t" "ds_read_b128 v[228:231], %0 offset:29696\n\t" "ds_read_b128 v[232:235], %0 offset:30720\n\t" "ds_read_b128 v[236:239], %0 offset:31744\n\t"
                     : : "v"(lds + ad[3] + ((s + 1) & 1) * 32768), VALL : "memory");
        if (s >= 0) {
            follow(phi, (int)(tt & 0xffffu) + 5 * (v >> 2) / 2 - VISV, (u32)tp8, cfg.G);
            if (vb == 0 && lane < 8) gst8(fl, (F_CUR + lane) * 128, u32x2{(u32)s - 1, phi + (tt & 0xffffu)});
        }
        phi += tt & 0xffffu;
        memp = mnx; up = ub + uslot(sd, s + 1);
        zero();
        tt = sched_ld0(s + 1);
        clk.calib();
        while (s >= 0) {
            u32 h0;
            spin_to(clk.at(phi + (u32)cfg.dV));
            asm volatile("global_load_dword %0, %1, %2 offset:-4096 sc1\n\ts_sleep 5" : "=&v"(h0) : "v"(ho), "s"(memp) : "memory");
            edge(clk.r1, clk.m1);
            asm volatile("s_waitcnt vmcnt(0)" : "+v"(h0) : : "memory");
            if (__builtin_amdgcn_ballot_w64(h0 == 0xffffffffu) == 0 || --budget < 0) break;
            phi += (u32)cfg.G;
        }
        asm volatile(".Lkm_lv:");
    }
}

DI void xcopy_task(const Ptrs& p, int j, int w) {
    const int tid = 64 * w + lanex();
    Sync y;
    sync_ld(y, p.ws);
    u32x4 xv[10];
    sfor<0, 10>([&](auto I) { gld16<I.v * 128>(xv[I.v], p.x + (size_t)j * 40960, (u32)((tid & 31) * 1280 + (tid >> 5) * 16)); });
    sfor<0, 10>([&](auto I) { waitv<9 - I.v>(xv[I.v]); });
    asm volatile("" : "+v"(y.r), "+v"(y.g), "+v"(y.e));
    sfor<0, 10>([&](auto I) { gst16(p.ws + OFF_XF + (size_t)j * 40960 + I.v * 4096, (u32)tid * 16, xv[I.v]); });
    waitvm<0>();
    barrier();
    const u64 t = sync_tag(y.e);
    if (tid == 0) gst8(p.ws + OFF_LAT, 1536 + (u32)j * 8, u32x2{(u32)t, (u32)(t >> 32)});
}
DI void xcopy_wait(const char* ws, u64 t) {
#pragma nounroll
    for (int it = 0; it < (1 << 16); ++it) {
        u32x2 r;
        asm volatile("s_nop 4\n\tglobal_load_dwordx2 %0, %1, %2 offset:1536 sc1\n\ts_waitcnt vmcnt(0)" : "=&v"(r) : "v"(lanex() * 8), "s"(ws + OFF_LAT) : "memory");
        if (__builtin_amdgcn_ballot_w64(r[0] != (u32)t || r[1] != (u32)(t >> 32)) == 0) break;
        __builtin_amdgcn_s_sleep(16);
    }
}
DI void init_task(const Ptrs& p, int j, u32 lds, int w) {
    const int lane = lanex(), tid = 64 * w + lane;
    Sync y;
    sync_ld(y, p.ws);
    const u32x4 ff = u32x4{~0u, ~0u, ~0u, ~0u};
    const int i = j * 256 + tid, r = j < 8 ? j & 1 : min(j / 2 - 2, NREG - 1);
    char* const vb = p.ws + (r < 2 ? 0 : OFF_U - (4u << 20)) + ((size_t)r << 21) + 1984 * 1024 + (j < 8 ? j & 6 : (j & 1) * 2 + (j >> 1 == 15) * 4) * 8192;
    const u32 vo = (u32)((w >> 1) * 8192 + (w & 1) * 1024);
    gst16(vb, vo + lane * 16, ff);
    if (i < F_N * 8) gst16(p.ws + OFF_ST, (u32)i * 16, u32x4{i == F_ONE * 8 ? 1000u : ~0u, ~0u, ~0u, ~0u});
    gst16(p.ws + 65536, (u32)i * 16, ff);
    if (i < 4096) {
        const int k = (i & 511) + ((i >> 1) & 0x600);
        u32x4 m;
        gld16<0>(m, p.mem0, (u32)((2 * ((k >> 2) & 3) + (k & 1)) * 1280 + min(2 * (k >> 4) + ((k >> 1) & 1), 79) * 16));
        waitv<0>(m);
        gst16(p.ws, (u32)i * 16, k < 640 ? u32x4{min(m[0], 0xfffffffeu), min(m[1], 0xfffffffeu), min(m[2], 0xfffffffeu), min(m[3], 0xfffffffeu)} : ff);
    }
#pragma unroll
    for (int c = 0; c < 6; ++c) gst16(p.ws + OFF_U + c * 131072 - (c == 5) * 122880, (u32)i * 16, ff);
    waitvm<0>();
    asm volatile("" : "+v"(y.r), "+v"(y.g), "+v"(y.e));
    barrier();
    if (w) return;
    u32 dt[2] = {0xffffffu, 0xffffffu};
#pragma unroll
    for (int c = 0; c < 2 * NSAMP; ++c) {
        u32 v;
        const u32 t0 = mt();
        asm volatile("global_load_dword %0, %1, %2 sc1\n\ts_waitcnt vmcnt(0)" : "=&v"(v) : "v"((c & 1) * 8192 + (c >> 1) * 256), "s"(vb) : "memory");
        dt[c & 1] = min(dt[c & 1], mt() - t0);
    }
    const u64 t = sync_tag(y.e);
    const u32 vote = (u32)((dt[0] > dt[1]) ^ xhalf() ^ (int)((vb - p.ws) >> 21)) & 1u;
    if (tid == 0) {
        if (j == 0) gst8(p.ws + OFF_LAT, 512, u32x2{(u32)t, rt()});
        gst8(p.ws + OFF_LAT, (u32)j * 8, u32x2{(u32)t | vote, (u32)(t >> 32)});
    }
}

DI float xmax(float v) {
    const auto a = __builtin_amdgcn_permlane32_swap(__float_as_uint(v), __float_as_uint(v), false, false);
    v = fmaxf(__uint_as_float(a[0]), __uint_as_float(a[1]));
    const auto b = __builtin_amdgcn_permlane16_swap(__float_as_uint(v), __float_as_uint(v), false, false);
    return fmaxf(__uint_as_float(b[0]), __uint_as_float(b[1]));
}
DI float xsum(float v) {
    const auto a = __builtin_amdgcn_permlane32_swap(__float_as_uint(v), __float_as_uint(v), false, false);
    v = __uint_as_float(a[0]) + __uint_as_float(a[1]);
    const auto b = __builtin_amdgcn_permlane16_swap(__float_as_uint(v), __float_as_uint(v), false, false);
    return __uint_as_float(b[0]) + __uint_as_float(b[1]);
}
DI void gst16c(char* sbase, u32 voff, u32x4 v) {
    asm volatile("global_store_dwordx4 %0, %1, %2 sc1\n\ts_nop 1" : : "v"(voff), "v"(v), "s"(sbase) : "memory");
}
DI void gst8c(char* sbase, u32 voff, u32x2 v) {
    asm volatile("global_store_dwordx2 %0, %1, %2 sc1\n\ts_nop 1" : : "v"(voff), "v"(v), "s"(sbase) : "memory");
}
#define WAIT6(N, r, o) asm volatile("s_waitcnt vmcnt(" #N ")" : "+v"(r[o]), "+v"(r[o + 1]), "+v"(r[o + 2]), "+v"(r[o + 3]), "+v"(r[o + 4]), "+v"(r[o + 5]) : : "memory")
#define WSLEEP 90
DI void wg_wait(const char* fl, int w, int i0, int t0, int i1, int t1, int i2, int t2, int i3, int t3, int dt, bool acq) {
    if (w == 0) {
        const int lane = lanex(), tg = lane == 1 ? t1 : (lane == 2 ? t2 : (lane == 3 ? t3 : t0));
        const int ix = tg <= 0 ? i0 : (lane == 1 ? i1 : (lane == 2 ? i2 : (lane == 3 ? i3 : i0)));
#pragma nounroll
        for (int it = 0; it < (1 << 14); ++it) {
            u32 v;
            flag_ld(v, fl, ix);
            asm volatile("s_waitcnt vmcnt(0)" : "+v"(v) : : "memory");
            if (__builtin_amdgcn_ballot_w64((int)(v + 1u) < tg) == 0) break;
            const u32 wake = rt() + (u32)(dt + 180 * min(max(t0 - 2 - uni((int)v), 0), 64));
            while ((int)(rt() - wake) < 0) __builtin_amdgcn_s_sleep(4);
        }
        if (acq) acquire();
    }
    barrier();
}
#define TEST 172
DI void until(u32 t) { while ((int)(rt() - t) < 0) __builtin_amdgcn_s_sleep(1); }
DI u32 step_wait(const char* fl, int w, int gr, int t, u32 lds) {
    if (w == 0) {
        u32x2 v;
#pragma nounroll
        for (int it = 0; it < (1 << 14); ++it) {
            asm volatile("s_nop 4\n\tglobal_load_dwordx2 %0, %1, %2 sc1\n\ts_waitcnt vmcnt(0)" : "=&v"(v) : "v"((F_CUR + gr) * 128), "s"(fl) : "memory");
            const int d = t - 1 - uni((int)v[0]);
            if (d <= 0) { v[1] += (u32)(d * TEST); break; }
            const u32 now = rt();
            nap(min(max((int)((u32)uni((int)v[1]) - now) + d * TEST + 10, 40), 2000));
        }
        *LDSP(u32, lds + GRING * GBUF + 4) = v[1];
    }
    barrier();
    return (u32)uni((int)*LDSP(u32, lds + GRING * GBUF + 4));
}
#define PDL 135
#define ODL 280
#define TDT 35
template <bool LATE>
DI void px_wave(const Ptrs& p, const ChainCfg& cfg, int s, int h, int slow, int w, u32 lds, bool keep) {
    const int lane = lanex(), n = lane & 15, g = lane >> 4;
    const char* qb = p.ws + OFF_QX + (size_t)(s * NH + h) * 12288;
    const char* kb = p.ws + OFF_KX + (size_t)(s * NH + h) * 12288;
    const char* mb = p.ws + OFF_KM + (size_t)(s * NH + h) * 3072;
    const u32 ko = (u32)((n >> 3) * 6144 + (8 * ((n >> 2) & 1) + (n & 3) + 16 * g) * 16);
    const u32 mo = (u32)(g * 128 + (n & 7) * 16);
    u32x4 qf[12], kf[12], mf[6];
    sfor<0, 12>([&](auto I) { gld16c<(I.v % 3) * 1024>(qf[I.v], qb + (I.v / 3) * 3072, lane * 16); gap<SMP>(slow); });
    if (!SMP) pace(slow);
    sfor<0, 12>([&](auto I) { gld16c<(I.v % 3) * 1024 + (I.v / 6) * 64>(kf[I.v], kb + ((I.v / 3) & 1) * 3072, ko); gap<SMP>(slow); });
    if (!SMP) pace(slow);
    if constexpr (LATE) { WAIT6(0, qf, 0); WAIT6(0, qf, 6); WAIT6(0, kf, 0); }
    else {
        sfor<0, 6>([&](auto I) { gld16c<I.v * 512>(mf[I.v], mb, mo); });
        WAIT6(24, qf, 0); WAIT6(18, qf, 6); WAIT6(12, kf, 0);
    }
    f32x4 sc[2][3];
#pragma unroll
    for (int tt = 0; tt < 2; ++tt)
#pragma unroll
        for (int T = 0; T < 3; ++T) sc[tt][T] = f32x4{0.f, 0.f, 0.f, 0.f};
#pragma unroll
    for (int ks = 0; ks < 6; ++ks) { sc[0][0] = mfma16(kf[ks], qf[ks], sc[0][0]); sc[1][0] = mfma16(kf[ks], qf[6 + ks], sc[1][0]); }
    WAIT6(6, kf, 6);
#pragma unroll
    for (int ks = 0; ks < 6; ++ks) { sc[0][1] = mfma16(kf[6 + ks], qf[ks], sc[0][1]); sc[1][1] = mfma16(kf[6 + ks], qf[6 + ks], sc[1][1]); }
    if constexpr (LATE) {
        until(step_wait(p.ws + OFF_ST, w, h & 7, s - 1, lds) + (u32)(s == NSEG - 1 ? cfg.T : cfg.T + PDX));
        bool ok = false;
#pragma nounroll
        for (int it = 0; it < (1 << 14); ++it) {
            u32 v;
            sfor<0, 6>([&](auto I) { gld16c<I.v * 512>(mf[I.v], mb, mo); });
            flag_ldm(v, p.ws + OFF_ST, (s < NSEG - 2 ? F_CUR : F_PROG + 8 * s) + (h & 7), it > 3);
            WAIT6(0, mf, 0);
            asm volatile("" : "+v"(v));
            u32 m = 0;
#pragma unroll
            for (int i = 0; i < 24; ++i) m = max(m, mf[i >> 2][i & 3]);
            if (ok || __builtin_amdgcn_ballot_w64(m == ~0u) == 0) break;
            ok = it > 3 && uni((int)v) >= (s < NSEG - 2 ? s : 7);
            if (s < NSEG - 1) nap(60);
        }
    }
    WAIT6(0, mf, 0);
#pragma unroll
    for (int ks = 0; ks < 6; ++ks) { sc[0][2] = mfma16(mf[ks], qf[ks], sc[0][2]); sc[1][2] = mfma16(mf[ks], qf[6 + ks], sc[1][2]); }
    const float kscale = 0.07216878364870322f;
    char* pb = p.ws + OFF_PX + (size_t)s * 81920;
#pragma unroll
    for (int tt = 0; tt < 2; ++tt) {
        float v[12], mx = -3.0e38f;
#pragma unroll
        for (int i = 0; i < 12; ++i) {
            const float t = rbf(rbf(sc[tt][i >> 2][i & 3]) * kscale);
            v[i] = (i >= 8 || 8 * g + i <= 16 * tt + n) ? t : -3.0e38f;
            mx = fmaxf(mx, v[i]);
        }
        mx = xmax(mx);
        float sx = 0.f, sm = 0.f;
#pragma unroll
        for (int i = 0; i < 12; ++i) {
            v[i] = __builtin_amdgcn_exp2f((v[i] - mx) * 1.4426950408889634f);
            if (i < 8) sx += v[i]; else sm += v[i];
        }
        const float inv = __builtin_amdgcn_rcpf(xsum(sx + 0.5f * sm));
        const u32x4 px = u32x4{cvtpk(v[0] * inv, v[1] * inv), cvtpk(v[2] * inv, v[3] * inv), cvtpk(v[4] * inv, v[5] * inv), cvtpk(v[6] * inv, v[7] * inv)};
        const u32x2 pm = u32x2{cvtpk(v[9] * inv, v[8] * inv), cvtpk(v[11] * inv, v[10] * inv)};
        const int ox = h * 2048 + tt * 1024, om = 65536 + (h >> 2) * 2048 + (h & 3) * 256 + tt * 1024;
        if (!LATE && keep) {
            *LDSP(u32x4, lds + ox + lane * 16) = px;
            *LDSP(u32x2, lds + om + n * 16 + (g & 1) * 8) = pm;
        } else {
            gst16c(pb + ox, lane * 16, px);
            gst8c(pb + om, (u32)(n * 16 + (g & 1) * 8), pm);
        }
    }
}

#define WAIT10(N, r) asm volatile("s_waitcnt vmcnt(" #N ")" : "+v"(r[0]), "+v"(r[1]), "+v"(r[2]), "+v"(r[3]), "+v"(r[4]), "+v"(r[5]), "+v"(r[6]), "+v"(r[7]), "+v"(r[8]), "+v"(r[9]) : : "memory")
DI void out_task(const Ptrs& p, int s, int cg0, int cg1, u32 lds, int slow, int w, int sd) {
    u32x4 pf[2][10];
    if (cg1 - cg0 > 1) {
        const u32 la = lds + lanex() * 16;
#pragma unroll
        for (int i = 0; i < 20; ++i) pf[i & 1][i >> 1] = ldsr16(la + (i < 16 ? w * 16384 + i * 1024 : 65536 + w * 4096 + (i - 16) * 1024));
    } else {
        const char* pb = p.ws + OFF_PX + (size_t)s * 81920;
        const u32 l16 = lanex() * 16;
        sfor<0, 20>([&](auto I) { gld16c<(I.v & 3) * 1024>(pf[I.v & 1][I.v >> 1], pb + (I.v < 16 ? w * 16384 + (I.v >> 2) * 4096 : 65536 + w * 4096), l16); gap<SMO>(slow); if (!SMO && I.v == 9) pace(slow); });
        WAIT10(0, pf[0]); WAIT10(0, pf[1]);
    }
#pragma nounroll
    for (int cg = cg0; cg < cg1; ++cg) {
    const int lane = lanex(), tid = 64 * w + lane, n = lane & 15, g = lane >> 4;
    const char* db = p.ws + OFF_D + s * D_STRIDE + (size_t)(16 * cg) * 16384 + w * 4096;
    const char* ub = p.ws + uslot(sd, s) + (size_t)(16 * cg) * 4096 + w * 1024;
    const u32 dof = (u32)((n >> 3) * 16384 + g * 128 + (n & 7) * 16), uof = (u32)((n >> 3) * 4096 + g * 128 + (n & 7) * 16);
    u32x4 af[3][10];
    auto aload = [&](u32x4 (&r)[10], int ct) {
        if (!SMO) pace(slow);
        sfor<0, 8>([&](auto I) { gld16c<I.v * 512>(r[I.v], db + ct * 32768, dof); gap<SMO>(slow); });
        gld16c<0>(r[8], ub + ct * 8192, uof); gap<SMO>(slow); gld16c<512>(r[9], ub + ct * 8192, uof); gap<SMO>(slow);
    };
    aload(af[0], 0);
    aload(af[1], 1);
    aload(af[2], 2);
    const int tok = 32 * s + (tid >> 3), col = 128 * cg + 8 * (tid & 7);
    u32x4 bb[2]; u32 gv;
    gld16c<0>(bb[0], p.ws + OFF_BOUT, (u32)(s * 40960 + col * 64 + (tid >> 3) * 16)); gld16c<0>(bb[1], p.ws + OFF_BOUT + 4096, (u32)(s * 40960 + col * 64 + (tid >> 3) * 16));
    asm volatile("global_load_dword %0, %1, %2 sc1" : "=v"(gv) : "v"(tok * 4), "s"(p.ws + OFF_GATE) : "memory");
    barrier();
    const u32 wa = lds + (u32)w * 16896 + n * 528 + g * 16;
    sfor<0, 8>([&](auto I) {
        constexpr int ct = I.v;
        u32x4 (&r)[10] = af[ct % 3];
        if constexpr (ct == 0) WAIT10(23, r);
        else if constexpr (ct < 3) WAIT10(23, r);
        else if constexpr (ct < 6) WAIT10(20, r);
        else if constexpr (ct == 6) WAIT10(10, r);
        else WAIT10(0, r);
        f32x4 c0 = f32x4{0.f, 0.f, 0.f, 0.f}, c1 = c0;
#pragma unroll
        for (int i = 0; i < 10; ++i) { c0 = mfma16(r[i], pf[0][i], c0); c1 = mfma16(r[i], pf[1][i], c1); }
        *LDSP(f32x4, wa + ct * 64) = c0;
        *LDSP(f32x4, wa + ct * 64 + 16 * 528) = c1;
        PIN();
        if constexpr (ct + 3 < 8) aload(r, ct + 3);
        PIN();
    });
    asm volatile("" : "+v"(bb[0]), "+v"(bb[1]), "+v"(gv));
    barrier();
    const u32 ra = lds + (u32)(tid >> 3) * 528 + (tid & 7) * 32;
    const float gt = __uint_as_float(gv), g1 = rbf(1.f - gt);
    u32 ov[8];
#pragma unroll
    for (int i = 0; i < 4; ++i) {
        const u32 ri = ra + 16 * (i & 1) + 256 * (i >> 1);
        const f32x4 z = (*LDSP(f32x4, ri) + *LDSP(f32x4, ri + 16896)) + (*LDSP(f32x4, ri + 2 * 16896) + *LDSP(f32x4, ri + 3 * 16896));
#pragma unroll
        for (int j = 0; j < 2; ++j) {
            const u32 b = bb[i >> 1][2 * (i & 1) + j];
            const u32 a = cvtpk(z[2 * j], z[2 * j + 1]);
            const u32 ga = cvtpk(gt * __uint_as_float(a << 16), gt * __uint_as_float(a & 0xffff0000u));
            const u32 gb = cvtpk(g1 * __uint_as_float(b << 16), g1 * __uint_as_float(b & 0xffff0000u));
            ov[2 * i + j] = cvtpk(__uint_as_float(ga << 16) + __uint_as_float(gb << 16), __uint_as_float(ga & 0xffff0000u) + __uint_as_float(gb & 0xffff0000u));
        }
    }
    gst16p(p.out, (u32)(tok * 1280 + col * 2), u32x4{ov[0], ov[1], ov[2], ov[3]});
    gst16p(p.out, (u32)(tok * 1280 + col * 2 + 128), u32x4{ov[4], ov[5], ov[6], ov[7]});
    }
}

DI void out_tail(const Ptrs& p, const ChainCfg& cfg, int s, int cg, u32 lds, int w, int sd, int gr) {
    const int lane = lanex(), tid = 64 * w + lane, n = lane & 15, g = lane >> 4;
    const char* db = p.ws + OFF_D + s * D_STRIDE + (size_t)(4 * cg) * 16384 + w * 4096;
    const char* ub = p.ws + uslot(sd, s) + (size_t)(4 * cg) * 4096 + w * 1024;
    const char* pb = p.ws + OFF_PX + (size_t)s * 81920;
    const u32 dof = (u32)((n >> 3) * 16384 + g * 128 + (n & 7) * 16), uof = (u32)((n >> 3) * 4096 + g * 128 + (n & 7) * 16);
    u32x4 pf[2][10], af[2][10];
    sfor<0, 16>([&](auto I) { gld16c<(I.v & 7) * 512>(af[I.v >> 3][I.v & 7], db + (I.v >> 3) * 32768, dof); });
    const int tok = 32 * s + (tid >> 3), col = 32 * cg + 4 * (tid & 7);
    u32x2 bb; u32 gv;
    asm volatile("global_load_dwordx2 %0, %2, %3 sc1\n\tglobal_load_dword %1, %4, %5 sc1\n\ts_waitcnt vmcnt(0)" : "=&v"(bb), "=&v"(gv)
                 : "v"(s * 40960 + (col >> 3) * 512 + (tid >> 3) * 16 + (col & 7) * 2), "s"(p.ws + OFF_BOUT), "v"(tok * 4), "s"(p.ws + OFF_GATE) : "memory");
    sfor<0, 16>([&](auto I) { waitv<0>(af[I.v >> 3][I.v & 7]); });
    until(step_wait(p.ws + OFF_ST, w, gr, s - 1, lds) + (u32)(s == NSEG - 1 ? cfg.cR : cfg.cR + 2 * PDX));
    const u32 wa = lds + (u32)w * 4608 + n * 144 + g * 16;
#pragma nounroll
    for (int it = 0; it < (1 << 14); ++it) {
        if (it == 4) {
            flag_wait(p.ws + OFF_ST, F_PX + s, 8);
            flag_wait(p.ws + OFF_ST, s < NSEG - 2 ? F_CUR + gr : F_UD + 2 * (s & 1), s < NSEG - 2 ? s + 1 : 16);
            flag_wait(p.ws + OFF_ST, s < NSEG - 2 ? F_ONE : F_UD + 2 * (s & 1) + 1, 16);
        }
        sfor<0, 4>([&](auto I) { gld16c<(I.v & 1) * 512>(af[I.v >> 1][8 + (I.v & 1)], ub + (I.v >> 1) * 8192, uof); });
        sfor<0, 8>([&](auto I) { gld16c<(I.v & 1) * 2048>(pf[0][I.v], pb + w * 16384 + (I.v >> 1) * 4096, lane * 16); });
        gld16c<0>(pf[0][8], pb + 65536 + w * 4096, lane * 16); gld16c<2048>(pf[0][9], pb + 65536 + w * 4096, lane * 16);
        sfor<0, 8>([&](auto I) { gld16c<(I.v & 1) * 2048 + 1024>(pf[1][I.v], pb + w * 16384 + (I.v >> 1) * 4096, lane * 16); });
        gld16c<1024>(pf[1][8], pb + 65536 + w * 4096, lane * 16); gld16c<3072>(pf[1][9], pb + 65536 + w * 4096, lane * 16);
        WAIT10(10, pf[0]); WAIT10(10, af[0]); WAIT10(10, af[1]);
        float chk = 0.f;
#pragma unroll
        for (int tt = 0; tt < 2; ++tt) {
            if (tt) WAIT10(0, pf[1]);
            f32x4 c0 = f32x4{0.f, 0.f, 0.f, 0.f}, c1 = c0;
#pragma unroll
            for (int i = 0; i < 10; ++i) { c0 = mfma16(af[0][i], pf[tt][i], c0); c1 = mfma16(af[1][i], pf[tt][i], c1); }
            *LDSP(f32x4, wa + tt * 2304) = c0;
            *LDSP(f32x4, wa + tt * 2304 + 64) = c1;
            const f32x4 c = c0 + c1;
            chk += (c[0] + c[1]) + (c[2] + c[3]);
            PIN();
        }
        if (it >= 4 || __builtin_amdgcn_ballot_w64((__float_as_uint(chk) & 0x7fffffffu) > 0x7f800000u) == 0) break;
        if (s < NSEG - 1) nap(60);
    }
    barrier();
    const u32 ra = lds + (u32)(tid >> 3) * 144 + (tid & 7) * 16;
    const float gt = __uint_as_float(gv), g1 = rbf(1.f - gt);
    const f32x4 z = (*LDSP(f32x4, ra) + *LDSP(f32x4, ra + 4608)) + (*LDSP(f32x4, ra + 2 * 4608) + *LDSP(f32x4, ra + 3 * 4608));
    u32x2 ov;
#pragma unroll
    for (int j = 0; j < 2; ++j) {
        const u32 b = bb[j], a = cvtpk(z[2 * j], z[2 * j + 1]);
        const u32 ga = cvtpk(gt * __uint_as_float(a << 16), gt * __uint_as_float(a & 0xffff0000u));
        const u32 gb = cvtpk(g1 * __uint_as_float(b << 16), g1 * __uint_as_float(b & 0xffff0000u));
        ov[j] = cvtpk(__uint_as_float(ga << 16) + __uint_as_float(gb << 16), __uint_as_float(ga & 0xffff0000u) + __uint_as_float(gb & 0xffff0000u));
    }
    *(u32x2*)(p.out + tok * 1280 + col * 2) = ov;
}

DI float dot2bf(u32 a, u32 b, float c) { return __builtin_amdgcn_fdot2_f32_bf16(__builtin_bit_cast(bf16x2, a), __builtin_bit_cast(bf16x2, b), c, false); }
DI void battn_task(const Ptrs& p, int t0, u32 lds, int w) {
    const int lane = lanex(), tid = 64 * w + lane, tl = tid & 31, t = t0 + tl;
    const char* const qkv = p.ws + OFF_QKVB + (size_t)(t0 >> 5) * 122880;
    {
        u32 so[3];
#pragma unroll
        for (int i = 0; i < 3; ++i) {
            const int e = 64 * i + lane, sl = e % 48;
            so[i] = (u32)((e / 48) * 512 + (sl < 16 ? (t0 ? 0 : 122880) + 256 + sl * 16 : 122880 - 256 + sl * 16));
        }
#pragma unroll
        for (int i = 0; i < 30; ++i) { glds16c(qkv - 122880 + 40960 + (10 * w + i / 3) * 2048, so[i % 3], lds + (30 * w + i) * 1024); gap<SMB>(-1); }
    }
    u32x4 q[2][5], xv[10], gv[10];
    sfor<0, 10>([&](auto I) { gld16c<(I.v % 5) * 512>(q[I.v / 5][I.v % 5], qkv + (I.v / 5) * 20480, (u32)((tid >> 5) * 2560 + tl * 16)); gap<SMB>(-1); });
    sfor<0, 10>([&](auto I) { gld16<I.v * 128>(xv[I.v], p.x + (size_t)t0 * 1280, (u32)((tid >> 3) * 1280 + (tid & 7) * 16)); gld16<I.v * 128>(gv[I.v], p.gw, (u32)((tid & 7) * 16)); gap<SMB>(-1); });
    sfor<0, 10>([&](auto I) { waitv<0>(q[I.v / 5][I.v % 5]); waitv<0>(xv[I.v]); waitv<0>(gv[I.v]); });
    barrier();
    {
        float a = 0.f;
#pragma unroll
        for (int i = 0; i < 10; ++i)
#pragma unroll
            for (int j = 0; j < 4; ++j) {
                const u32 r = cvtpk(__uint_as_float(xv[i][j] << 16) * __uint_as_float(gv[i][j] << 16), __uint_as_float(xv[i][j] & 0xffff0000u) * __uint_as_float(gv[i][j] & 0xffff0000u));
                a += __uint_as_float(r << 16) + __uint_as_float(r & 0xffff0000u);
            }
        a += __shfl_xor(a, 1); a += __shfl_xor(a, 2); a += __shfl_xor(a, 4);
        const float z = rbf(a);
        if ((tid & 7) == 0) gst4c(p.ws + OFF_GATE, (u32)((t0 + (tid >> 3)) * 4), __float_as_uint(rbf(1.0f / (1.0f + __expf(-z)))));
    }
#pragma unroll
    for (int it = 0; it < 2; ++it) {
        const int m = (tid >> 5) + 8 * it;
        u32 ka = lds + (u32)(m * 3840 + (2 + tl) * 16), va = ka + 61440;
        asm volatile("" : "+v"(ka), "+v"(va));
        u32x4 k[5][8];
#pragma unroll
        for (int i = 0; i < 40; ++i) k[i >> 3][i & 7] = ldsr16(ka + (i >> 3) * 768 + (7 - (i & 7)) * 32);
        PIN();
        float lg[2][8], mx[2] = {-3.0e38f, -3.0e38f};
#pragma unroll
        for (int i = 0; i < 16; ++i) lg[i >> 3][i & 7] = 0.f;
#pragma unroll
        for (int i = 0; i < 5; ++i) {
#pragma unroll
            for (int j = 0; j < 4; ++j)
#pragma unroll
                for (int wv = 0; wv < 8; ++wv) lg[4 * i + j >= 10][wv] = dot2bf(q[it][i][j], k[i][wv][j], lg[4 * i + j >= 10][wv]);
            PIN();
#pragma unroll
            for (int wv = 0; wv < 8; ++wv) k[i][wv] = ldsr16(va + i * 768 + (7 - wv) * 32);
            PIN();
        }
#pragma unroll
        for (int i = 0; i < 16; ++i) {
            float& l = lg[i >> 3][i & 7];
            l = rbf(rbf(l) * 0.22360679774997896f);
            asm volatile("" : "+v"(l));
            l = t - 2 * (i & 7) >= 0 ? l : -3.0e38f;
            mx[i >> 3] = fmaxf(mx[i >> 3], l);
        }
        u32 pk[2][4];
#pragma unroll
        for (int u = 0; u < 2; ++u) {
            float sum = 0.f;
#pragma unroll
            for (int wv = 0; wv < 8; ++wv) { lg[u][wv] = __builtin_amdgcn_exp2f((lg[u][wv] - mx[u]) * 1.4426950408889634f); sum += lg[u][wv]; }
            const float inv = __builtin_amdgcn_rcpf(sum);
#pragma unroll
            for (int wv = 0; wv < 4; ++wv) pk[u][wv] = cvtpk(lg[u][2 * wv] * inv, lg[u][2 * wv + 1] * inv);
        }
#pragma unroll
        for (int i = 0; i < 5; ++i) {
            float o[8];
#pragma unroll
            for (int d = 0; d < 8; ++d) o[d] = 0.f;
#pragma unroll
            for (int wp = 0; wp < 4; ++wp) {
                const u32x4 v0 = k[i][2 * wp], v1 = k[i][2 * wp + 1];
#pragma unroll
                for (int j = 0; j < 4; ++j) {
                    const u32 pw = pk[4 * i + j >= 10][wp];
                    o[2 * j] = dot2bf(__builtin_amdgcn_perm(v1[j], v0[j], 0x05040100u), pw, o[2 * j]);
                    o[2 * j + 1] = dot2bf(__builtin_amdgcn_perm(v1[j], v0[j], 0x07060302u), pw, o[2 * j + 1]);
                }
            }
            gst16c(p.ws + OFF_OB + (size_t)(t0 >> 5) * 40960 + 512 * i, (u32)(m * 2560 + tl * 16), u32x4{cvtpk(o[0], o[1]), cvtpk(o[2], o[3]), cvtpk(o[4], o[5]), cvtpk(o[6], o[7])});
        }
    }
}

#define XPF 1
#define PFQ 0
#define PFN 8
#define OLAG 3
constexpr int QN = 176, QF = 19;
struct QTab { u16 e[QN]; int n, nf; u32 tt[64]; };
typedef const __attribute__((address_space(4))) int* KI;
constexpr int QT_OFF = (88 + 48) / 4;
DI int pick_task(char* fl, KI k, int gr) {
    u32 r;
    asm volatile("s_mov_b64 exec, 1\n\ts_nop 3\n\tglobal_atomic_add %0, %1, %2, %3 sc0 sc1\n\ts_mov_b64 exec, -1\n\ts_waitcnt vmcnt(0)"
                 : "=&v"(r) : "v"((F_QH + gr) * 128), "v"(1), "s"(fl) : "memory");
    const int x = uni((int)r) + 1 + QF;
    if (x >= k[QT_OFF + QN / 2]) return -1;
    const u32 e2 = (u32)k[QT_OFF + (x >> 1)];
    return (int)(x & 1 ? e2 >> 16 : e2 & 0xffffu);
}
__global__ __launch_bounds__(256, 1) void k_main(Ptrs p0, ChainCfg cfg, QTab tab) {
    u64 tk8; asm volatile("s_memrealtime %0\n\ts_waitcnt lgkmcnt(0)" : "=s"(tk8) : : "memory");
    extern __shared__ __attribute__((aligned(16))) char smem[];
    u32 lds = (u32)(size_t)LDSP(char, smem);
#define HALF 0
    int n = HALF ? (blockIdx.x & 4) * 32 + (int)(blockIdx.x >> 3) * 4 + (blockIdx.x & 3) : blockIdx.x, gr = n & 7;
    int w = uni(threadIdx.x >> 6);
    {
        u64 pc; int len;
        asm volatile("s_getpc_b64 %0\n.Lkm_first:\n\ts_mov_b32 %1, .Lkm_last-.Lkm_first\n\ts_max_i32 %1, %1, .Lkm_lu-.Lkm_first\n\ts_max_i32 %1, %1, .Lkm_lv-.Lkm_first\n\t"
                     "s_max_i32 %1, %1, .Lkm_g2-.Lkm_first\n\ts_max_i32 %1, %1, .Lkm_g4-.Lkm_first" : "=s"(pc), "=s"(len) : : "scc");
        if (w == 3) pfl((const char*)pc, (u32)min((int)(blockIdx.x >> 3) * 8192 + (int)(threadIdx.x & 63) * 128, max(len, 0)), lds);
    }
    karg_warm();
    if (n < NH) { score_role(p0, cfg, n, lds, w, (u32)tk8); return; }
    if (n < NS) { proj_role(p0, cfg, n - NH, lds, w); return; }
    const bool isv = n < NS + NVB;
#define qslot (lds + GRING * GBUF)
    int sdw = -1;
    int en = isv ? Q_VD + (n & 24) : tab.e[(n - NS - NVB) >> 3] & 511;
    u32 phi0 = 0;
    Sync y;
    if (XPF && n < 160 && w >= 2) pfl(p0.x, (u32)min(((n - NS) >> 3) * 128 + (w & 1) * 64 + lanex(), 1279) * 128, lds);
    if (n >= 160) {
        const int lane = lanex();
        if (n >= 192 && (n & 8)) {
            init_task(p0, (n & 7) + ((n - 192) >> 4) * 8, lds, w);
            barrier();
#pragma unroll
            for (int i = 0; i < 3; ++i) pfl(p0.wqkvb, (u32)min((((n - 192) >> 4) * 8 + (n & 7)) * 768 + i * 256 + w * 64 + lane, 19199 + 0) * 128, lds);
            pfl(p0.wob, (u32)min((((n - 192) >> 4) * 8 + (n & 7)) * 256 + w * 64 + lane, 6399) * 128, lds);
        } else {
            const int h = n < 192 ? n & 31 : (n & 7) + ((n - 192) >> 4) * 8;
            xcopy_task(p0, h + (n < 192 ? 0 : 32), w);
#pragma unroll
            for (int i = 0; i < PFN; ++i) {
                if (n >= 192) break;
                const int l = 64 * min(i * 4 + w, 29) + lane;
                pfl(p0.wo + (size_t)h * 245760, (u32)(l * 128), lds);
                if (PFQ) pfl(p0.wq + h * 384, (u32)((l / 3) * 12288 + (l % 3) * 128), lds);
            }
        }
        waitvm<0>();
        if (w == 0) {
            sync_ld(y, p0.ws);
            waitvm<0>();
            *LDSP(u32, qslot) = (u32)sync_end(y, p0.ws, phi0, true);
            xcopy_wait(p0.ws, sync_tag(y.e));
        }
        barrier();
        sdw = uni((int)*LDSP(u32, qslot));
        barrier();
    }
    while (true) {
        typedef char* const __attribute__((address_space(4)))* KP;
        KP kp = (KP)__builtin_amdgcn_kernarg_segment_ptr();
        asm volatile("" : "+s"(kp), "+s"(lds), "+s"(w), "+s"(gr), "+s"(n));
        __builtin_assume(w >= 0 && w < 4 && gr >= 0 && gr < 8);
        const Ptrs p{kp[0], kp[1], kp[2], kp[3], kp[4], kp[5], kp[6], kp[7], kp[8], kp[9], kp[10]};
        const int ty = en & 7, gate = en >> 9;
        const bool fu = ty == Q_PX && (en >> 3 & 63) < 8;
        const int ix = fu ? (en & 504) + gr : en >> 3 & 63, sf = ((KI)kp)[QT_OFF + QN / 2 + 1] & 255;
        char* const fl = p.ws + OFF_ST;
        const bool raw = sdw < 0;
        int t0 = gate - 1, f1 = F_ONE, t1 = 0, f2 = F_ONE, t2 = 0, f3 = F_ONE, t3 = 0, dt = cfg.T2;
        const int os = ix >= 40 ? 44 + (8 * ix + gr) / 20 : (8 * ix + gr) / 5;
        const int bb = ty == Q_BO ? (8 * ix + gr) >> 2 : os;
        if (ty == Q_BA) { f1 = F_B + (gr & RM) | ix << 16; t1 = 5; f2 = F_B + (gr & RM) | max(ix - 1, 0) << 16; t2 = 5; }
        if (ty == Q_BO) { f1 = F_B + (gr & RM) | (8 + bb) << 16; t1 = 8; }
        if (ty == Q_PX) { f1 = F_Q + pblk(ix) * 8 + (fu ? gr & RM : gr) | fu << 16; t1 = fu ? 32 : 4; if (fu) t0 += gr * (((KI)kp)[QT_OFF + QN / 2 + 1] >> 8) >> 3; if (ix < PXT) t0 = max(t0, ix + 1); }
        if (ty == Q_OUT) { f3 = F_B + (gr & RM) | (16 + (os >> 3)) << 16; t3 = 4; if (os < 60) { f1 = F_PX + os; t1 = 8; t0 = max(t0, os + (os < PXT ? OLAG : 1)); if (os >= PXT) dt = cfg.s1; } else { f1 = F_VD + 64 + (gr & RM); t1 = 32; } }
        const bool skip = fu ? ix >= sf : ty == Q_OUT && ix < 40 && (os < sf || os >= 60);
        if ((gate || ty >= Q_BA) && !skip) wg_wait(fl, w, F_CUR + gr, t0, f1, t1, f2, t2, f3, t3, dt, false);
        int sig = F_END, nrep = 1;
        if (skip) {}
        else if (ty <= Q_QKVB || ty == Q_BO) {
            const bool ab = ty <= Q_KQ;
            sig = ty == Q_VD ? F_VD + (ix >> 2) * 8 : (ty == Q_KQ ? F_Q + (ix >> 2) * 8 : (ix & 4 ? F_Q : F_K + 8 * ix)) + gr;
            if (!ab) sig = F_B | (bb + (ty == Q_BO ? 16 : 0)) << 16;
            nrep = ty == Q_VD || !ab ? RM + 1 : 1;
            if (ty == Q_VD && ix < 4) { sig += n & 32 ? 4 : 0; nrep = 4; }
            run_gemm(p, ty, ab ? ix >> 2 : bb, ab ? gr + 8 * (ix & 3) : 8 * ix + gr - (ty == Q_BO ? 4 : 5) * bb, (n >> 5) & 1, raw, y, lds, w);
            if (raw) sdw = sync_end(y, p.ws, phi0, w == 0);
        } else {
            if (ty == Q_BA) {
                battn_task(p, (8 * ix + gr) * 32, lds, w);
                sig = F_B | (8 + ix) << 16; nrep = RM + 1;
            } else if (ty == Q_PX) {
                if (ix < PXT) {
#pragma nounroll
                    for (int i = fu ? 0 : gr; i < (fu ? 8 : gr + 1); ++i) px_wave<false>(p, cfg, ix, i + 8 * w, ix - cfg.fast, w, lds, fu);
                } else px_wave<true>(p, cfg, ix, gr + 8 * w, -1, w, lds, false);
                sig = F_PX + ix;
                if (fu) wg_wait(fl, w, F_ONE, 0, F_B + (gr & RM) | (16 + (ix >> 3)) << 16, 4, F_ONE, 0, F_ONE, 0, dt, false);
            }
            if (ty == Q_OUT || fu) {
                const int cg = 8 * ix + gr - 5 * os;
                if (ix >= 40 && !fu) out_tail(p, cfg, os, 8 * ix + gr - 20 * (os - 44), lds, w, sdw, gr);
                else out_task(p, fu ? ix : os, fu ? 0 : cg, fu ? 5 : cg + 1, lds, (fu ? ix : os) + 1 - cfg.fast, w, sdw);
                sig = F_END;
            }
            waitvm<0>();
        }
        barrier();
        if (w == 0 && sig != F_END) flag_add(fl, sig, nrep);
        if (w == 1 && sig >= F_Q && sig < F_N) flag_add(fl, sig - gr | 1 << 16, RM + 1);
        if (raw && !isv && w == 0) xcopy_wait(p.ws, sync_tag(y.e));
        if (isv) break;
        if (w == 0) *LDSP(int, qslot) = pick_task(fl, (KI)kp, gr);
        barrier();
        en = uni(*LDSP(int, qslot));
        if (en < 0) return;
        barrier();
    }
    value_role(p0, cfg, n - NS, sdw, phi0, lds, w);
}

#define QCFG 14, 11, 3, 5, 0, 50, 36
#define LVDEF "00>==>>>;;<<<<==;988889::::::::::::::;;;;;;;;;;;:988776666668886"
constexpr int DX = 240, DUA = 330, DUB = 40, DUC = 4;
static ChainCfg dev_cfg(ChainCfg c) { c.T = PDL; c.cR = ODL; c.s1 = TDT; c.T2 = WSLEEP; c.cR2 = DX; c.dU = DUA; c.s2 = DUB; c.T3 = DUC; c.cR3 = 4 | 4 << 8 | 0 << 16; return c; }
static QTab set_sched(QTab t, const ChainCfg c, const char* lv) {
    for (int s = 0; s < 64; ++s) {
        const int d = lv[s] - '0';
        t.tt[s] = (u32)((s < c.s1 ? c.T : (s < c.s2 ? c.T2 : c.T3)) + 4 * d) | (u32)((s < c.s1 ? c.cR : (s < c.s2 ? c.cR2 : c.cR3)) + 5 * d / 2) << 16;
    }
    t.tt[0] = (u32)270 | (u32)165 << 16;
    t.tt[1] = (u32)230 | (u32)152 << 16;
    return t;
}
#define QORD "bbqV3bK3m44bm55bm66m77m88"
#define GATES "11 13 15 17 22 24 32 36 19 22 29 42 13 17 21 26 33 41"
static QTab make_tab(int lv, int lk, int b0, int d1, int d2, int lg, int nf) {
    QTab t{};
    int n = 0, dl[QN], nq = 0;
    auto add = [&](int ty, int ix) { t.e[n] = (u16)(ty | ix << 3); dl[n++] = ty == Q_VD || ty == Q_KQ ? 8 * (ix / 4 - 1) - (ty == Q_VD ? lv : lk) - (ix >= 32 ? 0 : 0) : 99; };
    for (int i = 1; i < 4; ++i) add(Q_VD, i);
    for (int i = 0; i < 16; ++i) add(i < 4 ? Q_H : ((i & 8) ? Q_VD : Q_KQ), i < 8 ? i : 4 + (i & 1) * 4 + ((i - 8) >> 1));
    for (int i = 0; i < 4; ++i) add(Q_KQ, 8 + i);
    for (const char* c = QORD; *c; ++c) {
        if (*c == 'b') add(Q_QKVB, nq++);
        else if (*c == 'q') for (int i = 4; i < 8; ++i) add(Q_H, i);
        else if (*c == '.') dl[n - 1] |= 1 << 20;
        else if (*c == 'n') { const int b = *++c - '0'; for (int i = 0; i < 8; ++i) add(i & 1 ? Q_KQ : Q_VD, 4 * b + (i >> 1) - (i & 1) * 2); }
        else if (*c == 'h' || *c == 'e') { const int b = 4 * (c[1] - '0') + (*c == 'e') * 2; ++c; add(Q_KQ, b); add(Q_KQ, b + 1); }
        else if (*c == 'm') { const int b = c[1] - '0', b2 = c[2] - '0'; c += 2; for (int i = 0; i < 8; ++i) add(i & 1 ? Q_KQ : Q_VD, 4 * (i & 1 ? b2 : b) + ((i >> 1) ^ (i & 2 ? 0 : 0))); }
        else {
            const int ty = (*c | 32) == 'v' ? Q_VD : Q_KQ, two = *c >> 5 & 1, b = *++c - '0';
            for (int i = 0; i < (4 << two); ++i) add(ty, 4 * (b + (i & two)) + (i >> two));
        }
    }
    const int gap = 8;
    for (int i = n - 1, m = 999; i >= 0; --i) { const int pz = dl[i] > 999; m = min(8 * (pz ? dl[i] - (1 << 20) : dl[i]), m - gap - 8 * pz); dl[i] = max(m, 0); }
    auto put = [&](int ty, int ix, int g, int pos) {
        int i = n++;
        for (; dl[i - 1] > pos; --i) { t.e[i] = t.e[i - 1]; dl[i] = dl[i - 1]; }
        t.e[i] = (u16)(ty | ix << 3 | (g + 1) << 9); dl[i] = pos;
    };
    for (int i = 0; i < n; ++i) if (dl[i] >= 8) t.e[i] |= (u16)((dl[i] / 8 + 1) << 9);
    const char* gs = GATES;
    auto gt = [&](int i) { return 10 * (gs[3 * i] - '0') + gs[3 * i + 1] - '0'; };
    for (int i = 0; i < 8; ++i) put(Q_BA, i, gt(i), 8 * gt(i));
    for (int i = 0; i < 4; ++i) put(Q_BO, i, gt(8 + i), 8 * gt(8 + i));
    for (int i = 0; 8 * i < nf; ++i) put(Q_PX, i, gt(12 + i), 8 * (max(gt(12 + i), 8 * i + 1) + b0));
    for (int s = nf; s < NSEG; ++s) { const int g = s < PXT ? s + 1 : max(s - 3, lg); put(Q_PX, s, g, 8 * g); }
    for (int x = 5 * nf / 8; x < 38; ++x) { const int os = (8 * x + 4) / 5; put(Q_OUT, x, 0, 8 * (os + (os < PXT ? OLAG : 1))); }
    for (int x = 40; x < 50; ++x) { const int g = max(44 + 8 * x / 20 - 3, lg); put(Q_OUT, x, g, 8 * g + 4); }
    t.n = n; t.nf = nf | d1 << 8;
    return t;
}

extern "C" void kernel_launch(void* const* d_in, const int* in_sizes, int n_in, void* d_out, int out_size, void* d_ws,
                              size_t ws_size, hipStream_t stream) {
    Ptrs p{(const char*)d_in[0], (const char*)d_in[1], (const char*)d_in[2], (const char*)d_in[3], (const char*)d_in[4], (const char*)d_in[5],
           (const char*)d_in[8], (const char*)d_in[9], (const char*)d_in[10], (char*)d_out, (char*)(((size_t)d_ws + 0x1fffff) & ~(size_t)0x1fffff)};
    (void)hipFuncSetAttribute((const void*)k_main, hipFuncAttributeMaxDynamicSharedMemorySize, S_LDS);
    k_main<<<256, 256, S_LDS, stream>>>(p, dev_cfg(ChainCfg{CFG}), set_sched(make_tab(QCFG), ChainCfg{CFG}, LVDEF));
    static_assert(sizeof(Ptrs) + sizeof(ChainCfg) + sizeof(QTab) == TT_OFF + 256, "");
}
```

```cpp
#include <hip/hip_runtime.h>
#pragma clang diagnostic ignored "-Wint-to-pointer-cast"

typedef unsigned short u16;
typedef unsigned int u32;
typedef unsigned long long u64;
typedef __bf16 bf16x8 __attribute__((ext_vector_type(8)));
typedef __bf16 bf16x16 __attribute__((ext_vector_type(16)));
typedef short s16x4 __attribute__((ext_vector_type(4)));
typedef float f32x4 __attribute__((ext_vector_type(4)));
typedef unsigned u32x4 __attribute__((ext_vector_type(4)));
typedef unsigned u32x2 __attribute__((ext_vector_type(2)));
typedef unsigned u32x8 __attribute__((ext_vector_type(8)));
typedef unsigned u32x16 __attribute__((ext_vector_type(16)));
typedef unsigned u32x32 __attribute__((ext_vector_type(32)));
#define DI __device__ __forceinline__
#define LDSP(T, a) ((__attribute__((address_space(3))) T*)(a))

constexpr int S_LEN = 2048, DM = 640, NH = 32, DH = 192, NSEG = 64, HD = NH * DH;
#define NSAMP 4
constexpr int NREG = 13;
constexpr int NV = 80, NVB = 40;
constexpr int NS = 64;
#define CFG 152, 102, 100, 20, 57, 16, 152, 102, 52, 152, 102, 6

constexpr size_t U_STRIDE = NV * 4096, D_STRIDE = NV * 16384;
constexpr size_t HO_STRIDE = 65536;
constexpr size_t OFF_ST = NSEG * HO_STRIDE;
enum { F_ACK = 0, F_ONE = 3, F_VD = 16, F_QH = 96, F_B = 104, F_CUR = 128, F_PROG = 136 - 8 * 62, F_UD = 152, F_PX = 160, F_K = 224, F_END = 512, F_Q = 520, F_N = 592 };
constexpr size_t OFF_LAT = OFF_ST + F_N * 128;
constexpr size_t OFF_KX = 5u << 20;
constexpr size_t OFF_QX = OFF_KX + (size_t)S_LEN * HD * 2;
constexpr size_t OFF_D = OFF_QX + (size_t)S_LEN * HD * 2;
constexpr size_t OFF_U = (OFF_D + NSEG * D_STRIDE + 0x1fffff) & ~(size_t)0x1fffff;
constexpr size_t OFF_KM = OFF_U + (22u << 20);
constexpr size_t OFF_PX = OFF_KM + (size_t)NSEG * NH * 3072;
constexpr size_t OFF_QKVB = OFF_PX + (size_t)NSEG * 81920;
constexpr size_t OFF_OB = OFF_QKVB + (size_t)S_LEN * 1920 * 2;
constexpr size_t OFF_BOUT = OFF_OB + (size_t)S_LEN * DM * 2;
constexpr size_t OFF_GATE = OFF_BOUT + (size_t)S_LEN * DM * 2;
constexpr size_t OFF_XF = OFF_GATE + S_LEN * 4;
constexpr size_t WS_END = OFF_XF + (size_t)S_LEN * DM * 2;
static_assert(WS_END + (2u << 20) <= (256u << 20), "workspace");

typedef __bf16 bf16x2 __attribute__((ext_vector_type(2)));
typedef float f32x2 __attribute__((ext_vector_type(2)));
DI u32 cvtpk(float lo, float hi) { return __builtin_bit_cast(u32, __builtin_convertvector(f32x2{lo, hi}, bf16x2)); }
DI float rbf(float f) { return __uint_as_float(cvtpk(f, f) << 16); }
DI f32x4 mfma16(u32x4 a, u32x4 b, f32x4 c) {
    return __builtin_amdgcn_mfma_f32_16x16x32_bf16(__builtin_bit_cast(bf16x8, a), __builtin_bit_cast(bf16x8, b), c, 0, 0, 0);
}
DI u32x2 trread(u32 a) {
    return __builtin_bit_cast(u32x2, __builtin_amdgcn_ds_read_tr16_b64_v4i16(LDSP(s16x4, a)));
}
DI void glds16c(const char* sbase, u32 voff, u32 lds) {
    u32 sv;
    asm volatile("s_mov_b32 %0, m0\n\ts_mov_b32 m0, %3\n\ts_nop 0\n\tglobal_load_lds_dwordx4 %1, %2 sc1\n\ts_mov_b32 m0, %0"
                 : "=&s"(sv) : "v"(voff), "s"(sbase), "s"(lds) : "memory");
}
template <int IMM>
DI void gld16(u32x4& r, const char* sbase, u32 voff) {
    asm volatile("s_nop 4\n\tglobal_load_dwordx4 %0, %1, %2 offset:%3" : "=v"(r) : "v"(voff), "s"(sbase), "i"(IMM) : "memory");
}
DI void gst16(char* sbase, u32 voff, u32x4 v) {
    asm volatile("global_store_dwordx4 %0, %1, %2 sc1\n\ts_nop 1" : : "v"(voff), "v"(v), "s"(sbase) : "memory");
}
#define OST " nt"
DI void gst16p(char* sbase, u32 voff, u32x4 v) {
    asm volatile("s_nop 4\n\tglobal_store_dwordx4 %0, %1, %2" OST "\n\ts_nop 1" : : "v"(voff), "v"(v), "s"(sbase) : "memory");
}
DI void gst8(char* sbase, u32 voff, u32x2 v) {
    asm volatile("global_store_dwordx2 %0, %1, %2 sc1\n\ts_nop 1" : : "v"(voff), "v"(v), "s"(sbase) : "memory");
}
template <int N>
DI void waitvm() { asm volatile("s_waitcnt vmcnt(%0)" : : "i"(N) : "memory"); }
DI void barrier() { asm volatile("s_waitcnt lgkmcnt(0)\n\ts_barrier" : : : "memory"); }
#define PIN() __builtin_amdgcn_sched_barrier(0)
DI int uni(int v) { return __builtin_amdgcn_readfirstlane(v); }
DI int lanex() {
    int t;
    asm volatile("v_mbcnt_lo_u32_b32 %0, -1, 0\n\tv_mbcnt_hi_u32_b32 %0, -1, %0" : "=v"(t));
    __builtin_assume(t >= 0 && t < 64);
    return t;
}
DI void flag_ld(u32& r, const char* fl, int idx) {
    asm volatile("s_nop 4\n\tglobal_load_dword %0, %1, %2 sc1" : "=v"(r) : "v"((idx & 0xffff) * 128 + (idx >> 14)), "s"(fl) : "memory");
}
DI void flag_ldm(u32& r, const char* fl, int idx, bool on) {
    asm volatile("v_mov_b32 %0, 1000\n\ts_mov_b32 exec_lo, %3\n\ts_mov_b32 exec_hi, 0\n\tglobal_load_dword %0, %1, %2 sc1\n\ts_mov_b64 exec, -1" : "=&v"(r) : "v"(idx * 128), "s"(fl), "s"(uni(on ? 1 : 0)) : "memory");
}
DI void flag_wait(const char* fl, int idx, int target) {
#pragma nounroll
    for (int it = 0; it < (1 << 16); ++it) {
        u32 v;
        flag_ld(v, fl, idx);
        asm volatile("s_waitcnt vmcnt(0)" : "+v"(v) : : "memory");
        if (uni((int)(v + 1u)) >= target) break;
        __builtin_amdgcn_s_sleep(6);
    }
}
DI void flag_add(char* fl, int idx, int nrep) {
    const int lane = lanex();
    if (lane < nrep) asm volatile("s_nop 4\n\tglobal_atomic_add %0, %1, %2 sc1" : : "v"(((idx & 0xffff) + lane) * 128 + (idx >> 14)), "v"(1), "s"(fl) : "memory");
}
DI void acquire() { asm volatile("buffer_inv sc1\n\ts_waitcnt vmcnt(0)" : : : "memory"); }

template <int V> struct IC { static constexpr int v = V; };
template <int B, int E, class Fn>
DI void sfor(Fn&& fn) { if constexpr (B < E) { fn(IC<B>{}); sfor<B + 1, E>(fn); } }
enum { T_QK = 0, T_VD = 1, T_BL = 3 };
constexpr int GBUF = 24576, GRING = 6;
struct GemmArgs {
    const char *w1, *w1b;
    const char* w2;
    const char* x;
    char *out, *outb;
    int ldw;
    char* sig;
    int type, np1, tn;
    int sV, sP, sQ;
    int xb;
};
DI const char* unip(const char* p) {
    const u64 v = (u64)p;
    return (const char*)(((u64)(u32)uni((int)(v >> 32)) << 32) | (u32)uni((int)v));
}
template <int Z>
DI void mf1(f32x4& c, const u32x4& a, const u32x4& b) {
    if constexpr (Z) asm volatile("v_mfma_f32_16x16x32_bf16 %0, %1, %2, 0" : "=&v"(c) : "v"(a), "a"(b));
    else asm volatile("v_mfma_f32_16x16x32_bf16 %0, %1, %2, %0" : "+v"(c) : "v"(a), "a"(b));
}
template <int Z>
DI void mf2(f32x4& c, const u32x4& a, const u32x4& b) {
    if constexpr (Z) asm volatile("v_mfma_f32_16x16x32_bf16 %0, %1, %2, 0" : "=&v"(c) : "v"(a), "v"(b));
    else asm volatile("v_mfma_f32_16x16x32_bf16 %0, %1, %2, %0" : "+v"(c) : "v"(a), "v"(b));
}
template <int IMM>
DI void gld16a(u32x4& r, const char* sbase, u32 voff) {
    asm volatile("global_load_dwordx4 %0, %1, %2 offset:%3 sc1" : "+a"(r) : "v"(voff), "s"(sbase), "i"(IMM) : "memory");
}
DI void setm0(u32 v) { asm volatile("s_mov_b32 m0, %0" : : "s"(v) : "memory"); }
template <int IMM>
DI void gldsm(const char* sbase, u32 voff) {
    asm volatile("global_load_lds_dwordx4 %0, %1 offset:%2" : : "v"(voff), "s"(sbase), "i"(IMM) : "memory");
}
DI void gst16n(char* sbase, u32 voff, u32x4 v) {
    asm volatile("global_store_dwordx4 %0, %1, %2 sc1" : : "v"(voff), "v"(v), "s"(sbase) : "memory");
}
template <int N>
DI void waitbar() { asm volatile("s_waitcnt vmcnt(%0)\n\ts_barrier" : : "i"(N) : "memory"); }
#define LGKM(n) __builtin_amdgcn_s_waitcnt(0xC07F | ((n) << 8))

DI int xhalf() { u32 x; asm volatile("s_getreg_b32 %0, hwreg(HW_REG_XCC_ID)" : "=s"(x)); return (int)((x >> 2) & 1); }
struct Sync { u32x2 r, g, e; };
DI void sync_ld(Sync& y, const char* ws) {
    asm volatile("s_nop 4\n\tglobal_load_dwordx2 %0, %3, %5 sc1\n\tglobal_load_dwordx2 %1, %4, %5 offset:512 sc1\n\tglobal_load_dwordx2 %2, %4, %5 offset:1024 sc1"
                 : "=&v"(y.r), "=&v"(y.g), "=&v"(y.e) : "v"((lanex() & 31) * 8), "v"(0), "s"(ws + OFF_LAT) : "memory");
}
DI u64 sync_tag(u32x2 e) { return (((u64)e[1] << 32 | e[0]) + 1) << 1; }
DI int sync_end(Sync& y, const char* ws, u32& phi, bool ack) {
    asm volatile("" : "+v"(y.r), "+v"(y.g), "+v"(y.e));
#pragma nounroll
    for (int it = 0; it < (1 << 16); ++it) {
        const u64 t = sync_tag(y.e);
        if (__builtin_amdgcn_ballot_w64((y.r[0] & ~1u) != (u32)t || y.r[1] != (u32)(t >> 32) || y.g[0] != (u32)t) == 0) break;
        __builtin_amdgcn_s_sleep(24);
        sync_ld(y, ws);
        asm volatile("s_waitcnt vmcnt(0)" : "+v"(y.r), "+v"(y.g), "+v"(y.e) : : "memory");
    }
    if (ack) flag_add((char*)ws + OFF_ST, F_ACK, 1);
    phi = (u32)uni((int)y.g[1]);
    const u32 b = (u32)__builtin_amdgcn_ballot_w64(y.r[0] & 1), all = __builtin_popcount(b) > 16;
    u32 q = 0;
#pragma unroll
    for (int r = 0; r < NREG; ++r) {
        const u32 c = r < 2 ? __builtin_popcount(b & (0x55u << r)) + all : 2 * __builtin_popcount((b >> (4 + 2 * r)) & 3) + all;
        q |= ((c > 2) ^ (u32)((r < 2 ? r : r + (OFF_U >> 21)) & 1)) << r;
    }
    return uni((int)(q ^ 3u * xhalf()));
}
DI void pace(int on) {
    asm volatile("s_cmp_ge_i32 %0, 0\n\ts_cbranch_scc1 .Lp%=\n\ts_waitcnt vmcnt(0)\n.Lp%=:" : : "s"(on) : "memory", "scc");
}
#define SMO 48
#define RM 7
#define BLS 1
#define EPG 1
#define PXT 58
#define PDX 40
#define SMB 48
#define SMP SMO
template <int C>
DI void gap(int on) {
    if constexpr (C > 0) asm volatile("s_cmp_ge_i32 %0, 0\n\ts_cbranch_scc1 .Lg%=\n\ts_nop %1\n\ts_nop %2\n\ts_nop %3\n.Lg%=:" : : "s"(on), "n"(C / 12 - 1), "n"(C / 12 - 1), "n"(C / 12 - 1) : "memory", "scc");
}
DI void early_add(char* ctr, int c) {
    asm volatile("s_cmp_lg_u32 %1, 1\n\ts_cbranch_scc1 .Le%=\n\ts_mov_b64 exec, 1\n\ts_nop 1\n\tglobal_atomic_add %2, %3, %0 sc1\n\ts_mov_b64 exec, -1\n.Le%=:"
                 : : "s"(ctr), "s"(c), "v"(0), "v"(1) : "memory", "scc");
}
#define XL 2
template <int NT>
DI void gemm_task(const GemmArgs& g, Sync& y, const char* ws, u32 lds, int w) {
    constexpr int FD = 5, NX = 2 * NT, OPS = NX + 6, BI = 24 - FD;
    const int lane = lanex();
    const int n = lane & 15, q = lane >> 4, qq = (lane >> 2) & 3, pp = lane & 3;
    const bool p2 = g.type == T_VD;
    const int rw = lane >> 3, sp = (lane >> 1) & 3;
    const u32 v2b = (u32)(rw * 1280 + 16 * (lane & 1) + 5120);
    auto v2o = [&](int i) { return v2b + (u32)(((sp ^ (rw >> 1)) << 5) + i * 9216); };
    u32 wv[6];
#pragma unroll
    for (int i = 0; i < 6; ++i)
        wv[i] = (u32)((rw + 8 * (i / 3)) * g.ldw + ((sp ^ (((rw >> 1) & 1) + 2 * (i / 3))) << 5) + 16 * (lane & 1) + (i % 3) * 128 + 5120 - 1024 * i);
    const char* const wp2 = g.w2 + (size_t)(48 * w) * 1280 - 2048;
    const char* wp = unip(g.w1 + (size_t)(16 * w) * g.ldw - 2048);
    int wstep = uni(64 * g.ldw);
    u32 wdst = lds + (u32)w * 6144 + 3072;
    const u32 wdlast = wdst + (GRING - 1) * GBUF;
    auto wring = [&]() { wdst = wdst == wdlast ? wdst - (GRING - 1) * GBUF : wdst + GBUF; };
    auto wload = [&](int i) {
        setm0(wdst);
        wring();
        asm volatile("s_nop 0");
        sfor<0, 6>([&](auto I) { gldsm<1024 * (I.v - 3)>(wp, wv[I.v]); });
        wp += wstep;
    };
    u32x4 vf[NT][6];
    u32x4 fr[FD + 1];
    u32x4 xr[5][NX];
    auto xtie = [&](u32x4 (&x)[NX]) {
#pragma unroll
        for (int i = 0; i < NX; i += 4) asm volatile("" : "+a"(x[i]), "+a"(x[i + 1]), "+a"(x[i + 2]), "+a"(x[i + 3]));
    };
#pragma unroll
    for (int i = 0; i < 5 * NX; ++i) asm volatile("" : "=a"(xr[i / NX][i % NX]));
    u32x2 flo;
    u32 c[4], slot = 0;
    int sd = GBUF;
    auto sring = [&]() { sd = slot == (GRING - 1) * GBUF ? -(GRING - 1) * GBUF : GBUF; slot += (u32)sd; };
    {
        u32 xo[NT];
#pragma unroll
        for (int tt = 0; tt < NT; ++tt) {
            const int tok = (g.type == T_VD) ? 8 * (n >> 2) + 4 * (tt & 1) + (n & 3) : 16 * (tt & 1) + n;
            xo[tt] = (u32)((NT / 2 * w + (tt >> 1)) * 40960 + (g.xb ? q * 512 + tok * 16 : q * 16 + tok * 1280));
        }
        f32x4 acc[12][NT];
        const bool rm = NT == 4 && g.type == T_BL;
        const int sw = (qq >> 1) + 2 * (q & 1);
#pragma unroll
        for (int t = 0; t < 4; ++t)
            c[t] = lds + q * 3072 + qq * 128 + (rm ? ((((t & 2) + (pp >> 1)) ^ sw) << 5) + 16 * (pp & 1) + 8 * ((pp >> 1) ^ (t & 1)) : ((t ^ sw) << 5) + 8 * pp);
        auto rlo = [&](int f) { flo = trread(c[f & 3] + (f / 12) * 12288 + ((f % 12) >> 2) * 1024); };
        auto rhi = [&](int f) {
            const u32x2 hi = trread(c[f & 3] + (f / 12) * 12288 + ((f % 12) >> 2) * 1024 + 512);
            fr[f % (FD + 1)] = u32x4{flo[0], flo[1], hi[0], hi[1]};
        };
        const int xk = uni(g.xb ? 2048 : 64);
        const char *xp = g.x, *xq = xp + xk;
        sfor<0, 4>([&](auto J) {
            if constexpr (J.v < XL) { sfor<0, NX>([&](auto I) { gld16a<0>(xr[J.v][I.v], I.v & 1 ? xq : xp, xo[I.v >> 1]); }); xp += 2 * xk; xq += 2 * xk; }
            wload(J.v);
        });
        waitbar<18 + (XL - 1) * NX>();
        xtie(xr[0]);
#pragma unroll
        for (int i = 0; i < FD; ++i) { rlo(i); rhi(i); }
        char* po = g.out;
#pragma nounroll
        for (int ph = g.np1; ph > 0; --ph) {
            sfor<0, 10>([&](auto J) {
                constexpr int j = J.v;
                u32x4 (&xc)[NX] = xr[j % 5];
                u32x4 (&xn)[NX] = xr[(j + XL) % 5];
                sfor<0, 24>([&](auto I) {
                    constexpr int idx = I.v, kk = idx / 12, T = idx % 12, z = j == 0 && kk == 0, f = (idx + FD) % 24;
                    constexpr int mo = NT == 4 ? (idx < BI && idx % 4 != 2 ? idx - (idx + 1) / 4 : -1) : (idx < BI && idx % 2 == 0 ? idx / 2 : -1);
                    auto mem = [&]() {
                        if constexpr (mo >= NX) gldsm<1024 * (mo - NX - 3)>(wp, wv[mo - NX]);
                        else if constexpr (mo >= 0) gld16a<0>(xn[mo], mo & 1 ? xq : xp, xo[mo >> 1]);
                    };
                    auto misc = [&]() {
                        if constexpr (idx == 1) wring();
                        if constexpr (idx == 13) sring();
                        if constexpr (j == 1 && idx == 22) early_add(g.sig, ph + w);
                        if constexpr (idx >= 15 && idx < BI) { c[idx - 15] += (u32)sd; asm volatile("" : "+v"(c[idx - 15])); }
                        const bool more = ph > 1;
                        if constexpr (j != 5) {
                            if constexpr (idx == 20) wp += wstep;
                        } else {
                            if constexpr (idx == 19) { wp = more ? g.w1b + (size_t)(16 * w) * g.ldw - 2048 : wp2; wstep = more ? wstep : (p2 ? 128 : 0); }
                            if constexpr (idx >= 20 && idx < 23) {
#pragma unroll
                                for (int i = 2 * (idx - 20); i < 2 * (idx - 19); ++i) { wv[i] = more ? wv[i] : (p2 ? v2o(i) : 5120u); asm volatile("" : "+v"(wv[i])); }
                            }
                        }
                        if constexpr (j != 9 - XL) {
                            if constexpr (idx == 21) xp += 2 * xk;
                            if constexpr (idx == 22) xq += 2 * xk;
                        } else if constexpr (idx == 23) {
                            xp = g.x; xq = xp + xk;
#pragma unroll
                            for (int i = 0; i < NT; ++i) { xo[i] = more ? xo[i] : 0u; asm volatile("" : "+v"(xo[i])); }
                        }
                    };
                    if constexpr (idx == BI) { waitbar<6 + (XL - 1) * OPS>(); xtie(xr[(j + 1) % 5]); }
                    if constexpr (idx % 2 == 0) LGKM(6);
                    PIN();
                    const u32x4 a = fr[idx % (FD + 1)];
                    mf1<z>(acc[T][0], a, xc[kk]); PIN();
                    rlo(f);
                    if constexpr (idx == 0) setm0(wdst);
                    if constexpr (NT == 2) rhi(f);
                    PIN();
                    mf1<z>(acc[T][1], a, xc[2 + kk]); PIN();
                    if constexpr (NT == 4) {
                        rhi(f); PIN();
                        mf1<z>(acc[T][2], a, xc[4 + kk]); PIN();
                        mem(); PIN();
                        mf1<z>(acc[T][3], a, xc[6 + kk]); PIN();
                    } else { mem(); PIN(); }
                    misc();
                    PIN();
                });
            });
            asm volatile("s_nop 7\n\ts_nop 1");
            PIN();
#pragma unroll
            for (int tt = 0; tt < NT; ++tt)
#pragma unroll
                for (int ks = 0; ks < 6; ++ks)
                    vf[tt][ks] = u32x4{cvtpk(acc[2 * ks][tt][0], acc[2 * ks][tt][1]), cvtpk(acc[2 * ks][tt][2], acc[2 * ks][tt][3]),
                                       cvtpk(acc[2 * ks + 1][tt][0], acc[2 * ks + 1][tt][1]), cvtpk(acc[2 * ks + 1][tt][2], acc[2 * ks + 1][tt][3])};
            if (g.type == T_QK) {
#pragma unroll
                for (int tt = 0; tt < NT; ++tt)
#pragma unroll
                    for (int ks = 0; ks < 6; ++ks) {
                        gst16(po + (size_t)(NT / 2 * w + (tt >> 1)) * (NH * 12288) + (tt & 1) * 6144 + ks * 1024, lane * 16,
                              u32x4{vf[tt][ks][0], vf[tt][ks][2], vf[tt][ks][1], vf[tt][ks][3]});
                        if (NT == 4 && EPG) asm volatile("s_sleep %0" : : "n"(EPG));
                    }
            } else if (rm) {
                const bool sx = q >= 2;
#pragma unroll
                for (int i = 0; i < 3 * NT; ++i) {
                    const int tp = i / 6, ks = i % 6;
                    u32x4 o[2];
#pragma unroll
                    for (int j = 0; j < 4; ++j) {
                        const u32x4 &v = vf[2 * tp][ks], &u = vf[2 * tp + 1][ks];
                        if (BLS) {
                            const auto r = __builtin_amdgcn_permlane32_swap(v[j], u[j], false, false);
                            o[0][j] = r[0]; o[1][j ^ 2] = r[1];
                        } else { o[0][j] = sx ? v[j ^ 2] : v[j]; o[1][j] = sx ? u[j ^ 2] : u[j]; }
                    }
#pragma unroll
                    for (int j = 0; j < 2; ++j) {
                        gst16(po + (size_t)(2 * w + tp) * g.sV + j * (BLS ? 1024 : 256) + ks * 2048, BLS ? (u32)((q & 1) * 512 + (q >> 1) * 256 + n * 16) : (u32)(q * 512 + n * 16), o[j]);
                        if (EPG) asm volatile("s_sleep %0" : : "n"(EPG));
                    }
                }
            }
            po = g.outb;
            PIN();
        }
    }
    if (p2) {
        constexpr int HI = 2048;
        const int r = 4 * (q & 1) + qq, sw = r >> 1;
#pragma unroll
        for (int t = 0; t < 4; ++t) c[t] = lds + slot + (q >> 1) * 1024 + r * 128 + ((t ^ sw) << 5) + 8 * pp;
        sring();
        u32 so = (u32)((n >> 3) * g.sV + q * g.sQ + (n & 7) * 16);
        char* const o0 = g.out + (size_t)(NT / 2 * w) * g.sP;
        char* const o1 = o0 + g.sP;
        const int sv2 = 2 * g.sV;
        auto rlo = [&](int f) { flo = trread(c[f / 6] + (f % 6) * 4096); };
        auto rhi = [&](int f) {
            const u32x2 hi = trread(c[f / 6] + (f % 6) * 4096 + HI);
            fr[f % (FD + 1)] = u32x4{flo[0], flo[1], hi[0], hi[1]};
        };
        f32x4 a2[2][NT];
        u32 e[NT / 2][4];
        auto cv = [&](f32x4 (&a)[NT], int i) { e[i >> 2][i & 3] = cvtpk(a[i >> 1][2 * (i & 1)], a[i >> 1][2 * (i & 1) + 1]); asm volatile("" : "+v"(e[i >> 2][i & 3])); };
        auto st = [&](int pl) { gst16n(pl ? o1 : o0, so, u32x4{e[pl][0], e[pl][1], e[pl][2], e[pl][3]}); };
        PIN();
#pragma unroll
        for (int i = 0; i < FD; ++i) { rlo(i); rhi(i); }
#pragma unroll
        for (int tt = 0; tt < NT; ++tt) a2[1][tt] = f32x4{0.f, 0.f, 0.f, 0.f};
#pragma nounroll
        for (int t = 0; t < g.tn; ++t) {
            sfor<0, 24>([&](auto I) {
                constexpr int idx = I.v, cb = idx / 6, ks = idx % 6, f = (idx + FD) % 24;
                f32x4 (&ac)[NT] = a2[cb & 1];
                f32x4 (&ap)[NT] = a2[(cb & 1) ^ 1];
                constexpr int mo = cb < 2 ? (ks == 0 ? 3 * cb : (ks >= 4 ? 3 * cb + ks - 3 : -1)) : -1;
                auto mem = [&]() {
                    if constexpr (mo >= 0) gldsm<1024 * (mo - 3)>(wp, wv[mo]);
                    if constexpr (ks == 3 || (NT == 4 && ks == 2)) { if (cb > 0 || t > 0) st(NT == 4 ? ks - 2 : 0); }
                };
                auto misc = [&]() {
                    if constexpr (idx == 12) wring();
                    if constexpr (idx == 23) sring();
                    if constexpr (idx == 16) { wp += t < g.tn - 5 ? 128 : 0; }
                    if constexpr (ks == 0) { c[cb] += (u32)sd; asm volatile("" : "+v"(c[cb])); }
                    if constexpr (ks == 4) { so += (cb > 0 || t > 0) ? (u32)sv2 : 0u; asm volatile("" : "+v"(so)); }
                };
                if constexpr (idx == BI) waitbar<NT == 4 ? 38 : 28>();
                if constexpr (idx % 2 == 0) LGKM(6);
                PIN();
                const u32x4 b = fr[idx % (FD + 1)];
                mf2<ks == 0>(ac[0], vf[0][ks], b); PIN();
                rlo(f);
                if constexpr (idx == 0) setm0(wdst);
                if constexpr (NT == 2) rhi(f);
                if constexpr (NT == 4 && ks >= 1 && ks <= 3) cv(ap, ks == 1 ? 0 : (ks == 2 ? 4 : 7));
                PIN();
                mf2<ks == 0>(ac[1], vf[1][ks], b); PIN();
                if constexpr (NT == 4) {
                    rhi(f);
                    if constexpr (ks == 1 || ks == 2) cv(ap, ks == 1 ? 1 : 5);
                    PIN();
                    mf2<ks == 0>(ac[2], vf[2][ks], b); PIN();
                    mem();
                    if constexpr (ks == 1) { cv(ap, 2); cv(ap, 3); }
                    PIN();
                    mf2<ks == 0>(ac[3], vf[3][ks], b); PIN();
                    if constexpr (ks == 2) cv(ap, 6);
                } else {
                    if constexpr (ks == 1 || ks == 2) { cv(ap, 2 * ks - 2); cv(ap, 2 * ks - 1); }
                    mem(); PIN();
                }
                misc();
                PIN();
            });
            asm volatile("s_nop 7\n\ts_nop 1");
        }
        PIN();
#pragma unroll
        for (int i = 0; i < 2 * NT; ++i) cv(a2[1], i);
#pragma unroll
        for (int pl = 0; pl < NT / 2; ++pl) st(pl);
        asm volatile("s_nop 1");
    }
    if constexpr (NT == 2) asm volatile(".Lkm_g2:"); else asm volatile(".Lkm_g4:");
    sync_ld(y, ws);
    waitvm<0>();
    asm volatile("" : "+v"(y.r), "+v"(y.g), "+v"(y.e));
    sfor<0, 5>([&](auto I) { xtie(xr[I.v]); });
}

struct Ptrs {
    const char *x, *mem0, *wq, *wk, *wv, *wo, *wqkvb, *wob, *gw;
    char* out;
    char* ws;
};

enum { Q_H = 0, Q_VD = 1, Q_KQ = 2, Q_QKVB = 3, Q_BA = 4, Q_BO = 5, Q_PX = 6, Q_OUT = 7 };
DI void run_gemm(const Ptrs& p, int ty, int b, int i, int hf, bool raw, Sync& y, u32 lds, int w) {
    GemmArgs g;
    g.sV = g.sP = g.sQ = 0; g.w2 = p.wo; g.ldw = HD * 2; g.np1 = 1; g.tn = 10;
    g.sig = p.ws + OFF_ST + F_END * 128;
    const char* xf = raw ? p.x : p.ws + OFF_XF;
    g.xb = !raw;
    int s0 = 8 * b;
    if (ty == Q_VD) {
        s0 = b < 2 ? 4 * b : s0 - 8;
        g.type = T_VD; g.w1 = g.w1b = p.wv + i * DH * 2;
        g.w2 = p.wo + (size_t)i * DH * 1280;
        g.out = g.outb = p.ws + OFF_D + (size_t)s0 * D_STRIDE + i * 512;
        g.sV = 16384; g.sP = (int)D_STRIDE; g.sQ = 128;
        if (b == 0) { g.tn = 5; g.w2 += hf * 640; g.out += hf * 40 * 16384; }
    } else if (ty == Q_H) {
        s0 = 0;
        g.type = T_QK; g.w1 = g.w1b = (b ? p.wq : p.wk) + i * DH * 2;
        g.out = g.outb = p.ws + (b ? OFF_QX : OFF_KX) + (size_t)i * 12288;
        b = 0;
    } else if (ty == Q_KQ) {
        s0 = b < 2 ? 4 * b : s0 - 8;
        g.type = T_QK; g.w1 = p.wk + i * DH * 2; g.w1b = p.wq + i * DH * 2;
        g.out = p.ws + OFF_KX + (size_t)(s0 * NH + i) * 12288;
        g.outb = g.out + (OFF_QX - OFF_KX);
        g.np1 = 2;
        g.sig = p.ws + OFF_ST + (F_K + b * 32 + i) * 128;
    } else if (ty == Q_QKVB) {
        g.type = T_BL; g.w1 = p.wqkvb + i * 768; g.w1b = g.w1 + 384; g.ldw = 3840;
        g.sV = 1920 * 64; g.np1 = 2;
        g.out = p.ws + OFF_QKVB + (size_t)s0 * g.sV + i * 48 * 512; g.outb = g.out + 24 * 512;
    } else {
        const int c0 = min(i * DH, DM - DH);
        g.type = T_BL; g.w1 = g.w1b = p.wob + c0 * 2; g.ldw = 1280;
        xf = p.ws + OFF_OB;
        g.sV = 40960;
        g.out = g.outb = p.ws + OFF_BOUT + (size_t)s0 * 40960 + c0 * 64;
    }
    g.x = xf + (size_t)s0 * 40960;
    if (ty <= Q_KQ && b < 2) gemm_task<2>(g, y, p.ws, lds, w);
    else gemm_task<4>(g, y, p.ws, lds, w);
}
DI int pblk(int s) { return s < 8 ? s >> 2 : min(s >> 3, 7) + 1; }

template <int IMM>
DI void gld16c(u32x4& r, const char* sbase, u32 voff) {
    asm volatile("global_load_dwordx4 %0, %1, %2 offset:%3 sc1" : "=v"(r) : "v"(voff), "s"(sbase), "i"(IMM) : "memory");
}
DI void gst4c(char* sbase, u32 voff, u32 v) {
    asm volatile("global_store_dword %0, %1, %2 sc1" : : "v"(voff), "v"(v), "s"(sbase) : "memory");
}
template <int N>
DI void waitv(u32x4& r) { asm volatile("s_waitcnt vmcnt(%1)" : "+v"(r) : "i"(N) : "memory"); }
DI u32 mt() { return (u32)__builtin_amdgcn_s_memtime(); }
DI u32 rt() { return (u32)__builtin_amdgcn_s_memrealtime(); }
DI void spin_to(u32 tgt) { while ((int)(mt() - tgt) < 0) {} }
DI u32 max3u(u32 a, u32 b, u32 c) { return max(max(a, b), c); }
DI u32x4 ldsr16(u32 a) { return *LDSP(u32x4, a); }
DI void clocks(u32& r, u32& m) {
    u64 a, b;
    asm volatile("s_memrealtime %0\n\ts_memtime %1\n\ts_waitcnt lgkmcnt(0)" : "=s"(a), "=s"(b) : : "memory");
    r = (u32)a; m = (u32)b;
}
#define SLATE 20
DI void edge(u32& r, u32& m) {
    u64 a, t[9];
    u32 n = 0;
#define RT(i) "s_memrealtime %" #i "\n\ts_nop 1\n\t"
#define RC(i) "\n\ts_cmp_eq_u64 %" #i ", %1\n\ts_addc_u32 %10, %10, 0"
    asm volatile("s_memtime %0\n\t" RT(1) RT(2) RT(3) RT(4) RT(5) RT(6) RT(7) RT(8) "s_memrealtime %9\n\ts_waitcnt lgkmcnt(0)" RC(2) RC(3) RC(4) RC(5) RC(6) RC(7) RC(8) RC(9)
                 : "=&s"(a), "=&s"(t[0]), "=&s"(t[1]), "=&s"(t[2]), "=&s"(t[3]), "=&s"(t[4]), "=&s"(t[5]), "=&s"(t[6]), "=&s"(t[7]), "=&s"(t[8]), "+s"(n) : : "scc", "memory");
    m = (u32)a + 12 * n;
    r = (u32)t[0] + 4;
}
#define SPB 90
struct Clock {
    u32 r0, m0, r1, m1; int cpt;
    DI void init() { edge(r0, m0); r1 = r0; m1 = m0; cpt = 24 * 64; }
    DI u32 sched(u32& phi, int ofs, int G, int mode) {
        if (mode == 0) edge(r1, m1);
        u32 r = r1, m = m1;
        if (mode == 2) clocks(r, m);
        int rem = (int)(phi + (u32)ofs - r);
        if (__builtin_expect(rem < -SLATE, 0)) { const int k = (-rem / G + 1) * G; phi += (u32)k; rem += k; }
        return m + (u32)((min(rem, 3000) * cpt) >> 6) - SPB;
    }
    DI u32 at(u32 t) { u32 r, m; clocks(r, m); return m + (u32)((min(max((int)(t - r), 0), 3000) * cpt) >> 6); }
    DI void calib() {
        const int dr = (int)(r1 - r0);
        if (dr >= 60) { int c = (int)fminf(fmaxf((float)(int)(m1 - m0) * 64.f * __builtin_amdgcn_rcpf((float)dr), 256.f), 2560.f); asm volatile("" : "+v"(c)); cpt = uni(c); r0 = r1; m0 = m1; }
    }
};
#define VISS 0
#define VISV 0
DI void follow(u32& phi, int ofs, u32 t, int G) { const int late = (int)(t - phi) - ofs; if (__builtin_expect(late > 0, 0)) phi += (u32)((late / G + 1) * G); }
DI bool unwritten(const char* base, u32 off) {
#pragma nounroll
    for (int i = 0; i < 10; ++i) {
        u32x4 t;
        gld16c<-4096>(t, base + (i + (i & 8)) * 1024, off); waitv<0>(t);
        if (__builtin_amdgcn_ballot_w64(max3u(max(t[0], t[1]), t[2], t[3]) == 0xffffffffu) != 0) return true;
    }
    return false;
}

DI size_t uslot(int sd, int s) { const int r = (s * 43) >> 8; return OFF_U + (u32)uni(s * (int)U_STRIDE + r * (int)((2u << 20) - 6 * U_STRIDE) + ((sd >> (2 + r)) & 1) * 8192); }
DI size_t mslot(int sd, int s) { return (u32)uni(s * (int)HO_STRIDE + ((sd >> (s >> 5)) & 1) * 8192 + 4096); }
struct ChainCfg { int T, cR, G, dV, fast, s1, T2, cR2, s2, T3, cR3, dU; };
constexpr int TT_OFF = 88 + 48 + 360;
DI void karg_warm() {
    u32 a[13];
    asm volatile("s_load_dword %0, %13, 0\n\t" "s_load_dword %1, %13, 64\n\t" "s_load_dword %2, %13, 128\n\t" "s_load_dword %3, %13, 192\n\t" "s_load_dword %4, %13, 256\n\t" "s_load_dword %5, %13, 320\n\t" "s_load_dword %6, %13, 384\n\t" "s_load_dword %7, %13, 448\n\t" "s_load_dword %8, %13, 512\n\t" "s_load_dword %9, %13, 576\n\t" "s_load_dword %10, %13, 640\n\t" "s_load_dword %11, %13, 704\n\t" "s_load_dword %12, %13, 748\n\t" "s_waitcnt lgkmcnt(0)"
                 : "=&s"(a[0]), "=&s"(a[1]), "=&s"(a[2]), "=&s"(a[3]), "=&s"(a[4]), "=&s"(a[5]), "=&s"(a[6]), "=&s"(a[7]), "=&s"(a[8]), "=&s"(a[9]), "=&s"(a[10]), "=&s"(a[11]), "=&s"(a[12])
                 : "s"(__builtin_amdgcn_kernarg_segment_ptr()) : "memory");
}
DI u32 sched_ld0(int s) {
    u32 r;
    asm volatile("s_load_dword %0, %1, %2 offset:%3\n\ts_waitcnt lgkmcnt(0)" : "=s"(r) : "s"(__builtin_amdgcn_kernarg_segment_ptr()), "s"(4 * s), "n"(TT_OFF) : "memory");
    return r;
}
#define HP 60
#define HQ 96

constexpr int SL_W = 0;
constexpr int SL_IMG = 131072;
constexpr int SL_KX = SL_IMG + 6656;
constexpr int SL_PF = SL_KX + 12288 + 1024;
constexpr int S_LDS = SL_PF + 256;
DI void nap(int t) { const u32 wake = rt() + (u32)t; while ((int)(rt() - wake) < 0) __builtin_amdgcn_s_sleep(4); }
DI void dly(int n) {
    asm volatile(".Ld%=:\n\ts_sub_u32 %0, %0, 1\n\ts_nop 0\n\ts_cbranch_scc0 .Ld%=" : "+s"(n) : : "scc", "memory");
}
DI void pfl(const char* sbase, u32 voff, u32 lds) {
    asm volatile("s_mov_b32 m0, %2\n\ts_nop 3\n\tglobal_load_lds_dword %0, %1" : : "v"(voff), "s"(sbase), "s"(lds + SL_PF) : "memory");
}
DI void score_role(const Ptrs& p, const ChainCfg cfg, int h, u32 lds, int w) {
    const int lane = lanex(), tid = 64 * w + lane;
    const int n = lane & 15, g = lane >> 4, qq = (lane >> 2) & 3, pp = lane & 3;
    u32x32 wa[8], wv[3];
    const u32 wreg = lds + SL_W + (u32)w * 32768;
    const u32 lfa = wreg + lane * 16;
    const char* kxp = p.ws + OFF_KX + (size_t)h * 12288 + w * 3072;
    const u32 kxl = lds + SL_KX;
    char* const fl = p.ws + OFF_ST;
    const u32 kxw = kxl + w * 3072 - (w < 2 ? 16 : 0);
    auto kxdma = [&]() {
        asm volatile("s_mov_b32 m0, %2\n\ts_nop 3\n\tglobal_load_lds_dwordx4 %0, %1 sc1\n\tglobal_load_lds_dwordx4 %0, %1 offset:1024 sc1\n\tglobal_load_lds_dwordx4 %0, %1 offset:2048 sc1"
                     : : "v"(lanex() * 16), "s"(kxp), "s"(kxw) : "memory");
    };
    u32 kfl, phi;
    int sd;
    {
        constexpr int GS = 3104;
        Sync y;
        if (w < 3) {
#pragma unroll
            for (int i = PXT; i < NSEG; ++i) gst16(p.ws + OFF_KM + (i * NH + h) * 3072 + w * 1024, lane * 16, u32x4{~0u, ~0u, ~0u, ~0u});
#pragma unroll
            for (int i = NSEG - 4; i < NSEG; ++i) gst16(p.ws + OFF_PX + i * 81920 + (w < 2 ? h * 2048 + w * 1024 : 65536 + h * 512), w < 2 ? lane * 16 : (lane * 16) & 511, u32x4{~0u, ~0u, ~0u, ~0u});
        }
        auto dvo = [&](int i) { const int ix = i * 64 + lane; return (u32)((ix / 24) * (HD * 2) + (ix % 24) * 16); };
        const u32 tra = lds + (2 * (g >> 1) + (qq >> 1)) * GS + (4 * (g & 1) + (qq & 1)) * 384 + 8 * pp;
        u32x8 pa = u32x8{dvo(0), dvo(1), dvo(2), tra + 96 * w + 32 * (w & 1), tra + 96 * w - 64 * (w & 1), tra + 32 * w, lfa, (u32)(lane & 31) * 8};
        asm volatile("s_nop 4\n\t" "s_add_u32 s42, s46, 0x0\n\t" "s_addc_u32 s43, s47, 0\n\t" "s_add_u32 m0, %18, 0x0\n\t" "s_nop 0\n\t" "global_load_lds_dwordx4 v224, s[42:43]\n\t" "s_add_u32 m0, %18, 0x400\n\t"
                     "s_nop 0\n\t" "global_load_lds_dwordx4 v225, s[42:43]\n\t" "s_add_u32 m0, %18, 0x800\n\t" "s_nop 0\n\t" "global_load_lds_dwordx4 v226, s[42:43]\n\t" "s_add_u32 s42, s46, 0x18000\n\t"
                     "s_addc_u32 s43, s47, 0\n\t" "s_add_u32 m0, %18, 0xc20\n\t" "s_nop 0\n\t" "global_load_lds_dwordx4 v224, s[42:43]\n\t" "s_add_u32 m0, %18, 0x1020\n\t" "s_nop 0\n\t"
                     "global_load_lds_dwordx4 v225, s[42:43]\n\t" "s_add_u32 m0, %18, 0x1420\n\t" "s_nop 0\n\t" "global_load_lds_dwordx4 v226, s[42:43]\n\t" "s_add_u32 s42, s46, 0xc0000\n\t" "s_addc_u32 s43, s47, 0\n\t"
                     "s_add_u32 m0, %18, 0x6100\n\t" "s_nop 0\n\t" "global_load_lds_dwordx4 v224, s[42:43]\n\t" "s_add_u32 m0, %18, 0x6500\n\t" "s_nop 0\n\t" "global_load_lds_dwordx4 v225, s[42:43]\n\t"
                     "s_add_u32 m0, %18, 0x6900\n\t" "s_nop 0\n\t" "global_load_lds_dwordx4 v226, s[42:43]\n\t" "s_add_u32 s42, s46, 0xd8000\n\t" "s_addc_u32 s43, s47, 0\n\t" "s_add_u32 m0, %18, 0x6d20\n\t" "s_nop 0\n\t"
                     "global_load_lds_dwordx4 v224, s[42:43]\n\t" "s_add_u32 m0, %18, 0x7120\n\t" "s_nop 0\n\t" "global_load_lds_dwordx4 v225, s[42:43]\n\t" "s_add_u32 m0, %18, 0x7520\n\t" "s_nop 0\n\t"
                     "global_load_lds_dwordx4 v226, s[42:43]\n\t" "s_add_u32 s42, s46, 0x180000\n\t" "s_addc_u32 s43, s47, 0\n\t" "s_add_u32 m0, %18, 0xc200\n\t" "s_nop 0\n\t" "global_load_lds_dwordx4 v224, s[42:43]\n\t"
                     "s_add_u32 m0, %18, 0xc600\n\t" "s_nop 0\n\t" "global_load_lds_dwordx4 v225, s[42:43]\n\t" "s_add_u32 m0, %18, 0xca00\n\t" "s_nop 0\n\t" "global_load_lds_dwordx4 v226, s[42:43]\n\t"
                     "s_add_u32 s42, s46, 0x198000\n\t" "s_addc_u32 s43, s47, 0\n\t" "s_add_u32 m0, %18, 0xce20\n\t" "s_nop 0\n\t" "global_load_lds_dwordx4 v224, s[42:43]\n\t" "s_add_u32 m0, %18, 0xd220\n\t" "s_nop 0\n\t"
                     "global_load_lds_dwordx4 v225, s[42:43]\n\t" "s_add_u32 m0, %18, 0xd620\n\t" "s_nop 0\n\t" "global_load_lds_dwordx4 v226, s[42:43]\n\t" "s_add_u32 s42, s46, 0x240000\n\t" "s_addc_u32 s43, s47, 0\n\t"
                     "s_add_u32 m0, %18, 0x12300\n\t" "s_nop 0\n\t" "global_load_lds_dwordx4 v224, s[42:43]\n\t" "s_add_u32 m0, %18, 0x12700\n\t" "s_nop 0\n\t" "global_load_lds_dwordx4 v225, s[42:43]\n\t"
                     "s_add_u32 m0, %18, 0x12b00\n\t" "s_nop 0\n\t" "global_load_lds_dwordx4 v226, s[42:43]\n\t" "s_add_u32 s42, s46, 0x258000\n\t" "s_addc_u32 s43, s47, 0\n\t" "s_add_u32 m0, %18, 0x12f20\n\t"
                     "s_nop 0\n\t" "global_load_lds_dwordx4 v224, s[42:43]\n\t" "s_add_u32 m0, %18, 0x13320\n\t" "s_nop 0\n\t" "global_load_lds_dwordx4 v225, s[42:43]\n\t" "s_add_u32 m0, %18, 0x13720\n\t" "s_nop 0\n\t"
                     "global_load_lds_dwordx4 v226, s[42:43]\n\t" "s_waitcnt vmcnt(12) lgkmcnt(0)\n\t" "s_barrier\n\t" "s_add_u32 s42, s46, 0x300000\n\t" "s_addc_u32 s43, s47, 0\n\t" "s_add_u32 m0, %18, 0x18400\n\t"
                     "s_nop 0\n\t" "global_load_lds_dwordx4 v224, s[42:43]\n\t" "s_add_u32 m0, %18, 0x18800\n\t" "s_nop 0\n\t" "global_load_lds_dwordx4 v225, s[42:43]\n\t" "s_add_u32 m0, %18, 0x18c00\n\t" "s_nop 0\n\t"
                     "global_load_lds_dwordx4 v226, s[42:43]\n\t" "s_add_u32 s42, s46, 0x318000\n\t" "s_addc_u32 s43, s47, 0\n\t" "s_add_u32 m0, %18, 0x19020\n\t" "s_nop 0\n\t" "global_load_lds_dwordx4 v224, s[42:43]\n\t"
                     "s_add_u32 m0, %18, 0x19420\n\t" "s_nop 0\n\t" "global_load_lds_dwordx4 v225, s[42:43]\n\t" "s_add_u32 m0, %18, 0x19820\n\t" "s_nop 0\n\t" "global_load_lds_dwordx4 v226, s[42:43]\n\t"
                     "s_add_u32 s42, s46, 0x3c0000\n\t" "s_addc_u32 s43, s47, 0\n\t" "s_add_u32 m0, %18, 0x1e500\n\t" "s_nop 0\n\t" "global_load_lds_dwordx4 v224, s[42:43]\n\t" "s_add_u32 m0, %18, 0x1e900\n\t"
                     "s_nop 0\n\t" "global_load_lds_dwordx4 v225, s[42:43]\n\t" "s_add_u32 m0, %18, 0x1ed00\n\t" "s_nop 0\n\t" "global_load_lds_dwordx4 v226, s[42:43]\n\t" "s_add_u32 s42, s46, 0x3d8000\n\t"
                     "s_addc_u32 s43, s47, 0\n\t" "s_add_u32 m0, %18, 0x1f120\n\t" "s_nop 0\n\t" "global_load_lds_dwordx4 v224, s[42:43]\n\t" "s_add_u32 m0, %18, 0x1f520\n\t" "s_nop 0\n\t"
                     "global_load_lds_dwordx4 v225, s[42:43]\n\t" "s_add_u32 m0, %18, 0x1f920\n\t" "s_nop 0\n\t" "global_load_lds_dwordx4 v226, s[42:43]\n\t" "v_add_u32 v232, 0x0, v229\n\t"
                     "ds_read_b64_tr_b16 v[8:9], v232\n\t" "ds_read_b64_tr_b16 v[10:11], v232 offset:768\n\t" "ds_read_b64_tr_b16 v[12:13], v232 offset:12416\n\t" "ds_read_b64_tr_b16 v[14:15], v232 offset:13184\n\t"
                     "ds_read_b64_tr_b16 a[96:97], v232 offset:128\n\t" "ds_read_b64_tr_b16 a[98:99], v232 offset:896\n\t" "ds_read_b64_tr_b16 a[100:101], v232 offset:12544\n\t"
                     "ds_read_b64_tr_b16 a[102:103], v232 offset:13312\n\t" "ds_read_b64_tr_b16 a[176:177], v232 offset:256\n\t" "ds_read_b64_tr_b16 a[178:179], v232 offset:1024\n\t"
                     "ds_read_b64_tr_b16 a[180:181], v232 offset:12672\n\t" "ds_read_b64_tr_b16 a[182:183], v232 offset:13440\n\t" "ds_read_b64_tr_b16 v[24:25], v232 offset:24832\n\t"
                     "ds_read_b64_tr_b16 v[26:27], v232 offset:25600\n\t" "ds_read_b64_tr_b16 v[28:29], v232 offset:37248\n\t" "ds_read_b64_tr_b16 v[30:31], v232 offset:38016\n\t"
                     "ds_read_b64_tr_b16 a[104:105], v232 offset:24960\n\t" "ds_read_b64_tr_b16 a[106:107], v232 offset:25728\n\t" "ds_read_b64_tr_b16 a[108:109], v232 offset:37376\n\t"
                     "ds_read_b64_tr_b16 a[110:111], v232 offset:38144\n\t" "ds_read_b64_tr_b16 a[184:185], v232 offset:25088\n\t" "ds_read_b64_tr_b16 a[186:187], v232 offset:25856\n\t"
                     "ds_read_b64_tr_b16 a[188:189], v232 offset:37504\n\t" "ds_read_b64_tr_b16 a[190:191], v232 offset:38272\n\t" "s_waitcnt vmcnt(12) lgkmcnt(0)\n\t" "s_barrier\n\t" "s_add_u32 s42, s46, 0x480000\n\t"
                     "s_addc_u32 s43, s47, 0\n\t" "s_add_u32 m0, %18, 0x0\n\t" "s_nop 0\n\t" "global_load_lds_dwordx4 v224, s[42:43]\n\t" "s_add_u32 m0, %18, 0x400\n\t" "s_nop 0\n\t"
                     "global_load_lds_dwordx4 v225, s[42:43]\n\t" "s_add_u32 m0, %18, 0x800\n\t" "s_nop 0\n\t" "global_load_lds_dwordx4 v226, s[42:43]\n\t" "s_add_u32 s42, s46, 0x498000\n\t" "s_addc_u32 s43, s47, 0\n\t"
                     "s_add_u32 m0, %18, 0xc20\n\t" "s_nop 0\n\t" "global_load_lds_dwordx4 v224, s[42:43]\n\t" "s_add_u32 m0, %18, 0x1020\n\t" "s_nop 0\n\t" "global_load_lds_dwordx4 v225, s[42:43]\n\t"
                     "s_add_u32 m0, %18, 0x1420\n\t" "s_nop 0\n\t" "global_load_lds_dwordx4 v226, s[42:43]\n\t" "s_add_u32 s42, s46, 0x540000\n\t" "s_addc_u32 s43, s47, 0\n\t" "s_add_u32 m0, %18, 0x6100\n\t" "s_nop 0\n\t"
                     "global_load_lds_dwordx4 v224, s[42:43]\n\t" "s_add_u32 m0, %18, 0x6500\n\t" "s_nop 0\n\t" "global_load_lds_dwordx4 v225, s[42:43]\n\t" "s_add_u32 m0, %18, 0x6900\n\t" "s_nop 0\n\t"
                     "global_load_lds_dwordx4 v226, s[42:43]\n\t" "s_add_u32 s42, s46, 0x558000\n\t" "s_addc_u32 s43, s47, 0\n\t" "s_add_u32 m0, %18, 0x6d20\n\t" "s_nop 0\n\t" "global_load_lds_dwordx4 v224, s[42:43]\n\t"
                     "s_add_u32 m0, %18, 0x7120\n\t" "s_nop 0\n\t" "global_load_lds_dwordx4 v225, s[42:43]\n\t" "s_add_u32 m0, %18, 0x7520\n\t" "s_nop 0\n\t" "global_load_lds_dwordx4 v226, s[42:43]\n\t"
                     "v_add_u32 v232, 0xc200, v229\n\t" "ds_read_b64_tr_b16 v[40:41], v232\n\t" "ds_read_b64_tr_b16 v[42:43], v232 offset:768\n\t" "ds_read_b64_tr_b16 v[44:45], v232 offset:12416\n\t"
                     "ds_read_b64_tr_b16 v[46:47], v232 offset:13184\n\t" "ds_read_b64_tr_b16 a[112:113], v232 offset:128\n\t" "ds_read_b64_tr_b16 a[114:115], v232 offset:896\n\t"
                     "ds_read_b64_tr_b16 a[116:117], v232 offset:12544\n\t" "ds_read_b64_tr_b16 a[118:119], v232 offset:13312\n\t" "ds_read_b64_tr_b16 a[192:193], v232 offset:256\n\t"
                     "ds_read_b64_tr_b16 a[194:195], v232 offset:1024\n\t" "ds_read_b64_tr_b16 a[196:197], v232 offset:12672\n\t" "ds_read_b64_tr_b16 a[198:199], v232 offset:13440\n\t"
                     "ds_read_b64_tr_b16 v[56:57], v232 offset:24832\n\t" "ds_read_b64_tr_b16 v[58:59], v232 offset:25600\n\t" "ds_read_b64_tr_b16 v[60:61], v232 offset:37248\n\t"
                     "ds_read_b64_tr_b16 v[62:63], v232 offset:38016\n\t" "ds_read_b64_tr_b16 a[120:121], v232 offset:24960\n\t" "ds_read_b64_tr_b16 a[122:123], v232 offset:25728\n\t"
                     "ds_read_b64_tr_b16 a[124:125], v232 offset:37376\n\t" "ds_read_b64_tr_b16 a[126:127], v232 offset:38144\n\t" "ds_read_b64_tr_b16 a[200:201], v232 offset:25088\n\t"
                     "ds_read_b64_tr_b16 a[202:203], v232 offset:25856\n\t" "ds_read_b64_tr_b16 a[204:205], v232 offset:37504\n\t" "ds_read_b64_tr_b16 a[206:207], v232 offset:38272\n\t" "s_waitcnt vmcnt(12) lgkmcnt(0)\n\t"
                     "s_barrier\n\t" "s_add_u32 s42, s46, 0x600000\n\t" "s_addc_u32 s43, s47, 0\n\t" "s_add_u32 m0, %18, 0xc200\n\t" "s_nop 0\n\t" "global_load_lds_dwordx4 v224, s[42:43]\n\t"
                     "s_add_u32 m0, %18, 0xc600\n\t" "s_nop 0\n\t" "global_load_lds_dwordx4 v225, s[42:43]\n\t" "s_add_u32 m0, %18, 0xca00\n\t" "s_nop 0\n\t" "global_load_lds_dwordx4 v226, s[42:43]\n\t"
                     "s_add_u32 s42, s46, 0x618000\n\t" "s_addc_u32 s43, s47, 0\n\t" "s_add_u32 m0, %18, 0xce20\n\t" "s_nop 0\n\t" "global_load_lds_dwordx4 v224, s[42:43]\n\t" "s_add_u32 m0, %18, 0xd220\n\t" "s_nop 0\n\t"
                     "global_load_lds_dwordx4 v225, s[42:43]\n\t" "s_add_u32 m0, %18, 0xd620\n\t" "s_nop 0\n\t" "global_load_lds_dwordx4 v226, s[42:43]\n\t" "s_add_u32 s42, s46, 0x6c0000\n\t" "s_addc_u32 s43, s47, 0\n\t"
                     "s_add_u32 m0, %18, 0x12300\n\t" "s_nop 0\n\t" "global_load_lds_dwordx4 v224, s[42:43]\n\t" "s_add_u32 m0, %18, 0x12700\n\t" "s_nop 0\n\t" "global_load_lds_dwordx4 v225, s[42:43]\n\t"
                     "s_add_u32 m0, %18, 0x12b00\n\t" "s_nop 0\n\t" "global_load_lds_dwordx4 v226, s[42:43]\n\t" "s_add_u32 s42, s46, 0x6d8000\n\t" "s_addc_u32 s43, s47, 0\n\t" "s_add_u32 m0, %18, 0x12f20\n\t"
                     "s_nop 0\n\t" "global_load_lds_dwordx4 v224, s[42:43]\n\t" "s_add_u32 m0, %18, 0x13320\n\t" "s_nop 0\n\t" "global_load_lds_dwordx4 v225, s[42:43]\n\t" "s_add_u32 m0, %18, 0x13720\n\t" "s_nop 0\n\t"
                     "global_load_lds_dwordx4 v226, s[42:43]\n\t" "v_add_u32 v232, 0x18400, v229\n\t" "ds_read_b64_tr_b16 v[72:73], v232\n\t" "ds_read_b64_tr_b16 v[74:75], v232 offset:768\n\t"
                     "ds_read_b64_tr_b16 v[76:77], v232 offset:12416\n\t" "ds_read_b64_tr_b16 v[78:79], v232 offset:13184\n\t" "ds_read_b64_tr_b16 a[128:129], v232 offset:128\n\t"
                     "ds_read_b64_tr_b16 a[130:131], v232 offset:896\n\t" "ds_read_b64_tr_b16 a[132:133], v232 offset:12544\n\t" "ds_read_b64_tr_b16 a[134:135], v232 offset:13312\n\t"
                     "ds_read_b64_tr_b16 a[208:209], v232 offset:256\n\t" "ds_read_b64_tr_b16 a[210:211], v232 offset:1024\n\t" "ds_read_b64_tr_b16 a[212:213], v232 offset:12672\n\t"
                     "ds_read_b64_tr_b16 a[214:215], v232 offset:13440\n\t" "ds_read_b64_tr_b16 v[88:89], v232 offset:24832\n\t" "ds_read_b64_tr_b16 v[90:91], v232 offset:25600\n\t"
                     "ds_read_b64_tr_b16 v[92:93], v232 offset:37248\n\t" "ds_read_b64_tr_b16 v[94:95], v232 offset:38016\n\t" "ds_read_b64_tr_b16 a[136:137], v232 offset:24960\n\t"
                     "ds_read_b64_tr_b16 a[138:139], v232 offset:25728\n\t" "ds_read_b64_tr_b16 a[140:141], v232 offset:37376\n\t" "ds_read_b64_tr_b16 a[142:143], v232 offset:38144\n\t"
                     "ds_read_b64_tr_b16 a[216:217], v232 offset:25088\n\t" "ds_read_b64_tr_b16 a[218:219], v232 offset:25856\n\t" "ds_read_b64_tr_b16 a[220:221], v232 offset:37504\n\t"
                     "ds_read_b64_tr_b16 a[222:223], v232 offset:38272\n\t" "s_waitcnt vmcnt(12) lgkmcnt(0)\n\t" "s_barrier\n\t" "s_add_u32 s42, s44, 0x0\n\t" "s_addc_u32 s43, s45, 0\n\t" "s_add_u32 m0, %18, 0x18400\n\t"
                     "s_nop 0\n\t" "global_load_lds_dwordx4 v224, s[42:43]\n\t" "s_add_u32 m0, %18, 0x18800\n\t" "s_nop 0\n\t" "global_load_lds_dwordx4 v225, s[42:43]\n\t" "s_add_u32 m0, %18, 0x18c00\n\t" "s_nop 0\n\t"
                     "global_load_lds_dwordx4 v226, s[42:43]\n\t" "s_add_u32 s42, s44, 0x18000\n\t" "s_addc_u32 s43, s45, 0\n\t" "s_add_u32 m0, %18, 0x19020\n\t" "s_nop 0\n\t" "global_load_lds_dwordx4 v224, s[42:43]\n\t"
                     "s_add_u32 m0, %18, 0x19420\n\t" "s_nop 0\n\t" "global_load_lds_dwordx4 v225, s[42:43]\n\t" "s_add_u32 m0, %18, 0x19820\n\t" "s_nop 0\n\t" "global_load_lds_dwordx4 v226, s[42:43]\n\t"
                     "s_add_u32 s42, s44, 0xc0000\n\t" "s_addc_u32 s43, s45, 0\n\t" "s_add_u32 m0, %18, 0x1e500\n\t" "s_nop 0\n\t" "global_load_lds_dwordx4 v224, s[42:43]\n\t" "s_add_u32 m0, %18, 0x1e900\n\t" "s_nop 0\n\t"
                     "global_load_lds_dwordx4 v225, s[42:43]\n\t" "s_add_u32 m0, %18, 0x1ed00\n\t" "s_nop 0\n\t" "global_load_lds_dwordx4 v226, s[42:43]\n\t" "s_add_u32 s42, s44, 0xd8000\n\t" "s_addc_u32 s43, s45, 0\n\t"
                     "s_add_u32 m0, %18, 0x1f120\n\t" "s_nop 0\n\t" "global_load_lds_dwordx4 v224, s[42:43]\n\t" "s_add_u32 m0, %18, 0x1f520\n\t" "s_nop 0\n\t" "global_load_lds_dwordx4 v225, s[42:43]\n\t"
                     "s_add_u32 m0, %18, 0x1f920\n\t" "s_nop 0\n\t" "global_load_lds_dwordx4 v226, s[42:43]\n\t" "v_add_u32 v232, 0x0, v229\n\t" "ds_read_b64_tr_b16 v[104:105], v232\n\t"
                     "ds_read_b64_tr_b16 v[106:107], v232 offset:768\n\t" "ds_read_b64_tr_b16 v[108:109], v232 offset:12416\n\t" "ds_read_b64_tr_b16 v[110:111], v232 offset:13184\n\t"
                     "ds_read_b64_tr_b16 a[144:145], v232 offset:128\n\t" "ds_read_b64_tr_b16 a[146:147], v232 offset:896\n\t" "ds_read_b64_tr_b16 a[148:149], v232 offset:12544\n\t"
                     "ds_read_b64_tr_b16 a[150:151], v232 offset:13312\n\t" "ds_read_b64_tr_b16 a[224:225], v232 offset:256\n\t" "ds_read_b64_tr_b16 a[226:227], v232 offset:1024\n\t"
                     "ds_read_b64_tr_b16 a[228:229], v232 offset:12672\n\t" "ds_read_b64_tr_b16 a[230:231], v232 offset:13440\n\t" "ds_read_b64_tr_b16 v[120:121], v232 offset:24832\n\t"
                     "ds_read_b64_tr_b16 v[122:123], v232 offset:25600\n\t" "ds_read_b64_tr_b16 v[124:125], v232 offset:37248\n\t" "ds_read_b64_tr_b16 v[126:127], v232 offset:38016\n\t"
                     "ds_read_b64_tr_b16 a[152:153], v232 offset:24960\n\t" "ds_read_b64_tr_b16 a[154:155], v232 offset:25728\n\t" "ds_read_b64_tr_b16 a[156:157], v232 offset:37376\n\t"
                     "ds_read_b64_tr_b16 a[158:159], v232 offset:38144\n\t" "ds_read_b64_tr_b16 a[232:233], v232 offset:25088\n\t" "ds_read_b64_tr_b16 a[234:235], v232 offset:25856\n\t"
                     "ds_read_b64_tr_b16 a[236:237], v232 offset:37504\n\t" "ds_read_b64_tr_b16 a[238:239], v232 offset:38272\n\t" "s_waitcnt vmcnt(12) lgkmcnt(0)\n\t" "s_barrier\n\t" "s_add_u32 s42, s44, 0x180000\n\t"
                     "s_addc_u32 s43, s45, 0\n\t" "s_add_u32 m0, %18, 0x0\n\t" "s_nop 0\n\t" "global_load_lds_dwordx4 v224, s[42:43]\n\t" "s_add_u32 m0, %18, 0x400\n\t" "s_nop 0\n\t"
                     "global_load_lds_dwordx4 v225, s[42:43]\n\t" "s_add_u32 m0, %18, 0x800\n\t" "s_nop 0\n\t" "global_load_lds_dwordx4 v226, s[42:43]\n\t" "s_add_u32 s42, s44, 0x198000\n\t" "s_addc_u32 s43, s45, 0\n\t"
                     "s_add_u32 m0, %18, 0xc20\n\t" "s_nop 0\n\t" "global_load_lds_dwordx4 v224, s[42:43]\n\t" "s_add_u32 m0, %18, 0x1020\n\t" "s_nop 0\n\t" "global_load_lds_dwordx4 v225, s[42:43]\n\t"
                     "s_add_u32 m0, %18, 0x1420\n\t" "s_nop 0\n\t" "global_load_lds_dwordx4 v226, s[42:43]\n\t" "s_add_u32 s42, s44, 0x240000\n\t" "s_addc_u32 s43, s45, 0\n\t" "s_add_u32 m0, %18, 0x6100\n\t" "s_nop 0\n\t"
                     "global_load_lds_dwordx4 v224, s[42:43]\n\t" "s_add_u32 m0, %18, 0x6500\n\t" "s_nop 0\n\t" "global_load_lds_dwordx4 v225, s[42:43]\n\t" "s_add_u32 m0, %18, 0x6900\n\t" "s_nop 0\n\t"
                     "global_load_lds_dwordx4 v226, s[42:43]\n\t" "s_add_u32 s42, s44, 0x258000\n\t" "s_addc_u32 s43, s45, 0\n\t" "s_add_u32 m0, %18, 0x6d20\n\t" "s_nop 0\n\t" "global_load_lds_dwordx4 v224, s[42:43]\n\t"
                     "s_add_u32 m0, %18, 0x7120\n\t" "s_nop 0\n\t" "global_load_lds_dwordx4 v225, s[42:43]\n\t" "s_add_u32 m0, %18, 0x7520\n\t" "s_nop 0\n\t" "global_load_lds_dwordx4 v226, s[42:43]\n\t"
                     "v_add_u32 v232, 0xc200, v229\n\t" "ds_read_b64_tr_b16 a[56:57], v232\n\t" "ds_read_b64_tr_b16 a[58:59], v232 offset:768\n\t" "ds_read_b64_tr_b16 a[60:61], v232 offset:12416\n\t"
                     "ds_read_b64_tr_b16 a[62:63], v232 offset:13184\n\t" "ds_read_b64_tr_b16 a[160:161], v232 offset:128\n\t" "ds_read_b64_tr_b16 a[162:163], v232 offset:896\n\t"
                     "ds_read_b64_tr_b16 a[164:165], v232 offset:12544\n\t" "ds_read_b64_tr_b16 a[166:167], v232 offset:13312\n\t" "ds_read_b64_tr_b16 a[240:241], v232 offset:256\n\t"
                     "ds_read_b64_tr_b16 a[242:243], v232 offset:1024\n\t" "ds_read_b64_tr_b16 a[244:245], v232 offset:12672\n\t" "ds_read_b64_tr_b16 a[246:247], v232 offset:13440\n\t"
                     "ds_read_b64_tr_b16 a[88:89], v232 offset:24832\n\t" "ds_read_b64_tr_b16 a[90:91], v232 offset:25600\n\t" "ds_read_b64_tr_b16 a[92:93], v232 offset:37248\n\t"
                     "ds_read_b64_tr_b16 a[94:95], v232 offset:38016\n\t" "ds_read_b64_tr_b16 a[168:169], v232 offset:24960\n\t" "ds_read_b64_tr_b16 a[170:171], v232 offset:25728\n\t"
                     "ds_read_b64_tr_b16 a[172:173], v232 offset:37376\n\t" "ds_read_b64_tr_b16 a[174:175], v232 offset:38144\n\t" "ds_read_b64_tr_b16 a[248:249], v232 offset:25088\n\t"
                     "ds_read_b64_tr_b16 a[250:251], v232 offset:25856\n\t" "ds_read_b64_tr_b16 a[252:253], v232 offset:37504\n\t" "ds_read_b64_tr_b16 a[254:255], v232 offset:38272\n\t" "s_waitcnt vmcnt(12) lgkmcnt(0)\n\t"
                     "s_barrier\n\t" "s_add_u32 s42, s44, 0x300000\n\t" "s_addc_u32 s43, s45, 0\n\t" "s_add_u32 m0, %18, 0xc200\n\t" "s_nop 0\n\t" "global_load_lds_dwordx4 v224, s[42:43]\n\t"
                     "s_add_u32 m0, %18, 0xc600\n\t" "s_nop 0\n\t" "global_load_lds_dwordx4 v225, s[42:43]\n\t" "s_add_u32 m0, %18, 0xca00\n\t" "s_nop 0\n\t" "global_load_lds_dwordx4 v226, s[42:43]\n\t"
                     "s_add_u32 s42, s44, 0x318000\n\t" "s_addc_u32 s43, s45, 0\n\t" "s_add_u32 m0, %18, 0xce20\n\t" "s_nop 0\n\t" "global_load_lds_dwordx4 v224, s[42:43]\n\t" "s_add_u32 m0, %18, 0xd220\n\t" "s_nop 0\n\t"
                     "global_load_lds_dwordx4 v225, s[42:43]\n\t" "s_add_u32 m0, %18, 0xd620\n\t" "s_nop 0\n\t" "global_load_lds_dwordx4 v226, s[42:43]\n\t" "s_add_u32 s42, s44, 0x3c0000\n\t" "s_addc_u32 s43, s45, 0\n\t"
                     "s_add_u32 m0, %18, 0x12300\n\t" "s_nop 0\n\t" "global_load_lds_dwordx4 v224, s[42:43]\n\t" "s_add_u32 m0, %18, 0x12700\n\t" "s_nop 0\n\t" "global_load_lds_dwordx4 v225, s[42:43]\n\t"
                     "s_add_u32 m0, %18, 0x12b00\n\t" "s_nop 0\n\t" "global_load_lds_dwordx4 v226, s[42:43]\n\t" "s_add_u32 s42, s44, 0x3d8000\n\t" "s_addc_u32 s43, s45, 0\n\t" "s_add_u32 m0, %18, 0x12f20\n\t"
                     "s_nop 0\n\t" "global_load_lds_dwordx4 v224, s[42:43]\n\t" "s_add_u32 m0, %18, 0x13320\n\t" "s_nop 0\n\t" "global_load_lds_dwordx4 v225, s[42:43]\n\t" "s_add_u32 m0, %18, 0x13720\n\t" "s_nop 0\n\t"
                     "global_load_lds_dwordx4 v226, s[42:43]\n\t" "v_add_u32 v232, 0x18400, v227\n\t" "v_add_u32 v233, 0x18400, v228\n\t" "ds_read_b64_tr_b16 v[128:129], v232\n\t"
                     "ds_read_b64_tr_b16 v[130:131], v232 offset:768\n\t" "ds_read_b64_tr_b16 v[132:133], v232 offset:12416\n\t" "ds_read_b64_tr_b16 v[134:135], v232 offset:13184\n\t"
                     "ds_read_b64_tr_b16 v[0:1], v232 offset:32\n\t" "ds_read_b64_tr_b16 v[2:3], v232 offset:800\n\t" "ds_read_b64_tr_b16 v[4:5], v232 offset:12448\n\t" "ds_read_b64_tr_b16 v[6:7], v232 offset:13216\n\t"
                     "ds_read_b64_tr_b16 v[136:137], v233 offset:64\n\t" "ds_read_b64_tr_b16 v[138:139], v233 offset:832\n\t" "ds_read_b64_tr_b16 v[140:141], v233 offset:12480\n\t"
                     "ds_read_b64_tr_b16 v[142:143], v233 offset:13248\n\t" "ds_read_b64_tr_b16 v[144:145], v232 offset:24832\n\t" "ds_read_b64_tr_b16 v[146:147], v232 offset:25600\n\t"
                     "ds_read_b64_tr_b16 v[148:149], v232 offset:37248\n\t" "ds_read_b64_tr_b16 v[150:151], v232 offset:38016\n\t" "ds_read_b64_tr_b16 v[16:17], v232 offset:24864\n\t"
                     "ds_read_b64_tr_b16 v[18:19], v232 offset:25632\n\t" "ds_read_b64_tr_b16 v[20:21], v232 offset:37280\n\t" "ds_read_b64_tr_b16 v[22:23], v232 offset:38048\n\t"
                     "ds_read_b64_tr_b16 v[152:153], v233 offset:24896\n\t" "ds_read_b64_tr_b16 v[154:155], v233 offset:25664\n\t" "ds_read_b64_tr_b16 v[156:157], v233 offset:37312\n\t"
                     "ds_read_b64_tr_b16 v[158:159], v233 offset:38080\n\t" "s_waitcnt vmcnt(12) lgkmcnt(0)\n\t" "s_barrier\n\t" "v_mov_b32 %14, 0\n\t" "global_load_dwordx2 %11, v231, %19 sc1\n\t"
                     "global_load_dwordx2 %12, %14, %19 offset:512 sc1\n\t" "global_load_dwordx2 %13, %14, %19 offset:1024 sc1\n\t" "s_add_u32 s42, s44, 0x480000\n\t" "s_addc_u32 s43, s45, 0\n\t"
                     "s_add_u32 m0, %18, 0x18400\n\t" "s_nop 0\n\t" "global_load_lds_dwordx4 v224, s[42:43]\n\t" "s_add_u32 m0, %18, 0x18800\n\t" "s_nop 0\n\t" "global_load_lds_dwordx4 v225, s[42:43]\n\t"
                     "s_add_u32 m0, %18, 0x18c00\n\t" "s_nop 0\n\t" "global_load_lds_dwordx4 v226, s[42:43]\n\t" "s_add_u32 s42, s44, 0x498000\n\t" "s_addc_u32 s43, s45, 0\n\t" "s_add_u32 m0, %18, 0x19020\n\t"
                     "s_nop 0\n\t" "global_load_lds_dwordx4 v224, s[42:43]\n\t" "s_add_u32 m0, %18, 0x19420\n\t" "s_nop 0\n\t" "global_load_lds_dwordx4 v225, s[42:43]\n\t" "s_add_u32 m0, %18, 0x19820\n\t" "s_nop 0\n\t"
                     "global_load_lds_dwordx4 v226, s[42:43]\n\t" "s_add_u32 s42, s44, 0x540000\n\t" "s_addc_u32 s43, s45, 0\n\t" "s_add_u32 m0, %18, 0x1e500\n\t" "s_nop 0\n\t" "global_load_lds_dwordx4 v224, s[42:43]\n\t"
                     "s_add_u32 m0, %18, 0x1e900\n\t" "s_nop 0\n\t" "global_load_lds_dwordx4 v225, s[42:43]\n\t" "s_add_u32 m0, %18, 0x1ed00\n\t" "s_nop 0\n\t" "global_load_lds_dwordx4 v226, s[42:43]\n\t"
                     "s_add_u32 s42, s44, 0x558000\n\t" "s_addc_u32 s43, s45, 0\n\t" "s_add_u32 m0, %18, 0x1f120\n\t" "s_nop 0\n\t" "global_load_lds_dwordx4 v224, s[42:43]\n\t" "s_add_u32 m0, %18, 0x1f520\n\t"
                     "s_nop 0\n\t" "global_load_lds_dwordx4 v225, s[42:43]\n\t" "s_add_u32 m0, %18, 0x1f920\n\t" "s_nop 0\n\t" "global_load_lds_dwordx4 v226, s[42:43]\n\t" "v_add_u32 v232, 0x0, v227\n\t"
                     "v_add_u32 v233, 0x0, v228\n\t" "ds_read_b64_tr_b16 v[160:161], v232\n\t" "ds_read_b64_tr_b16 v[162:163], v232 offset:768\n\t" "ds_read_b64_tr_b16 v[164:165], v232 offset:12416\n\t"
                     "ds_read_b64_tr_b16 v[166:167], v232 offset:13184\n\t" "ds_read_b64_tr_b16 v[32:33], v232 offset:32\n\t" "ds_read_b64_tr_b16 v[34:35], v232 offset:800\n\t"
                     "ds_read_b64_tr_b16 v[36:37], v232 offset:12448\n\t" "ds_read_b64_tr_b16 v[38:39], v232 offset:13216\n\t" "ds_read_b64_tr_b16 v[168:169], v233 offset:64\n\t"
                     "ds_read_b64_tr_b16 v[170:171], v233 offset:832\n\t" "ds_read_b64_tr_b16 v[172:173], v233 offset:12480\n\t" "ds_read_b64_tr_b16 v[174:175], v233 offset:13248\n\t"
                     "ds_read_b64_tr_b16 v[176:177], v232 offset:24832\n\t" "ds_read_b64_tr_b16 v[178:179], v232 offset:25600\n\t" "ds_read_b64_tr_b16 v[180:181], v232 offset:37248\n\t"
                     "ds_read_b64_tr_b16 v[182:183], v232 offset:38016\n\t" "ds_read_b64_tr_b16 v[48:49], v232 offset:24864\n\t" "ds_read_b64_tr_b16 v[50:51], v232 offset:25632\n\t"
                     "ds_read_b64_tr_b16 v[52:53], v232 offset:37280\n\t" "ds_read_b64_tr_b16 v[54:55], v232 offset:38048\n\t" "ds_read_b64_tr_b16 v[184:185], v233 offset:24896\n\t"
                     "ds_read_b64_tr_b16 v[186:187], v233 offset:25664\n\t" "ds_read_b64_tr_b16 v[188:189], v233 offset:37312\n\t" "ds_read_b64_tr_b16 v[190:191], v233 offset:38080\n\t" "s_waitcnt vmcnt(12) lgkmcnt(0)\n\t"
                     "s_barrier\n\t" "s_add_u32 s42, s44, 0x600000\n\t" "s_addc_u32 s43, s45, 0\n\t" "s_add_u32 m0, %18, 0x0\n\t" "s_nop 0\n\t" "global_load_lds_dwordx4 v224, s[42:43]\n\t" "s_add_u32 m0, %18, 0x400\n\t"
                     "s_nop 0\n\t" "global_load_lds_dwordx4 v225, s[42:43]\n\t" "s_add_u32 m0, %18, 0x800\n\t" "s_nop 0\n\t" "global_load_lds_dwordx4 v226, s[42:43]\n\t" "s_add_u32 s42, s44, 0x618000\n\t"
                     "s_addc_u32 s43, s45, 0\n\t" "s_add_u32 m0, %18, 0xc20\n\t" "s_nop 0\n\t" "global_load_lds_dwordx4 v224, s[42:43]\n\t" "s_add_u32 m0, %18, 0x1020\n\t" "s_nop 0\n\t"
                     "global_load_lds_dwordx4 v225, s[42:43]\n\t" "s_add_u32 m0, %18, 0x1420\n\t" "s_nop 0\n\t" "global_load_lds_dwordx4 v226, s[42:43]\n\t" "s_add_u32 s42, s44, 0x6c0000\n\t" "s_addc_u32 s43, s45, 0\n\t"
                     "s_add_u32 m0, %18, 0x6100\n\t" "s_nop 0\n\t" "global_load_lds_dwordx4 v224, s[42:43]\n\t" "s_add_u32 m0, %18, 0x6500\n\t" "s_nop 0\n\t" "global_load_lds_dwordx4 v225, s[42:43]\n\t"
                     "s_add_u32 m0, %18, 0x6900\n\t" "s_nop 0\n\t" "global_load_lds_dwordx4 v226, s[42:43]\n\t" "s_add_u32 s42, s44, 0x6d8000\n\t" "s_addc_u32 s43, s45, 0\n\t" "s_add_u32 m0, %18, 0x6d20\n\t" "s_nop 0\n\t"
                     "global_load_lds_dwordx4 v224, s[42:43]\n\t" "s_add_u32 m0, %18, 0x7120\n\t" "s_nop 0\n\t" "global_load_lds_dwordx4 v225, s[42:43]\n\t" "s_add_u32 m0, %18, 0x7520\n\t" "s_nop 0\n\t"
                     "global_load_lds_dwordx4 v226, s[42:43]\n\t" "global_load_dword %14, %14, %20 sc1\n\t" "v_add_u32 v232, 0xc200, v227\n\t" "v_add_u32 v233, 0xc200, v228\n\t" "ds_read_b64_tr_b16 v[192:193], v232\n\t"
                     "ds_read_b64_tr_b16 v[194:195], v232 offset:768\n\t" "ds_read_b64_tr_b16 v[196:197], v232 offset:12416\n\t" "ds_read_b64_tr_b16 v[198:199], v232 offset:13184\n\t"
                     "ds_read_b64_tr_b16 v[64:65], v232 offset:32\n\t" "ds_read_b64_tr_b16 v[66:67], v232 offset:800\n\t" "ds_read_b64_tr_b16 v[68:69], v232 offset:12448\n\t"
                     "ds_read_b64_tr_b16 v[70:71], v232 offset:13216\n\t" "ds_read_b64_tr_b16 v[200:201], v233 offset:64\n\t" "ds_read_b64_tr_b16 v[202:203], v233 offset:832\n\t"
                     "ds_read_b64_tr_b16 v[204:205], v233 offset:12480\n\t" "ds_read_b64_tr_b16 v[206:207], v233 offset:13248\n\t" "ds_read_b64_tr_b16 v[208:209], v232 offset:24832\n\t"
                     "ds_read_b64_tr_b16 v[210:211], v232 offset:25600\n\t" "ds_read_b64_tr_b16 v[212:213], v232 offset:37248\n\t" "ds_read_b64_tr_b16 v[214:215], v232 offset:38016\n\t"
                     "ds_read_b64_tr_b16 v[80:81], v232 offset:24864\n\t" "ds_read_b64_tr_b16 v[82:83], v232 offset:25632\n\t" "ds_read_b64_tr_b16 v[84:85], v232 offset:37280\n\t"
                     "ds_read_b64_tr_b16 v[86:87], v232 offset:38048\n\t" "ds_read_b64_tr_b16 v[216:217], v233 offset:24896\n\t" "ds_read_b64_tr_b16 v[218:219], v233 offset:25664\n\t"
                     "ds_read_b64_tr_b16 v[220:221], v233 offset:37312\n\t" "ds_read_b64_tr_b16 v[222:223], v233 offset:38080\n\t" "s_waitcnt vmcnt(13) lgkmcnt(0)\n\t" "s_barrier\n\t" "v_add_u32 v232, 0x18400, v227\n\t"
                     "v_add_u32 v233, 0x18400, v228\n\t" "ds_read_b64_tr_b16 a[0:1], v232\n\t" "ds_read_b64_tr_b16 a[2:3], v232 offset:768\n\t" "ds_read_b64_tr_b16 a[4:5], v232 offset:12416\n\t"
                     "ds_read_b64_tr_b16 a[6:7], v232 offset:13184\n\t" "ds_read_b64_tr_b16 v[96:97], v232 offset:32\n\t" "ds_read_b64_tr_b16 v[98:99], v232 offset:800\n\t"
                     "ds_read_b64_tr_b16 v[100:101], v232 offset:12448\n\t" "ds_read_b64_tr_b16 v[102:103], v232 offset:13216\n\t" "ds_read_b64_tr_b16 a[8:9], v233 offset:64\n\t"
                     "ds_read_b64_tr_b16 a[10:11], v233 offset:832\n\t" "ds_read_b64_tr_b16 a[12:13], v233 offset:12480\n\t" "ds_read_b64_tr_b16 a[14:15], v233 offset:13248\n\t"
                     "ds_read_b64_tr_b16 a[16:17], v232 offset:24832\n\t" "ds_read_b64_tr_b16 a[18:19], v232 offset:25600\n\t" "ds_read_b64_tr_b16 a[20:21], v232 offset:37248\n\t"
                     "ds_read_b64_tr_b16 a[22:23], v232 offset:38016\n\t" "ds_read_b64_tr_b16 v[112:113], v232 offset:24864\n\t" "ds_read_b64_tr_b16 v[114:115], v232 offset:25632\n\t"
                     "ds_read_b64_tr_b16 v[116:117], v232 offset:37280\n\t" "ds_read_b64_tr_b16 v[118:119], v232 offset:38048\n\t" "ds_read_b64_tr_b16 a[24:25], v233 offset:24896\n\t"
                     "ds_read_b64_tr_b16 a[26:27], v233 offset:25664\n\t" "ds_read_b64_tr_b16 a[28:29], v233 offset:37312\n\t" "ds_read_b64_tr_b16 a[30:31], v233 offset:38080\n\t" "s_waitcnt vmcnt(0) lgkmcnt(0)\n\t"
                     "s_barrier\n\t" "v_add_u32 v232, 0x0, v227\n\t" "v_add_u32 v233, 0x0, v228\n\t" "ds_read_b64_tr_b16 a[32:33], v232\n\t" "ds_read_b64_tr_b16 a[34:35], v232 offset:768\n\t"
                     "ds_read_b64_tr_b16 a[36:37], v232 offset:12416\n\t" "ds_read_b64_tr_b16 a[38:39], v232 offset:13184\n\t" "ds_read_b64_tr_b16 a[40:41], v232 offset:32\n\t"
                     "ds_read_b64_tr_b16 a[42:43], v232 offset:800\n\t" "ds_read_b64_tr_b16 a[44:45], v232 offset:12448\n\t" "ds_read_b64_tr_b16 a[46:47], v232 offset:13216\n\t"
                     "ds_read_b64_tr_b16 a[48:49], v233 offset:64\n\t" "ds_read_b64_tr_b16 a[50:51], v233 offset:832\n\t" "ds_read_b64_tr_b16 a[52:53], v233 offset:12480\n\t"
                     "ds_read_b64_tr_b16 a[54:55], v233 offset:13248\n\t" "ds_read_b64_tr_b16 a[64:65], v232 offset:24832\n\t" "ds_read_b64_tr_b16 a[66:67], v232 offset:25600\n\t"
                     "ds_read_b64_tr_b16 a[68:69], v232 offset:37248\n\t" "ds_read_b64_tr_b16 a[70:71], v232 offset:38016\n\t" "ds_read_b64_tr_b16 a[72:73], v232 offset:24864\n\t"
                     "ds_read_b64_tr_b16 a[74:75], v232 offset:25632\n\t" "ds_read_b64_tr_b16 a[76:77], v232 offset:37280\n\t" "ds_read_b64_tr_b16 a[78:79], v232 offset:38048\n\t"
                     "ds_read_b64_tr_b16 a[80:81], v233 offset:24896\n\t" "ds_read_b64_tr_b16 a[82:83], v233 offset:25664\n\t" "ds_read_b64_tr_b16 a[84:85], v233 offset:37312\n\t"
                     "ds_read_b64_tr_b16 a[86:87], v233 offset:38080\n\t" "s_waitcnt lgkmcnt(0)\n\t" "s_barrier\n\t" "ds_write_b128 v230, v[0:3]\n\t" "ds_write_b128 v230, v[4:7] offset:1024\n\t"
                     "ds_write_b128 v230, v[8:11] offset:2048\n\t" "ds_write_b128 v230, v[12:15] offset:3072\n\t" "ds_write_b128 v230, v[16:19] offset:4096\n\t" "ds_write_b128 v230, v[20:23] offset:5120\n\t"
                     "ds_write_b128 v230, v[24:27] offset:6144\n\t" "ds_write_b128 v230, v[28:31] offset:7168\n\t" "ds_write_b128 v230, v[32:35] offset:8192\n\t" "ds_write_b128 v230, v[36:39] offset:9216\n\t"
                     "ds_write_b128 v230, v[40:43] offset:10240\n\t" "ds_write_b128 v230, v[44:47] offset:11264\n\t" "ds_write_b128 v230, v[48:51] offset:12288\n\t" "ds_write_b128 v230, v[52:55] offset:13312\n\t"
                     "ds_write_b128 v230, v[56:59] offset:14336\n\t" "ds_write_b128 v230, v[60:63] offset:15360\n\t" "ds_write_b128 v230, v[64:67] offset:16384\n\t" "ds_write_b128 v230, v[68:71] offset:17408\n\t"
                     "ds_write_b128 v230, v[72:75] offset:18432\n\t" "ds_write_b128 v230, v[76:79] offset:19456\n\t" "ds_write_b128 v230, v[80:83] offset:20480\n\t" "ds_write_b128 v230, v[84:87] offset:21504\n\t"
                     "ds_write_b128 v230, v[88:91] offset:22528\n\t" "ds_write_b128 v230, v[92:95] offset:23552\n\t" "ds_write_b128 v230, v[96:99] offset:24576\n\t" "ds_write_b128 v230, v[100:103] offset:25600\n\t"
                     "ds_write_b128 v230, v[104:107] offset:26624\n\t" "ds_write_b128 v230, v[108:111] offset:27648\n\t" "ds_write_b128 v230, v[112:115] offset:28672\n\t" "ds_write_b128 v230, v[116:119] offset:29696\n\t"
                     "ds_write_b128 v230, v[120:123] offset:30720\n\t" "ds_write_b128 v230, v[124:127] offset:31744\n\t"
                     : "=&{a[0:31]}"(wa[0]), "=&{a[32:63]}"(wa[1]), "=&{a[64:95]}"(wa[2]), "=&{a[96:127]}"(wa[3]), "=&{a[128:159]}"(wa[4]), "=&{a[160:191]}"(wa[5]), "=&{a[192:223]}"(wa[6]), "=&{a[224:255]}"(wa[7]), "=&{v[128:159]}"(wv[0]), "=&{v[160:191]}"(wv[1]), "=&{v[192:223]}"(wv[2]), "=&v"(y.r), "=&v"(y.g), "=&v"(y.e), "=&v"(kfl)
                     : "{v[224:231]}"(pa), "{s[44:45]}"(p.wq + h * DH * 2 + (size_t)(16 * w) * (HD * 2)), "{s[46:47]}"(p.wk + h * DH * 2 + (size_t)(16 * w) * (HD * 2)),
                       "s"(lds + 2 * w * GS), "s"(p.ws + OFF_LAT), "s"(fl + (F_K + h) * 128)
                     : "v0", "v1", "v2", "v3", "v4", "v5", "v6", "v7", "v8", "v9", "v10", "v11", "v12", "v13", "v14", "v15", "v16", "v17", "v18", "v19", "v20", "v21", "v22", "v23", "v24", "v25", "v26", "v27", "v28", "v29", "v30", "v31", "v32", "v33", "v34", "v35", "v36", "v37", "v38", "v39", "v40", "v41", "v42", "v43", "v44", "v45", "v46", "v47", "v48", "v49", "v50", "v51", "v52", "v53", "v54", "v55", "v56", "v57", "v58", "v59", "v60", "v61", "v62", "v63", "v64", "v65", "v66", "v67", "v68", "v69", "v70", "v71", "v72", "v73", "v74", "v75", "v76", "v77", "v78", "v79", "v80", "v81", "v82", "v83", "v84", "v85", "v86", "v87", "v88", "v89", "v90", "v91", "v92", "v93", "v94", "v95", "v96", "v97", "v98", "v99", "v100", "v101", "v102", "v103", "v104", "v105", "v106", "v107", "v108", "v109", "v110", "v111", "v112", "v113", "v114", "v115", "v116", "v117", "v118", "v119", "v120", "v121", "v122", "v123", "v124", "v125", "v126", "v127", "v232", "v233", "s42", "s43", "scc", "memory");
        const u64 t = sync_tag(y.e);
        const bool rdy = __builtin_amdgcn_ballot_w64((y.r[0] & ~1u) != (u32)t || y.r[1] != (u32)(t >> 32) || y.g[0] != (u32)t) == 0 && uni((int)kfl) != -1;
        if (rdy) kxdma();
        sd = sync_end(y, p.ws, phi, w == 0);
        if (!rdy) { flag_wait(fl, F_K + h, 1); kxdma(); }
        kxp += NH * 12288;
    }
    const u32 sidx = (lane & 2) ? 0xEEEEu : 0x4444u;
    const u32 moff = lane * 16;
    u32 kxa = kxl - 16 + (n >> 3) * 6160 + (16 * g + 4 * (n & 3) + ((n >> 1) & 2)) * 16;
    const u64 mb0 = 0xffff0000ffff0000ull;
    const int tp = (3 * w + (w & 1)) >> 1, ts = 1 + 3 * (w >> 1);
    u32 qw0 = lds + SL_IMG + tp * 512 + lane * 8, qw1 = lds + SL_IMG + ts * 512 + lane * 8 + 4 * (w & 1);
    const u32 kw0 = lds + SL_IMG + 3072 + w * 288 + n * 16 + 4 * g;
    const u32 qr0 = lds + SL_IMG + (g >> 1) * 512 + w * 128 + 64 * (g & 1) + 8 * qq;
    const u32 kr0 = lds + SL_IMG + 3072 + (qq >> 1) * 288 + g * 64 + (qq & 1) * 16 + 8 * (pp & 1);
    const u32 xso = 32768 + (h >> 4) * 16384 + (h & 15) * 512 + (w >> 1) * 256 + (n >> 3) * 64 + (n & 4) * 32 + (w & 1) * 32 + (g & 1) * 16 + (n & 3) * 4 + (g >> 1) * 8192;
    const u32 pso = (n < 8 ? 20480 + (h >> 1) * 256 + (n & 2) * 16 + (h & 1) * 8 + (n & 4) : 18432 + (h >> 2) * 256 + (h & 1) * 8 + (h & 2) * 2) + w * 64 + (g & 1) * 16 + (g >> 1) * 8192;
    const u64 psm = 0x01aa01aa01aa01aaull;
    const u32 kmo = (u32)(((n >> 3) * 4 + g) * 128 + (n & 7) * 16);
    u32x16 ad = u32x16{moff, sidx, lfa, kxa, qw0, qw1, kw0, kr0 + 1152 * min(w, 2) + (pp >> 1) * 576, qr0, kr0, xso, pso, 0x3dd53b94u, kmo, kmo, kmo};
    asm volatile("" : "+{v[224:239]}"(ad));
    const char* memp = p.ws + mslot(sd, 0);
    char* kmp = p.ws + OFF_KM + h * 3072 + min(w, 2) * 1024;
    const float kscale = 0.07216878364870322f;
    int budget = 30000;
    Clock clk; clk.init();
    u32x32 wk[3];
    asm volatile("" : "={v[64:95]}"(wk[0]), "={v[96:127]}"(wk[1]), "={v[32:63]}"(wk[2]));
    const int kfo = (F_K + 32 + h) * 128;
    for (int s = 0; s < NSEG; ++s) {
        int tries = 0;
        u64 bad, tp8, en = ~0ull;
        u32 tt;
        int kf;
        while (true) {
            const u32 tgt = clk.sched(phi, 0, cfg.G, tries ? 2 : 0);
            asm volatile("s_cmp_lg_u32 %12, 0\n\t" "s_cbranch_scc1 .Lrt_s\n\t" "s_waitcnt vmcnt(0) lgkmcnt(0)\n\t" "s_barrier\n\t" "s_cmp_lg_u32 %13, 0\n\t" "s_cbranch_scc1 .Lsp_s\n\t" ".Lrt_s:\n\t"
                         "ds_read_b128 v[64:67], v226\n\t" "v_mov_b64 v[40:41], 0\n\t" "ds_read_b128 v[68:71], v226 offset:1024\n\t" "v_mov_b64 v[42:43], 0\n\t" "ds_read_b128 v[72:75], v226 offset:2048\n\t"
                         "v_mov_b64 v[44:45], 0\n\t" "ds_read_b128 v[76:79], v226 offset:3072\n\t" "v_mov_b64 v[46:47], 0\n\t" "ds_read_b128 v[80:83], v226 offset:4096\n\t" "v_mov_b64 v[48:49], 0\n\t"
                         "ds_read_b128 v[84:87], v226 offset:5120\n\t" "v_mov_b64 v[50:51], 0\n\t" "ds_read_b128 v[88:91], v226 offset:6144\n\t" "v_mov_b64 v[52:53], 0\n\t" "ds_read_b128 v[92:95], v226 offset:7168\n\t"
                         "v_mov_b64 v[54:55], 0\n\t" "ds_read_b128 v[96:99], v226 offset:8192\n\t" "v_mov_b64 v[56:57], 0\n\t" "ds_read_b128 v[100:103], v226 offset:9216\n\t" "v_mov_b64 v[58:59], 0\n\t"
                         "ds_read_b128 v[104:107], v226 offset:10240\n\t" "v_mov_b64 v[60:61], 0\n\t" "ds_read_b128 v[108:111], v226 offset:11264\n\t" "v_mov_b64 v[62:63], 0\n\t"
                         "ds_read_b128 v[112:115], v226 offset:12288\n\t" "ds_read_b128 v[116:119], v226 offset:13312\n\t" "ds_read_b128 v[120:123], v226 offset:14336\n\t" "ds_read_b128 v[124:127], v226 offset:15360\n\t"
                         ".Lsp_s:\n\t" "s_memtime vcc\n\t" "s_waitcnt lgkmcnt(0)\n\t" "s_sub_i32 s42, %4, vcc_lo\n\t" "s_cmp_gt_i32 s42, 80\n\t" "s_cbranch_scc1 .Lsp_s\n\t" "s_max_i32 s42, s42, 0\n\t"
                         "s_sub_i32 s42, 92, s42\n\t" "s_and_b32 s42, s42, -4\n\t" "s_getpc_b64 vcc\n\t" "s_add_u32 vcc_lo, vcc_lo, s42\n\t" "s_addc_u32 vcc_hi, vcc_hi, 0\n\t" "s_setpc_b64 vcc\n\t" "s_nop 0\n\t" "s_nop 0\n\t"
                         "s_nop 0\n\t" "s_nop 0\n\t" "s_nop 0\n\t" "s_nop 0\n\t" "s_nop 0\n\t" "s_nop 0\n\t" "s_nop 0\n\t" "s_nop 0\n\t" "s_nop 0\n\t" "s_nop 0\n\t" "s_nop 0\n\t" "s_nop 0\n\t" "s_nop 0\n\t" "s_nop 0\n\t"
                         "s_nop 0\n\t" "s_nop 0\n\t" "s_nop 0\n\t" "s_nop 0\n\t" "global_load_dwordx4 v[0:3], v224, %5 offset:-4096 sc1\n\t" "s_nop 10\n\t" "global_load_dwordx4 v[4:7], v224, %5 offset:-3072 sc1\n\t"
                         "s_nop 10\n\t" "global_load_dwordx4 v[8:11], v224, %5 offset:-2048 sc1\n\t" "s_nop 10\n\t" "global_load_dwordx4 v[12:15], v224, %5 offset:-1024 sc1\n\t" "s_nop 10\n\t"
                         "global_load_dwordx4 v[16:19], v224, %5 sc1\n\t" "s_nop 10\n\t" "global_load_dwordx4 v[20:23], v224, %5 offset:1024 sc1\n\t" "s_nop 10\n\t" "global_load_dwordx4 v[24:27], v224, %5 offset:2048 sc1\n\t"
                         "s_nop 10\n\t" "global_load_dwordx4 v[28:31], v224, %5 offset:3072 sc1\n\t" "s_nop 10\n\t" "global_load_dwordx4 v[32:35], v224, %6 offset:-4096 sc1\n\t" "s_nop 10\n\t"
                         "global_load_dwordx4 v[36:39], v224, %6 offset:-3072 sc1\n\t" "s_waitcnt vmcnt(9)\n\t" "v_smfmac_f32_16x16x64_bf16 v[40:43], v[0:3], v[128:135], v225\n\t"
                         "v_smfmac_f32_16x16x64_bf16 v[44:47], v[0:3], v[64:71], v225\n\t" "ds_read_b128 v[64:67], v226 offset:16384\n\t" "v_smfmac_f32_16x16x64_bf16 v[48:51], v[0:3], v[136:143], v225\n\t"
                         "ds_read_b128 v[68:71], v226 offset:17408\n\t" "v_smfmac_f32_16x16x64_bf16 v[52:55], v[0:3], v[72:79], v225\n\t" "ds_read_b128 v[72:75], v226 offset:18432\n\t" "s_waitcnt vmcnt(8)\n\t"
                         "v_smfmac_f32_16x16x64_bf16 v[40:43], v[4:7], v[144:151], v225\n\t" "ds_read_b128 v[76:79], v226 offset:19456\n\t" "v_smfmac_f32_16x16x64_bf16 v[44:47], v[4:7], v[80:87], v225\n\t"
                         "ds_read_b128 v[80:83], v226 offset:20480\n\t" "v_smfmac_f32_16x16x64_bf16 v[48:51], v[4:7], v[152:159], v225\n\t" "ds_read_b128 v[84:87], v226 offset:21504\n\t"
                         "v_smfmac_f32_16x16x64_bf16 v[52:55], v[4:7], v[88:95], v225\n\t" "ds_read_b128 v[88:91], v226 offset:22528\n\t" "s_waitcnt vmcnt(7)\n\t"
                         "v_smfmac_f32_16x16x64_bf16 v[40:43], v[8:11], v[160:167], v225\n\t" "ds_read_b128 v[92:95], v226 offset:23552\n\t" "v_max_u32 v238, v0, v4\n\t"
                         "v_smfmac_f32_16x16x64_bf16 v[44:47], v[8:11], v[96:103], v225\n\t" "ds_read_b128 v[96:99], v226 offset:24576\n\t" "v_cmp_eq_u32 vcc, -1, v238\n\t"
                         "v_smfmac_f32_16x16x64_bf16 v[48:51], v[8:11], v[168:175], v225\n\t" "ds_read_b128 v[100:103], v226 offset:25600\n\t" "v_smfmac_f32_16x16x64_bf16 v[52:55], v[8:11], v[104:111], v225\n\t"
                         "s_and_b64 vcc, vcc, %11\n\t" "s_cbranch_scc1 .Lsk_s\n\t" "ds_read_b128 v[104:107], v226 offset:26624\n\t" "s_waitcnt vmcnt(6)\n\t" "v_smfmac_f32_16x16x64_bf16 v[40:43], v[12:15], v[176:183], v225\n\t"
                         "ds_read_b128 v[108:111], v226 offset:27648\n\t" "v_smfmac_f32_16x16x64_bf16 v[44:47], v[12:15], v[112:119], v225\n\t" "ds_read_b128 v[112:115], v226 offset:28672\n\t"
                         "v_smfmac_f32_16x16x64_bf16 v[48:51], v[12:15], v[184:191], v225\n\t" "ds_read_b128 v[116:119], v226 offset:29696\n\t" "v_smfmac_f32_16x16x64_bf16 v[52:55], v[12:15], v[120:127], v225\n\t"
                         "ds_read_b128 v[120:123], v226 offset:30720\n\t" "s_waitcnt vmcnt(5)\n\t" "v_smfmac_f32_16x16x64_bf16 v[40:43], v[16:19], v[192:199], v225\n\t" "ds_read_b128 v[124:127], v226 offset:31744\n\t"
                         "s_waitcnt lgkmcnt(14)\n\t" "v_smfmac_f32_16x16x64_bf16 v[44:47], v[16:19], v[64:71], v225\n\t" "ds_read_b128 v[64:67], v227\n\t" "v_smfmac_f32_16x16x64_bf16 v[48:51], v[16:19], v[200:207], v225\n\t"
                         "ds_read_b128 v[68:71], v227 offset:1024\n\t" "s_waitcnt lgkmcnt(14)\n\t" "v_smfmac_f32_16x16x64_bf16 v[52:55], v[16:19], v[72:79], v225\n\t" "ds_read_b128 v[72:75], v227 offset:16\n\t"
                         "s_waitcnt vmcnt(4)\n\t" "v_smfmac_f32_16x16x64_bf16 v[40:43], v[20:23], v[208:215], v225\n\t" "ds_read_b128 v[76:79], v227 offset:1040\n\t" "s_waitcnt lgkmcnt(14)\n\t"
                         "v_smfmac_f32_16x16x64_bf16 v[44:47], v[20:23], v[80:87], v225\n\t" "ds_read_b128 v[80:83], v227 offset:2048\n\t" "v_smfmac_f32_16x16x64_bf16 v[48:51], v[20:23], v[216:223], v225\n\t"
                         "ds_read_b128 v[84:87], v227 offset:3072\n\t" "s_waitcnt lgkmcnt(14)\n\t" "v_smfmac_f32_16x16x64_bf16 v[52:55], v[20:23], v[88:95], v225\n\t" "ds_read_b128 v[88:91], v227 offset:2064\n\t"
                         "s_waitcnt vmcnt(3)\n\t" "v_smfmac_f32_16x16x64_bf16 v[40:43], v[24:27], a[0:7], v225\n\t" "ds_read_b128 v[92:95], v227 offset:3088\n\t" "s_waitcnt lgkmcnt(14)\n\t"
                         "v_smfmac_f32_16x16x64_bf16 v[44:47], v[24:27], v[96:103], v225\n\t" "ds_read_b128 v[96:99], v227 offset:4096\n\t" "v_smfmac_f32_16x16x64_bf16 v[48:51], v[24:27], a[8:15], v225\n\t"
                         "ds_read_b128 v[100:103], v227 offset:5120\n\t" "s_waitcnt lgkmcnt(14)\n\t" "v_smfmac_f32_16x16x64_bf16 v[52:55], v[24:27], v[104:111], v225\n\t" "ds_read_b128 v[104:107], v227 offset:4112\n\t"
                         "s_waitcnt vmcnt(2)\n\t" "v_smfmac_f32_16x16x64_bf16 v[40:43], v[28:31], a[16:23], v225\n\t" "ds_read_b128 v[108:111], v227 offset:5136\n\t" "s_waitcnt lgkmcnt(14)\n\t"
                         "v_smfmac_f32_16x16x64_bf16 v[44:47], v[28:31], v[112:119], v225\n\t" "v_smfmac_f32_16x16x64_bf16 v[48:51], v[28:31], a[24:31], v225\n\t" "s_waitcnt lgkmcnt(12)\n\t"
                         "v_smfmac_f32_16x16x64_bf16 v[52:55], v[28:31], v[120:127], v225\n\t" "s_waitcnt vmcnt(1)\n\t" "v_smfmac_f32_16x16x64_bf16 v[40:43], v[32:35], a[32:39], v225\n\t" "v_mov_b64 v[112:113], 0\n\t"
                         "v_smfmac_f32_16x16x64_bf16 v[44:47], v[32:35], a[40:47], v225\n\t" "v_mov_b64 v[114:115], 0\n\t" "v_smfmac_f32_16x16x64_bf16 v[48:51], v[32:35], a[48:55], v225\n\t" "v_mov_b64 v[116:117], 0\n\t"
                         "v_smfmac_f32_16x16x64_bf16 v[52:55], v[32:35], a[56:63], v225\n\t" "v_mov_b64 v[118:119], 0\n\t" "s_waitcnt vmcnt(0)\n\t" "v_smfmac_f32_16x16x64_bf16 v[40:43], v[36:39], a[64:71], v225\n\t"
                         "v_mov_b64 v[120:121], 0\n\t" "v_smfmac_f32_16x16x64_bf16 v[44:47], v[36:39], a[72:79], v225\n\t" "v_mov_b64 v[122:123], 0\n\t" "v_smfmac_f32_16x16x64_bf16 v[48:51], v[36:39], a[80:87], v225\n\t"
                         "v_mov_b64 v[124:125], 0\n\t" "v_smfmac_f32_16x16x64_bf16 v[52:55], v[36:39], a[88:95], v225\n\t" "v_add_f32 v40, v40, v42\n\t" "v_add_f32 v41, v41, v43\n\t"
                         "v_smfmac_f32_16x16x64_bf16 v[56:59], v[0:3], a[96:103], v225\n\t" "v_cmp_u_f32 vcc, v40, v41\n\t" "v_smfmac_f32_16x16x64_bf16 v[56:59], v[4:7], a[104:111], v225\n\t"
                         "v_cvt_pk_bf16_f32 v42, v40, v41\n\t" "v_add_f32 v44, v44, v46\n\t" "v_smfmac_f32_16x16x64_bf16 v[56:59], v[8:11], a[112:119], v225\n\t" "s_and_b64 vcc, vcc, %11\n\t" "s_cbranch_scc1 .Lsk_s\n\t"
                         "v_smfmac_f32_16x16x64_bf16 v[56:59], v[12:15], a[120:127], v225\n\t" "v_add_f32 v45, v45, v47\n\t" "v_cvt_pk_bf16_f32 v43, v44, v45\n\t"
                         "v_smfmac_f32_16x16x64_bf16 v[56:59], v[16:19], a[128:135], v225\n\t" "ds_write_b64 v228, v[42:43]\n\t" "v_add_f32 v48, v48, v50\n\t"
                         "v_smfmac_f32_16x16x64_bf16 v[56:59], v[20:23], a[136:143], v225\n\t" "v_add_f32 v49, v49, v51\n\t" "v_cvt_pk_bf16_f32 v46, v48, v49\n\t"
                         "v_smfmac_f32_16x16x64_bf16 v[56:59], v[24:27], a[144:151], v225\n\t" "ds_write_b32 v229, v46\n\t" "v_add_f32 v52, v52, v54\n\t" "v_smfmac_f32_16x16x64_bf16 v[56:59], v[28:31], a[152:159], v225\n\t"
                         "v_add_f32 v53, v53, v55\n\t" "v_cvt_pk_bf16_f32 v54, v52, v53\n\t" "v_smfmac_f32_16x16x64_bf16 v[56:59], v[32:35], a[160:167], v225\n\t" "ds_write_b32 v230, v54\n\t"
                         "v_smfmac_f32_16x16x64_bf16 v[56:59], v[36:39], a[168:175], v225\n\t" "v_smfmac_f32_16x16x64_bf16 v[60:63], v[0:3], a[176:183], v225\n\t"
                         "v_smfmac_f32_16x16x64_bf16 v[60:63], v[4:7], a[184:191], v225\n\t" "v_add_f32 v56, v56, v58\n\t" "v_smfmac_f32_16x16x64_bf16 v[60:63], v[8:11], a[192:199], v225\n\t" "s_waitcnt lgkmcnt(0)\n\t"
                         "s_barrier\n\t" "v_smfmac_f32_16x16x64_bf16 v[60:63], v[12:15], a[200:207], v225\n\t" "ds_read_b64_tr_b16 v[40:41], v232\n\t" "v_add_f32 v57, v57, v59\n\t"
                         "v_smfmac_f32_16x16x64_bf16 v[60:63], v[16:19], a[208:215], v225\n\t" "ds_read_b64_tr_b16 v[42:43], v232 offset:32\n\t" "v_cvt_pk_bf16_f32 v58, v56, v57\n\t"
                         "v_smfmac_f32_16x16x64_bf16 v[60:63], v[20:23], a[216:223], v225\n\t" "ds_write_b32 v230, v58 offset:1152\n\t" "v_smfmac_f32_16x16x64_bf16 v[60:63], v[24:27], a[224:231], v225\n\t"
                         "ds_read_b64_tr_b16 v[44:45], v232 offset:1024\n\t" "ds_read_b64_tr_b16 v[46:47], v232 offset:1056\n\t" "v_smfmac_f32_16x16x64_bf16 v[60:63], v[28:31], a[232:239], v225\n\t"
                         "ds_read_b64_tr_b16 v[48:49], v232 offset:2048\n\t" "ds_read_b64_tr_b16 v[50:51], v232 offset:2080\n\t" "v_smfmac_f32_16x16x64_bf16 v[60:63], v[32:35], a[240:247], v225\n\t"
                         "ds_read_b64_tr_b16 v[0:1], v233\n\t" "ds_read_b64_tr_b16 v[2:3], v233 offset:32\n\t" "v_smfmac_f32_16x16x64_bf16 v[60:63], v[36:39], a[248:255], v225\n\t"
                         "ds_read_b64_tr_b16 v[4:5], v233 offset:576\n\t" "ds_read_b64_tr_b16 v[6:7], v233 offset:608\n\t" "s_waitcnt lgkmcnt(9)\n\t" "v_smfmac_f32_16x16x64_bf16 v[112:115], v[40:43], v[64:71], v225\n\t"
                         "s_waitcnt lgkmcnt(8)\n\t" "s_barrier\n\t" "v_smfmac_f32_16x16x64_bf16 v[116:119], v[40:43], v[72:79], v225\n\t" "ds_read_b64_tr_b16 v[8:9], v233 offset:1152\n\t" "s_waitcnt lgkmcnt(7)\n\t"
                         "v_add_f32 v60, v60, v62\n\t" "v_add_f32 v61, v61, v63\n\t" "v_smfmac_f32_16x16x64_bf16 v[112:115], v[44:47], v[80:87], v225\n\t" "ds_read_b64_tr_b16 v[10:11], v233 offset:1184\n\t"
                         "v_cvt_pk_bf16_f32 v62, v60, v61\n\t" "v_smfmac_f32_16x16x64_bf16 v[116:119], v[44:47], v[88:95], v225\n\t" "ds_write_b32 v230, v62 offset:2304\n\t" "s_waitcnt lgkmcnt(7)\n\t"
                         "v_smfmac_f32_16x16x64_bf16 v[112:115], v[48:51], v[96:103], v225\n\t" "ds_read_b64_tr_b16 v[12:13], v233 offset:1728\n\t" "v_smfmac_f32_16x16x64_bf16 v[116:119], v[48:51], v[104:111], v225\n\t"
                         "ds_read_b64_tr_b16 v[14:15], v233 offset:1760\n\t" "s_waitcnt lgkmcnt(5)\n\t" "v_smfmac_f32_16x16x64_bf16 v[120:123], v[40:43], v[0:7], v225\n\t" "v_cndmask_b32 v24, v112, v113, %8\n\t"
                         "v_cndmask_b32 v25, v114, v115, %8\n\t" "v_add_f32 v24, v24, v25\n\t" "v_pk_add_f32 v[26:27], v[116:117], v[118:119]\n\t" "v_cndmask_b32 v25, v26, v27, %8\n\t" "v_max_f32 v26, v24, v25\n\t"
                         "s_waitcnt lgkmcnt(2)\n\t" "s_barrier\n\t" "ds_read_b64_tr_b16 v[16:17], v233 offset:2304\n\t" "v_max_f32_dpp v27, v26, v26 row_ror:4 row_mask:0xf bank_mask:0xf\n\t"
                         "ds_read_b64_tr_b16 v[18:19], v233 offset:2336\n\t" "v_max_f32_dpp v27, v26, v27 row_ror:8 row_mask:0xf bank_mask:0xf\n\t" "v_max_f32_dpp v27, v26, v27 row_ror:12 row_mask:0xf bank_mask:0xf\n\t"
                         "ds_read_b64_tr_b16 v[20:21], v233 offset:2880\n\t" "ds_read_b64_tr_b16 v[22:23], v233 offset:2912\n\t" "v_max_f32_dpp v30, v27, v27 row_ror:1 row_mask:0xf bank_mask:0xf\n\t"
                         "v_max_f32_dpp v30, v27, v30 row_ror:2 row_mask:0xf bank_mask:0xf\n\t" "v_max_f32_dpp v30, v27, v30 row_ror:3 row_mask:0xf bank_mask:0xf\n\t" "s_waitcnt lgkmcnt(4)\n\t"
                         "v_pk_add_f32 v[24:25], v[24:25], v[30:31] op_sel_hi:[1,0] neg_lo:[0,1] neg_hi:[0,1]\n\t" "v_pk_mul_f32 v[24:25], v[24:25], v[236:237] op_sel_hi:[1,0]\n\t" "v_exp_f32 v24, v24\n\t"
                         "v_exp_f32 v25, v25\n\t" "v_mul_f32 v31, %10, v30\n\t" "v_cvt_pk_bf16_f32 v28, |v24|, |v25|\n\t" "v_smfmac_f32_16x16x64_bf16 v[120:123], v[44:47], v[8:15], v225\n\t"
                         "global_store_dword v234, v28, %7 offset:-4096 sc1\n\t" "s_waitcnt lgkmcnt(0)\n\t" "v_add_f32 v26, v24, v25\n\t" "v_smfmac_f32_16x16x64_bf16 v[120:123], v[48:51], v[16:23], v225\n\t"
                         "s_nop 0\n\t" "v_add_f32_dpp v27, v26, v26 row_ror:4 row_mask:0xf bank_mask:0xf\n\t" "v_add_f32_dpp v27, v26, v27 row_ror:8 row_mask:0xf bank_mask:0xf\n\t"
                         "v_add_f32_dpp v27, v26, v27 row_ror:12 row_mask:0xf bank_mask:0xf\n\t" "s_nop 1\n\t" "v_add_f32_dpp v29, v27, v27 row_ror:1 row_mask:0xf bank_mask:0xf\n\t"
                         "v_pk_add_f32 v[24:25], v[120:121], v[122:123]\n\t" "v_cndmask_b32 v24, v24, v25, %8\n\t" "v_max_f32 v26, v24, v30\n\t" "v_add_f32_dpp v29, v27, v29 row_ror:2 row_mask:0xf bank_mask:0xf\n\t"
                         "v_add_f32_dpp v29, v27, v29 row_ror:3 row_mask:0xf bank_mask:0xf\n\t" "v_max_f32_dpp v27, v26, v26 row_ror:1 row_mask:0xf bank_mask:0xf\n\t"
                         "v_max_f32_dpp v27, v26, v27 row_ror:2 row_mask:0xf bank_mask:0xf\n\t" "v_max_f32_dpp v27, v26, v27 row_ror:3 row_mask:0xf bank_mask:0xf\n\t" "v_mul_f32 v24, %10, v24\n\t"
                         "v_mul_f32 v29, 0x3e000000, v29\n\t" "v_max_f32_dpp v26, v27, v27 row_ror:4 row_mask:0xf bank_mask:0xf\n\t" "v_fma_f32 v24, -v26, %10, v24\n\t" "v_fma_f32 v25, -v26, %10, v31\n\t"
                         "v_exp_f32 v24, v24\n\t" "v_exp_f32 v25, v25\n\t" "s_nop 0\n\t" "v_fma_f32 v26, v25, v29, v24\n\t" "v_mov_b32_dpp v124, v24 quad_perm:[0,1,2,3] row_mask:0xf bank_mask:0x3\n\t"
                         "v_mov_b32_dpp v125, v24 quad_perm:[1,0,3,2] row_mask:0xf bank_mask:0x3\n\t" "v_add_f32_dpp v27, v26, v26 row_ror:1 row_mask:0xf bank_mask:0xf\n\t"
                         "v_add_f32_dpp v27, v26, v27 row_ror:2 row_mask:0xf bank_mask:0xf\n\t" "v_add_f32_dpp v27, v26, v27 row_ror:3 row_mask:0xf bank_mask:0xf\n\t"
                         "v_mov_b32_dpp v124, v25 quad_perm:[0,1,2,3] row_mask:0x5 bank_mask:0x4\n\t" "v_mov_b32_dpp v125, v25 quad_perm:[0,1,2,3] row_mask:0xa bank_mask:0x4\n\t"
                         "v_add_f32_dpp v26, v27, v27 row_ror:4 row_mask:0xf bank_mask:0xf\n\t" "v_rcp_f32 v26, v26\n\t" "s_mov_b64 exec, %9\n\t" "v_pk_mul_f32 v[124:125], v[124:125], v[26:27] op_sel_hi:[1,0]\n\t"
                         "v_cvt_pk_bf16_f32 v24, |v124|, |v125|\n\t" "global_store_dword v235, v24, %7 offset:-4096 sc1\n\t" "s_mov_b64 exec, -1\n\t" "s_memrealtime %1\n\t" "s_load_dword %2, %14, %13 offset:%15\n\t"
                         "ds_read_b64_tr_b16 v[36:37], v231\n\t" "ds_read_b64_tr_b16 v[38:39], v231 offset:32\n\t" "v_readfirstlane_b32 %3, v237\n\t" "s_mov_b32 m0, %17\n\t" "v_mov_b64 v[40:41], 0\n\t"
                         "v_mov_b64 v[42:43], 0\n\t" "v_mov_b64 v[44:45], 0\n\t" "v_mov_b64 v[46:47], 0\n\t" "v_mov_b64 v[48:49], 0\n\t" "v_mov_b64 v[50:51], 0\n\t" "v_mov_b64 v[52:53], 0\n\t" "v_mov_b64 v[54:55], 0\n\t"
                         "v_mov_b64 v[56:57], 0\n\t" "v_mov_b64 v[58:59], 0\n\t" "v_mov_b64 v[60:61], 0\n\t" "v_mov_b64 v[62:63], 0\n\t" "v_mov_b32 v237, %18\n\t" "s_waitcnt lgkmcnt(0)\n\t" "ds_read_b128 v[64:67], v226\n\t"
                         "ds_read_b128 v[68:71], v226 offset:1024\n\t" "ds_read_b128 v[72:75], v226 offset:2048\n\t" "ds_read_b128 v[76:79], v226 offset:3072\n\t" "ds_read_b128 v[80:83], v226 offset:4096\n\t"
                         "ds_read_b128 v[84:87], v226 offset:5120\n\t" "s_cmp_eq_u32 %3, -1\n\t" "s_cbranch_scc1 .Lnk_s\n\t" "global_load_lds_dwordx4 v224, %16 sc1\n\t" "global_load_lds_dwordx4 v224, %16 offset:1024 sc1\n\t"
                         "global_load_lds_dwordx4 v224, %16 offset:2048 sc1\n\t" ".Lnk_s:\n\t" "global_store_dwordx4 v239, v[36:39], %21 sc1\n\t" "s_and_b64 exec, %19, 1\n\t" "global_load_dword v237, v237, %20 sc1\n\t"
                         "s_mov_b64 exec, -1\n\t" "ds_read_b128 v[88:91], v226 offset:6144\n\t" "ds_read_b128 v[92:95], v226 offset:7168\n\t" "ds_read_b128 v[96:99], v226 offset:8192\n\t"
                         "ds_read_b128 v[100:103], v226 offset:9216\n\t" "ds_read_b128 v[104:107], v226 offset:10240\n\t" "ds_read_b128 v[108:111], v226 offset:11264\n\t" "ds_read_b128 v[112:115], v226 offset:12288\n\t"
                         "ds_read_b128 v[116:119], v226 offset:13312\n\t" "ds_read_b128 v[120:123], v226 offset:14336\n\t" "ds_read_b128 v[124:127], v226 offset:15360\n\t" ".Lsk_s:\n\t" "s_mov_b64 %0, vcc\n\t"
                         : "=&s"(bad), "=&s"(tp8), "=&s"(tt), "=&s"(kf)
                         : "s"(uni((int)tgt)), "s"(memp), "s"(memp + 16384), "s"((u64)memp & ~8192ull), "s"(mb0), "s"(psm), "s"(kscale * 1.4426950408889634f), "s"(en), "s"(tries), "s"(4 * s),
                           "s"(__builtin_amdgcn_kernarg_segment_ptr()), "n"(TT_OFF), "s"(kxp), "s"(kxw), "s"(kfo + (s < 6 ? 0 : (s + 2) >> 3) * 4096), "s"(0x0040404040404044ull >> s), "s"(fl), "s"(kmp),
                           "{v[224:239]}"(ad), "{v[64:95]}"(wk[0]), "{v[96:127]}"(wk[1]), "{v[32:63]}"(wk[2]),
                           "{a[0:31]}"(wa[0]), "{a[32:63]}"(wa[1]), "{a[64:95]}"(wa[2]), "{a[96:127]}"(wa[3]), "{a[128:159]}"(wa[4]), "{a[160:191]}"(wa[5]), "{a[192:223]}"(wa[6]), "{a[224:255]}"(wa[7]), "{v[128:159]}"(wv[0]), "{v[160:191]}"(wv[1]), "{v[192:223]}"(wv[2])
                         : "v0", "v1", "v2", "v3", "v4", "v5", "v6", "v7", "v8", "v9", "v10", "v11", "v12", "v13", "v14", "v15", "v16", "v17", "v18", "v19", "v20", "v21", "v22", "v23", "v24", "v25", "v26", "v27", "v28", "v29", "v30", "v31", "vcc", "scc", "s42", "memory");
            if (__builtin_expect(bad == 0, 1)) break;
            waitvm<0>();
            if (((++tries & 15) == 0 && !unwritten(memp, ad[0])) || --budget < 0) en = 0;
            phi += (u32)cfg.G;
        }
        if (__builtin_expect(kf == -1, 0)) { flag_wait(fl, F_K + pblk(s + 1) * 32 + h, 1); kxdma(); }
        kxp += s < NSEG - 2 ? NH * 12288 : 0;
        clk.calib();
        if (__builtin_expect(s >= NSEG - 2, 0)) {
            waitvm<0>();
            barrier();
            if (w == 3) flag_add(fl, F_PROG + 8 * s + (h & 7), 1);
        }
        follow(phi, (int)(tt >> 16) - VISS, (u32)tp8, cfg.G);
        phi += tt & 0xffffu;
        memp = p.ws + mslot(sd, s + 1); kmp += NH * 3072;
    }
    if (h == 0 && w == 0) {
        flag_wait(fl, F_ACK, 256);
        Sync y;
        sync_ld(y, p.ws);
        asm volatile("s_waitcnt vmcnt(0)" : "+v"(y.r), "+v"(y.g), "+v"(y.e) : : "memory");
        const u64 t = ((u64)y.e[1] << 32 | y.e[0]) + 1;
        if (lanex() == 0) gst8(p.ws + OFF_LAT, 1024, u32x2{(u32)t, (u32)(t >> 32)});
    }
    asm volatile(".Lkm_last:\n\ts_waitcnt vmcnt(0)" : : : "memory");
}

DI void proj_role(const Ptrs& p, const ChainCfg cfg, int h, u32 lds, int w) {
    u32x32 wa[8], wv[4];
    u32 phi;
    int sd;
    {
        const int lane = lanex(), tid = 64 * w + lane;
        const int g = lane >> 4, qq = (lane >> 2) & 3, pp = lane & 3;
        constexpr int GS = 3104;
        auto dvo = [&](int i) { const int ix = i * 64 + lane; return (u32)((ix / 24) * (HD * 2) + (ix % 24) * 16); };
        auto ovo = [&](int i) { const int u = i * 64 + lane, r = u / 80; return (u32)(r * 1280 + (((u % 80) ^ (2 * (r ^ (w & 2)))) << 4)); };
        const u32 tra = lds + (2 * (g >> 1) + (qq >> 1)) * GS + (4 * (g & 1) + (qq & 1)) * 384 + 96 * w + 32 * (w & 1) + 8 * pp;
        const u32 tro = (4 * g + (qq & 1) + 16 * (qq >> 1)) * 1280 + 8 * pp + ((u32)(2 * w ^ 2 * ((4 * g + qq) & 7)) << 4);
        u32x16 pa = u32x16{dvo(0), dvo(1), dvo(2), tra, tra - 96 * (w & 1), tro, lds + (u32)((w & 1) * 24576 + (w >> 1) * 99328 + lane * 16), (u32)(lane & 31) * 8,
                           ovo(0), ovo(1), ovo(2), ovo(3), ovo(4), tro, tro, tro};
        Sync y;
        asm volatile("s_nop 4\n\t" "s_add_u32 s42, s44, 0x0\n\t" "s_addc_u32 s43, s45, 0\n\t" "s_add_u32 m0, %18, 0x0\n\t" "s_nop 0\n\t" "global_load_lds_dwordx4 v224, s[42:43]\n\t" "s_add_u32 m0, %18, 0x400\n\t"
                     "s_nop 0\n\t" "global_load_lds_dwordx4 v225, s[42:43]\n\t" "s_add_u32 m0, %18, 0x800\n\t" "s_nop 0\n\t" "global_load_lds_dwordx4 v226, s[42:43]\n\t" "s_add_u32 s42, s44, 0x18000\n\t"
                     "s_addc_u32 s43, s45, 0\n\t" "s_add_u32 m0, %18, 0xc20\n\t" "s_nop 0\n\t" "global_load_lds_dwordx4 v224, s[42:43]\n\t" "s_add_u32 m0, %18, 0x1020\n\t" "s_nop 0\n\t"
                     "global_load_lds_dwordx4 v225, s[42:43]\n\t" "s_add_u32 m0, %18, 0x1420\n\t" "s_nop 0\n\t" "global_load_lds_dwordx4 v226, s[42:43]\n\t" "s_add_u32 s42, s44, 0xc0000\n\t" "s_addc_u32 s43, s45, 0\n\t"
                     "s_add_u32 m0, %18, 0x6100\n\t" "s_nop 0\n\t" "global_load_lds_dwordx4 v224, s[42:43]\n\t" "s_add_u32 m0, %18, 0x6500\n\t" "s_nop 0\n\t" "global_load_lds_dwordx4 v225, s[42:43]\n\t"
                     "s_add_u32 m0, %18, 0x6900\n\t" "s_nop 0\n\t" "global_load_lds_dwordx4 v226, s[42:43]\n\t" "s_add_u32 s42, s44, 0xd8000\n\t" "s_addc_u32 s43, s45, 0\n\t" "s_add_u32 m0, %18, 0x6d20\n\t" "s_nop 0\n\t"
                     "global_load_lds_dwordx4 v224, s[42:43]\n\t" "s_add_u32 m0, %18, 0x7120\n\t" "s_nop 0\n\t" "global_load_lds_dwordx4 v225, s[42:43]\n\t" "s_add_u32 m0, %18, 0x7520\n\t" "s_nop 0\n\t"
                     "global_load_lds_dwordx4 v226, s[42:43]\n\t" "s_add_u32 s42, s44, 0x180000\n\t" "s_addc_u32 s43, s45, 0\n\t" "s_add_u32 m0, %18, 0xc200\n\t" "s_nop 0\n\t" "global_load_lds_dwordx4 v224, s[42:43]\n\t"
                     "s_add_u32 m0, %18, 0xc600\n\t" "s_nop 0\n\t" "global_load_lds_dwordx4 v225, s[42:43]\n\t" "s_add_u32 m0, %18, 0xca00\n\t" "s_nop 0\n\t" "global_load_lds_dwordx4 v226, s[42:43]\n\t"
                     "s_add_u32 s42, s44, 0x198000\n\t" "s_addc_u32 s43, s45, 0\n\t" "s_add_u32 m0, %18, 0xce20\n\t" "s_nop 0\n\t" "global_load_lds_dwordx4 v224, s[42:43]\n\t" "s_add_u32 m0, %18, 0xd220\n\t" "s_nop 0\n\t"
                     "global_load_lds_dwordx4 v225, s[42:43]\n\t" "s_add_u32 m0, %18, 0xd620\n\t" "s_nop 0\n\t" "global_load_lds_dwordx4 v226, s[42:43]\n\t" "s_add_u32 s42, s44, 0x240000\n\t" "s_addc_u32 s43, s45, 0\n\t"
                     "s_add_u32 m0, %18, 0x12300\n\t" "s_nop 0\n\t" "global_load_lds_dwordx4 v224, s[42:43]\n\t" "s_add_u32 m0, %18, 0x12700\n\t" "s_nop 0\n\t" "global_load_lds_dwordx4 v225, s[42:43]\n\t"
                     "s_add_u32 m0, %18, 0x12b00\n\t" "s_nop 0\n\t" "global_load_lds_dwordx4 v226, s[42:43]\n\t" "s_add_u32 s42, s44, 0x258000\n\t" "s_addc_u32 s43, s45, 0\n\t" "s_add_u32 m0, %18, 0x12f20\n\t"
                     "s_nop 0\n\t" "global_load_lds_dwordx4 v224, s[42:43]\n\t" "s_add_u32 m0, %18, 0x13320\n\t" "s_nop 0\n\t" "global_load_lds_dwordx4 v225, s[42:43]\n\t" "s_add_u32 m0, %18, 0x13720\n\t" "s_nop 0\n\t"
                     "global_load_lds_dwordx4 v226, s[42:43]\n\t" "s_waitcnt vmcnt(12) lgkmcnt(0)\n\t" "s_barrier\n\t" "s_add_u32 s42, s44, 0x300000\n\t" "s_addc_u32 s43, s45, 0\n\t" "s_add_u32 m0, %18, 0x18400\n\t"
                     "s_nop 0\n\t" "global_load_lds_dwordx4 v224, s[42:43]\n\t" "s_add_u32 m0, %18, 0x18800\n\t" "s_nop 0\n\t" "global_load_lds_dwordx4 v225, s[42:43]\n\t" "s_add_u32 m0, %18, 0x18c00\n\t" "s_nop 0\n\t"
                     "global_load_lds_dwordx4 v226, s[42:43]\n\t" "s_add_u32 s42, s44, 0x318000\n\t" "s_addc_u32 s43, s45, 0\n\t" "s_add_u32 m0, %18, 0x19020\n\t" "s_nop 0\n\t" "global_load_lds_dwordx4 v224, s[42:43]\n\t"
                     "s_add_u32 m0, %18, 0x19420\n\t" "s_nop 0\n\t" "global_load_lds_dwordx4 v225, s[42:43]\n\t" "s_add_u32 m0, %18, 0x19820\n\t" "s_nop 0\n\t" "global_load_lds_dwordx4 v226, s[42:43]\n\t"
                     "s_add_u32 s42, s44, 0x3c0000\n\t" "s_addc_u32 s43, s45, 0\n\t" "s_add_u32 m0, %18, 0x1e500\n\t" "s_nop 0\n\t" "global_load_lds_dwordx4 v224, s[42:43]\n\t" "s_add_u32 m0, %18, 0x1e900\n\t"
                     "s_nop 0\n\t" "global_load_lds_dwordx4 v225, s[42:43]\n\t" "s_add_u32 m0, %18, 0x1ed00\n\t" "s_nop 0\n\t" "global_load_lds_dwordx4 v226, s[42:43]\n\t" "s_add_u32 s42, s44, 0x3d8000\n\t"
                     "s_addc_u32 s43, s45, 0\n\t" "s_add_u32 m0, %18, 0x1f120\n\t" "s_nop 0\n\t" "global_load_lds_dwordx4 v224, s[42:43]\n\t" "s_add_u32 m0, %18, 0x1f520\n\t" "s_nop 0\n\t"
                     "global_load_lds_dwordx4 v225, s[42:43]\n\t" "s_add_u32 m0, %18, 0x1f920\n\t" "s_nop 0\n\t" "global_load_lds_dwordx4 v226, s[42:43]\n\t" "v_add_u32 v237, 0x0, v227\n\t" "v_add_u32 v238, 0x0, v228\n\t"
                     "ds_read_b64_tr_b16 a[0:1], v237\n\t" "ds_read_b64_tr_b16 a[2:3], v237 offset:768\n\t" "ds_read_b64_tr_b16 a[4:5], v237 offset:12416\n\t" "ds_read_b64_tr_b16 a[6:7], v237 offset:13184\n\t"
                     "ds_read_b64_tr_b16 v[0:1], v237 offset:32\n\t" "ds_read_b64_tr_b16 v[2:3], v237 offset:800\n\t" "ds_read_b64_tr_b16 v[4:5], v237 offset:12448\n\t" "ds_read_b64_tr_b16 v[6:7], v237 offset:13216\n\t"
                     "ds_read_b64_tr_b16 a[8:9], v238 offset:64\n\t" "ds_read_b64_tr_b16 a[10:11], v238 offset:832\n\t" "ds_read_b64_tr_b16 a[12:13], v238 offset:12480\n\t"
                     "ds_read_b64_tr_b16 a[14:15], v238 offset:13248\n\t" "ds_read_b64_tr_b16 a[16:17], v237 offset:24832\n\t" "ds_read_b64_tr_b16 a[18:19], v237 offset:25600\n\t"
                     "ds_read_b64_tr_b16 a[20:21], v237 offset:37248\n\t" "ds_read_b64_tr_b16 a[22:23], v237 offset:38016\n\t" "ds_read_b64_tr_b16 v[8:9], v237 offset:24864\n\t"
                     "ds_read_b64_tr_b16 v[10:11], v237 offset:25632\n\t" "ds_read_b64_tr_b16 v[12:13], v237 offset:37280\n\t" "ds_read_b64_tr_b16 v[14:15], v237 offset:38048\n\t"
                     "ds_read_b64_tr_b16 a[24:25], v238 offset:24896\n\t" "ds_read_b64_tr_b16 a[26:27], v238 offset:25664\n\t" "ds_read_b64_tr_b16 a[28:29], v238 offset:37312\n\t"
                     "ds_read_b64_tr_b16 a[30:31], v238 offset:38080\n\t" "s_waitcnt vmcnt(12) lgkmcnt(0)\n\t" "s_barrier\n\t" "s_add_u32 s42, s44, 0x480000\n\t" "s_addc_u32 s43, s45, 0\n\t" "s_add_u32 m0, %18, 0x0\n\t"
                     "s_nop 0\n\t" "global_load_lds_dwordx4 v224, s[42:43]\n\t" "s_add_u32 m0, %18, 0x400\n\t" "s_nop 0\n\t" "global_load_lds_dwordx4 v225, s[42:43]\n\t" "s_add_u32 m0, %18, 0x800\n\t" "s_nop 0\n\t"
                     "global_load_lds_dwordx4 v226, s[42:43]\n\t" "s_add_u32 s42, s44, 0x498000\n\t" "s_addc_u32 s43, s45, 0\n\t" "s_add_u32 m0, %18, 0xc20\n\t" "s_nop 0\n\t" "global_load_lds_dwordx4 v224, s[42:43]\n\t"
                     "s_add_u32 m0, %18, 0x1020\n\t" "s_nop 0\n\t" "global_load_lds_dwordx4 v225, s[42:43]\n\t" "s_add_u32 m0, %18, 0x1420\n\t" "s_nop 0\n\t" "global_load_lds_dwordx4 v226, s[42:43]\n\t"
                     "s_add_u32 s42, s44, 0x540000\n\t" "s_addc_u32 s43, s45, 0\n\t" "s_add_u32 m0, %18, 0x6100\n\t" "s_nop 0\n\t" "global_load_lds_dwordx4 v224, s[42:43]\n\t" "s_add_u32 m0, %18, 0x6500\n\t" "s_nop 0\n\t"
                     "global_load_lds_dwordx4 v225, s[42:43]\n\t" "s_add_u32 m0, %18, 0x6900\n\t" "s_nop 0\n\t" "global_load_lds_dwordx4 v226, s[42:43]\n\t" "s_add_u32 s42, s44, 0x558000\n\t" "s_addc_u32 s43, s45, 0\n\t"
                     "s_add_u32 m0, %18, 0x6d20\n\t" "s_nop 0\n\t" "global_load_lds_dwordx4 v224, s[42:43]\n\t" "s_add_u32 m0, %18, 0x7120\n\t" "s_nop 0\n\t" "global_load_lds_dwordx4 v225, s[42:43]\n\t"
                     "s_add_u32 m0, %18, 0x7520\n\t" "s_nop 0\n\t" "global_load_lds_dwordx4 v226, s[42:43]\n\t" "v_add_u32 v237, 0xc200, v227\n\t" "v_add_u32 v238, 0xc200, v228\n\t" "ds_read_b64_tr_b16 a[32:33], v237\n\t"
                     "ds_read_b64_tr_b16 a[34:35], v237 offset:768\n\t" "ds_read_b64_tr_b16 a[36:37], v237 offset:12416\n\t" "ds_read_b64_tr_b16 a[38:39], v237 offset:13184\n\t"
                     "ds_read_b64_tr_b16 v[16:17], v237 offset:32\n\t" "ds_read_b64_tr_b16 v[18:19], v237 offset:800\n\t" "ds_read_b64_tr_b16 v[20:21], v237 offset:12448\n\t"
                     "ds_read_b64_tr_b16 v[22:23], v237 offset:13216\n\t" "ds_read_b64_tr_b16 a[40:41], v238 offset:64\n\t" "ds_read_b64_tr_b16 a[42:43], v238 offset:832\n\t"
                     "ds_read_b64_tr_b16 a[44:45], v238 offset:12480\n\t" "ds_read_b64_tr_b16 a[46:47], v238 offset:13248\n\t" "ds_read_b64_tr_b16 a[48:49], v237 offset:24832\n\t"
                     "ds_read_b64_tr_b16 a[50:51], v237 offset:25600\n\t" "ds_read_b64_tr_b16 a[52:53], v237 offset:37248\n\t" "ds_read_b64_tr_b16 a[54:55], v237 offset:38016\n\t"
                     "ds_read_b64_tr_b16 v[24:25], v237 offset:24864\n\t" "ds_read_b64_tr_b16 v[26:27], v237 offset:25632\n\t" "ds_read_b64_tr_b16 v[28:29], v237 offset:37280\n\t"
                     "ds_read_b64_tr_b16 v[30:31], v237 offset:38048\n\t" "ds_read_b64_tr_b16 a[56:57], v238 offset:24896\n\t" "ds_read_b64_tr_b16 a[58:59], v238 offset:25664\n\t"
                     "ds_read_b64_tr_b16 a[60:61], v238 offset:37312\n\t" "ds_read_b64_tr_b16 a[62:63], v238 offset:38080\n\t" "s_waitcnt vmcnt(12) lgkmcnt(0)\n\t" "s_barrier\n\t" "s_add_u32 s42, s44, 0x600000\n\t"
                     "s_addc_u32 s43, s45, 0\n\t" "s_add_u32 m0, %18, 0xc200\n\t" "s_nop 0\n\t" "global_load_lds_dwordx4 v224, s[42:43]\n\t" "s_add_u32 m0, %18, 0xc600\n\t" "s_nop 0\n\t"
                     "global_load_lds_dwordx4 v225, s[42:43]\n\t" "s_add_u32 m0, %18, 0xca00\n\t" "s_nop 0\n\t" "global_load_lds_dwordx4 v226, s[42:43]\n\t" "s_add_u32 s42, s44, 0x618000\n\t" "s_addc_u32 s43, s45, 0\n\t"
                     "s_add_u32 m0, %18, 0xce20\n\t" "s_nop 0\n\t" "global_load_lds_dwordx4 v224, s[42:43]\n\t" "s_add_u32 m0, %18, 0xd220\n\t" "s_nop 0\n\t" "global_load_lds_dwordx4 v225, s[42:43]\n\t"
                     "s_add_u32 m0, %18, 0xd620\n\t" "s_nop 0\n\t" "global_load_lds_dwordx4 v226, s[42:43]\n\t" "s_add_u32 s42, s44, 0x6c0000\n\t" "s_addc_u32 s43, s45, 0\n\t" "s_add_u32 m0, %18, 0x12300\n\t" "s_nop 0\n\t"
                     "global_load_lds_dwordx4 v224, s[42:43]\n\t" "s_add_u32 m0, %18, 0x12700\n\t" "s_nop 0\n\t" "global_load_lds_dwordx4 v225, s[42:43]\n\t" "s_add_u32 m0, %18, 0x12b00\n\t" "s_nop 0\n\t"
                     "global_load_lds_dwordx4 v226, s[42:43]\n\t" "s_add_u32 s42, s44, 0x6d8000\n\t" "s_addc_u32 s43, s45, 0\n\t" "s_add_u32 m0, %18, 0x12f20\n\t" "s_nop 0\n\t" "global_load_lds_dwordx4 v224, s[42:43]\n\t"
                     "s_add_u32 m0, %18, 0x13320\n\t" "s_nop 0\n\t" "global_load_lds_dwordx4 v225, s[42:43]\n\t" "s_add_u32 m0, %18, 0x13720\n\t" "s_nop 0\n\t" "global_load_lds_dwordx4 v226, s[42:43]\n\t"
                     "v_add_u32 v237, 0x18400, v227\n\t" "v_add_u32 v238, 0x18400, v228\n\t" "ds_read_b64_tr_b16 a[64:65], v237\n\t" "ds_read_b64_tr_b16 a[66:67], v237 offset:768\n\t"
                     "ds_read_b64_tr_b16 a[68:69], v237 offset:12416\n\t" "ds_read_b64_tr_b16 a[70:71], v237 offset:13184\n\t" "ds_read_b64_tr_b16 v[32:33], v237 offset:32\n\t"
                     "ds_read_b64_tr_b16 v[34:35], v237 offset:800\n\t" "ds_read_b64_tr_b16 v[36:37], v237 offset:12448\n\t" "ds_read_b64_tr_b16 v[38:39], v237 offset:13216\n\t"
                     "ds_read_b64_tr_b16 a[72:73], v238 offset:64\n\t" "ds_read_b64_tr_b16 a[74:75], v238 offset:832\n\t" "ds_read_b64_tr_b16 a[76:77], v238 offset:12480\n\t"
                     "ds_read_b64_tr_b16 a[78:79], v238 offset:13248\n\t" "ds_read_b64_tr_b16 a[80:81], v237 offset:24832\n\t" "ds_read_b64_tr_b16 a[82:83], v237 offset:25600\n\t"
                     "ds_read_b64_tr_b16 a[84:85], v237 offset:37248\n\t" "ds_read_b64_tr_b16 a[86:87], v237 offset:38016\n\t" "ds_read_b64_tr_b16 v[40:41], v237 offset:24864\n\t"
                     "ds_read_b64_tr_b16 v[42:43], v237 offset:25632\n\t" "ds_read_b64_tr_b16 v[44:45], v237 offset:37280\n\t" "ds_read_b64_tr_b16 v[46:47], v237 offset:38048\n\t"
                     "ds_read_b64_tr_b16 a[88:89], v238 offset:24896\n\t" "ds_read_b64_tr_b16 a[90:91], v238 offset:25664\n\t" "ds_read_b64_tr_b16 a[92:93], v238 offset:37312\n\t"
                     "ds_read_b64_tr_b16 a[94:95], v238 offset:38080\n\t" "s_waitcnt vmcnt(12) lgkmcnt(0)\n\t" "s_barrier\n\t" "s_add_u32 s42, s46, 0x0\n\t" "s_addc_u32 s43, s47, 0\n\t" "s_add_u32 m0, %20, 0x18400\n\t"
                     "s_nop 0\n\t" "global_load_lds_dwordx4 v232, s[42:43]\n\t" "s_add_u32 m0, %20, 0x18800\n\t" "s_nop 0\n\t" "global_load_lds_dwordx4 v233, s[42:43]\n\t" "s_add_u32 m0, %20, 0x18c00\n\t" "s_nop 0\n\t"
                     "global_load_lds_dwordx4 v234, s[42:43]\n\t" "s_add_u32 m0, %20, 0x19000\n\t" "s_nop 0\n\t" "global_load_lds_dwordx4 v235, s[42:43]\n\t" "s_add_u32 m0, %20, 0x19400\n\t" "s_nop 0\n\t"
                     "global_load_lds_dwordx4 v236, s[42:43]\n\t" "s_add_u32 s42, s46, 0x1400\n\t" "s_addc_u32 s43, s47, 0\n\t" "s_add_u32 m0, %20, 0x19800\n\t" "v_xor_b32 v224, 0x80, v232\n\t"
                     "global_load_lds_dwordx4 v224, s[42:43]\n\t" "s_add_u32 m0, %20, 0x19c00\n\t" "v_xor_b32 v224, 0x80, v233\n\t" "global_load_lds_dwordx4 v224, s[42:43]\n\t" "s_add_u32 m0, %20, 0x1a000\n\t"
                     "v_xor_b32 v224, 0x80, v234\n\t" "global_load_lds_dwordx4 v224, s[42:43]\n\t" "s_add_u32 m0, %20, 0x1a400\n\t" "v_xor_b32 v224, 0x80, v235\n\t" "global_load_lds_dwordx4 v224, s[42:43]\n\t"
                     "s_add_u32 m0, %20, 0x1a800\n\t" "v_xor_b32 v224, 0x80, v236\n\t" "global_load_lds_dwordx4 v224, s[42:43]\n\t" "v_add_u32 v237, 0x0, v227\n\t" "v_add_u32 v238, 0x0, v228\n\t"
                     "ds_read_b64_tr_b16 a[96:97], v237\n\t" "ds_read_b64_tr_b16 a[98:99], v237 offset:768\n\t" "ds_read_b64_tr_b16 a[100:101], v237 offset:12416\n\t" "ds_read_b64_tr_b16 a[102:103], v237 offset:13184\n\t"
                     "ds_read_b64_tr_b16 v[48:49], v237 offset:32\n\t" "ds_read_b64_tr_b16 v[50:51], v237 offset:800\n\t" "ds_read_b64_tr_b16 v[52:53], v237 offset:12448\n\t"
                     "ds_read_b64_tr_b16 v[54:55], v237 offset:13216\n\t" "ds_read_b64_tr_b16 a[104:105], v238 offset:64\n\t" "ds_read_b64_tr_b16 a[106:107], v238 offset:832\n\t"
                     "ds_read_b64_tr_b16 a[108:109], v238 offset:12480\n\t" "ds_read_b64_tr_b16 a[110:111], v238 offset:13248\n\t" "ds_read_b64_tr_b16 a[112:113], v237 offset:24832\n\t"
                     "ds_read_b64_tr_b16 a[114:115], v237 offset:25600\n\t" "ds_read_b64_tr_b16 a[116:117], v237 offset:37248\n\t" "ds_read_b64_tr_b16 a[118:119], v237 offset:38016\n\t"
                     "ds_read_b64_tr_b16 v[56:57], v237 offset:24864\n\t" "ds_read_b64_tr_b16 v[58:59], v237 offset:25632\n\t" "ds_read_b64_tr_b16 v[60:61], v237 offset:37280\n\t"
                     "ds_read_b64_tr_b16 v[62:63], v237 offset:38048\n\t" "ds_read_b64_tr_b16 a[120:121], v238 offset:24896\n\t" "ds_read_b64_tr_b16 a[122:123], v238 offset:25664\n\t"
                     "ds_read_b64_tr_b16 a[124:125], v238 offset:37312\n\t" "ds_read_b64_tr_b16 a[126:127], v238 offset:38080\n\t" "s_waitcnt vmcnt(10) lgkmcnt(0)\n\t" "s_barrier\n\t" "s_add_u32 s42, s46, 0xa000\n\t"
                     "s_addc_u32 s43, s47, 0\n\t" "s_add_u32 m0, %20, 0x0\n\t" "s_nop 0\n\t" "global_load_lds_dwordx4 v232, s[42:43]\n\t" "s_add_u32 m0, %20, 0x400\n\t" "s_nop 0\n\t"
                     "global_load_lds_dwordx4 v233, s[42:43]\n\t" "s_add_u32 m0, %20, 0x800\n\t" "s_nop 0\n\t" "global_load_lds_dwordx4 v234, s[42:43]\n\t" "s_add_u32 m0, %20, 0xc00\n\t" "s_nop 0\n\t"
                     "global_load_lds_dwordx4 v235, s[42:43]\n\t" "s_add_u32 m0, %20, 0x1000\n\t" "s_nop 0\n\t" "global_load_lds_dwordx4 v236, s[42:43]\n\t" "s_add_u32 s42, s46, 0xb400\n\t" "s_addc_u32 s43, s47, 0\n\t"
                     "s_add_u32 m0, %20, 0x1400\n\t" "v_xor_b32 v224, 0x80, v232\n\t" "global_load_lds_dwordx4 v224, s[42:43]\n\t" "s_add_u32 m0, %20, 0x1800\n\t" "v_xor_b32 v224, 0x80, v233\n\t"
                     "global_load_lds_dwordx4 v224, s[42:43]\n\t" "s_add_u32 m0, %20, 0x1c00\n\t" "v_xor_b32 v224, 0x80, v234\n\t" "global_load_lds_dwordx4 v224, s[42:43]\n\t" "s_add_u32 m0, %20, 0x2000\n\t"
                     "v_xor_b32 v224, 0x80, v235\n\t" "global_load_lds_dwordx4 v224, s[42:43]\n\t" "s_add_u32 m0, %20, 0x2400\n\t" "v_xor_b32 v224, 0x80, v236\n\t" "global_load_lds_dwordx4 v224, s[42:43]\n\t"
                     "v_add_u32 v237, 0xc200, v227\n\t" "v_add_u32 v238, 0xc200, v228\n\t" "ds_read_b64_tr_b16 a[128:129], v237\n\t" "ds_read_b64_tr_b16 a[130:131], v237 offset:768\n\t"
                     "ds_read_b64_tr_b16 a[132:133], v237 offset:12416\n\t" "ds_read_b64_tr_b16 a[134:135], v237 offset:13184\n\t" "ds_read_b64_tr_b16 a[136:137], v237 offset:32\n\t"
                     "ds_read_b64_tr_b16 a[138:139], v237 offset:800\n\t" "ds_read_b64_tr_b16 a[140:141], v237 offset:12448\n\t" "ds_read_b64_tr_b16 a[142:143], v237 offset:13216\n\t"
                     "ds_read_b64_tr_b16 a[144:145], v238 offset:64\n\t" "ds_read_b64_tr_b16 a[146:147], v238 offset:832\n\t" "ds_read_b64_tr_b16 a[148:149], v238 offset:12480\n\t"
                     "ds_read_b64_tr_b16 a[150:151], v238 offset:13248\n\t" "ds_read_b64_tr_b16 a[152:153], v237 offset:24832\n\t" "ds_read_b64_tr_b16 a[154:155], v237 offset:25600\n\t"
                     "ds_read_b64_tr_b16 a[156:157], v237 offset:37248\n\t" "ds_read_b64_tr_b16 a[158:159], v237 offset:38016\n\t" "ds_read_b64_tr_b16 a[160:161], v237 offset:24864\n\t"
                     "ds_read_b64_tr_b16 a[162:163], v237 offset:25632\n\t" "ds_read_b64_tr_b16 a[164:165], v237 offset:37280\n\t" "ds_read_b64_tr_b16 a[166:167], v237 offset:38048\n\t"
                     "ds_read_b64_tr_b16 a[168:169], v238 offset:24896\n\t" "ds_read_b64_tr_b16 a[170:171], v238 offset:25664\n\t" "ds_read_b64_tr_b16 a[172:173], v238 offset:37312\n\t"
                     "ds_read_b64_tr_b16 a[174:175], v238 offset:38080\n\t" "s_waitcnt vmcnt(10) lgkmcnt(0)\n\t" "s_barrier\n\t" "s_add_u32 s42, s46, 0x14000\n\t" "s_addc_u32 s43, s47, 0\n\t"
                     "s_add_u32 m0, %20, 0xc200\n\t" "s_nop 0\n\t" "global_load_lds_dwordx4 v232, s[42:43]\n\t" "s_add_u32 m0, %20, 0xc600\n\t" "s_nop 0\n\t" "global_load_lds_dwordx4 v233, s[42:43]\n\t"
                     "s_add_u32 m0, %20, 0xca00\n\t" "s_nop 0\n\t" "global_load_lds_dwordx4 v234, s[42:43]\n\t" "s_add_u32 m0, %20, 0xce00\n\t" "s_nop 0\n\t" "global_load_lds_dwordx4 v235, s[42:43]\n\t"
                     "s_add_u32 m0, %20, 0xd200\n\t" "s_nop 0\n\t" "global_load_lds_dwordx4 v236, s[42:43]\n\t" "s_add_u32 s42, s46, 0x15400\n\t" "s_addc_u32 s43, s47, 0\n\t" "s_add_u32 m0, %20, 0xd600\n\t"
                     "v_xor_b32 v224, 0x80, v232\n\t" "global_load_lds_dwordx4 v224, s[42:43]\n\t" "s_add_u32 m0, %20, 0xda00\n\t" "v_xor_b32 v224, 0x80, v233\n\t" "global_load_lds_dwordx4 v224, s[42:43]\n\t"
                     "s_add_u32 m0, %20, 0xde00\n\t" "v_xor_b32 v224, 0x80, v234\n\t" "global_load_lds_dwordx4 v224, s[42:43]\n\t" "s_add_u32 m0, %20, 0xe200\n\t" "v_xor_b32 v224, 0x80, v235\n\t"
                     "global_load_lds_dwordx4 v224, s[42:43]\n\t" "s_add_u32 m0, %20, 0xe600\n\t" "v_xor_b32 v224, 0x80, v236\n\t" "global_load_lds_dwordx4 v224, s[42:43]\n\t" "s_add_u32 s42, %21, 0x18400\n\t"
                     "v_xor_b32 v237, 0x0, v229\n\t" "v_add_u32 v237, s42, v237\n\t" "v_xor_b32 v238, 0x40, v229\n\t" "v_add_u32 v238, s42, v238\n\t" "v_xor_b32 v239, 0x80, v229\n\t" "v_add_u32 v239, s42, v239\n\t"
                     "v_xor_b32 v226, 0xc0, v229\n\t" "v_add_u32 v226, s42, v226\n\t" "ds_read_b64_tr_b16 a[176:177], v237\n\t" "ds_read_b64_tr_b16 a[178:179], v238 offset:2560\n\t"
                     "ds_read_b64_tr_b16 a[200:201], v239\n\t" "ds_read_b64_tr_b16 a[202:203], v226 offset:2560\n\t" "ds_read_b64_tr_b16 a[224:225], v237 offset:256\n\t"
                     "ds_read_b64_tr_b16 a[226:227], v238 offset:2816\n\t" "ds_read_b64_tr_b16 a[248:249], v239 offset:256\n\t" "ds_read_b64_tr_b16 a[250:251], v226 offset:2816\n\t"
                     "ds_read_b64_tr_b16 v[112:113], v237 offset:512\n\t" "ds_read_b64_tr_b16 v[114:115], v238 offset:3072\n\t" "ds_read_b64_tr_b16 v[136:137], v239 offset:512\n\t"
                     "ds_read_b64_tr_b16 v[138:139], v226 offset:3072\n\t" "ds_read_b64_tr_b16 v[160:161], v237 offset:768\n\t" "ds_read_b64_tr_b16 v[162:163], v238 offset:3328\n\t"
                     "ds_read_b64_tr_b16 v[184:185], v239 offset:768\n\t" "ds_read_b64_tr_b16 v[186:187], v226 offset:3328\n\t" "ds_read_b64_tr_b16 v[208:209], v237 offset:1024\n\t"
                     "ds_read_b64_tr_b16 v[210:211], v238 offset:3584\n\t" "ds_read_b64_tr_b16 v[72:73], v239 offset:1024\n\t" "ds_read_b64_tr_b16 v[74:75], v226 offset:3584\n\t" "s_waitcnt vmcnt(10) lgkmcnt(0)\n\t"
                     "s_barrier\n\t" "s_add_u32 s42, s46, 0x1e000\n\t" "s_addc_u32 s43, s47, 0\n\t" "s_add_u32 m0, %20, 0x18400\n\t" "s_nop 0\n\t" "global_load_lds_dwordx4 v232, s[42:43]\n\t"
                     "s_add_u32 m0, %20, 0x18800\n\t" "s_nop 0\n\t" "global_load_lds_dwordx4 v233, s[42:43]\n\t" "s_add_u32 m0, %20, 0x18c00\n\t" "s_nop 0\n\t" "global_load_lds_dwordx4 v234, s[42:43]\n\t"
                     "s_add_u32 m0, %20, 0x19000\n\t" "s_nop 0\n\t" "global_load_lds_dwordx4 v235, s[42:43]\n\t" "s_add_u32 m0, %20, 0x19400\n\t" "s_nop 0\n\t" "global_load_lds_dwordx4 v236, s[42:43]\n\t"
                     "s_add_u32 s42, s46, 0x1f400\n\t" "s_addc_u32 s43, s47, 0\n\t" "s_add_u32 m0, %20, 0x19800\n\t" "v_xor_b32 v224, 0x80, v232\n\t" "global_load_lds_dwordx4 v224, s[42:43]\n\t"
                     "s_add_u32 m0, %20, 0x19c00\n\t" "v_xor_b32 v224, 0x80, v233\n\t" "global_load_lds_dwordx4 v224, s[42:43]\n\t" "s_add_u32 m0, %20, 0x1a000\n\t" "v_xor_b32 v224, 0x80, v234\n\t"
                     "global_load_lds_dwordx4 v224, s[42:43]\n\t" "s_add_u32 m0, %20, 0x1a400\n\t" "v_xor_b32 v224, 0x80, v235\n\t" "global_load_lds_dwordx4 v224, s[42:43]\n\t" "s_add_u32 m0, %20, 0x1a800\n\t"
                     "v_xor_b32 v224, 0x80, v236\n\t" "global_load_lds_dwordx4 v224, s[42:43]\n\t" "s_add_u32 s42, %21, 0x0\n\t" "v_xor_b32 v237, 0x0, v229\n\t" "v_add_u32 v237, s42, v237\n\t"
                     "v_xor_b32 v238, 0x40, v229\n\t" "v_add_u32 v238, s42, v238\n\t" "v_xor_b32 v239, 0x80, v229\n\t" "v_add_u32 v239, s42, v239\n\t" "v_xor_b32 v226, 0xc0, v229\n\t" "v_add_u32 v226, s42, v226\n\t"
                     "ds_read_b64_tr_b16 a[180:181], v237\n\t" "ds_read_b64_tr_b16 a[182:183], v238 offset:2560\n\t" "ds_read_b64_tr_b16 a[204:205], v239\n\t" "ds_read_b64_tr_b16 a[206:207], v226 offset:2560\n\t"
                     "ds_read_b64_tr_b16 a[228:229], v237 offset:256\n\t" "ds_read_b64_tr_b16 a[230:231], v238 offset:2816\n\t" "ds_read_b64_tr_b16 a[252:253], v239 offset:256\n\t"
                     "ds_read_b64_tr_b16 a[254:255], v226 offset:2816\n\t" "ds_read_b64_tr_b16 v[116:117], v237 offset:512\n\t" "ds_read_b64_tr_b16 v[118:119], v238 offset:3072\n\t"
                     "ds_read_b64_tr_b16 v[140:141], v239 offset:512\n\t" "ds_read_b64_tr_b16 v[142:143], v226 offset:3072\n\t" "ds_read_b64_tr_b16 v[164:165], v237 offset:768\n\t"
                     "ds_read_b64_tr_b16 v[166:167], v238 offset:3328\n\t" "ds_read_b64_tr_b16 v[188:189], v239 offset:768\n\t" "ds_read_b64_tr_b16 v[190:191], v226 offset:3328\n\t"
                     "ds_read_b64_tr_b16 v[212:213], v237 offset:1024\n\t" "ds_read_b64_tr_b16 v[214:215], v238 offset:3584\n\t" "ds_read_b64_tr_b16 v[76:77], v239 offset:1024\n\t"
                     "ds_read_b64_tr_b16 v[78:79], v226 offset:3584\n\t" "s_waitcnt vmcnt(10) lgkmcnt(0)\n\t" "s_barrier\n\t" "v_mov_b32 v225, 0\n\t" "global_load_dwordx2 %12, v231, %19 sc1\n\t"
                     "global_load_dwordx2 %13, v225, %19 offset:512 sc1\n\t" "global_load_dwordx2 %14, v225, %19 offset:1024 sc1\n\t" "s_add_u32 s42, s46, 0x28000\n\t" "s_addc_u32 s43, s47, 0\n\t"
                     "s_add_u32 m0, %20, 0x0\n\t" "s_nop 0\n\t" "global_load_lds_dwordx4 v232, s[42:43]\n\t" "s_add_u32 m0, %20, 0x400\n\t" "s_nop 0\n\t" "global_load_lds_dwordx4 v233, s[42:43]\n\t"
                     "s_add_u32 m0, %20, 0x800\n\t" "s_nop 0\n\t" "global_load_lds_dwordx4 v234, s[42:43]\n\t" "s_add_u32 m0, %20, 0xc00\n\t" "s_nop 0\n\t" "global_load_lds_dwordx4 v235, s[42:43]\n\t"
                     "s_add_u32 m0, %20, 0x1000\n\t" "s_nop 0\n\t" "global_load_lds_dwordx4 v236, s[42:43]\n\t" "s_add_u32 s42, s46, 0x29400\n\t" "s_addc_u32 s43, s47, 0\n\t" "s_add_u32 m0, %20, 0x1400\n\t"
                     "v_xor_b32 v224, 0x80, v232\n\t" "global_load_lds_dwordx4 v224, s[42:43]\n\t" "s_add_u32 m0, %20, 0x1800\n\t" "v_xor_b32 v224, 0x80, v233\n\t" "global_load_lds_dwordx4 v224, s[42:43]\n\t"
                     "s_add_u32 m0, %20, 0x1c00\n\t" "v_xor_b32 v224, 0x80, v234\n\t" "global_load_lds_dwordx4 v224, s[42:43]\n\t" "s_add_u32 m0, %20, 0x2000\n\t" "v_xor_b32 v224, 0x80, v235\n\t"
                     "global_load_lds_dwordx4 v224, s[42:43]\n\t" "s_add_u32 m0, %20, 0x2400\n\t" "v_xor_b32 v224, 0x80, v236\n\t" "global_load_lds_dwordx4 v224, s[42:43]\n\t" "s_add_u32 s42, %21, 0xc200\n\t"
                     "v_xor_b32 v237, 0x0, v229\n\t" "v_add_u32 v237, s42, v237\n\t" "v_xor_b32 v238, 0x40, v229\n\t" "v_add_u32 v238, s42, v238\n\t" "v_xor_b32 v239, 0x80, v229\n\t" "v_add_u32 v239, s42, v239\n\t"
                     "v_xor_b32 v226, 0xc0, v229\n\t" "v_add_u32 v226, s42, v226\n\t" "ds_read_b64_tr_b16 a[184:185], v237\n\t" "ds_read_b64_tr_b16 a[186:187], v238 offset:2560\n\t"
                     "ds_read_b64_tr_b16 a[208:209], v239\n\t" "ds_read_b64_tr_b16 a[210:211], v226 offset:2560\n\t" "ds_read_b64_tr_b16 a[232:233], v237 offset:256\n\t"
                     "ds_read_b64_tr_b16 a[234:235], v238 offset:2816\n\t" "ds_read_b64_tr_b16 v[96:97], v239 offset:256\n\t" "ds_read_b64_tr_b16 v[98:99], v226 offset:2816\n\t"
                     "ds_read_b64_tr_b16 v[120:121], v237 offset:512\n\t" "ds_read_b64_tr_b16 v[122:123], v238 offset:3072\n\t" "ds_read_b64_tr_b16 v[144:145], v239 offset:512\n\t"
                     "ds_read_b64_tr_b16 v[146:147], v226 offset:3072\n\t" "ds_read_b64_tr_b16 v[168:169], v237 offset:768\n\t" "ds_read_b64_tr_b16 v[170:171], v238 offset:3328\n\t"
                     "ds_read_b64_tr_b16 v[192:193], v239 offset:768\n\t" "ds_read_b64_tr_b16 v[194:195], v226 offset:3328\n\t" "ds_read_b64_tr_b16 v[216:217], v237 offset:1024\n\t"
                     "ds_read_b64_tr_b16 v[218:219], v238 offset:3584\n\t" "ds_read_b64_tr_b16 v[80:81], v239 offset:1024\n\t" "ds_read_b64_tr_b16 v[82:83], v226 offset:3584\n\t" "s_waitcnt vmcnt(10) lgkmcnt(0)\n\t"
                     "s_barrier\n\t" "s_add_u32 s42, s46, 0x32000\n\t" "s_addc_u32 s43, s47, 0\n\t" "s_add_u32 m0, %20, 0xc200\n\t" "s_nop 0\n\t" "global_load_lds_dwordx4 v232, s[42:43]\n\t" "s_add_u32 m0, %20, 0xc600\n\t"
                     "s_nop 0\n\t" "global_load_lds_dwordx4 v233, s[42:43]\n\t" "s_add_u32 m0, %20, 0xca00\n\t" "s_nop 0\n\t" "global_load_lds_dwordx4 v234, s[42:43]\n\t" "s_add_u32 m0, %20, 0xce00\n\t" "s_nop 0\n\t"
                     "global_load_lds_dwordx4 v235, s[42:43]\n\t" "s_add_u32 m0, %20, 0xd200\n\t" "s_nop 0\n\t" "global_load_lds_dwordx4 v236, s[42:43]\n\t" "s_add_u32 s42, s46, 0x33400\n\t" "s_addc_u32 s43, s47, 0\n\t"
                     "s_add_u32 m0, %20, 0xd600\n\t" "v_xor_b32 v224, 0x80, v232\n\t" "global_load_lds_dwordx4 v224, s[42:43]\n\t" "s_add_u32 m0, %20, 0xda00\n\t" "v_xor_b32 v224, 0x80, v233\n\t"
                     "global_load_lds_dwordx4 v224, s[42:43]\n\t" "s_add_u32 m0, %20, 0xde00\n\t" "v_xor_b32 v224, 0x80, v234\n\t" "global_load_lds_dwordx4 v224, s[42:43]\n\t" "s_add_u32 m0, %20, 0xe200\n\t"
                     "v_xor_b32 v224, 0x80, v235\n\t" "global_load_lds_dwordx4 v224, s[42:43]\n\t" "s_add_u32 m0, %20, 0xe600\n\t" "v_xor_b32 v224, 0x80, v236\n\t" "global_load_lds_dwordx4 v224, s[42:43]\n\t"
                     "s_add_u32 s42, %21, 0x18400\n\t" "v_xor_b32 v237, 0x0, v229\n\t" "v_add_u32 v237, s42, v237\n\t" "v_xor_b32 v238, 0x40, v229\n\t" "v_add_u32 v238, s42, v238\n\t" "v_xor_b32 v239, 0x80, v229\n\t"
                     "v_add_u32 v239, s42, v239\n\t" "v_xor_b32 v226, 0xc0, v229\n\t" "v_add_u32 v226, s42, v226\n\t" "ds_read_b64_tr_b16 a[188:189], v237\n\t" "ds_read_b64_tr_b16 a[190:191], v238 offset:2560\n\t"
                     "ds_read_b64_tr_b16 a[212:213], v239\n\t" "ds_read_b64_tr_b16 a[214:215], v226 offset:2560\n\t" "ds_read_b64_tr_b16 a[236:237], v237 offset:256\n\t"
                     "ds_read_b64_tr_b16 a[238:239], v238 offset:2816\n\t" "ds_read_b64_tr_b16 v[100:101], v239 offset:256\n\t" "ds_read_b64_tr_b16 v[102:103], v226 offset:2816\n\t"
                     "ds_read_b64_tr_b16 v[124:125], v237 offset:512\n\t" "ds_read_b64_tr_b16 v[126:127], v238 offset:3072\n\t" "ds_read_b64_tr_b16 v[148:149], v239 offset:512\n\t"
                     "ds_read_b64_tr_b16 v[150:151], v226 offset:3072\n\t" "ds_read_b64_tr_b16 v[172:173], v237 offset:768\n\t" "ds_read_b64_tr_b16 v[174:175], v238 offset:3328\n\t"
                     "ds_read_b64_tr_b16 v[196:197], v239 offset:768\n\t" "ds_read_b64_tr_b16 v[198:199], v226 offset:3328\n\t" "ds_read_b64_tr_b16 v[220:221], v237 offset:1024\n\t"
                     "ds_read_b64_tr_b16 v[222:223], v238 offset:3584\n\t" "ds_read_b64_tr_b16 v[84:85], v239 offset:1024\n\t" "ds_read_b64_tr_b16 v[86:87], v226 offset:3584\n\t" "s_waitcnt vmcnt(10) lgkmcnt(0)\n\t"
                     "s_barrier\n\t" "s_add_u32 s42, %21, 0x0\n\t" "v_xor_b32 v237, 0x0, v229\n\t" "v_add_u32 v237, s42, v237\n\t" "v_xor_b32 v238, 0x40, v229\n\t" "v_add_u32 v238, s42, v238\n\t"
                     "v_xor_b32 v239, 0x80, v229\n\t" "v_add_u32 v239, s42, v239\n\t" "v_xor_b32 v226, 0xc0, v229\n\t" "v_add_u32 v226, s42, v226\n\t" "ds_read_b64_tr_b16 a[192:193], v237\n\t"
                     "ds_read_b64_tr_b16 a[194:195], v238 offset:2560\n\t" "ds_read_b64_tr_b16 a[216:217], v239\n\t" "ds_read_b64_tr_b16 a[218:219], v226 offset:2560\n\t"
                     "ds_read_b64_tr_b16 a[240:241], v237 offset:256\n\t" "ds_read_b64_tr_b16 a[242:243], v238 offset:2816\n\t" "ds_read_b64_tr_b16 v[104:105], v239 offset:256\n\t"
                     "ds_read_b64_tr_b16 v[106:107], v226 offset:2816\n\t" "ds_read_b64_tr_b16 v[128:129], v237 offset:512\n\t" "ds_read_b64_tr_b16 v[130:131], v238 offset:3072\n\t"
                     "ds_read_b64_tr_b16 v[152:153], v239 offset:512\n\t" "ds_read_b64_tr_b16 v[154:155], v226 offset:3072\n\t" "ds_read_b64_tr_b16 v[176:177], v237 offset:768\n\t"
                     "ds_read_b64_tr_b16 v[178:179], v238 offset:3328\n\t" "ds_read_b64_tr_b16 v[200:201], v239 offset:768\n\t" "ds_read_b64_tr_b16 v[202:203], v226 offset:3328\n\t"
                     "ds_read_b64_tr_b16 v[64:65], v237 offset:1024\n\t" "ds_read_b64_tr_b16 v[66:67], v238 offset:3584\n\t" "ds_read_b64_tr_b16 v[88:89], v239 offset:1024\n\t"
                     "ds_read_b64_tr_b16 v[90:91], v226 offset:3584\n\t" "s_waitcnt vmcnt(0) lgkmcnt(0)\n\t" "s_barrier\n\t" "s_add_u32 s42, %21, 0xc200\n\t" "v_xor_b32 v237, 0x0, v229\n\t" "v_add_u32 v237, s42, v237\n\t"
                     "v_xor_b32 v238, 0x40, v229\n\t" "v_add_u32 v238, s42, v238\n\t" "v_xor_b32 v239, 0x80, v229\n\t" "v_add_u32 v239, s42, v239\n\t" "v_xor_b32 v226, 0xc0, v229\n\t" "v_add_u32 v226, s42, v226\n\t"
                     "ds_read_b64_tr_b16 a[196:197], v237\n\t" "ds_read_b64_tr_b16 a[198:199], v238 offset:2560\n\t" "ds_read_b64_tr_b16 a[220:221], v239\n\t" "ds_read_b64_tr_b16 a[222:223], v226 offset:2560\n\t"
                     "ds_read_b64_tr_b16 a[244:245], v237 offset:256\n\t" "ds_read_b64_tr_b16 a[246:247], v238 offset:2816\n\t" "ds_read_b64_tr_b16 v[108:109], v239 offset:256\n\t"
                     "ds_read_b64_tr_b16 v[110:111], v226 offset:2816\n\t" "ds_read_b64_tr_b16 v[132:133], v237 offset:512\n\t" "ds_read_b64_tr_b16 v[134:135], v238 offset:3072\n\t"
                     "ds_read_b64_tr_b16 v[156:157], v239 offset:512\n\t" "ds_read_b64_tr_b16 v[158:159], v226 offset:3072\n\t" "ds_read_b64_tr_b16 v[180:181], v237 offset:768\n\t"
                     "ds_read_b64_tr_b16 v[182:183], v238 offset:3328\n\t" "ds_read_b64_tr_b16 v[204:205], v239 offset:768\n\t" "ds_read_b64_tr_b16 v[206:207], v226 offset:3328\n\t"
                     "ds_read_b64_tr_b16 v[68:69], v237 offset:1024\n\t" "ds_read_b64_tr_b16 v[70:71], v238 offset:3584\n\t" "ds_read_b64_tr_b16 v[92:93], v239 offset:1024\n\t"
                     "ds_read_b64_tr_b16 v[94:95], v226 offset:3584\n\t" "s_waitcnt lgkmcnt(0)\n\t" "ds_write_b128 v230, v[0:3]\n\t" "ds_write_b128 v230, v[4:7] offset:1024\n\t"
                     "ds_write_b128 v230, v[8:11] offset:2048\n\t" "ds_write_b128 v230, v[12:15] offset:3072\n\t" "ds_write_b128 v230, v[16:19] offset:4096\n\t" "ds_write_b128 v230, v[20:23] offset:5120\n\t"
                     "ds_write_b128 v230, v[24:27] offset:6144\n\t" "ds_write_b128 v230, v[28:31] offset:7168\n\t" "ds_write_b128 v230, v[32:35] offset:8192\n\t" "ds_write_b128 v230, v[36:39] offset:9216\n\t"
                     "ds_write_b128 v230, v[40:43] offset:10240\n\t" "ds_write_b128 v230, v[44:47] offset:11264\n\t" "ds_write_b128 v230, v[48:51] offset:12288\n\t" "ds_write_b128 v230, v[52:55] offset:13312\n\t"
                     "ds_write_b128 v230, v[56:59] offset:14336\n\t" "ds_write_b128 v230, v[60:63] offset:15360\n\t" "ds_write_b128 v230, v[64:67] offset:16384\n\t" "ds_write_b128 v230, v[68:71] offset:17408\n\t"
                     "ds_write_b128 v230, v[72:75] offset:18432\n\t" "ds_write_b128 v230, v[76:79] offset:19456\n\t" "ds_write_b128 v230, v[80:83] offset:20480\n\t" "ds_write_b128 v230, v[84:87] offset:21504\n\t"
                     "ds_write_b128 v230, v[88:91] offset:22528\n\t" "ds_write_b128 v230, v[92:95] offset:23552\n\t"
                     : "=&{a[0:31]}"(wa[0]), "=&{a[32:63]}"(wa[1]), "=&{a[64:95]}"(wa[2]), "=&{a[96:127]}"(wa[3]), "=&{a[128:159]}"(wa[4]), "=&{a[160:191]}"(wa[5]), "=&{a[192:223]}"(wa[6]), "=&{a[224:255]}"(wa[7]), "=&{v[96:127]}"(wv[0]), "=&{v[128:159]}"(wv[1]), "=&{v[160:191]}"(wv[2]), "=&{v[192:223]}"(wv[3]), "=&v"(y.r), "=&v"(y.g), "=&v"(y.e), "+{v[224:239]}"(pa)
                     : "{s[44:45]}"(p.wv + h * DH * 2 + (size_t)(16 * w) * (HD * 2)), "{s[46:47]}"(p.wo + (size_t)(h * DH + 8 * w) * 1280),
                       "s"(lds + 2 * w * GS), "s"(p.ws + OFF_LAT), "s"(lds + w * 10240), "s"(lds)
                     : "v0", "v1", "v2", "v3", "v4", "v5", "v6", "v7", "v8", "v9", "v10", "v11", "v12", "v13", "v14", "v15", "v16", "v17", "v18", "v19", "v20", "v21", "v22", "v23", "v24", "v25", "v26", "v27", "v28", "v29", "v30", "v31", "v32", "v33", "v34", "v35", "v36", "v37", "v38", "v39", "v40", "v41", "v42", "v43", "v44", "v45", "v46", "v47", "v48", "v49", "v50", "v51", "v52", "v53", "v54", "v55", "v56", "v57", "v58", "v59", "v60", "v61", "v62", "v63", "v64", "v65", "v66", "v67", "v68", "v69", "v70", "v71", "v72", "v73", "v74", "v75", "v76", "v77", "v78", "v79", "v80", "v81", "v82", "v83", "v84", "v85", "v86", "v87", "v88", "v89", "v90", "v91", "v92", "v93", "v94", "v95", "s42", "s43", "scc", "memory");
        sd = sync_end(y, p.ws, phi, w == 0);
        barrier();
    }
    const int lane = lanex(), tid = 64 * w + lane;
    const int n = lane & 15, g = lane >> 4, qq = (lane >> 2) & 3, pp = lane & 3;
    char* const fl = p.ws + OFF_ST;
    const u32 iw = lds + 65536 + ((lane * 8) ^ (lane & 32));
    const u32 qr0 = lds + 65536 + (g >> 1) * 512 + pp * 128 + 64 * (g & 1) + 8 * qq + 32 * (pp >> 1);
    const u32 uo = (u32)((2 * w + (n >> 3)) * 4096 + h * 128 + (n & 7) * 16);
    u32x16 ad = u32x16{(u32)lane * 16, (lane & 2) ? 0xEEEEu : 0x4444u, lds + (u32)((w & 1) * 24576 + (w >> 1) * 99328 + lane * 16), iw + ((3 * w + (w & 1)) >> 1) * 512,
                       iw + (1 + 3 * (w >> 1)) * 512 + 4 * (w & 1), qr0, qr0 ^ 32, uo + (2 + g) * 32768, uo + (g & 1) * 32768 + (g >> 1) * 8, uo, uo, uo, uo, uo, uo, uo};
    asm volatile("" : "+{v[224:239]}"(ad));
    const char* memp = p.ws + mslot(sd, 0);
    int budget = 30000;
    Clock clk; clk.init();
    for (int s = 0; s < NSEG; ++s) {
        int tries = 0;
        u64 bad, tp8, en = ~0ull;
        while (true) {
            const u32 tgt = clk.sched(phi, 0, cfg.G, tries ? 2 : 0);
            char* up = p.ws + uslot(sd, s);
            asm volatile("ds_read_b128 v[64:67], v226\n\t" "ds_read_b128 v[68:71], v226 offset:1024\n\t" "ds_read_b128 v[72:75], v226 offset:2048\n\t" "ds_read_b128 v[76:79], v226 offset:3072\n\t"
                         "ds_read_b128 v[80:83], v226 offset:4096\n\t" "ds_read_b128 v[84:87], v226 offset:5120\n\t" "ds_read_b128 v[88:91], v226 offset:6144\n\t" "ds_read_b128 v[92:95], v226 offset:7168\n\t"
                         "v_mov_b64 v[40:41], 0\n\t" "v_mov_b64 v[42:43], 0\n\t" "v_mov_b64 v[44:45], 0\n\t" "v_mov_b64 v[46:47], 0\n\t" "v_mov_b64 v[48:49], 0\n\t" "v_mov_b64 v[50:51], 0\n\t" ".Lsp_u:\n\t" "s_memtime vcc\n\t"
                         "s_waitcnt lgkmcnt(0)\n\t" "s_sub_i32 s42, %3, vcc_lo\n\t" "s_cmp_gt_i32 s42, 80\n\t" "s_cbranch_scc1 .Lsp_u\n\t" "s_max_i32 s42, s42, 0\n\t" "s_sub_i32 s42, 92, s42\n\t" "s_and_b32 s42, s42, -4\n\t"
                         "s_getpc_b64 vcc\n\t" "s_add_u32 vcc_lo, vcc_lo, s42\n\t" "s_addc_u32 vcc_hi, vcc_hi, 0\n\t" "s_setpc_b64 vcc\n\t" "s_nop 0\n\t" "s_nop 0\n\t" "s_nop 0\n\t" "s_nop 0\n\t" "s_nop 0\n\t" "s_nop 0\n\t"
                         "s_nop 0\n\t" "s_nop 0\n\t" "s_nop 0\n\t" "s_nop 0\n\t" "s_nop 0\n\t" "s_nop 0\n\t" "s_nop 0\n\t" "s_nop 0\n\t" "s_nop 0\n\t" "s_nop 0\n\t" "s_nop 0\n\t" "s_nop 0\n\t" "s_nop 0\n\t" "s_nop 0\n\t"
                         "global_load_dwordx4 v[0:3], v224, %4 offset:-4096 sc1\n\t" "s_nop 10\n\t" "global_load_dwordx4 v[4:7], v224, %4 offset:-3072 sc1\n\t" "s_nop 10\n\t"
                         "global_load_dwordx4 v[8:11], v224, %4 offset:-2048 sc1\n\t" "s_nop 10\n\t" "global_load_dwordx4 v[12:15], v224, %4 offset:-1024 sc1\n\t" "s_nop 10\n\t" "global_load_dwordx4 v[16:19], v224, %4 sc1\n\t"
                         "s_nop 10\n\t" "global_load_dwordx4 v[20:23], v224, %4 offset:1024 sc1\n\t" "s_nop 10\n\t" "global_load_dwordx4 v[24:27], v224, %4 offset:2048 sc1\n\t" "s_nop 10\n\t"
                         "global_load_dwordx4 v[28:31], v224, %4 offset:3072 sc1\n\t" "s_nop 10\n\t" "global_load_dwordx4 v[32:35], v224, %5 offset:-4096 sc1\n\t" "s_nop 10\n\t"
                         "global_load_dwordx4 v[36:39], v224, %5 offset:-3072 sc1\n\t" "s_waitcnt vmcnt(9)\n\t" "v_smfmac_f32_16x16x64_bf16 v[40:43], v[0:3], a[0:7], v225\n\t"
                         "v_smfmac_f32_16x16x64_bf16 v[44:47], v[0:3], v[64:71], v225\n\t" "ds_read_b128 v[64:67], v226 offset:8192\n\t" "v_smfmac_f32_16x16x64_bf16 v[48:51], v[0:3], a[8:15], v225\n\t"
                         "ds_read_b128 v[68:71], v226 offset:9216\n\t" "v_cmp_eq_u32 vcc, -1, v0\n\t" "s_waitcnt vmcnt(8)\n\t" "v_smfmac_f32_16x16x64_bf16 v[40:43], v[4:7], a[16:23], v225\n\t" "v_mov_b64 v[0:1], 0\n\t"
                         "v_smfmac_f32_16x16x64_bf16 v[44:47], v[4:7], v[72:79], v225\n\t" "s_and_b64 vcc, vcc, %6\n\t" "s_cbranch_scc1 .Lsk_u\n\t" "ds_read_b128 v[72:75], v226 offset:10240\n\t" "v_mov_b64 v[2:3], 0\n\t"
                         "v_smfmac_f32_16x16x64_bf16 v[48:51], v[4:7], a[24:31], v225\n\t" "ds_read_b128 v[76:79], v226 offset:11264\n\t" "s_waitcnt vmcnt(7)\n\t"
                         "v_smfmac_f32_16x16x64_bf16 v[40:43], v[8:11], a[32:39], v225\n\t" "v_mov_b64 v[4:5], 0\n\t" "v_smfmac_f32_16x16x64_bf16 v[44:47], v[8:11], v[80:87], v225\n\t"
                         "ds_read_b128 v[80:83], v226 offset:12288\n\t" "v_mov_b64 v[6:7], 0\n\t" "v_smfmac_f32_16x16x64_bf16 v[48:51], v[8:11], a[40:47], v225\n\t" "ds_read_b128 v[84:87], v226 offset:13312\n\t"
                         "s_waitcnt vmcnt(6)\n\t" "v_smfmac_f32_16x16x64_bf16 v[40:43], v[12:15], a[48:55], v225\n\t" "v_mov_b64 v[8:9], 0\n\t" "v_smfmac_f32_16x16x64_bf16 v[44:47], v[12:15], v[88:95], v225\n\t"
                         "ds_read_b128 v[88:91], v226 offset:14336\n\t" "v_mov_b64 v[10:11], 0\n\t" "v_smfmac_f32_16x16x64_bf16 v[48:51], v[12:15], a[56:63], v225\n\t" "ds_read_b128 v[92:95], v226 offset:15360\n\t"
                         "s_waitcnt vmcnt(5)\n\t" "v_smfmac_f32_16x16x64_bf16 v[40:43], v[16:19], a[64:71], v225\n\t" "v_mov_b64 v[12:13], 0\n\t" "s_waitcnt lgkmcnt(6)\n\t"
                         "v_smfmac_f32_16x16x64_bf16 v[44:47], v[16:19], v[64:71], v225\n\t" "ds_read_b128 v[64:67], v226 offset:16384\n\t" "v_mov_b64 v[14:15], 0\n\t"
                         "v_smfmac_f32_16x16x64_bf16 v[48:51], v[16:19], a[72:79], v225\n\t" "ds_read_b128 v[68:71], v226 offset:17408\n\t" "s_waitcnt vmcnt(4)\n\t"
                         "v_smfmac_f32_16x16x64_bf16 v[40:43], v[20:23], a[80:87], v225\n\t" "v_mov_b64 v[16:17], 0\n\t" "s_waitcnt lgkmcnt(6)\n\t" "v_smfmac_f32_16x16x64_bf16 v[44:47], v[20:23], v[72:79], v225\n\t"
                         "ds_read_b128 v[72:75], v226 offset:18432\n\t" "v_mov_b64 v[18:19], 0\n\t" "v_smfmac_f32_16x16x64_bf16 v[48:51], v[20:23], a[88:95], v225\n\t" "ds_read_b128 v[76:79], v226 offset:19456\n\t"
                         "s_waitcnt vmcnt(3)\n\t" "v_smfmac_f32_16x16x64_bf16 v[40:43], v[24:27], a[96:103], v225\n\t" "v_mov_b64 v[20:21], 0\n\t" "s_waitcnt lgkmcnt(6)\n\t"
                         "v_smfmac_f32_16x16x64_bf16 v[44:47], v[24:27], v[80:87], v225\n\t" "ds_read_b128 v[80:83], v226 offset:20480\n\t" "v_mov_b64 v[22:23], 0\n\t"
                         "v_smfmac_f32_16x16x64_bf16 v[48:51], v[24:27], a[104:111], v225\n\t" "ds_read_b128 v[84:87], v226 offset:21504\n\t" "s_waitcnt vmcnt(2)\n\t"
                         "v_smfmac_f32_16x16x64_bf16 v[40:43], v[28:31], a[112:119], v225\n\t" "v_mov_b64 v[24:25], 0\n\t" "s_waitcnt lgkmcnt(6)\n\t" "v_smfmac_f32_16x16x64_bf16 v[44:47], v[28:31], v[88:95], v225\n\t"
                         "ds_read_b128 v[88:91], v226 offset:22528\n\t" "v_mov_b64 v[26:27], 0\n\t" "v_smfmac_f32_16x16x64_bf16 v[48:51], v[28:31], a[120:127], v225\n\t" "ds_read_b128 v[92:95], v226 offset:23552\n\t"
                         "s_waitcnt vmcnt(1)\n\t" "v_smfmac_f32_16x16x64_bf16 v[40:43], v[32:35], a[128:135], v225\n\t" "v_mov_b64 v[28:29], 0\n\t" "v_smfmac_f32_16x16x64_bf16 v[44:47], v[32:35], a[136:143], v225\n\t"
                         "v_mov_b64 v[30:31], 0\n\t" "v_smfmac_f32_16x16x64_bf16 v[48:51], v[32:35], a[144:151], v225\n\t" "s_waitcnt vmcnt(0)\n\t" "v_smfmac_f32_16x16x64_bf16 v[40:43], v[36:39], a[152:159], v225\n\t"
                         "v_mov_b64 v[32:33], 0\n\t" "v_smfmac_f32_16x16x64_bf16 v[44:47], v[36:39], a[160:167], v225\n\t" "v_mov_b64 v[34:35], 0\n\t" "v_smfmac_f32_16x16x64_bf16 v[48:51], v[36:39], a[168:175], v225\n\t"
                         "v_add_f32 v52, v40, v42\n\t" "v_add_f32 v53, v41, v43\n\t" "v_cmp_u_f32 vcc, v52, v53\n\t" "v_add_f32 v54, v44, v46\n\t" "v_add_f32 v55, v45, v47\n\t" "v_cvt_pk_bf16_f32 v58, v52, v53\n\t"
                         "v_pk_add_f32 v[56:57], v[48:49], v[50:51]\n\t" "v_cvt_pk_bf16_f32 v59, v54, v55\n\t" "v_cvt_pk_bf16_f32 v60, v56, v57\n\t" "ds_write_b64 v227, v[58:59]\n\t" "ds_write_b32 v228, v60\n\t"
                         "v_mov_b64 v[36:37], 0\n\t" "v_mov_b64 v[38:39], 0\n\t" "s_and_b64 vcc, vcc, %6\n\t" "v_mov_b32 v233, 0\n\t" "s_cbranch_scc1 .Lsk_u\n\t" "v_mov_b32 v233, 0\n\t" "v_mov_b32 v233, 0\n\t"
                         "v_mov_b32 v233, 0\n\t" "s_waitcnt lgkmcnt(0)\n\t" "s_barrier\n\t" "ds_read_b64_tr_b16 v[40:41], v229\n\t" "ds_read_b64_tr_b16 v[42:43], v230\n\t" "v_mov_b32 v233, 0\n\t"
                         "ds_read_b64_tr_b16 v[44:45], v229 offset:1024\n\t" "v_mov_b32 v233, 0\n\t" "ds_read_b64_tr_b16 v[46:47], v230 offset:1024\n\t" "v_mov_b32 v233, 0\n\t"
                         "ds_read_b64_tr_b16 v[48:49], v229 offset:2048\n\t" "v_mov_b32 v233, 0\n\t" "ds_read_b64_tr_b16 v[50:51], v230 offset:2048\n\t" "v_mov_b32 v233, 0\n\t" "s_waitcnt lgkmcnt(4)\n\t"
                         "v_smfmac_f32_16x16x64_bf16 v[0:3], v[40:43], a[176:183], v225\n\t" "v_smfmac_f32_16x16x64_bf16 v[4:7], v[40:43], a[200:207], v225\n\t" "s_waitcnt lgkmcnt(2)\n\t"
                         "v_smfmac_f32_16x16x64_bf16 v[0:3], v[44:47], a[184:191], v225\n\t" "v_smfmac_f32_16x16x64_bf16 v[4:7], v[44:47], a[208:215], v225\n\t" "s_waitcnt lgkmcnt(0)\n\t"
                         "v_smfmac_f32_16x16x64_bf16 v[0:3], v[48:51], a[192:199], v225\n\t" "v_smfmac_f32_16x16x64_bf16 v[4:7], v[48:51], a[216:223], v225\n\t"
                         "v_smfmac_f32_16x16x64_bf16 v[8:11], v[40:43], a[224:231], v225\n\t" "v_add_f32 v52, v0, v2\n\t" "v_add_f32 v53, v1, v3\n\t" "v_cvt_pk_bf16_f32 v54, v53, v52\n\t"
                         "v_smfmac_f32_16x16x64_bf16 v[8:11], v[44:47], a[232:239], v225\n\t" "v_add_f32 v52, v4, v6\n\t" "v_add_f32 v53, v5, v7\n\t" "v_cvt_pk_bf16_f32 v55, v53, v52\n\t" "s_nop 1\n\t"
                         "v_permlane16_swap_b32 v54, v55\n\t" "v_smfmac_f32_16x16x64_bf16 v[8:11], v[48:51], a[240:247], v225\n\t" "global_store_dwordx2 v232, v[54:55], %2 sc1\n\t" "s_add_u32 s40, s40, 0x10000\n\t"
                         "s_addc_u32 s41, s41, 0\n\t" "v_smfmac_f32_16x16x64_bf16 v[12:15], v[40:43], a[248:255], v225\n\t" "v_smfmac_f32_16x16x64_bf16 v[12:15], v[44:47], v[96:103], v225\n\t" "v_add_f32 v52, v8, v10\n\t"
                         "v_smfmac_f32_16x16x64_bf16 v[12:15], v[48:51], v[104:111], v225\n\t" "v_add_f32 v53, v9, v11\n\t" "v_smfmac_f32_16x16x64_bf16 v[16:19], v[40:43], v[112:119], v225\n\t"
                         "v_cvt_pk_bf16_f32 v56, v53, v52\n\t" "v_smfmac_f32_16x16x64_bf16 v[16:19], v[44:47], v[120:127], v225\n\t" "v_add_f32 v52, v12, v14\n\t" "v_add_f32 v53, v13, v15\n\t"
                         "v_cvt_pk_bf16_f32 v57, v53, v52\n\t" "s_nop 1\n\t" "v_permlane16_swap_b32 v56, v57\n\t" "v_smfmac_f32_16x16x64_bf16 v[16:19], v[48:51], v[128:135], v225\n\t"
                         "global_store_dwordx2 v232, v[56:57], %2 sc1\n\t" "s_add_u32 s40, s40, 0x10000\n\t" "s_addc_u32 s41, s41, 0\n\t" "v_smfmac_f32_16x16x64_bf16 v[20:23], v[40:43], v[136:143], v225\n\t"
                         "v_smfmac_f32_16x16x64_bf16 v[20:23], v[44:47], v[144:151], v225\n\t" "v_add_f32 v52, v16, v18\n\t" "v_smfmac_f32_16x16x64_bf16 v[20:23], v[48:51], v[152:159], v225\n\t" "v_add_f32 v53, v17, v19\n\t"
                         "v_smfmac_f32_16x16x64_bf16 v[24:27], v[40:43], v[160:167], v225\n\t" "v_cvt_pk_bf16_f32 v58, v53, v52\n\t" "v_smfmac_f32_16x16x64_bf16 v[24:27], v[44:47], v[168:175], v225\n\t"
                         "v_add_f32 v52, v20, v22\n\t" "v_add_f32 v53, v21, v23\n\t" "v_smfmac_f32_16x16x64_bf16 v[24:27], v[48:51], v[176:183], v225\n\t" "v_cvt_pk_bf16_f32 v59, v53, v52\n\t"
                         "v_smfmac_f32_16x16x64_bf16 v[28:31], v[40:43], v[184:191], v225\n\t" "s_nop 0\n\t" "v_permlane16_swap_b32 v58, v59\n\t" "v_smfmac_f32_16x16x64_bf16 v[28:31], v[44:47], v[192:199], v225\n\t"
                         "global_store_dwordx2 v232, v[58:59], %2 sc1\n\t" "v_add_f32 v52, v24, v26\n\t" "v_smfmac_f32_16x16x64_bf16 v[28:31], v[48:51], v[200:207], v225\n\t" "v_add_f32 v53, v25, v27\n\t"
                         "v_smfmac_f32_16x16x64_bf16 v[32:35], v[40:43], v[208:215], v225\n\t" "v_cvt_pk_bf16_f32 v60, v53, v52\n\t" "v_smfmac_f32_16x16x64_bf16 v[32:35], v[44:47], v[216:223], v225\n\t"
                         "v_add_f32 v52, v28, v30\n\t" "v_smfmac_f32_16x16x64_bf16 v[32:35], v[48:51], v[64:71], v225\n\t" "v_add_f32 v53, v29, v31\n\t" "v_smfmac_f32_16x16x64_bf16 v[36:39], v[40:43], v[72:79], v225\n\t"
                         "v_cvt_pk_bf16_f32 v61, v53, v52\n\t" "v_smfmac_f32_16x16x64_bf16 v[36:39], v[44:47], v[80:87], v225\n\t" "v_add_f32 v52, v32, v34\n\t"
                         "v_smfmac_f32_16x16x64_bf16 v[36:39], v[48:51], v[88:95], v225\n\t" "v_add_f32 v53, v33, v35\n\t" "v_cvt_pk_bf16_f32 v62, v53, v52\n\t" "v_mov_b32 v233, 0\n\t" "v_mov_b32 v233, 0\n\t"
                         "v_mov_b32 v233, 0\n\t" "v_mov_b32 v233, 0\n\t" "v_mov_b32 v233, 0\n\t" "v_pk_add_f32 v[52:53], v[36:37], v[38:39]\n\t" "v_cvt_pk_bf16_f32 v63, v53, v52\n\t" "v_permlane32_swap_b32 v60, v62\n\t"
                         "s_nop 0\n\t" "v_permlane32_swap_b32 v61, v63\n\t" "s_nop 1\n\t" "v_permlane16_swap_b32 v60, v61\n\t" "v_permlane16_swap_b32 v62, v63\n\t" "global_store_dwordx4 v231, v[60:63], %2 sc1\n\t"
                         ".Lsk_u:\n\t" "s_memrealtime %1\n\t" "s_mov_b64 %0, vcc\n\t" "s_waitcnt lgkmcnt(0)\n\t"
                         : "=&s"(bad), "=&s"(tp8), "+{s[40:41]}"(up)
                         : "s"(uni((int)tgt)), "s"(memp), "s"(memp + 16384), "s"(en), "{v[224:239]}"(ad),
                           "{a[0:31]}"(wa[0]), "{a[32:63]}"(wa[1]), "{a[64:95]}"(wa[2]), "{a[96:127]}"(wa[3]), "{a[128:159]}"(wa[4]), "{a[160:191]}"(wa[5]), "{a[192:223]}"(wa[6]), "{a[224:255]}"(wa[7]), "{v[96:127]}"(wv[0]), "{v[128:159]}"(wv[1]), "{v[160:191]}"(wv[2]), "{v[192:223]}"(wv[3])
                         : "v0", "v1", "v2", "v3", "v4", "v5", "v6", "v7", "v8", "v9", "v10", "v11", "v12", "v13", "v14", "v15", "v16", "v17", "v18", "v19", "v20", "v21", "v22", "v23", "v24", "v25", "v26", "v27", "v28", "v29", "v30", "v31", "v32", "v33", "v34", "v35", "v36", "v37", "v38", "v39", "v40", "v41", "v42", "v43", "v44", "v45", "v46", "v47", "v48", "v49", "v50", "v51", "v52", "v53", "v54", "v55", "v56", "v57", "v58", "v59", "v60", "v61", "v62", "v63", "v64", "v65", "v66", "v67", "v68", "v69", "v70", "v71", "v72", "v73", "v74", "v75", "v76", "v77", "v78", "v79", "v80", "v81", "v82", "v83", "v84", "v85", "v86", "v87", "v88", "v89", "v90", "v91", "v92", "v93", "v94", "v95", "vcc", "scc", "s42", "memory");
            if (__builtin_expect(bad == 0, 1)) break;
            waitvm<0>();
            if (--budget < 0 || ((++tries & 15) == 0 && !unwritten(memp, ad[0]))) en = 0;
            phi += (u32)cfg.G;
        }
        clk.calib();
        const u32 tt = sched_ld0(s);
        if (s >= NSEG - 2) waitvm<0>();
        barrier();
        if (s >= NSEG - 2 && w == 3) flag_add(fl, F_PROG + 8 * s + (h & 7), 1);
        if (s >= NSEG - 2 && w == 2) flag_add(fl, F_UD + 2 * (s & 1) + (h & 1), 1);
        follow(phi, (int)(tt >> 16) + 4 + cfg.T3 - VISS, (u32)tp8, cfg.G);
        phi += tt & 0xffffu;
        memp = p.ws + mslot(sd, s + 1);
    }
    asm volatile(".Lkm_lu:\n\ts_waitcnt vmcnt(0)" : : : "memory");
}

#define VALL "{v[0:3]}"(o), "{v[16:47]}"(rv[0]), "{v[48:79]}"(rv[1]), "{v[80:111]}"(rv[2]), "{v[112:143]}"(rv[3]), "{v[144:175]}"(rv[4]), "{v[176:207]}"(rv[5]), "{v[208:239]}"(rv[6]), "{a[0:31]}"(ra[0]), "{a[32:63]}"(ra[1]), "{a[64:95]}"(ra[2]), "{a[96:127]}"(ra[3])
DI void value_role(const Ptrs& p, const ChainCfg cfg, int vb, int sd, u32 phi, u32 lds, int w) {
    const int lane = lanex(), tid = 64 * w + lane, n = lane & 15, g = lane >> 4;
    const int v = 2 * (vb & 3) + ((vb >> 2) & 1) + (vb & ~7);
    const u32 moff = lane * 16;
    char* const fl = p.ws + OFF_ST;
    char* const ub = p.ws + (size_t)v * 8192;
    const int xh = (vb >> 2) & 1;
    if (w == 3) {
        const u32x4 ff = u32x4{~0u, ~0u, ~0u, ~0u};
#pragma nounroll
        for (int s = 0; s < NSEG - 2; ++s) {
            dly(cfg.cR3 >> 8 & 255);
            char* const mk = ub + uslot(sd, s + 2);
#pragma unroll
            for (int i = 0; i < 8; ++i) { gst16(mk + i * 1024, moff, ff); gap<HQ>(-1); }
            if (vb < 32 && s) {
                gst16(p.ws + (size_t)(s + 2) * HO_STRIDE + vb * 2048, moff, ff);
                gst16(p.ws + (size_t)(s + 2) * HO_STRIDE + vb * 2048 + 1024, moff, ff);
            }
            waitvm<0>();
            barrier();
        }
        return;
    }
    if (w) {
        const u32 dof = (u32)((n >> 3) * 16384 + g * 128 + (n & 7) * 16);
        const char* dp = p.ws + OFF_D + (size_t)v * 32768 - 4096 + w * 8192;
        flag_wait(fl, F_VD + (vb & 3) + (v < 20 ? 0 : 4), 32);
#pragma nounroll
        for (int s = 0; s < NSEG - 1; ++s) {
            u32x4 df[16];
            u32 dfl;
            dly(cfg.cR3 & 255);
            sfor<0, 16>([&](auto I) { gld16c<I.v * 512 - 4096>(df[I.v], dp, dof); gap<HP>(1 - s); });
            dp += D_STRIDE;
            flag_ldm(dfl, fl, F_VD + pblk(s + 1) * 8 + (vb & RM), pblk(s + 1) != pblk(s));
            const u32 la = lds + moff + (s & 1) * 32768 + (w - 1) * 16384;
            sfor<0, 8>([&](auto I) {
                constexpr int i = I.v;
                waitv<15 - 2 * i>(df[2 * i]); waitv<15 - 2 * i>(df[2 * i + 1]);
                const u32x4 x = df[2 * i], y = df[2 * i + 1];
                const u32 lb = i & 2 ? la - xh * 4096 : la + xh * 4096;
                *LDSP(u32x4, lb + 2048 * i) = u32x4{x[0], y[0], x[2], y[2]};
                *LDSP(u32x4, lb + 2048 * i + 1024) = u32x4{x[1], y[1], x[3], y[3]};
                PIN();
            });
            waitvm<0>();
            asm volatile("" : "+v"(dfl));
            barrier();
            if (uni((int)dfl) < 31) flag_wait(fl, F_VD + pblk(s + 1) * 8 + (vb & RM), 32);
        }
        return;
    }
    u32x32 rv[7], ra[4];
    u32x4 o;
    asm volatile("v_accvgpr_write_b32 a64, 0\n\t" "v_accvgpr_write_b32 a65, 0\n\t" "v_accvgpr_write_b32 a66, 0\n\t" "v_accvgpr_write_b32 a67, 0\n\t" "v_accvgpr_write_b32 a68, 0\n\t" "v_accvgpr_write_b32 a69, 0\n\t"
                 "v_accvgpr_write_b32 a70, 0\n\t" "v_accvgpr_write_b32 a71, 0\n\t" "v_accvgpr_write_b32 a72, 0\n\t" "v_accvgpr_write_b32 a73, 0\n\t" "v_accvgpr_write_b32 a74, 0\n\t" "v_accvgpr_write_b32 a75, 0\n\t"
                 "v_accvgpr_write_b32 a76, 0\n\t" "v_accvgpr_write_b32 a77, 0\n\t" "v_accvgpr_write_b32 a78, 0\n\t" "v_accvgpr_write_b32 a79, 0\n\t" "v_mov_b64 v[112:113], -1\n\t" "v_mov_b64 v[120:121], -1\n\t"
                 "v_mov_b64 v[128:129], -1\n\t" "v_mov_b64 v[136:137], -1\n\t" "v_mov_b64 v[144:145], -1\n\t" "v_mov_b64 v[152:153], -1\n\t" "v_mov_b64 v[160:161], -1\n\t" "v_mov_b64 v[168:169], -1\n\t"
                 "v_mov_b64 v[176:177], -1\n\t" "v_mov_b64 v[184:185], -1\n\t" "v_mov_b64 v[192:193], -1\n\t" "v_mov_b64 v[200:201], -1\n\t" "v_mov_b64 v[208:209], -1\n\t" "v_mov_b64 v[216:217], -1\n\t"
                 "v_mov_b64 v[224:225], -1\n\t" "v_mov_b64 v[232:233], -1\n\t"
                 : "={v[0:3]}"(o), "={v[16:47]}"(rv[0]), "={v[48:79]}"(rv[1]), "={v[80:111]}"(rv[2]), "={v[112:143]}"(rv[3]), "={v[144:175]}"(rv[4]), "={v[176:207]}"(rv[5]), "={v[208:239]}"(rv[6]), "={a[0:31]}"(ra[0]), "={a[32:63]}"(ra[1]), "={a[64:95]}"(ra[2]), "={a[96:127]}"(ra[3]));
    const u32 ho = (u32)(min(lane, 39) * 256 + (lane >> 5) * 8192);
    const u32 pof = (u32)((n & 4) * 128 + (n >> 3) * 256 + g * 64 + (n & 3) * 16 + 2048);
    const u32 uof = (u32)((n >> 3) * 4096 + g * 128 + (n & 7) * 16 + 512);
    u32x8 ad = u32x8{uof + xh * 512, pof + xh * 2048, (u32)(((g >> 1) ^ xh) * 256 + (n >> 2) * 64 + (n & 1) * 16), moff,
                     (lane & 2) ? 0xEEEEu : 0x4444u, (lane & 4) ? 0xEEEEu : 0x4444u, (u32)(v * 256 + (v >> 5) * 8192 + g * 64 + (n >> 3) * 32 + (n & 1) * 16 + (n & 6) * 2),
                     (lane & 1) ? 0x07060302u : 0x01000504u};
    u32 pob = pof - xh * 2048, uob = uof - xh * 512, pmo = moff ^ xh * 512;
    asm volatile("" : "+{v[4:11]}"(ad), "+v"(pob), "+v"(uob), "+v"(pmo));
    auto zero = [&]() {
        asm volatile("v_mov_b64 v[0:1], 0\n\t" "v_mov_b64 v[2:3], 0\n\t" "v_mov_b64 v[48:49], 0\n\t" "v_mov_b64 v[50:51], 0\n\t" "v_mov_b64 v[52:53], 0\n\t" "v_mov_b64 v[54:55], 0\n\t" "v_mov_b64 v[56:57], 0\n\t"
                     "v_mov_b64 v[58:59], 0\n\t" "v_mov_b64 v[60:61], 0\n\t" "v_mov_b64 v[62:63], 0\n\t" "v_mov_b64 v[64:65], 0\n\t" "v_mov_b64 v[66:67], 0\n\t" "v_mov_b64 v[68:69], 0\n\t" "v_mov_b64 v[70:71], 0\n\t"
                     "v_mov_b64 v[72:73], 0\n\t" "v_mov_b64 v[74:75], 0\n\t" "v_mov_b64 v[76:77], 0\n\t" "v_mov_b64 v[78:79], 0\n\t" "v_mov_b64 v[80:81], 0\n\t" "v_mov_b64 v[82:83], 0\n\t" "v_mov_b64 v[84:85], 0\n\t"
                     "v_mov_b64 v[86:87], 0\n\t" "v_mov_b64 v[88:89], 0\n\t" "v_mov_b64 v[90:91], 0\n\t" "v_mov_b64 v[92:93], 0\n\t" "v_mov_b64 v[94:95], 0\n\t" "v_mov_b64 v[96:97], 0\n\t" "v_mov_b64 v[98:99], 0\n\t"
                     "v_mov_b64 v[100:101], 0\n\t" "v_mov_b64 v[102:103], 0\n\t" "v_mov_b64 v[104:105], 0\n\t" "v_mov_b64 v[106:107], 0\n\t" "v_mov_b64 v[108:109], 0\n\t" "v_mov_b64 v[110:111], 0\n\t"
                     : : VALL : "memory");
    };
    const char* memp = p.ws + OFF_XF + 4096;
    char* up = ub + OFF_XF;
    int budget = 30000;
    Clock clk; clk.init();
    u32 tt = 0;
    for (int s = -1; s < NSEG - 1; ++s) {
        const char* const mnx = p.ws + mslot(sd, s + 1);
        bool all = false;
        int tries = 0;
        u64 tp8;
        while (true) {
            const u32 tgt = clk.sched(phi, (int)(tt >> 16), cfg.G, tries ? 2 : s > 0) - (s < 0 ? 1u << 20 : 0u);
            u64 ok;
            asm volatile("s_cmp_lg_u32 %11, 0\n\t" "s_cbranch_scc0 .Lsp_v0\n\t" ".Lsp_v3:\n\t" "s_memtime vcc\n\t" "s_waitcnt lgkmcnt(0)\n\t" "s_sub_i32 s42, %10, vcc_lo\n\t" "s_cmp_gt_i32 s42, 80\n\t"
                         "s_cbranch_scc1 .Lsp_v3\n\t" "s_max_i32 s42, s42, 0\n\t" "s_sub_i32 s42, 92, s42\n\t" "s_and_b32 s42, s42, -4\n\t" "s_getpc_b64 vcc\n\t" "s_add_u32 vcc_lo, vcc_lo, s42\n\t"
                         "s_addc_u32 vcc_hi, vcc_hi, 0\n\t" "s_setpc_b64 vcc\n\t" "s_nop 0\n\t" "s_nop 0\n\t" "s_nop 0\n\t" "s_nop 0\n\t" "s_nop 0\n\t" "s_nop 0\n\t" "s_nop 0\n\t" "s_nop 0\n\t" "s_nop 0\n\t" "s_nop 0\n\t"
                         "s_nop 0\n\t" "s_nop 0\n\t" "s_nop 0\n\t" "s_nop 0\n\t" "s_nop 0\n\t" "s_nop 0\n\t" "s_nop 0\n\t" "s_nop 0\n\t" "s_nop 0\n\t" "s_nop 0\n\t"
                         "global_load_dwordx4 a[0:3], v5, %7 offset:-4096 sc1\n\t" "global_load_dwordx4 a[4:7], v5, %7 offset:-3072 sc1\n\t" "global_load_dwordx4 a[16:19], v5, %7 sc1\n\t"
                         "global_load_dwordx4 a[20:23], v5, %7 offset:1024 sc1\n\t" "global_load_dwordx4 a[32:35], v5, %8 offset:-4096 sc1\n\t" "global_load_dwordx4 a[36:39], v5, %8 offset:-3072 sc1\n\t"
                         "global_load_dwordx4 a[48:51], v5, %8 sc1\n\t" "global_load_dwordx4 a[52:55], v5, %8 offset:1024 sc1\n\t" "s_nop 14\n\t" "global_load_dwordx4 a[8:11], %12, %7 offset:-2048 sc1\n\t"
                         "global_load_dwordx4 a[12:15], %12, %7 offset:-1024 sc1\n\t" "global_load_dwordx4 a[24:27], %12, %7 offset:2048 sc1\n\t" "global_load_dwordx4 a[28:31], %12, %7 offset:3072 sc1\n\t"
                         "global_load_dwordx4 a[40:43], %12, %8 offset:-2048 sc1\n\t" "global_load_dwordx4 a[44:47], %12, %8 offset:-1024 sc1\n\t" "global_load_dwordx4 a[56:59], %12, %8 offset:2048 sc1\n\t"
                         "global_load_dwordx4 a[60:63], %12, %8 offset:3072 sc1\n\t" "s_nop 15\n\t" "s_nop 5\n\t" "s_mov_b64 exec, %9\n\t" "global_load_dwordx4 a[64:67], v6, %6 offset:-2048 sc1\n\t"
                         "global_load_dwordx4 a[68:71], v6, %6 offset:-1536 sc1\n\t" "global_load_dwordx4 a[72:75], v6, %6 offset:-1024 sc1\n\t" "global_load_dwordx4 a[76:79], v6, %6 offset:-512 sc1\n\t"
                         "s_mov_b64 exec, -1\n\t" "global_load_dwordx4 a[80:83], %14, %6 sc1\n\t" "global_load_dwordx4 a[84:87], %14, %6 offset:1024 sc1\n\t" "global_load_dwordx4 a[88:91], %14, %6 offset:2048 sc1\n\t"
                         "global_load_dwordx4 a[92:95], %14, %6 offset:3072 sc1\n\t" "s_mov_b32 s42, %11\n\t" ".Lsp_v5:\n\t" "s_sub_u32 s42, s42, 1\n\t" "s_cmp_gt_i32 s42, 0\n\t" "s_cbranch_scc1 .Lsp_v5\n\t"
                         "global_load_dwordx4 a[96:99], v4, %3 offset:-512 sc1\n\t" "global_load_dwordx4 a[100:103], %13, %3 sc1\n\t" "global_load_dwordx4 a[104:107], v4, %3 offset:512 sc1\n\t"
                         "global_load_dwordx4 a[108:111], %13, %3 offset:1024 sc1\n\t" "global_load_dwordx4 a[112:115], v4, %3 offset:1536 sc1\n\t" "global_load_dwordx4 a[116:119], %13, %3 offset:2048 sc1\n\t"
                         "global_load_dwordx4 a[120:123], v4, %3 offset:2560 sc1\n\t" "global_load_dwordx4 a[124:127], %13, %3 offset:3072 sc1\n\t" "s_waitcnt vmcnt(31)\n\t"
                         "v_smfmac_f32_16x16x64_bf16 v[48:51], a[0:3], v[112:119], v9\n\t" "s_waitcnt vmcnt(30)\n\t" "v_smfmac_f32_16x16x64_bf16 v[52:55], a[4:7], v[120:127], v9\n\t" "s_waitcnt vmcnt(29)\n\t"
                         "v_smfmac_f32_16x16x64_bf16 v[64:67], a[16:19], v[144:151], v9\n\t" "s_waitcnt vmcnt(28)\n\t" "v_cvt_pk_bf16_f32 v16, v48, v49\n\t" "v_cvt_pk_bf16_f32 v17, v50, v51\n\t"
                         "v_smfmac_f32_16x16x64_bf16 v[68:71], a[20:23], v[152:159], v9\n\t" "s_waitcnt vmcnt(27)\n\t" "v_cvt_pk_bf16_f32 v18, v52, v53\n\t" "v_cvt_pk_bf16_f32 v19, v54, v55\n\t"
                         "v_smfmac_f32_16x16x64_bf16 v[80:83], a[32:35], v[176:183], v9\n\t" "s_waitcnt vmcnt(26)\n\t" "v_cvt_pk_bf16_f32 v24, v64, v65\n\t" "v_cvt_pk_bf16_f32 v25, v66, v67\n\t"
                         "v_smfmac_f32_16x16x64_bf16 v[84:87], a[36:39], v[184:191], v9\n\t" "s_waitcnt vmcnt(25)\n\t" "v_cvt_pk_bf16_f32 v26, v68, v69\n\t" "v_cvt_pk_bf16_f32 v27, v70, v71\n\t"
                         "v_smfmac_f32_16x16x64_bf16 v[96:99], a[48:51], v[208:215], v9\n\t" "s_waitcnt vmcnt(24)\n\t" "v_cvt_pk_bf16_f32 v32, v80, v81\n\t" "v_cvt_pk_bf16_f32 v33, v82, v83\n\t"
                         "v_smfmac_f32_16x16x64_bf16 v[100:103], a[52:55], v[216:223], v9\n\t" "s_waitcnt vmcnt(23)\n\t" "v_cvt_pk_bf16_f32 v34, v84, v85\n\t" "v_cvt_pk_bf16_f32 v35, v86, v87\n\t"
                         "v_smfmac_f32_16x16x64_bf16 v[56:59], a[8:11], v[128:135], v9\n\t" "s_waitcnt vmcnt(22)\n\t" "v_cvt_pk_bf16_f32 v40, v96, v97\n\t" "v_cvt_pk_bf16_f32 v41, v98, v99\n\t"
                         "v_smfmac_f32_16x16x64_bf16 v[60:63], a[12:15], v[136:143], v9\n\t" "s_waitcnt vmcnt(21)\n\t" "v_cvt_pk_bf16_f32 v42, v100, v101\n\t" "v_cvt_pk_bf16_f32 v43, v102, v103\n\t"
                         "v_smfmac_f32_16x16x64_bf16 v[72:75], a[24:27], v[160:167], v9\n\t" "s_waitcnt vmcnt(20)\n\t" "v_cvt_pk_bf16_f32 v20, v56, v57\n\t" "v_cvt_pk_bf16_f32 v21, v58, v59\n\t"
                         "v_smfmac_f32_16x16x64_bf16 v[76:79], a[28:31], v[168:175], v9\n\t" "s_waitcnt vmcnt(19)\n\t" "v_cvt_pk_bf16_f32 v22, v60, v61\n\t" "v_cvt_pk_bf16_f32 v23, v62, v63\n\t"
                         "v_smfmac_f32_16x16x64_bf16 v[88:91], a[40:43], v[192:199], v9\n\t" "s_waitcnt vmcnt(18)\n\t" "v_cvt_pk_bf16_f32 v28, v72, v73\n\t" "v_cvt_pk_bf16_f32 v29, v74, v75\n\t"
                         "v_smfmac_f32_16x16x64_bf16 v[92:95], a[44:47], v[200:207], v9\n\t" "s_waitcnt vmcnt(17)\n\t" "v_cvt_pk_bf16_f32 v30, v76, v77\n\t" "v_cvt_pk_bf16_f32 v31, v78, v79\n\t"
                         "v_smfmac_f32_16x16x64_bf16 v[104:107], a[56:59], v[224:231], v9\n\t" "s_waitcnt vmcnt(16)\n\t" "v_cvt_pk_bf16_f32 v36, v88, v89\n\t" "v_cvt_pk_bf16_f32 v37, v90, v91\n\t"
                         "v_smfmac_f32_16x16x64_bf16 v[108:111], a[60:63], v[232:239], v9\n\t" "s_waitcnt vmcnt(15)\n\t" "v_cvt_pk_bf16_f32 v38, v92, v93\n\t" "v_cvt_pk_bf16_f32 v39, v94, v95\n\t"
                         "v_smfmac_f32_16x16x64_bf16 v[0:3], a[64:67], v[16:23], v8\n\t" "s_waitcnt vmcnt(14)\n\t" "v_cvt_pk_bf16_f32 v44, v104, v105\n\t" "v_cvt_pk_bf16_f32 v45, v106, v107\n\t"
                         "v_smfmac_f32_16x16x64_bf16 v[0:3], a[68:71], v[24:31], v8\n\t" "s_waitcnt vmcnt(13)\n\t" "v_cvt_pk_bf16_f32 v46, v108, v109\n\t" "v_cvt_pk_bf16_f32 v47, v110, v111\n\t"
                         "v_smfmac_f32_16x16x64_bf16 v[0:3], a[72:75], v[32:39], v8\n\t" "s_waitcnt vmcnt(12)\n\t" "v_smfmac_f32_16x16x64_bf16 v[0:3], a[76:79], v[40:47], v8\n\t" "s_waitcnt vmcnt(6)\n\t"
                         "v_smfmac_f32_16x16x64_bf16 v[0:3], a[80:83], a[96:103], v8\n\t" "s_waitcnt vmcnt(4)\n\t" "v_smfmac_f32_16x16x64_bf16 v[0:3], a[84:87], a[104:111], v8\n\t" "s_waitcnt vmcnt(2)\n\t"
                         "v_smfmac_f32_16x16x64_bf16 v[0:3], a[88:91], a[112:119], v8\n\t" "s_waitcnt vmcnt(0)\n\t" "v_smfmac_f32_16x16x64_bf16 v[0:3], a[92:95], a[120:127], v8\n\t" "s_branch .Lv_end\n\t" ".Lsp_v0:\n\t"
                         "s_memtime vcc\n\t" "s_waitcnt lgkmcnt(0)\n\t" "s_sub_i32 s42, %10, vcc_lo\n\t" "s_cmp_gt_i32 s42, 80\n\t" "s_cbranch_scc1 .Lsp_v0\n\t" "s_max_i32 s42, s42, 0\n\t" "s_sub_i32 s42, 92, s42\n\t"
                         "s_and_b32 s42, s42, -4\n\t" "s_getpc_b64 vcc\n\t" "s_add_u32 vcc_lo, vcc_lo, s42\n\t" "s_addc_u32 vcc_hi, vcc_hi, 0\n\t" "s_setpc_b64 vcc\n\t" "s_nop 0\n\t" "s_nop 0\n\t" "s_nop 0\n\t" "s_nop 0\n\t"
                         "s_nop 0\n\t" "s_nop 0\n\t" "s_nop 0\n\t" "s_nop 0\n\t" "s_nop 0\n\t" "s_nop 0\n\t" "s_nop 0\n\t" "s_nop 0\n\t" "s_nop 0\n\t" "s_nop 0\n\t" "s_nop 0\n\t" "s_nop 0\n\t" "s_nop 0\n\t" "s_nop 0\n\t"
                         "s_nop 0\n\t" "s_nop 0\n\t" "global_load_dwordx4 a[96:99], v4, %3 offset:-512 sc1\n\t" "global_load_dwordx4 a[104:107], v4, %3 offset:512 sc1\n\t"
                         "global_load_dwordx4 a[112:115], v4, %3 offset:1536 sc1\n\t" "global_load_dwordx4 a[120:123], v4, %3 offset:2560 sc1\n\t" "global_load_dwordx4 a[100:103], %13, %3 sc1\n\t"
                         "global_load_dwordx4 a[108:111], %13, %3 offset:1024 sc1\n\t" "global_load_dwordx4 a[116:119], %13, %3 offset:2048 sc1\n\t" "global_load_dwordx4 a[124:127], %13, %3 offset:3072 sc1\n\t" "s_nop 9\n\t"
                         "global_load_dwordx4 a[0:3], v5, %7 offset:-4096 sc1\n\t" "global_load_dwordx4 a[4:7], v5, %7 offset:-3072 sc1\n\t" "global_load_dwordx4 a[16:19], v5, %7 sc1\n\t"
                         "global_load_dwordx4 a[20:23], v5, %7 offset:1024 sc1\n\t" "global_load_dwordx4 a[32:35], v5, %8 offset:-4096 sc1\n\t" "global_load_dwordx4 a[36:39], v5, %8 offset:-3072 sc1\n\t"
                         "global_load_dwordx4 a[48:51], v5, %8 sc1\n\t" "global_load_dwordx4 a[52:55], v5, %8 offset:1024 sc1\n\t" "s_nop 14\n\t" "global_load_dwordx4 a[8:11], %12, %7 offset:-2048 sc1\n\t"
                         "global_load_dwordx4 a[12:15], %12, %7 offset:-1024 sc1\n\t" "global_load_dwordx4 a[24:27], %12, %7 offset:2048 sc1\n\t" "global_load_dwordx4 a[28:31], %12, %7 offset:3072 sc1\n\t"
                         "global_load_dwordx4 a[40:43], %12, %8 offset:-2048 sc1\n\t" "global_load_dwordx4 a[44:47], %12, %8 offset:-1024 sc1\n\t" "global_load_dwordx4 a[56:59], %12, %8 offset:2048 sc1\n\t"
                         "global_load_dwordx4 a[60:63], %12, %8 offset:3072 sc1\n\t" "s_nop 15\n\t" "s_nop 5\n\t" "global_load_dwordx4 a[80:83], %14, %6 sc1\n\t"
                         "global_load_dwordx4 a[84:87], %14, %6 offset:1024 sc1\n\t" "global_load_dwordx4 a[88:91], %14, %6 offset:2048 sc1\n\t" "global_load_dwordx4 a[92:95], %14, %6 offset:3072 sc1\n\t"
                         "s_mov_b64 exec, %9\n\t" "global_load_dwordx4 a[64:67], v6, %6 offset:-2048 sc1\n\t" "global_load_dwordx4 a[68:71], v6, %6 offset:-1536 sc1\n\t"
                         "global_load_dwordx4 a[72:75], v6, %6 offset:-1024 sc1\n\t" "global_load_dwordx4 a[76:79], v6, %6 offset:-512 sc1\n\t" "s_mov_b64 exec, -1\n\t" "s_waitcnt vmcnt(23)\n\t"
                         "v_smfmac_f32_16x16x64_bf16 v[48:51], a[0:3], v[112:119], v9\n\t" "s_waitcnt vmcnt(22)\n\t" "v_smfmac_f32_16x16x64_bf16 v[52:55], a[4:7], v[120:127], v9\n\t" "s_waitcnt vmcnt(21)\n\t"
                         "v_smfmac_f32_16x16x64_bf16 v[64:67], a[16:19], v[144:151], v9\n\t" "s_waitcnt vmcnt(20)\n\t" "v_cvt_pk_bf16_f32 v16, v48, v49\n\t" "v_cvt_pk_bf16_f32 v17, v50, v51\n\t"
                         "v_smfmac_f32_16x16x64_bf16 v[68:71], a[20:23], v[152:159], v9\n\t" "s_waitcnt vmcnt(19)\n\t" "v_cvt_pk_bf16_f32 v18, v52, v53\n\t" "v_cvt_pk_bf16_f32 v19, v54, v55\n\t"
                         "v_smfmac_f32_16x16x64_bf16 v[80:83], a[32:35], v[176:183], v9\n\t" "s_waitcnt vmcnt(18)\n\t" "v_cvt_pk_bf16_f32 v24, v64, v65\n\t" "v_cvt_pk_bf16_f32 v25, v66, v67\n\t"
                         "v_smfmac_f32_16x16x64_bf16 v[84:87], a[36:39], v[184:191], v9\n\t" "s_waitcnt vmcnt(17)\n\t" "v_cvt_pk_bf16_f32 v26, v68, v69\n\t" "v_cvt_pk_bf16_f32 v27, v70, v71\n\t"
                         "v_smfmac_f32_16x16x64_bf16 v[96:99], a[48:51], v[208:215], v9\n\t" "s_waitcnt vmcnt(16)\n\t" "v_cvt_pk_bf16_f32 v32, v80, v81\n\t" "v_cvt_pk_bf16_f32 v33, v82, v83\n\t"
                         "v_smfmac_f32_16x16x64_bf16 v[100:103], a[52:55], v[216:223], v9\n\t" "s_waitcnt vmcnt(15)\n\t" "v_cvt_pk_bf16_f32 v34, v84, v85\n\t" "v_cvt_pk_bf16_f32 v35, v86, v87\n\t"
                         "v_smfmac_f32_16x16x64_bf16 v[56:59], a[8:11], v[128:135], v9\n\t" "s_waitcnt vmcnt(14)\n\t" "v_cvt_pk_bf16_f32 v40, v96, v97\n\t" "v_cvt_pk_bf16_f32 v41, v98, v99\n\t"
                         "v_smfmac_f32_16x16x64_bf16 v[60:63], a[12:15], v[136:143], v9\n\t" "s_waitcnt vmcnt(13)\n\t" "v_cvt_pk_bf16_f32 v42, v100, v101\n\t" "v_cvt_pk_bf16_f32 v43, v102, v103\n\t"
                         "v_smfmac_f32_16x16x64_bf16 v[72:75], a[24:27], v[160:167], v9\n\t" "s_waitcnt vmcnt(12)\n\t" "v_cvt_pk_bf16_f32 v20, v56, v57\n\t" "v_cvt_pk_bf16_f32 v21, v58, v59\n\t"
                         "v_smfmac_f32_16x16x64_bf16 v[76:79], a[28:31], v[168:175], v9\n\t" "s_waitcnt vmcnt(11)\n\t" "v_cvt_pk_bf16_f32 v22, v60, v61\n\t" "v_cvt_pk_bf16_f32 v23, v62, v63\n\t"
                         "v_smfmac_f32_16x16x64_bf16 v[88:91], a[40:43], v[192:199], v9\n\t" "s_waitcnt vmcnt(10)\n\t" "v_cvt_pk_bf16_f32 v28, v72, v73\n\t" "v_cvt_pk_bf16_f32 v29, v74, v75\n\t"
                         "v_smfmac_f32_16x16x64_bf16 v[92:95], a[44:47], v[200:207], v9\n\t" "s_waitcnt vmcnt(9)\n\t" "v_cvt_pk_bf16_f32 v30, v76, v77\n\t" "v_cvt_pk_bf16_f32 v31, v78, v79\n\t"
                         "v_smfmac_f32_16x16x64_bf16 v[104:107], a[56:59], v[224:231], v9\n\t" "s_waitcnt vmcnt(8)\n\t" "v_cvt_pk_bf16_f32 v36, v88, v89\n\t"
                         "v_smfmac_f32_16x16x64_bf16 v[108:111], a[60:63], v[232:239], v9\n\t" "s_waitcnt vmcnt(7)\n\t" "v_cvt_pk_bf16_f32 v37, v90, v91\n\t"
                         "v_smfmac_f32_16x16x64_bf16 v[0:3], a[80:83], a[96:103], v8\n\t" "s_waitcnt vmcnt(6)\n\t" "v_cvt_pk_bf16_f32 v38, v92, v93\n\t" "v_smfmac_f32_16x16x64_bf16 v[0:3], a[84:87], a[104:111], v8\n\t"
                         "s_waitcnt vmcnt(5)\n\t" "v_cvt_pk_bf16_f32 v39, v94, v95\n\t" "v_smfmac_f32_16x16x64_bf16 v[0:3], a[88:91], a[112:119], v8\n\t" "s_waitcnt vmcnt(4)\n\t" "v_cvt_pk_bf16_f32 v44, v104, v105\n\t"
                         "v_smfmac_f32_16x16x64_bf16 v[0:3], a[92:95], a[120:127], v8\n\t" "s_waitcnt vmcnt(3)\n\t" "v_cvt_pk_bf16_f32 v45, v106, v107\n\t" "v_smfmac_f32_16x16x64_bf16 v[0:3], a[64:67], v[16:23], v8\n\t"
                         "s_waitcnt vmcnt(2)\n\t" "v_cvt_pk_bf16_f32 v46, v108, v109\n\t" "v_smfmac_f32_16x16x64_bf16 v[0:3], a[68:71], v[24:31], v8\n\t" "s_waitcnt vmcnt(1)\n\t" "v_cvt_pk_bf16_f32 v47, v110, v111\n\t"
                         "v_smfmac_f32_16x16x64_bf16 v[0:3], a[72:75], v[32:39], v8\n\t" "s_waitcnt vmcnt(0)\n\t" "v_smfmac_f32_16x16x64_bf16 v[0:3], a[76:79], v[40:47], v8\n\t" ".Lv_end:\n\t" "s_nop 7\n\t"
                         "v_pk_add_f32 v[0:1], v[0:1], v[2:3]\n\t" "v_cvt_pk_bf16_f32 v2, v0, v1\n\t" "v_cmp_u_f32 vcc, v0, v1\n\t" "s_cmp_eq_u64 vcc, 0\n\t"
                         "v_mov_b32_dpp v3, v2 quad_perm:[1,0,3,2] row_mask:0xf bank_mask:0xf\n\t" "s_cselect_b64 %0, -1, 0\n\t" "v_perm_b32 v0, v2, v3, v11\n\t" "s_mov_b64 exec, %0\n\t"
                         "global_store_dword v10, v0, %5 offset:-4096 sc1\n\t" "global_store_dword v10, v0, %4 offset:-4096 sc1\n\t" "s_memrealtime %1\n\t" "s_mov_b64 exec, -1\n\t" "s_waitcnt lgkmcnt(0)\n\t"
                         : "=&s"(ok), "=&s"(tp8)
                         : "{v[4:11]}"(ad), "s"(up), "s"(mnx), "s"((u64)mnx ^ 8192), "s"(memp + 16384), "s"(memp + 32768 - 2048), "s"(memp + 49152 - 2048), "s"(0xc30000c3c30000c3ull),
                           "s"(uni((int)(tgt - (u32)(v < 16 ? cfg.dU - (v >> 3) * cfg.s2 : cfg.cR2)))), "s"(v < 16 ? 0 : (v < 24 ? 1 : cfg.T3)), "v"(pob), "v"(uob), "v"(pmo), VALL : "memory", "vcc", "scc", "s42");
            if (__builtin_expect(ok != 0, 1) || s < 0) break;
            if (all || --budget < 0) {
                asm volatile("v_min_u32 v0, -2, v0\n\tglobal_store_dword v10, v0, %0 offset:-4096 sc1\n\tglobal_store_dword v10, v0, %1 offset:-4096 sc1"
                             : : "s"(mnx), "s"((u64)mnx ^ 8192), "{v[4:11]}"(ad), VALL : "memory");
                break;
            }
            if ((++tries & 15) == 0) {
                u32 m = 0;
#pragma nounroll
                for (int i = 0; i < 32; ++i) {
                    u32x4 t;
                    gld16c<0>(t, i < 24 ? memp + 12288 : up, i < 20 ? (4 + i + ((4 + i) & 24)) * 1024 + ad[3] : (i < 24 ? i * 512 - 8192 + ad[2] : (i & 7) * 512 - 512 + (i & 1 ? uob : ad[0]))); waitv<0>(t);
                    m = max3u(max3u(m, t[0], t[1]), t[2], t[3]);
                }
                all = __builtin_amdgcn_ballot_w64(m == 0xffffffffu) == 0;
            }
            phi += (u32)cfg.G;
            zero();
        }
        asm volatile("s_barrier\n\t" "ds_read_b128 v[112:115], %0\n\t" "ds_read_b128 v[116:119], %0 offset:1024\n\t" "ds_read_b128 v[120:123], %0 offset:2048\n\t" "ds_read_b128 v[124:127], %0 offset:3072\n\t"
                     "ds_read_b128 v[128:131], %0 offset:4096\n\t" "ds_read_b128 v[132:135], %0 offset:5120\n\t" "ds_read_b128 v[136:139], %0 offset:6144\n\t" "ds_read_b128 v[140:143], %0 offset:7168\n\t"
                     "ds_read_b128 v[144:147], %0 offset:8192\n\t" "ds_read_b128 v[148:151], %0 offset:9216\n\t" "ds_read_b128 v[152:155], %0 offset:10240\n\t" "ds_read_b128 v[156:159], %0 offset:11264\n\t"
                     "ds_read_b128 v[160:163], %0 offset:12288\n\t" "ds_read_b128 v[164:167], %0 offset:13312\n\t" "ds_read_b128 v[168:171], %0 offset:14336\n\t" "ds_read_b128 v[172:175], %0 offset:15360\n\t"
                     "ds_read_b128 v[176:179], %0 offset:16384\n\t" "ds_read_b128 v[180:183], %0 offset:17408\n\t" "ds_read_b128 v[184:187], %0 offset:18432\n\t" "ds_read_b128 v[188:191], %0 offset:19456\n\t"
                     "ds_read_b128 v[192:195], %0 offset:20480\n\t" "ds_read_b128 v[196:199], %0 offset:21504\n\t" "ds_read_b128 v[200:203], %0 offset:22528\n\t" "ds_read_b128 v[204:207], %0 offset:23552\n\t"
                     "ds_read_b128 v[208:211], %0 offset:24576\n\t" "ds_read_b128 v[212:215], %0 offset:25600\n\t" "ds_read_b128 v[216:219], %0 offset:26624\n\t" "ds_read_b128 v[220:223], %0 offset:27648\n\t"
                     "ds_read_b128 v[224:227], %0 offset:28672\n\t" "ds_read_b128 v[228:231], %0 offset:29696\n\t" "ds_read_b128 v[232:235], %0 offset:30720\n\t" "ds_read_b128 v[236:239], %0 offset:31744\n\t"
                     : : "v"(lds + ad[3] + ((s + 1) & 1) * 32768), VALL : "memory");
        if (s >= 0) {
            follow(phi, (int)(tt & 0xffffu) + 5 * (v >> 2) / 2 - VISV, (u32)tp8, cfg.G);
            if (vb == 0 && lane < 8) gst8(fl, (F_CUR + lane) * 128, u32x2{(u32)s - 1, phi + (tt & 0xffffu)});
        }
        phi += tt & 0xffffu;
        memp = mnx; up = ub + uslot(sd, s + 1);
        zero();
        tt = sched_ld0(s + 1);
        clk.calib();
        while (s >= 0) {
            u32 h0;
            spin_to(clk.at(phi + (u32)cfg.dV));
            asm volatile("global_load_dword %0, %1, %2 offset:-4096 sc1\n\ts_sleep 5" : "=&v"(h0) : "v"(ho), "s"(memp) : "memory");
            edge(clk.r1, clk.m1);
            asm volatile("s_waitcnt vmcnt(0)" : "+v"(h0) : : "memory");
            if (__builtin_amdgcn_ballot_w64(h0 == 0xffffffffu) == 0 || --budget < 0) break;
            phi += (u32)cfg.G;
        }
        asm volatile(".Lkm_lv:");
    }
}

DI void xcopy_task(const Ptrs& p, int j, int w) {
    const int tid = 64 * w + lanex();
    Sync y;
    sync_ld(y, p.ws);
    u32x4 xv[10];
    sfor<0, 10>([&](auto I) { gld16<I.v * 128>(xv[I.v], p.x + (size_t)j * 40960, (u32)((tid & 31) * 1280 + (tid >> 5) * 16)); });
    sfor<0, 10>([&](auto I) { waitv<9 - I.v>(xv[I.v]); });
    asm volatile("" : "+v"(y.r), "+v"(y.g), "+v"(y.e));
    sfor<0, 10>([&](auto I) { gst16(p.ws + OFF_XF + (size_t)j * 40960 + I.v * 4096, (u32)tid * 16, xv[I.v]); });
    waitvm<0>();
    barrier();
    const u64 t = sync_tag(y.e);
    if (tid == 0) gst8(p.ws + OFF_LAT, 1536 + (u32)j * 8, u32x2{(u32)t, (u32)(t >> 32)});
}
DI void xcopy_wait(const char* ws, u64 t) {
#pragma nounroll
    for (int it = 0; it < (1 << 16); ++it) {
        u32x2 r;
        asm volatile("s_nop 4\n\tglobal_load_dwordx2 %0, %1, %2 offset:1536 sc1\n\ts_waitcnt vmcnt(0)" : "=&v"(r) : "v"(lanex() * 8), "s"(ws + OFF_LAT) : "memory");
        if (__builtin_amdgcn_ballot_w64(r[0] != (u32)t || r[1] != (u32)(t >> 32)) == 0) break;
        __builtin_amdgcn_s_sleep(16);
    }
}
DI void init_task(const Ptrs& p, int j, u32 lds, int w) {
    const int lane = lanex(), tid = 64 * w + lane;
    Sync y;
    sync_ld(y, p.ws);
    const u32x4 ff = u32x4{~0u, ~0u, ~0u, ~0u};
    const int i = j * 256 + tid, r = j < 8 ? j & 1 : min(j / 2 - 2, NREG - 1);
    char* const vb = p.ws + (r < 2 ? 0 : OFF_U - (4u << 20)) + ((size_t)r << 21) + 1984 * 1024 + (j < 8 ? j & 6 : (j & 1) * 2 + (j >> 1 == 15) * 4) * 8192;
    const u32 vo = (u32)((w >> 1) * 8192 + (w & 1) * 1024);
    gst16(vb, vo + lane * 16, ff);
    if (i < F_N * 8) gst16(p.ws + OFF_ST, (u32)i * 16, u32x4{i == F_ONE * 8 ? 1000u : ~0u, ~0u, ~0u, ~0u});
    gst16(p.ws + 65536, (u32)i * 16, ff);
    if (i < 4096) {
        const int k = (i & 511) + ((i >> 1) & 0x600);
        u32x4 m;
        gld16<0>(m, p.mem0, (u32)((2 * ((k >> 2) & 3) + (k & 1)) * 1280 + min(2 * (k >> 4) + ((k >> 1) & 1), 79) * 16));
        waitv<0>(m);
        gst16(p.ws, (u32)i * 16, k < 640 ? u32x4{min(m[0], 0xfffffffeu), min(m[1], 0xfffffffeu), min(m[2], 0xfffffffeu), min(m[3], 0xfffffffeu)} : ff);
    }
#pragma unroll
    for (int c = 0; c < 6; ++c) gst16(p.ws + OFF_U + c * 131072 - (c == 5) * 122880, (u32)i * 16, ff);
    waitvm<0>();
    asm volatile("" : "+v"(y.r), "+v"(y.g), "+v"(y.e));
    barrier();
    if (w) return;
    u32 dt[2] = {0xffffffu, 0xffffffu};
#pragma unroll
    for (int c = 0; c < 2 * NSAMP; ++c) {
        u32 v;
        const u32 t0 = mt();
        asm volatile("global_load_dword %0, %1, %2 sc1\n\ts_waitcnt vmcnt(0)" : "=&v"(v) : "v"((c & 1) * 8192 + (c >> 1) * 256), "s"(vb) : "memory");
        dt[c & 1] = min(dt[c & 1], mt() - t0);
    }
    const u64 t = sync_tag(y.e);
    const u32 vote = (u32)((dt[0] > dt[1]) ^ xhalf() ^ (int)((vb - p.ws) >> 21)) & 1u;
    if (tid == 0) {
        if (j == 0) gst8(p.ws + OFF_LAT, 512, u32x2{(u32)t, rt()});
        gst8(p.ws + OFF_LAT, (u32)j * 8, u32x2{(u32)t | vote, (u32)(t >> 32)});
    }
}

DI float xmax(float v) {
    const auto a = __builtin_amdgcn_permlane32_swap(__float_as_uint(v), __float_as_uint(v), false, false);
    v = fmaxf(__uint_as_float(a[0]), __uint_as_float(a[1]));
    const auto b = __builtin_amdgcn_permlane16_swap(__float_as_uint(v), __float_as_uint(v), false, false);
    return fmaxf(__uint_as_float(b[0]), __uint_as_float(b[1]));
}
DI float xsum(float v) {
    const auto a = __builtin_amdgcn_permlane32_swap(__float_as_uint(v), __float_as_uint(v), false, false);
    v = __uint_as_float(a[0]) + __uint_as_float(a[1]);
    const auto b = __builtin_amdgcn_permlane16_swap(__float_as_uint(v), __float_as_uint(v), false, false);
    return __uint_as_float(b[0]) + __uint_as_float(b[1]);
}
DI void gst16c(char* sbase, u32 voff, u32x4 v) {
    asm volatile("global_store_dwordx4 %0, %1, %2 sc1\n\ts_nop 1" : : "v"(voff), "v"(v), "s"(sbase) : "memory");
}
DI void gst8c(char* sbase, u32 voff, u32x2 v) {
    asm volatile("global_store_dwordx2 %0, %1, %2 sc1\n\ts_nop 1" : : "v"(voff), "v"(v), "s"(sbase) : "memory");
}
#define WAIT6(N, r, o) asm volatile("s_waitcnt vmcnt(" #N ")" : "+v"(r[o]), "+v"(r[o + 1]), "+v"(r[o + 2]), "+v"(r[o + 3]), "+v"(r[o + 4]), "+v"(r[o + 5]) : : "memory")
#define WSLEEP 90
DI void wg_wait(const char* fl, int w, int i0, int t0, int i1, int t1, int i2, int t2, int i3, int t3, int dt, bool acq) {
    if (w == 0) {
        const int lane = lanex(), tg = lane == 1 ? t1 : (lane == 2 ? t2 : (lane == 3 ? t3 : t0));
        const int ix = tg <= 0 ? i0 : (lane == 1 ? i1 : (lane == 2 ? i2 : (lane == 3 ? i3 : i0)));
#pragma nounroll
        for (int it = 0; it < (1 << 14); ++it) {
            u32 v;
            flag_ld(v, fl, ix);
            asm volatile("s_waitcnt vmcnt(0)" : "+v"(v) : : "memory");
            if (__builtin_amdgcn_ballot_w64((int)(v + 1u) < tg) == 0) break;
            const u32 wake = rt() + (u32)(dt + 180 * min(max(t0 - 2 - uni((int)v), 0), 64));
            while ((int)(rt() - wake) < 0) __builtin_amdgcn_s_sleep(4);
        }
        if (acq) acquire();
    }
    barrier();
}
#define TEST 172
DI void until(u32 t) { while ((int)(rt() - t) < 0) __builtin_amdgcn_s_sleep(1); }
DI u32 step_wait(const char* fl, int w, int gr, int t, u32 lds) {
    if (w == 0) {
        u32x2 v;
#pragma nounroll
        for (int it = 0; it < (1 << 14); ++it) {
            asm volatile("s_nop 4\n\tglobal_load_dwordx2 %0, %1, %2 sc1\n\ts_waitcnt vmcnt(0)" : "=&v"(v) : "v"((F_CUR + gr) * 128), "s"(fl) : "memory");
            const int d = t - 1 - uni((int)v[0]);
            if (d <= 0) { v[1] += (u32)(d * TEST); break; }
            const u32 now = rt();
            nap(min(max((int)((u32)uni((int)v[1]) - now) + d * TEST + 10, 40), 2000));
        }
        *LDSP(u32, lds + GRING * GBUF + 4) = v[1];
    }
    barrier();
    return (u32)uni((int)*LDSP(u32, lds + GRING * GBUF + 4));
}
#define PDL 135
#define ODL 280
#define TDT 35
template <bool LATE>
DI void px_wave(const Ptrs& p, const ChainCfg& cfg, int s, int h, int slow, int w, u32 lds, bool keep) {
    const int lane = lanex(), n = lane & 15, g = lane >> 4;
    const char* qb = p.ws + OFF_QX + (size_t)(s * NH + h) * 12288;
    const char* kb = p.ws + OFF_KX + (size_t)(s * NH + h) * 12288;
    const char* mb = p.ws + OFF_KM + (size_t)(s * NH + h) * 3072;
    const u32 ko = (u32)((n >> 3) * 6144 + (8 * ((n >> 2) & 1) + (n & 3) + 16 * g) * 16);
    const u32 mo = (u32)(g * 128 + (n & 7) * 16);
    u32x4 qf[12], kf[12], mf[6];
    sfor<0, 12>([&](auto I) { gld16c<(I.v % 3) * 1024>(qf[I.v], qb + (I.v / 3) * 3072, lane * 16); gap<SMP>(slow); });
    if (!SMP) pace(slow);
    sfor<0, 12>([&](auto I) { gld16c<(I.v % 3) * 1024 + (I.v / 6) * 64>(kf[I.v], kb + ((I.v / 3) & 1) * 3072, ko); gap<SMP>(slow); });
    if (!SMP) pace(slow);
    if constexpr (LATE) { WAIT6(0, qf, 0); WAIT6(0, qf, 6); WAIT6(0, kf, 0); }
    else {
        sfor<0, 6>([&](auto I) { gld16c<I.v * 512>(mf[I.v], mb, mo); });
        WAIT6(24, qf, 0); WAIT6(18, qf, 6); WAIT6(12, kf, 0);
    }
    f32x4 sc[2][3];
#pragma unroll
    for (int tt = 0; tt < 2; ++tt)
#pragma unroll
        for (int T = 0; T < 3; ++T) sc[tt][T] = f32x4{0.f, 0.f, 0.f, 0.f};
#pragma unroll
    for (int ks = 0; ks < 6; ++ks) { sc[0][0] = mfma16(kf[ks], qf[ks], sc[0][0]); sc[1][0] = mfma16(kf[ks], qf[6 + ks], sc[1][0]); }
    WAIT6(6, kf, 6);
#pragma unroll
    for (int ks = 0; ks < 6; ++ks) { sc[0][1] = mfma16(kf[6 + ks], qf[ks], sc[0][1]); sc[1][1] = mfma16(kf[6 + ks], qf[6 + ks], sc[1][1]); }
    if constexpr (LATE) {
        until(step_wait(p.ws + OFF_ST, w, h & 7, s - 1, lds) + (u32)(s == NSEG - 1 ? cfg.T : cfg.T + PDX));
        bool ok = false;
#pragma nounroll
        for (int it = 0; it < (1 << 14); ++it) {
            u32 v;
            sfor<0, 6>([&](auto I) { gld16c<I.v * 512>(mf[I.v], mb, mo); });
            flag_ldm(v, p.ws + OFF_ST, (s < NSEG - 2 ? F_CUR : F_PROG + 8 * s) + (h & 7), it > 3);
            WAIT6(0, mf, 0);
            asm volatile("" : "+v"(v));
            u32 m = 0;
#pragma unroll
            for (int i = 0; i < 24; ++i) m = max(m, mf[i >> 2][i & 3]);
            if (ok || __builtin_amdgcn_ballot_w64(m == ~0u) == 0) break;
            ok = it > 3 && uni((int)v) >= (s < NSEG - 2 ? s : 7);
            if (s < NSEG - 1) nap(60);
        }
    }
    WAIT6(0, mf, 0);
#pragma unroll
    for (int ks = 0; ks < 6; ++ks) { sc[0][2] = mfma16(mf[ks], qf[ks], sc[0][2]); sc[1][2] = mfma16(mf[ks], qf[6 + ks], sc[1][2]); }
    const float kscale = 0.07216878364870322f;
    char* pb = p.ws + OFF_PX + (size_t)s * 81920;
#pragma unroll
    for (int tt = 0; tt < 2; ++tt) {
        float v[12], mx = -3.0e38f;
#pragma unroll
        for (int i = 0; i < 12; ++i) {
            const float t = rbf(rbf(sc[tt][i >> 2][i & 3]) * kscale);
            v[i] = (i >= 8 || 8 * g + i <= 16 * tt + n) ? t : -3.0e38f;
            mx = fmaxf(mx, v[i]);
        }
        mx = xmax(mx);
        float sx = 0.f, sm = 0.f;
#pragma unroll
        for (int i = 0; i < 12; ++i) {
            v[i] = __builtin_amdgcn_exp2f((v[i] - mx) * 1.4426950408889634f);
            if (i < 8) sx += v[i]; else sm += v[i];
        }
        const float inv = __builtin_amdgcn_rcpf(xsum(sx + 0.5f * sm));
        const u32x4 px = u32x4{cvtpk(v[0] * inv, v[1] * inv), cvtpk(v[2] * inv, v[3] * inv), cvtpk(v[4] * inv, v[5] * inv), cvtpk(v[6] * inv, v[7] * inv)};
        const u32x2 pm = u32x2{cvtpk(v[9] * inv, v[8] * inv), cvtpk(v[11] * inv, v[10] * inv)};
        const int ox = h * 2048 + tt * 1024, om = 65536 + (h >> 2) * 2048 + (h & 3) * 256 + tt * 1024;
        if (!LATE && keep) {
            *LDSP(u32x4, lds + ox + lane * 16) = px;
            *LDSP(u32x2, lds + om + n * 16 + (g & 1) * 8) = pm;
        } else {
            gst16c(pb + ox, lane * 16, px);
            gst8c(pb + om, (u32)(n * 16 + (g & 1) * 8), pm);
        }
    }
}

#define WAIT10(N, r) asm volatile("s_waitcnt vmcnt(" #N ")" : "+v"(r[0]), "+v"(r[1]), "+v"(r[2]), "+v"(r[3]), "+v"(r[4]), "+v"(r[5]), "+v"(r[6]), "+v"(r[7]), "+v"(r[8]), "+v"(r[9]) : : "memory")
DI void out_task(const Ptrs& p, int s, int cg0, int cg1, u32 lds, int slow, int w, int sd) {
    u32x4 pf[2][10];
    if (cg1 - cg0 > 1) {
        const u32 la = lds + lanex() * 16;
#pragma unroll
        for (int i = 0; i < 20; ++i) pf[i & 1][i >> 1] = ldsr16(la + (i < 16 ? w * 16384 + i * 1024 : 65536 + w * 4096 + (i - 16) * 1024));
    } else {
        const char* pb = p.ws + OFF_PX + (size_t)s * 81920;
        const u32 l16 = lanex() * 16;
        sfor<0, 20>([&](auto I) { gld16c<(I.v & 3) * 1024>(pf[I.v & 1][I.v >> 1], pb + (I.v < 16 ? w * 16384 + (I.v >> 2) * 4096 : 65536 + w * 4096), l16); gap<SMO>(slow); if (!SMO && I.v == 9) pace(slow); });
        WAIT10(0, pf[0]); WAIT10(0, pf[1]);
    }
#pragma nounroll
    for (int cg = cg0; cg < cg1; ++cg) {
    const int lane = lanex(), tid = 64 * w + lane, n = lane & 15, g = lane >> 4;
    const char* db = p.ws + OFF_D + s * D_STRIDE + (size_t)(16 * cg) * 16384 + w * 4096;
    const char* ub = p.ws + uslot(sd, s) + (size_t)(16 * cg) * 4096 + w * 1024;
    const u32 dof = (u32)((n >> 3) * 16384 + g * 128 + (n & 7) * 16), uof = (u32)((n >> 3) * 4096 + g * 128 + (n & 7) * 16);
    u32x4 af[3][10];
    auto aload = [&](u32x4 (&r)[10], int ct) {
        if (!SMO) pace(slow);
        sfor<0, 8>([&](auto I) { gld16c<I.v * 512>(r[I.v], db + ct * 32768, dof); gap<SMO>(slow); });
        gld16c<0>(r[8], ub + ct * 8192, uof); gap<SMO>(slow); gld16c<512>(r[9], ub + ct * 8192, uof); gap<SMO>(slow);
    };
    aload(af[0], 0);
    aload(af[1], 1);
    aload(af[2], 2);
    const int tok = 32 * s + (tid >> 3), col = 128 * cg + 8 * (tid & 7);
    u32x4 bb[2]; u32 gv;
    gld16c<0>(bb[0], p.ws + OFF_BOUT, (u32)(s * 40960 + col * 64 + (tid >> 3) * 16)); gld16c<0>(bb[1], p.ws + OFF_BOUT + 4096, (u32)(s * 40960 + col * 64 + (tid >> 3) * 16));
    asm volatile("global_load_dword %0, %1, %2 sc1" : "=v"(gv) : "v"(tok * 4), "s"(p.ws + OFF_GATE) : "memory");
    barrier();
    const u32 wa = lds + (u32)w * 16896 + n * 528 + g * 16;
    sfor<0, 8>([&](auto I) {
        constexpr int ct = I.v;
        u32x4 (&r)[10] = af[ct % 3];
        if constexpr (ct == 0) WAIT10(23, r);
        else if constexpr (ct < 3) WAIT10(23, r);
        else if constexpr (ct < 6) WAIT10(20, r);
        else if constexpr (ct == 6) WAIT10(10, r);
        else WAIT10(0, r);
        f32x4 c0 = f32x4{0.f, 0.f, 0.f, 0.f}, c1 = c0;
#pragma unroll
        for (int i = 0; i < 10; ++i) { c0 = mfma16(r[i], pf[0][i], c0); c1 = mfma16(r[i], pf[1][i], c1); }
        *LDSP(f32x4, wa + ct * 64) = c0;
        *LDSP(f32x4, wa + ct * 64 + 16 * 528) = c1;
        PIN();
        if constexpr (ct + 3 < 8) aload(r, ct + 3);
        PIN();
    });
    asm volatile("" : "+v"(bb[0]), "+v"(bb[1]), "+v"(gv));
    barrier();
    const u32 ra = lds + (u32)(tid >> 3) * 528 + (tid & 7) * 32;
    const float gt = __uint_as_float(gv), g1 = rbf(1.f - gt);
    u32 ov[8];
#pragma unroll
    for (int i = 0; i < 4; ++i) {
        const u32 ri = ra + 16 * (i & 1) + 256 * (i >> 1);
        const f32x4 z = (*LDSP(f32x4, ri) + *LDSP(f32x4, ri + 16896)) + (*LDSP(f32x4, ri + 2 * 16896) + *LDSP(f32x4, ri + 3 * 16896));
#pragma unroll
        for (int j = 0; j < 2; ++j) {
            const u32 b = bb[i >> 1][2 * (i & 1) + j];
            const u32 a = cvtpk(z[2 * j], z[2 * j + 1]);
            const u32 ga = cvtpk(gt * __uint_as_float(a << 16), gt * __uint_as_float(a & 0xffff0000u));
            const u32 gb = cvtpk(g1 * __uint_as_float(b << 16), g1 * __uint_as_float(b & 0xffff0000u));
            ov[2 * i + j] = cvtpk(__uint_as_float(ga << 16) + __uint_as_float(gb << 16), __uint_as_float(ga & 0xffff0000u) + __uint_as_float(gb & 0xffff0000u));
        }
    }
    gst16p(p.out, (u32)(tok * 1280 + col * 2), u32x4{ov[0], ov[1], ov[2], ov[3]});
    gst16p(p.out, (u32)(tok * 1280 + col * 2 + 128), u32x4{ov[4], ov[5], ov[6], ov[7]});
    }
}

DI void out_tail(const Ptrs& p, const ChainCfg& cfg, int s, int cg, u32 lds, int w, int sd, int gr) {
    const int lane = lanex(), tid = 64 * w + lane, n = lane & 15, g = lane >> 4;
    const char* db = p.ws + OFF_D + s * D_STRIDE + (size_t)(4 * cg) * 16384 + w * 4096;
    const char* ub = p.ws + uslot(sd, s) + (size_t)(4 * cg) * 4096 + w * 1024;
    const char* pb = p.ws + OFF_PX + (size_t)s * 81920;
    const u32 dof = (u32)((n >> 3) * 16384 + g * 128 + (n & 7) * 16), uof = (u32)((n >> 3) * 4096 + g * 128 + (n & 7) * 16);
    u32x4 pf[2][10], af[2][10];
    sfor<0, 16>([&](auto I) { gld16c<(I.v & 7) * 512>(af[I.v >> 3][I.v & 7], db + (I.v >> 3) * 32768, dof); });
    const int tok = 32 * s + (tid >> 3), col = 32 * cg + 4 * (tid & 7);
    u32x2 bb; u32 gv;
    asm volatile("global_load_dwordx2 %0, %2, %3 sc1\n\tglobal_load_dword %1, %4, %5 sc1\n\ts_waitcnt vmcnt(0)" : "=&v"(bb), "=&v"(gv)
                 : "v"(s * 40960 + (col >> 3) * 512 + (tid >> 3) * 16 + (col & 7) * 2), "s"(p.ws + OFF_BOUT), "v"(tok * 4), "s"(p.ws + OFF_GATE) : "memory");
    sfor<0, 16>([&](auto I) { waitv<0>(af[I.v >> 3][I.v & 7]); });
    until(step_wait(p.ws + OFF_ST, w, gr, s - 1, lds) + (u32)(s == NSEG - 1 ? cfg.cR : cfg.cR + 2 * PDX));
    const u32 wa = lds + (u32)w * 4608 + n * 144 + g * 16;
#pragma nounroll
    for (int it = 0; it < (1 << 14); ++it) {
        if (it == 4) {
            flag_wait(p.ws + OFF_ST, F_PX + s, 8);
            flag_wait(p.ws + OFF_ST, s < NSEG - 2 ? F_CUR + gr : F_UD + 2 * (s & 1), s < NSEG - 2 ? s + 1 : 16);
            flag_wait(p.ws + OFF_ST, s < NSEG - 2 ? F_ONE : F_UD + 2 * (s & 1) + 1, 16);
        }
        sfor<0, 4>([&](auto I) { gld16c<(I.v & 1) * 512>(af[I.v >> 1][8 + (I.v & 1)], ub + (I.v >> 1) * 8192, uof); });
        sfor<0, 8>([&](auto I) { gld16c<(I.v & 1) * 2048>(pf[0][I.v], pb + w * 16384 + (I.v >> 1) * 4096, lane * 16); });
        gld16c<0>(pf[0][8], pb + 65536 + w * 4096, lane * 16); gld16c<2048>(pf[0][9], pb + 65536 + w * 4096, lane * 16);
        sfor<0, 8>([&](auto I) { gld16c<(I.v & 1) * 2048 + 1024>(pf[1][I.v], pb + w * 16384 + (I.v >> 1) * 4096, lane * 16); });
        gld16c<1024>(pf[1][8], pb + 65536 + w * 4096, lane * 16); gld16c<3072>(pf[1][9], pb + 65536 + w * 4096, lane * 16);
        WAIT10(10, pf[0]); WAIT10(10, af[0]); WAIT10(10, af[1]);
        float chk = 0.f;
#pragma unroll
        for (int tt = 0; tt < 2; ++tt) {
            if (tt) WAIT10(0, pf[1]);
            f32x4 c0 = f32x4{0.f, 0.f, 0.f, 0.f}, c1 = c0;
#pragma unroll
            for (int i = 0; i < 10; ++i) { c0 = mfma16(af[0][i], pf[tt][i], c0); c1 = mfma16(af[1][i], pf[tt][i], c1); }
            *LDSP(f32x4, wa + tt * 2304) = c0;
            *LDSP(f32x4, wa + tt * 2304 + 64) = c1;
            const f32x4 c = c0 + c1;
            chk += (c[0] + c[1]) + (c[2] + c[3]);
            PIN();
        }
        if (it >= 4 || __builtin_amdgcn_ballot_w64((__float_as_uint(chk) & 0x7fffffffu) > 0x7f800000u) == 0) break;
        if (s < NSEG - 1) nap(60);
    }
    barrier();
    const u32 ra = lds + (u32)(tid >> 3) * 144 + (tid & 7) * 16;
    const float gt = __uint_as_float(gv), g1 = rbf(1.f - gt);
    const f32x4 z = (*LDSP(f32x4, ra) + *LDSP(f32x4, ra + 4608)) + (*LDSP(f32x4, ra + 2 * 4608) + *LDSP(f32x4, ra + 3 * 4608));
    u32x2 ov;
#pragma unroll
    for (int j = 0; j < 2; ++j) {
        const u32 b = bb[j], a = cvtpk(z[2 * j], z[2 * j + 1]);
        const u32 ga = cvtpk(gt * __uint_as_float(a << 16), gt * __uint_as_float(a & 0xffff0000u));
        const u32 gb = cvtpk(g1 * __uint_as_float(b << 16), g1 * __uint_as_float(b & 0xffff0000u));
        ov[j] = cvtpk(__uint_as_float(ga << 16) + __uint_as_float(gb << 16), __uint_as_float(ga & 0xffff0000u) + __uint_as_float(gb & 0xffff0000u));
    }
    *(u32x2*)(p.out + tok * 1280 + col * 2) = ov;
}

DI float dot2bf(u32 a, u32 b, float c) { return __builtin_amdgcn_fdot2_f32_bf16(__builtin_bit_cast(bf16x2, a), __builtin_bit_cast(bf16x2, b), c, false); }
DI void battn_task(const Ptrs& p, int t0, u32 lds, int w) {
    const int lane = lanex(), tid = 64 * w + lane, tl = tid & 31, t = t0 + tl;
    const char* const qkv = p.ws + OFF_QKVB + (size_t)(t0 >> 5) * 122880;
    {
        u32 so[3];
#pragma unroll
        for (int i = 0; i < 3; ++i) {
            const int e = 64 * i + lane, sl = e % 48;
            so[i] = (u32)((e / 48) * 512 + (sl < 16 ? (t0 ? 0 : 122880) + 256 + sl * 16 : 122880 - 256 + sl * 16));
        }
#pragma unroll
        for (int i = 0; i < 30; ++i) { glds16c(qkv - 122880 + 40960 + (10 * w + i / 3) * 2048, so[i % 3], lds + (30 * w + i) * 1024); gap<SMB>(-1); }
    }
    u32x4 q[2][5], xv[10], gv[10];
    sfor<0, 10>([&](auto I) { gld16c<(I.v % 5) * 512>(q[I.v / 5][I.v % 5], qkv + (I.v / 5) * 20480, (u32)((tid >> 5) * 2560 + tl * 16)); gap<SMB>(-1); });
    sfor<0, 10>([&](auto I) { gld16<I.v * 128>(xv[I.v], p.x + (size_t)t0 * 1280, (u32)((tid >> 3) * 1280 + (tid & 7) * 16)); gld16<I.v * 128>(gv[I.v], p.gw, (u32)((tid & 7) * 16)); gap<SMB>(-1); });
    sfor<0, 10>([&](auto I) { waitv<0>(q[I.v / 5][I.v % 5]); waitv<0>(xv[I.v]); waitv<0>(gv[I.v]); });
    barrier();
    {
        float a = 0.f;
#pragma unroll
        for (int i = 0; i < 10; ++i)
#pragma unroll
            for (int j = 0; j < 4; ++j) {
                const u32 r = cvtpk(__uint_as_float(xv[i][j] << 16) * __uint_as_float(gv[i][j] << 16), __uint_as_float(xv[i][j] & 0xffff0000u) * __uint_as_float(gv[i][j] & 0xffff0000u));
                a += __uint_as_float(r << 16) + __uint_as_float(r & 0xffff0000u);
            }
        a += __shfl_xor(a, 1); a += __shfl_xor(a, 2); a += __shfl_xor(a, 4);
        const float z = rbf(a);
        if ((tid & 7) == 0) gst4c(p.ws + OFF_GATE, (u32)((t0 + (tid >> 3)) * 4), __float_as_uint(rbf(1.0f / (1.0f + __expf(-z)))));
    }
#pragma unroll
    for (int it = 0; it < 2; ++it) {
        const int m = (tid >> 5) + 8 * it;
        u32 ka = lds + (u32)(m * 3840 + (2 + tl) * 16), va = ka + 61440;
        asm volatile("" : "+v"(ka), "+v"(va));
        u32x4 k[5][8];
#pragma unroll
        for (int i = 0; i < 40; ++i) k[i >> 3][i & 7] = ldsr16(ka + (i >> 3) * 768 + (7 - (i & 7)) * 32);
        PIN();
        float lg[2][8], mx[2] = {-3.0e38f, -3.0e38f};
#pragma unroll
        for (int i = 0; i < 16; ++i) lg[i >> 3][i & 7] = 0.f;
#pragma unroll
        for (int i = 0; i < 5; ++i) {
#pragma unroll
            for (int j = 0; j < 4; ++j)
#pragma unroll
                for (int wv = 0; wv < 8; ++wv) lg[4 * i + j >= 10][wv] = dot2bf(q[it][i][j], k[i][wv][j], lg[4 * i + j >= 10][wv]);
            PIN();
#pragma unroll
            for (int wv = 0; wv < 8; ++wv) k[i][wv] = ldsr16(va + i * 768 + (7 - wv) * 32);
            PIN();
        }
#pragma unroll
        for (int i = 0; i < 16; ++i) {
            float& l = lg[i >> 3][i & 7];
            l = rbf(rbf(l) * 0.22360679774997896f);
            asm volatile("" : "+v"(l));
            l = t - 2 * (i & 7) >= 0 ? l : -3.0e38f;
            mx[i >> 3] = fmaxf(mx[i >> 3], l);
        }
        u32 pk[2][4];
#pragma unroll
        for (int u = 0; u < 2; ++u) {
            float sum = 0.f;
#pragma unroll
            for (int wv = 0; wv < 8; ++wv) { lg[u][wv] = __builtin_amdgcn_exp2f((lg[u][wv] - mx[u]) * 1.4426950408889634f); sum += lg[u][wv]; }
            const float inv = __builtin_amdgcn_rcpf(sum);
#pragma unroll
            for (int wv = 0; wv < 4; ++wv) pk[u][wv] = cvtpk(lg[u][2 * wv] * inv, lg[u][2 * wv + 1] * inv);
        }
#pragma unroll
        for (int i = 0; i < 5; ++i) {
            float o[8];
#pragma unroll
            for (int d = 0; d < 8; ++d) o[d] = 0.f;
#pragma unroll
            for (int wp = 0; wp < 4; ++wp) {
                const u32x4 v0 = k[i][2 * wp], v1 = k[i][2 * wp + 1];
#pragma unroll
                for (int j = 0; j < 4; ++j) {
                    const u32 pw = pk[4 * i + j >= 10][wp];
                    o[2 * j] = dot2bf(__builtin_amdgcn_perm(v1[j], v0[j], 0x05040100u), pw, o[2 * j]);
                    o[2 * j + 1] = dot2bf(__builtin_amdgcn_perm(v1[j], v0[j], 0x07060302u), pw, o[2 * j + 1]);
                }
            }
            gst16c(p.ws + OFF_OB + (size_t)(t0 >> 5) * 40960 + 512 * i, (u32)(m * 2560 + tl * 16), u32x4{cvtpk(o[0], o[1]), cvtpk(o[2], o[3]), cvtpk(o[4], o[5]), cvtpk(o[6], o[7])});
        }
    }
}

#define XPF 1
#define PFQ 0
#define PFN 8
#define OLAG 3
constexpr int QN = 176, QF = 19;
struct QTab { u16 e[QN]; int n, nf; u32 tt[64]; };
typedef const __attribute__((address_space(4))) int* KI;
constexpr int QT_OFF = (88 + 48) / 4;
DI int pick_task(char* fl, KI k, int gr) {
    u32 r;
    asm volatile("s_mov_b64 exec, 1\n\ts_nop 3\n\tglobal_atomic_add %0, %1, %2, %3 sc0 sc1\n\ts_mov_b64 exec, -1\n\ts_waitcnt vmcnt(0)"
                 : "=&v"(r) : "v"((F_QH + gr) * 128), "v"(1), "s"(fl) : "memory");
    const int x = uni((int)r) + 1 + QF;
    if (x >= k[QT_OFF + QN / 2]) return -1;
    const u32 e2 = (u32)k[QT_OFF + (x >> 1)];
    return (int)(x & 1 ? e2 >> 16 : e2 & 0xffffu);
}
__global__ __launch_bounds__(256, 1) void k_main(Ptrs p0, ChainCfg cfg, QTab tab) {
    extern __shared__ __attribute__((aligned(16))) char smem[];
    u32 lds = (u32)(size_t)LDSP(char, smem);
#define HALF 0
    int n = HALF ? (blockIdx.x & 4) * 32 + (int)(blockIdx.x >> 3) * 4 + (blockIdx.x & 3) : blockIdx.x, gr = n & 7;
    int w = uni(threadIdx.x >> 6);
    {
        u64 pc; int len;
        asm volatile("s_getpc_b64 %0\n.Lkm_first:\n\ts_mov_b32 %1, .Lkm_last-.Lkm_first\n\ts_max_i32 %1, %1, .Lkm_lu-.Lkm_first\n\ts_max_i32 %1, %1, .Lkm_lv-.Lkm_first\n\t"
                     "s_max_i32 %1, %1, .Lkm_g2-.Lkm_first\n\ts_max_i32 %1, %1, .Lkm_g4-.Lkm_first" : "=s"(pc), "=s"(len) : : "scc");
        if (w == 3) pfl((const char*)pc, (u32)min((int)(blockIdx.x >> 3) * 8192 + (int)(threadIdx.x & 63) * 128, max(len, 0)), lds);
    }
    karg_warm();
    if (n < NH) { score_role(p0, cfg, n, lds, w); return; }
    if (n < NS) { proj_role(p0, cfg, n - NH, lds, w); return; }
    const bool isv = n < NS + NVB;
#define qslot (lds + GRING * GBUF)
    int sdw = -1;
    int en = isv ? Q_VD + (n & 24) : tab.e[(n - NS - NVB) >> 3] & 511;
    u32 phi0 = 0;
    Sync y;
    if (XPF && n < 160 && w >= 2) pfl(p0.x, (u32)min(((n - NS) >> 3) * 128 + (w & 1) * 64 + lanex(), 1279) * 128, lds);
    if (n >= 160) {
        const int lane = lanex();
        if (n >= 192 && (n & 8)) {
            init_task(p0, (n & 7) + ((n - 192) >> 4) * 8, lds, w);
            barrier();
#pragma unroll
            for (int i = 0; i < 3; ++i) pfl(p0.wqkvb, (u32)min((((n - 192) >> 4) * 8 + (n & 7)) * 768 + i * 256 + w * 64 + lane, 19199 + 0) * 128, lds);
            pfl(p0.wob, (u32)min((((n - 192) >> 4) * 8 + (n & 7)) * 256 + w * 64 + lane, 6399) * 128, lds);
        } else {
            const int h = n < 192 ? n & 31 : (n & 7) + ((n - 192) >> 4) * 8;
            xcopy_task(p0, h + (n < 192 ? 0 : 32), w);
#pragma unroll
            for (int i = 0; i < PFN; ++i) {
                if (n >= 192) break;
                const int l = 64 * min(i * 4 + w, 29) + lane;
                pfl(p0.wo + (size_t)h * 245760, (u32)(l * 128), lds);
                if (PFQ) pfl(p0.wq + h * 384, (u32)((l / 3) * 12288 + (l % 3) * 128), lds);
            }
        }
        waitvm<0>();
        if (w == 0) {
            sync_ld(y, p0.ws);
            waitvm<0>();
            *LDSP(u32, qslot) = (u32)sync_end(y, p0.ws, phi0, true);
            xcopy_wait(p0.ws, sync_tag(y.e));
        }
        barrier();
        sdw = uni((int)*LDSP(u32, qslot));
        barrier();
    }
    while (true) {
        typedef char* const __attribute__((address_space(4)))* KP;
        KP kp = (KP)__builtin_amdgcn_kernarg_segment_ptr();
        asm volatile("" : "+s"(kp), "+s"(lds), "+s"(w), "+s"(gr), "+s"(n));
        __builtin_assume(w >= 0 && w < 4 && gr >= 0 && gr < 8);
        const Ptrs p{kp[0], kp[1], kp[2], kp[3], kp[4], kp[5], kp[6], kp[7], kp[8], kp[9], kp[10]};
        const int ty = en & 7, gate = en >> 9;
        const bool fu = ty == Q_PX && (en >> 3 & 63) < 8;
        const int ix = fu ? (en & 504) + gr : en >> 3 & 63, sf = ((KI)kp)[QT_OFF + QN / 2 + 1] & 255;
        char* const fl = p.ws + OFF_ST;
        const bool raw = sdw < 0;
        int t0 = gate - 1, f1 = F_ONE, t1 = 0, f2 = F_ONE, t2 = 0, f3 = F_ONE, t3 = 0, dt = cfg.T2;
        const int os = ix >= 40 ? 44 + (8 * ix + gr) / 20 : (8 * ix + gr) / 5;
        const int bb = ty == Q_BO ? (8 * ix + gr) >> 2 : os;
        if (ty == Q_BA) { f1 = F_B + (gr & RM) | ix << 16; t1 = 5; f2 = F_B + (gr & RM) | max(ix - 1, 0) << 16; t2 = 5; }
        if (ty == Q_BO) { f1 = F_B + (gr & RM) | (8 + bb) << 16; t1 = 8; }
        if (ty == Q_PX) { f1 = F_Q + pblk(ix) * 8 + (fu ? gr & RM : gr) | fu << 16; t1 = fu ? 32 : 4; if (fu) t0 += gr * (((KI)kp)[QT_OFF + QN / 2 + 1] >> 8) >> 3; if (ix < PXT) t0 = max(t0, ix + 1); }
        if (ty == Q_OUT) { f3 = F_B + (gr & RM) | (16 + (os >> 3)) << 16; t3 = 4; if (os < 60) { f1 = F_PX + os; t1 = 8; t0 = max(t0, os + (os < PXT ? OLAG : 1)); if (os >= PXT) dt = cfg.s1; } else { f1 = F_VD + 64 + (gr & RM); t1 = 32; } }
        const bool skip = fu ? ix >= sf : ty == Q_OUT && ix < 40 && (os < sf || os >= 60);
        if ((gate || ty >= Q_BA) && !skip) wg_wait(fl, w, F_CUR + gr, t0, f1, t1, f2, t2, f3, t3, dt, false);
        int sig = F_END, nrep = 1;
        if (skip) {}
        else if (ty <= Q_QKVB || ty == Q_BO) {
            const bool ab = ty <= Q_KQ;
            sig = ty == Q_VD ? F_VD + (ix >> 2) * 8 : (ty == Q_KQ ? F_Q + (ix >> 2) * 8 : (ix & 4 ? F_Q : F_K + 8 * ix)) + gr;
            if (!ab) sig = F_B | (bb + (ty == Q_BO ? 16 : 0)) << 16;
            nrep = ty == Q_VD || !ab ? RM + 1 : 1;
            if (ty == Q_VD && ix < 4) { sig += n & 32 ? 4 : 0; nrep = 4; }
            run_gemm(p, ty, ab ? ix >> 2 : bb, ab ? gr + 8 * (ix & 3) : 8 * ix + gr - (ty == Q_BO ? 4 : 5) * bb, (n >> 5) & 1, raw, y, lds, w);
            if (raw) sdw = sync_end(y, p.ws, phi0, w == 0);
        } else {
            if (ty == Q_BA) {
                battn_task(p, (8 * ix + gr) * 32, lds, w);
                sig = F_B | (8 + ix) << 16; nrep = RM + 1;
            } else if (ty == Q_PX) {
                if (ix < PXT) {
#pragma nounroll
                    for (int i = fu ? 0 : gr; i < (fu ? 8 : gr + 1); ++i) px_wave<false>(p, cfg, ix, i + 8 * w, ix - cfg.fast, w, lds, fu);
                } else px_wave<true>(p, cfg, ix, gr + 8 * w, -1, w, lds, false);
                sig = F_PX + ix;
                if (fu) wg_wait(fl, w, F_ONE, 0, F_B + (gr & RM) | (16 + (ix >> 3)) << 16, 4, F_ONE, 0, F_ONE, 0, dt, false);
            }
            if (ty == Q_OUT || fu) {
                const int cg = 8 * ix + gr - 5 * os;
                if (ix >= 40 && !fu) out_tail(p, cfg, os, 8 * ix + gr - 20 * (os - 44), lds, w, sdw, gr);
                else out_task(p, fu ? ix : os, fu ? 0 : cg, fu ? 5 : cg + 1, lds, (fu ? ix : os) + 1 - cfg.fast, w, sdw);
                sig = F_END;
            }
            waitvm<0>();
        }
        barrier();
        if (w == 0 && sig != F_END) flag_add(fl, sig, nrep);
        if (w == 1 && sig >= F_Q && sig < F_N) flag_add(fl, sig - gr | 1 << 16, RM + 1);
        if (raw && !isv && w == 0) xcopy_wait(p.ws, sync_tag(y.e));
        if (isv) break;
        if (w == 0) *LDSP(int, qslot) = pick_task(fl, (KI)kp, gr);
        barrier();
        en = uni(*LDSP(int, qslot));
        if (en < 0) return;
        barrier();
    }
    value_role(p0, cfg, n - NS, sdw, phi0, lds, w);
}

#define QCFG 14, 11, 3, 5, 0, 50, 36
#define LVDEF "00>==>>>;;<<<<==;988889::::::::::::::;;;;;;;;;;;:988776666668886"
constexpr int DX = 240, DUA = 330, DUB = 40, DUC = 4;
static ChainCfg dev_cfg(ChainCfg c) { c.T = PDL; c.cR = ODL; c.s1 = TDT; c.T2 = WSLEEP; c.cR2 = DX; c.dU = DUA; c.s2 = DUB; c.T3 = DUC; c.cR3 = 4 | 4 << 8 | 0 << 16; return c; }
static QTab set_sched(QTab t, const ChainCfg c, const char* lv) {
    for (int s = 0; s < 64; ++s) {
        const int d = lv[s] - '0';
        t.tt[s] = (u32)((s < c.s1 ? c.T : (s < c.s2 ? c.T2 : c.T3)) + 4 * d) | (u32)((s < c.s1 ? c.cR : (s < c.s2 ? c.cR2 : c.cR3)) + 5 * d / 2) << 16;
    }
    t.tt[0] = (u32)270 | (u32)165 << 16;
    t.tt[1] = (u32)230 | (u32)152 << 16;
    return t;
}
#define QORD "bbqV3bK3m44bm55bm66m77m88"
#define GATES "11 13 15 17 22 24 32 36 19 22 29 42 13 17 21 26 33 41"
static QTab make_tab(int lv, int lk, int b0, int d1, int d2, int lg, int nf) {
    QTab t{};
    int n = 0, dl[QN], nq = 0;
    auto add = [&](int ty, int ix) { t.e[n] = (u16)(ty | ix << 3); dl[n++] = ty == Q_VD || ty == Q_KQ ? 8 * (ix / 4 - 1) - (ty == Q_VD ? lv : lk) - (ix >= 32 ? 0 : 0) : 99; };
    for (int i = 1; i < 4; ++i) add(Q_VD, i);
    for (int i = 0; i < 16; ++i) add(i < 4 ? Q_H : ((i & 8) ? Q_VD : Q_KQ), i < 8 ? i : 4 + (i & 1) * 4 + ((i - 8) >> 1));
    for (int i = 0; i < 4; ++i) add(Q_KQ, 8 + i);
    for (const char* c = QORD; *c; ++c) {
        if (*c == 'b') add(Q_QKVB, nq++);
        else if (*c == 'q') for (int i = 4; i < 8; ++i) add(Q_H, i);
        else if (*c == '.') dl[n - 1] |= 1 << 20;
        else if (*c == 'n') { const int b = *++c - '0'; for (int i = 0; i < 8; ++i) add(i & 1 ? Q_KQ : Q_VD, 4 * b + (i >> 1) - (i & 1) * 2); }
        else if (*c == 'h' || *c == 'e') { const int b = 4 * (c[1] - '0') + (*c == 'e') * 2; ++c; add(Q_KQ, b); add(Q_KQ, b + 1); }
        else if (*c == 'm') { const int b = c[1] - '0', b2 = c[2] - '0'; c += 2; for (int i = 0; i < 8; ++i) add(i & 1 ? Q_KQ : Q_VD, 4 * (i & 1 ? b2 : b) + ((i >> 1) ^ (i & 2 ? 0 : 0))); }
        else {
            const int ty = (*c | 32) == 'v' ? Q_VD : Q_KQ, two = *c >> 5 & 1, b = *++c - '0';
            for (int i = 0; i < (4 << two); ++i) add(ty, 4 * (b + (i & two)) + (i >> two));
        }
    }
    const int gap = 8;
    for (int i = n - 1, m = 999; i >= 0; --i) { const int pz = dl[i] > 999; m = min(8 * (pz ? dl[i] - (1 << 20) : dl[i]), m - gap - 8 * pz); dl[i] = max(m, 0); }
    auto put = [&](int ty, int ix, int g, int pos) {
        int i = n++;
        for (; dl[i - 1] > pos; --i) { t.e[i] = t.e[i - 1]; dl[i] = dl[i - 1]; }
        t.e[i] = (u16)(ty | ix << 3 | (g + 1) << 9); dl[i] = pos;
    };
    for (int i = 0; i < n; ++i) if (dl[i] >= 8) t.e[i] |= (u16)((dl[i] / 8 + 1) << 9);
    const char* gs = GATES;
    auto gt = [&](int i) { return 10 * (gs[3 * i] - '0') + gs[3 * i + 1] - '0'; };
    for (int i = 0; i < 8; ++i) put(Q_BA, i, gt(i), 8 * gt(i));
    for (int i = 0; i < 4; ++i) put(Q_BO, i, gt(8 + i), 8 * gt(8 + i));
    for (int i = 0; 8 * i < nf; ++i) put(Q_PX, i, gt(12 + i), 8 * (max(gt(12 + i), 8 * i + 1) + b0));
    for (int s = nf; s < NSEG; ++s) { const int g = s < PXT ? s + 1 : max(s - 3, lg); put(Q_PX, s, g, 8 * g); }
    for (int x = 5 * nf / 8; x < 38; ++x) { const int os = (8 * x + 4) / 5; put(Q_OUT, x, 0, 8 * (os + (os < PXT ? OLAG : 1))); }
    for (int x = 40; x < 50; ++x) { const int g = max(44 + 8 * x / 20 - 3, lg); put(Q_OUT, x, g, 8 * g + 4); }
    t.n = n; t.nf = nf | d1 << 8;
    return t;
}

extern "C" void kernel_launch(void* const* d_in, const int* in_sizes, int n_in, void* d_out, int out_size, void* d_ws,
                              size_t ws_size, hipStream_t stream) {
    Ptrs p{(const char*)d_in[0], (const char*)d_in[1], (const char*)d_in[2], (const char*)d_in[3], (const char*)d_in[4], (const char*)d_in[5],
           (const char*)d_in[8], (const char*)d_in[9], (const char*)d_in[10], (char*)d_out, (char*)(((size_t)d_ws + 0x1fffff) & ~(size_t)0x1fffff)};
    (void)hipFuncSetAttribute((const void*)k_main, hipFuncAttributeMaxDynamicSharedMemorySize, S_LDS);
    k_main<<<256, 256, S_LDS, stream>>>(p, dev_cfg(ChainCfg{CFG}), set_sched(make_tab(QCFG), ChainCfg{CFG}, LVDEF));
    static_assert(sizeof(Ptrs) + sizeof(ChainCfg) + sizeof(QTab) == TT_OFF + 256, "");
}
```

```cpp
#include <hip/hip_runtime.h>
#pragma clang diagnostic ignored "-Wint-to-pointer-cast"

typedef unsigned short u16;
typedef unsigned int u32;
typedef unsigned long long u64;
typedef __bf16 bf16x8 __attribute__((ext_vector_type(8)));
typedef __bf16 bf16x16 __attribute__((ext_vector_type(16)));
typedef short s16x4 __attribute__((ext_vector_type(4)));
typedef float f32x4 __attribute__((ext_vector_type(4)));
typedef unsigned u32x4 __attribute__((ext_vector_type(4)));
typedef unsigned u32x2 __attribute__((ext_vector_type(2)));
typedef unsigned u32x8 __attribute__((ext_vector_type(8)));
typedef unsigned u32x16 __attribute__((ext_vector_type(16)));
typedef unsigned u32x32 __attribute__((ext_vector_type(32)));
#define DI __device__ __forceinline__
#define LDSP(T, a) ((__attribute__((address_space(3))) T*)(a))

constexpr int S_LEN = 2048, DM = 640, NH = 32, DH = 192, NSEG = 64, HD = NH * DH;
#define NSAMP 4
constexpr int NREG = 13;
constexpr int NV = 80, NVB = 40;
constexpr int NS = 64;
#define CFG 148, 106, 106, 20, 57, 16, 148, 106, 52, 148, 106, 6

constexpr size_t U_STRIDE = NV * 4096, D_STRIDE = NV * 16384;
constexpr size_t HO_STRIDE = 65536;
constexpr size_t OFF_ST = NSEG * HO_STRIDE;
enum { F_ACK = 0, F_ONE = 3, F_VD = 16, F_QH = 96, F_B = 104, F_CUR = 128, F_PROG = 136 - 8 * 62, F_UD = 152, F_PX = 160, F_K = 224, F_END = 512, F_Q = 520, F_N = 592 };
constexpr size_t OFF_LAT = OFF_ST + F_N * 128;
constexpr size_t OFF_KX = 5u << 20;
constexpr size_t OFF_QX = OFF_KX + (size_t)S_LEN * HD * 2;
constexpr size_t OFF_D = OFF_QX + (size_t)S_LEN * HD * 2;
constexpr size_t OFF_U = (OFF_D + NSEG * D_STRIDE + 0x1fffff) & ~(size_t)0x1fffff;
constexpr size_t OFF_KM = OFF_U + (22u << 20);
constexpr size_t OFF_PX = OFF_KM + (size_t)NSEG * NH * 3072;
constexpr size_t OFF_QKVB = OFF_PX + (size_t)NSEG * 81920;
constexpr size_t OFF_OB = OFF_QKVB + (size_t)S_LEN * 1920 * 2;
constexpr size_t OFF_BOUT = OFF_OB + (size_t)S_LEN * DM * 2;
constexpr size_t OFF_GATE = OFF_BOUT + (size_t)S_LEN * DM * 2;
constexpr size_t OFF_XF = OFF_GATE + S_LEN * 4;
constexpr size_t WS_END = OFF_XF + (size_t)S_LEN * DM * 2;
static_assert(WS_END + (2u << 20) <= (256u << 20), "workspace");

typedef __bf16 bf16x2 __attribute__((ext_vector_type(2)));
typedef float f32x2 __attribute__((ext_vector_type(2)));
DI u32 cvtpk(float lo, float hi) { return __builtin_bit_cast(u32, __builtin_convertvector(f32x2{lo, hi}, bf16x2)); }
DI float rbf(float f) { return __uint_as_float(cvtpk(f, f) << 16); }
DI f32x4 mfma16(u32x4 a, u32x4 b, f32x4 c) {
    return __builtin_amdgcn_mfma_f32_16x16x32_bf16(__builtin_bit_cast(bf16x8, a), __builtin_bit_cast(bf16x8, b), c, 0, 0, 0);
}
DI u32x2 trread(u32 a) {
    return __builtin_bit_cast(u32x2, __builtin_amdgcn_ds_read_tr16_b64_v4i16(LDSP(s16x4, a)));
}
DI void glds16c(const char* sbase, u32 voff, u32 lds) {
    u32 sv;
    asm volatile("s_mov_b32 %0, m0\n\ts_mov_b32 m0, %3\n\ts_nop 0\n\tglobal_load_lds_dwordx4 %1, %2 sc1\n\ts_mov_b32 m0, %0"
                 : "=&s"(sv) : "v"(voff), "s"(sbase), "s"(lds) : "memory");
}
template <int IMM>
DI void gld16(u32x4& r, const char* sbase, u32 voff) {
    asm volatile("s_nop 4\n\tglobal_load_dwordx4 %0, %1, %2 offset:%3" : "=v"(r) : "v"(voff), "s"(sbase), "i"(IMM) : "memory");
}
DI void gst16(char* sbase, u32 voff, u32x4 v) {
    asm volatile("global_store_dwordx4 %0, %1, %2 sc1\n\ts_nop 1" : : "v"(voff), "v"(v), "s"(sbase) : "memory");
}
#define OST " nt"
DI void gst16p(char* sbase, u32 voff, u32x4 v) {
    asm volatile("s_nop 4\n\tglobal_store_dwordx4 %0, %1, %2" OST "\n\ts_nop 1" : : "v"(voff), "v"(v), "s"(sbase) : "memory");
}
DI void gst8(char* sbase, u32 voff, u32x2 v) {
    asm volatile("global_store_dwordx2 %0, %1, %2 sc1\n\ts_nop 1" : : "v"(voff), "v"(v), "s"(sbase) : "memory");
}
template <int N>
DI void waitvm() { asm volatile("s_waitcnt vmcnt(%0)" : : "i"(N) : "memory"); }
DI void barrier() { asm volatile("s_waitcnt lgkmcnt(0)\n\ts_barrier" : : : "memory"); }
#define PIN() __builtin_amdgcn_sched_barrier(0)
DI int uni(int v) { return __builtin_amdgcn_readfirstlane(v); }
DI int lanex() {
    int t;
    asm volatile("v_mbcnt_lo_u32_b32 %0, -1, 0\n\tv_mbcnt_hi_u32_b32 %0, -1, %0" : "=v"(t));
    __builtin_assume(t >= 0 && t < 64);
    return t;
}
DI void flag_ld(u32& r, const char* fl, int idx) {
    asm volatile("s_nop 4\n\tglobal_load_dword %0, %1, %2 sc1" : "=v"(r) : "v"((idx & 0xffff) * 128 + (idx >> 14)), "s"(fl) : "memory");
}
DI void flag_ldm(u32& r, const char* fl, int idx, bool on) {
    asm volatile("v_mov_b32 %0, 1000\n\ts_mov_b32 exec_lo, %3\n\ts_mov_b32 exec_hi, 0\n\tglobal_load_dword %0, %1, %2 sc1\n\ts_mov_b64 exec, -1" : "=&v"(r) : "v"(idx * 128), "s"(fl), "s"(uni(on ? 1 : 0)) : "memory");
}
DI void flag_wait(const char* fl, int idx, int target) {
#pragma nounroll
    for (int it = 0; it < (1 << 16); ++it) {
        u32 v;
        flag_ld(v, fl, idx);
        asm volatile("s_waitcnt vmcnt(0)" : "+v"(v) : : "memory");
        if (uni((int)(v + 1u)) >= target) break;
        __builtin_amdgcn_s_sleep(6);
    }
}
DI void flag_add(char* fl, int idx, int nrep) {
    const int lane = lanex();
    if (lane < nrep) asm volatile("s_nop 4\n\tglobal_atomic_add %0, %1, %2 sc1" : : "v"(((idx & 0xffff) + lane) * 128 + (idx >> 14)), "v"(1), "s"(fl) : "memory");
}
DI void acquire() { asm volatile("buffer_inv sc1\n\ts_waitcnt vmcnt(0)" : : : "memory"); }

template <int V> struct IC { static constexpr int v = V; };
template <int B, int E, class Fn>
DI void sfor(Fn&& fn) { if constexpr (B < E) { fn(IC<B>{}); sfor<B + 1, E>(fn); } }
enum { T_QK = 0, T_VD = 1, T_BL = 3 };
constexpr int GBUF = 24576, GRING = 6;
struct GemmArgs {
    const char *w1, *w1b;
    const char* w2;
    const char* x;
    char *out, *outb;
    int ldw;
    char* sig;
    int type, np1, tn;
    int sV, sP, sQ;
    int xb;
};
DI const char* unip(const char* p) {
    const u64 v = (u64)p;
    return (const char*)(((u64)(u32)uni((int)(v >> 32)) << 32) | (u32)uni((int)v));
}
template <int Z>
DI void mf1(f32x4& c, const u32x4& a, const u32x4& b) {
    if constexpr (Z) asm volatile("v_mfma_f32_16x16x32_bf16 %0, %1, %2, 0" : "=&v"(c) : "v"(a), "a"(b));
    else asm volatile("v_mfma_f32_16x16x32_bf16 %0, %1, %2, %0" : "+v"(c) : "v"(a), "a"(b));
}
template <int Z>
DI void mf2(f32x4& c, const u32x4& a, const u32x4& b) {
    if constexpr (Z) asm volatile("v_mfma_f32_16x16x32_bf16 %0, %1, %2, 0" : "=&v"(c) : "v"(a), "v"(b));
    else asm volatile("v_mfma_f32_16x16x32_bf16 %0, %1, %2, %0" : "+v"(c) : "v"(a), "v"(b));
}
template <int IMM>
DI void gld16a(u32x4& r, const char* sbase, u32 voff) {
    asm volatile("global_load_dwordx4 %0, %1, %2 offset:%3 sc1" : "+a"(r) : "v"(voff), "s"(sbase), "i"(IMM) : "memory");
}
DI void setm0(u32 v) { asm volatile("s_mov_b32 m0, %0" : : "s"(v) : "memory"); }
template <int IMM>
DI void gldsm(const char* sbase, u32 voff) {
    asm volatile("global_load_lds_dwordx4 %0, %1 offset:%2" : : "v"(voff), "s"(sbase), "i"(IMM) : "memory");
}
DI void gst16n(char* sbase, u32 voff, u32x4 v) {
    asm volatile("global_store_dwordx4 %0, %1, %2 sc1" : : "v"(voff), "v"(v), "s"(sbase) : "memory");
}
template <int N>
DI void waitbar() { asm volatile("s_waitcnt vmcnt(%0)\n\ts_barrier" : : "i"(N) : "memory"); }
#define LGKM(n) __builtin_amdgcn_s_waitcnt(0xC07F | ((n) << 8))

DI int xhalf() { u32 x; asm volatile("s_getreg_b32 %0, hwreg(HW_REG_XCC_ID)" : "=s"(x)); return (int)((x >> 2) & 1); }
struct Sync { u32x2 r, g, e; };
DI void sync_ld(Sync& y, const char* ws) {
    asm volatile("s_nop 4\n\tglobal_load_dwordx2 %0, %3, %5 sc1\n\tglobal_load_dwordx2 %1, %4, %5 offset:512 sc1\n\tglobal_load_dwordx2 %2, %4, %5 offset:1024 sc1"
                 : "=&v"(y.r), "=&v"(y.g), "=&v"(y.e) : "v"((lanex() & 31) * 8), "v"(0), "s"(ws + OFF_LAT) : "memory");
}
DI u64 sync_tag(u32x2 e) { return (((u64)e[1] << 32 | e[0]) + 1) << 1; }
DI int sync_end(Sync& y, const char* ws, u32& phi, bool ack) {
    asm volatile("" : "+v"(y.r), "+v"(y.g), "+v"(y.e));
#pragma nounroll
    for (int it = 0; it < (1 << 16); ++it) {
        const u64 t = sync_tag(y.e);
        if (__builtin_amdgcn_ballot_w64((y.r[0] & ~1u) != (u32)t || y.r[1] != (u32)(t >> 32) || y.g[0] != (u32)t) == 0) break;
        __builtin_amdgcn_s_sleep(24);
        sync_ld(y, ws);
        asm volatile("s_waitcnt vmcnt(0)" : "+v"(y.r), "+v"(y.g), "+v"(y.e) : : "memory");
    }
    if (ack) flag_add((char*)ws + OFF_ST, F_ACK, 1);
    phi = (u32)uni((int)y.g[1]);
    const u32 b = (u32)__builtin_amdgcn_ballot_w64(y.r[0] & 1), all = __builtin_popcount(b) > 16;
    u32 q = 0;
#pragma unroll
    for (int r = 0; r < NREG; ++r) {
        const u32 c = r < 2 ? __builtin_popcount(b & (0x55u << r)) + all : 2 * __builtin_popcount((b >> (4 + 2 * r)) & 3) + all;
        q |= ((c > 2) ^ (u32)((r < 2 ? r : r + (OFF_U >> 21)) & 1)) << r;
    }
    return uni((int)(q ^ 3u * xhalf()));
}
DI void pace(int on) {
    asm volatile("s_cmp_ge_i32 %0, 0\n\ts_cbranch_scc1 .Lp%=\n\ts_waitcnt vmcnt(0)\n.Lp%=:" : : "s"(on) : "memory", "scc");
}
#define SMO 48
#define RM 7
#define BLS 1
#define EPG 1
#define PXT 58
#define PDX 40
#define SPL 0
#define HP0 48
#define SMB 48
#define SMP SMO
template <int C>
DI void gap(int on) {
    if constexpr (C > 0) asm volatile("s_cmp_ge_i32 %0, 0\n\ts_cbranch_scc1 .Lg%=\n\ts_nop %1\n\ts_nop %2\n\ts_nop %3\n.Lg%=:" : : "s"(on), "n"(C / 12 - 1), "n"(C / 12 - 1), "n"(C / 12 - 1) : "memory", "scc");
}
DI void early_add(char* ctr, int c) {
    asm volatile("s_cmp_lg_u32 %1, 1\n\ts_cbranch_scc1 .Le%=\n\ts_mov_b64 exec, 1\n\ts_nop 1\n\tglobal_atomic_add %2, %3, %0 sc1\n\ts_mov_b64 exec, -1\n.Le%=:"
                 : : "s"(ctr), "s"(c), "v"(0), "v"(1) : "memory", "scc");
}
#define XL 2
template <int NT>
DI void gemm_task(const GemmArgs& g, Sync& y, const char* ws, u32 lds, int w) {
    constexpr int FD = 5, NX = 2 * NT, OPS = NX + 6, BI = 24 - FD;
    const int lane = lanex();
    const int n = lane & 15, q = lane >> 4, qq = (lane >> 2) & 3, pp = lane & 3;
    const bool p2 = g.type == T_VD;
    const int rw = lane >> 3, sp = (lane >> 1) & 3;
    const u32 v2b = (u32)(rw * 1280 + 16 * (lane & 1) + 5120);
    auto v2o = [&](int i) { return v2b + (u32)(((sp ^ (rw >> 1)) << 5) + i * 9216); };
    u32 wv[6];
#pragma unroll
    for (int i = 0; i < 6; ++i)
        wv[i] = (u32)((rw + 8 * (i / 3)) * g.ldw + ((sp ^ (((rw >> 1) & 1) + 2 * (i / 3))) << 5) + 16 * (lane & 1) + (i % 3) * 128 + 5120 - 1024 * i);
    const char* const wp2 = g.w2 + (size_t)(48 * w) * 1280 - 2048;
    const char* wp = unip(g.w1 + (size_t)(16 * w) * g.ldw - 2048);
    int wstep = uni(64 * g.ldw);
    u32 wdst = lds + (u32)w * 6144 + 3072;
    const u32 wdlast = wdst + (GRING - 1) * GBUF;
    auto wring = [&]() { wdst = wdst == wdlast ? wdst - (GRING - 1) * GBUF : wdst + GBUF; };
    auto wload = [&](int i) {
        setm0(wdst);
        wring();
        asm volatile("s_nop 0");
        sfor<0, 6>([&](auto I) { gldsm<1024 * (I.v - 3)>(wp, wv[I.v]); });
        wp += wstep;
    };
    u32x4 vf[NT][6];
    u32x4 fr[FD + 1];
    u32x4 xr[5][NX];
    auto xtie = [&](u32x4 (&x)[NX]) {
#pragma unroll
        for (int i = 0; i < NX; i += 4) asm volatile("" : "+a"(x[i]), "+a"(x[i + 1]), "+a"(x[i + 2]), "+a"(x[i + 3]));
    };
#pragma unroll
    for (int i = 0; i < 5 * NX; ++i) asm volatile("" : "=a"(xr[i / NX][i % NX]));
    u32x2 flo;
    u32 c[4], slot = 0;
    int sd = GBUF;
    auto sring = [&]() { sd = slot == (GRING - 1) * GBUF ? -(GRING - 1) * GBUF : GBUF; slot += (u32)sd; };
    {
        u32 xo[NT];
#pragma unroll
        for (int tt = 0; tt < NT; ++tt) {
            const int tok = (g.type == T_VD) ? 8 * (n >> 2) + 4 * (tt & 1) + (n & 3) : 16 * (tt & 1) + n;
            xo[tt] = (u32)((NT / 2 * w + (tt >> 1)) * 40960 + (g.xb ? q * 512 + tok * 16 : q * 16 + tok * 1280));
        }
        f32x4 acc[12][NT];
        const bool rm = NT == 4 && g.type == T_BL;
        const int sw = (qq >> 1) + 2 * (q & 1);
#pragma unroll
        for (int t = 0; t < 4; ++t)
            c[t] = lds + q * 3072 + qq * 128 + (rm ? ((((t & 2) + (pp >> 1)) ^ sw) << 5) + 16 * (pp & 1) + 8 * ((pp >> 1) ^ (t & 1)) : ((t ^ sw) << 5) + 8 * pp);
        auto rlo = [&](int f) { flo = trread(c[f & 3] + (f / 12) * 12288 + ((f % 12) >> 2) * 1024); };
        auto rhi = [&](int f) {
            const u32x2 hi = trread(c[f & 3] + (f / 12) * 12288 + ((f % 12) >> 2) * 1024 + 512);
            fr[f % (FD + 1)] = u32x4{flo[0], flo[1], hi[0], hi[1]};
        };
        const int xk = uni(g.xb ? 2048 : 64);
        const char *xp = g.x, *xq = xp + xk;
        sfor<0, 4>([&](auto J) {
            if constexpr (J.v < XL) { sfor<0, NX>([&](auto I) { gld16a<0>(xr[J.v][I.v], I.v & 1 ? xq : xp, xo[I.v >> 1]); }); xp += 2 * xk; xq += 2 * xk; }
            wload(J.v);
        });
        waitbar<18 + (XL - 1) * NX>();
        xtie(xr[0]);
#pragma unroll
        for (int i = 0; i < FD; ++i) { rlo(i); rhi(i); }
        char* po = g.out;
#pragma nounroll
        for (int ph = g.np1; ph > 0; --ph) {
            sfor<0, 10>([&](auto J) {
                constexpr int j = J.v;
                u32x4 (&xc)[NX] = xr[j % 5];
                u32x4 (&xn)[NX] = xr[(j + XL) % 5];
                sfor<0, 24>([&](auto I) {
                    constexpr int idx = I.v, kk = idx / 12, T = idx % 12, z = j == 0 && kk == 0, f = (idx + FD) % 24;
                    constexpr int mo = NT == 4 ? (idx < BI && idx % 4 != 2 ? idx - (idx + 1) / 4 : -1) : (idx < BI && idx % 2 == 0 ? idx / 2 : -1);
                    auto mem = [&]() {
                        if constexpr (mo >= NX) gldsm<1024 * (mo - NX - 3)>(wp, wv[mo - NX]);
                        else if constexpr (mo >= 0) gld16a<0>(xn[mo], mo & 1 ? xq : xp, xo[mo >> 1]);
                    };
                    auto misc = [&]() {
                        if constexpr (idx == 1) wring();
                        if constexpr (idx == 13) sring();
                        if constexpr (j == 1 && idx == 22) early_add(g.sig, ph + w);
                        if constexpr (idx >= 15 && idx < BI) { c[idx - 15] += (u32)sd; asm volatile("" : "+v"(c[idx - 15])); }
                        const bool more = ph > 1;
                        if constexpr (j != 5) {
                            if constexpr (idx == 20) wp += wstep;
                        } else {
                            if constexpr (idx == 19) { wp = more ? g.w1b + (size_t)(16 * w) * g.ldw - 2048 : wp2; wstep = more ? wstep : (p2 ? 128 : 0); }
                            if constexpr (idx >= 20 && idx < 23) {
#pragma unroll
                                for (int i = 2 * (idx - 20); i < 2 * (idx - 19); ++i) { wv[i] = more ? wv[i] : (p2 ? v2o(i) : 5120u); asm volatile("" : "+v"(wv[i])); }
                            }
                        }
                        if constexpr (j != 9 - XL) {
                            if constexpr (idx == 21) xp += 2 * xk;
                            if constexpr (idx == 22) xq += 2 * xk;
                        } else if constexpr (idx == 23) {
                            xp = g.x; xq = xp + xk;
#pragma unroll
                            for (int i = 0; i < NT; ++i) { xo[i] = more ? xo[i] : 0u; asm volatile("" : "+v"(xo[i])); }
                        }
                    };
                    if constexpr (idx == BI) { waitbar<6 + (XL - 1) * OPS>(); xtie(xr[(j + 1) % 5]); }
                    if constexpr (idx % 2 == 0) LGKM(6);
                    PIN();
                    const u32x4 a = fr[idx % (FD + 1)];
                    mf1<z>(acc[T][0], a, xc[kk]); PIN();
                    rlo(f);
                    if constexpr (idx == 0) setm0(wdst);
                    if constexpr (NT == 2) rhi(f);
                    PIN();
                    mf1<z>(acc[T][1], a, xc[2 + kk]); PIN();
                    if constexpr (NT == 4) {
                        rhi(f); PIN();
                        mf1<z>(acc[T][2], a, xc[4 + kk]); PIN();
                        mem(); PIN();
                        mf1<z>(acc[T][3], a, xc[6 + kk]); PIN();
                    } else { mem(); PIN(); }
                    misc();
                    PIN();
                });
            });
            asm volatile("s_nop 7\n\ts_nop 1");
            PIN();
#pragma unroll
            for (int tt = 0; tt < NT; ++tt)
#pragma unroll
                for (int ks = 0; ks < 6; ++ks)
                    vf[tt][ks] = u32x4{cvtpk(acc[2 * ks][tt][0], acc[2 * ks][tt][1]), cvtpk(acc[2 * ks][tt][2], acc[2 * ks][tt][3]),
                                       cvtpk(acc[2 * ks + 1][tt][0], acc[2 * ks + 1][tt][1]), cvtpk(acc[2 * ks + 1][tt][2], acc[2 * ks + 1][tt][3])};
            if (g.type == T_QK) {
#pragma unroll
                for (int tt = 0; tt < NT; ++tt)
#pragma unroll
                    for (int ks = 0; ks < 6; ++ks) {
                        gst16(po + (size_t)(NT / 2 * w + (tt >> 1)) * (NH * 12288) + (tt & 1) * 6144 + ks * 1024, lane * 16,
                              u32x4{vf[tt][ks][0], vf[tt][ks][2], vf[tt][ks][1], vf[tt][ks][3]});
                        if (NT == 4 && EPG) asm volatile("s_sleep %0" : : "n"(EPG));
                    }
            } else if (rm) {
                const bool sx = q >= 2;
#pragma unroll
                for (int i = 0; i < 3 * NT; ++i) {
                    const int tp = i / 6, ks = i % 6;
                    u32x4 o[2];
#pragma unroll
                    for (int j = 0; j < 4; ++j) {
                        const u32x4 &v = vf[2 * tp][ks], &u = vf[2 * tp + 1][ks];
                        if (BLS) {
                            const auto r = __builtin_amdgcn_permlane32_swap(v[j], u[j], false, false);
                            o[0][j] = r[0]; o[1][j ^ 2] = r[1];
                        } else { o[0][j] = sx ? v[j ^ 2] : v[j]; o[1][j] = sx ? u[j ^ 2] : u[j]; }
                    }
#pragma unroll
                    for (int j = 0; j < 2; ++j) {
                        gst16(po + (size_t)(2 * w + tp) * g.sV + j * (BLS ? 1024 : 256) + ks * 2048, BLS ? (u32)((q & 1) * 512 + (q >> 1) * 256 + n * 16) : (u32)(q * 512 + n * 16), o[j]);
                        if (EPG) asm volatile("s_sleep %0" : : "n"(EPG));
                    }
                }
            }
            po = g.outb;
            PIN();
        }
    }
    if (p2) {
        constexpr int HI = 2048;
        const int r = 4 * (q & 1) + qq, sw = r >> 1;
#pragma unroll
        for (int t = 0; t < 4; ++t) c[t] = lds + slot + (q >> 1) * 1024 + r * 128 + ((t ^ sw) << 5) + 8 * pp;
        sring();
        u32 so = (u32)((n >> 3) * g.sV + q * g.sQ + (n & 7) * 16);
        char* const o0 = g.out + (size_t)(NT / 2 * w) * g.sP;
        char* const o1 = o0 + g.sP;
        const int sv2 = 2 * g.sV;
        auto rlo = [&](int f) { flo = trread(c[f / 6] + (f % 6) * 4096); };
        auto rhi = [&](int f) {
            const u32x2 hi = trread(c[f / 6] + (f % 6) * 4096 + HI);
            fr[f % (FD + 1)] = u32x4{flo[0], flo[1], hi[0], hi[1]};
        };
        f32x4 a2[2][NT];
        u32 e[NT / 2][4];
        auto cv = [&](f32x4 (&a)[NT], int i) { e[i >> 2][i & 3] = cvtpk(a[i >> 1][2 * (i & 1)], a[i >> 1][2 * (i & 1) + 1]); asm volatile("" : "+v"(e[i >> 2][i & 3])); };
        auto st = [&](int pl) { gst16n(pl ? o1 : o0, so, u32x4{e[pl][0], e[pl][1], e[pl][2], e[pl][3]}); };
        PIN();
#pragma unroll
        for (int i = 0; i < FD; ++i) { rlo(i); rhi(i); }
#pragma unroll
        for (int tt = 0; tt < NT; ++tt) a2[1][tt] = f32x4{0.f, 0.f, 0.f, 0.f};
#pragma nounroll
        for (int t = 0; t < g.tn; ++t) {
            sfor<0, 24>([&](auto I) {
                constexpr int idx = I.v, cb = idx / 6, ks = idx % 6, f = (idx + FD) % 24;
                f32x4 (&ac)[NT] = a2[cb & 1];
                f32x4 (&ap)[NT] = a2[(cb & 1) ^ 1];
                constexpr int mo = cb < 2 ? (ks == 0 ? 3 * cb : (ks >= 4 ? 3 * cb + ks - 3 : -1)) : -1;
                auto mem = [&]() {
                    if constexpr (mo >= 0) gldsm<1024 * (mo - 3)>(wp, wv[mo]);
                    if constexpr (ks == 3 || (NT == 4 && ks == 2)) { if (cb > 0 || t > 0) st(NT == 4 ? ks - 2 : 0); }
                };
                auto misc = [&]() {
                    if constexpr (idx == 12) wring();
                    if constexpr (idx == 23) sring();
                    if constexpr (idx == 16) { wp += t < g.tn - 5 ? 128 : 0; }
                    if constexpr (ks == 0) { c[cb] += (u32)sd; asm volatile("" : "+v"(c[cb])); }
                    if constexpr (ks == 4) { so += (cb > 0 || t > 0) ? (u32)sv2 : 0u; asm volatile("" : "+v"(so)); }
                };
                if constexpr (idx == BI) waitbar<NT == 4 ? 38 : 28>();
                if constexpr (idx % 2 == 0) LGKM(6);
                PIN();
                const u32x4 b = fr[idx % (FD + 1)];
                mf2<ks == 0>(ac[0], vf[0][ks], b); PIN();
                rlo(f);
                if constexpr (idx == 0) setm0(wdst);
                if constexpr (NT == 2) rhi(f);
                if constexpr (NT == 4 && ks >= 1 && ks <= 3) cv(ap, ks == 1 ? 0 : (ks == 2 ? 4 : 7));
                PIN();
                mf2<ks == 0>(ac[1], vf[1][ks], b); PIN();
                if constexpr (NT == 4) {
                    rhi(f);
                    if constexpr (ks == 1 || ks == 2) cv(ap, ks == 1 ? 1 : 5);
                    PIN();
                    mf2<ks == 0>(ac[2], vf[2][ks], b); PIN();
                    mem();
                    if constexpr (ks == 1) { cv(ap, 2); cv(ap, 3); }
                    PIN();
                    mf2<ks == 0>(ac[3], vf[3][ks], b); PIN();
                    if constexpr (ks == 2) cv(ap, 6);
                } else {
                    if constexpr (ks == 1 || ks == 2) { cv(ap, 2 * ks - 2); cv(ap, 2 * ks - 1); }
                    mem(); PIN();
                }
                misc();
                PIN();
            });
            asm volatile("s_nop 7\n\ts_nop 1");
        }
        PIN();
#pragma unroll
        for (int i = 0; i < 2 * NT; ++i) cv(a2[1], i);
#pragma unroll
        for (int pl = 0; pl < NT / 2; ++pl) st(pl);
        asm volatile("s_nop 1");
    }
    if constexpr (NT == 2) asm volatile(".Lkm_g2:"); else asm volatile(".Lkm_g4:");
    sync_ld(y, ws);
    waitvm<0>();
    asm volatile("" : "+v"(y.r), "+v"(y.g), "+v"(y.e));
    sfor<0, 5>([&](auto I) { xtie(xr[I.v]); });
}

struct Ptrs {
    const char *x, *mem0, *wq, *wk, *wv, *wo, *wqkvb, *wob, *gw;
    char* out;
    char* ws;
};

enum { Q_H = 0, Q_VD = 1, Q_KQ = 2, Q_QKVB = 3, Q_BA = 4, Q_BO = 5, Q_PX = 6, Q_OUT = 7 };
DI void run_gemm(const Ptrs& p, int ty, int b, int i, int hf, bool raw, Sync& y, u32 lds, int w) {
    GemmArgs g;
    g.sV = g.sP = g.sQ = 0; g.w2 = p.wo; g.ldw = HD * 2; g.np1 = 1; g.tn = 10;
    g.sig = p.ws + OFF_ST + F_END * 128;
    const char* xf = raw ? p.x : p.ws + OFF_XF;
    g.xb = !raw;
    int s0 = 8 * b;
    if (ty == Q_VD) {
        s0 = b < 2 ? 4 * b : s0 - 8;
        g.type = T_VD; g.w1 = g.w1b = p.wv + i * DH * 2;
        g.w2 = p.wo + (size_t)i * DH * 1280;
        g.out = g.outb = p.ws + OFF_D + (size_t)s0 * D_STRIDE + i * 512;
        g.sV = 16384; g.sP = (int)D_STRIDE; g.sQ = 128;
        if (b == 0) { g.tn = 5; g.w2 += hf * 640; g.out += hf * 40 * 16384; }
    } else if (ty == Q_H) {
        s0 = 0;
        g.type = T_QK; g.w1 = g.w1b = (b ? p.wq : p.wk) + i * DH * 2;
        g.out = g.outb = p.ws + (b ? OFF_QX : OFF_KX) + (size_t)i * 12288;
        b = 0;
    } else if (ty == Q_KQ) {
        s0 = b < 2 ? 4 * b : s0 - 8;
        g.type = T_QK; g.w1 = p.wk + i * DH * 2; g.w1b = p.wq + i * DH * 2;
        g.out = p.ws + OFF_KX + (size_t)(s0 * NH + i) * 12288;
        g.outb = g.out + (OFF_QX - OFF_KX);
        g.np1 = 2;
        g.sig = p.ws + OFF_ST + (F_K + b * 32 + i) * 128;
    } else if (ty == Q_QKVB) {
        g.type = T_BL; g.w1 = p.wqkvb + i * 768; g.w1b = g.w1 + 384; g.ldw = 3840;
        g.sV = 1920 * 64; g.np1 = 2;
        g.out = p.ws + OFF_QKVB + (size_t)s0 * g.sV + i * 48 * 512; g.outb = g.out + 24 * 512;
    } else {
        const int c0 = min(i * DH, DM - DH);
        g.type = T_BL; g.w1 = g.w1b = p.wob + c0 * 2; g.ldw = 1280;
        xf = p.ws + OFF_OB;
        g.sV = 40960;
        g.out = g.outb = p.ws + OFF_BOUT + (size_t)s0 * 40960 + c0 * 64;
    }
    g.x = xf + (size_t)s0 * 40960;
    if (ty <= Q_KQ && b < 2) gemm_task<2>(g, y, p.ws, lds, w);
    else gemm_task<4>(g, y, p.ws, lds, w);
}
DI int pblk(int s) { return s < 8 ? s >> 2 : min(s >> 3, 7) + 1; }

template <int IMM>
DI void gld16c(u32x4& r, const char* sbase, u32 voff) {
    asm volatile("global_load_dwordx4 %0, %1, %2 offset:%3 sc1" : "=v"(r) : "v"(voff), "s"(sbase), "i"(IMM) : "memory");
}
DI void gst4c(char* sbase, u32 voff, u32 v) {
    asm volatile("global_store_dword %0, %1, %2 sc1" : : "v"(voff), "v"(v), "s"(sbase) : "memory");
}
template <int N>
DI void waitv(u32x4& r) { asm volatile("s_waitcnt vmcnt(%1)" : "+v"(r) : "i"(N) : "memory"); }
DI u32 mt() { return (u32)__builtin_amdgcn_s_memtime(); }
DI u32 rt() { return (u32)__builtin_amdgcn_s_memrealtime(); }
DI void spin_to(u32 tgt) { while ((int)(mt() - tgt) < 0) {} }
DI u32 max3u(u32 a, u32 b, u32 c) { return max(max(a, b), c); }
DI u32x4 ldsr16(u32 a) { return *LDSP(u32x4, a); }
DI void clocks(u32& r, u32& m) {
    u64 a, b;
    asm volatile("s_memrealtime %0\n\ts_memtime %1\n\ts_waitcnt lgkmcnt(0)" : "=s"(a), "=s"(b) : : "memory");
    r = (u32)a; m = (u32)b;
}
#define SLATE 20
DI void edge(u32& r, u32& m) {
    u64 a, t[9];
    u32 n = 0;
#define RT(i) "s_memrealtime %" #i "\n\ts_nop 1\n\t"
#define RC(i) "\n\ts_cmp_eq_u64 %" #i ", %1\n\ts_addc_u32 %10, %10, 0"
    asm volatile("s_memtime %0\n\t" RT(1) RT(2) RT(3) RT(4) RT(5) RT(6) RT(7) RT(8) "s_memrealtime %9\n\ts_waitcnt lgkmcnt(0)" RC(2) RC(3) RC(4) RC(5) RC(6) RC(7) RC(8) RC(9)
                 : "=&s"(a), "=&s"(t[0]), "=&s"(t[1]), "=&s"(t[2]), "=&s"(t[3]), "=&s"(t[4]), "=&s"(t[5]), "=&s"(t[6]), "=&s"(t[7]), "=&s"(t[8]), "+s"(n) : : "scc", "memory");
    m = (u32)a + 12 * n;
    r = (u32)t[0] + 4;
}
#define SPB 90
struct Clock {
    u32 r0, m0, r1, m1; int cpt;
    DI void init() { edge(r0, m0); r1 = r0; m1 = m0; cpt = 24 * 64; }
    DI u32 sched(u32& phi, int ofs, int G, int mode) {
        if (mode == 0) edge(r1, m1);
        u32 r = r1, m = m1;
        if (mode == 2) clocks(r, m);
        int rem = (int)(phi + (u32)ofs - r);
        if (__builtin_expect(rem < -SLATE, 0)) { const int k = (-rem / G + 1) * G; phi += (u32)k; rem += k; }
        return m + (u32)((min(rem, 3000) * cpt) >> 6) - SPB;
    }
    DI u32 at(u32 t) { u32 r, m; clocks(r, m); return m + (u32)((min(max((int)(t - r), 0), 3000) * cpt) >> 6); }
    DI void calib() {
        const int dr = (int)(r1 - r0);
        if (dr >= 60) { int c = (int)fminf(fmaxf((float)(int)(m1 - m0) * 64.f * __builtin_amdgcn_rcpf((float)dr), 256.f), 2560.f); asm volatile("" : "+v"(c)); cpt = uni(c); r0 = r1; m0 = m1; }
    }
};
#define VISS 0
#define VISV 0
DI void follow(u32& phi, int ofs, u32 t, int G) { const int late = (int)(t - phi) - ofs; if (__builtin_expect(late > 0, 0)) phi += (u32)((late / G + 1) * G); }
DI bool unwritten(const char* base, u32 off) {
#pragma nounroll
    for (int i = 0; i < 10; ++i) {
        u32x4 t;
        gld16c<-4096>(t, base + (i + (i & 8)) * 1024, off); waitv<0>(t);
        if (__builtin_amdgcn_ballot_w64(max3u(max(t[0], t[1]), t[2], t[3]) == 0xffffffffu) != 0) return true;
    }
    return false;
}

DI size_t uslot(int sd, int s) { const int r = (s * 43) >> 8; return OFF_U + (u32)uni(s * (int)U_STRIDE + r * (int)((2u << 20) - 6 * U_STRIDE) + ((sd >> (2 + r)) & 1) * 8192); }
DI int uchunk(int v) { return (2 * (v & 3) + ((v >> 2) & 1) + (v & ~7)) * 8192; }
DI size_t mslot(int sd, int s) { return (u32)uni(s * (int)HO_STRIDE + ((sd >> (s >> 5)) & 1) * 8192 + 4096); }
struct ChainCfg { int T, cR, G, dV, fast, s1, T2, cR2, s2, T3, cR3, dU; };
constexpr int TT_OFF = 88 + 48 + 360;
DI void karg_warm() {
    u32 a[13];
    asm volatile("s_load_dword %0, %13, 0\n\t" "s_load_dword %1, %13, 64\n\t" "s_load_dword %2, %13, 128\n\t" "s_load_dword %3, %13, 192\n\t" "s_load_dword %4, %13, 256\n\t" "s_load_dword %5, %13, 320\n\t" "s_load_dword %6, %13, 384\n\t" "s_load_dword %7, %13, 448\n\t" "s_load_dword %8, %13, 512\n\t" "s_load_dword %9, %13, 576\n\t" "s_load_dword %10, %13, 640\n\t" "s_load_dword %11, %13, 704\n\t" "s_load_dword %12, %13, 748\n\t" "s_waitcnt lgkmcnt(0)"
                 : "=&s"(a[0]), "=&s"(a[1]), "=&s"(a[2]), "=&s"(a[3]), "=&s"(a[4]), "=&s"(a[5]), "=&s"(a[6]), "=&s"(a[7]), "=&s"(a[8]), "=&s"(a[9]), "=&s"(a[10]), "=&s"(a[11]), "=&s"(a[12])
                 : "s"(__builtin_amdgcn_kernarg_segment_ptr()) : "memory");
}
DI u32 sched_ld0(int s) {
    u32 r;
    asm volatile("s_load_dword %0, %1, %2 offset:%3\n\ts_waitcnt lgkmcnt(0)" : "=s"(r) : "s"(__builtin_amdgcn_kernarg_segment_ptr()), "s"(4 * s), "n"(TT_OFF) : "memory");
    return r;
}
#define HP 60
#define HQ 96

constexpr int SL_W = 0;
constexpr int SL_IMG = 131072;
constexpr int SL_KX = SL_IMG + 6656;
constexpr int SL_PF = SL_KX + 12288 + 1024;
constexpr int S_LDS = SL_PF + 256;
DI void nap(int t) { const u32 wake = rt() + (u32)t; while ((int)(rt() - wake) < 0) __builtin_amdgcn_s_sleep(4); }
DI void dly(int n) {
    asm volatile(".Ld%=:\n\ts_sub_u32 %0, %0, 1\n\ts_nop 0\n\ts_cbranch_scc0 .Ld%=" : "+s"(n) : : "scc", "memory");
}
DI void pfl(const char* sbase, u32 voff, u32 lds) {
    asm volatile("s_mov_b32 m0, %2\n\ts_nop 3\n\tglobal_load_lds_dword %0, %1" : : "v"(voff), "s"(sbase), "s"(lds + SL_PF) : "memory");
}
DI void score_role(const Ptrs& p, const ChainCfg cfg, int h, u32 lds, int w) {
    const int lane = lanex(), tid = 64 * w + lane;
    const int n = lane & 15, g = lane >> 4, qq = (lane >> 2) & 3, pp = lane & 3;
    u32x32 wa[8], wv[3];
    const u32 wreg = lds + SL_W + (u32)w * 32768;
    const u32 lfa = wreg + lane * 16;
    const char* kxp = p.ws + OFF_KX + (size_t)h * 12288 + w * 3072;
    const u32 kxl = lds + SL_KX;
    char* const fl = p.ws + OFF_ST;
    const u32 kxw = kxl + w * 3072 - (w < 2 ? 16 : 0);
    auto kxdma = [&]() {
        asm volatile("s_mov_b32 m0, %2\n\ts_nop 3\n\tglobal_load_lds_dwordx4 %0, %1 sc1\n\tglobal_load_lds_dwordx4 %0, %1 offset:1024 sc1\n\tglobal_load_lds_dwordx4 %0, %1 offset:2048 sc1"
                     : : "v"(lanex() * 16), "s"(kxp), "s"(kxw) : "memory");
    };
    u32 kfl, phi;
    int sd;
    {
        constexpr int GS = 3104;
        Sync y;
        if (w < 3) {
#pragma unroll
            for (int i = PXT; i < NSEG; ++i) gst16(p.ws + OFF_KM + (i * NH + h) * 3072 + w * 1024, lane * 16, u32x4{~0u, ~0u, ~0u, ~0u});
#pragma unroll
            for (int i = NSEG - 4; i < NSEG; ++i) gst16(p.ws + OFF_PX + i * 81920 + (w < 2 ? h * 2048 + w * 1024 : 65536 + h * 512), w < 2 ? lane * 16 : (lane * 16) & 511, u32x4{~0u, ~0u, ~0u, ~0u});
        }
        auto dvo = [&](int i) { const int ix = i * 64 + lane; return (u32)((ix / 24) * (HD * 2) + (ix % 24) * 16); };
        const u32 tra = lds + (2 * (g >> 1) + (qq >> 1)) * GS + (4 * (g & 1) + (qq & 1)) * 384 + 8 * pp;
        u32x8 pa = u32x8{dvo(0), dvo(1), dvo(2), tra + 96 * w + 32 * (w & 1), tra + 96 * w - 64 * (w & 1), tra + 32 * w, lfa, (u32)(lane & 31) * 8};
        asm volatile("s_nop 4\n\t" "s_add_u32 s42, %21, 0x0\n\t" "s_add_u32 s42, s46, s42\n\t" "s_addc_u32 s43, s47, 0\n\t" "s_add_u32 m0, %18, 0x0\n\t" "s_nop 0\n\t" "global_load_lds_dwordx4 v224, s[42:43]\n\t"
                     "s_add_u32 m0, %18, 0x400\n\t" "s_nop 0\n\t" "global_load_lds_dwordx4 v225, s[42:43]\n\t" "s_add_u32 m0, %18, 0x800\n\t" "s_nop 0\n\t" "global_load_lds_dwordx4 v226, s[42:43]\n\t"
                     "s_add_u32 s42, %21, 0x18000\n\t" "s_add_u32 s42, s46, s42\n\t" "s_addc_u32 s43, s47, 0\n\t" "s_add_u32 m0, %18, 0xc20\n\t" "s_nop 0\n\t" "global_load_lds_dwordx4 v224, s[42:43]\n\t"
                     "s_add_u32 m0, %18, 0x1020\n\t" "s_nop 0\n\t" "global_load_lds_dwordx4 v225, s[42:43]\n\t" "s_add_u32 m0, %18, 0x1420\n\t" "s_nop 0\n\t" "global_load_lds_dwordx4 v226, s[42:43]\n\t"
                     "s_sub_u32 s42, 0xc0000, %21\n\t" "s_add_u32 s42, s46, s42\n\t" "s_addc_u32 s43, s47, 0\n\t" "s_add_u32 m0, %18, 0x6100\n\t" "s_nop 0\n\t" "global_load_lds_dwordx4 v224, s[42:43]\n\t"
                     "s_add_u32 m0, %18, 0x6500\n\t" "s_nop 0\n\t" "global_load_lds_dwordx4 v225, s[42:43]\n\t" "s_add_u32 m0, %18, 0x6900\n\t" "s_nop 0\n\t" "global_load_lds_dwordx4 v226, s[42:43]\n\t"
                     "s_sub_u32 s42, 0xd8000, %21\n\t" "s_add_u32 s42, s46, s42\n\t" "s_addc_u32 s43, s47, 0\n\t" "s_add_u32 m0, %18, 0x6d20\n\t" "s_nop 0\n\t" "global_load_lds_dwordx4 v224, s[42:43]\n\t"
                     "s_add_u32 m0, %18, 0x7120\n\t" "s_nop 0\n\t" "global_load_lds_dwordx4 v225, s[42:43]\n\t" "s_add_u32 m0, %18, 0x7520\n\t" "s_nop 0\n\t" "global_load_lds_dwordx4 v226, s[42:43]\n\t"
                     "s_add_u32 s42, %21, 0x180000\n\t" "s_add_u32 s42, s46, s42\n\t" "s_addc_u32 s43, s47, 0\n\t" "s_add_u32 m0, %18, 0xc200\n\t" "s_nop 0\n\t" "global_load_lds_dwordx4 v224, s[42:43]\n\t"
                     "s_add_u32 m0, %18, 0xc600\n\t" "s_nop 0\n\t" "global_load_lds_dwordx4 v225, s[42:43]\n\t" "s_add_u32 m0, %18, 0xca00\n\t" "s_nop 0\n\t" "global_load_lds_dwordx4 v226, s[42:43]\n\t"
                     "s_add_u32 s42, %21, 0x198000\n\t" "s_add_u32 s42, s46, s42\n\t" "s_addc_u32 s43, s47, 0\n\t" "s_add_u32 m0, %18, 0xce20\n\t" "s_nop 0\n\t" "global_load_lds_dwordx4 v224, s[42:43]\n\t"
                     "s_add_u32 m0, %18, 0xd220\n\t" "s_nop 0\n\t" "global_load_lds_dwordx4 v225, s[42:43]\n\t" "s_add_u32 m0, %18, 0xd620\n\t" "s_nop 0\n\t" "global_load_lds_dwordx4 v226, s[42:43]\n\t"
                     "s_sub_u32 s42, 0x240000, %21\n\t" "s_add_u32 s42, s46, s42\n\t" "s_addc_u32 s43, s47, 0\n\t" "s_add_u32 m0, %18, 0x12300\n\t" "s_nop 0\n\t" "global_load_lds_dwordx4 v224, s[42:43]\n\t"
                     "s_add_u32 m0, %18, 0x12700\n\t" "s_nop 0\n\t" "global_load_lds_dwordx4 v225, s[42:43]\n\t" "s_add_u32 m0, %18, 0x12b00\n\t" "s_nop 0\n\t" "global_load_lds_dwordx4 v226, s[42:43]\n\t"
                     "s_sub_u32 s42, 0x258000, %21\n\t" "s_add_u32 s42, s46, s42\n\t" "s_addc_u32 s43, s47, 0\n\t" "s_add_u32 m0, %18, 0x12f20\n\t" "s_nop 0\n\t" "global_load_lds_dwordx4 v224, s[42:43]\n\t"
                     "s_add_u32 m0, %18, 0x13320\n\t" "s_nop 0\n\t" "global_load_lds_dwordx4 v225, s[42:43]\n\t" "s_add_u32 m0, %18, 0x13720\n\t" "s_nop 0\n\t" "global_load_lds_dwordx4 v226, s[42:43]\n\t"
                     "s_waitcnt vmcnt(12) lgkmcnt(0)\n\t" "s_barrier\n\t" "s_add_u32 s42, %21, 0x300000\n\t" "s_add_u32 s42, s46, s42\n\t" "s_addc_u32 s43, s47, 0\n\t" "s_add_u32 m0, %18, 0x18400\n\t" "s_nop 0\n\t"
                     "global_load_lds_dwordx4 v224, s[42:43]\n\t" "s_add_u32 m0, %18, 0x18800\n\t" "s_nop 0\n\t" "global_load_lds_dwordx4 v225, s[42:43]\n\t" "s_add_u32 m0, %18, 0x18c00\n\t" "s_nop 0\n\t"
                     "global_load_lds_dwordx4 v226, s[42:43]\n\t" "s_add_u32 s42, %21, 0x318000\n\t" "s_add_u32 s42, s46, s42\n\t" "s_addc_u32 s43, s47, 0\n\t" "s_add_u32 m0, %18, 0x19020\n\t" "s_nop 0\n\t"
                     "global_load_lds_dwordx4 v224, s[42:43]\n\t" "s_add_u32 m0, %18, 0x19420\n\t" "s_nop 0\n\t" "global_load_lds_dwordx4 v225, s[42:43]\n\t" "s_add_u32 m0, %18, 0x19820\n\t" "s_nop 0\n\t"
                     "global_load_lds_dwordx4 v226, s[42:43]\n\t" "s_sub_u32 s42, 0x3c0000, %21\n\t" "s_add_u32 s42, s46, s42\n\t" "s_addc_u32 s43, s47, 0\n\t" "s_add_u32 m0, %18, 0x1e500\n\t" "s_nop 0\n\t"
                     "global_load_lds_dwordx4 v224, s[42:43]\n\t" "s_add_u32 m0, %18, 0x1e900\n\t" "s_nop 0\n\t" "global_load_lds_dwordx4 v225, s[42:43]\n\t" "s_add_u32 m0, %18, 0x1ed00\n\t" "s_nop 0\n\t"
                     "global_load_lds_dwordx4 v226, s[42:43]\n\t" "s_sub_u32 s42, 0x3d8000, %21\n\t" "s_add_u32 s42, s46, s42\n\t" "s_addc_u32 s43, s47, 0\n\t" "s_add_u32 m0, %18, 0x1f120\n\t" "s_nop 0\n\t"
                     "global_load_lds_dwordx4 v224, s[42:43]\n\t" "s_add_u32 m0, %18, 0x1f520\n\t" "s_nop 0\n\t" "global_load_lds_dwordx4 v225, s[42:43]\n\t" "s_add_u32 m0, %18, 0x1f920\n\t" "s_nop 0\n\t"
                     "global_load_lds_dwordx4 v226, s[42:43]\n\t" "v_add_u32 v232, 0x0, v229\n\t" "ds_read_b64_tr_b16 v[8:9], v232\n\t" "ds_read_b64_tr_b16 v[10:11], v232 offset:768\n\t"
                     "ds_read_b64_tr_b16 v[12:13], v232 offset:12416\n\t" "ds_read_b64_tr_b16 v[14:15], v232 offset:13184\n\t" "ds_read_b64_tr_b16 a[96:97], v232 offset:128\n\t"
                     "ds_read_b64_tr_b16 a[98:99], v232 offset:896\n\t" "ds_read_b64_tr_b16 a[100:101], v232 offset:12544\n\t" "ds_read_b64_tr_b16 a[102:103], v232 offset:13312\n\t"
                     "ds_read_b64_tr_b16 a[176:177], v232 offset:256\n\t" "ds_read_b64_tr_b16 a[178:179], v232 offset:1024\n\t" "ds_read_b64_tr_b16 a[180:181], v232 offset:12672\n\t"
                     "ds_read_b64_tr_b16 a[182:183], v232 offset:13440\n\t" "ds_read_b64_tr_b16 v[40:41], v232 offset:24832\n\t" "ds_read_b64_tr_b16 v[42:43], v232 offset:25600\n\t"
                     "ds_read_b64_tr_b16 v[44:45], v232 offset:37248\n\t" "ds_read_b64_tr_b16 v[46:47], v232 offset:38016\n\t" "ds_read_b64_tr_b16 a[112:113], v232 offset:24960\n\t"
                     "ds_read_b64_tr_b16 a[114:115], v232 offset:25728\n\t" "ds_read_b64_tr_b16 a[116:117], v232 offset:37376\n\t" "ds_read_b64_tr_b16 a[118:119], v232 offset:38144\n\t"
                     "ds_read_b64_tr_b16 a[192:193], v232 offset:25088\n\t" "ds_read_b64_tr_b16 a[194:195], v232 offset:25856\n\t" "ds_read_b64_tr_b16 a[196:197], v232 offset:37504\n\t"
                     "ds_read_b64_tr_b16 a[198:199], v232 offset:38272\n\t" "s_waitcnt vmcnt(12) lgkmcnt(0)\n\t" "s_barrier\n\t" "s_add_u32 s42, %21, 0x480000\n\t" "s_add_u32 s42, s46, s42\n\t" "s_addc_u32 s43, s47, 0\n\t"
                     "s_add_u32 m0, %18, 0x0\n\t" "s_nop 0\n\t" "global_load_lds_dwordx4 v224, s[42:43]\n\t" "s_add_u32 m0, %18, 0x400\n\t" "s_nop 0\n\t" "global_load_lds_dwordx4 v225, s[42:43]\n\t"
                     "s_add_u32 m0, %18, 0x800\n\t" "s_nop 0\n\t" "global_load_lds_dwordx4 v226, s[42:43]\n\t" "s_add_u32 s42, %21, 0x498000\n\t" "s_add_u32 s42, s46, s42\n\t" "s_addc_u32 s43, s47, 0\n\t"
                     "s_add_u32 m0, %18, 0xc20\n\t" "s_nop 0\n\t" "global_load_lds_dwordx4 v224, s[42:43]\n\t" "s_add_u32 m0, %18, 0x1020\n\t" "s_nop 0\n\t" "global_load_lds_dwordx4 v225, s[42:43]\n\t"
                     "s_add_u32 m0, %18, 0x1420\n\t" "s_nop 0\n\t" "global_load_lds_dwordx4 v226, s[42:43]\n\t" "s_sub_u32 s42, 0x540000, %21\n\t" "s_add_u32 s42, s46, s42\n\t" "s_addc_u32 s43, s47, 0\n\t"
                     "s_add_u32 m0, %18, 0x6100\n\t" "s_nop 0\n\t" "global_load_lds_dwordx4 v224, s[42:43]\n\t" "s_add_u32 m0, %18, 0x6500\n\t" "s_nop 0\n\t" "global_load_lds_dwordx4 v225, s[42:43]\n\t"
                     "s_add_u32 m0, %18, 0x6900\n\t" "s_nop 0\n\t" "global_load_lds_dwordx4 v226, s[42:43]\n\t" "s_sub_u32 s42, 0x558000, %21\n\t" "s_add_u32 s42, s46, s42\n\t" "s_addc_u32 s43, s47, 0\n\t"
                     "s_add_u32 m0, %18, 0x6d20\n\t" "s_nop 0\n\t" "global_load_lds_dwordx4 v224, s[42:43]\n\t" "s_add_u32 m0, %18, 0x7120\n\t" "s_nop 0\n\t" "global_load_lds_dwordx4 v225, s[42:43]\n\t"
                     "s_add_u32 m0, %18, 0x7520\n\t" "s_nop 0\n\t" "global_load_lds_dwordx4 v226, s[42:43]\n\t" "v_add_u32 v232, 0xc200, v229\n\t" "ds_read_b64_tr_b16 v[24:25], v232\n\t"
                     "ds_read_b64_tr_b16 v[26:27], v232 offset:768\n\t" "ds_read_b64_tr_b16 v[28:29], v232 offset:12416\n\t" "ds_read_b64_tr_b16 v[30:31], v232 offset:13184\n\t"
                     "ds_read_b64_tr_b16 a[104:105], v232 offset:128\n\t" "ds_read_b64_tr_b16 a[106:107], v232 offset:896\n\t" "ds_read_b64_tr_b16 a[108:109], v232 offset:12544\n\t"
                     "ds_read_b64_tr_b16 a[110:111], v232 offset:13312\n\t" "ds_read_b64_tr_b16 a[184:185], v232 offset:256\n\t" "ds_read_b64_tr_b16 a[186:187], v232 offset:1024\n\t"
                     "ds_read_b64_tr_b16 a[188:189], v232 offset:12672\n\t" "ds_read_b64_tr_b16 a[190:191], v232 offset:13440\n\t" "ds_read_b64_tr_b16 v[56:57], v232 offset:24832\n\t"
                     "ds_read_b64_tr_b16 v[58:59], v232 offset:25600\n\t" "ds_read_b64_tr_b16 v[60:61], v232 offset:37248\n\t" "ds_read_b64_tr_b16 v[62:63], v232 offset:38016\n\t"
                     "ds_read_b64_tr_b16 a[120:121], v232 offset:24960\n\t" "ds_read_b64_tr_b16 a[122:123], v232 offset:25728\n\t" "ds_read_b64_tr_b16 a[124:125], v232 offset:37376\n\t"
                     "ds_read_b64_tr_b16 a[126:127], v232 offset:38144\n\t" "ds_read_b64_tr_b16 a[200:201], v232 offset:25088\n\t" "ds_read_b64_tr_b16 a[202:203], v232 offset:25856\n\t"
                     "ds_read_b64_tr_b16 a[204:205], v232 offset:37504\n\t" "ds_read_b64_tr_b16 a[206:207], v232 offset:38272\n\t" "s_waitcnt vmcnt(12) lgkmcnt(0)\n\t" "s_barrier\n\t" "s_add_u32 s42, %21, 0x600000\n\t"
                     "s_add_u32 s42, s46, s42\n\t" "s_addc_u32 s43, s47, 0\n\t" "s_add_u32 m0, %18, 0xc200\n\t" "s_nop 0\n\t" "global_load_lds_dwordx4 v224, s[42:43]\n\t" "s_add_u32 m0, %18, 0xc600\n\t" "s_nop 0\n\t"
                     "global_load_lds_dwordx4 v225, s[42:43]\n\t" "s_add_u32 m0, %18, 0xca00\n\t" "s_nop 0\n\t" "global_load_lds_dwordx4 v226, s[42:43]\n\t" "s_add_u32 s42, %21, 0x618000\n\t" "s_add_u32 s42, s46, s42\n\t"
                     "s_addc_u32 s43, s47, 0\n\t" "s_add_u32 m0, %18, 0xce20\n\t" "s_nop 0\n\t" "global_load_lds_dwordx4 v224, s[42:43]\n\t" "s_add_u32 m0, %18, 0xd220\n\t" "s_nop 0\n\t"
                     "global_load_lds_dwordx4 v225, s[42:43]\n\t" "s_add_u32 m0, %18, 0xd620\n\t" "s_nop 0\n\t" "global_load_lds_dwordx4 v226, s[42:43]\n\t" "s_sub_u32 s42, 0x6c0000, %21\n\t" "s_add_u32 s42, s46, s42\n\t"
                     "s_addc_u32 s43, s47, 0\n\t" "s_add_u32 m0, %18, 0x12300\n\t" "s_nop 0\n\t" "global_load_lds_dwordx4 v224, s[42:43]\n\t" "s_add_u32 m0, %18, 0x12700\n\t" "s_nop 0\n\t"
                     "global_load_lds_dwordx4 v225, s[42:43]\n\t" "s_add_u32 m0, %18, 0x12b00\n\t" "s_nop 0\n\t" "global_load_lds_dwordx4 v226, s[42:43]\n\t" "s_sub_u32 s42, 0x6d8000, %21\n\t" "s_add_u32 s42, s46, s42\n\t"
                     "s_addc_u32 s43, s47, 0\n\t" "s_add_u32 m0, %18, 0x12f20\n\t" "s_nop 0\n\t" "global_load_lds_dwordx4 v224, s[42:43]\n\t" "s_add_u32 m0, %18, 0x13320\n\t" "s_nop 0\n\t"
                     "global_load_lds_dwordx4 v225, s[42:43]\n\t" "s_add_u32 m0, %18, 0x13720\n\t" "s_nop 0\n\t" "global_load_lds_dwordx4 v226, s[42:43]\n\t" "v_add_u32 v232, 0x18400, v229\n\t"
                     "ds_read_b64_tr_b16 v[72:73], v232\n\t" "ds_read_b64_tr_b16 v[74:75], v232 offset:768\n\t" "ds_read_b64_tr_b16 v[76:77], v232 offset:12416\n\t" "ds_read_b64_tr_b16 v[78:79], v232 offset:13184\n\t"
                     "ds_read_b64_tr_b16 a[128:129], v232 offset:128\n\t" "ds_read_b64_tr_b16 a[130:131], v232 offset:896\n\t" "ds_read_b64_tr_b16 a[132:133], v232 offset:12544\n\t"
                     "ds_read_b64_tr_b16 a[134:135], v232 offset:13312\n\t" "ds_read_b64_tr_b16 a[208:209], v232 offset:256\n\t" "ds_read_b64_tr_b16 a[210:211], v232 offset:1024\n\t"
                     "ds_read_b64_tr_b16 a[212:213], v232 offset:12672\n\t" "ds_read_b64_tr_b16 a[214:215], v232 offset:13440\n\t" "ds_read_b64_tr_b16 v[88:89], v232 offset:24832\n\t"
                     "ds_read_b64_tr_b16 v[90:91], v232 offset:25600\n\t" "ds_read_b64_tr_b16 v[92:93], v232 offset:37248\n\t" "ds_read_b64_tr_b16 v[94:95], v232 offset:38016\n\t"
                     "ds_read_b64_tr_b16 a[136:137], v232 offset:24960\n\t" "ds_read_b64_tr_b16 a[138:139], v232 offset:25728\n\t" "ds_read_b64_tr_b16 a[140:141], v232 offset:37376\n\t"
                     "ds_read_b64_tr_b16 a[142:143], v232 offset:38144\n\t" "ds_read_b64_tr_b16 a[216:217], v232 offset:25088\n\t" "ds_read_b64_tr_b16 a[218:219], v232 offset:25856\n\t"
                     "ds_read_b64_tr_b16 a[220:221], v232 offset:37504\n\t" "ds_read_b64_tr_b16 a[222:223], v232 offset:38272\n\t" "s_waitcnt vmcnt(12) lgkmcnt(0)\n\t" "s_barrier\n\t" "s_add_u32 s42, %21, 0x0\n\t"
                     "s_add_u32 s42, s44, s42\n\t" "s_addc_u32 s43, s45, 0\n\t" "s_add_u32 m0, %18, 0x18400\n\t" "s_nop 0\n\t" "global_load_lds_dwordx4 v224, s[42:43]\n\t" "s_add_u32 m0, %18, 0x18800\n\t" "s_nop 0\n\t"
                     "global_load_lds_dwordx4 v225, s[42:43]\n\t" "s_add_u32 m0, %18, 0x18c00\n\t" "s_nop 0\n\t" "global_load_lds_dwordx4 v226, s[42:43]\n\t" "s_add_u32 s42, %21, 0x18000\n\t" "s_add_u32 s42, s44, s42\n\t"
                     "s_addc_u32 s43, s45, 0\n\t" "s_add_u32 m0, %18, 0x19020\n\t" "s_nop 0\n\t" "global_load_lds_dwordx4 v224, s[42:43]\n\t" "s_add_u32 m0, %18, 0x19420\n\t" "s_nop 0\n\t"
                     "global_load_lds_dwordx4 v225, s[42:43]\n\t" "s_add_u32 m0, %18, 0x19820\n\t" "s_nop 0\n\t" "global_load_lds_dwordx4 v226, s[42:43]\n\t" "s_sub_u32 s42, 0xc0000, %21\n\t" "s_add_u32 s42, s44, s42\n\t"
                     "s_addc_u32 s43, s45, 0\n\t" "s_add_u32 m0, %18, 0x1e500\n\t" "s_nop 0\n\t" "global_load_lds_dwordx4 v224, s[42:43]\n\t" "s_add_u32 m0, %18, 0x1e900\n\t" "s_nop 0\n\t"
                     "global_load_lds_dwordx4 v225, s[42:43]\n\t" "s_add_u32 m0, %18, 0x1ed00\n\t" "s_nop 0\n\t" "global_load_lds_dwordx4 v226, s[42:43]\n\t" "s_sub_u32 s42, 0xd8000, %21\n\t" "s_add_u32 s42, s44, s42\n\t"
                     "s_addc_u32 s43, s45, 0\n\t" "s_add_u32 m0, %18, 0x1f120\n\t" "s_nop 0\n\t" "global_load_lds_dwordx4 v224, s[42:43]\n\t" "s_add_u32 m0, %18, 0x1f520\n\t" "s_nop 0\n\t"
                     "global_load_lds_dwordx4 v225, s[42:43]\n\t" "s_add_u32 m0, %18, 0x1f920\n\t" "s_nop 0\n\t" "global_load_lds_dwordx4 v226, s[42:43]\n\t" "v_add_u32 v232, 0x0, v229\n\t"
                     "ds_read_b64_tr_b16 v[104:105], v232\n\t" "ds_read_b64_tr_b16 v[106:107], v232 offset:768\n\t" "ds_read_b64_tr_b16 v[108:109], v232 offset:12416\n\t"
                     "ds_read_b64_tr_b16 v[110:111], v232 offset:13184\n\t" "ds_read_b64_tr_b16 a[144:145], v232 offset:128\n\t" "ds_read_b64_tr_b16 a[146:147], v232 offset:896\n\t"
                     "ds_read_b64_tr_b16 a[148:149], v232 offset:12544\n\t" "ds_read_b64_tr_b16 a[150:151], v232 offset:13312\n\t" "ds_read_b64_tr_b16 a[224:225], v232 offset:256\n\t"
                     "ds_read_b64_tr_b16 a[226:227], v232 offset:1024\n\t" "ds_read_b64_tr_b16 a[228:229], v232 offset:12672\n\t" "ds_read_b64_tr_b16 a[230:231], v232 offset:13440\n\t"
                     "ds_read_b64_tr_b16 v[120:121], v232 offset:24832\n\t" "ds_read_b64_tr_b16 v[122:123], v232 offset:25600\n\t" "ds_read_b64_tr_b16 v[124:125], v232 offset:37248\n\t"
                     "ds_read_b64_tr_b16 v[126:127], v232 offset:38016\n\t" "ds_read_b64_tr_b16 a[152:153], v232 offset:24960\n\t" "ds_read_b64_tr_b16 a[154:155], v232 offset:25728\n\t"
                     "ds_read_b64_tr_b16 a[156:157], v232 offset:37376\n\t" "ds_read_b64_tr_b16 a[158:159], v232 offset:38144\n\t" "ds_read_b64_tr_b16 a[232:233], v232 offset:25088\n\t"
                     "ds_read_b64_tr_b16 a[234:235], v232 offset:25856\n\t" "ds_read_b64_tr_b16 a[236:237], v232 offset:37504\n\t" "ds_read_b64_tr_b16 a[238:239], v232 offset:38272\n\t" "s_waitcnt vmcnt(12) lgkmcnt(0)\n\t"
                     "s_barrier\n\t" "s_add_u32 s42, %21, 0x180000\n\t" "s_add_u32 s42, s44, s42\n\t" "s_addc_u32 s43, s45, 0\n\t" "s_add_u32 m0, %18, 0x0\n\t" "s_nop 0\n\t" "global_load_lds_dwordx4 v224, s[42:43]\n\t"
                     "s_add_u32 m0, %18, 0x400\n\t" "s_nop 0\n\t" "global_load_lds_dwordx4 v225, s[42:43]\n\t" "s_add_u32 m0, %18, 0x800\n\t" "s_nop 0\n\t" "global_load_lds_dwordx4 v226, s[42:43]\n\t"
                     "s_add_u32 s42, %21, 0x198000\n\t" "s_add_u32 s42, s44, s42\n\t" "s_addc_u32 s43, s45, 0\n\t" "s_add_u32 m0, %18, 0xc20\n\t" "s_nop 0\n\t" "global_load_lds_dwordx4 v224, s[42:43]\n\t"
                     "s_add_u32 m0, %18, 0x1020\n\t" "s_nop 0\n\t" "global_load_lds_dwordx4 v225, s[42:43]\n\t" "s_add_u32 m0, %18, 0x1420\n\t" "s_nop 0\n\t" "global_load_lds_dwordx4 v226, s[42:43]\n\t"
                     "s_sub_u32 s42, 0x240000, %21\n\t" "s_add_u32 s42, s44, s42\n\t" "s_addc_u32 s43, s45, 0\n\t" "s_add_u32 m0, %18, 0x6100\n\t" "s_nop 0\n\t" "global_load_lds_dwordx4 v224, s[42:43]\n\t"
                     "s_add_u32 m0, %18, 0x6500\n\t" "s_nop 0\n\t" "global_load_lds_dwordx4 v225, s[42:43]\n\t" "s_add_u32 m0, %18, 0x6900\n\t" "s_nop 0\n\t" "global_load_lds_dwordx4 v226, s[42:43]\n\t"
                     "s_sub_u32 s42, 0x258000, %21\n\t" "s_add_u32 s42, s44, s42\n\t" "s_addc_u32 s43, s45, 0\n\t" "s_add_u32 m0, %18, 0x6d20\n\t" "s_nop 0\n\t" "global_load_lds_dwordx4 v224, s[42:43]\n\t"
                     "s_add_u32 m0, %18, 0x7120\n\t" "s_nop 0\n\t" "global_load_lds_dwordx4 v225, s[42:43]\n\t" "s_add_u32 m0, %18, 0x7520\n\t" "s_nop 0\n\t" "global_load_lds_dwordx4 v226, s[42:43]\n\t"
                     "v_add_u32 v232, 0xc200, v229\n\t" "ds_read_b64_tr_b16 a[56:57], v232\n\t" "ds_read_b64_tr_b16 a[58:59], v232 offset:768\n\t" "ds_read_b64_tr_b16 a[60:61], v232 offset:12416\n\t"
                     "ds_read_b64_tr_b16 a[62:63], v232 offset:13184\n\t" "ds_read_b64_tr_b16 a[160:161], v232 offset:128\n\t" "ds_read_b64_tr_b16 a[162:163], v232 offset:896\n\t"
                     "ds_read_b64_tr_b16 a[164:165], v232 offset:12544\n\t" "ds_read_b64_tr_b16 a[166:167], v232 offset:13312\n\t" "ds_read_b64_tr_b16 a[240:241], v232 offset:256\n\t"
                     "ds_read_b64_tr_b16 a[242:243], v232 offset:1024\n\t" "ds_read_b64_tr_b16 a[244:245], v232 offset:12672\n\t" "ds_read_b64_tr_b16 a[246:247], v232 offset:13440\n\t"
                     "ds_read_b64_tr_b16 a[88:89], v232 offset:24832\n\t" "ds_read_b64_tr_b16 a[90:91], v232 offset:25600\n\t" "ds_read_b64_tr_b16 a[92:93], v232 offset:37248\n\t"
                     "ds_read_b64_tr_b16 a[94:95], v232 offset:38016\n\t" "ds_read_b64_tr_b16 a[168:169], v232 offset:24960\n\t" "ds_read_b64_tr_b16 a[170:171], v232 offset:25728\n\t"
                     "ds_read_b64_tr_b16 a[172:173], v232 offset:37376\n\t" "ds_read_b64_tr_b16 a[174:175], v232 offset:38144\n\t" "ds_read_b64_tr_b16 a[248:249], v232 offset:25088\n\t"
                     "ds_read_b64_tr_b16 a[250:251], v232 offset:25856\n\t" "ds_read_b64_tr_b16 a[252:253], v232 offset:37504\n\t" "ds_read_b64_tr_b16 a[254:255], v232 offset:38272\n\t" "s_waitcnt vmcnt(12) lgkmcnt(0)\n\t"
                     "s_barrier\n\t" "s_add_u32 s42, %21, 0x300000\n\t" "s_add_u32 s42, s44, s42\n\t" "s_addc_u32 s43, s45, 0\n\t" "s_add_u32 m0, %18, 0xc200\n\t" "s_nop 0\n\t" "global_load_lds_dwordx4 v224, s[42:43]\n\t"
                     "s_add_u32 m0, %18, 0xc600\n\t" "s_nop 0\n\t" "global_load_lds_dwordx4 v225, s[42:43]\n\t" "s_add_u32 m0, %18, 0xca00\n\t" "s_nop 0\n\t" "global_load_lds_dwordx4 v226, s[42:43]\n\t"
                     "s_add_u32 s42, %21, 0x318000\n\t" "s_add_u32 s42, s44, s42\n\t" "s_addc_u32 s43, s45, 0\n\t" "s_add_u32 m0, %18, 0xce20\n\t" "s_nop 0\n\t" "global_load_lds_dwordx4 v224, s[42:43]\n\t"
                     "s_add_u32 m0, %18, 0xd220\n\t" "s_nop 0\n\t" "global_load_lds_dwordx4 v225, s[42:43]\n\t" "s_add_u32 m0, %18, 0xd620\n\t" "s_nop 0\n\t" "global_load_lds_dwordx4 v226, s[42:43]\n\t"
                     "s_sub_u32 s42, 0x3c0000, %21\n\t" "s_add_u32 s42, s44, s42\n\t" "s_addc_u32 s43, s45, 0\n\t" "s_add_u32 m0, %18, 0x12300\n\t" "s_nop 0\n\t" "global_load_lds_dwordx4 v224, s[42:43]\n\t"
                     "s_add_u32 m0, %18, 0x12700\n\t" "s_nop 0\n\t" "global_load_lds_dwordx4 v225, s[42:43]\n\t" "s_add_u32 m0, %18, 0x12b00\n\t" "s_nop 0\n\t" "global_load_lds_dwordx4 v226, s[42:43]\n\t"
                     "s_sub_u32 s42, 0x3d8000, %21\n\t" "s_add_u32 s42, s44, s42\n\t" "s_addc_u32 s43, s45, 0\n\t" "s_add_u32 m0, %18, 0x12f20\n\t" "s_nop 0\n\t" "global_load_lds_dwordx4 v224, s[42:43]\n\t"
                     "s_add_u32 m0, %18, 0x13320\n\t" "s_nop 0\n\t" "global_load_lds_dwordx4 v225, s[42:43]\n\t" "s_add_u32 m0, %18, 0x13720\n\t" "s_nop 0\n\t" "global_load_lds_dwordx4 v226, s[42:43]\n\t"
                     "v_add_u32 v232, 0x18400, v227\n\t" "v_add_u32 v233, 0x18400, v228\n\t" "ds_read_b64_tr_b16 v[128:129], v232\n\t" "ds_read_b64_tr_b16 v[130:131], v232 offset:768\n\t"
                     "ds_read_b64_tr_b16 v[132:133], v232 offset:12416\n\t" "ds_read_b64_tr_b16 v[134:135], v232 offset:13184\n\t" "ds_read_b64_tr_b16 v[0:1], v232 offset:32\n\t"
                     "ds_read_b64_tr_b16 v[2:3], v232 offset:800\n\t" "ds_read_b64_tr_b16 v[4:5], v232 offset:12448\n\t" "ds_read_b64_tr_b16 v[6:7], v232 offset:13216\n\t"
                     "ds_read_b64_tr_b16 v[136:137], v233 offset:64\n\t" "ds_read_b64_tr_b16 v[138:139], v233 offset:832\n\t" "ds_read_b64_tr_b16 v[140:141], v233 offset:12480\n\t"
                     "ds_read_b64_tr_b16 v[142:143], v233 offset:13248\n\t" "ds_read_b64_tr_b16 v[160:161], v232 offset:24832\n\t" "ds_read_b64_tr_b16 v[162:163], v232 offset:25600\n\t"
                     "ds_read_b64_tr_b16 v[164:165], v232 offset:37248\n\t" "ds_read_b64_tr_b16 v[166:167], v232 offset:38016\n\t" "ds_read_b64_tr_b16 v[32:33], v232 offset:24864\n\t"
                     "ds_read_b64_tr_b16 v[34:35], v232 offset:25632\n\t" "ds_read_b64_tr_b16 v[36:37], v232 offset:37280\n\t" "ds_read_b64_tr_b16 v[38:39], v232 offset:38048\n\t"
                     "ds_read_b64_tr_b16 v[168:169], v233 offset:24896\n\t" "ds_read_b64_tr_b16 v[170:171], v233 offset:25664\n\t" "ds_read_b64_tr_b16 v[172:173], v233 offset:37312\n\t"
                     "ds_read_b64_tr_b16 v[174:175], v233 offset:38080\n\t" "s_waitcnt vmcnt(12) lgkmcnt(0)\n\t" "s_barrier\n\t" "v_mov_b32 %14, 0\n\t" "global_load_dwordx2 %11, v231, %19 sc1\n\t"
                     "global_load_dwordx2 %12, %14, %19 offset:512 sc1\n\t" "global_load_dwordx2 %13, %14, %19 offset:1024 sc1\n\t" "s_add_u32 s42, %21, 0x480000\n\t" "s_add_u32 s42, s44, s42\n\t"
                     "s_addc_u32 s43, s45, 0\n\t" "s_add_u32 m0, %18, 0x18400\n\t" "s_nop 0\n\t" "global_load_lds_dwordx4 v224, s[42:43]\n\t" "s_add_u32 m0, %18, 0x18800\n\t" "s_nop 0\n\t"
                     "global_load_lds_dwordx4 v225, s[42:43]\n\t" "s_add_u32 m0, %18, 0x18c00\n\t" "s_nop 0\n\t" "global_load_lds_dwordx4 v226, s[42:43]\n\t" "s_add_u32 s42, %21, 0x498000\n\t" "s_add_u32 s42, s44, s42\n\t"
                     "s_addc_u32 s43, s45, 0\n\t" "s_add_u32 m0, %18, 0x19020\n\t" "s_nop 0\n\t" "global_load_lds_dwordx4 v224, s[42:43]\n\t" "s_add_u32 m0, %18, 0x19420\n\t" "s_nop 0\n\t"
                     "global_load_lds_dwordx4 v225, s[42:43]\n\t" "s_add_u32 m0, %18, 0x19820\n\t" "s_nop 0\n\t" "global_load_lds_dwordx4 v226, s[42:43]\n\t" "s_sub_u32 s42, 0x540000, %21\n\t" "s_add_u32 s42, s44, s42\n\t"
                     "s_addc_u32 s43, s45, 0\n\t" "s_add_u32 m0, %18, 0x1e500\n\t" "s_nop 0\n\t" "global_load_lds_dwordx4 v224, s[42:43]\n\t" "s_add_u32 m0, %18, 0x1e900\n\t" "s_nop 0\n\t"
                     "global_load_lds_dwordx4 v225, s[42:43]\n\t" "s_add_u32 m0, %18, 0x1ed00\n\t" "s_nop 0\n\t" "global_load_lds_dwordx4 v226, s[42:43]\n\t" "s_sub_u32 s42, 0x558000, %21\n\t" "s_add_u32 s42, s44, s42\n\t"
                     "s_addc_u32 s43, s45, 0\n\t" "s_add_u32 m0, %18, 0x1f120\n\t" "s_nop 0\n\t" "global_load_lds_dwordx4 v224, s[42:43]\n\t" "s_add_u32 m0, %18, 0x1f520\n\t" "s_nop 0\n\t"
                     "global_load_lds_dwordx4 v225, s[42:43]\n\t" "s_add_u32 m0, %18, 0x1f920\n\t" "s_nop 0\n\t" "global_load_lds_dwordx4 v226, s[42:43]\n\t" "v_add_u32 v232, 0x0, v227\n\t" "v_add_u32 v233, 0x0, v228\n\t"
                     "ds_read_b64_tr_b16 v[144:145], v232\n\t" "ds_read_b64_tr_b16 v[146:147], v232 offset:768\n\t" "ds_read_b64_tr_b16 v[148:149], v232 offset:12416\n\t"
                     "ds_read_b64_tr_b16 v[150:151], v232 offset:13184\n\t" "ds_read_b64_tr_b16 v[16:17], v232 offset:32\n\t" "ds_read_b64_tr_b16 v[18:19], v232 offset:800\n\t"
                     "ds_read_b64_tr_b16 v[20:21], v232 offset:12448\n\t" "ds_read_b64_tr_b16 v[22:23], v232 offset:13216\n\t" "ds_read_b64_tr_b16 v[152:153], v233 offset:64\n\t"
                     "ds_read_b64_tr_b16 v[154:155], v233 offset:832\n\t" "ds_read_b64_tr_b16 v[156:157], v233 offset:12480\n\t" "ds_read_b64_tr_b16 v[158:159], v233 offset:13248\n\t"
                     "ds_read_b64_tr_b16 v[176:177], v232 offset:24832\n\t" "ds_read_b64_tr_b16 v[178:179], v232 offset:25600\n\t" "ds_read_b64_tr_b16 v[180:181], v232 offset:37248\n\t"
                     "ds_read_b64_tr_b16 v[182:183], v232 offset:38016\n\t" "ds_read_b64_tr_b16 v[48:49], v232 offset:24864\n\t" "ds_read_b64_tr_b16 v[50:51], v232 offset:25632\n\t"
                     "ds_read_b64_tr_b16 v[52:53], v232 offset:37280\n\t" "ds_read_b64_tr_b16 v[54:55], v232 offset:38048\n\t" "ds_read_b64_tr_b16 v[184:185], v233 offset:24896\n\t"
                     "ds_read_b64_tr_b16 v[186:187], v233 offset:25664\n\t" "ds_read_b64_tr_b16 v[188:189], v233 offset:37312\n\t" "ds_read_b64_tr_b16 v[190:191], v233 offset:38080\n\t" "s_waitcnt vmcnt(12) lgkmcnt(0)\n\t"
                     "s_barrier\n\t" "s_add_u32 s42, %21, 0x600000\n\t" "s_add_u32 s42, s44, s42\n\t" "s_addc_u32 s43, s45, 0\n\t" "s_add_u32 m0, %18, 0x0\n\t" "s_nop 0\n\t" "global_load_lds_dwordx4 v224, s[42:43]\n\t"
                     "s_add_u32 m0, %18, 0x400\n\t" "s_nop 0\n\t" "global_load_lds_dwordx4 v225, s[42:43]\n\t" "s_add_u32 m0, %18, 0x800\n\t" "s_nop 0\n\t" "global_load_lds_dwordx4 v226, s[42:43]\n\t"
                     "s_add_u32 s42, %21, 0x618000\n\t" "s_add_u32 s42, s44, s42\n\t" "s_addc_u32 s43, s45, 0\n\t" "s_add_u32 m0, %18, 0xc20\n\t" "s_nop 0\n\t" "global_load_lds_dwordx4 v224, s[42:43]\n\t"
                     "s_add_u32 m0, %18, 0x1020\n\t" "s_nop 0\n\t" "global_load_lds_dwordx4 v225, s[42:43]\n\t" "s_add_u32 m0, %18, 0x1420\n\t" "s_nop 0\n\t" "global_load_lds_dwordx4 v226, s[42:43]\n\t"
                     "s_sub_u32 s42, 0x6c0000, %21\n\t" "s_add_u32 s42, s44, s42\n\t" "s_addc_u32 s43, s45, 0\n\t" "s_add_u32 m0, %18, 0x6100\n\t" "s_nop 0\n\t" "global_load_lds_dwordx4 v224, s[42:43]\n\t"
                     "s_add_u32 m0, %18, 0x6500\n\t" "s_nop 0\n\t" "global_load_lds_dwordx4 v225, s[42:43]\n\t" "s_add_u32 m0, %18, 0x6900\n\t" "s_nop 0\n\t" "global_load_lds_dwordx4 v226, s[42:43]\n\t"
                     "s_sub_u32 s42, 0x6d8000, %21\n\t" "s_add_u32 s42, s44, s42\n\t" "s_addc_u32 s43, s45, 0\n\t" "s_add_u32 m0, %18, 0x6d20\n\t" "s_nop 0\n\t" "global_load_lds_dwordx4 v224, s[42:43]\n\t"
                     "s_add_u32 m0, %18, 0x7120\n\t" "s_nop 0\n\t" "global_load_lds_dwordx4 v225, s[42:43]\n\t" "s_add_u32 m0, %18, 0x7520\n\t" "s_nop 0\n\t" "global_load_lds_dwordx4 v226, s[42:43]\n\t"
                     "global_load_dword %14, %14, %20 sc1\n\t" "v_add_u32 v232, 0xc200, v227\n\t" "v_add_u32 v233, 0xc200, v228\n\t" "ds_read_b64_tr_b16 v[192:193], v232\n\t"
                     "ds_read_b64_tr_b16 v[194:195], v232 offset:768\n\t" "ds_read_b64_tr_b16 v[196:197], v232 offset:12416\n\t" "ds_read_b64_tr_b16 v[198:199], v232 offset:13184\n\t"
                     "ds_read_b64_tr_b16 v[64:65], v232 offset:32\n\t" "ds_read_b64_tr_b16 v[66:67], v232 offset:800\n\t" "ds_read_b64_tr_b16 v[68:69], v232 offset:12448\n\t"
                     "ds_read_b64_tr_b16 v[70:71], v232 offset:13216\n\t" "ds_read_b64_tr_b16 v[200:201], v233 offset:64\n\t" "ds_read_b64_tr_b16 v[202:203], v233 offset:832\n\t"
                     "ds_read_b64_tr_b16 v[204:205], v233 offset:12480\n\t" "ds_read_b64_tr_b16 v[206:207], v233 offset:13248\n\t" "ds_read_b64_tr_b16 v[208:209], v232 offset:24832\n\t"
                     "ds_read_b64_tr_b16 v[210:211], v232 offset:25600\n\t" "ds_read_b64_tr_b16 v[212:213], v232 offset:37248\n\t" "ds_read_b64_tr_b16 v[214:215], v232 offset:38016\n\t"
                     "ds_read_b64_tr_b16 v[80:81], v232 offset:24864\n\t" "ds_read_b64_tr_b16 v[82:83], v232 offset:25632\n\t" "ds_read_b64_tr_b16 v[84:85], v232 offset:37280\n\t"
                     "ds_read_b64_tr_b16 v[86:87], v232 offset:38048\n\t" "ds_read_b64_tr_b16 v[216:217], v233 offset:24896\n\t" "ds_read_b64_tr_b16 v[218:219], v233 offset:25664\n\t"
                     "ds_read_b64_tr_b16 v[220:221], v233 offset:37312\n\t" "ds_read_b64_tr_b16 v[222:223], v233 offset:38080\n\t" "s_waitcnt vmcnt(13) lgkmcnt(0)\n\t" "s_barrier\n\t" "v_add_u32 v232, 0x18400, v227\n\t"
                     "v_add_u32 v233, 0x18400, v228\n\t" "ds_read_b64_tr_b16 a[0:1], v232\n\t" "ds_read_b64_tr_b16 a[2:3], v232 offset:768\n\t" "ds_read_b64_tr_b16 a[4:5], v232 offset:12416\n\t"
                     "ds_read_b64_tr_b16 a[6:7], v232 offset:13184\n\t" "ds_read_b64_tr_b16 v[96:97], v232 offset:32\n\t" "ds_read_b64_tr_b16 v[98:99], v232 offset:800\n\t"
                     "ds_read_b64_tr_b16 v[100:101], v232 offset:12448\n\t" "ds_read_b64_tr_b16 v[102:103], v232 offset:13216\n\t" "ds_read_b64_tr_b16 a[8:9], v233 offset:64\n\t"
                     "ds_read_b64_tr_b16 a[10:11], v233 offset:832\n\t" "ds_read_b64_tr_b16 a[12:13], v233 offset:12480\n\t" "ds_read_b64_tr_b16 a[14:15], v233 offset:13248\n\t"
                     "ds_read_b64_tr_b16 a[16:17], v232 offset:24832\n\t" "ds_read_b64_tr_b16 a[18:19], v232 offset:25600\n\t" "ds_read_b64_tr_b16 a[20:21], v232 offset:37248\n\t"
                     "ds_read_b64_tr_b16 a[22:23], v232 offset:38016\n\t" "ds_read_b64_tr_b16 v[112:113], v232 offset:24864\n\t" "ds_read_b64_tr_b16 v[114:115], v232 offset:25632\n\t"
                     "ds_read_b64_tr_b16 v[116:117], v232 offset:37280\n\t" "ds_read_b64_tr_b16 v[118:119], v232 offset:38048\n\t" "ds_read_b64_tr_b16 a[24:25], v233 offset:24896\n\t"
                     "ds_read_b64_tr_b16 a[26:27], v233 offset:25664\n\t" "ds_read_b64_tr_b16 a[28:29], v233 offset:37312\n\t" "ds_read_b64_tr_b16 a[30:31], v233 offset:38080\n\t" "s_waitcnt vmcnt(0) lgkmcnt(0)\n\t"
                     "s_barrier\n\t" "v_add_u32 v232, 0x0, v227\n\t" "v_add_u32 v233, 0x0, v228\n\t" "ds_read_b64_tr_b16 a[32:33], v232\n\t" "ds_read_b64_tr_b16 a[34:35], v232 offset:768\n\t"
                     "ds_read_b64_tr_b16 a[36:37], v232 offset:12416\n\t" "ds_read_b64_tr_b16 a[38:39], v232 offset:13184\n\t" "ds_read_b64_tr_b16 a[40:41], v232 offset:32\n\t"
                     "ds_read_b64_tr_b16 a[42:43], v232 offset:800\n\t" "ds_read_b64_tr_b16 a[44:45], v232 offset:12448\n\t" "ds_read_b64_tr_b16 a[46:47], v232 offset:13216\n\t"
                     "ds_read_b64_tr_b16 a[48:49], v233 offset:64\n\t" "ds_read_b64_tr_b16 a[50:51], v233 offset:832\n\t" "ds_read_b64_tr_b16 a[52:53], v233 offset:12480\n\t"
                     "ds_read_b64_tr_b16 a[54:55], v233 offset:13248\n\t" "ds_read_b64_tr_b16 a[64:65], v232 offset:24832\n\t" "ds_read_b64_tr_b16 a[66:67], v232 offset:25600\n\t"
                     "ds_read_b64_tr_b16 a[68:69], v232 offset:37248\n\t" "ds_read_b64_tr_b16 a[70:71], v232 offset:38016\n\t" "ds_read_b64_tr_b16 a[72:73], v232 offset:24864\n\t"
                     "ds_read_b64_tr_b16 a[74:75], v232 offset:25632\n\t" "ds_read_b64_tr_b16 a[76:77], v232 offset:37280\n\t" "ds_read_b64_tr_b16 a[78:79], v232 offset:38048\n\t"
                     "ds_read_b64_tr_b16 a[80:81], v233 offset:24896\n\t" "ds_read_b64_tr_b16 a[82:83], v233 offset:25664\n\t" "ds_read_b64_tr_b16 a[84:85], v233 offset:37312\n\t"
                     "ds_read_b64_tr_b16 a[86:87], v233 offset:38080\n\t" "s_waitcnt lgkmcnt(0)\n\t" "s_barrier\n\t" "ds_write_b128 v230, v[0:3]\n\t" "ds_write_b128 v230, v[4:7] offset:1024\n\t"
                     "ds_write_b128 v230, v[8:11] offset:2048\n\t" "ds_write_b128 v230, v[12:15] offset:3072\n\t" "ds_write_b128 v230, v[16:19] offset:4096\n\t" "ds_write_b128 v230, v[20:23] offset:5120\n\t"
                     "ds_write_b128 v230, v[24:27] offset:6144\n\t" "ds_write_b128 v230, v[28:31] offset:7168\n\t" "ds_write_b128 v230, v[32:35] offset:8192\n\t" "ds_write_b128 v230, v[36:39] offset:9216\n\t"
                     "ds_write_b128 v230, v[40:43] offset:10240\n\t" "ds_write_b128 v230, v[44:47] offset:11264\n\t" "ds_write_b128 v230, v[48:51] offset:12288\n\t" "ds_write_b128 v230, v[52:55] offset:13312\n\t"
                     "ds_write_b128 v230, v[56:59] offset:14336\n\t" "ds_write_b128 v230, v[60:63] offset:15360\n\t" "ds_write_b128 v230, v[64:67] offset:16384\n\t" "ds_write_b128 v230, v[68:71] offset:17408\n\t"
                     "ds_write_b128 v230, v[72:75] offset:18432\n\t" "ds_write_b128 v230, v[76:79] offset:19456\n\t" "ds_write_b128 v230, v[80:83] offset:20480\n\t" "ds_write_b128 v230, v[84:87] offset:21504\n\t"
                     "ds_write_b128 v230, v[88:91] offset:22528\n\t" "ds_write_b128 v230, v[92:95] offset:23552\n\t" "ds_write_b128 v230, v[96:99] offset:24576\n\t" "ds_write_b128 v230, v[100:103] offset:25600\n\t"
                     "ds_write_b128 v230, v[104:107] offset:26624\n\t" "ds_write_b128 v230, v[108:111] offset:27648\n\t" "ds_write_b128 v230, v[112:115] offset:28672\n\t" "ds_write_b128 v230, v[116:119] offset:29696\n\t"
                     "ds_write_b128 v230, v[120:123] offset:30720\n\t" "ds_write_b128 v230, v[124:127] offset:31744\n\t"
                     : "=&{a[0:31]}"(wa[0]), "=&{a[32:63]}"(wa[1]), "=&{a[64:95]}"(wa[2]), "=&{a[96:127]}"(wa[3]), "=&{a[128:159]}"(wa[4]), "=&{a[160:191]}"(wa[5]), "=&{a[192:223]}"(wa[6]), "=&{a[224:255]}"(wa[7]), "=&{v[128:159]}"(wv[0]), "=&{v[160:191]}"(wv[1]), "=&{v[192:223]}"(wv[2]), "=&v"(y.r), "=&v"(y.g), "=&v"(y.e), "=&v"(kfl)
                     : "{v[224:231]}"(pa), "{s[44:45]}"(p.wq + h * DH * 2 + (size_t)(16 * w) * (HD * 2)), "{s[46:47]}"(p.wk + h * DH * 2 + (size_t)(16 * w) * (HD * 2)),
                       "s"(lds + 2 * w * GS), "s"(p.ws + OFF_LAT), "s"(fl + (F_K + h) * 128), "s"((h & 4) * 16 * HD * 2)
                     : "v0", "v1", "v2", "v3", "v4", "v5", "v6", "v7", "v8", "v9", "v10", "v11", "v12", "v13", "v14", "v15", "v16", "v17", "v18", "v19", "v20", "v21", "v22", "v23", "v24", "v25", "v26", "v27", "v28", "v29", "v30", "v31", "v32", "v33", "v34", "v35", "v36", "v37", "v38", "v39", "v40", "v41", "v42", "v43", "v44", "v45", "v46", "v47", "v48", "v49", "v50", "v51", "v52", "v53", "v54", "v55", "v56", "v57", "v58", "v59", "v60", "v61", "v62", "v63", "v64", "v65", "v66", "v67", "v68", "v69", "v70", "v71", "v72", "v73", "v74", "v75", "v76", "v77", "v78", "v79", "v80", "v81", "v82", "v83", "v84", "v85", "v86", "v87", "v88", "v89", "v90", "v91", "v92", "v93", "v94", "v95", "v96", "v97", "v98", "v99", "v100", "v101", "v102", "v103", "v104", "v105", "v106", "v107", "v108", "v109", "v110", "v111", "v112", "v113", "v114", "v115", "v116", "v117", "v118", "v119", "v120", "v121", "v122", "v123", "v124", "v125", "v126", "v127", "v232", "v233", "s42", "s43", "scc", "memory");
        const u64 t = sync_tag(y.e);
        const bool rdy = __builtin_amdgcn_ballot_w64((y.r[0] & ~1u) != (u32)t || y.r[1] != (u32)(t >> 32) || y.g[0] != (u32)t) == 0 && uni((int)kfl) != -1;
        if (rdy) kxdma();
        sd = sync_end(y, p.ws, phi, w == 0);
        if (!rdy) { flag_wait(fl, F_K + h, 1); kxdma(); }
        kxp += NH * 12288;
    }
    const u32 sidx = (lane & 2) ? 0xEEEEu : 0x4444u;
    const u32 moff = lane * 16;
    u32 kxa = kxl - 16 + (n >> 3) * 6160 + (16 * g + 4 * (n & 3) + ((n >> 1) & 2)) * 16;
    const u64 mb0 = 0xffff0000ffff0000ull;
    const int tp = (3 * w + (w & 1)) >> 1, ts = 1 + 3 * (w >> 1);
    u32 qw0 = lds + SL_IMG + tp * 512 + lane * 8, qw1 = lds + SL_IMG + ts * 512 + lane * 8 + 4 * (w & 1);
    const u32 kw0 = lds + SL_IMG + 3072 + w * 288 + n * 16 + 4 * g;
    const u32 qr0 = lds + SL_IMG + (g >> 1) * 512 + w * 128 + 64 * (g & 1) + 8 * qq;
    const u32 kr0 = lds + SL_IMG + 3072 + (qq >> 1) * 288 + g * 64 + (qq & 1) * 16 + 8 * (pp & 1);
    const u32 xso = 32768 + (h >> 4) * 16384 + (h & 15) * 512 + (w >> 1) * 256 + (n >> 3) * 64 + (n & 4) * 32 + (w & 1) * 32 + (g & 1) * 16 + (n & 3) * 4 + (g >> 1) * 8192;
    const u32 pso = (n < 8 ? 20480 + (h >> 1) * 256 + (n & 2) * 16 + (h & 1) * 8 + (n & 4) : 18432 + (h >> 2) * 256 + (h & 1) * 8 + (h & 2) * 2) + w * 64 + (g & 1) * 16 + (g >> 1) * 8192;
    const u64 psm = 0x01aa01aa01aa01aaull;
    const u32 kmo = (u32)(((n >> 3) * 4 + g) * 128 + (n & 7) * 16);
    u32x16 ad = u32x16{moff, sidx, lfa, kxa, qw0, qw1, kw0, kr0 + 1152 * min(w, 2) + (pp >> 1) * 576, qr0, kr0, xso, pso, 0x3dd53b94u, kmo, kmo, kmo};
    asm volatile("" : "+{v[224:239]}"(ad));
    const char* memp = p.ws + mslot(sd, 0);
    char* kmp = p.ws + OFF_KM + h * 3072 + min(w, 2) * 1024;
    const float kscale = 0.07216878364870322f;
    int budget = 30000;
    Clock clk; clk.init();
    u32x32 wk[3];
    asm volatile("" : "={v[64:95]}"(wk[0]), "={v[96:127]}"(wk[1]), "={v[32:63]}"(wk[2]));
    const int kfo = (F_K + 32 + h) * 128;
    for (int s = 0; s < NSEG; ++s) {
        int tries = 0;
        u64 bad, tp8, en = ~0ull;
        u32 tt;
        int kf;
        while (true) {
            const u32 tgt = clk.sched(phi, 0, cfg.G, tries ? 2 : 0);
            asm volatile("s_cmp_lg_u32 %12, 0\n\t" "s_cbranch_scc1 .Lrt_s\n\t" "s_waitcnt vmcnt(0) lgkmcnt(0)\n\t" "s_barrier\n\t" "s_cmp_lg_u32 %13, 0\n\t" "s_cbranch_scc1 .Lsp_s\n\t" ".Lrt_s:\n\t"
                         "ds_read_b128 v[64:67], v226\n\t" "v_mov_b64 v[40:41], 0\n\t" "ds_read_b128 v[68:71], v226 offset:1024\n\t" "v_mov_b64 v[42:43], 0\n\t" "ds_read_b128 v[72:75], v226 offset:2048\n\t"
                         "v_mov_b64 v[44:45], 0\n\t" "ds_read_b128 v[76:79], v226 offset:3072\n\t" "v_mov_b64 v[46:47], 0\n\t" "ds_read_b128 v[80:83], v226 offset:4096\n\t" "v_mov_b64 v[48:49], 0\n\t"
                         "ds_read_b128 v[84:87], v226 offset:5120\n\t" "v_mov_b64 v[50:51], 0\n\t" "ds_read_b128 v[88:91], v226 offset:6144\n\t" "v_mov_b64 v[52:53], 0\n\t" "ds_read_b128 v[92:95], v226 offset:7168\n\t"
                         "v_mov_b64 v[54:55], 0\n\t" "ds_read_b128 v[96:99], v226 offset:8192\n\t" "v_mov_b64 v[56:57], 0\n\t" "ds_read_b128 v[100:103], v226 offset:9216\n\t" "v_mov_b64 v[58:59], 0\n\t"
                         "ds_read_b128 v[104:107], v226 offset:10240\n\t" "v_mov_b64 v[60:61], 0\n\t" "ds_read_b128 v[108:111], v226 offset:11264\n\t" "v_mov_b64 v[62:63], 0\n\t"
                         "ds_read_b128 v[112:115], v226 offset:12288\n\t" "ds_read_b128 v[116:119], v226 offset:13312\n\t" "ds_read_b128 v[120:123], v226 offset:14336\n\t" "ds_read_b128 v[124:127], v226 offset:15360\n\t"
                         ".Lsp_s:\n\t" "s_memtime vcc\n\t" "s_waitcnt lgkmcnt(0)\n\t" "s_sub_i32 s42, %4, vcc_lo\n\t" "s_cmp_gt_i32 s42, 80\n\t" "s_cbranch_scc1 .Lsp_s\n\t" "s_max_i32 s42, s42, 0\n\t"
                         "s_sub_i32 s42, 92, s42\n\t" "s_and_b32 s42, s42, -4\n\t" "s_getpc_b64 vcc\n\t" "s_add_u32 vcc_lo, vcc_lo, s42\n\t" "s_addc_u32 vcc_hi, vcc_hi, 0\n\t" "s_setpc_b64 vcc\n\t" "s_nop 0\n\t" "s_nop 0\n\t"
                         "s_nop 0\n\t" "s_nop 0\n\t" "s_nop 0\n\t" "s_nop 0\n\t" "s_nop 0\n\t" "s_nop 0\n\t" "s_nop 0\n\t" "s_nop 0\n\t" "s_nop 0\n\t" "s_nop 0\n\t" "s_nop 0\n\t" "s_nop 0\n\t" "s_nop 0\n\t" "s_nop 0\n\t"
                         "s_nop 0\n\t" "s_nop 0\n\t" "s_nop 0\n\t" "s_nop 0\n\t" "global_load_dwordx4 v[0:3], v224, %5 offset:-4096 sc1\n\t" "v_add_u32 v238, 0x4000, v224\n\t" "s_nop 9\n\t"
                         "global_load_dwordx4 v[4:7], v224, %5 offset:-2048 sc1\n\t" "s_nop 15\n\t" "global_load_dwordx4 v[8:11], v224, %6 offset:-3072 sc1\n\t" "s_nop 10\n\t"
                         "global_load_dwordx4 v[12:15], v224, %6 offset:-1024 sc1\n\t" "s_nop 10\n\t" "global_load_dwordx4 v[16:19], v224, %5 sc1\n\t" "s_nop 10\n\t" "global_load_dwordx4 v[20:23], v224, %6 offset:1024 sc1\n\t"
                         "s_nop 10\n\t" "global_load_dwordx4 v[24:27], v224, %5 offset:2048 sc1\n\t" "s_nop 10\n\t" "global_load_dwordx4 v[28:31], v224, %6 offset:3072 sc1\n\t" "s_nop 10\n\t"
                         "global_load_dwordx4 v[32:35], v238, %5 offset:-4096 sc1\n\t" "s_nop 10\n\t" "global_load_dwordx4 v[36:39], v238, %6 offset:-3072 sc1\n\t" "s_waitcnt vmcnt(9)\n\t"
                         "v_smfmac_f32_16x16x64_bf16 v[40:43], v[0:3], v[128:135], v225\n\t" "v_smfmac_f32_16x16x64_bf16 v[44:47], v[0:3], v[64:71], v225\n\t" "ds_read_b128 v[64:67], v226 offset:16384\n\t"
                         "v_smfmac_f32_16x16x64_bf16 v[48:51], v[0:3], v[136:143], v225\n\t" "ds_read_b128 v[68:71], v226 offset:17408\n\t" "v_smfmac_f32_16x16x64_bf16 v[52:55], v[0:3], v[72:79], v225\n\t"
                         "ds_read_b128 v[72:75], v226 offset:18432\n\t" "s_waitcnt vmcnt(8)\n\t" "v_smfmac_f32_16x16x64_bf16 v[40:43], v[4:7], v[144:151], v225\n\t" "ds_read_b128 v[76:79], v226 offset:19456\n\t"
                         "v_smfmac_f32_16x16x64_bf16 v[44:47], v[4:7], v[80:87], v225\n\t" "ds_read_b128 v[80:83], v226 offset:20480\n\t" "v_smfmac_f32_16x16x64_bf16 v[48:51], v[4:7], v[152:159], v225\n\t"
                         "ds_read_b128 v[84:87], v226 offset:21504\n\t" "v_smfmac_f32_16x16x64_bf16 v[52:55], v[4:7], v[88:95], v225\n\t" "ds_read_b128 v[88:91], v226 offset:22528\n\t" "s_waitcnt vmcnt(7)\n\t"
                         "v_smfmac_f32_16x16x64_bf16 v[40:43], v[8:11], v[160:167], v225\n\t" "ds_read_b128 v[92:95], v226 offset:23552\n\t" "v_max_u32 v238, v0, v4\n\t"
                         "v_smfmac_f32_16x16x64_bf16 v[44:47], v[8:11], v[96:103], v225\n\t" "ds_read_b128 v[96:99], v226 offset:24576\n\t" "v_cmp_eq_u32 vcc, -1, v238\n\t"
                         "v_smfmac_f32_16x16x64_bf16 v[48:51], v[8:11], v[168:175], v225\n\t" "ds_read_b128 v[100:103], v226 offset:25600\n\t" "v_smfmac_f32_16x16x64_bf16 v[52:55], v[8:11], v[104:111], v225\n\t"
                         "s_and_b64 vcc, vcc, %11\n\t" "s_cbranch_scc1 .Lsk_s\n\t" "ds_read_b128 v[104:107], v226 offset:26624\n\t" "s_waitcnt vmcnt(6)\n\t" "v_smfmac_f32_16x16x64_bf16 v[40:43], v[12:15], v[176:183], v225\n\t"
                         "ds_read_b128 v[108:111], v226 offset:27648\n\t" "v_smfmac_f32_16x16x64_bf16 v[44:47], v[12:15], v[112:119], v225\n\t" "ds_read_b128 v[112:115], v226 offset:28672\n\t"
                         "v_smfmac_f32_16x16x64_bf16 v[48:51], v[12:15], v[184:191], v225\n\t" "ds_read_b128 v[116:119], v226 offset:29696\n\t" "v_smfmac_f32_16x16x64_bf16 v[52:55], v[12:15], v[120:127], v225\n\t"
                         "ds_read_b128 v[120:123], v226 offset:30720\n\t" "s_waitcnt vmcnt(5)\n\t" "v_smfmac_f32_16x16x64_bf16 v[40:43], v[16:19], v[192:199], v225\n\t" "ds_read_b128 v[124:127], v226 offset:31744\n\t"
                         "s_waitcnt lgkmcnt(14)\n\t" "v_smfmac_f32_16x16x64_bf16 v[44:47], v[16:19], v[64:71], v225\n\t" "ds_read_b128 v[64:67], v227\n\t" "v_smfmac_f32_16x16x64_bf16 v[48:51], v[16:19], v[200:207], v225\n\t"
                         "ds_read_b128 v[68:71], v227 offset:1024\n\t" "s_waitcnt lgkmcnt(14)\n\t" "v_smfmac_f32_16x16x64_bf16 v[52:55], v[16:19], v[72:79], v225\n\t" "ds_read_b128 v[72:75], v227 offset:16\n\t"
                         "s_waitcnt vmcnt(4)\n\t" "v_smfmac_f32_16x16x64_bf16 v[40:43], v[20:23], v[208:215], v225\n\t" "ds_read_b128 v[76:79], v227 offset:1040\n\t" "s_waitcnt lgkmcnt(14)\n\t"
                         "v_smfmac_f32_16x16x64_bf16 v[44:47], v[20:23], v[80:87], v225\n\t" "ds_read_b128 v[80:83], v227 offset:2048\n\t" "v_smfmac_f32_16x16x64_bf16 v[48:51], v[20:23], v[216:223], v225\n\t"
                         "ds_read_b128 v[84:87], v227 offset:3072\n\t" "s_waitcnt lgkmcnt(14)\n\t" "v_smfmac_f32_16x16x64_bf16 v[52:55], v[20:23], v[88:95], v225\n\t" "ds_read_b128 v[88:91], v227 offset:2064\n\t"
                         "s_waitcnt vmcnt(3)\n\t" "v_smfmac_f32_16x16x64_bf16 v[40:43], v[24:27], a[0:7], v225\n\t" "ds_read_b128 v[92:95], v227 offset:3088\n\t" "s_waitcnt lgkmcnt(14)\n\t"
                         "v_smfmac_f32_16x16x64_bf16 v[44:47], v[24:27], v[96:103], v225\n\t" "ds_read_b128 v[96:99], v227 offset:4096\n\t" "v_smfmac_f32_16x16x64_bf16 v[48:51], v[24:27], a[8:15], v225\n\t"
                         "ds_read_b128 v[100:103], v227 offset:5120\n\t" "s_waitcnt lgkmcnt(14)\n\t" "v_smfmac_f32_16x16x64_bf16 v[52:55], v[24:27], v[104:111], v225\n\t" "ds_read_b128 v[104:107], v227 offset:4112\n\t"
                         "s_waitcnt vmcnt(2)\n\t" "v_smfmac_f32_16x16x64_bf16 v[40:43], v[28:31], a[16:23], v225\n\t" "ds_read_b128 v[108:111], v227 offset:5136\n\t" "s_waitcnt lgkmcnt(14)\n\t"
                         "v_smfmac_f32_16x16x64_bf16 v[44:47], v[28:31], v[112:119], v225\n\t" "v_smfmac_f32_16x16x64_bf16 v[48:51], v[28:31], a[24:31], v225\n\t" "s_waitcnt lgkmcnt(12)\n\t"
                         "v_smfmac_f32_16x16x64_bf16 v[52:55], v[28:31], v[120:127], v225\n\t" "s_waitcnt vmcnt(1)\n\t" "v_smfmac_f32_16x16x64_bf16 v[40:43], v[32:35], a[32:39], v225\n\t" "v_mov_b64 v[112:113], 0\n\t"
                         "v_smfmac_f32_16x16x64_bf16 v[44:47], v[32:35], a[40:47], v225\n\t" "v_mov_b64 v[114:115], 0\n\t" "v_smfmac_f32_16x16x64_bf16 v[48:51], v[32:35], a[48:55], v225\n\t" "v_mov_b64 v[116:117], 0\n\t"
                         "v_smfmac_f32_16x16x64_bf16 v[52:55], v[32:35], a[56:63], v225\n\t" "v_mov_b64 v[118:119], 0\n\t" "s_waitcnt vmcnt(0)\n\t" "v_smfmac_f32_16x16x64_bf16 v[40:43], v[36:39], a[64:71], v225\n\t"
                         "v_mov_b64 v[120:121], 0\n\t" "v_smfmac_f32_16x16x64_bf16 v[44:47], v[36:39], a[72:79], v225\n\t" "v_mov_b64 v[122:123], 0\n\t" "v_smfmac_f32_16x16x64_bf16 v[48:51], v[36:39], a[80:87], v225\n\t"
                         "v_mov_b64 v[124:125], 0\n\t" "v_smfmac_f32_16x16x64_bf16 v[52:55], v[36:39], a[88:95], v225\n\t" "v_add_f32 v40, v40, v42\n\t" "v_add_f32 v41, v41, v43\n\t"
                         "v_smfmac_f32_16x16x64_bf16 v[56:59], v[0:3], a[96:103], v225\n\t" "v_cmp_u_f32 vcc, v40, v41\n\t" "v_smfmac_f32_16x16x64_bf16 v[56:59], v[4:7], a[104:111], v225\n\t"
                         "v_cvt_pk_bf16_f32 v42, v40, v41\n\t" "v_add_f32 v44, v44, v46\n\t" "v_smfmac_f32_16x16x64_bf16 v[56:59], v[8:11], a[112:119], v225\n\t" "s_and_b64 vcc, vcc, %11\n\t" "s_cbranch_scc1 .Lsk_s\n\t"
                         "v_smfmac_f32_16x16x64_bf16 v[56:59], v[12:15], a[120:127], v225\n\t" "v_add_f32 v45, v45, v47\n\t" "v_cvt_pk_bf16_f32 v43, v44, v45\n\t"
                         "v_smfmac_f32_16x16x64_bf16 v[56:59], v[16:19], a[128:135], v225\n\t" "ds_write_b64 v228, v[42:43]\n\t" "v_add_f32 v48, v48, v50\n\t"
                         "v_smfmac_f32_16x16x64_bf16 v[56:59], v[20:23], a[136:143], v225\n\t" "v_add_f32 v49, v49, v51\n\t" "v_cvt_pk_bf16_f32 v46, v48, v49\n\t"
                         "v_smfmac_f32_16x16x64_bf16 v[56:59], v[24:27], a[144:151], v225\n\t" "ds_write_b32 v229, v46\n\t" "v_add_f32 v52, v52, v54\n\t" "v_smfmac_f32_16x16x64_bf16 v[56:59], v[28:31], a[152:159], v225\n\t"
                         "v_add_f32 v53, v53, v55\n\t" "v_cvt_pk_bf16_f32 v54, v52, v53\n\t" "v_smfmac_f32_16x16x64_bf16 v[56:59], v[32:35], a[160:167], v225\n\t" "ds_write_b32 v230, v54\n\t"
                         "v_smfmac_f32_16x16x64_bf16 v[56:59], v[36:39], a[168:175], v225\n\t" "v_smfmac_f32_16x16x64_bf16 v[60:63], v[0:3], a[176:183], v225\n\t"
                         "v_smfmac_f32_16x16x64_bf16 v[60:63], v[4:7], a[184:191], v225\n\t" "v_add_f32 v56, v56, v58\n\t" "v_smfmac_f32_16x16x64_bf16 v[60:63], v[8:11], a[192:199], v225\n\t" "s_waitcnt lgkmcnt(0)\n\t"
                         "s_barrier\n\t" "v_smfmac_f32_16x16x64_bf16 v[60:63], v[12:15], a[200:207], v225\n\t" "ds_read_b64_tr_b16 v[40:41], v232\n\t" "v_add_f32 v57, v57, v59\n\t"
                         "v_smfmac_f32_16x16x64_bf16 v[60:63], v[16:19], a[208:215], v225\n\t" "ds_read_b64_tr_b16 v[42:43], v232 offset:32\n\t" "v_cvt_pk_bf16_f32 v58, v56, v57\n\t"
                         "v_smfmac_f32_16x16x64_bf16 v[60:63], v[20:23], a[216:223], v225\n\t" "ds_write_b32 v230, v58 offset:1152\n\t" "v_smfmac_f32_16x16x64_bf16 v[60:63], v[24:27], a[224:231], v225\n\t"
                         "ds_read_b64_tr_b16 v[44:45], v232 offset:1024\n\t" "ds_read_b64_tr_b16 v[46:47], v232 offset:1056\n\t" "v_smfmac_f32_16x16x64_bf16 v[60:63], v[28:31], a[232:239], v225\n\t"
                         "ds_read_b64_tr_b16 v[48:49], v232 offset:2048\n\t" "ds_read_b64_tr_b16 v[50:51], v232 offset:2080\n\t" "v_smfmac_f32_16x16x64_bf16 v[60:63], v[32:35], a[240:247], v225\n\t"
                         "ds_read_b64_tr_b16 v[0:1], v233\n\t" "ds_read_b64_tr_b16 v[2:3], v233 offset:32\n\t" "v_smfmac_f32_16x16x64_bf16 v[60:63], v[36:39], a[248:255], v225\n\t"
                         "ds_read_b64_tr_b16 v[4:5], v233 offset:576\n\t" "ds_read_b64_tr_b16 v[6:7], v233 offset:608\n\t" "s_waitcnt lgkmcnt(9)\n\t" "v_smfmac_f32_16x16x64_bf16 v[112:115], v[40:43], v[64:71], v225\n\t"
                         "s_waitcnt lgkmcnt(8)\n\t" "s_barrier\n\t" "v_smfmac_f32_16x16x64_bf16 v[116:119], v[40:43], v[72:79], v225\n\t" "ds_read_b64_tr_b16 v[8:9], v233 offset:1152\n\t" "s_waitcnt lgkmcnt(7)\n\t"
                         "v_add_f32 v60, v60, v62\n\t" "v_add_f32 v61, v61, v63\n\t" "v_smfmac_f32_16x16x64_bf16 v[112:115], v[44:47], v[80:87], v225\n\t" "ds_read_b64_tr_b16 v[10:11], v233 offset:1184\n\t"
                         "v_cvt_pk_bf16_f32 v62, v60, v61\n\t" "v_smfmac_f32_16x16x64_bf16 v[116:119], v[44:47], v[88:95], v225\n\t" "ds_write_b32 v230, v62 offset:2304\n\t" "s_waitcnt lgkmcnt(7)\n\t"
                         "v_smfmac_f32_16x16x64_bf16 v[112:115], v[48:51], v[96:103], v225\n\t" "ds_read_b64_tr_b16 v[12:13], v233 offset:1728\n\t" "v_smfmac_f32_16x16x64_bf16 v[116:119], v[48:51], v[104:111], v225\n\t"
                         "ds_read_b64_tr_b16 v[14:15], v233 offset:1760\n\t" "s_waitcnt lgkmcnt(5)\n\t" "v_smfmac_f32_16x16x64_bf16 v[120:123], v[40:43], v[0:7], v225\n\t" "v_cndmask_b32 v24, v112, v113, %8\n\t"
                         "v_cndmask_b32 v25, v114, v115, %8\n\t" "v_add_f32 v24, v24, v25\n\t" "v_pk_add_f32 v[26:27], v[116:117], v[118:119]\n\t" "v_cndmask_b32 v25, v26, v27, %8\n\t" "v_max_f32 v26, v24, v25\n\t"
                         "s_waitcnt lgkmcnt(2)\n\t" "s_barrier\n\t" "ds_read_b64_tr_b16 v[16:17], v233 offset:2304\n\t" "v_max_f32_dpp v27, v26, v26 row_ror:4 row_mask:0xf bank_mask:0xf\n\t"
                         "ds_read_b64_tr_b16 v[18:19], v233 offset:2336\n\t" "v_max_f32_dpp v27, v26, v27 row_ror:8 row_mask:0xf bank_mask:0xf\n\t" "v_max_f32_dpp v27, v26, v27 row_ror:12 row_mask:0xf bank_mask:0xf\n\t"
                         "ds_read_b64_tr_b16 v[20:21], v233 offset:2880\n\t" "ds_read_b64_tr_b16 v[22:23], v233 offset:2912\n\t" "v_max_f32_dpp v30, v27, v27 row_ror:1 row_mask:0xf bank_mask:0xf\n\t"
                         "v_max_f32_dpp v30, v27, v30 row_ror:2 row_mask:0xf bank_mask:0xf\n\t" "v_max_f32_dpp v30, v27, v30 row_ror:3 row_mask:0xf bank_mask:0xf\n\t" "s_waitcnt lgkmcnt(4)\n\t"
                         "v_pk_add_f32 v[24:25], v[24:25], v[30:31] op_sel_hi:[1,0] neg_lo:[0,1] neg_hi:[0,1]\n\t" "v_pk_mul_f32 v[24:25], v[24:25], v[236:237] op_sel_hi:[1,0]\n\t" "v_exp_f32 v24, v24\n\t"
                         "v_exp_f32 v25, v25\n\t" "v_mul_f32 v31, %10, v30\n\t" "v_cvt_pk_bf16_f32 v28, |v24|, |v25|\n\t" "v_smfmac_f32_16x16x64_bf16 v[120:123], v[44:47], v[8:15], v225\n\t"
                         "global_store_dword v234, v28, %7 offset:-4096 sc1\n\t" "s_waitcnt lgkmcnt(0)\n\t" "v_add_f32 v26, v24, v25\n\t" "v_smfmac_f32_16x16x64_bf16 v[120:123], v[48:51], v[16:23], v225\n\t"
                         "s_nop 0\n\t" "v_add_f32_dpp v27, v26, v26 row_ror:4 row_mask:0xf bank_mask:0xf\n\t" "v_add_f32_dpp v27, v26, v27 row_ror:8 row_mask:0xf bank_mask:0xf\n\t"
                         "v_add_f32_dpp v27, v26, v27 row_ror:12 row_mask:0xf bank_mask:0xf\n\t" "s_nop 1\n\t" "v_add_f32_dpp v29, v27, v27 row_ror:1 row_mask:0xf bank_mask:0xf\n\t"
                         "v_pk_add_f32 v[24:25], v[120:121], v[122:123]\n\t" "v_cndmask_b32 v24, v24, v25, %8\n\t" "v_max_f32 v26, v24, v30\n\t" "v_add_f32_dpp v29, v27, v29 row_ror:2 row_mask:0xf bank_mask:0xf\n\t"
                         "v_add_f32_dpp v29, v27, v29 row_ror:3 row_mask:0xf bank_mask:0xf\n\t" "v_max_f32_dpp v27, v26, v26 row_ror:1 row_mask:0xf bank_mask:0xf\n\t"
                         "v_max_f32_dpp v27, v26, v27 row_ror:2 row_mask:0xf bank_mask:0xf\n\t" "v_max_f32_dpp v27, v26, v27 row_ror:3 row_mask:0xf bank_mask:0xf\n\t" "v_mul_f32 v24, %10, v24\n\t"
                         "v_mul_f32 v29, 0x3e000000, v29\n\t" "v_max_f32_dpp v26, v27, v27 row_ror:4 row_mask:0xf bank_mask:0xf\n\t" "v_fma_f32 v24, -v26, %10, v24\n\t" "v_fma_f32 v25, -v26, %10, v31\n\t"
                         "v_exp_f32 v24, v24\n\t" "v_exp_f32 v25, v25\n\t" "s_nop 0\n\t" "v_fma_f32 v26, v25, v29, v24\n\t" "v_mov_b32_dpp v124, v24 quad_perm:[0,1,2,3] row_mask:0xf bank_mask:0x3\n\t"
                         "v_mov_b32_dpp v125, v24 quad_perm:[1,0,3,2] row_mask:0xf bank_mask:0x3\n\t" "v_add_f32_dpp v27, v26, v26 row_ror:1 row_mask:0xf bank_mask:0xf\n\t"
                         "v_add_f32_dpp v27, v26, v27 row_ror:2 row_mask:0xf bank_mask:0xf\n\t" "v_add_f32_dpp v27, v26, v27 row_ror:3 row_mask:0xf bank_mask:0xf\n\t"
                         "v_mov_b32_dpp v124, v25 quad_perm:[0,1,2,3] row_mask:0x5 bank_mask:0x4\n\t" "v_mov_b32_dpp v125, v25 quad_perm:[0,1,2,3] row_mask:0xa bank_mask:0x4\n\t"
                         "v_add_f32_dpp v26, v27, v27 row_ror:4 row_mask:0xf bank_mask:0xf\n\t" "v_rcp_f32 v26, v26\n\t" "s_mov_b64 exec, %9\n\t" "v_pk_mul_f32 v[124:125], v[124:125], v[26:27] op_sel_hi:[1,0]\n\t"
                         "v_cvt_pk_bf16_f32 v24, |v124|, |v125|\n\t" "global_store_dword v235, v24, %7 offset:-4096 sc1\n\t" "s_mov_b64 exec, -1\n\t" "s_memrealtime %1\n\t" "s_load_dword %2, %14, %13 offset:%15\n\t"
                         "ds_read_b64_tr_b16 v[36:37], v231\n\t" "ds_read_b64_tr_b16 v[38:39], v231 offset:32\n\t" "v_readfirstlane_b32 %3, v237\n\t" "s_mov_b32 m0, %17\n\t" "v_mov_b64 v[40:41], 0\n\t"
                         "v_mov_b64 v[42:43], 0\n\t" "v_mov_b64 v[44:45], 0\n\t" "v_mov_b64 v[46:47], 0\n\t" "v_mov_b64 v[48:49], 0\n\t" "v_mov_b64 v[50:51], 0\n\t" "v_mov_b64 v[52:53], 0\n\t" "v_mov_b64 v[54:55], 0\n\t"
                         "v_mov_b64 v[56:57], 0\n\t" "v_mov_b64 v[58:59], 0\n\t" "v_mov_b64 v[60:61], 0\n\t" "v_mov_b64 v[62:63], 0\n\t" "v_mov_b32 v237, %18\n\t" "s_waitcnt lgkmcnt(0)\n\t" "ds_read_b128 v[64:67], v226\n\t"
                         "ds_read_b128 v[68:71], v226 offset:1024\n\t" "ds_read_b128 v[72:75], v226 offset:2048\n\t" "ds_read_b128 v[76:79], v226 offset:3072\n\t" "ds_read_b128 v[80:83], v226 offset:4096\n\t"
                         "ds_read_b128 v[84:87], v226 offset:5120\n\t" "s_cmp_eq_u32 %3, -1\n\t" "s_cbranch_scc1 .Lnk_s\n\t" "global_load_lds_dwordx4 v224, %16 sc1\n\t" "global_load_lds_dwordx4 v224, %16 offset:1024 sc1\n\t"
                         "global_load_lds_dwordx4 v224, %16 offset:2048 sc1\n\t" ".Lnk_s:\n\t" "global_store_dwordx4 v239, v[36:39], %21 sc1\n\t" "s_and_b64 exec, %19, 1\n\t" "global_load_dword v237, v237, %20 sc1\n\t"
                         "s_mov_b64 exec, -1\n\t" "ds_read_b128 v[88:91], v226 offset:6144\n\t" "ds_read_b128 v[92:95], v226 offset:7168\n\t" "ds_read_b128 v[96:99], v226 offset:8192\n\t"
                         "ds_read_b128 v[100:103], v226 offset:9216\n\t" "ds_read_b128 v[104:107], v226 offset:10240\n\t" "ds_read_b128 v[108:111], v226 offset:11264\n\t" "ds_read_b128 v[112:115], v226 offset:12288\n\t"
                         "ds_read_b128 v[116:119], v226 offset:13312\n\t" "ds_read_b128 v[120:123], v226 offset:14336\n\t" "ds_read_b128 v[124:127], v226 offset:15360\n\t" ".Lsk_s:\n\t" "s_mov_b64 %0, vcc\n\t"
                         : "=&s"(bad), "=&s"(tp8), "=&s"(tt), "=&s"(kf)
                         : "s"(uni((int)tgt)), "s"(memp + (h & 4) * 256), "s"(memp - (h & 4) * 256), "s"((u64)memp & ~8192ull), "s"(mb0), "s"(psm), "s"(kscale * 1.4426950408889634f), "s"(en), "s"(tries), "s"(4 * s),
                           "s"(__builtin_amdgcn_kernarg_segment_ptr()), "n"(TT_OFF), "s"(kxp), "s"(kxw), "s"(kfo + (s < 6 ? 0 : (s + 2) >> 3) * 4096), "s"(0x0040404040404044ull >> s), "s"(fl), "s"(kmp),
                           "{v[224:239]}"(ad), "{v[64:95]}"(wk[0]), "{v[96:127]}"(wk[1]), "{v[32:63]}"(wk[2]),
                           "{a[0:31]}"(wa[0]), "{a[32:63]}"(wa[1]), "{a[64:95]}"(wa[2]), "{a[96:127]}"(wa[3]), "{a[128:159]}"(wa[4]), "{a[160:191]}"(wa[5]), "{a[192:223]}"(wa[6]), "{a[224:255]}"(wa[7]), "{v[128:159]}"(wv[0]), "{v[160:191]}"(wv[1]), "{v[192:223]}"(wv[2])
                         : "v0", "v1", "v2", "v3", "v4", "v5", "v6", "v7", "v8", "v9", "v10", "v11", "v12", "v13", "v14", "v15", "v16", "v17", "v18", "v19", "v20", "v21", "v22", "v23", "v24", "v25", "v26", "v27", "v28", "v29", "v30", "v31", "vcc", "scc", "s42", "memory");
            if (__builtin_expect(bad == 0, 1)) break;
            waitvm<0>();
            if (((++tries & 15) == 0 && !unwritten(memp, ad[0])) || --budget < 0) en = 0;
            phi += (u32)cfg.G;
        }
        if (__builtin_expect(kf == -1, 0)) { flag_wait(fl, F_K + pblk(s + 1) * 32 + h, 1); kxdma(); }
        kxp += s < NSEG - 2 ? NH * 12288 : 0;
        clk.calib();
        if (__builtin_expect(s >= NSEG - 2, 0)) {
            waitvm<0>();
            barrier();
            if (w == 3) flag_add(fl, F_PROG + 8 * s + (h & 7), 1);
        }
        follow(phi, (int)(tt >> 16) - VISS, (u32)tp8, cfg.G);
        phi += tt & 0xffffu;
        memp = p.ws + mslot(sd, s + 1); kmp += NH * 3072;
    }
    if (h == 0 && w == 0) {
        flag_wait(fl, F_ACK, 256);
        Sync y;
        sync_ld(y, p.ws);
        asm volatile("s_waitcnt vmcnt(0)" : "+v"(y.r), "+v"(y.g), "+v"(y.e) : : "memory");
        const u64 t = ((u64)y.e[1] << 32 | y.e[0]) + 1;
        if (lanex() == 0) gst8(p.ws + OFF_LAT, 1024, u32x2{(u32)t, (u32)(t >> 32)});
    }
    asm volatile(".Lkm_last:\n\ts_waitcnt vmcnt(0)" : : : "memory");
}

DI void proj_role(const Ptrs& p, const ChainCfg cfg, int h, u32 lds, int w) {
    u32x32 wa[8], wv[4];
    u32 phi;
    int sd;
    {
        const int lane = lanex(), tid = 64 * w + lane;
        const int g = lane >> 4, qq = (lane >> 2) & 3, pp = lane & 3;
        constexpr int GS = 3104;
        auto dvo = [&](int i) { const int ix = i * 64 + lane; return (u32)((ix / 24) * (HD * 2) + (ix % 24) * 16); };
        auto ovo = [&](int i) { const int u = i * 64 + lane, r = u / 80; return (u32)(r * 1280 + (((u % 80) ^ (2 * (r ^ (w & 2)))) << 4)); };
        const u32 tra = lds + (2 * (g >> 1) + (qq >> 1)) * GS + (4 * (g & 1) + (qq & 1)) * 384 + 96 * w + 32 * (w & 1) + 8 * pp;
        const u32 tro = (4 * g + (qq & 1) + 16 * (qq >> 1)) * 1280 + 8 * pp + ((u32)(2 * w ^ 2 * ((4 * g + qq) & 7)) << 4);
        u32x16 pa = u32x16{dvo(0), dvo(1), dvo(2), tra, tra - 96 * (w & 1), tro, lds + (u32)((w & 1) * 24576 + (w >> 1) * 99328 + lane * 16), (u32)(lane & 31) * 8,
                           ovo(0), ovo(1), ovo(2), ovo(3), ovo(4), tro, tro, tro};
        Sync y;
        asm volatile("s_nop 4\n\t" "s_add_u32 s42, %22, 0x0\n\t" "s_add_u32 s42, s44, s42\n\t" "s_addc_u32 s43, s45, 0\n\t" "s_add_u32 m0, %18, 0x0\n\t" "s_nop 0\n\t" "global_load_lds_dwordx4 v224, s[42:43]\n\t"
                     "s_add_u32 m0, %18, 0x400\n\t" "s_nop 0\n\t" "global_load_lds_dwordx4 v225, s[42:43]\n\t" "s_add_u32 m0, %18, 0x800\n\t" "s_nop 0\n\t" "global_load_lds_dwordx4 v226, s[42:43]\n\t"
                     "s_add_u32 s42, %22, 0x18000\n\t" "s_add_u32 s42, s44, s42\n\t" "s_addc_u32 s43, s45, 0\n\t" "s_add_u32 m0, %18, 0xc20\n\t" "s_nop 0\n\t" "global_load_lds_dwordx4 v224, s[42:43]\n\t"
                     "s_add_u32 m0, %18, 0x1020\n\t" "s_nop 0\n\t" "global_load_lds_dwordx4 v225, s[42:43]\n\t" "s_add_u32 m0, %18, 0x1420\n\t" "s_nop 0\n\t" "global_load_lds_dwordx4 v226, s[42:43]\n\t"
                     "s_sub_u32 s42, 0xc0000, %22\n\t" "s_add_u32 s42, s44, s42\n\t" "s_addc_u32 s43, s45, 0\n\t" "s_add_u32 m0, %18, 0x6100\n\t" "s_nop 0\n\t" "global_load_lds_dwordx4 v224, s[42:43]\n\t"
                     "s_add_u32 m0, %18, 0x6500\n\t" "s_nop 0\n\t" "global_load_lds_dwordx4 v225, s[42:43]\n\t" "s_add_u32 m0, %18, 0x6900\n\t" "s_nop 0\n\t" "global_load_lds_dwordx4 v226, s[42:43]\n\t"
                     "s_sub_u32 s42, 0xd8000, %22\n\t" "s_add_u32 s42, s44, s42\n\t" "s_addc_u32 s43, s45, 0\n\t" "s_add_u32 m0, %18, 0x6d20\n\t" "s_nop 0\n\t" "global_load_lds_dwordx4 v224, s[42:43]\n\t"
                     "s_add_u32 m0, %18, 0x7120\n\t" "s_nop 0\n\t" "global_load_lds_dwordx4 v225, s[42:43]\n\t" "s_add_u32 m0, %18, 0x7520\n\t" "s_nop 0\n\t" "global_load_lds_dwordx4 v226, s[42:43]\n\t"
                     "s_add_u32 s42, %22, 0x180000\n\t" "s_add_u32 s42, s44, s42\n\t" "s_addc_u32 s43, s45, 0\n\t" "s_add_u32 m0, %18, 0xc200\n\t" "s_nop 0\n\t" "global_load_lds_dwordx4 v224, s[42:43]\n\t"
                     "s_add_u32 m0, %18, 0xc600\n\t" "s_nop 0\n\t" "global_load_lds_dwordx4 v225, s[42:43]\n\t" "s_add_u32 m0, %18, 0xca00\n\t" "s_nop 0\n\t" "global_load_lds_dwordx4 v226, s[42:43]\n\t"
                     "s_add_u32 s42, %22, 0x198000\n\t" "s_add_u32 s42, s44, s42\n\t" "s_addc_u32 s43, s45, 0\n\t" "s_add_u32 m0, %18, 0xce20\n\t" "s_nop 0\n\t" "global_load_lds_dwordx4 v224, s[42:43]\n\t"
                     "s_add_u32 m0, %18, 0xd220\n\t" "s_nop 0\n\t" "global_load_lds_dwordx4 v225, s[42:43]\n\t" "s_add_u32 m0, %18, 0xd620\n\t" "s_nop 0\n\t" "global_load_lds_dwordx4 v226, s[42:43]\n\t"
                     "s_sub_u32 s42, 0x240000, %22\n\t" "s_add_u32 s42, s44, s42\n\t" "s_addc_u32 s43, s45, 0\n\t" "s_add_u32 m0, %18, 0x12300\n\t" "s_nop 0\n\t" "global_load_lds_dwordx4 v224, s[42:43]\n\t"
                     "s_add_u32 m0, %18, 0x12700\n\t" "s_nop 0\n\t" "global_load_lds_dwordx4 v225, s[42:43]\n\t" "s_add_u32 m0, %18, 0x12b00\n\t" "s_nop 0\n\t" "global_load_lds_dwordx4 v226, s[42:43]\n\t"
                     "s_sub_u32 s42, 0x258000, %22\n\t" "s_add_u32 s42, s44, s42\n\t" "s_addc_u32 s43, s45, 0\n\t" "s_add_u32 m0, %18, 0x12f20\n\t" "s_nop 0\n\t" "global_load_lds_dwordx4 v224, s[42:43]\n\t"
                     "s_add_u32 m0, %18, 0x13320\n\t" "s_nop 0\n\t" "global_load_lds_dwordx4 v225, s[42:43]\n\t" "s_add_u32 m0, %18, 0x13720\n\t" "s_nop 0\n\t" "global_load_lds_dwordx4 v226, s[42:43]\n\t"
                     "s_waitcnt vmcnt(12) lgkmcnt(0)\n\t" "s_barrier\n\t" "s_add_u32 s42, %22, 0x300000\n\t" "s_add_u32 s42, s44, s42\n\t" "s_addc_u32 s43, s45, 0\n\t" "s_add_u32 m0, %18, 0x18400\n\t" "s_nop 0\n\t"
                     "global_load_lds_dwordx4 v224, s[42:43]\n\t" "s_add_u32 m0, %18, 0x18800\n\t" "s_nop 0\n\t" "global_load_lds_dwordx4 v225, s[42:43]\n\t" "s_add_u32 m0, %18, 0x18c00\n\t" "s_nop 0\n\t"
                     "global_load_lds_dwordx4 v226, s[42:43]\n\t" "s_add_u32 s42, %22, 0x318000\n\t" "s_add_u32 s42, s44, s42\n\t" "s_addc_u32 s43, s45, 0\n\t" "s_add_u32 m0, %18, 0x19020\n\t" "s_nop 0\n\t"
                     "global_load_lds_dwordx4 v224, s[42:43]\n\t" "s_add_u32 m0, %18, 0x19420\n\t" "s_nop 0\n\t" "global_load_lds_dwordx4 v225, s[42:43]\n\t" "s_add_u32 m0, %18, 0x19820\n\t" "s_nop 0\n\t"
                     "global_load_lds_dwordx4 v226, s[42:43]\n\t" "s_sub_u32 s42, 0x3c0000, %22\n\t" "s_add_u32 s42, s44, s42\n\t" "s_addc_u32 s43, s45, 0\n\t" "s_add_u32 m0, %18, 0x1e500\n\t" "s_nop 0\n\t"
                     "global_load_lds_dwordx4 v224, s[42:43]\n\t" "s_add_u32 m0, %18, 0x1e900\n\t" "s_nop 0\n\t" "global_load_lds_dwordx4 v225, s[42:43]\n\t" "s_add_u32 m0, %18, 0x1ed00\n\t" "s_nop 0\n\t"
                     "global_load_lds_dwordx4 v226, s[42:43]\n\t" "s_sub_u32 s42, 0x3d8000, %22\n\t" "s_add_u32 s42, s44, s42\n\t" "s_addc_u32 s43, s45, 0\n\t" "s_add_u32 m0, %18, 0x1f120\n\t" "s_nop 0\n\t"
                     "global_load_lds_dwordx4 v224, s[42:43]\n\t" "s_add_u32 m0, %18, 0x1f520\n\t" "s_nop 0\n\t" "global_load_lds_dwordx4 v225, s[42:43]\n\t" "s_add_u32 m0, %18, 0x1f920\n\t" "s_nop 0\n\t"
                     "global_load_lds_dwordx4 v226, s[42:43]\n\t" "v_add_u32 v237, 0x0, v227\n\t" "v_add_u32 v238, 0x0, v228\n\t" "ds_read_b64_tr_b16 a[0:1], v237\n\t" "ds_read_b64_tr_b16 a[2:3], v237 offset:768\n\t"
                     "ds_read_b64_tr_b16 a[4:5], v237 offset:12416\n\t" "ds_read_b64_tr_b16 a[6:7], v237 offset:13184\n\t" "ds_read_b64_tr_b16 v[0:1], v237 offset:32\n\t" "ds_read_b64_tr_b16 v[2:3], v237 offset:800\n\t"
                     "ds_read_b64_tr_b16 v[4:5], v237 offset:12448\n\t" "ds_read_b64_tr_b16 v[6:7], v237 offset:13216\n\t" "ds_read_b64_tr_b16 a[8:9], v238 offset:64\n\t" "ds_read_b64_tr_b16 a[10:11], v238 offset:832\n\t"
                     "ds_read_b64_tr_b16 a[12:13], v238 offset:12480\n\t" "ds_read_b64_tr_b16 a[14:15], v238 offset:13248\n\t" "ds_read_b64_tr_b16 a[32:33], v237 offset:24832\n\t"
                     "ds_read_b64_tr_b16 a[34:35], v237 offset:25600\n\t" "ds_read_b64_tr_b16 a[36:37], v237 offset:37248\n\t" "ds_read_b64_tr_b16 a[38:39], v237 offset:38016\n\t"
                     "ds_read_b64_tr_b16 v[16:17], v237 offset:24864\n\t" "ds_read_b64_tr_b16 v[18:19], v237 offset:25632\n\t" "ds_read_b64_tr_b16 v[20:21], v237 offset:37280\n\t"
                     "ds_read_b64_tr_b16 v[22:23], v237 offset:38048\n\t" "ds_read_b64_tr_b16 a[40:41], v238 offset:24896\n\t" "ds_read_b64_tr_b16 a[42:43], v238 offset:25664\n\t"
                     "ds_read_b64_tr_b16 a[44:45], v238 offset:37312\n\t" "ds_read_b64_tr_b16 a[46:47], v238 offset:38080\n\t" "s_waitcnt vmcnt(12) lgkmcnt(0)\n\t" "s_barrier\n\t" "s_add_u32 s42, %22, 0x480000\n\t"
                     "s_add_u32 s42, s44, s42\n\t" "s_addc_u32 s43, s45, 0\n\t" "s_add_u32 m0, %18, 0x0\n\t" "s_nop 0\n\t" "global_load_lds_dwordx4 v224, s[42:43]\n\t" "s_add_u32 m0, %18, 0x400\n\t" "s_nop 0\n\t"
                     "global_load_lds_dwordx4 v225, s[42:43]\n\t" "s_add_u32 m0, %18, 0x800\n\t" "s_nop 0\n\t" "global_load_lds_dwordx4 v226, s[42:43]\n\t" "s_add_u32 s42, %22, 0x498000\n\t" "s_add_u32 s42, s44, s42\n\t"
                     "s_addc_u32 s43, s45, 0\n\t" "s_add_u32 m0, %18, 0xc20\n\t" "s_nop 0\n\t" "global_load_lds_dwordx4 v224, s[42:43]\n\t" "s_add_u32 m0, %18, 0x1020\n\t" "s_nop 0\n\t"
                     "global_load_lds_dwordx4 v225, s[42:43]\n\t" "s_add_u32 m0, %18, 0x1420\n\t" "s_nop 0\n\t" "global_load_lds_dwordx4 v226, s[42:43]\n\t" "s_sub_u32 s42, 0x540000, %22\n\t" "s_add_u32 s42, s44, s42\n\t"
                     "s_addc_u32 s43, s45, 0\n\t" "s_add_u32 m0, %18, 0x6100\n\t" "s_nop 0\n\t" "global_load_lds_dwordx4 v224, s[42:43]\n\t" "s_add_u32 m0, %18, 0x6500\n\t" "s_nop 0\n\t"
                     "global_load_lds_dwordx4 v225, s[42:43]\n\t" "s_add_u32 m0, %18, 0x6900\n\t" "s_nop 0\n\t" "global_load_lds_dwordx4 v226, s[42:43]\n\t" "s_sub_u32 s42, 0x558000, %22\n\t" "s_add_u32 s42, s44, s42\n\t"
                     "s_addc_u32 s43, s45, 0\n\t" "s_add_u32 m0, %18, 0x6d20\n\t" "s_nop 0\n\t" "global_load_lds_dwordx4 v224, s[42:43]\n\t" "s_add_u32 m0, %18, 0x7120\n\t" "s_nop 0\n\t"
                     "global_load_lds_dwordx4 v225, s[42:43]\n\t" "s_add_u32 m0, %18, 0x7520\n\t" "s_nop 0\n\t" "global_load_lds_dwordx4 v226, s[42:43]\n\t" "v_add_u32 v237, 0xc200, v227\n\t"
                     "v_add_u32 v238, 0xc200, v228\n\t" "ds_read_b64_tr_b16 a[16:17], v237\n\t" "ds_read_b64_tr_b16 a[18:19], v237 offset:768\n\t" "ds_read_b64_tr_b16 a[20:21], v237 offset:12416\n\t"
                     "ds_read_b64_tr_b16 a[22:23], v237 offset:13184\n\t" "ds_read_b64_tr_b16 v[8:9], v237 offset:32\n\t" "ds_read_b64_tr_b16 v[10:11], v237 offset:800\n\t"
                     "ds_read_b64_tr_b16 v[12:13], v237 offset:12448\n\t" "ds_read_b64_tr_b16 v[14:15], v237 offset:13216\n\t" "ds_read_b64_tr_b16 a[24:25], v238 offset:64\n\t"
                     "ds_read_b64_tr_b16 a[26:27], v238 offset:832\n\t" "ds_read_b64_tr_b16 a[28:29], v238 offset:12480\n\t" "ds_read_b64_tr_b16 a[30:31], v238 offset:13248\n\t"
                     "ds_read_b64_tr_b16 a[48:49], v237 offset:24832\n\t" "ds_read_b64_tr_b16 a[50:51], v237 offset:25600\n\t" "ds_read_b64_tr_b16 a[52:53], v237 offset:37248\n\t"
                     "ds_read_b64_tr_b16 a[54:55], v237 offset:38016\n\t" "ds_read_b64_tr_b16 v[24:25], v237 offset:24864\n\t" "ds_read_b64_tr_b16 v[26:27], v237 offset:25632\n\t"
                     "ds_read_b64_tr_b16 v[28:29], v237 offset:37280\n\t" "ds_read_b64_tr_b16 v[30:31], v237 offset:38048\n\t" "ds_read_b64_tr_b16 a[56:57], v238 offset:24896\n\t"
                     "ds_read_b64_tr_b16 a[58:59], v238 offset:25664\n\t" "ds_read_b64_tr_b16 a[60:61], v238 offset:37312\n\t" "ds_read_b64_tr_b16 a[62:63], v238 offset:38080\n\t" "s_waitcnt vmcnt(12) lgkmcnt(0)\n\t"
                     "s_barrier\n\t" "s_add_u32 s42, %22, 0x600000\n\t" "s_add_u32 s42, s44, s42\n\t" "s_addc_u32 s43, s45, 0\n\t" "s_add_u32 m0, %18, 0xc200\n\t" "s_nop 0\n\t" "global_load_lds_dwordx4 v224, s[42:43]\n\t"
                     "s_add_u32 m0, %18, 0xc600\n\t" "s_nop 0\n\t" "global_load_lds_dwordx4 v225, s[42:43]\n\t" "s_add_u32 m0, %18, 0xca00\n\t" "s_nop 0\n\t" "global_load_lds_dwordx4 v226, s[42:43]\n\t"
                     "s_add_u32 s42, %22, 0x618000\n\t" "s_add_u32 s42, s44, s42\n\t" "s_addc_u32 s43, s45, 0\n\t" "s_add_u32 m0, %18, 0xce20\n\t" "s_nop 0\n\t" "global_load_lds_dwordx4 v224, s[42:43]\n\t"
                     "s_add_u32 m0, %18, 0xd220\n\t" "s_nop 0\n\t" "global_load_lds_dwordx4 v225, s[42:43]\n\t" "s_add_u32 m0, %18, 0xd620\n\t" "s_nop 0\n\t" "global_load_lds_dwordx4 v226, s[42:43]\n\t"
                     "s_sub_u32 s42, 0x6c0000, %22\n\t" "s_add_u32 s42, s44, s42\n\t" "s_addc_u32 s43, s45, 0\n\t" "s_add_u32 m0, %18, 0x12300\n\t" "s_nop 0\n\t" "global_load_lds_dwordx4 v224, s[42:43]\n\t"
                     "s_add_u32 m0, %18, 0x12700\n\t" "s_nop 0\n\t" "global_load_lds_dwordx4 v225, s[42:43]\n\t" "s_add_u32 m0, %18, 0x12b00\n\t" "s_nop 0\n\t" "global_load_lds_dwordx4 v226, s[42:43]\n\t"
                     "s_sub_u32 s42, 0x6d8000, %22\n\t" "s_add_u32 s42, s44, s42\n\t" "s_addc_u32 s43, s45, 0\n\t" "s_add_u32 m0, %18, 0x12f20\n\t" "s_nop 0\n\t" "global_load_lds_dwordx4 v224, s[42:43]\n\t"
                     "s_add_u32 m0, %18, 0x13320\n\t" "s_nop 0\n\t" "global_load_lds_dwordx4 v225, s[42:43]\n\t" "s_add_u32 m0, %18, 0x13720\n\t" "s_nop 0\n\t" "global_load_lds_dwordx4 v226, s[42:43]\n\t"
                     "v_add_u32 v237, 0x18400, v227\n\t" "v_add_u32 v238, 0x18400, v228\n\t" "ds_read_b64_tr_b16 a[64:65], v237\n\t" "ds_read_b64_tr_b16 a[66:67], v237 offset:768\n\t"
                     "ds_read_b64_tr_b16 a[68:69], v237 offset:12416\n\t" "ds_read_b64_tr_b16 a[70:71], v237 offset:13184\n\t" "ds_read_b64_tr_b16 v[32:33], v237 offset:32\n\t"
                     "ds_read_b64_tr_b16 v[34:35], v237 offset:800\n\t" "ds_read_b64_tr_b16 v[36:37], v237 offset:12448\n\t" "ds_read_b64_tr_b16 v[38:39], v237 offset:13216\n\t"
                     "ds_read_b64_tr_b16 a[72:73], v238 offset:64\n\t" "ds_read_b64_tr_b16 a[74:75], v238 offset:832\n\t" "ds_read_b64_tr_b16 a[76:77], v238 offset:12480\n\t"
                     "ds_read_b64_tr_b16 a[78:79], v238 offset:13248\n\t" "ds_read_b64_tr_b16 a[80:81], v237 offset:24832\n\t" "ds_read_b64_tr_b16 a[82:83], v237 offset:25600\n\t"
                     "ds_read_b64_tr_b16 a[84:85], v237 offset:37248\n\t" "ds_read_b64_tr_b16 a[86:87], v237 offset:38016\n\t" "ds_read_b64_tr_b16 v[40:41], v237 offset:24864\n\t"
                     "ds_read_b64_tr_b16 v[42:43], v237 offset:25632\n\t" "ds_read_b64_tr_b16 v[44:45], v237 offset:37280\n\t" "ds_read_b64_tr_b16 v[46:47], v237 offset:38048\n\t"
                     "ds_read_b64_tr_b16 a[88:89], v238 offset:24896\n\t" "ds_read_b64_tr_b16 a[90:91], v238 offset:25664\n\t" "ds_read_b64_tr_b16 a[92:93], v238 offset:37312\n\t"
                     "ds_read_b64_tr_b16 a[94:95], v238 offset:38080\n\t" "s_waitcnt vmcnt(12) lgkmcnt(0)\n\t" "s_barrier\n\t" "s_add_u32 s42, s46, 0x0\n\t" "s_addc_u32 s43, s47, 0\n\t" "s_add_u32 m0, %20, 0x18400\n\t"
                     "s_nop 0\n\t" "global_load_lds_dwordx4 v232, s[42:43]\n\t" "s_add_u32 m0, %20, 0x18800\n\t" "s_nop 0\n\t" "global_load_lds_dwordx4 v233, s[42:43]\n\t" "s_add_u32 m0, %20, 0x18c00\n\t" "s_nop 0\n\t"
                     "global_load_lds_dwordx4 v234, s[42:43]\n\t" "s_add_u32 m0, %20, 0x19000\n\t" "s_nop 0\n\t" "global_load_lds_dwordx4 v235, s[42:43]\n\t" "s_add_u32 m0, %20, 0x19400\n\t" "s_nop 0\n\t"
                     "global_load_lds_dwordx4 v236, s[42:43]\n\t" "s_add_u32 s42, s46, 0x1400\n\t" "s_addc_u32 s43, s47, 0\n\t" "s_add_u32 m0, %20, 0x19800\n\t" "v_xor_b32 v224, 0x80, v232\n\t"
                     "global_load_lds_dwordx4 v224, s[42:43]\n\t" "s_add_u32 m0, %20, 0x19c00\n\t" "v_xor_b32 v224, 0x80, v233\n\t" "global_load_lds_dwordx4 v224, s[42:43]\n\t" "s_add_u32 m0, %20, 0x1a000\n\t"
                     "v_xor_b32 v224, 0x80, v234\n\t" "global_load_lds_dwordx4 v224, s[42:43]\n\t" "s_add_u32 m0, %20, 0x1a400\n\t" "v_xor_b32 v224, 0x80, v235\n\t" "global_load_lds_dwordx4 v224, s[42:43]\n\t"
                     "s_add_u32 m0, %20, 0x1a800\n\t" "v_xor_b32 v224, 0x80, v236\n\t" "global_load_lds_dwordx4 v224, s[42:43]\n\t" "v_add_u32 v237, 0x0, v227\n\t" "v_add_u32 v238, 0x0, v228\n\t"
                     "ds_read_b64_tr_b16 a[96:97], v237\n\t" "ds_read_b64_tr_b16 a[98:99], v237 offset:768\n\t" "ds_read_b64_tr_b16 a[100:101], v237 offset:12416\n\t" "ds_read_b64_tr_b16 a[102:103], v237 offset:13184\n\t"
                     "ds_read_b64_tr_b16 v[48:49], v237 offset:32\n\t" "ds_read_b64_tr_b16 v[50:51], v237 offset:800\n\t" "ds_read_b64_tr_b16 v[52:53], v237 offset:12448\n\t"
                     "ds_read_b64_tr_b16 v[54:55], v237 offset:13216\n\t" "ds_read_b64_tr_b16 a[104:105], v238 offset:64\n\t" "ds_read_b64_tr_b16 a[106:107], v238 offset:832\n\t"
                     "ds_read_b64_tr_b16 a[108:109], v238 offset:12480\n\t" "ds_read_b64_tr_b16 a[110:111], v238 offset:13248\n\t" "ds_read_b64_tr_b16 a[112:113], v237 offset:24832\n\t"
                     "ds_read_b64_tr_b16 a[114:115], v237 offset:25600\n\t" "ds_read_b64_tr_b16 a[116:117], v237 offset:37248\n\t" "ds_read_b64_tr_b16 a[118:119], v237 offset:38016\n\t"
                     "ds_read_b64_tr_b16 v[56:57], v237 offset:24864\n\t" "ds_read_b64_tr_b16 v[58:59], v237 offset:25632\n\t" "ds_read_b64_tr_b16 v[60:61], v237 offset:37280\n\t"
                     "ds_read_b64_tr_b16 v[62:63], v237 offset:38048\n\t" "ds_read_b64_tr_b16 a[120:121], v238 offset:24896\n\t" "ds_read_b64_tr_b16 a[122:123], v238 offset:25664\n\t"
                     "ds_read_b64_tr_b16 a[124:125], v238 offset:37312\n\t" "ds_read_b64_tr_b16 a[126:127], v238 offset:38080\n\t" "s_waitcnt vmcnt(10) lgkmcnt(0)\n\t" "s_barrier\n\t" "s_add_u32 s42, s46, 0xa000\n\t"
                     "s_addc_u32 s43, s47, 0\n\t" "s_add_u32 m0, %20, 0x0\n\t" "s_nop 0\n\t" "global_load_lds_dwordx4 v232, s[42:43]\n\t" "s_add_u32 m0, %20, 0x400\n\t" "s_nop 0\n\t"
                     "global_load_lds_dwordx4 v233, s[42:43]\n\t" "s_add_u32 m0, %20, 0x800\n\t" "s_nop 0\n\t" "global_load_lds_dwordx4 v234, s[42:43]\n\t" "s_add_u32 m0, %20, 0xc00\n\t" "s_nop 0\n\t"
                     "global_load_lds_dwordx4 v235, s[42:43]\n\t" "s_add_u32 m0, %20, 0x1000\n\t" "s_nop 0\n\t" "global_load_lds_dwordx4 v236, s[42:43]\n\t" "s_add_u32 s42, s46, 0xb400\n\t" "s_addc_u32 s43, s47, 0\n\t"
                     "s_add_u32 m0, %20, 0x1400\n\t" "v_xor_b32 v224, 0x80, v232\n\t" "global_load_lds_dwordx4 v224, s[42:43]\n\t" "s_add_u32 m0, %20, 0x1800\n\t" "v_xor_b32 v224, 0x80, v233\n\t"
                     "global_load_lds_dwordx4 v224, s[42:43]\n\t" "s_add_u32 m0, %20, 0x1c00\n\t" "v_xor_b32 v224, 0x80, v234\n\t" "global_load_lds_dwordx4 v224, s[42:43]\n\t" "s_add_u32 m0, %20, 0x2000\n\t"
                     "v_xor_b32 v224, 0x80, v235\n\t" "global_load_lds_dwordx4 v224, s[42:43]\n\t" "s_add_u32 m0, %20, 0x2400\n\t" "v_xor_b32 v224, 0x80, v236\n\t" "global_load_lds_dwordx4 v224, s[42:43]\n\t"
                     "v_add_u32 v237, 0xc200, v227\n\t" "v_add_u32 v238, 0xc200, v228\n\t" "ds_read_b64_tr_b16 a[128:129], v237\n\t" "ds_read_b64_tr_b16 a[130:131], v237 offset:768\n\t"
                     "ds_read_b64_tr_b16 a[132:133], v237 offset:12416\n\t" "ds_read_b64_tr_b16 a[134:135], v237 offset:13184\n\t" "ds_read_b64_tr_b16 a[136:137], v237 offset:32\n\t"
                     "ds_read_b64_tr_b16 a[138:139], v237 offset:800\n\t" "ds_read_b64_tr_b16 a[140:141], v237 offset:12448\n\t" "ds_read_b64_tr_b16 a[142:143], v237 offset:13216\n\t"
                     "ds_read_b64_tr_b16 a[144:145], v238 offset:64\n\t" "ds_read_b64_tr_b16 a[146:147], v238 offset:832\n\t" "ds_read_b64_tr_b16 a[148:149], v238 offset:12480\n\t"
                     "ds_read_b64_tr_b16 a[150:151], v238 offset:13248\n\t" "ds_read_b64_tr_b16 a[152:153], v237 offset:24832\n\t" "ds_read_b64_tr_b16 a[154:155], v237 offset:25600\n\t"
                     "ds_read_b64_tr_b16 a[156:157], v237 offset:37248\n\t" "ds_read_b64_tr_b16 a[158:159], v237 offset:38016\n\t" "ds_read_b64_tr_b16 a[160:161], v237 offset:24864\n\t"
                     "ds_read_b64_tr_b16 a[162:163], v237 offset:25632\n\t" "ds_read_b64_tr_b16 a[164:165], v237 offset:37280\n\t" "ds_read_b64_tr_b16 a[166:167], v237 offset:38048\n\t"
                     "ds_read_b64_tr_b16 a[168:169], v238 offset:24896\n\t" "ds_read_b64_tr_b16 a[170:171], v238 offset:25664\n\t" "ds_read_b64_tr_b16 a[172:173], v238 offset:37312\n\t"
                     "ds_read_b64_tr_b16 a[174:175], v238 offset:38080\n\t" "s_waitcnt vmcnt(10) lgkmcnt(0)\n\t" "s_barrier\n\t" "s_add_u32 s42, s46, 0x14000\n\t" "s_addc_u32 s43, s47, 0\n\t"
                     "s_add_u32 m0, %20, 0xc200\n\t" "s_nop 0\n\t" "global_load_lds_dwordx4 v232, s[42:43]\n\t" "s_add_u32 m0, %20, 0xc600\n\t" "s_nop 0\n\t" "global_load_lds_dwordx4 v233, s[42:43]\n\t"
                     "s_add_u32 m0, %20, 0xca00\n\t" "s_nop 0\n\t" "global_load_lds_dwordx4 v234, s[42:43]\n\t" "s_add_u32 m0, %20, 0xce00\n\t" "s_nop 0\n\t" "global_load_lds_dwordx4 v235, s[42:43]\n\t"
                     "s_add_u32 m0, %20, 0xd200\n\t" "s_nop 0\n\t" "global_load_lds_dwordx4 v236, s[42:43]\n\t" "s_add_u32 s42, s46, 0x15400\n\t" "s_addc_u32 s43, s47, 0\n\t" "s_add_u32 m0, %20, 0xd600\n\t"
                     "v_xor_b32 v224, 0x80, v232\n\t" "global_load_lds_dwordx4 v224, s[42:43]\n\t" "s_add_u32 m0, %20, 0xda00\n\t" "v_xor_b32 v224, 0x80, v233\n\t" "global_load_lds_dwordx4 v224, s[42:43]\n\t"
                     "s_add_u32 m0, %20, 0xde00\n\t" "v_xor_b32 v224, 0x80, v234\n\t" "global_load_lds_dwordx4 v224, s[42:43]\n\t" "s_add_u32 m0, %20, 0xe200\n\t" "v_xor_b32 v224, 0x80, v235\n\t"
                     "global_load_lds_dwordx4 v224, s[42:43]\n\t" "s_add_u32 m0, %20, 0xe600\n\t" "v_xor_b32 v224, 0x80, v236\n\t" "global_load_lds_dwordx4 v224, s[42:43]\n\t" "s_add_u32 s42, %21, 0x18400\n\t"
                     "v_xor_b32 v237, 0x0, v229\n\t" "v_add_u32 v237, s42, v237\n\t" "v_xor_b32 v238, 0x40, v229\n\t" "v_add_u32 v238, s42, v238\n\t" "v_xor_b32 v239, 0x80, v229\n\t" "v_add_u32 v239, s42, v239\n\t"
                     "v_xor_b32 v226, 0xc0, v229\n\t" "v_add_u32 v226, s42, v226\n\t" "ds_read_b64_tr_b16 a[176:177], v237\n\t" "ds_read_b64_tr_b16 a[178:179], v238 offset:2560\n\t"
                     "ds_read_b64_tr_b16 a[200:201], v239\n\t" "ds_read_b64_tr_b16 a[202:203], v226 offset:2560\n\t" "ds_read_b64_tr_b16 a[224:225], v237 offset:256\n\t"
                     "ds_read_b64_tr_b16 a[226:227], v238 offset:2816\n\t" "ds_read_b64_tr_b16 a[248:249], v239 offset:256\n\t" "ds_read_b64_tr_b16 a[250:251], v226 offset:2816\n\t"
                     "ds_read_b64_tr_b16 v[112:113], v237 offset:512\n\t" "ds_read_b64_tr_b16 v[114:115], v238 offset:3072\n\t" "ds_read_b64_tr_b16 v[136:137], v239 offset:512\n\t"
                     "ds_read_b64_tr_b16 v[138:139], v226 offset:3072\n\t" "ds_read_b64_tr_b16 v[160:161], v237 offset:768\n\t" "ds_read_b64_tr_b16 v[162:163], v238 offset:3328\n\t"
                     "ds_read_b64_tr_b16 v[184:185], v239 offset:768\n\t" "ds_read_b64_tr_b16 v[186:187], v226 offset:3328\n\t" "ds_read_b64_tr_b16 v[208:209], v237 offset:1024\n\t"
                     "ds_read_b64_tr_b16 v[210:211], v238 offset:3584\n\t" "ds_read_b64_tr_b16 v[72:73], v239 offset:1024\n\t" "ds_read_b64_tr_b16 v[74:75], v226 offset:3584\n\t" "s_waitcnt vmcnt(10) lgkmcnt(0)\n\t"
                     "s_barrier\n\t" "s_add_u32 s42, s46, 0x1e000\n\t" "s_addc_u32 s43, s47, 0\n\t" "s_add_u32 m0, %20, 0x18400\n\t" "s_nop 0\n\t" "global_load_lds_dwordx4 v232, s[42:43]\n\t"
                     "s_add_u32 m0, %20, 0x18800\n\t" "s_nop 0\n\t" "global_load_lds_dwordx4 v233, s[42:43]\n\t" "s_add_u32 m0, %20, 0x18c00\n\t" "s_nop 0\n\t" "global_load_lds_dwordx4 v234, s[42:43]\n\t"
                     "s_add_u32 m0, %20, 0x19000\n\t" "s_nop 0\n\t" "global_load_lds_dwordx4 v235, s[42:43]\n\t" "s_add_u32 m0, %20, 0x19400\n\t" "s_nop 0\n\t" "global_load_lds_dwordx4 v236, s[42:43]\n\t"
                     "s_add_u32 s42, s46, 0x1f400\n\t" "s_addc_u32 s43, s47, 0\n\t" "s_add_u32 m0, %20, 0x19800\n\t" "v_xor_b32 v224, 0x80, v232\n\t" "global_load_lds_dwordx4 v224, s[42:43]\n\t"
                     "s_add_u32 m0, %20, 0x19c00\n\t" "v_xor_b32 v224, 0x80, v233\n\t" "global_load_lds_dwordx4 v224, s[42:43]\n\t" "s_add_u32 m0, %20, 0x1a000\n\t" "v_xor_b32 v224, 0x80, v234\n\t"
                     "global_load_lds_dwordx4 v224, s[42:43]\n\t" "s_add_u32 m0, %20, 0x1a400\n\t" "v_xor_b32 v224, 0x80, v235\n\t" "global_load_lds_dwordx4 v224, s[42:43]\n\t" "s_add_u32 m0, %20, 0x1a800\n\t"
                     "v_xor_b32 v224, 0x80, v236\n\t" "global_load_lds_dwordx4 v224, s[42:43]\n\t" "s_add_u32 s42, %21, 0x0\n\t" "v_xor_b32 v237, 0x0, v229\n\t" "v_add_u32 v237, s42, v237\n\t"
                     "v_xor_b32 v238, 0x40, v229\n\t" "v_add_u32 v238, s42, v238\n\t" "v_xor_b32 v239, 0x80, v229\n\t" "v_add_u32 v239, s42, v239\n\t" "v_xor_b32 v226, 0xc0, v229\n\t" "v_add_u32 v226, s42, v226\n\t"
                     "ds_read_b64_tr_b16 a[180:181], v237\n\t" "ds_read_b64_tr_b16 a[182:183], v238 offset:2560\n\t" "ds_read_b64_tr_b16 a[204:205], v239\n\t" "ds_read_b64_tr_b16 a[206:207], v226 offset:2560\n\t"
                     "ds_read_b64_tr_b16 a[228:229], v237 offset:256\n\t" "ds_read_b64_tr_b16 a[230:231], v238 offset:2816\n\t" "ds_read_b64_tr_b16 a[252:253], v239 offset:256\n\t"
                     "ds_read_b64_tr_b16 a[254:255], v226 offset:2816\n\t" "ds_read_b64_tr_b16 v[116:117], v237 offset:512\n\t" "ds_read_b64_tr_b16 v[118:119], v238 offset:3072\n\t"
                     "ds_read_b64_tr_b16 v[140:141], v239 offset:512\n\t" "ds_read_b64_tr_b16 v[142:143], v226 offset:3072\n\t" "ds_read_b64_tr_b16 v[164:165], v237 offset:768\n\t"
                     "ds_read_b64_tr_b16 v[166:167], v238 offset:3328\n\t" "ds_read_b64_tr_b16 v[188:189], v239 offset:768\n\t" "ds_read_b64_tr_b16 v[190:191], v226 offset:3328\n\t"
                     "ds_read_b64_tr_b16 v[212:213], v237 offset:1024\n\t" "ds_read_b64_tr_b16 v[214:215], v238 offset:3584\n\t" "ds_read_b64_tr_b16 v[76:77], v239 offset:1024\n\t"
                     "ds_read_b64_tr_b16 v[78:79], v226 offset:3584\n\t" "s_waitcnt vmcnt(10) lgkmcnt(0)\n\t" "s_barrier\n\t" "v_mov_b32 v225, 0\n\t" "global_load_dwordx2 %12, v231, %19 sc1\n\t"
                     "global_load_dwordx2 %13, v225, %19 offset:512 sc1\n\t" "global_load_dwordx2 %14, v225, %19 offset:1024 sc1\n\t" "s_add_u32 s42, s46, 0x28000\n\t" "s_addc_u32 s43, s47, 0\n\t"
                     "s_add_u32 m0, %20, 0x0\n\t" "s_nop 0\n\t" "global_load_lds_dwordx4 v232, s[42:43]\n\t" "s_add_u32 m0, %20, 0x400\n\t" "s_nop 0\n\t" "global_load_lds_dwordx4 v233, s[42:43]\n\t"
                     "s_add_u32 m0, %20, 0x800\n\t" "s_nop 0\n\t" "global_load_lds_dwordx4 v234, s[42:43]\n\t" "s_add_u32 m0, %20, 0xc00\n\t" "s_nop 0\n\t" "global_load_lds_dwordx4 v235, s[42:43]\n\t"
                     "s_add_u32 m0, %20, 0x1000\n\t" "s_nop 0\n\t" "global_load_lds_dwordx4 v236, s[42:43]\n\t" "s_add_u32 s42, s46, 0x29400\n\t" "s_addc_u32 s43, s47, 0\n\t" "s_add_u32 m0, %20, 0x1400\n\t"
                     "v_xor_b32 v224, 0x80, v232\n\t" "global_load_lds_dwordx4 v224, s[42:43]\n\t" "s_add_u32 m0, %20, 0x1800\n\t" "v_xor_b32 v224, 0x80, v233\n\t" "global_load_lds_dwordx4 v224, s[42:43]\n\t"
                     "s_add_u32 m0, %20, 0x1c00\n\t" "v_xor_b32 v224, 0x80, v234\n\t" "global_load_lds_dwordx4 v224, s[42:43]\n\t" "s_add_u32 m0, %20, 0x2000\n\t" "v_xor_b32 v224, 0x80, v235\n\t"
                     "global_load_lds_dwordx4 v224, s[42:43]\n\t" "s_add_u32 m0, %20, 0x2400\n\t" "v_xor_b32 v224, 0x80, v236\n\t" "global_load_lds_dwordx4 v224, s[42:43]\n\t" "s_add_u32 s42, %21, 0xc200\n\t"
                     "v_xor_b32 v237, 0x0, v229\n\t" "v_add_u32 v237, s42, v237\n\t" "v_xor_b32 v238, 0x40, v229\n\t" "v_add_u32 v238, s42, v238\n\t" "v_xor_b32 v239, 0x80, v229\n\t" "v_add_u32 v239, s42, v239\n\t"
                     "v_xor_b32 v226, 0xc0, v229\n\t" "v_add_u32 v226, s42, v226\n\t" "ds_read_b64_tr_b16 a[184:185], v237\n\t" "ds_read_b64_tr_b16 a[186:187], v238 offset:2560\n\t"
                     "ds_read_b64_tr_b16 a[208:209], v239\n\t" "ds_read_b64_tr_b16 a[210:211], v226 offset:2560\n\t" "ds_read_b64_tr_b16 a[232:233], v237 offset:256\n\t"
                     "ds_read_b64_tr_b16 a[234:235], v238 offset:2816\n\t" "ds_read_b64_tr_b16 v[96:97], v239 offset:256\n\t" "ds_read_b64_tr_b16 v[98:99], v226 offset:2816\n\t"
                     "ds_read_b64_tr_b16 v[120:121], v237 offset:512\n\t" "ds_read_b64_tr_b16 v[122:123], v238 offset:3072\n\t" "ds_read_b64_tr_b16 v[144:145], v239 offset:512\n\t"
                     "ds_read_b64_tr_b16 v[146:147], v226 offset:3072\n\t" "ds_read_b64_tr_b16 v[168:169], v237 offset:768\n\t" "ds_read_b64_tr_b16 v[170:171], v238 offset:3328\n\t"
                     "ds_read_b64_tr_b16 v[192:193], v239 offset:768\n\t" "ds_read_b64_tr_b16 v[194:195], v226 offset:3328\n\t" "ds_read_b64_tr_b16 v[216:217], v237 offset:1024\n\t"
                     "ds_read_b64_tr_b16 v[218:219], v238 offset:3584\n\t" "ds_read_b64_tr_b16 v[80:81], v239 offset:1024\n\t" "ds_read_b64_tr_b16 v[82:83], v226 offset:3584\n\t" "s_waitcnt vmcnt(10) lgkmcnt(0)\n\t"
                     "s_barrier\n\t" "s_add_u32 s42, s46, 0x32000\n\t" "s_addc_u32 s43, s47, 0\n\t" "s_add_u32 m0, %20, 0xc200\n\t" "s_nop 0\n\t" "global_load_lds_dwordx4 v232, s[42:43]\n\t" "s_add_u32 m0, %20, 0xc600\n\t"
                     "s_nop 0\n\t" "global_load_lds_dwordx4 v233, s[42:43]\n\t" "s_add_u32 m0, %20, 0xca00\n\t" "s_nop 0\n\t" "global_load_lds_dwordx4 v234, s[42:43]\n\t" "s_add_u32 m0, %20, 0xce00\n\t" "s_nop 0\n\t"
                     "global_load_lds_dwordx4 v235, s[42:43]\n\t" "s_add_u32 m0, %20, 0xd200\n\t" "s_nop 0\n\t" "global_load_lds_dwordx4 v236, s[42:43]\n\t" "s_add_u32 s42, s46, 0x33400\n\t" "s_addc_u32 s43, s47, 0\n\t"
                     "s_add_u32 m0, %20, 0xd600\n\t" "v_xor_b32 v224, 0x80, v232\n\t" "global_load_lds_dwordx4 v224, s[42:43]\n\t" "s_add_u32 m0, %20, 0xda00\n\t" "v_xor_b32 v224, 0x80, v233\n\t"
                     "global_load_lds_dwordx4 v224, s[42:43]\n\t" "s_add_u32 m0, %20, 0xde00\n\t" "v_xor_b32 v224, 0x80, v234\n\t" "global_load_lds_dwordx4 v224, s[42:43]\n\t" "s_add_u32 m0, %20, 0xe200\n\t"
                     "v_xor_b32 v224, 0x80, v235\n\t" "global_load_lds_dwordx4 v224, s[42:43]\n\t" "s_add_u32 m0, %20, 0xe600\n\t" "v_xor_b32 v224, 0x80, v236\n\t" "global_load_lds_dwordx4 v224, s[42:43]\n\t"
                     "s_add_u32 s42, %21, 0x18400\n\t" "v_xor_b32 v237, 0x0, v229\n\t" "v_add_u32 v237, s42, v237\n\t" "v_xor_b32 v238, 0x40, v229\n\t" "v_add_u32 v238, s42, v238\n\t" "v_xor_b32 v239, 0x80, v229\n\t"
                     "v_add_u32 v239, s42, v239\n\t" "v_xor_b32 v226, 0xc0, v229\n\t" "v_add_u32 v226, s42, v226\n\t" "ds_read_b64_tr_b16 a[188:189], v237\n\t" "ds_read_b64_tr_b16 a[190:191], v238 offset:2560\n\t"
                     "ds_read_b64_tr_b16 a[212:213], v239\n\t" "ds_read_b64_tr_b16 a[214:215], v226 offset:2560\n\t" "ds_read_b64_tr_b16 a[236:237], v237 offset:256\n\t"
                     "ds_read_b64_tr_b16 a[238:239], v238 offset:2816\n\t" "ds_read_b64_tr_b16 v[100:101], v239 offset:256\n\t" "ds_read_b64_tr_b16 v[102:103], v226 offset:2816\n\t"
                     "ds_read_b64_tr_b16 v[124:125], v237 offset:512\n\t" "ds_read_b64_tr_b16 v[126:127], v238 offset:3072\n\t" "ds_read_b64_tr_b16 v[148:149], v239 offset:512\n\t"
                     "ds_read_b64_tr_b16 v[150:151], v226 offset:3072\n\t" "ds_read_b64_tr_b16 v[172:173], v237 offset:768\n\t" "ds_read_b64_tr_b16 v[174:175], v238 offset:3328\n\t"
                     "ds_read_b64_tr_b16 v[196:197], v239 offset:768\n\t" "ds_read_b64_tr_b16 v[198:199], v226 offset:3328\n\t" "ds_read_b64_tr_b16 v[220:221], v237 offset:1024\n\t"
                     "ds_read_b64_tr_b16 v[222:223], v238 offset:3584\n\t" "ds_read_b64_tr_b16 v[84:85], v239 offset:1024\n\t" "ds_read_b64_tr_b16 v[86:87], v226 offset:3584\n\t" "s_waitcnt vmcnt(10) lgkmcnt(0)\n\t"
                     "s_barrier\n\t" "s_add_u32 s42, %21, 0x0\n\t" "v_xor_b32 v237, 0x0, v229\n\t" "v_add_u32 v237, s42, v237\n\t" "v_xor_b32 v238, 0x40, v229\n\t" "v_add_u32 v238, s42, v238\n\t"
                     "v_xor_b32 v239, 0x80, v229\n\t" "v_add_u32 v239, s42, v239\n\t" "v_xor_b32 v226, 0xc0, v229\n\t" "v_add_u32 v226, s42, v226\n\t" "ds_read_b64_tr_b16 a[192:193], v237\n\t"
                     "ds_read_b64_tr_b16 a[194:195], v238 offset:2560\n\t" "ds_read_b64_tr_b16 a[216:217], v239\n\t" "ds_read_b64_tr_b16 a[218:219], v226 offset:2560\n\t"
                     "ds_read_b64_tr_b16 a[240:241], v237 offset:256\n\t" "ds_read_b64_tr_b16 a[242:243], v238 offset:2816\n\t" "ds_read_b64_tr_b16 v[104:105], v239 offset:256\n\t"
                     "ds_read_b64_tr_b16 v[106:107], v226 offset:2816\n\t" "ds_read_b64_tr_b16 v[128:129], v237 offset:512\n\t" "ds_read_b64_tr_b16 v[130:131], v238 offset:3072\n\t"
                     "ds_read_b64_tr_b16 v[152:153], v239 offset:512\n\t" "ds_read_b64_tr_b16 v[154:155], v226 offset:3072\n\t" "ds_read_b64_tr_b16 v[176:177], v237 offset:768\n\t"
                     "ds_read_b64_tr_b16 v[178:179], v238 offset:3328\n\t" "ds_read_b64_tr_b16 v[200:201], v239 offset:768\n\t" "ds_read_b64_tr_b16 v[202:203], v226 offset:3328\n\t"
                     "ds_read_b64_tr_b16 v[64:65], v237 offset:1024\n\t" "ds_read_b64_tr_b16 v[66:67], v238 offset:3584\n\t" "ds_read_b64_tr_b16 v[88:89], v239 offset:1024\n\t"
                     "ds_read_b64_tr_b16 v[90:91], v226 offset:3584\n\t" "s_waitcnt vmcnt(0) lgkmcnt(0)\n\t" "s_barrier\n\t" "s_add_u32 s42, %21, 0xc200\n\t" "v_xor_b32 v237, 0x0, v229\n\t" "v_add_u32 v237, s42, v237\n\t"
                     "v_xor_b32 v238, 0x40, v229\n\t" "v_add_u32 v238, s42, v238\n\t" "v_xor_b32 v239, 0x80, v229\n\t" "v_add_u32 v239, s42, v239\n\t" "v_xor_b32 v226, 0xc0, v229\n\t" "v_add_u32 v226, s42, v226\n\t"
                     "ds_read_b64_tr_b16 a[196:197], v237\n\t" "ds_read_b64_tr_b16 a[198:199], v238 offset:2560\n\t" "ds_read_b64_tr_b16 a[220:221], v239\n\t" "ds_read_b64_tr_b16 a[222:223], v226 offset:2560\n\t"
                     "ds_read_b64_tr_b16 a[244:245], v237 offset:256\n\t" "ds_read_b64_tr_b16 a[246:247], v238 offset:2816\n\t" "ds_read_b64_tr_b16 v[108:109], v239 offset:256\n\t"
                     "ds_read_b64_tr_b16 v[110:111], v226 offset:2816\n\t" "ds_read_b64_tr_b16 v[132:133], v237 offset:512\n\t" "ds_read_b64_tr_b16 v[134:135], v238 offset:3072\n\t"
                     "ds_read_b64_tr_b16 v[156:157], v239 offset:512\n\t" "ds_read_b64_tr_b16 v[158:159], v226 offset:3072\n\t" "ds_read_b64_tr_b16 v[180:181], v237 offset:768\n\t"
                     "ds_read_b64_tr_b16 v[182:183], v238 offset:3328\n\t" "ds_read_b64_tr_b16 v[204:205], v239 offset:768\n\t" "ds_read_b64_tr_b16 v[206:207], v226 offset:3328\n\t"
                     "ds_read_b64_tr_b16 v[68:69], v237 offset:1024\n\t" "ds_read_b64_tr_b16 v[70:71], v238 offset:3584\n\t" "ds_read_b64_tr_b16 v[92:93], v239 offset:1024\n\t"
                     "ds_read_b64_tr_b16 v[94:95], v226 offset:3584\n\t" "s_waitcnt lgkmcnt(0)\n\t" "ds_write_b128 v230, v[0:3]\n\t" "ds_write_b128 v230, v[4:7] offset:1024\n\t"
                     "ds_write_b128 v230, v[8:11] offset:2048\n\t" "ds_write_b128 v230, v[12:15] offset:3072\n\t" "ds_write_b128 v230, v[16:19] offset:4096\n\t" "ds_write_b128 v230, v[20:23] offset:5120\n\t"
                     "ds_write_b128 v230, v[24:27] offset:6144\n\t" "ds_write_b128 v230, v[28:31] offset:7168\n\t" "ds_write_b128 v230, v[32:35] offset:8192\n\t" "ds_write_b128 v230, v[36:39] offset:9216\n\t"
                     "ds_write_b128 v230, v[40:43] offset:10240\n\t" "ds_write_b128 v230, v[44:47] offset:11264\n\t" "ds_write_b128 v230, v[48:51] offset:12288\n\t" "ds_write_b128 v230, v[52:55] offset:13312\n\t"
                     "ds_write_b128 v230, v[56:59] offset:14336\n\t" "ds_write_b128 v230, v[60:63] offset:15360\n\t" "ds_write_b128 v230, v[64:67] offset:16384\n\t" "ds_write_b128 v230, v[68:71] offset:17408\n\t"
                     "ds_write_b128 v230, v[72:75] offset:18432\n\t" "ds_write_b128 v230, v[76:79] offset:19456\n\t" "ds_write_b128 v230, v[80:83] offset:20480\n\t" "ds_write_b128 v230, v[84:87] offset:21504\n\t"
                     "ds_write_b128 v230, v[88:91] offset:22528\n\t" "ds_write_b128 v230, v[92:95] offset:23552\n\t"
                     : "=&{a[0:31]}"(wa[0]), "=&{a[32:63]}"(wa[1]), "=&{a[64:95]}"(wa[2]), "=&{a[96:127]}"(wa[3]), "=&{a[128:159]}"(wa[4]), "=&{a[160:191]}"(wa[5]), "=&{a[192:223]}"(wa[6]), "=&{a[224:255]}"(wa[7]), "=&{v[96:127]}"(wv[0]), "=&{v[128:159]}"(wv[1]), "=&{v[160:191]}"(wv[2]), "=&{v[192:223]}"(wv[3]), "=&v"(y.r), "=&v"(y.g), "=&v"(y.e), "+{v[224:239]}"(pa)
                     : "{s[44:45]}"(p.wv + h * DH * 2 + (size_t)(16 * w) * (HD * 2)), "{s[46:47]}"(p.wo + (size_t)(h * DH + 8 * w) * 1280),
                       "s"(lds + 2 * w * GS), "s"(p.ws + OFF_LAT), "s"(lds + w * 10240), "s"(lds), "s"((h & 4) * 16 * HD * 2)
                     : "v0", "v1", "v2", "v3", "v4", "v5", "v6", "v7", "v8", "v9", "v10", "v11", "v12", "v13", "v14", "v15", "v16", "v17", "v18", "v19", "v20", "v21", "v22", "v23", "v24", "v25", "v26", "v27", "v28", "v29", "v30", "v31", "v32", "v33", "v34", "v35", "v36", "v37", "v38", "v39", "v40", "v41", "v42", "v43", "v44", "v45", "v46", "v47", "v48", "v49", "v50", "v51", "v52", "v53", "v54", "v55", "v56", "v57", "v58", "v59", "v60", "v61", "v62", "v63", "v64", "v65", "v66", "v67", "v68", "v69", "v70", "v71", "v72", "v73", "v74", "v75", "v76", "v77", "v78", "v79", "v80", "v81", "v82", "v83", "v84", "v85", "v86", "v87", "v88", "v89", "v90", "v91", "v92", "v93", "v94", "v95", "s42", "s43", "scc", "memory");
        sd = sync_end(y, p.ws, phi, w == 0);
        barrier();
    }
    const int lane = lanex(), tid = 64 * w + lane;
    const int n = lane & 15, g = lane >> 4, qq = (lane >> 2) & 3, pp = lane & 3;
    char* const fl = p.ws + OFF_ST;
    const u32 iw = lds + 65536 + ((lane * 8) ^ (lane & 32));
    const u32 qr0 = lds + 65536 + (g >> 1) * 512 + pp * 128 + 64 * (g & 1) + 8 * qq + 32 * (pp >> 1);
    const u32 uo = (u32)((4 * w + (n >> 3)) * 4096 + h * 128 + (n & 7) * 16);
    u32x16 ad = u32x16{(u32)lane * 16 + 1024 + (h & 4) * 256, (lane & 2) ? 0xEEEEu : 0x4444u, lds + (u32)((w & 1) * 24576 + (w >> 1) * 99328 + lane * 16), iw + ((3 * w + (w & 1)) >> 1) * 512,
                       iw + (1 + 3 * (w >> 1)) * 512 + 4 * (w & 1), qr0, qr0 ^ 32, uo + (g & 1) * 8192 + (1 + (g >> 1)) * 65536, uo + (g & 1) * 8192 + (g >> 1) * 8, uo, (u32)lane * 16 + 1024 - (h & 4) * 256, uo, uo, uo, uo, uo};
    asm volatile("" : "+{v[224:239]}"(ad));
    const char* memp = p.ws + mslot(sd, 0);
    int budget = 30000;
    Clock clk; clk.init();
    for (int s = 0; s < NSEG; ++s) {
        int tries = 0;
        u64 bad, tp8, en = ~0ull;
        while (true) {
            const u32 tgt = clk.sched(phi, 0, cfg.G, tries ? 2 : 0);
            char* up = p.ws + uslot(sd, s);
            asm volatile("ds_read_b128 v[64:67], v226\n\t" "ds_read_b128 v[68:71], v226 offset:1024\n\t" "ds_read_b128 v[72:75], v226 offset:2048\n\t" "ds_read_b128 v[76:79], v226 offset:3072\n\t"
                         "ds_read_b128 v[80:83], v226 offset:4096\n\t" "ds_read_b128 v[84:87], v226 offset:5120\n\t" "ds_read_b128 v[88:91], v226 offset:6144\n\t" "ds_read_b128 v[92:95], v226 offset:7168\n\t"
                         "v_mov_b64 v[40:41], 0\n\t" "v_mov_b64 v[42:43], 0\n\t" "v_mov_b64 v[44:45], 0\n\t" "v_mov_b64 v[46:47], 0\n\t" "v_mov_b64 v[48:49], 0\n\t" "v_mov_b64 v[50:51], 0\n\t" ".Lsp_u:\n\t" "s_memtime vcc\n\t"
                         "s_waitcnt lgkmcnt(0)\n\t" "s_sub_i32 s42, %3, vcc_lo\n\t" "s_cmp_gt_i32 s42, 80\n\t" "s_cbranch_scc1 .Lsp_u\n\t" "s_max_i32 s42, s42, 0\n\t" "s_sub_i32 s42, 92, s42\n\t" "s_and_b32 s42, s42, -4\n\t"
                         "s_getpc_b64 vcc\n\t" "s_add_u32 vcc_lo, vcc_lo, s42\n\t" "s_addc_u32 vcc_hi, vcc_hi, 0\n\t" "s_setpc_b64 vcc\n\t" "s_nop 0\n\t" "s_nop 0\n\t" "s_nop 0\n\t" "s_nop 0\n\t" "s_nop 0\n\t" "s_nop 0\n\t"
                         "s_nop 0\n\t" "s_nop 0\n\t" "s_nop 0\n\t" "s_nop 0\n\t" "s_nop 0\n\t" "s_nop 0\n\t" "s_nop 0\n\t" "s_nop 0\n\t" "s_nop 0\n\t" "s_nop 0\n\t" "s_nop 0\n\t" "s_nop 0\n\t" "s_nop 0\n\t" "s_nop 0\n\t"
                         "global_load_dwordx4 v[0:3], v224, %4 offset:-4096 sc1\n\t" "s_nop 10\n\t" "global_load_dwordx4 v[4:7], v224, %4 offset:-2048 sc1\n\t" "s_nop 15\n\t"
                         "global_load_dwordx4 v[8:11], v234, %4 offset:-3072 sc1\n\t" "s_nop 10\n\t" "global_load_dwordx4 v[12:15], v234, %4 offset:-1024 sc1\n\t" "s_nop 10\n\t" "global_load_dwordx4 v[16:19], v224, %4 sc1\n\t"
                         "s_nop 10\n\t" "global_load_dwordx4 v[20:23], v234, %4 offset:1024 sc1\n\t" "s_nop 10\n\t" "global_load_dwordx4 v[24:27], v224, %4 offset:2048 sc1\n\t" "s_nop 10\n\t"
                         "global_load_dwordx4 v[28:31], v234, %4 offset:3072 sc1\n\t" "s_nop 10\n\t" "global_load_dwordx4 v[32:35], v224, %5 offset:-4096 sc1\n\t" "s_nop 10\n\t"
                         "global_load_dwordx4 v[36:39], v234, %5 offset:-3072 sc1\n\t" "s_waitcnt vmcnt(9)\n\t" "v_smfmac_f32_16x16x64_bf16 v[40:43], v[0:3], a[0:7], v225\n\t"
                         "v_smfmac_f32_16x16x64_bf16 v[44:47], v[0:3], v[64:71], v225\n\t" "ds_read_b128 v[64:67], v226 offset:8192\n\t" "v_smfmac_f32_16x16x64_bf16 v[48:51], v[0:3], a[8:15], v225\n\t"
                         "ds_read_b128 v[68:71], v226 offset:9216\n\t" "v_cmp_eq_u32 vcc, -1, v0\n\t" "s_waitcnt vmcnt(8)\n\t" "v_smfmac_f32_16x16x64_bf16 v[40:43], v[4:7], a[16:23], v225\n\t" "v_mov_b64 v[0:1], 0\n\t"
                         "v_smfmac_f32_16x16x64_bf16 v[44:47], v[4:7], v[72:79], v225\n\t" "s_and_b64 vcc, vcc, %6\n\t" "s_cbranch_scc1 .Lsk_u\n\t" "ds_read_b128 v[72:75], v226 offset:10240\n\t" "v_mov_b64 v[2:3], 0\n\t"
                         "v_smfmac_f32_16x16x64_bf16 v[48:51], v[4:7], a[24:31], v225\n\t" "ds_read_b128 v[76:79], v226 offset:11264\n\t" "s_waitcnt vmcnt(7)\n\t"
                         "v_smfmac_f32_16x16x64_bf16 v[40:43], v[8:11], a[32:39], v225\n\t" "v_mov_b64 v[4:5], 0\n\t" "v_smfmac_f32_16x16x64_bf16 v[44:47], v[8:11], v[80:87], v225\n\t"
                         "ds_read_b128 v[80:83], v226 offset:12288\n\t" "v_mov_b64 v[6:7], 0\n\t" "v_smfmac_f32_16x16x64_bf16 v[48:51], v[8:11], a[40:47], v225\n\t" "ds_read_b128 v[84:87], v226 offset:13312\n\t"
                         "s_waitcnt vmcnt(6)\n\t" "v_smfmac_f32_16x16x64_bf16 v[40:43], v[12:15], a[48:55], v225\n\t" "v_mov_b64 v[8:9], 0\n\t" "v_smfmac_f32_16x16x64_bf16 v[44:47], v[12:15], v[88:95], v225\n\t"
                         "ds_read_b128 v[88:91], v226 offset:14336\n\t" "v_mov_b64 v[10:11], 0\n\t" "v_smfmac_f32_16x16x64_bf16 v[48:51], v[12:15], a[56:63], v225\n\t" "ds_read_b128 v[92:95], v226 offset:15360\n\t"
                         "s_waitcnt vmcnt(5)\n\t" "v_smfmac_f32_16x16x64_bf16 v[40:43], v[16:19], a[64:71], v225\n\t" "v_mov_b64 v[12:13], 0\n\t" "s_waitcnt lgkmcnt(6)\n\t"
                         "v_smfmac_f32_16x16x64_bf16 v[44:47], v[16:19], v[64:71], v225\n\t" "ds_read_b128 v[64:67], v226 offset:16384\n\t" "v_mov_b64 v[14:15], 0\n\t"
                         "v_smfmac_f32_16x16x64_bf16 v[48:51], v[16:19], a[72:79], v225\n\t" "ds_read_b128 v[68:71], v226 offset:17408\n\t" "s_waitcnt vmcnt(4)\n\t"
                         "v_smfmac_f32_16x16x64_bf16 v[40:43], v[20:23], a[80:87], v225\n\t" "v_mov_b64 v[16:17], 0\n\t" "s_waitcnt lgkmcnt(6)\n\t" "v_smfmac_f32_16x16x64_bf16 v[44:47], v[20:23], v[72:79], v225\n\t"
                         "ds_read_b128 v[72:75], v226 offset:18432\n\t" "v_mov_b64 v[18:19], 0\n\t" "v_smfmac_f32_16x16x64_bf16 v[48:51], v[20:23], a[88:95], v225\n\t" "ds_read_b128 v[76:79], v226 offset:19456\n\t"
                         "s_waitcnt vmcnt(3)\n\t" "v_smfmac_f32_16x16x64_bf16 v[40:43], v[24:27], a[96:103], v225\n\t" "v_mov_b64 v[20:21], 0\n\t" "s_waitcnt lgkmcnt(6)\n\t"
                         "v_smfmac_f32_16x16x64_bf16 v[44:47], v[24:27], v[80:87], v225\n\t" "ds_read_b128 v[80:83], v226 offset:20480\n\t" "v_mov_b64 v[22:23], 0\n\t"
                         "v_smfmac_f32_16x16x64_bf16 v[48:51], v[24:27], a[104:111], v225\n\t" "ds_read_b128 v[84:87], v226 offset:21504\n\t" "s_waitcnt vmcnt(2)\n\t"
                         "v_smfmac_f32_16x16x64_bf16 v[40:43], v[28:31], a[112:119], v225\n\t" "v_mov_b64 v[24:25], 0\n\t" "s_waitcnt lgkmcnt(6)\n\t" "v_smfmac_f32_16x16x64_bf16 v[44:47], v[28:31], v[88:95], v225\n\t"
                         "ds_read_b128 v[88:91], v226 offset:22528\n\t" "v_mov_b64 v[26:27], 0\n\t" "v_smfmac_f32_16x16x64_bf16 v[48:51], v[28:31], a[120:127], v225\n\t" "ds_read_b128 v[92:95], v226 offset:23552\n\t"
                         "s_waitcnt vmcnt(1)\n\t" "v_smfmac_f32_16x16x64_bf16 v[40:43], v[32:35], a[128:135], v225\n\t" "v_mov_b64 v[28:29], 0\n\t" "v_smfmac_f32_16x16x64_bf16 v[44:47], v[32:35], a[136:143], v225\n\t"
                         "v_mov_b64 v[30:31], 0\n\t" "v_smfmac_f32_16x16x64_bf16 v[48:51], v[32:35], a[144:151], v225\n\t" "s_waitcnt vmcnt(0)\n\t" "v_smfmac_f32_16x16x64_bf16 v[40:43], v[36:39], a[152:159], v225\n\t"
                         "v_mov_b64 v[32:33], 0\n\t" "v_smfmac_f32_16x16x64_bf16 v[44:47], v[36:39], a[160:167], v225\n\t" "v_mov_b64 v[34:35], 0\n\t" "v_smfmac_f32_16x16x64_bf16 v[48:51], v[36:39], a[168:175], v225\n\t"
                         "v_add_f32 v52, v40, v42\n\t" "v_add_f32 v53, v41, v43\n\t" "v_cmp_u_f32 vcc, v52, v53\n\t" "v_add_f32 v54, v44, v46\n\t" "v_add_f32 v55, v45, v47\n\t" "v_cvt_pk_bf16_f32 v58, v52, v53\n\t"
                         "v_pk_add_f32 v[56:57], v[48:49], v[50:51]\n\t" "v_cvt_pk_bf16_f32 v59, v54, v55\n\t" "v_cvt_pk_bf16_f32 v60, v56, v57\n\t" "ds_write_b64 v227, v[58:59]\n\t" "ds_write_b32 v228, v60\n\t"
                         "v_mov_b64 v[36:37], 0\n\t" "v_mov_b64 v[38:39], 0\n\t" "s_and_b64 vcc, vcc, %6\n\t" "v_mov_b32 v233, 0\n\t" "s_cbranch_scc1 .Lsk_u\n\t" "v_mov_b32 v233, 0\n\t" "v_mov_b32 v233, 0\n\t"
                         "v_mov_b32 v233, 0\n\t" "s_waitcnt lgkmcnt(0)\n\t" "s_barrier\n\t" "ds_read_b64_tr_b16 v[40:41], v229\n\t" "ds_read_b64_tr_b16 v[42:43], v230\n\t" "v_mov_b32 v233, 0\n\t"
                         "ds_read_b64_tr_b16 v[44:45], v229 offset:1024\n\t" "v_mov_b32 v233, 0\n\t" "ds_read_b64_tr_b16 v[46:47], v230 offset:1024\n\t" "v_mov_b32 v233, 0\n\t"
                         "ds_read_b64_tr_b16 v[48:49], v229 offset:2048\n\t" "v_mov_b32 v233, 0\n\t" "ds_read_b64_tr_b16 v[50:51], v230 offset:2048\n\t" "v_mov_b32 v233, 0\n\t" "s_waitcnt lgkmcnt(4)\n\t"
                         "v_smfmac_f32_16x16x64_bf16 v[0:3], v[40:43], a[176:183], v225\n\t" "v_smfmac_f32_16x16x64_bf16 v[4:7], v[40:43], a[200:207], v225\n\t" "s_waitcnt lgkmcnt(2)\n\t"
                         "v_smfmac_f32_16x16x64_bf16 v[0:3], v[44:47], a[184:191], v225\n\t" "v_smfmac_f32_16x16x64_bf16 v[4:7], v[44:47], a[208:215], v225\n\t" "s_waitcnt lgkmcnt(0)\n\t"
                         "v_smfmac_f32_16x16x64_bf16 v[0:3], v[48:51], a[192:199], v225\n\t" "v_smfmac_f32_16x16x64_bf16 v[4:7], v[48:51], a[216:223], v225\n\t"
                         "v_smfmac_f32_16x16x64_bf16 v[8:11], v[40:43], a[224:231], v225\n\t" "v_add_f32 v52, v0, v2\n\t" "v_add_f32 v53, v1, v3\n\t" "v_cvt_pk_bf16_f32 v54, v53, v52\n\t"
                         "v_smfmac_f32_16x16x64_bf16 v[8:11], v[44:47], a[232:239], v225\n\t" "v_add_f32 v52, v4, v6\n\t" "v_add_f32 v53, v5, v7\n\t" "v_cvt_pk_bf16_f32 v55, v53, v52\n\t" "s_nop 1\n\t"
                         "v_permlane16_swap_b32 v54, v55\n\t" "v_smfmac_f32_16x16x64_bf16 v[8:11], v[48:51], a[240:247], v225\n\t" "global_store_dwordx2 v232, v[54:55], %2 sc1\n\t" "s_add_u32 s40, s40, 0x10000\n\t"
                         "s_addc_u32 s41, s41, 0\n\t" "v_smfmac_f32_16x16x64_bf16 v[12:15], v[40:43], a[248:255], v225\n\t" "v_smfmac_f32_16x16x64_bf16 v[12:15], v[44:47], v[96:103], v225\n\t" "v_add_f32 v52, v8, v10\n\t"
                         "v_smfmac_f32_16x16x64_bf16 v[12:15], v[48:51], v[104:111], v225\n\t" "v_add_f32 v53, v9, v11\n\t" "v_smfmac_f32_16x16x64_bf16 v[16:19], v[40:43], v[112:119], v225\n\t"
                         "v_cvt_pk_bf16_f32 v56, v53, v52\n\t" "v_smfmac_f32_16x16x64_bf16 v[16:19], v[44:47], v[120:127], v225\n\t" "v_add_f32 v52, v12, v14\n\t" "v_add_f32 v53, v13, v15\n\t"
                         "v_cvt_pk_bf16_f32 v57, v53, v52\n\t" "s_nop 1\n\t" "v_permlane16_swap_b32 v56, v57\n\t" "v_smfmac_f32_16x16x64_bf16 v[16:19], v[48:51], v[128:135], v225\n\t"
                         "global_store_dwordx2 v232, v[56:57], %2 sc1\n\t" "s_add_u32 s40, s40, 0x10000\n\t" "s_addc_u32 s41, s41, 0\n\t" "v_smfmac_f32_16x16x64_bf16 v[20:23], v[40:43], v[136:143], v225\n\t"
                         "v_smfmac_f32_16x16x64_bf16 v[20:23], v[44:47], v[144:151], v225\n\t" "v_add_f32 v52, v16, v18\n\t" "v_smfmac_f32_16x16x64_bf16 v[20:23], v[48:51], v[152:159], v225\n\t" "v_add_f32 v53, v17, v19\n\t"
                         "v_smfmac_f32_16x16x64_bf16 v[24:27], v[40:43], v[160:167], v225\n\t" "v_cvt_pk_bf16_f32 v58, v53, v52\n\t" "v_smfmac_f32_16x16x64_bf16 v[24:27], v[44:47], v[168:175], v225\n\t"
                         "v_add_f32 v52, v20, v22\n\t" "v_add_f32 v53, v21, v23\n\t" "v_smfmac_f32_16x16x64_bf16 v[24:27], v[48:51], v[176:183], v225\n\t" "v_cvt_pk_bf16_f32 v59, v53, v52\n\t"
                         "v_smfmac_f32_16x16x64_bf16 v[28:31], v[40:43], v[184:191], v225\n\t" "s_nop 0\n\t" "v_permlane16_swap_b32 v58, v59\n\t" "v_smfmac_f32_16x16x64_bf16 v[28:31], v[44:47], v[192:199], v225\n\t"
                         "global_store_dwordx2 v232, v[58:59], %2 sc1\n\t" "v_add_f32 v52, v24, v26\n\t" "v_smfmac_f32_16x16x64_bf16 v[28:31], v[48:51], v[200:207], v225\n\t" "v_add_f32 v53, v25, v27\n\t"
                         "v_smfmac_f32_16x16x64_bf16 v[32:35], v[40:43], v[208:215], v225\n\t" "v_cvt_pk_bf16_f32 v60, v53, v52\n\t" "v_smfmac_f32_16x16x64_bf16 v[32:35], v[44:47], v[216:223], v225\n\t"
                         "v_add_f32 v52, v28, v30\n\t" "v_smfmac_f32_16x16x64_bf16 v[32:35], v[48:51], v[64:71], v225\n\t" "v_add_f32 v53, v29, v31\n\t" "v_smfmac_f32_16x16x64_bf16 v[36:39], v[40:43], v[72:79], v225\n\t"
                         "v_cvt_pk_bf16_f32 v61, v53, v52\n\t" "v_smfmac_f32_16x16x64_bf16 v[36:39], v[44:47], v[80:87], v225\n\t" "v_add_f32 v52, v32, v34\n\t"
                         "v_smfmac_f32_16x16x64_bf16 v[36:39], v[48:51], v[88:95], v225\n\t" "v_add_f32 v53, v33, v35\n\t" "v_cvt_pk_bf16_f32 v62, v53, v52\n\t" "v_mov_b32 v233, 0\n\t" "v_mov_b32 v233, 0\n\t"
                         "v_mov_b32 v233, 0\n\t" "v_mov_b32 v233, 0\n\t" "v_mov_b32 v233, 0\n\t" "v_pk_add_f32 v[52:53], v[36:37], v[38:39]\n\t" "v_cvt_pk_bf16_f32 v63, v53, v52\n\t" "v_permlane32_swap_b32 v60, v62\n\t"
                         "s_nop 0\n\t" "v_permlane32_swap_b32 v61, v63\n\t" "s_nop 1\n\t" "v_permlane16_swap_b32 v60, v61\n\t" "v_permlane16_swap_b32 v62, v63\n\t" "global_store_dwordx4 v231, v[60:63], %2 sc1\n\t"
                         ".Lsk_u:\n\t" "s_memrealtime %1\n\t" "s_mov_b64 %0, vcc\n\t" "s_waitcnt lgkmcnt(0)\n\t"
                         : "=&s"(bad), "=&s"(tp8), "+{s[40:41]}"(up)
                         : "s"(uni((int)tgt)), "s"(memp - 1024), "s"(memp + 15360), "s"(en), "{v[224:239]}"(ad),
                           "{a[0:31]}"(wa[0]), "{a[32:63]}"(wa[1]), "{a[64:95]}"(wa[2]), "{a[96:127]}"(wa[3]), "{a[128:159]}"(wa[4]), "{a[160:191]}"(wa[5]), "{a[192:223]}"(wa[6]), "{a[224:255]}"(wa[7]), "{v[96:127]}"(wv[0]), "{v[128:159]}"(wv[1]), "{v[160:191]}"(wv[2]), "{v[192:223]}"(wv[3])
                         : "v0", "v1", "v2", "v3", "v4", "v5", "v6", "v7", "v8", "v9", "v10", "v11", "v12", "v13", "v14", "v15", "v16", "v17", "v18", "v19", "v20", "v21", "v22", "v23", "v24", "v25", "v26", "v27", "v28", "v29", "v30", "v31", "v32", "v33", "v34", "v35", "v36", "v37", "v38", "v39", "v40", "v41", "v42", "v43", "v44", "v45", "v46", "v47", "v48", "v49", "v50", "v51", "v52", "v53", "v54", "v55", "v56", "v57", "v58", "v59", "v60", "v61", "v62", "v63", "v64", "v65", "v66", "v67", "v68", "v69", "v70", "v71", "v72", "v73", "v74", "v75", "v76", "v77", "v78", "v79", "v80", "v81", "v82", "v83", "v84", "v85", "v86", "v87", "v88", "v89", "v90", "v91", "v92", "v93", "v94", "v95", "vcc", "scc", "s42", "memory");
            if (__builtin_expect(bad == 0, 1)) break;
            waitvm<0>();
            if (--budget < 0 || ((++tries & 15) == 0 && !unwritten(memp, lanex() * 16))) en = 0;
            phi += (u32)cfg.G;
        }
        clk.calib();
        const u32 tt = sched_ld0(s);
        if (s >= NSEG - 2) waitvm<0>();
        barrier();
        if (s >= NSEG - 2 && w == 3) flag_add(fl, F_PROG + 8 * s + (h & 7), 1);
        if (s >= NSEG - 2 && w == 2) flag_add(fl, F_UD + 2 * (s & 1) + (h & 1), 1);
        follow(phi, (int)(tt >> 16) + 4 + cfg.T3 - VISS, (u32)tp8, cfg.G);
        phi += tt & 0xffffu;
        memp = p.ws + mslot(sd, s + 1);
    }
    asm volatile(".Lkm_lu:\n\ts_waitcnt vmcnt(0)" : : : "memory");
}

#define VALL "{v[0:3]}"(o), "{v[16:47]}"(rv[0]), "{v[48:79]}"(rv[1]), "{v[80:111]}"(rv[2]), "{v[112:143]}"(rv[3]), "{v[144:175]}"(rv[4]), "{v[176:207]}"(rv[5]), "{v[208:239]}"(rv[6]), "{a[0:31]}"(ra[0]), "{a[32:63]}"(ra[1]), "{a[64:95]}"(ra[2]), "{a[96:127]}"(ra[3])
DI void value_role(const Ptrs& p, const ChainCfg cfg, int vb, int sd, u32 phi, u32 lds, int w) {
    const int lane = lanex(), tid = 64 * w + lane, n = lane & 15, g = lane >> 4;
    const int v = vb;
    const u32 moff = lane * 16;
    char* const fl = p.ws + OFF_ST;
    char* const ub = p.ws + uchunk(v);
    const int xh = (vb >> 2) & 1;
    if (w == 3) {
        const u32x4 ff = u32x4{~0u, ~0u, ~0u, ~0u};
        if (SPL) barrier();
#pragma nounroll
        for (int s = 0; s < NSEG - 2; ++s) {
            dly(cfg.cR3 >> 8 & 255);
            char* const mk = ub + uslot(sd, s + 2);
#pragma unroll
            for (int i = 0; i < 8; ++i) { gst16(mk + i * 1024, moff, ff); gap<HQ>(-1); }
            if (vb < 32 && s) {
                gst16(p.ws + (size_t)(s + 2) * HO_STRIDE + vb * 2048, moff, ff);
                gst16(p.ws + (size_t)(s + 2) * HO_STRIDE + vb * 2048 + 1024, moff, ff);
            }
            waitvm<0>();
            if (SPL && s) barrier();
            barrier();
        }
        return;
    }
    if (w) {
        const u32 dof = (u32)((n >> 3) * 16384 + g * 128 + (n & 7) * 16);
        const char* dp = p.ws + OFF_D + (size_t)v * 32768 - 4096 + w * 8192;
        if (SPL) barrier();
        flag_wait(fl, F_VD + (vb & 3) + (v < 20 ? 0 : 4), 32);
#pragma nounroll
        for (int s = 0; s < NSEG - 1; ++s) {
            u32x4 df[16];
            u32 dfl;
            const int on = uni(1 - s);
            sfor<0, SPL>([&](auto I) { gld16c<I.v * 512 - 4096>(df[I.v], dp, dof); gap<HP0>(on); });
            if (SPL) asm volatile("s_cmp_ge_i32 %0, 0\n\ts_cbranch_scc1 .Lhb\n\ts_barrier\n.Lhb:" : : "s"(on) : "memory", "scc");
            dly(cfg.cR3 & 255);
            sfor<SPL, 16>([&](auto I) { gld16c<I.v * 512 - 4096>(df[I.v], dp, dof); gap<HP>(on); });
            dp += D_STRIDE;
            flag_ldm(dfl, fl, F_VD + pblk(s + 1) * 8 + (vb & RM), pblk(s + 1) != pblk(s));
            const u32 la = lds + moff + (s & 1) * 32768 + (w - 1) * 16384;
            sfor<0, 8>([&](auto I) {
                constexpr int i = I.v;
                waitv<15 - 2 * i>(df[2 * i]); waitv<15 - 2 * i>(df[2 * i + 1]);
                const u32x4 x = df[2 * i], y = df[2 * i + 1];
                const u32 lb = i & 2 ? la - xh * 4096 : la + xh * 4096;
                *LDSP(u32x4, lb + 2048 * i) = u32x4{x[0], y[0], x[2], y[2]};
                *LDSP(u32x4, lb + 2048 * i + 1024) = u32x4{x[1], y[1], x[3], y[3]};
                PIN();
            });
            waitvm<0>();
            asm volatile("" : "+v"(dfl));
            barrier();
            if (uni((int)dfl) < 31) flag_wait(fl, F_VD + pblk(s + 1) * 8 + (vb & RM), 32);
        }
        return;
    }
    u32x32 rv[7], ra[4];
    u32x4 o;
    asm volatile("v_accvgpr_write_b32 a64, 0\n\t" "v_accvgpr_write_b32 a65, 0\n\t" "v_accvgpr_write_b32 a66, 0\n\t" "v_accvgpr_write_b32 a67, 0\n\t" "v_accvgpr_write_b32 a68, 0\n\t" "v_accvgpr_write_b32 a69, 0\n\t"
                 "v_accvgpr_write_b32 a70, 0\n\t" "v_accvgpr_write_b32 a71, 0\n\t" "v_accvgpr_write_b32 a72, 0\n\t" "v_accvgpr_write_b32 a73, 0\n\t" "v_accvgpr_write_b32 a74, 0\n\t" "v_accvgpr_write_b32 a75, 0\n\t"
                 "v_accvgpr_write_b32 a76, 0\n\t" "v_accvgpr_write_b32 a77, 0\n\t" "v_accvgpr_write_b32 a78, 0\n\t" "v_accvgpr_write_b32 a79, 0\n\t" "v_mov_b64 v[112:113], -1\n\t" "v_mov_b64 v[120:121], -1\n\t"
                 "v_mov_b64 v[128:129], -1\n\t" "v_mov_b64 v[136:137], -1\n\t" "v_mov_b64 v[144:145], -1\n\t" "v_mov_b64 v[152:153], -1\n\t" "v_mov_b64 v[160:161], -1\n\t" "v_mov_b64 v[168:169], -1\n\t"
                 "v_mov_b64 v[176:177], -1\n\t" "v_mov_b64 v[184:185], -1\n\t" "v_mov_b64 v[192:193], -1\n\t" "v_mov_b64 v[200:201], -1\n\t" "v_mov_b64 v[208:209], -1\n\t" "v_mov_b64 v[216:217], -1\n\t"
                 "v_mov_b64 v[224:225], -1\n\t" "v_mov_b64 v[232:233], -1\n\t"
                 : "={v[0:3]}"(o), "={v[16:47]}"(rv[0]), "={v[48:79]}"(rv[1]), "={v[80:111]}"(rv[2]), "={v[112:143]}"(rv[3]), "={v[144:175]}"(rv[4]), "={v[176:207]}"(rv[5]), "={v[208:239]}"(rv[6]), "={a[0:31]}"(ra[0]), "={a[32:63]}"(ra[1]), "={a[64:95]}"(ra[2]), "={a[96:127]}"(ra[3]));
    const u32 ho = (u32)(min(lane, 39) * 256 + (lane >> 5) * 8192);
    const u32 pof = (u32)((n & 4) * 128 + (n >> 3) * 256 + g * 64 + (n & 3) * 16 + 2048);
    const u32 uof = (u32)((n >> 3) * 4096 + g * 128 + (n & 7) * 16 + 512);
    u32x8 ad = u32x8{uof + xh * 512, pof + xh * 2048, (u32)(((g >> 1) ^ xh) * 256 + (n >> 2) * 64 + (n & 1) * 16), moff,
                     (lane & 2) ? 0xEEEEu : 0x4444u, (lane & 4) ? 0xEEEEu : 0x4444u, (u32)(v * 256 + (v >> 5) * 8192 + g * 64 + (n >> 3) * 32 + (n & 1) * 16 + (n & 6) * 2),
                     (lane & 1) ? 0x07060302u : 0x01000504u};
    u32 pob = pof - xh * 2048, uob = uof - xh * 512, pmo = moff ^ xh * 512;
    asm volatile("" : "+{v[4:11]}"(ad), "+v"(pob), "+v"(uob), "+v"(pmo));
    auto zero = [&]() {
        asm volatile("v_mov_b64 v[0:1], 0\n\t" "v_mov_b64 v[2:3], 0\n\t" "v_mov_b64 v[48:49], 0\n\t" "v_mov_b64 v[50:51], 0\n\t" "v_mov_b64 v[52:53], 0\n\t" "v_mov_b64 v[54:55], 0\n\t" "v_mov_b64 v[56:57], 0\n\t"
                     "v_mov_b64 v[58:59], 0\n\t" "v_mov_b64 v[60:61], 0\n\t" "v_mov_b64 v[62:63], 0\n\t" "v_mov_b64 v[64:65], 0\n\t" "v_mov_b64 v[66:67], 0\n\t" "v_mov_b64 v[68:69], 0\n\t" "v_mov_b64 v[70:71], 0\n\t"
                     "v_mov_b64 v[72:73], 0\n\t" "v_mov_b64 v[74:75], 0\n\t" "v_mov_b64 v[76:77], 0\n\t" "v_mov_b64 v[78:79], 0\n\t" "v_mov_b64 v[80:81], 0\n\t" "v_mov_b64 v[82:83], 0\n\t" "v_mov_b64 v[84:85], 0\n\t"
                     "v_mov_b64 v[86:87], 0\n\t" "v_mov_b64 v[88:89], 0\n\t" "v_mov_b64 v[90:91], 0\n\t" "v_mov_b64 v[92:93], 0\n\t" "v_mov_b64 v[94:95], 0\n\t" "v_mov_b64 v[96:97], 0\n\t" "v_mov_b64 v[98:99], 0\n\t"
                     "v_mov_b64 v[100:101], 0\n\t" "v_mov_b64 v[102:103], 0\n\t" "v_mov_b64 v[104:105], 0\n\t" "v_mov_b64 v[106:107], 0\n\t" "v_mov_b64 v[108:109], 0\n\t" "v_mov_b64 v[110:111], 0\n\t"
                     : : VALL : "memory");
    };
    const char* memp = p.ws + OFF_XF + 4096;
    char* up = ub + OFF_XF;
    int budget = 30000;
    Clock clk; clk.init();
    u32 tt = 0;
    for (int s = -1; s < NSEG - 1; ++s) {
        const char* const mnx = p.ws + mslot(sd, s + 1);
        bool all = false;
        int tries = 0;
        u64 tp8;
        while (true) {
            const u32 tgt = clk.sched(phi, (int)(tt >> 16), cfg.G, tries ? 2 : s > 0) - (s < 0 ? 1u << 20 : 0u);
            u64 ok;
            asm volatile("s_cmp_lg_u32 %11, 0\n\t" "s_cbranch_scc0 .Lsp_v0\n\t" ".Lsp_v3:\n\t" "s_memtime vcc\n\t" "s_waitcnt lgkmcnt(0)\n\t" "s_sub_i32 s42, %10, vcc_lo\n\t" "s_cmp_gt_i32 s42, 80\n\t"
                         "s_cbranch_scc1 .Lsp_v3\n\t" "s_max_i32 s42, s42, 0\n\t" "s_sub_i32 s42, 92, s42\n\t" "s_and_b32 s42, s42, -4\n\t" "s_getpc_b64 vcc\n\t" "s_add_u32 vcc_lo, vcc_lo, s42\n\t"
                         "s_addc_u32 vcc_hi, vcc_hi, 0\n\t" "s_setpc_b64 vcc\n\t" "s_nop 0\n\t" "s_nop 0\n\t" "s_nop 0\n\t" "s_nop 0\n\t" "s_nop 0\n\t" "s_nop 0\n\t" "s_nop 0\n\t" "s_nop 0\n\t" "s_nop 0\n\t" "s_nop 0\n\t"
                         "s_nop 0\n\t" "s_nop 0\n\t" "s_nop 0\n\t" "s_nop 0\n\t" "s_nop 0\n\t" "s_nop 0\n\t" "s_nop 0\n\t" "s_nop 0\n\t" "s_nop 0\n\t" "s_nop 0\n\t"
                         "global_load_dwordx4 a[0:3], v5, %7 offset:-4096 sc1\n\t" "global_load_dwordx4 a[4:7], v5, %7 offset:-3072 sc1\n\t" "global_load_dwordx4 a[16:19], v5, %7 sc1\n\t"
                         "global_load_dwordx4 a[20:23], v5, %7 offset:1024 sc1\n\t" "global_load_dwordx4 a[32:35], v5, %8 offset:-4096 sc1\n\t" "global_load_dwordx4 a[36:39], v5, %8 offset:-3072 sc1\n\t"
                         "global_load_dwordx4 a[48:51], v5, %8 sc1\n\t" "global_load_dwordx4 a[52:55], v5, %8 offset:1024 sc1\n\t" "s_nop 14\n\t" "global_load_dwordx4 a[8:11], %12, %7 offset:-2048 sc1\n\t"
                         "global_load_dwordx4 a[12:15], %12, %7 offset:-1024 sc1\n\t" "global_load_dwordx4 a[24:27], %12, %7 offset:2048 sc1\n\t" "global_load_dwordx4 a[28:31], %12, %7 offset:3072 sc1\n\t"
                         "global_load_dwordx4 a[40:43], %12, %8 offset:-2048 sc1\n\t" "global_load_dwordx4 a[44:47], %12, %8 offset:-1024 sc1\n\t" "global_load_dwordx4 a[56:59], %12, %8 offset:2048 sc1\n\t"
                         "global_load_dwordx4 a[60:63], %12, %8 offset:3072 sc1\n\t" "s_nop 15\n\t" "s_nop 15\n\t" "s_nop 14\n\t" "global_load_dwordx4 a[80:83], %14, %6 sc1\n\t"
                         "global_load_dwordx4 a[84:87], %14, %6 offset:1024 sc1\n\t" "global_load_dwordx4 a[88:91], %14, %6 offset:2048 sc1\n\t" "global_load_dwordx4 a[92:95], %14, %6 offset:3072 sc1\n\t"
                         "s_mov_b64 exec, %9\n\t" "global_load_dwordx4 a[64:67], v6, %6 offset:-2048 sc1\n\t" "global_load_dwordx4 a[68:71], v6, %6 offset:-1536 sc1\n\t"
                         "global_load_dwordx4 a[72:75], v6, %6 offset:-1024 sc1\n\t" "global_load_dwordx4 a[76:79], v6, %6 offset:-512 sc1\n\t" "s_mov_b64 exec, -1\n\t" "s_mov_b32 s42, %11\n\t" ".Lsp_v5:\n\t"
                         "s_sub_u32 s42, s42, 1\n\t" "s_cmp_gt_i32 s42, 0\n\t" "s_cbranch_scc1 .Lsp_v5\n\t" "global_load_dwordx4 a[96:99], v4, %3 offset:-512 sc1\n\t" "global_load_dwordx4 a[100:103], %13, %3 sc1\n\t"
                         "global_load_dwordx4 a[104:107], v4, %3 offset:512 sc1\n\t" "global_load_dwordx4 a[108:111], %13, %3 offset:1024 sc1\n\t" "global_load_dwordx4 a[112:115], v4, %3 offset:1536 sc1\n\t"
                         "global_load_dwordx4 a[116:119], %13, %3 offset:2048 sc1\n\t" "global_load_dwordx4 a[120:123], v4, %3 offset:2560 sc1\n\t" "global_load_dwordx4 a[124:127], %13, %3 offset:3072 sc1\n\t"
                         "s_waitcnt vmcnt(31)\n\t" "v_smfmac_f32_16x16x64_bf16 v[48:51], a[0:3], v[112:119], v9\n\t" "s_waitcnt vmcnt(30)\n\t" "v_smfmac_f32_16x16x64_bf16 v[52:55], a[4:7], v[120:127], v9\n\t"
                         "s_waitcnt vmcnt(29)\n\t" "v_smfmac_f32_16x16x64_bf16 v[64:67], a[16:19], v[144:151], v9\n\t" "s_waitcnt vmcnt(28)\n\t" "v_cvt_pk_bf16_f32 v16, v48, v49\n\t" "v_cvt_pk_bf16_f32 v17, v50, v51\n\t"
                         "v_smfmac_f32_16x16x64_bf16 v[68:71], a[20:23], v[152:159], v9\n\t" "s_waitcnt vmcnt(27)\n\t" "v_cvt_pk_bf16_f32 v18, v52, v53\n\t" "v_cvt_pk_bf16_f32 v19, v54, v55\n\t"
                         "v_smfmac_f32_16x16x64_bf16 v[80:83], a[32:35], v[176:183], v9\n\t" "s_waitcnt vmcnt(26)\n\t" "v_cvt_pk_bf16_f32 v24, v64, v65\n\t" "v_cvt_pk_bf16_f32 v25, v66, v67\n\t"
                         "v_smfmac_f32_16x16x64_bf16 v[84:87], a[36:39], v[184:191], v9\n\t" "s_waitcnt vmcnt(25)\n\t" "v_cvt_pk_bf16_f32 v26, v68, v69\n\t" "v_cvt_pk_bf16_f32 v27, v70, v71\n\t"
                         "v_smfmac_f32_16x16x64_bf16 v[96:99], a[48:51], v[208:215], v9\n\t" "s_waitcnt vmcnt(24)\n\t" "v_cvt_pk_bf16_f32 v32, v80, v81\n\t" "v_cvt_pk_bf16_f32 v33, v82, v83\n\t"
                         "v_smfmac_f32_16x16x64_bf16 v[100:103], a[52:55], v[216:223], v9\n\t" "s_waitcnt vmcnt(23)\n\t" "v_cvt_pk_bf16_f32 v34, v84, v85\n\t" "v_cvt_pk_bf16_f32 v35, v86, v87\n\t"
                         "v_smfmac_f32_16x16x64_bf16 v[56:59], a[8:11], v[128:135], v9\n\t" "s_waitcnt vmcnt(22)\n\t" "v_cvt_pk_bf16_f32 v40, v96, v97\n\t" "v_cvt_pk_bf16_f32 v41, v98, v99\n\t"
                         "v_smfmac_f32_16x16x64_bf16 v[60:63], a[12:15], v[136:143], v9\n\t" "s_waitcnt vmcnt(21)\n\t" "v_cvt_pk_bf16_f32 v42, v100, v101\n\t" "v_cvt_pk_bf16_f32 v43, v102, v103\n\t"
                         "v_smfmac_f32_16x16x64_bf16 v[72:75], a[24:27], v[160:167], v9\n\t" "s_waitcnt vmcnt(20)\n\t" "v_cvt_pk_bf16_f32 v20, v56, v57\n\t" "v_cvt_pk_bf16_f32 v21, v58, v59\n\t"
                         "v_smfmac_f32_16x16x64_bf16 v[76:79], a[28:31], v[168:175], v9\n\t" "s_waitcnt vmcnt(19)\n\t" "v_cvt_pk_bf16_f32 v22, v60, v61\n\t" "v_cvt_pk_bf16_f32 v23, v62, v63\n\t"
                         "v_smfmac_f32_16x16x64_bf16 v[88:91], a[40:43], v[192:199], v9\n\t" "s_waitcnt vmcnt(18)\n\t" "v_cvt_pk_bf16_f32 v28, v72, v73\n\t" "v_cvt_pk_bf16_f32 v29, v74, v75\n\t"
                         "v_smfmac_f32_16x16x64_bf16 v[92:95], a[44:47], v[200:207], v9\n\t" "s_waitcnt vmcnt(17)\n\t" "v_cvt_pk_bf16_f32 v30, v76, v77\n\t" "v_cvt_pk_bf16_f32 v31, v78, v79\n\t"
                         "v_smfmac_f32_16x16x64_bf16 v[104:107], a[56:59], v[224:231], v9\n\t" "s_waitcnt vmcnt(16)\n\t" "v_cvt_pk_bf16_f32 v36, v88, v89\n\t" "v_cvt_pk_bf16_f32 v37, v90, v91\n\t"
                         "v_smfmac_f32_16x16x64_bf16 v[108:111], a[60:63], v[232:239], v9\n\t" "s_waitcnt vmcnt(11)\n\t" "v_cvt_pk_bf16_f32 v38, v92, v93\n\t" "v_cvt_pk_bf16_f32 v39, v94, v95\n\t"
                         "v_smfmac_f32_16x16x64_bf16 v[0:3], a[64:67], v[16:23], v8\n\t" "s_waitcnt vmcnt(10)\n\t" "v_cvt_pk_bf16_f32 v44, v104, v105\n\t" "v_cvt_pk_bf16_f32 v45, v106, v107\n\t"
                         "v_smfmac_f32_16x16x64_bf16 v[0:3], a[68:71], v[24:31], v8\n\t" "s_waitcnt vmcnt(9)\n\t" "v_cvt_pk_bf16_f32 v46, v108, v109\n\t" "v_cvt_pk_bf16_f32 v47, v110, v111\n\t"
                         "v_smfmac_f32_16x16x64_bf16 v[0:3], a[72:75], v[32:39], v8\n\t" "s_waitcnt vmcnt(8)\n\t" "v_smfmac_f32_16x16x64_bf16 v[0:3], a[76:79], v[40:47], v8\n\t" "s_waitcnt vmcnt(6)\n\t"
                         "v_smfmac_f32_16x16x64_bf16 v[0:3], a[80:83], a[96:103], v8\n\t" "s_waitcnt vmcnt(4)\n\t" "v_smfmac_f32_16x16x64_bf16 v[0:3], a[84:87], a[104:111], v8\n\t" "s_waitcnt vmcnt(2)\n\t"
                         "v_smfmac_f32_16x16x64_bf16 v[0:3], a[88:91], a[112:119], v8\n\t" "s_waitcnt vmcnt(0)\n\t" "v_smfmac_f32_16x16x64_bf16 v[0:3], a[92:95], a[120:127], v8\n\t" "s_branch .Lv_end\n\t" ".Lsp_v0:\n\t"
                         "s_memtime vcc\n\t" "s_waitcnt lgkmcnt(0)\n\t" "s_sub_i32 s42, %10, vcc_lo\n\t" "s_cmp_gt_i32 s42, 80\n\t" "s_cbranch_scc1 .Lsp_v0\n\t" "s_max_i32 s42, s42, 0\n\t" "s_sub_i32 s42, 92, s42\n\t"
                         "s_and_b32 s42, s42, -4\n\t" "s_getpc_b64 vcc\n\t" "s_add_u32 vcc_lo, vcc_lo, s42\n\t" "s_addc_u32 vcc_hi, vcc_hi, 0\n\t" "s_setpc_b64 vcc\n\t" "s_nop 0\n\t" "s_nop 0\n\t" "s_nop 0\n\t" "s_nop 0\n\t"
                         "s_nop 0\n\t" "s_nop 0\n\t" "s_nop 0\n\t" "s_nop 0\n\t" "s_nop 0\n\t" "s_nop 0\n\t" "s_nop 0\n\t" "s_nop 0\n\t" "s_nop 0\n\t" "s_nop 0\n\t" "s_nop 0\n\t" "s_nop 0\n\t" "s_nop 0\n\t" "s_nop 0\n\t"
                         "s_nop 0\n\t" "s_nop 0\n\t" "global_load_dwordx4 a[96:99], v4, %3 offset:-512 sc1\n\t" "global_load_dwordx4 a[104:107], v4, %3 offset:512 sc1\n\t"
                         "global_load_dwordx4 a[112:115], v4, %3 offset:1536 sc1\n\t" "global_load_dwordx4 a[120:123], v4, %3 offset:2560 sc1\n\t" "global_load_dwordx4 a[100:103], %13, %3 sc1\n\t"
                         "global_load_dwordx4 a[108:111], %13, %3 offset:1024 sc1\n\t" "global_load_dwordx4 a[116:119], %13, %3 offset:2048 sc1\n\t" "global_load_dwordx4 a[124:127], %13, %3 offset:3072 sc1\n\t" "s_nop 9\n\t"
                         "global_load_dwordx4 a[0:3], v5, %7 offset:-4096 sc1\n\t" "global_load_dwordx4 a[4:7], v5, %7 offset:-3072 sc1\n\t" "global_load_dwordx4 a[16:19], v5, %7 sc1\n\t"
                         "global_load_dwordx4 a[20:23], v5, %7 offset:1024 sc1\n\t" "global_load_dwordx4 a[32:35], v5, %8 offset:-4096 sc1\n\t" "global_load_dwordx4 a[36:39], v5, %8 offset:-3072 sc1\n\t"
                         "global_load_dwordx4 a[48:51], v5, %8 sc1\n\t" "global_load_dwordx4 a[52:55], v5, %8 offset:1024 sc1\n\t" "s_nop 14\n\t" "global_load_dwordx4 a[8:11], %12, %7 offset:-2048 sc1\n\t"
                         "global_load_dwordx4 a[12:15], %12, %7 offset:-1024 sc1\n\t" "global_load_dwordx4 a[24:27], %12, %7 offset:2048 sc1\n\t" "global_load_dwordx4 a[28:31], %12, %7 offset:3072 sc1\n\t"
                         "global_load_dwordx4 a[40:43], %12, %8 offset:-2048 sc1\n\t" "global_load_dwordx4 a[44:47], %12, %8 offset:-1024 sc1\n\t" "global_load_dwordx4 a[56:59], %12, %8 offset:2048 sc1\n\t"
                         "global_load_dwordx4 a[60:63], %12, %8 offset:3072 sc1\n\t" "s_nop 15\n\t" "s_nop 15\n\t" "s_nop 14\n\t" "global_load_dwordx4 a[80:83], %14, %6 sc1\n\t"
                         "global_load_dwordx4 a[84:87], %14, %6 offset:1024 sc1\n\t" "global_load_dwordx4 a[88:91], %14, %6 offset:2048 sc1\n\t" "global_load_dwordx4 a[92:95], %14, %6 offset:3072 sc1\n\t"
                         "s_mov_b64 exec, %9\n\t" "global_load_dwordx4 a[64:67], v6, %6 offset:-2048 sc1\n\t" "global_load_dwordx4 a[68:71], v6, %6 offset:-1536 sc1\n\t"
                         "global_load_dwordx4 a[72:75], v6, %6 offset:-1024 sc1\n\t" "global_load_dwordx4 a[76:79], v6, %6 offset:-512 sc1\n\t" "s_mov_b64 exec, -1\n\t" "s_waitcnt vmcnt(23)\n\t"
                         "v_smfmac_f32_16x16x64_bf16 v[48:51], a[0:3], v[112:119], v9\n\t" "s_waitcnt vmcnt(22)\n\t" "v_smfmac_f32_16x16x64_bf16 v[52:55], a[4:7], v[120:127], v9\n\t" "s_waitcnt vmcnt(21)\n\t"
                         "v_smfmac_f32_16x16x64_bf16 v[64:67], a[16:19], v[144:151], v9\n\t" "s_waitcnt vmcnt(20)\n\t" "v_cvt_pk_bf16_f32 v16, v48, v49\n\t" "v_cvt_pk_bf16_f32 v17, v50, v51\n\t"
                         "v_smfmac_f32_16x16x64_bf16 v[68:71], a[20:23], v[152:159], v9\n\t" "s_waitcnt vmcnt(19)\n\t" "v_cvt_pk_bf16_f32 v18, v52, v53\n\t" "v_cvt_pk_bf16_f32 v19, v54, v55\n\t"
                         "v_smfmac_f32_16x16x64_bf16 v[80:83], a[32:35], v[176:183], v9\n\t" "s_waitcnt vmcnt(18)\n\t" "v_cvt_pk_bf16_f32 v24, v64, v65\n\t" "v_cvt_pk_bf16_f32 v25, v66, v67\n\t"
                         "v_smfmac_f32_16x16x64_bf16 v[84:87], a[36:39], v[184:191], v9\n\t" "s_waitcnt vmcnt(17)\n\t" "v_cvt_pk_bf16_f32 v26, v68, v69\n\t" "v_cvt_pk_bf16_f32 v27, v70, v71\n\t"
                         "v_smfmac_f32_16x16x64_bf16 v[96:99], a[48:51], v[208:215], v9\n\t" "s_waitcnt vmcnt(16)\n\t" "v_cvt_pk_bf16_f32 v32, v80, v81\n\t" "v_cvt_pk_bf16_f32 v33, v82, v83\n\t"
                         "v_smfmac_f32_16x16x64_bf16 v[100:103], a[52:55], v[216:223], v9\n\t" "s_waitcnt vmcnt(15)\n\t" "v_cvt_pk_bf16_f32 v34, v84, v85\n\t" "v_cvt_pk_bf16_f32 v35, v86, v87\n\t"
                         "v_smfmac_f32_16x16x64_bf16 v[56:59], a[8:11], v[128:135], v9\n\t" "s_waitcnt vmcnt(14)\n\t" "v_cvt_pk_bf16_f32 v40, v96, v97\n\t" "v_cvt_pk_bf16_f32 v41, v98, v99\n\t"
                         "v_smfmac_f32_16x16x64_bf16 v[60:63], a[12:15], v[136:143], v9\n\t" "s_waitcnt vmcnt(13)\n\t" "v_cvt_pk_bf16_f32 v42, v100, v101\n\t" "v_cvt_pk_bf16_f32 v43, v102, v103\n\t"
                         "v_smfmac_f32_16x16x64_bf16 v[72:75], a[24:27], v[160:167], v9\n\t" "s_waitcnt vmcnt(12)\n\t" "v_cvt_pk_bf16_f32 v20, v56, v57\n\t" "v_cvt_pk_bf16_f32 v21, v58, v59\n\t"
                         "v_smfmac_f32_16x16x64_bf16 v[76:79], a[28:31], v[168:175], v9\n\t" "s_waitcnt vmcnt(11)\n\t" "v_cvt_pk_bf16_f32 v22, v60, v61\n\t" "v_cvt_pk_bf16_f32 v23, v62, v63\n\t"
                         "v_smfmac_f32_16x16x64_bf16 v[88:91], a[40:43], v[192:199], v9\n\t" "s_waitcnt vmcnt(10)\n\t" "v_cvt_pk_bf16_f32 v28, v72, v73\n\t" "v_cvt_pk_bf16_f32 v29, v74, v75\n\t"
                         "v_smfmac_f32_16x16x64_bf16 v[92:95], a[44:47], v[200:207], v9\n\t" "s_waitcnt vmcnt(9)\n\t" "v_cvt_pk_bf16_f32 v30, v76, v77\n\t" "v_cvt_pk_bf16_f32 v31, v78, v79\n\t"
                         "v_smfmac_f32_16x16x64_bf16 v[104:107], a[56:59], v[224:231], v9\n\t" "s_waitcnt vmcnt(8)\n\t" "v_cvt_pk_bf16_f32 v36, v88, v89\n\t"
                         "v_smfmac_f32_16x16x64_bf16 v[108:111], a[60:63], v[232:239], v9\n\t" "s_waitcnt vmcnt(7)\n\t" "v_cvt_pk_bf16_f32 v37, v90, v91\n\t"
                         "v_smfmac_f32_16x16x64_bf16 v[0:3], a[80:83], a[96:103], v8\n\t" "s_waitcnt vmcnt(6)\n\t" "v_cvt_pk_bf16_f32 v38, v92, v93\n\t" "v_smfmac_f32_16x16x64_bf16 v[0:3], a[84:87], a[104:111], v8\n\t"
                         "s_waitcnt vmcnt(5)\n\t" "v_cvt_pk_bf16_f32 v39, v94, v95\n\t" "v_smfmac_f32_16x16x64_bf16 v[0:3], a[88:91], a[112:119], v8\n\t" "s_waitcnt vmcnt(4)\n\t" "v_cvt_pk_bf16_f32 v44, v104, v105\n\t"
                         "v_smfmac_f32_16x16x64_bf16 v[0:3], a[92:95], a[120:127], v8\n\t" "s_waitcnt vmcnt(3)\n\t" "v_cvt_pk_bf16_f32 v45, v106, v107\n\t" "v_smfmac_f32_16x16x64_bf16 v[0:3], a[64:67], v[16:23], v8\n\t"
                         "s_waitcnt vmcnt(2)\n\t" "v_cvt_pk_bf16_f32 v46, v108, v109\n\t" "v_smfmac_f32_16x16x64_bf16 v[0:3], a[68:71], v[24:31], v8\n\t" "s_waitcnt vmcnt(1)\n\t" "v_cvt_pk_bf16_f32 v47, v110, v111\n\t"
                         "v_smfmac_f32_16x16x64_bf16 v[0:3], a[72:75], v[32:39], v8\n\t" "s_waitcnt vmcnt(0)\n\t" "v_smfmac_f32_16x16x64_bf16 v[0:3], a[76:79], v[40:47], v8\n\t" ".Lv_end:\n\t" "s_nop 7\n\t"
                         "v_pk_add_f32 v[0:1], v[0:1], v[2:3]\n\t" "v_cvt_pk_bf16_f32 v2, v0, v1\n\t" "v_cmp_u_f32 vcc, v0, v1\n\t" "s_cmp_eq_u64 vcc, 0\n\t"
                         "v_mov_b32_dpp v3, v2 quad_perm:[1,0,3,2] row_mask:0xf bank_mask:0xf\n\t" "s_cselect_b64 %0, -1, 0\n\t" "v_perm_b32 v0, v2, v3, v11\n\t" "s_mov_b64 exec, %0\n\t"
                         "global_store_dword v10, v0, %5 offset:-4096 sc1\n\t" "global_store_dword v10, v0, %4 offset:-4096 sc1\n\t" "s_memrealtime %1\n\t" "s_mov_b64 exec, -1\n\t" "s_waitcnt lgkmcnt(0)\n\t"
                         : "=&s"(ok), "=&s"(tp8)
                         : "{v[4:11]}"(ad), "s"(up), "s"(mnx), "s"((u64)mnx ^ 8192), "s"(memp + 16384), "s"(memp + 32768 - 2048), "s"(memp + 49152 - 2048), "s"(0xc30000c3c30000c3ull),
                           "s"(uni((int)(tgt - (u32)(v < 16 ? cfg.dU - (v >> 3) * cfg.s2 : cfg.cR2)))), "s"(v < 16 ? 0 : (v < 24 ? 1 : cfg.T3)), "v"(pob), "v"(uob), "v"(pmo), "s"(tries), VALL : "memory", "vcc", "scc", "s42");
            if (__builtin_expect(ok != 0, 1) || s < 0) break;
            if (all || --budget < 0) {
                asm volatile("v_min_u32 v0, -2, v0\n\tglobal_store_dword v10, v0, %0 offset:-4096 sc1\n\tglobal_store_dword v10, v0, %1 offset:-4096 sc1"
                             : : "s"(mnx), "s"((u64)mnx ^ 8192), "{v[4:11]}"(ad), VALL : "memory");
                break;
            }
            if ((++tries & 15) == 0) {
                u32 m = 0;
#pragma nounroll
                for (int i = 0; i < 32; ++i) {
                    u32x4 t;
                    gld16c<0>(t, i < 24 ? memp + 12288 : up, i < 20 ? (4 + i + ((4 + i) & 24)) * 1024 + ad[3] : (i < 24 ? i * 512 - 8192 + ad[2] : (i & 7) * 512 - 512 + (i & 1 ? uob : ad[0]))); waitv<0>(t);
                    m = max3u(max3u(m, t[0], t[1]), t[2], t[3]);
                }
                all = __builtin_amdgcn_ballot_w64(m == 0xffffffffu) == 0;
            }
            phi += (u32)cfg.G;
            zero();
        }
        asm volatile("s_barrier\n\t" "ds_read_b128 v[112:115], %0\n\t" "ds_read_b128 v[116:119], %0 offset:1024\n\t" "ds_read_b128 v[120:123], %0 offset:2048\n\t" "ds_read_b128 v[124:127], %0 offset:3072\n\t"
                     "ds_read_b128 v[128:131], %0 offset:4096\n\t" "ds_read_b128 v[132:135], %0 offset:5120\n\t" "ds_read_b128 v[136:139], %0 offset:6144\n\t" "ds_read_b128 v[140:143], %0 offset:7168\n\t"
                     "ds_read_b128 v[144:147], %0 offset:8192\n\t" "ds_read_b128 v[148:151], %0 offset:9216\n\t" "ds_read_b128 v[152:155], %0 offset:10240\n\t" "ds_read_b128 v[156:159], %0 offset:11264\n\t"
                     "ds_read_b128 v[160:163], %0 offset:12288\n\t" "ds_read_b128 v[164:167], %0 offset:13312\n\t" "ds_read_b128 v[168:171], %0 offset:14336\n\t" "ds_read_b128 v[172:175], %0 offset:15360\n\t"
                     "ds_read_b128 v[176:179], %0 offset:16384\n\t" "ds_read_b128 v[180:183], %0 offset:17408\n\t" "ds_read_b128 v[184:187], %0 offset:18432\n\t" "ds_read_b128 v[188:191], %0 offset:19456\n\t"
                     "ds_read_b128 v[192:195], %0 offset:20480\n\t" "ds_read_b128 v[196:199], %0 offset:21504\n\t" "ds_read_b128 v[200:203], %0 offset:22528\n\t" "ds_read_b128 v[204:207], %0 offset:23552\n\t"
                     "ds_read_b128 v[208:211], %0 offset:24576\n\t" "ds_read_b128 v[212:215], %0 offset:25600\n\t" "ds_read_b128 v[216:219], %0 offset:26624\n\t" "ds_read_b128 v[220:223], %0 offset:27648\n\t"
                     "ds_read_b128 v[224:227], %0 offset:28672\n\t" "ds_read_b128 v[228:231], %0 offset:29696\n\t" "ds_read_b128 v[232:235], %0 offset:30720\n\t" "ds_read_b128 v[236:239], %0 offset:31744\n\t"
                     : : "v"(lds + ad[3] + ((s + 1) & 1) * 32768), VALL : "memory");
        if (s >= 0) {
            follow(phi, (int)(tt & 0xffffu) + 5 * (v >> 2) / 2 - VISV, (u32)tp8, cfg.G);
            if (vb == 0 && lane < 8) gst8(fl, (F_CUR + lane) * 128, u32x2{(u32)s - 1, phi + (tt & 0xffffu)});
        }
        phi += tt & 0xffffu;
        memp = mnx; up = ub + uslot(sd, s + 1);
        zero();
        tt = sched_ld0(s + 1);
        clk.calib();
        while (s >= 0) {
            u32 h0;
            spin_to(clk.at(phi + (u32)cfg.dV));
            asm volatile("global_load_dword %0, %1, %2 offset:-4096 sc1\n\ts_sleep 5" : "=&v"(h0) : "v"(ho), "s"(memp) : "memory");
            edge(clk.r1, clk.m1);
            asm volatile("s_waitcnt vmcnt(0)" : "+v"(h0) : : "memory");
            if (__builtin_amdgcn_ballot_w64(h0 == 0xffffffffu) == 0 || --budget < 0) break;
            phi += (u32)cfg.G;
        }
        asm volatile(".Lkm_lv:");
    }
}

DI void xcopy_task(const Ptrs& p, int j, int w) {
    const int tid = 64 * w + lanex();
    Sync y;
    sync_ld(y, p.ws);
    u32x4 xv[10];
    sfor<0, 10>([&](auto I) { gld16<I.v * 128>(xv[I.v], p.x + (size_t)j * 40960, (u32)((tid & 31) * 1280 + (tid >> 5) * 16)); });
    sfor<0, 10>([&](auto I) { waitv<9 - I.v>(xv[I.v]); });
    asm volatile("" : "+v"(y.r), "+v"(y.g), "+v"(y.e));
    sfor<0, 10>([&](auto I) { gst16(p.ws + OFF_XF + (size_t)j * 40960 + I.v * 4096, (u32)tid * 16, xv[I.v]); });
    waitvm<0>();
    barrier();
    const u64 t = sync_tag(y.e);
    if (tid == 0) gst8(p.ws + OFF_LAT, 1536 + (u32)j * 8, u32x2{(u32)t, (u32)(t >> 32)});
}
DI void xcopy_wait(const char* ws, u64 t) {
#pragma nounroll
    for (int it = 0; it < (1 << 16); ++it) {
        u32x2 r;
        asm volatile("s_nop 4\n\tglobal_load_dwordx2 %0, %1, %2 offset:1536 sc1\n\ts_waitcnt vmcnt(0)" : "=&v"(r) : "v"(lanex() * 8), "s"(ws + OFF_LAT) : "memory");
        if (__builtin_amdgcn_ballot_w64(r[0] != (u32)t || r[1] != (u32)(t >> 32)) == 0) break;
        __builtin_amdgcn_s_sleep(16);
    }
}
DI void init_task(const Ptrs& p, int j, u32 lds, int w) {
    const int lane = lanex(), tid = 64 * w + lane;
    Sync y;
    sync_ld(y, p.ws);
    const u32x4 ff = u32x4{~0u, ~0u, ~0u, ~0u};
    const int i = j * 256 + tid, r = j < 8 ? j & 1 : min(j / 2 - 2, NREG - 1);
    char* const vb = p.ws + (r < 2 ? 0 : OFF_U - (4u << 20)) + ((size_t)r << 21) + 1984 * 1024 + (j < 8 ? j & 6 : (j & 1) * 2 + (j >> 1 == 15) * 4) * 8192;
    const u32 vo = (u32)((w >> 1) * 8192 + (w & 1) * 1024);
    gst16(vb, vo + lane * 16, ff);
    if (i < F_N * 8) gst16(p.ws + OFF_ST, (u32)i * 16, u32x4{i == F_ONE * 8 ? 1000u : ~0u, ~0u, ~0u, ~0u});
    gst16(p.ws + 65536, (u32)i * 16, ff);
    if (i < 4096) {
        const int k = (i & 511) + ((i >> 1) & 0x600);
        u32x4 m;
        gld16<0>(m, p.mem0, (u32)((2 * ((k >> 2) & 3) + (k & 1)) * 1280 + min(2 * (k >> 4) + ((k >> 1) & 1), 79) * 16));
        waitv<0>(m);
        gst16(p.ws, (u32)i * 16, k < 640 ? u32x4{min(m[0], 0xfffffffeu), min(m[1], 0xfffffffeu), min(m[2], 0xfffffffeu), min(m[3], 0xfffffffeu)} : ff);
    }
#pragma unroll
    for (int c = 0; c < 6; ++c) gst16(p.ws + OFF_U + c * 131072 - (c == 5) * 122880, (u32)i * 16, ff);
    waitvm<0>();
    asm volatile("" : "+v"(y.r), "+v"(y.g), "+v"(y.e));
    barrier();
    if (w) return;
    u32 dt[2] = {0xffffffu, 0xffffffu};
#pragma unroll
    for (int c = 0; c < 2 * NSAMP; ++c) {
        u32 v;
        const u32 t0 = mt();
        asm volatile("global_load_dword %0, %1, %2 sc1\n\ts_waitcnt vmcnt(0)" : "=&v"(v) : "v"((c & 1) * 8192 + (c >> 1) * 256), "s"(vb) : "memory");
        dt[c & 1] = min(dt[c & 1], mt() - t0);
    }
    const u64 t = sync_tag(y.e);
    const u32 vote = (u32)((dt[0] > dt[1]) ^ xhalf() ^ (int)((vb - p.ws) >> 21)) & 1u;
    if (tid == 0) {
        if (j == 0) gst8(p.ws + OFF_LAT, 512, u32x2{(u32)t, rt()});
        gst8(p.ws + OFF_LAT, (u32)j * 8, u32x2{(u32)t | vote, (u32)(t >> 32)});
    }
}

DI float xmax(float v) {
    const auto a = __builtin_amdgcn_permlane32_swap(__float_as_uint(v), __float_as_uint(v), false, false);
    v = fmaxf(__uint_as_float(a[0]), __uint_as_float(a[1]));
    const auto b = __builtin_amdgcn_permlane16_swap(__float_as_uint(v), __float_as_uint(v), false, false);
    return fmaxf(__uint_as_float(b[0]), __uint_as_float(b[1]));
}
DI float xsum(float v) {
    const auto a = __builtin_amdgcn_permlane32_swap(__float_as_uint(v), __float_as_uint(v), false, false);
    v = __uint_as_float(a[0]) + __uint_as_float(a[1]);
    const auto b = __builtin_amdgcn_permlane16_swap(__float_as_uint(v), __float_as_uint(v), false, false);
    return __uint_as_float(b[0]) + __uint_as_float(b[1]);
}
DI void gst16c(char* sbase, u32 voff, u32x4 v) {
    asm volatile("global_store_dwordx4 %0, %1, %2 sc1\n\ts_nop 1" : : "v"(voff), "v"(v), "s"(sbase) : "memory");
}
DI void gst8c(char* sbase, u32 voff, u32x2 v) {
    asm volatile("global_store_dwordx2 %0, %1, %2 sc1\n\ts_nop 1" : : "v"(voff), "v"(v), "s"(sbase) : "memory");
}
#define WAIT6(N, r, o) asm volatile("s_waitcnt vmcnt(" #N ")" : "+v"(r[o]), "+v"(r[o + 1]), "+v"(r[o + 2]), "+v"(r[o + 3]), "+v"(r[o + 4]), "+v"(r[o + 5]) : : "memory")
#define WSLEEP 90
DI void wg_wait(const char* fl, int w, int i0, int t0, int i1, int t1, int i2, int t2, int i3, int t3, int dt, bool acq) {
    if (w == 0) {
        const int lane = lanex(), tg = lane == 1 ? t1 : (lane == 2 ? t2 : (lane == 3 ? t3 : t0));
        const int ix = tg <= 0 ? i0 : (lane == 1 ? i1 : (lane == 2 ? i2 : (lane == 3 ? i3 : i0)));
#pragma nounroll
        for (int it = 0; it < (1 << 14); ++it) {
            u32 v;
            flag_ld(v, fl, ix);
            asm volatile("s_waitcnt vmcnt(0)" : "+v"(v) : : "memory");
            if (__builtin_amdgcn_ballot_w64((int)(v + 1u) < tg) == 0) break;
            const u32 wake = rt() + (u32)(dt + 180 * min(max(t0 - 2 - uni((int)v), 0), 64));
            while ((int)(rt() - wake) < 0) __builtin_amdgcn_s_sleep(4);
        }
        if (acq) acquire();
    }
    barrier();
}
#define TEST 172
DI void until(u32 t) { while ((int)(rt() - t) < 0) __builtin_amdgcn_s_sleep(1); }
DI u32 step_wait(const char* fl, int w, int gr, int t, u32 lds) {
    if (w == 0) {
        u32x2 v;
#pragma nounroll
        for (int it = 0; it < (1 << 14); ++it) {
            asm volatile("s_nop 4\n\tglobal_load_dwordx2 %0, %1, %2 sc1\n\ts_waitcnt vmcnt(0)" : "=&v"(v) : "v"((F_CUR + gr) * 128), "s"(fl) : "memory");
            const int d = t - 1 - uni((int)v[0]);
            if (d <= 0) { v[1] += (u32)(d * TEST); break; }
            const u32 now = rt();
            nap(min(max((int)((u32)uni((int)v[1]) - now) + d * TEST + 10, 40), 2000));
        }
        *LDSP(u32, lds + GRING * GBUF + 4) = v[1];
    }
    barrier();
    return (u32)uni((int)*LDSP(u32, lds + GRING * GBUF + 4));
}
#define PDL 135
#define ODL 280
#define TDT 35
template <bool LATE>
DI void px_wave(const Ptrs& p, const ChainCfg& cfg, int s, int h, int slow, int w, u32 lds, bool keep) {
    const int lane = lanex(), n = lane & 15, g = lane >> 4;
    const char* qb = p.ws + OFF_QX + (size_t)(s * NH + h) * 12288;
    const char* kb = p.ws + OFF_KX + (size_t)(s * NH + h) * 12288;
    const char* mb = p.ws + OFF_KM + (size_t)(s * NH + h) * 3072;
    const u32 ko = (u32)((n >> 3) * 6144 + (8 * ((n >> 2) & 1) + (n & 3) + 16 * g) * 16);
    const u32 mo = (u32)(g * 128 + (n & 7) * 16);
    u32x4 qf[12], kf[12], mf[6];
    sfor<0, 12>([&](auto I) { gld16c<(I.v % 3) * 1024>(qf[I.v], qb + (I.v / 3) * 3072, lane * 16); gap<SMP>(slow); });
    if (!SMP) pace(slow);
    sfor<0, 12>([&](auto I) { gld16c<(I.v % 3) * 1024 + (I.v / 6) * 64>(kf[I.v], kb + ((I.v / 3) & 1) * 3072, ko); gap<SMP>(slow); });
    if (!SMP) pace(slow);
    if constexpr (LATE) { WAIT6(0, qf, 0); WAIT6(0, qf, 6); WAIT6(0, kf, 0); }
    else {
        sfor<0, 6>([&](auto I) { gld16c<I.v * 512>(mf[I.v], mb, mo); });
        WAIT6(24, qf, 0); WAIT6(18, qf, 6); WAIT6(12, kf, 0);
    }
    f32x4 sc[2][3];
#pragma unroll
    for (int tt = 0; tt < 2; ++tt)
#pragma unroll
        for (int T = 0; T < 3; ++T) sc[tt][T] = f32x4{0.f, 0.f, 0.f, 0.f};
#pragma unroll
    for (int ks = 0; ks < 6; ++ks) { sc[0][0] = mfma16(kf[ks], qf[ks], sc[0][0]); sc[1][0] = mfma16(kf[ks], qf[6 + ks], sc[1][0]); }
    WAIT6(6, kf, 6);
#pragma unroll
    for (int ks = 0; ks < 6; ++ks) { sc[0][1] = mfma16(kf[6 + ks], qf[ks], sc[0][1]); sc[1][1] = mfma16(kf[6 + ks], qf[6 + ks], sc[1][1]); }
    if constexpr (LATE) {
        until(step_wait(p.ws + OFF_ST, w, h & 7, s - 1, lds) + (u32)(s == NSEG - 1 ? cfg.T : cfg.T + PDX));
        bool ok = false;
#pragma nounroll
        for (int it = 0; it < (1 << 14); ++it) {
            u32 v;
            sfor<0, 6>([&](auto I) { gld16c<I.v * 512>(mf[I.v], mb, mo); });
            flag_ldm(v, p.ws + OFF_ST, (s < NSEG - 2 ? F_CUR : F_PROG + 8 * s) + (h & 7), it > 3);
            WAIT6(0, mf, 0);
            asm volatile("" : "+v"(v));
            u32 m = 0;
#pragma unroll
            for (int i = 0; i < 24; ++i) m = max(m, mf[i >> 2][i & 3]);
            if (ok || __builtin_amdgcn_ballot_w64(m == ~0u) == 0) break;
            ok = it > 3 && uni((int)v) >= (s < NSEG - 2 ? s : 7);
            if (s < NSEG - 1) nap(60);
        }
    }
    WAIT6(0, mf, 0);
#pragma unroll
    for (int ks = 0; ks < 6; ++ks) { sc[0][2] = mfma16(mf[ks], qf[ks], sc[0][2]); sc[1][2] = mfma16(mf[ks], qf[6 + ks], sc[1][2]); }
    const float kscale = 0.07216878364870322f;
    char* pb = p.ws + OFF_PX + (size_t)s * 81920;
#pragma unroll
    for (int tt = 0; tt < 2; ++tt) {
        float v[12], mx = -3.0e38f;
#pragma unroll
        for (int i = 0; i < 12; ++i) {
            const float t = rbf(rbf(sc[tt][i >> 2][i & 3]) * kscale);
            v[i] = (i >= 8 || 8 * g + i <= 16 * tt + n) ? t : -3.0e38f;
            mx = fmaxf(mx, v[i]);
        }
        mx = xmax(mx);
        float sx = 0.f, sm = 0.f;
#pragma unroll
        for (int i = 0; i < 12; ++i) {
            v[i] = __builtin_amdgcn_exp2f((v[i] - mx) * 1.4426950408889634f);
            if (i < 8) sx += v[i]; else sm += v[i];
        }
        const float inv = __builtin_amdgcn_rcpf(xsum(sx + 0.5f * sm));
        const u32x4 px = u32x4{cvtpk(v[0] * inv, v[1] * inv), cvtpk(v[2] * inv, v[3] * inv), cvtpk(v[4] * inv, v[5] * inv), cvtpk(v[6] * inv, v[7] * inv)};
        const u32x2 pm = u32x2{cvtpk(v[9] * inv, v[8] * inv), cvtpk(v[11] * inv, v[10] * inv)};
        const int ox = h * 2048 + tt * 1024, om = 65536 + (h >> 2) * 2048 + (h & 3) * 256 + tt * 1024;
        if (!LATE && keep) {
            *LDSP(u32x4, lds + ox + lane * 16) = px;
            *LDSP(u32x2, lds + om + n * 16 + (g & 1) * 8) = pm;
        } else {
            gst16c(pb + ox, lane * 16, px);
            gst8c(pb + om, (u32)(n * 16 + (g & 1) * 8), pm);
        }
    }
}

#define WAIT10(N, r) asm volatile("s_waitcnt vmcnt(" #N ")" : "+v"(r[0]), "+v"(r[1]), "+v"(r[2]), "+v"(r[3]), "+v"(r[4]), "+v"(r[5]), "+v"(r[6]), "+v"(r[7]), "+v"(r[8]), "+v"(r[9]) : : "memory")
DI void out_task(const Ptrs& p, int s, int cg0, int cg1, u32 lds, int slow, int w, int sd) {
    u32x4 pf[2][10];
    if (cg1 - cg0 > 1) {
        const u32 la = lds + lanex() * 16;
#pragma unroll
        for (int i = 0; i < 20; ++i) pf[i & 1][i >> 1] = ldsr16(la + (i < 16 ? w * 16384 + i * 1024 : 65536 + w * 4096 + (i - 16) * 1024));
    } else {
        const char* pb = p.ws + OFF_PX + (size_t)s * 81920;
        const u32 l16 = lanex() * 16;
        sfor<0, 20>([&](auto I) { gld16c<(I.v & 3) * 1024>(pf[I.v & 1][I.v >> 1], pb + (I.v < 16 ? w * 16384 + (I.v >> 2) * 4096 : 65536 + w * 4096), l16); gap<SMO>(slow); if (!SMO && I.v == 9) pace(slow); });
        WAIT10(0, pf[0]); WAIT10(0, pf[1]);
    }
#pragma nounroll
    for (int cg = cg0; cg < cg1; ++cg) {
    const int lane = lanex(), tid = 64 * w + lane, n = lane & 15, g = lane >> 4;
    const char* db = p.ws + OFF_D + s * D_STRIDE + (size_t)(16 * cg) * 16384 + w * 4096;
    const char* ub = p.ws + uslot(sd, s) + (size_t)(16 * cg) * 4096 + w * 1024;
    const u32 dof = (u32)((n >> 3) * 16384 + g * 128 + (n & 7) * 16), uof = (u32)((n >> 3) * 4096 + g * 128 + (n & 7) * 16);
    u32x4 af[3][10];
    auto aload = [&](u32x4 (&r)[10], int ct) {
        if (!SMO) pace(slow);
        sfor<0, 8>([&](auto I) { gld16c<I.v * 512>(r[I.v], db + ct * 32768, dof); gap<SMO>(slow); });
        gld16c<0>(r[8], ub + uchunk(ct), uof); gap<SMO>(slow); gld16c<512>(r[9], ub + uchunk(ct), uof); gap<SMO>(slow);
    };
    aload(af[0], 0);
    aload(af[1], 1);
    aload(af[2], 2);
    const int tok = 32 * s + (tid >> 3), col = 128 * cg + 8 * (tid & 7);
    u32x4 bb[2]; u32 gv;
    gld16c<0>(bb[0], p.ws + OFF_BOUT, (u32)(s * 40960 + col * 64 + (tid >> 3) * 16)); gld16c<0>(bb[1], p.ws + OFF_BOUT + 4096, (u32)(s * 40960 + col * 64 + (tid >> 3) * 16));
    asm volatile("global_load_dword %0, %1, %2 sc1" : "=v"(gv) : "v"(tok * 4), "s"(p.ws + OFF_GATE) : "memory");
    barrier();
    const u32 wa = lds + (u32)w * 16896 + n * 528 + g * 16;
    sfor<0, 8>([&](auto I) {
        constexpr int ct = I.v;
        u32x4 (&r)[10] = af[ct % 3];
        if constexpr (ct == 0) WAIT10(23, r);
        else if constexpr (ct < 3) WAIT10(23, r);
        else if constexpr (ct < 6) WAIT10(20, r);
        else if constexpr (ct == 6) WAIT10(10, r);
        else WAIT10(0, r);
        f32x4 c0 = f32x4{0.f, 0.f, 0.f, 0.f}, c1 = c0;
#pragma unroll
        for (int i = 0; i < 10; ++i) { c0 = mfma16(r[i], pf[0][i], c0); c1 = mfma16(r[i], pf[1][i], c1); }
        *LDSP(f32x4, wa + ct * 64) = c0;
        *LDSP(f32x4, wa + ct * 64 + 16 * 528) = c1;
        PIN();
        if constexpr (ct + 3 < 8) aload(r, ct + 3);
        PIN();
    });
    asm volatile("" : "+v"(bb[0]), "+v"(bb[1]), "+v"(gv));
    barrier();
    const u32 ra = lds + (u32)(tid >> 3) * 528 + (tid & 7) * 32;
    const float gt = __uint_as_float(gv), g1 = rbf(1.f - gt);
    u32 ov[8];
#pragma unroll
    for (int i = 0; i < 4; ++i) {
        const u32 ri = ra + 16 * (i & 1) + 256 * (i >> 1);
        const f32x4 z = (*LDSP(f32x4, ri) + *LDSP(f32x4, ri + 16896)) + (*LDSP(f32x4, ri + 2 * 16896) + *LDSP(f32x4, ri + 3 * 16896));
#pragma unroll
        for (int j = 0; j < 2; ++j) {
            const u32 b = bb[i >> 1][2 * (i & 1) + j];
            const u32 a = cvtpk(z[2 * j], z[2 * j + 1]);
            const u32 ga = cvtpk(gt * __uint_as_float(a << 16), gt * __uint_as_float(a & 0xffff0000u));
            const u32 gb = cvtpk(g1 * __uint_as_float(b << 16), g1 * __uint_as_float(b & 0xffff0000u));
            ov[2 * i + j] = cvtpk(__uint_as_float(ga << 16) + __uint_as_float(gb << 16), __uint_as_float(ga & 0xffff0000u) + __uint_as_float(gb & 0xffff0000u));
        }
    }
    gst16p(p.out, (u32)(tok * 1280 + col * 2), u32x4{ov[0], ov[1], ov[2], ov[3]});
    gst16p(p.out, (u32)(tok * 1280 + col * 2 + 128), u32x4{ov[4], ov[5], ov[6], ov[7]});
    }
}

DI void out_tail(const Ptrs& p, const ChainCfg& cfg, int s, int cg, u32 lds, int w, int sd, int gr) {
    const int lane = lanex(), tid = 64 * w + lane, n = lane & 15, g = lane >> 4;
    const char* db = p.ws + OFF_D + s * D_STRIDE + (size_t)(4 * cg) * 16384 + w * 4096;
    const char* ub = p.ws + uslot(sd, s) + uchunk(2 * cg) + w * 1024;
    const char* pb = p.ws + OFF_PX + (size_t)s * 81920;
    const u32 dof = (u32)((n >> 3) * 16384 + g * 128 + (n & 7) * 16), uof = (u32)((n >> 3) * 4096 + g * 128 + (n & 7) * 16);
    u32x4 pf[2][10], af[2][10];
    sfor<0, 16>([&](auto I) { gld16c<(I.v & 7) * 512>(af[I.v >> 3][I.v & 7], db + (I.v >> 3) * 32768, dof); });
    const int tok = 32 * s + (tid >> 3), col = 32 * cg + 4 * (tid & 7);
    u32x2 bb; u32 gv;
    asm volatile("global_load_dwordx2 %0, %2, %3 sc1\n\tglobal_load_dword %1, %4, %5 sc1\n\ts_waitcnt vmcnt(0)" : "=&v"(bb), "=&v"(gv)
                 : "v"(s * 40960 + (col >> 3) * 512 + (tid >> 3) * 16 + (col & 7) * 2), "s"(p.ws + OFF_BOUT), "v"(tok * 4), "s"(p.ws + OFF_GATE) : "memory");
    sfor<0, 16>([&](auto I) { waitv<0>(af[I.v >> 3][I.v & 7]); });
    until(step_wait(p.ws + OFF_ST, w, gr, s - 1, lds) + (u32)(s == NSEG - 1 ? cfg.cR : cfg.cR + 2 * PDX));
    const u32 wa = lds + (u32)w * 4608 + n * 144 + g * 16;
#pragma nounroll
    for (int it = 0; it < (1 << 14); ++it) {
        if (it == 4) {
            flag_wait(p.ws + OFF_ST, F_PX + s, 8);
            flag_wait(p.ws + OFF_ST, s < NSEG - 2 ? F_CUR + gr : F_UD + 2 * (s & 1), s < NSEG - 2 ? s + 1 : 16);
            flag_wait(p.ws + OFF_ST, s < NSEG - 2 ? F_ONE : F_UD + 2 * (s & 1) + 1, 16);
        }
        sfor<0, 4>([&](auto I) { gld16c<(I.v & 1) * 512>(af[I.v >> 1][8 + (I.v & 1)], ub + (I.v >> 1) * 16384, uof); });
        sfor<0, 8>([&](auto I) { gld16c<(I.v & 1) * 2048>(pf[0][I.v], pb + w * 16384 + (I.v >> 1) * 4096, lane * 16); });
        gld16c<0>(pf[0][8], pb + 65536 + w * 4096, lane * 16); gld16c<2048>(pf[0][9], pb + 65536 + w * 4096, lane * 16);
        sfor<0, 8>([&](auto I) { gld16c<(I.v & 1) * 2048 + 1024>(pf[1][I.v], pb + w * 16384 + (I.v >> 1) * 4096, lane * 16); });
        gld16c<1024>(pf[1][8], pb + 65536 + w * 4096, lane * 16); gld16c<3072>(pf[1][9], pb + 65536 + w * 4096, lane * 16);
        WAIT10(10, pf[0]); WAIT10(10, af[0]); WAIT10(10, af[1]);
        float chk = 0.f;
#pragma unroll
        for (int tt = 0; tt < 2; ++tt) {
            if (tt) WAIT10(0, pf[1]);
            f32x4 c0 = f32x4{0.f, 0.f, 0.f, 0.f}, c1 = c0;
#pragma unroll
            for (int i = 0; i < 10; ++i) { c0 = mfma16(af[0][i], pf[tt][i], c0); c1 = mfma16(af[1][i], pf[tt][i], c1); }
            *LDSP(f32x4, wa + tt * 2304) = c0;
            *LDSP(f32x4, wa + tt * 2304 + 64) = c1;
            const f32x4 c = c0 + c1;
            chk += (c[0] + c[1]) + (c[2] + c[3]);
            PIN();
        }
        if (it >= 4 || __builtin_amdgcn_ballot_w64((__float_as_uint(chk) & 0x7fffffffu) > 0x7f800000u) == 0) break;
        if (s < NSEG - 1) nap(60);
    }
    barrier();
    const u32 ra = lds + (u32)(tid >> 3) * 144 + (tid & 7) * 16;
    const float gt = __uint_as_float(gv), g1 = rbf(1.f - gt);
    const f32x4 z = (*LDSP(f32x4, ra) + *LDSP(f32x4, ra + 4608)) + (*LDSP(f32x4, ra + 2 * 4608) + *LDSP(f32x4, ra + 3 * 4608));
    u32x2 ov;
#pragma unroll
    for (int j = 0; j < 2; ++j) {
        const u32 b = bb[j], a = cvtpk(z[2 * j], z[2 * j + 1]);
        const u32 ga = cvtpk(gt * __uint_as_float(a << 16), gt * __uint_as_float(a & 0xffff0000u));
        const u32 gb = cvtpk(g1 * __uint_as_float(b << 16), g1 * __uint_as_float(b & 0xffff0000u));
        ov[j] = cvtpk(__uint_as_float(ga << 16) + __uint_as_float(gb << 16), __uint_as_float(ga & 0xffff0000u) + __uint_as_float(gb & 0xffff0000u));
    }
    *(u32x2*)(p.out + tok * 1280 + col * 2) = ov;
}

DI float dot2bf(u32 a, u32 b, float c) { return __builtin_amdgcn_fdot2_f32_bf16(__builtin_bit_cast(bf16x2, a), __builtin_bit_cast(bf16x2, b), c, false); }
DI void battn_task(const Ptrs& p, int t0, u32 lds, int w) {
    const int lane = lanex(), tid = 64 * w + lane, tl = tid & 31, t = t0 + tl;
    const char* const qkv = p.ws + OFF_QKVB + (size_t)(t0 >> 5) * 122880;
    {
        u32 so[3];
#pragma unroll
        for (int i = 0; i < 3; ++i) {
            const int e = 64 * i + lane, sl = e % 48;
            so[i] = (u32)((e / 48) * 512 + (sl < 16 ? (t0 ? 0 : 122880) + 256 + sl * 16 : 122880 - 256 + sl * 16));
        }
#pragma unroll
        for (int i = 0; i < 30; ++i) { glds16c(qkv - 122880 + 40960 + (10 * w + i / 3) * 2048, so[i % 3], lds + (30 * w + i) * 1024); gap<SMB>(-1); }
    }
    u32x4 q[2][5], xv[10], gv[10];
    sfor<0, 10>([&](auto I) { gld16c<(I.v % 5) * 512>(q[I.v / 5][I.v % 5], qkv + (I.v / 5) * 20480, (u32)((tid >> 5) * 2560 + tl * 16)); gap<SMB>(-1); });
    sfor<0, 10>([&](auto I) { gld16<I.v * 128>(xv[I.v], p.x + (size_t)t0 * 1280, (u32)((tid >> 3) * 1280 + (tid & 7) * 16)); gld16<I.v * 128>(gv[I.v], p.gw, (u32)((tid & 7) * 16)); gap<SMB>(-1); });
    sfor<0, 10>([&](auto I) { waitv<0>(q[I.v / 5][I.v % 5]); waitv<0>(xv[I.v]); waitv<0>(gv[I.v]); });
    barrier();
    {
        float a = 0.f;
#pragma unroll
        for (int i = 0; i < 10; ++i)
#pragma unroll
            for (int j = 0; j < 4; ++j) {
                const u32 r = cvtpk(__uint_as_float(xv[i][j] << 16) * __uint_as_float(gv[i][j] << 16), __uint_as_float(xv[i][j] & 0xffff0000u) * __uint_as_float(gv[i][j] & 0xffff0000u));
                a += __uint_as_float(r << 16) + __uint_as_float(r & 0xffff0000u);
            }
        a += __shfl_xor(a, 1); a += __shfl_xor(a, 2); a += __shfl_xor(a, 4);
        const float z = rbf(a);
        if ((tid & 7) == 0) gst4c(p.ws + OFF_GATE, (u32)((t0 + (tid >> 3)) * 4), __float_as_uint(rbf(1.0f / (1.0f + __expf(-z)))));
    }
#pragma unroll
    for (int it = 0; it < 2; ++it) {
        const int m = (tid >> 5) + 8 * it;
        u32 ka = lds + (u32)(m * 3840 + (2 + tl) * 16), va = ka + 61440;
        asm volatile("" : "+v"(ka), "+v"(va));
        u32x4 k[5][8];
#pragma unroll
        for (int i = 0; i < 40; ++i) k[i >> 3][i & 7] = ldsr16(ka + (i >> 3) * 768 + (7 - (i & 7)) * 32);
        PIN();
        float lg[2][8], mx[2] = {-3.0e38f, -3.0e38f};
#pragma unroll
        for (int i = 0; i < 16; ++i) lg[i >> 3][i & 7] = 0.f;
#pragma unroll
        for (int i = 0; i < 5; ++i) {
#pragma unroll
            for (int j = 0; j < 4; ++j)
#pragma unroll
                for (int wv = 0; wv < 8; ++wv) lg[4 * i + j >= 10][wv] = dot2bf(q[it][i][j], k[i][wv][j], lg[4 * i + j >= 10][wv]);
            PIN();
#pragma unroll
            for (int wv = 0; wv < 8; ++wv) k[i][wv] = ldsr16(va + i * 768 + (7 - wv) * 32);
            PIN();
        }
#pragma unroll
        for (int i = 0; i < 16; ++i) {
            float& l = lg[i >> 3][i & 7];
            l = rbf(rbf(l) * 0.22360679774997896f);
            asm volatile("" : "+v"(l));
            l = t - 2 * (i & 7) >= 0 ? l : -3.0e38f;
            mx[i >> 3] = fmaxf(mx[i >> 3], l);
        }
        u32 pk[2][4];
#pragma unroll
        for (int u = 0; u < 2; ++u) {
            float sum = 0.f;
#pragma unroll
            for (int wv = 0; wv < 8; ++wv) { lg[u][wv] = __builtin_amdgcn_exp2f((lg[u][wv] - mx[u]) * 1.4426950408889634f); sum += lg[u][wv]; }
            const float inv = __builtin_amdgcn_rcpf(sum);
#pragma unroll
            for (int wv = 0; wv < 4; ++wv) pk[u][wv] = cvtpk(lg[u][2 * wv] * inv, lg[u][2 * wv + 1] * inv);
        }
#pragma unroll
        for (int i = 0; i < 5; ++i) {
            float o[8];
#pragma unroll
            for (int d = 0; d < 8; ++d) o[d] = 0.f;
#pragma unroll
            for (int wp = 0; wp < 4; ++wp) {
                const u32x4 v0 = k[i][2 * wp], v1 = k[i][2 * wp + 1];
#pragma unroll
                for (int j = 0; j < 4; ++j) {
                    const u32 pw = pk[4 * i + j >= 10][wp];
                    o[2 * j] = dot2bf(__builtin_amdgcn_perm(v1[j], v0[j], 0x05040100u), pw, o[2 * j]);
                    o[2 * j + 1] = dot2bf(__builtin_amdgcn_perm(v1[j], v0[j], 0x07060302u), pw, o[2 * j + 1]);
                }
            }
            gst16c(p.ws + OFF_OB + (size_t)(t0 >> 5) * 40960 + 512 * i, (u32)(m * 2560 + tl * 16), u32x4{cvtpk(o[0], o[1]), cvtpk(o[2], o[3]), cvtpk(o[4], o[5]), cvtpk(o[6], o[7])});
        }
    }
}

#define XPF 1
#define PFQ 0
#define PFN 8
#define OLAG 3
constexpr int QN = 176, QF = 19;
struct QTab { u16 e[QN]; int n, nf; u32 tt[64]; };
typedef const __attribute__((address_space(4))) int* KI;
constexpr int QT_OFF = (88 + 48) / 4;
DI int pick_task(char* fl, KI k, int gr) {
    u32 r;
    asm volatile("s_mov_b64 exec, 1\n\ts_nop 3\n\tglobal_atomic_add %0, %1, %2, %3 sc0 sc1\n\ts_mov_b64 exec, -1\n\ts_waitcnt vmcnt(0)"
                 : "=&v"(r) : "v"((F_QH + gr) * 128), "v"(1), "s"(fl) : "memory");
    const int x = uni((int)r) + 1 + QF;
    if (x >= k[QT_OFF + QN / 2]) return -1;
    const u32 e2 = (u32)k[QT_OFF + (x >> 1)];
    return (int)(x & 1 ? e2 >> 16 : e2 & 0xffffu);
}
__global__ __launch_bounds__(256, 1) void k_main(Ptrs p0, ChainCfg cfg, QTab tab) {
    extern __shared__ __attribute__((aligned(16))) char smem[];
    u32 lds = (u32)(size_t)LDSP(char, smem);
#define HALF 0
    int n = HALF ? (blockIdx.x & 4) * 32 + (int)(blockIdx.x >> 3) * 4 + (blockIdx.x & 3) : blockIdx.x, gr = n & 7;
    int w = uni(threadIdx.x >> 6);
    {
        u64 pc; int len;
        asm volatile("s_getpc_b64 %0\n.Lkm_first:\n\ts_mov_b32 %1, .Lkm_last-.Lkm_first\n\ts_max_i32 %1, %1, .Lkm_lu-.Lkm_first\n\ts_max_i32 %1, %1, .Lkm_lv-.Lkm_first\n\t"
                     "s_max_i32 %1, %1, .Lkm_g2-.Lkm_first\n\ts_max_i32 %1, %1, .Lkm_g4-.Lkm_first" : "=s"(pc), "=s"(len) : : "scc");
        if (w == 3) pfl((const char*)pc, (u32)min((int)(blockIdx.x >> 3) * 8192 + (int)(threadIdx.x & 63) * 128, max(len, 0)), lds);
    }
    karg_warm();
    if (n < NH) { score_role(p0, cfg, n, lds, w); return; }
    if (n < NS) { proj_role(p0, cfg, n - NH, lds, w); return; }
    const bool isv = n < NS + NVB;
#define qslot (lds + GRING * GBUF)
    int sdw = -1;
    int en = isv ? Q_VD + (n & 24) : tab.e[(n - NS - NVB) >> 3] & 511;
    u32 phi0 = 0;
    Sync y;
    if (XPF && n < 160 && w >= 2) pfl(p0.x, (u32)min(((n - NS) >> 3) * 128 + (w & 1) * 64 + lanex(), 1279) * 128, lds);
    if (n >= 160) {
        const int lane = lanex();
        if (n >= 192 && (n & 8)) {
            init_task(p0, (n & 7) + ((n - 192) >> 4) * 8, lds, w);
            barrier();
#pragma unroll
            for (int i = 0; i < 3; ++i) pfl(p0.wqkvb, (u32)min((((n - 192) >> 4) * 8 + (n & 7)) * 768 + i * 256 + w * 64 + lane, 19199 + 0) * 128, lds);
            pfl(p0.wob, (u32)min((((n - 192) >> 4) * 8 + (n & 7)) * 256 + w * 64 + lane, 6399) * 128, lds);
        } else {
            const int h = n < 192 ? n & 31 : (n & 7) + ((n - 192) >> 4) * 8;
            xcopy_task(p0, h + (n < 192 ? 0 : 32), w);
#pragma unroll
            for (int i = 0; i < PFN; ++i) {
                if (n >= 192) break;
                const int l = 64 * min(i * 4 + w, 29) + lane;
                pfl(p0.wo + (size_t)h * 245760, (u32)(l * 128), lds);
                if (PFQ) pfl(p0.wq + h * 384, (u32)((l / 3) * 12288 + (l % 3) * 128), lds);
            }
        }
        waitvm<0>();
        if (w == 0) {
            sync_ld(y, p0.ws);
            waitvm<0>();
            *LDSP(u32, qslot) = (u32)sync_end(y, p0.ws, phi0, true);
            xcopy_wait(p0.ws, sync_tag(y.e));
        }
        barrier();
        sdw = uni((int)*LDSP(u32, qslot));
        barrier();
    }
    while (true) {
        typedef char* const __attribute__((address_space(4)))* KP;
        KP kp = (KP)__builtin_amdgcn_kernarg_segment_ptr();
        asm volatile("" : "+s"(kp), "+s"(lds), "+s"(w), "+s"(gr), "+s"(n));
        __builtin_assume(w >= 0 && w < 4 && gr >= 0 && gr < 8);
        const Ptrs p{kp[0], kp[1], kp[2], kp[3], kp[4], kp[5], kp[6], kp[7], kp[8], kp[9], kp[10]};
        const int ty = en & 7, gate = en >> 9;
        const bool fu = ty == Q_PX && (en >> 3 & 63) < 8;
        const int ix = fu ? (en & 504) + gr : en >> 3 & 63, sf = ((KI)kp)[QT_OFF + QN / 2 + 1] & 255;
        char* const fl = p.ws + OFF_ST;
        const bool raw = sdw < 0;
        int t0 = gate - 1, f1 = F_ONE, t1 = 0, f2 = F_ONE, t2 = 0, f3 = F_ONE, t3 = 0, dt = cfg.T2;
        const int os = ix >= 40 ? 44 + (8 * ix + gr) / 20 : (8 * ix + gr) / 5;
        const int bb = ty == Q_BO ? (8 * ix + gr) >> 2 : os;
        if (ty == Q_BA) { f1 = F_B + (gr & RM) | ix << 16; t1 = 5; f2 = F_B + (gr & RM) | max(ix - 1, 0) << 16; t2 = 5; }
        if (ty == Q_BO) { f1 = F_B + (gr & RM) | (8 + bb) << 16; t1 = 8; }
        if (ty == Q_PX) { f1 = F_Q + pblk(ix) * 8 + (fu ? gr & RM : gr) | fu << 16; t1 = fu ? 32 : 4; if (fu) t0 += gr * (((KI)kp)[QT_OFF + QN / 2 + 1] >> 8) >> 3; if (ix < PXT) t0 = max(t0, ix + 1); }
        if (ty == Q_OUT) { f3 = F_B + (gr & RM) | (16 + (os >> 3)) << 16; t3 = 4; if (os < 60) { f1 = F_PX + os; t1 = 8; t0 = max(t0, os + (os < PXT ? OLAG : 1)); if (os >= PXT) dt = cfg.s1; } else { f1 = F_VD + 64 + (gr & RM); t1 = 32; } }
        const bool skip = fu ? ix >= sf : ty == Q_OUT && ix < 40 && (os < sf || os >= 60);
        if ((gate || ty >= Q_BA) && !skip) wg_wait(fl, w, F_CUR + gr, t0, f1, t1, f2, t2, f3, t3, dt, false);
        int sig = F_END, nrep = 1;
        if (skip) {}
        else if (ty <= Q_QKVB || ty == Q_BO) {
            const bool ab = ty <= Q_KQ;
            sig = ty == Q_VD ? F_VD + (ix >> 2) * 8 : (ty == Q_KQ ? F_Q + (ix >> 2) * 8 : (ix & 4 ? F_Q : F_K + 8 * ix)) + gr;
            if (!ab) sig = F_B | (bb + (ty == Q_BO ? 16 : 0)) << 16;
            nrep = ty == Q_VD || !ab ? RM + 1 : 1;
            if (ty == Q_VD && ix < 4) { sig += n & 32 ? 4 : 0; nrep = 4; }
            run_gemm(p, ty, ab ? ix >> 2 : bb, ab ? gr + 8 * (ix & 3) : 8 * ix + gr - (ty == Q_BO ? 4 : 5) * bb, (n >> 5) & 1, raw, y, lds, w);
            if (raw) sdw = sync_end(y, p.ws, phi0, w == 0);
        } else {
            if (ty == Q_BA) {
                battn_task(p, (8 * ix + gr) * 32, lds, w);
                sig = F_B | (8 + ix) << 16; nrep = RM + 1;
            } else if (ty == Q_PX) {
                if (ix < PXT) {
#pragma nounroll
                    for (int i = fu ? 0 : gr; i < (fu ? 8 : gr + 1); ++i) px_wave<false>(p, cfg, ix, i + 8 * w, ix - cfg.fast, w, lds, fu);
                } else px_wave<true>(p, cfg, ix, gr + 8 * w, -1, w, lds, false);
                sig = F_PX + ix;
                if (fu) wg_wait(fl, w, F_ONE, 0, F_B + (gr & RM) | (16 + (ix >> 3)) << 16, 4, F_ONE, 0, F_ONE, 0, dt, false);
            }
            if (ty == Q_OUT || fu) {
                const int cg = 8 * ix + gr - 5 * os;
                if (ix >= 40 && !fu) out_tail(p, cfg, os, 8 * ix + gr - 20 * (os - 44), lds, w, sdw, gr);
                else out_task(p, fu ? ix : os, fu ? 0 : cg, fu ? 5 : cg + 1, lds, (fu ? ix : os) + 1 - cfg.fast, w, sdw);
                sig = F_END;
            }
            waitvm<0>();
        }
        barrier();
        if (w == 0 && sig != F_END) flag_add(fl, sig, nrep);
        if (w == 1 && sig >= F_Q && sig < F_N) flag_add(fl, sig - gr | 1 << 16, RM + 1);
        if (raw && !isv && w == 0) xcopy_wait(p.ws, sync_tag(y.e));
        if (isv) break;
        if (w == 0) *LDSP(int, qslot) = pick_task(fl, (KI)kp, gr);
        barrier();
        en = uni(*LDSP(int, qslot));
        if (en < 0) return;
        barrier();
    }
    value_role(p0, cfg, n - NS, sdw, phi0, lds, w);
}

#define QCFG 14, 11, 3, 5, 0, 50, 36
#define LVDEF "00@@@?@>=<<<<<<<<;9889;<<<<;::::;<<<<<<<;:99887777789:;;:9888886"
constexpr int DX = 340, DUA = 430, DUB = 0, DUC = 4;
static ChainCfg dev_cfg(ChainCfg c) { c.T = PDL; c.cR = ODL; c.s1 = TDT; c.T2 = WSLEEP; c.cR2 = DX; c.dU = DUA; c.s2 = DUB; c.T3 = DUC; c.cR3 = 4 | 4 << 8 | 0 << 16; return c; }
static QTab set_sched(QTab t, const ChainCfg c, const char* lv) {
    for (int s = 0; s < 64; ++s) {
        const int d = lv[s] - '0';
        t.tt[s] = (u32)((s < c.s1 ? c.T : (s < c.s2 ? c.T2 : c.T3)) + 4 * d) | (u32)((s < c.s1 ? c.cR : (s < c.s2 ? c.cR2 : c.cR3)) + 5 * d / 2) << 16;
    }
    t.tt[0] = (u32)270 | (u32)165 << 16;
    t.tt[1] = (u32)230 | (u32)152 << 16;
    return t;
}
#define QORD "bbqV3bK3m44bm55bm66m77m88"
#define GATES "11 13 15 17 22 24 32 36 19 22 29 42 13 17 21 26 33 41"
static QTab make_tab(int lv, int lk, int b0, int d1, int d2, int lg, int nf) {
    QTab t{};
    int n = 0, dl[QN], nq = 0;
    auto add = [&](int ty, int ix) { t.e[n] = (u16)(ty | ix << 3); dl[n++] = ty == Q_VD || ty == Q_KQ ? 8 * (ix / 4 - 1) - (ty == Q_VD ? lv : lk) - (ix >= 32 ? 0 : 0) : 99; };
    for (int i = 1; i < 4; ++i) add(Q_VD, i);
    for (int i = 0; i < 16; ++i) add(i < 4 ? Q_H : ((i & 8) ? Q_VD : Q_KQ), i < 8 ? i : 4 + (i & 1) * 4 + ((i - 8) >> 1));
    for (int i = 0; i < 4; ++i) add(Q_KQ, 8 + i);
    for (const char* c = QORD; *c; ++c) {
        if (*c == 'b') add(Q_QKVB, nq++);
        else if (*c == 'q') for (int i = 4; i < 8; ++i) add(Q_H, i);
        else if (*c == '.') dl[n - 1] |= 1 << 20;
        else if (*c == 'n') { const int b = *++c - '0'; for (int i = 0; i < 8; ++i) add(i & 1 ? Q_KQ : Q_VD, 4 * b + (i >> 1) - (i & 1) * 2); }
        else if (*c == 'h' || *c == 'e') { const int b = 4 * (c[1] - '0') + (*c == 'e') * 2; ++c; add(Q_KQ, b); add(Q_KQ, b + 1); }
        else if (*c == 'm') { const int b = c[1] - '0', b2 = c[2] - '0'; c += 2; for (int i = 0; i < 8; ++i) add(i & 1 ? Q_KQ : Q_VD, 4 * (i & 1 ? b2 : b) + ((i >> 1) ^ (i & 2 ? 0 : 0))); }
        else {
            const int ty = (*c | 32) == 'v' ? Q_VD : Q_KQ, two = *c >> 5 & 1, b = *++c - '0';
            for (int i = 0; i < (4 << two); ++i) add(ty, 4 * (b + (i & two)) + (i >> two));
        }
    }
    const int gap = 8;
    for (int i = n - 1, m = 999; i >= 0; --i) { const int pz = dl[i] > 999; m = min(8 * (pz ? dl[i] - (1 << 20) : dl[i]), m - gap - 8 * pz); dl[i] = max(m, 0); }
    auto put = [&](int ty, int ix, int g, int pos) {
        int i = n++;
        for (; dl[i - 1] > pos; --i) { t.e[i] = t.e[i - 1]; dl[i] = dl[i - 1]; }
        t.e[i] = (u16)(ty | ix << 3 | (g + 1) << 9); dl[i] = pos;
    };
    for (int i = 0; i < n; ++i) if (dl[i] >= 8) t.e[i] |= (u16)((dl[i] / 8 + 1) << 9);
    const char* gs = GATES;
    auto gt = [&](int i) { return 10 * (gs[3 * i] - '0') + gs[3 * i + 1] - '0'; };
    for (int i = 0; i < 8; ++i) put(Q_BA, i, gt(i), 8 * gt(i));
    for (int i = 0; i < 4; ++i) put(Q_BO, i, gt(8 + i), 8 * gt(8 + i));
    for (int i = 0; 8 * i < nf; ++i) put(Q_PX, i, gt(12 + i), 8 * (max(gt(12 + i), 8 * i + 1) + b0));
    auto pg = [&](int s) { return max(8 * s + 8, 8 * 48 + (s - nf) * 3); };
    for (int s = nf; s < NSEG; ++s) { const int g = s < PXT ? pg(s) : 8 * max(s - 3, lg); put(Q_PX, s, g / 8, g); }
    for (int x = 5 * nf / 8; x < 38; ++x) { const int os = (8 * x + 4) / 5, g = pg(min((8 * x + 7) / 5, PXT - 1)); put(Q_OUT, x, g > 8 * ((8 * x + 7) / 5) + 8 ? g / 8 + OLAG - 1 : 0, os < PXT ? pg(os) + 8 * (OLAG - 1) : 8 * (os + 1)); }
    for (int x = 40; x < 50; ++x) { const int g = max(44 + 8 * x / 20 - 3, lg); put(Q_OUT, x, g, 8 * g + 4); }
    t.n = n; t.nf = nf | d1 << 8;
    return t;
}

extern "C" void kernel_launch(void* const* d_in, const int* in_sizes, int n_in, void* d_out, int out_size, void* d_ws,
                              size_t ws_size, hipStream_t stream) {
    Ptrs p{(const char*)d_in[0], (const char*)d_in[1], (const char*)d_in[2], (const char*)d_in[3], (const char*)d_in[4], (const char*)d_in[5],
           (const char*)d_in[8], (const char*)d_in[9], (const char*)d_in[10], (char*)d_out, (char*)(((size_t)d_ws + 0x1fffff) & ~(size_t)0x1fffff)};
    (void)hipFuncSetAttribute((const void*)k_main, hipFuncAttributeMaxDynamicSharedMemorySize, S_LDS);
    k_main<<<256, 256, S_LDS, stream>>>(p, dev_cfg(ChainCfg{CFG}), set_sched(make_tab(QCFG), ChainCfg{CFG}, LVDEF));
    static_assert(sizeof(Ptrs) + sizeof(ChainCfg) + sizeof(QTab) == TT_OFF + 256, "");
}
```

```cpp
#include <hip/hip_runtime.h>
#pragma clang diagnostic ignored "-Wint-to-pointer-cast"

typedef unsigned short u16;
typedef unsigned int u32;
typedef unsigned long long u64;
typedef __bf16 bf16x8 __attribute__((ext_vector_type(8)));
typedef __bf16 bf16x16 __attribute__((ext_vector_type(16)));
typedef short s16x4 __attribute__((ext_vector_type(4)));
typedef float f32x4 __attribute__((ext_vector_type(4)));
typedef unsigned u32x4 __attribute__((ext_vector_type(4)));
typedef unsigned u32x2 __attribute__((ext_vector_type(2)));
typedef unsigned u32x8 __attribute__((ext_vector_type(8)));
typedef unsigned u32x16 __attribute__((ext_vector_type(16)));
typedef unsigned u32x32 __attribute__((ext_vector_type(32)));
#define DI __device__ __forceinline__
#define LDSP(T, a) ((__attribute__((address_space(3))) T*)(a))

constexpr int S_LEN = 2048, DM = 640, NH = 32, DH = 192, NSEG = 64, HD = NH * DH;
#define NSAMP 4
constexpr int NREG = 13;
constexpr int NV = 80, NVB = 40;
constexpr int NS = 64;
#define CFG 147, 105, 104, 20, 57, 16, 147, 105, 52, 147, 105, 6

constexpr size_t U_STRIDE = NV * 4096, D_STRIDE = NV * 16384;
constexpr size_t HO_STRIDE = 65536;
constexpr size_t OFF_ST = NSEG * HO_STRIDE;
enum { F_ACK = 0, F_ONE = 3, F_VD = 16, F_QH = 96, F_B = 104, F_CUR = 128, F_PROG = 136 - 8 * 62, F_UD = 152, F_PX = 160, F_K = 224, F_END = 512, F_Q = 520, F_N = 592 };
constexpr size_t OFF_LAT = OFF_ST + F_N * 128;
constexpr size_t OFF_KX = 5u << 20;
constexpr size_t OFF_QX = OFF_KX + (size_t)S_LEN * HD * 2;
constexpr size_t OFF_D = OFF_QX + (size_t)S_LEN * HD * 2;
constexpr size_t OFF_U = (OFF_D + NSEG * D_STRIDE + 0x1fffff) & ~(size_t)0x1fffff;
constexpr size_t OFF_KM = OFF_U + (22u << 20);
constexpr size_t OFF_PX = OFF_KM + (size_t)NSEG * NH * 3072;
constexpr size_t OFF_QKVB = OFF_PX + (size_t)NSEG * 81920;
constexpr size_t OFF_OB = OFF_QKVB + (size_t)S_LEN * 1920 * 2;
constexpr size_t OFF_BOUT = OFF_OB + (size_t)S_LEN * DM * 2;
constexpr size_t OFF_GATE = OFF_BOUT + (size_t)S_LEN * DM * 2;
constexpr size_t OFF_XF = OFF_GATE + S_LEN * 4;
constexpr size_t WS_END = OFF_XF + (size_t)S_LEN * DM * 2;
static_assert(WS_END + (2u << 20) <= (256u << 20), "workspace");

typedef __bf16 bf16x2 __attribute__((ext_vector_type(2)));
typedef float f32x2 __attribute__((ext_vector_type(2)));
DI u32 cvtpk(float lo, float hi) { return __builtin_bit_cast(u32, __builtin_convertvector(f32x2{lo, hi}, bf16x2)); }
DI float rbf(float f) { return __uint_as_float(cvtpk(f, f) << 16); }
DI f32x4 mfma16(u32x4 a, u32x4 b, f32x4 c) {
    return __builtin_amdgcn_mfma_f32_16x16x32_bf16(__builtin_bit_cast(bf16x8, a), __builtin_bit_cast(bf16x8, b), c, 0, 0, 0);
}
DI u32x2 trread(u32 a) {
    return __builtin_bit_cast(u32x2, __builtin_amdgcn_ds_read_tr16_b64_v4i16(LDSP(s16x4, a)));
}
DI void glds16c(const char* sbase, u32 voff, u32 lds) {
    u32 sv;
    asm volatile("s_mov_b32 %0, m0\n\ts_mov_b32 m0, %3\n\ts_nop 0\n\tglobal_load_lds_dwordx4 %1, %2 sc1\n\ts_mov_b32 m0, %0"
                 : "=&s"(sv) : "v"(voff), "s"(sbase), "s"(lds) : "memory");
}
template <int IMM>
DI void gld16(u32x4& r, const char* sbase, u32 voff) {
    asm volatile("s_nop 4\n\tglobal_load_dwordx4 %0, %1, %2 offset:%3" : "=v"(r) : "v"(voff), "s"(sbase), "i"(IMM) : "memory");
}
DI void gst16(char* sbase, u32 voff, u32x4 v) {
    asm volatile("global_store_dwordx4 %0, %1, %2 sc1\n\ts_nop 1" : : "v"(voff), "v"(v), "s"(sbase) : "memory");
}
#define OST " nt"
DI void gst16p(char* sbase, u32 voff, u32x4 v) {
    asm volatile("s_nop 4\n\tglobal_store_dwordx4 %0, %1, %2" OST "\n\ts_nop 1" : : "v"(voff), "v"(v), "s"(sbase) : "memory");
}
DI void gst8(char* sbase, u32 voff, u32x2 v) {
    asm volatile("global_store_dwordx2 %0, %1, %2 sc1\n\ts_nop 1" : : "v"(voff), "v"(v), "s"(sbase) : "memory");
}
template <int N>
DI void waitvm() { asm volatile("s_waitcnt vmcnt(%0)" : : "i"(N) : "memory"); }
DI void barrier() { asm volatile("s_waitcnt lgkmcnt(0)\n\ts_barrier" : : : "memory"); }
#define PIN() __builtin_amdgcn_sched_barrier(0)
DI int uni(int v) { return __builtin_amdgcn_readfirstlane(v); }
DI int lanex() {
    int t;
    asm volatile("v_mbcnt_lo_u32_b32 %0, -1, 0\n\tv_mbcnt_hi_u32_b32 %0, -1, %0" : "=v"(t));
    __builtin_assume(t >= 0 && t < 64);
    return t;
}
DI void flag_ld(u32& r, const char* fl, int idx) {
    asm volatile("s_nop 4\n\tglobal_load_dword %0, %1, %2 sc1" : "=v"(r) : "v"((idx & 0xffff) * 128 + (idx >> 14)), "s"(fl) : "memory");
}
DI void flag_ldm(u32& r, const char* fl, int idx, bool on) {
    asm volatile("v_mov_b32 %0, 1000\n\ts_mov_b32 exec_lo, %3\n\ts_mov_b32 exec_hi, 0\n\tglobal_load_dword %0, %1, %2 sc1\n\ts_mov_b64 exec, -1" : "=&v"(r) : "v"(idx * 128), "s"(fl), "s"(uni(on ? 1 : 0)) : "memory");
}
DI void flag_wait(const char* fl, int idx, int target) {
#pragma nounroll
    for (int it = 0; it < (1 << 16); ++it) {
        u32 v;
        flag_ld(v, fl, idx);
        asm volatile("s_waitcnt vmcnt(0)" : "+v"(v) : : "memory");
        if (uni((int)(v + 1u)) >= target) break;
        __builtin_amdgcn_s_sleep(6);
    }
}
DI void flag_add(char* fl, int idx, int nrep) {
    const int lane = lanex();
    if (lane < nrep) asm volatile("s_nop 4\n\tglobal_atomic_add %0, %1, %2 sc1" : : "v"(((idx & 0xffff) + lane) * 128 + (idx >> 14)), "v"(1), "s"(fl) : "memory");
}
DI void acquire() { asm volatile("buffer_inv sc1\n\ts_waitcnt vmcnt(0)" : : : "memory"); }

template <int V> struct IC { static constexpr int v = V; };
template <int B, int E, class Fn>
DI void sfor(Fn&& fn) { if constexpr (B < E) { fn(IC<B>{}); sfor<B + 1, E>(fn); } }
enum { T_QK = 0, T_VD = 1, T_BL = 3 };
constexpr int GBUF = 24576, GRING = 6;
struct GemmArgs {
    const char *w1, *w1b;
    const char* w2;
    const char* x;
    char *out, *outb;
    int ldw;
    char* sig;
    int type, np1, tn;
    int sV, sP, sQ;
    int xb;
};
DI const char* unip(const char* p) {
    const u64 v = (u64)p;
    return (const char*)(((u64)(u32)uni((int)(v >> 32)) << 32) | (u32)uni((int)v));
}
template <int Z>
DI void mf1(f32x4& c, const u32x4& a, const u32x4& b) {
    if constexpr (Z) asm volatile("v_mfma_f32_16x16x32_bf16 %0, %1, %2, 0" : "=&v"(c) : "v"(a), "a"(b));
    else asm volatile("v_mfma_f32_16x16x32_bf16 %0, %1, %2, %0" : "+v"(c) : "v"(a), "a"(b));
}
template <int Z>
DI void mf2(f32x4& c, const u32x4& a, const u32x4& b) {
    if constexpr (Z) asm volatile("v_mfma_f32_16x16x32_bf16 %0, %1, %2, 0" : "=&v"(c) : "v"(a), "v"(b));
    else asm volatile("v_mfma_f32_16x16x32_bf16 %0, %1, %2, %0" : "+v"(c) : "v"(a), "v"(b));
}
template <int IMM>
DI void gld16a(u32x4& r, const char* sbase, u32 voff) {
    asm volatile("global_load_dwordx4 %0, %1, %2 offset:%3 sc1" : "+a"(r) : "v"(voff), "s"(sbase), "i"(IMM) : "memory");
}
DI void setm0(u32 v) { asm volatile("s_mov_b32 m0, %0" : : "s"(v) : "memory"); }
template <int IMM>
DI void gldsm(const char* sbase, u32 voff) {
    asm volatile("global_load_lds_dwordx4 %0, %1 offset:%2" : : "v"(voff), "s"(sbase), "i"(IMM) : "memory");
}
DI void gst16n(char* sbase, u32 voff, u32x4 v) {
    asm volatile("global_store_dwordx4 %0, %1, %2 sc1" : : "v"(voff), "v"(v), "s"(sbase) : "memory");
}
template <int N>
DI void waitbar() { asm volatile("s_waitcnt vmcnt(%0)\n\ts_barrier" : : "i"(N) : "memory"); }
#define LGKM(n) __builtin_amdgcn_s_waitcnt(0xC07F | ((n) << 8))

#define XSL 10
DI int xhalf() { u32 x; asm volatile("s_getreg_b32 %0, hwreg(HW_REG_XCC_ID)" : "=s"(x)); return (int)((x >> 2) & 1); }
struct Sync { u32x2 r, g, e; };
DI void sync_ld(Sync& y, const char* ws) {
    asm volatile("s_nop 4\n\tglobal_load_dwordx2 %0, %3, %5 sc1\n\tglobal_load_dwordx2 %1, %4, %5 offset:512 sc1\n\tglobal_load_dwordx2 %2, %4, %5 offset:1024 sc1"
                 : "=&v"(y.r), "=&v"(y.g), "=&v"(y.e) : "v"((lanex() & 31) * 8), "v"(0), "s"(ws + OFF_LAT) : "memory");
}
DI u64 sync_tag(u32x2 e) { return (((u64)e[1] << 32 | e[0]) + 1) << 1; }
DI int sync_end(Sync& y, const char* ws, u32& phi, bool ack) {
    asm volatile("" : "+v"(y.r), "+v"(y.g), "+v"(y.e));
#pragma nounroll
    for (int it = 0; it < (1 << 16); ++it) {
        const u64 t = sync_tag(y.e);
        if (__builtin_amdgcn_ballot_w64((y.r[0] & ~1u) != (u32)t || y.r[1] != (u32)(t >> 32) || y.g[0] != (u32)t) == 0) break;
        __builtin_amdgcn_s_sleep(XSL);
        sync_ld(y, ws);
        asm volatile("s_waitcnt vmcnt(0)" : "+v"(y.r), "+v"(y.g), "+v"(y.e) : : "memory");
    }
    if (ack) flag_add((char*)ws + OFF_ST, F_ACK, 1);
    phi = (u32)uni((int)y.g[1]);
    const u32 b = (u32)__builtin_amdgcn_ballot_w64(y.r[0] & 1), all = __builtin_popcount(b) > 16;
    u32 q = 0;
#pragma unroll
    for (int r = 0; r < NREG; ++r) {
        const u32 c = r < 2 ? __builtin_popcount(b & (0x55u << r)) + all : 2 * __builtin_popcount((b >> (4 + 2 * r)) & 3) + all;
        q |= ((c > 2) ^ (u32)((r < 2 ? r : r + (OFF_U >> 21)) & 1)) << r;
    }
    return uni((int)(q ^ 3u * xhalf()));
}
DI void pace(int on) {
    asm volatile("s_cmp_ge_i32 %0, 0\n\ts_cbranch_scc1 .Lp%=\n\ts_waitcnt vmcnt(0)\n.Lp%=:" : : "s"(on) : "memory", "scc");
}
#define SMO 48
#define RM 7
#define BLS 1
#define EPG 1
#define PXT 58
#define PDX 40
#define SPL 0
#define HP0 48
#define UPF 5
#define BPF 1
#define FRD 25
#define SMB 48
#define SMP SMO
template <int C>
DI void gap(int on) {
    if constexpr (C > 0) asm volatile("s_cmp_ge_i32 %0, 0\n\ts_cbranch_scc1 .Lg%=\n\ts_nop %1\n\ts_nop %2\n\ts_nop %3\n.Lg%=:" : : "s"(on), "n"(C / 12 - 1), "n"(C / 12 - 1), "n"(C / 12 - 1) : "memory", "scc");
}
DI void early_add(char* ctr, int c) {
    asm volatile("s_cmp_lg_u32 %1, 1\n\ts_cbranch_scc1 .Le%=\n\ts_mov_b64 exec, 1\n\ts_nop 1\n\tglobal_atomic_add %2, %3, %0 sc1\n\ts_mov_b64 exec, -1\n.Le%=:"
                 : : "s"(ctr), "s"(c), "v"(0), "v"(1) : "memory", "scc");
}
#define XL 2
template <int NT>
DI void gemm_task(const GemmArgs& g, Sync& y, const char* ws, u32 lds, int w) {
    constexpr int FD = 5, NX = 2 * NT, OPS = NX + 6, BI = 24 - FD;
    const int lane = lanex();
    const int n = lane & 15, q = lane >> 4, qq = (lane >> 2) & 3, pp = lane & 3;
    const bool p2 = g.type == T_VD;
    const int rw = lane >> 3, sp = (lane >> 1) & 3;
    const u32 v2b = (u32)(rw * 1280 + 16 * (lane & 1) + 5120);
    auto v2o = [&](int i) { return v2b + (u32)(((sp ^ (rw >> 1)) << 5) + i * 9216); };
    u32 wv[6];
#pragma unroll
    for (int i = 0; i < 6; ++i)
        wv[i] = (u32)((rw + 8 * (i / 3)) * g.ldw + ((sp ^ (((rw >> 1) & 1) + 2 * (i / 3))) << 5) + 16 * (lane & 1) + (i % 3) * 128 + 5120 - 1024 * i);
    const char* const wp2 = g.w2 + (size_t)(48 * w) * 1280 - 2048;
    const char* wp = unip(g.w1 + (size_t)(16 * w) * g.ldw - 2048);
    int wstep = uni(64 * g.ldw);
    u32 wdst = lds + (u32)w * 6144 + 3072;
    const u32 wdlast = wdst + (GRING - 1) * GBUF;
    auto wring = [&]() { wdst = wdst == wdlast ? wdst - (GRING - 1) * GBUF : wdst + GBUF; };
    auto wload = [&](int i) {
        setm0(wdst);
        wring();
        asm volatile("s_nop 0");
        sfor<0, 6>([&](auto I) { gldsm<1024 * (I.v - 3)>(wp, wv[I.v]); });
        wp += wstep;
    };
    u32x4 vf[NT][6];
    u32x4 fr[FD + 1];
    u32x4 xr[5][NX];
    auto xtie = [&](u32x4 (&x)[NX]) {
#pragma unroll
        for (int i = 0; i < NX; i += 4) asm volatile("" : "+a"(x[i]), "+a"(x[i + 1]), "+a"(x[i + 2]), "+a"(x[i + 3]));
    };
#pragma unroll
    for (int i = 0; i < 5 * NX; ++i) asm volatile("" : "=a"(xr[i / NX][i % NX]));
    u32x2 flo;
    u32 c[4], slot = 0;
    int sd = GBUF;
    auto sring = [&]() { sd = slot == (GRING - 1) * GBUF ? -(GRING - 1) * GBUF : GBUF; slot += (u32)sd; };
    {
        u32 xo[NT];
#pragma unroll
        for (int tt = 0; tt < NT; ++tt) {
            const int tok = (g.type == T_VD) ? 8 * (n >> 2) + 4 * (tt & 1) + (n & 3) : 16 * (tt & 1) + n;
            xo[tt] = (u32)((NT / 2 * w + (tt >> 1)) * 40960 + (g.xb ? q * 512 + tok * 16 : q * 16 + tok * 1280));
        }
        f32x4 acc[12][NT];
        const bool rm = NT == 4 && g.type == T_BL;
        const int sw = (qq >> 1) + 2 * (q & 1);
#pragma unroll
        for (int t = 0; t < 4; ++t)
            c[t] = lds + q * 3072 + qq * 128 + (rm ? ((((t & 2) + (pp >> 1)) ^ sw) << 5) + 16 * (pp & 1) + 8 * ((pp >> 1) ^ (t & 1)) : ((t ^ sw) << 5) + 8 * pp);
        auto rlo = [&](int f) { flo = trread(c[f & 3] + (f / 12) * 12288 + ((f % 12) >> 2) * 1024); };
        auto rhi = [&](int f) {
            const u32x2 hi = trread(c[f & 3] + (f / 12) * 12288 + ((f % 12) >> 2) * 1024 + 512);
            fr[f % (FD + 1)] = u32x4{flo[0], flo[1], hi[0], hi[1]};
        };
        const int xk = uni(g.xb ? 2048 : 64);
        const char *xp = g.x, *xq = xp + xk;
        sfor<0, 4>([&](auto J) {
            if constexpr (J.v < XL) { sfor<0, NX>([&](auto I) { gld16a<0>(xr[J.v][I.v], I.v & 1 ? xq : xp, xo[I.v >> 1]); }); xp += 2 * xk; xq += 2 * xk; }
            wload(J.v);
        });
        waitbar<18 + (XL - 1) * NX>();
        xtie(xr[0]);
#pragma unroll
        for (int i = 0; i < FD; ++i) { rlo(i); rhi(i); }
        char* po = g.out;
#pragma nounroll
        for (int ph = g.np1; ph > 0; --ph) {
            sfor<0, 10>([&](auto J) {
                constexpr int j = J.v;
                u32x4 (&xc)[NX] = xr[j % 5];
                u32x4 (&xn)[NX] = xr[(j + XL) % 5];
                sfor<0, 24>([&](auto I) {
                    constexpr int idx = I.v, kk = idx / 12, T = idx % 12, z = j == 0 && kk == 0, f = (idx + FD) % 24;
                    constexpr int mo = NT == 4 ? (idx < BI && idx % 4 != 2 ? idx - (idx + 1) / 4 : -1) : (idx < BI && idx % 2 == 0 ? idx / 2 : -1);
                    auto mem = [&]() {
                        if constexpr (mo >= NX) gldsm<1024 * (mo - NX - 3)>(wp, wv[mo - NX]);
                        else if constexpr (mo >= 0) gld16a<0>(xn[mo], mo & 1 ? xq : xp, xo[mo >> 1]);
                    };
                    auto misc = [&]() {
                        if constexpr (idx == 1) wring();
                        if constexpr (idx == 13) sring();
                        if constexpr (j == 1 && idx == 22) early_add(g.sig, ph + w);
                        if constexpr (idx >= 15 && idx < BI) { c[idx - 15] += (u32)sd; asm volatile("" : "+v"(c[idx - 15])); }
                        const bool more = ph > 1;
                        if constexpr (j != 5) {
                            if constexpr (idx == 20) wp += wstep;
                        } else {
                            if constexpr (idx == 19) { wp = more ? g.w1b + (size_t)(16 * w) * g.ldw - 2048 : wp2; wstep = more ? wstep : (p2 ? 128 : 0); }
                            if constexpr (idx >= 20 && idx < 23) {
#pragma unroll
                                for (int i = 2 * (idx - 20); i < 2 * (idx - 19); ++i) { wv[i] = more ? wv[i] : (p2 ? v2o(i) : 5120u); asm volatile("" : "+v"(wv[i])); }
                            }
                        }
                        if constexpr (j != 9 - XL) {
                            if constexpr (idx == 21) xp += 2 * xk;
                            if constexpr (idx == 22) xq += 2 * xk;
                        } else if constexpr (idx == 23) {
                            xp = g.x; xq = xp + xk;
#pragma unroll
                            for (int i = 0; i < NT; ++i) { xo[i] = more ? xo[i] : 0u; asm volatile("" : "+v"(xo[i])); }
                        }
                    };
                    if constexpr (idx == BI) { waitbar<6 + (XL - 1) * OPS>(); xtie(xr[(j + 1) % 5]); }
                    if constexpr (idx % 2 == 0) LGKM(6);
                    PIN();
                    const u32x4 a = fr[idx % (FD + 1)];
                    mf1<z>(acc[T][0], a, xc[kk]); PIN();
                    rlo(f);
                    if constexpr (idx == 0) setm0(wdst);
                    if constexpr (NT == 2) rhi(f);
                    PIN();
                    mf1<z>(acc[T][1], a, xc[2 + kk]); PIN();
                    if constexpr (NT == 4) {
                        rhi(f); PIN();
                        mf1<z>(acc[T][2], a, xc[4 + kk]); PIN();
                        mem(); PIN();
                        mf1<z>(acc[T][3], a, xc[6 + kk]); PIN();
                    } else { mem(); PIN(); }
                    misc();
                    PIN();
                });
            });
            asm volatile("s_nop 7\n\ts_nop 1");
            PIN();
#pragma unroll
            for (int tt = 0; tt < NT; ++tt)
#pragma unroll
                for (int ks = 0; ks < 6; ++ks)
                    vf[tt][ks] = u32x4{cvtpk(acc[2 * ks][tt][0], acc[2 * ks][tt][1]), cvtpk(acc[2 * ks][tt][2], acc[2 * ks][tt][3]),
                                       cvtpk(acc[2 * ks + 1][tt][0], acc[2 * ks + 1][tt][1]), cvtpk(acc[2 * ks + 1][tt][2], acc[2 * ks + 1][tt][3])};
            if (g.type == T_QK) {
#pragma unroll
                for (int tt = 0; tt < NT; ++tt)
#pragma unroll
                    for (int ks = 0; ks < 6; ++ks) {
                        gst16(po + (size_t)(NT / 2 * w + (tt >> 1)) * (NH * 12288) + (tt & 1) * 6144 + ks * 1024, lane * 16,
                              u32x4{vf[tt][ks][0], vf[tt][ks][2], vf[tt][ks][1], vf[tt][ks][3]});
                        if (NT == 4 && EPG) asm volatile("s_sleep %0" : : "n"(EPG));
                    }
            } else if (rm) {
                const bool sx = q >= 2;
#pragma unroll
                for (int i = 0; i < 3 * NT; ++i) {
                    const int tp = i / 6, ks = i % 6;
                    u32x4 o[2];
#pragma unroll
                    for (int j = 0; j < 4; ++j) {
                        const u32x4 &v = vf[2 * tp][ks], &u = vf[2 * tp + 1][ks];
                        if (BLS) {
                            const auto r = __builtin_amdgcn_permlane32_swap(v[j], u[j], false, false);
                            o[0][j] = r[0]; o[1][j ^ 2] = r[1];
                        } else { o[0][j] = sx ? v[j ^ 2] : v[j]; o[1][j] = sx ? u[j ^ 2] : u[j]; }
                    }
#pragma unroll
                    for (int j = 0; j < 2; ++j) {
                        gst16(po + (size_t)(2 * w + tp) * g.sV + j * (BLS ? 1024 : 256) + ks * 2048, BLS ? (u32)((q & 1) * 512 + (q >> 1) * 256 + n * 16) : (u32)(q * 512 + n * 16), o[j]);
                        if (EPG) asm volatile("s_sleep %0" : : "n"(EPG));
                    }
                }
            }
            po = g.outb;
            PIN();
        }
    }
    if (p2) {
        constexpr int HI = 2048;
        const int r = 4 * (q & 1) + qq, sw = r >> 1;
#pragma unroll
        for (int t = 0; t < 4; ++t) c[t] = lds + slot + (q >> 1) * 1024 + r * 128 + ((t ^ sw) << 5) + 8 * pp;
        sring();
        u32 so = (u32)((n >> 3) * g.sV + q * g.sQ + (n & 7) * 16);
        char* const o0 = g.out + (size_t)(NT / 2 * w) * g.sP;
        char* const o1 = o0 + g.sP;
        const int sv2 = 2 * g.sV;
        auto rlo = [&](int f) { flo = trread(c[f / 6] + (f % 6) * 4096); };
        auto rhi = [&](int f) {
            const u32x2 hi = trread(c[f / 6] + (f % 6) * 4096 + HI);
            fr[f % (FD + 1)] = u32x4{flo[0], flo[1], hi[0], hi[1]};
        };
        f32x4 a2[2][NT];
        u32 e[NT / 2][4];
        auto cv = [&](f32x4 (&a)[NT], int i) { e[i >> 2][i & 3] = cvtpk(a[i >> 1][2 * (i & 1)], a[i >> 1][2 * (i & 1) + 1]); asm volatile("" : "+v"(e[i >> 2][i & 3])); };
        auto st = [&](int pl) { gst16n(pl ? o1 : o0, so, u32x4{e[pl][0], e[pl][1], e[pl][2], e[pl][3]}); };
        PIN();
#pragma unroll
        for (int i = 0; i < FD; ++i) { rlo(i); rhi(i); }
#pragma unroll
        for (int tt = 0; tt < NT; ++tt) a2[1][tt] = f32x4{0.f, 0.f, 0.f, 0.f};
#pragma nounroll
        for (int t = 0; t < g.tn; ++t) {
            sfor<0, 24>([&](auto I) {
                constexpr int idx = I.v, cb = idx / 6, ks = idx % 6, f = (idx + FD) % 24;
                f32x4 (&ac)[NT] = a2[cb & 1];
                f32x4 (&ap)[NT] = a2[(cb & 1) ^ 1];
                constexpr int mo = cb < 2 ? (ks == 0 ? 3 * cb : (ks >= 4 ? 3 * cb + ks - 3 : -1)) : -1;
                auto mem = [&]() {
                    if constexpr (mo >= 0) gldsm<1024 * (mo - 3)>(wp, wv[mo]);
                    if constexpr (ks == 3 || (NT == 4 && ks == 2)) { if (cb > 0 || t > 0) st(NT == 4 ? ks - 2 : 0); }
                };
                auto misc = [&]() {
                    if constexpr (idx == 12) wring();
                    if constexpr (idx == 23) sring();
                    if constexpr (idx == 16) { wp += t < g.tn - 5 ? 128 : 0; }
                    if constexpr (ks == 0) { c[cb] += (u32)sd; asm volatile("" : "+v"(c[cb])); }
                    if constexpr (ks == 4) { so += (cb > 0 || t > 0) ? (u32)sv2 : 0u; asm volatile("" : "+v"(so)); }
                };
                if constexpr (idx == BI) waitbar<NT == 4 ? 38 : 28>();
                if constexpr (idx % 2 == 0) LGKM(6);
                PIN();
                const u32x4 b = fr[idx % (FD + 1)];
                mf2<ks == 0>(ac[0], vf[0][ks], b); PIN();
                rlo(f);
                if constexpr (idx == 0) setm0(wdst);
                if constexpr (NT == 2) rhi(f);
                if constexpr (NT == 4 && ks >= 1 && ks <= 3) cv(ap, ks == 1 ? 0 : (ks == 2 ? 4 : 7));
                PIN();
                mf2<ks == 0>(ac[1], vf[1][ks], b); PIN();
                if constexpr (NT == 4) {
                    rhi(f);
                    if constexpr (ks == 1 || ks == 2) cv(ap, ks == 1 ? 1 : 5);
                    PIN();
                    mf2<ks == 0>(ac[2], vf[2][ks], b); PIN();
                    mem();
                    if constexpr (ks == 1) { cv(ap, 2); cv(ap, 3); }
                    PIN();
                    mf2<ks == 0>(ac[3], vf[3][ks], b); PIN();
                    if constexpr (ks == 2) cv(ap, 6);
                } else {
                    if constexpr (ks == 1 || ks == 2) { cv(ap, 2 * ks - 2); cv(ap, 2 * ks - 1); }
                    mem(); PIN();
                }
                misc();
                PIN();
            });
            asm volatile("s_nop 7\n\ts_nop 1");
        }
        PIN();
#pragma unroll
        for (int i = 0; i < 2 * NT; ++i) cv(a2[1], i);
#pragma unroll
        for (int pl = 0; pl < NT / 2; ++pl) st(pl);
        asm volatile("s_nop 1");
    }
    if constexpr (NT == 2) asm volatile(".Lkm_g2:"); else asm volatile(".Lkm_g4:");
    sync_ld(y, ws);
    waitvm<0>();
    asm volatile("" : "+v"(y.r), "+v"(y.g), "+v"(y.e));
    sfor<0, 5>([&](auto I) { xtie(xr[I.v]); });
}

struct Ptrs {
    const char *x, *mem0, *wq, *wk, *wv, *wo, *wqkvb, *wob, *gw;
    char* out;
    char* ws;
};

enum { Q_H = 0, Q_VD = 1, Q_KQ = 2, Q_QKVB = 3, Q_BA = 4, Q_BO = 5, Q_PX = 6, Q_OUT = 7 };
DI void run_gemm(const Ptrs& p, int ty, int b, int i, int hf, bool raw, Sync& y, u32 lds, int w) {
    GemmArgs g;
    g.sV = g.sP = g.sQ = 0; g.w2 = p.wo; g.ldw = HD * 2; g.np1 = 1; g.tn = 10;
    g.sig = p.ws + OFF_ST + F_END * 128;
    const char* xf = raw ? p.x : p.ws + OFF_XF;
    g.xb = !raw;
    int s0 = 8 * b;
    if (ty == Q_VD) {
        s0 = b < 2 ? 4 * b : s0 - 8;
        g.type = T_VD; g.w1 = g.w1b = p.wv + i * DH * 2;
        g.w2 = p.wo + (size_t)i * DH * 1280;
        g.out = g.outb = p.ws + OFF_D + (size_t)s0 * D_STRIDE + i * 512;
        g.sV = 16384; g.sP = (int)D_STRIDE; g.sQ = 128;
        if (b == 0) { g.tn = 5; g.w2 += hf * 640; g.out += hf * 40 * 16384; }
    } else if (ty == Q_H) {
        s0 = 0;
        g.type = T_QK; g.w1 = g.w1b = (b ? p.wq : p.wk) + i * DH * 2;
        g.out = g.outb = p.ws + (b ? OFF_QX : OFF_KX) + (size_t)i * 12288;
        b = 0;
    } else if (ty == Q_KQ) {
        s0 = b < 2 ? 4 * b : s0 - 8;
        g.type = T_QK; g.w1 = p.wk + i * DH * 2; g.w1b = p.wq + i * DH * 2;
        g.out = p.ws + OFF_KX + (size_t)(s0 * NH + i) * 12288;
        g.outb = g.out + (OFF_QX - OFF_KX);
        g.np1 = 2;
        g.sig = p.ws + OFF_ST + (F_K + b * 32 + i) * 128;
    } else if (ty == Q_QKVB) {
        g.type = T_BL; g.w1 = p.wqkvb + i * 768; g.w1b = g.w1 + 384; g.ldw = 3840;
        g.sV = 1920 * 64; g.np1 = 2;
        g.out = p.ws + OFF_QKVB + (size_t)s0 * g.sV + i * 48 * 512; g.outb = g.out + 24 * 512;
    } else {
        const int c0 = min(i * DH, DM - DH);
        g.type = T_BL; g.w1 = g.w1b = p.wob + c0 * 2; g.ldw = 1280;
        xf = p.ws + OFF_OB;
        g.sV = 40960;
        g.out = g.outb = p.ws + OFF_BOUT + (size_t)s0 * 40960 + c0 * 64;
    }
    g.x = xf + (size_t)s0 * 40960;
    if (ty <= Q_KQ && b < 2) gemm_task<2>(g, y, p.ws, lds, w);
    else gemm_task<4>(g, y, p.ws, lds, w);
}
DI int pblk(int s) { return s < 8 ? s >> 2 : min(s >> 3, 7) + 1; }

template <int IMM>
DI void gld16c(u32x4& r, const char* sbase, u32 voff) {
    asm volatile("global_load_dwordx4 %0, %1, %2 offset:%3 sc1" : "=v"(r) : "v"(voff), "s"(sbase), "i"(IMM) : "memory");
}
DI void gst4c(char* sbase, u32 voff, u32 v) {
    asm volatile("global_store_dword %0, %1, %2 sc1" : : "v"(voff), "v"(v), "s"(sbase) : "memory");
}
template <int N>
DI void waitv(u32x4& r) { asm volatile("s_waitcnt vmcnt(%1)" : "+v"(r) : "i"(N) : "memory"); }
DI u32 mt() { return (u32)__builtin_amdgcn_s_memtime(); }
DI u32 rt() { return (u32)__builtin_amdgcn_s_memrealtime(); }
DI void spin_to(u32 tgt) { while ((int)(mt() - tgt) < 0) {} }
DI u32 max3u(u32 a, u32 b, u32 c) { return max(max(a, b), c); }
DI u32x4 ldsr16(u32 a) { return *LDSP(u32x4, a); }
DI void clocks(u32& r, u32& m) {
    u64 a, b;
    asm volatile("s_memrealtime %0\n\ts_memtime %1\n\ts_waitcnt lgkmcnt(0)" : "=s"(a), "=s"(b) : : "memory");
    r = (u32)a; m = (u32)b;
}
#define SLATE 20
DI void edge(u32& r, u32& m) {
    u64 a, t[9];
    u32 n = 0;
#define RT(i) "s_memrealtime %" #i "\n\ts_nop 1\n\t"
#define RC(i) "\n\ts_cmp_eq_u64 %" #i ", %1\n\ts_addc_u32 %10, %10, 0"
    asm volatile("s_memtime %0\n\t" RT(1) RT(2) RT(3) RT(4) RT(5) RT(6) RT(7) RT(8) "s_memrealtime %9\n\ts_waitcnt lgkmcnt(0)" RC(2) RC(3) RC(4) RC(5) RC(6) RC(7) RC(8) RC(9)
                 : "=&s"(a), "=&s"(t[0]), "=&s"(t[1]), "=&s"(t[2]), "=&s"(t[3]), "=&s"(t[4]), "=&s"(t[5]), "=&s"(t[6]), "=&s"(t[7]), "=&s"(t[8]), "+s"(n) : : "scc", "memory");
    m = (u32)a + 12 * n;
    r = (u32)t[0] + 4;
}
#define SPB 90
struct Clock {
    u32 r0, m0, r1, m1; int cpt;
    DI void init() { edge(r0, m0); r1 = r0; m1 = m0; cpt = 24 * 64; }
    DI u32 sched(u32& phi, int ofs, int G, int mode) {
        if (mode == 0) edge(r1, m1);
        u32 r = r1, m = m1;
        if (mode == 2) clocks(r, m);
        int rem = (int)(phi + (u32)ofs - r);
        if (__builtin_expect(rem < -SLATE, 0)) { const int k = (-rem / G + 1) * G; phi += (u32)k; rem += k; }
        return m + (u32)((min(rem, 3000) * cpt) >> 6) - SPB;
    }
    DI u32 at(u32 t) { u32 r, m; clocks(r, m); return m + (u32)((min(max((int)(t - r), 0), 3000) * cpt) >> 6); }
    DI void calib() {
        const int dr = (int)(r1 - r0);
        if (dr >= 60) { int c = (int)fminf(fmaxf((float)(int)(m1 - m0) * 64.f * __builtin_amdgcn_rcpf((float)dr), 256.f), 2560.f); asm volatile("" : "+v"(c)); cpt = uni(c); r0 = r1; m0 = m1; }
    }
};
#define VISS 0
DI void follow(u32& phi, int ofs, u32 t, int G) { const int late = (int)(t - phi) - ofs; if (__builtin_expect(late > 0, 0)) phi += (u32)((late / G + 1) * G); }
DI bool unwritten(const char* base, u32 off) {
#pragma nounroll
    for (int i = 0; i < 10; ++i) {
        u32x4 t;
        gld16c<-4096>(t, base + (i + (i & 8)) * 1024, off); waitv<0>(t);
        if (__builtin_amdgcn_ballot_w64(max3u(max(t[0], t[1]), t[2], t[3]) == 0xffffffffu) != 0) return true;
    }
    return false;
}

DI size_t uslot(int sd, int s) { const int r = (s * 43) >> 8; return OFF_U + (u32)uni(s * (int)U_STRIDE + r * (int)((2u << 20) - 6 * U_STRIDE) + ((sd >> (2 + r)) & 1) * 8192); }
DI int uchunk(int v) { return (2 * (v & 3) + ((v >> 2) & 1) + (v & ~7)) * 8192; }
DI size_t mslot(int sd, int s) { return (u32)uni(s * (int)HO_STRIDE + ((sd >> (s >> 5)) & 1) * 8192 + 4096); }
struct ChainCfg { int T, cR, G, dV, fast, s1, T2, cR2, s2, T3, cR3, dU; };
constexpr int TT_OFF = 88 + 48 + 360;
DI void karg_warm() {
    u32 a[13];
    asm volatile("s_load_dword %0, %13, 0\n\t" "s_load_dword %1, %13, 64\n\t" "s_load_dword %2, %13, 128\n\t" "s_load_dword %3, %13, 192\n\t" "s_load_dword %4, %13, 256\n\t" "s_load_dword %5, %13, 320\n\t" "s_load_dword %6, %13, 384\n\t" "s_load_dword %7, %13, 448\n\t" "s_load_dword %8, %13, 512\n\t" "s_load_dword %9, %13, 576\n\t" "s_load_dword %10, %13, 640\n\t" "s_load_dword %11, %13, 704\n\t" "s_load_dword %12, %13, 748\n\t" "s_waitcnt lgkmcnt(0)"
                 : "=&s"(a[0]), "=&s"(a[1]), "=&s"(a[2]), "=&s"(a[3]), "=&s"(a[4]), "=&s"(a[5]), "=&s"(a[6]), "=&s"(a[7]), "=&s"(a[8]), "=&s"(a[9]), "=&s"(a[10]), "=&s"(a[11]), "=&s"(a[12])
                 : "s"(__builtin_amdgcn_kernarg_segment_ptr()) : "memory");
}
DI u32 sched_ld0(int s) {
    u32 r;
    asm volatile("s_load_dword %0, %1, %2 offset:%3\n\ts_waitcnt lgkmcnt(0)" : "=s"(r) : "s"(__builtin_amdgcn_kernarg_segment_ptr()), "s"(4 * s), "n"(TT_OFF) : "memory");
    return r;
}
#define HP 60
#define HQ 96

constexpr int SL_W = 0;
constexpr int SL_IMG = 131072;
constexpr int SL_KX = SL_IMG + 6656;
constexpr int SL_PF = SL_KX + 12288 + 1024;
constexpr int S_LDS = SL_PF + 256;
DI void nap(int t) { const u32 wake = rt() + (u32)t; while ((int)(rt() - wake) < 0) __builtin_amdgcn_s_sleep(4); }
DI void dly(int n) {
    asm volatile(".Ld%=:\n\ts_sub_u32 %0, %0, 1\n\ts_nop 0\n\ts_cbranch_scc0 .Ld%=" : "+s"(n) : : "scc", "memory");
}
DI void pfl(const char* sbase, u32 voff, u32 lds) {
    asm volatile("s_mov_b32 m0, %2\n\ts_nop 3\n\tglobal_load_lds_dword %0, %1" : : "v"(voff), "s"(sbase), "s"(lds + SL_PF) : "memory");
}
DI void score_role(const Ptrs& p, const ChainCfg cfg, int h, u32 lds, int w) {
    const int lane = lanex(), tid = 64 * w + lane;
    const int n = lane & 15, g = lane >> 4, qq = (lane >> 2) & 3, pp = lane & 3;
    u32x32 wa[8], wv[3];
    const u32 wreg = lds + SL_W + (u32)w * 32768;
    const u32 lfa = wreg + lane * 16;
    const char* kxp = p.ws + OFF_KX + (size_t)h * 12288 + w * 3072;
    const u32 kxl = lds + SL_KX;
    char* const fl = p.ws + OFF_ST;
    const u32 kxw = kxl + w * 3072 - (w < 2 ? 16 : 0);
    auto kxdma = [&]() {
        asm volatile("s_mov_b32 m0, %2\n\ts_nop 3\n\tglobal_load_lds_dwordx4 %0, %1 sc1\n\tglobal_load_lds_dwordx4 %0, %1 offset:1024 sc1\n\tglobal_load_lds_dwordx4 %0, %1 offset:2048 sc1"
                     : : "v"(lanex() * 16), "s"(kxp), "s"(kxw) : "memory");
    };
    u32 kfl, phi;
    int sd;
    {
        constexpr int GS = 3104;
        Sync y;
        if (w < 3) {
#pragma unroll
            for (int i = PXT; i < NSEG; ++i) gst16(p.ws + OFF_KM + (i * NH + h) * 3072 + w * 1024, lane * 16, u32x4{~0u, ~0u, ~0u, ~0u});
#pragma unroll
            for (int i = NSEG - 4; i < NSEG; ++i) gst16(p.ws + OFF_PX + i * 81920 + (w < 2 ? h * 2048 + w * 1024 : 65536 + h * 512), w < 2 ? lane * 16 : (lane * 16) & 511, u32x4{~0u, ~0u, ~0u, ~0u});
        }
        auto dvo = [&](int i) { const int ix = i * 64 + lane; return (u32)((ix / 24) * (HD * 2) + (ix % 24) * 16); };
        const u32 tra = lds + (2 * (g >> 1) + (qq >> 1)) * GS + (4 * (g & 1) + (qq & 1)) * 384 + 8 * pp;
        u32x8 pa = u32x8{dvo(0), dvo(1), dvo(2), tra + 96 * w + 32 * (w & 1), tra + 96 * w - 64 * (w & 1), tra + 32 * w, lfa, (u32)(lane & 31) * 8};
        asm volatile("s_nop 4\n\t" "s_add_u32 s42, %21, 0x0\n\t" "s_add_u32 s42, s46, s42\n\t" "s_addc_u32 s43, s47, 0\n\t" "s_add_u32 m0, %18, 0x0\n\t" "s_nop 0\n\t" "global_load_lds_dwordx4 v224, s[42:43]\n\t"
                     "s_add_u32 m0, %18, 0x400\n\t" "s_nop 0\n\t" "global_load_lds_dwordx4 v225, s[42:43]\n\t" "s_add_u32 m0, %18, 0x800\n\t" "s_nop 0\n\t" "global_load_lds_dwordx4 v226, s[42:43]\n\t"
                     "s_add_u32 s42, %21, 0x18000\n\t" "s_add_u32 s42, s46, s42\n\t" "s_addc_u32 s43, s47, 0\n\t" "s_add_u32 m0, %18, 0xc20\n\t" "s_nop 0\n\t" "global_load_lds_dwordx4 v224, s[42:43]\n\t"
                     "s_add_u32 m0, %18, 0x1020\n\t" "s_nop 0\n\t" "global_load_lds_dwordx4 v225, s[42:43]\n\t" "s_add_u32 m0, %18, 0x1420\n\t" "s_nop 0\n\t" "global_load_lds_dwordx4 v226, s[42:43]\n\t"
                     "s_sub_u32 s42, 0xc0000, %21\n\t" "s_add_u32 s42, s46, s42\n\t" "s_addc_u32 s43, s47, 0\n\t" "s_add_u32 m0, %18, 0x6100\n\t" "s_nop 0\n\t" "global_load_lds_dwordx4 v224, s[42:43]\n\t"
                     "s_add_u32 m0, %18, 0x6500\n\t" "s_nop 0\n\t" "global_load_lds_dwordx4 v225, s[42:43]\n\t" "s_add_u32 m0, %18, 0x6900\n\t" "s_nop 0\n\t" "global_load_lds_dwordx4 v226, s[42:43]\n\t"
                     "s_sub_u32 s42, 0xd8000, %21\n\t" "s_add_u32 s42, s46, s42\n\t" "s_addc_u32 s43, s47, 0\n\t" "s_add_u32 m0, %18, 0x6d20\n\t" "s_nop 0\n\t" "global_load_lds_dwordx4 v224, s[42:43]\n\t"
                     "s_add_u32 m0, %18, 0x7120\n\t" "s_nop 0\n\t" "global_load_lds_dwordx4 v225, s[42:43]\n\t" "s_add_u32 m0, %18, 0x7520\n\t" "s_nop 0\n\t" "global_load_lds_dwordx4 v226, s[42:43]\n\t"
                     "s_add_u32 s42, %21, 0x180000\n\t" "s_add_u32 s42, s46, s42\n\t" "s_addc_u32 s43, s47, 0\n\t" "s_add_u32 m0, %18, 0xc200\n\t" "s_nop 0\n\t" "global_load_lds_dwordx4 v224, s[42:43]\n\t"
                     "s_add_u32 m0, %18, 0xc600\n\t" "s_nop 0\n\t" "global_load_lds_dwordx4 v225, s[42:43]\n\t" "s_add_u32 m0, %18, 0xca00\n\t" "s_nop 0\n\t" "global_load_lds_dwordx4 v226, s[42:43]\n\t"
                     "s_add_u32 s42, %21, 0x198000\n\t" "s_add_u32 s42, s46, s42\n\t" "s_addc_u32 s43, s47, 0\n\t" "s_add_u32 m0, %18, 0xce20\n\t" "s_nop 0\n\t" "global_load_lds_dwordx4 v224, s[42:43]\n\t"
                     "s_add_u32 m0, %18, 0xd220\n\t" "s_nop 0\n\t" "global_load_lds_dwordx4 v225, s[42:43]\n\t" "s_add_u32 m0, %18, 0xd620\n\t" "s_nop 0\n\t" "global_load_lds_dwordx4 v226, s[42:43]\n\t"
                     "s_sub_u32 s42, 0x240000, %21\n\t" "s_add_u32 s42, s46, s42\n\t" "s_addc_u32 s43, s47, 0\n\t" "s_add_u32 m0, %18, 0x12300\n\t" "s_nop 0\n\t" "global_load_lds_dwordx4 v224, s[42:43]\n\t"
                     "s_add_u32 m0, %18, 0x12700\n\t" "s_nop 0\n\t" "global_load_lds_dwordx4 v225, s[42:43]\n\t" "s_add_u32 m0, %18, 0x12b00\n\t" "s_nop 0\n\t" "global_load_lds_dwordx4 v226, s[42:43]\n\t"
                     "s_sub_u32 s42, 0x258000, %21\n\t" "s_add_u32 s42, s46, s42\n\t" "s_addc_u32 s43, s47, 0\n\t" "s_add_u32 m0, %18, 0x12f20\n\t" "s_nop 0\n\t" "global_load_lds_dwordx4 v224, s[42:43]\n\t"
                     "s_add_u32 m0, %18, 0x13320\n\t" "s_nop 0\n\t" "global_load_lds_dwordx4 v225, s[42:43]\n\t" "s_add_u32 m0, %18, 0x13720\n\t" "s_nop 0\n\t" "global_load_lds_dwordx4 v226, s[42:43]\n\t"
                     "s_waitcnt vmcnt(12) lgkmcnt(0)\n\t" "s_barrier\n\t" "s_add_u32 s42, %21, 0x300000\n\t" "s_add_u32 s42, s46, s42\n\t" "s_addc_u32 s43, s47, 0\n\t" "s_add_u32 m0, %18, 0x18400\n\t" "s_nop 0\n\t"
                     "global_load_lds_dwordx4 v224, s[42:43]\n\t" "s_add_u32 m0, %18, 0x18800\n\t" "s_nop 0\n\t" "global_load_lds_dwordx4 v225, s[42:43]\n\t" "s_add_u32 m0, %18, 0x18c00\n\t" "s_nop 0\n\t"
                     "global_load_lds_dwordx4 v226, s[42:43]\n\t" "s_add_u32 s42, %21, 0x318000\n\t" "s_add_u32 s42, s46, s42\n\t" "s_addc_u32 s43, s47, 0\n\t" "s_add_u32 m0, %18, 0x19020\n\t" "s_nop 0\n\t"
                     "global_load_lds_dwordx4 v224, s[42:43]\n\t" "s_add_u32 m0, %18, 0x19420\n\t" "s_nop 0\n\t" "global_load_lds_dwordx4 v225, s[42:43]\n\t" "s_add_u32 m0, %18, 0x19820\n\t" "s_nop 0\n\t"
                     "global_load_lds_dwordx4 v226, s[42:43]\n\t" "s_sub_u32 s42, 0x3c0000, %21\n\t" "s_add_u32 s42, s46, s42\n\t" "s_addc_u32 s43, s47, 0\n\t" "s_add_u32 m0, %18, 0x1e500\n\t" "s_nop 0\n\t"
                     "global_load_lds_dwordx4 v224, s[42:43]\n\t" "s_add_u32 m0, %18, 0x1e900\n\t" "s_nop 0\n\t" "global_load_lds_dwordx4 v225, s[42:43]\n\t" "s_add_u32 m0, %18, 0x1ed00\n\t" "s_nop 0\n\t"
                     "global_load_lds_dwordx4 v226, s[42:43]\n\t" "s_sub_u32 s42, 0x3d8000, %21\n\t" "s_add_u32 s42, s46, s42\n\t" "s_addc_u32 s43, s47, 0\n\t" "s_add_u32 m0, %18, 0x1f120\n\t" "s_nop 0\n\t"
                     "global_load_lds_dwordx4 v224, s[42:43]\n\t" "s_add_u32 m0, %18, 0x1f520\n\t" "s_nop 0\n\t" "global_load_lds_dwordx4 v225, s[42:43]\n\t" "s_add_u32 m0, %18, 0x1f920\n\t" "s_nop 0\n\t"
                     "global_load_lds_dwordx4 v226, s[42:43]\n\t" "v_add_u32 v232, 0x0, v229\n\t" "ds_read_b64_tr_b16 v[8:9], v232\n\t" "ds_read_b64_tr_b16 v[10:11], v232 offset:768\n\t"
                     "ds_read_b64_tr_b16 v[12:13], v232 offset:12416\n\t" "ds_read_b64_tr_b16 v[14:15], v232 offset:13184\n\t" "ds_read_b64_tr_b16 a[96:97], v232 offset:128\n\t"
                     "ds_read_b64_tr_b16 a[98:99], v232 offset:896\n\t" "ds_read_b64_tr_b16 a[100:101], v232 offset:12544\n\t" "ds_read_b64_tr_b16 a[102:103], v232 offset:13312\n\t"
                     "ds_read_b64_tr_b16 a[176:177], v232 offset:256\n\t" "ds_read_b64_tr_b16 a[178:179], v232 offset:1024\n\t" "ds_read_b64_tr_b16 a[180:181], v232 offset:12672\n\t"
                     "ds_read_b64_tr_b16 a[182:183], v232 offset:13440\n\t" "ds_read_b64_tr_b16 v[40:41], v232 offset:24832\n\t" "ds_read_b64_tr_b16 v[42:43], v232 offset:25600\n\t"
                     "ds_read_b64_tr_b16 v[44:45], v232 offset:37248\n\t" "ds_read_b64_tr_b16 v[46:47], v232 offset:38016\n\t" "ds_read_b64_tr_b16 a[112:113], v232 offset:24960\n\t"
                     "ds_read_b64_tr_b16 a[114:115], v232 offset:25728\n\t" "ds_read_b64_tr_b16 a[116:117], v232 offset:37376\n\t" "ds_read_b64_tr_b16 a[118:119], v232 offset:38144\n\t"
                     "ds_read_b64_tr_b16 a[192:193], v232 offset:25088\n\t" "ds_read_b64_tr_b16 a[194:195], v232 offset:25856\n\t" "ds_read_b64_tr_b16 a[196:197], v232 offset:37504\n\t"
                     "ds_read_b64_tr_b16 a[198:199], v232 offset:38272\n\t" "s_waitcnt vmcnt(12) lgkmcnt(0)\n\t" "s_barrier\n\t" "s_add_u32 s42, %21, 0x480000\n\t" "s_add_u32 s42, s46, s42\n\t" "s_addc_u32 s43, s47, 0\n\t"
                     "s_add_u32 m0, %18, 0x0\n\t" "s_nop 0\n\t" "global_load_lds_dwordx4 v224, s[42:43]\n\t" "s_add_u32 m0, %18, 0x400\n\t" "s_nop 0\n\t" "global_load_lds_dwordx4 v225, s[42:43]\n\t"
                     "s_add_u32 m0, %18, 0x800\n\t" "s_nop 0\n\t" "global_load_lds_dwordx4 v226, s[42:43]\n\t" "s_add_u32 s42, %21, 0x498000\n\t" "s_add_u32 s42, s46, s42\n\t" "s_addc_u32 s43, s47, 0\n\t"
                     "s_add_u32 m0, %18, 0xc20\n\t" "s_nop 0\n\t" "global_load_lds_dwordx4 v224, s[42:43]\n\t" "s_add_u32 m0, %18, 0x1020\n\t" "s_nop 0\n\t" "global_load_lds_dwordx4 v225, s[42:43]\n\t"
                     "s_add_u32 m0, %18, 0x1420\n\t" "s_nop 0\n\t" "global_load_lds_dwordx4 v226, s[42:43]\n\t" "s_sub_u32 s42, 0x540000, %21\n\t" "s_add_u32 s42, s46, s42\n\t" "s_addc_u32 s43, s47, 0\n\t"
                     "s_add_u32 m0, %18, 0x6100\n\t" "s_nop 0\n\t" "global_load_lds_dwordx4 v224, s[42:43]\n\t" "s_add_u32 m0, %18, 0x6500\n\t" "s_nop 0\n\t" "global_load_lds_dwordx4 v225, s[42:43]\n\t"
                     "s_add_u32 m0, %18, 0x6900\n\t" "s_nop 0\n\t" "global_load_lds_dwordx4 v226, s[42:43]\n\t" "s_sub_u32 s42, 0x558000, %21\n\t" "s_add_u32 s42, s46, s42\n\t" "s_addc_u32 s43, s47, 0\n\t"
                     "s_add_u32 m0, %18, 0x6d20\n\t" "s_nop 0\n\t" "global_load_lds_dwordx4 v224, s[42:43]\n\t" "s_add_u32 m0, %18, 0x7120\n\t" "s_nop 0\n\t" "global_load_lds_dwordx4 v225, s[42:43]\n\t"
                     "s_add_u32 m0, %18, 0x7520\n\t" "s_nop 0\n\t" "global_load_lds_dwordx4 v226, s[42:43]\n\t" "v_add_u32 v232, 0xc200, v229\n\t" "ds_read_b64_tr_b16 v[24:25], v232\n\t"
                     "ds_read_b64_tr_b16 v[26:27], v232 offset:768\n\t" "ds_read_b64_tr_b16 v[28:29], v232 offset:12416\n\t" "ds_read_b64_tr_b16 v[30:31], v232 offset:13184\n\t"
                     "ds_read_b64_tr_b16 a[104:105], v232 offset:128\n\t" "ds_read_b64_tr_b16 a[106:107], v232 offset:896\n\t" "ds_read_b64_tr_b16 a[108:109], v232 offset:12544\n\t"
                     "ds_read_b64_tr_b16 a[110:111], v232 offset:13312\n\t" "ds_read_b64_tr_b16 a[184:185], v232 offset:256\n\t" "ds_read_b64_tr_b16 a[186:187], v232 offset:1024\n\t"
                     "ds_read_b64_tr_b16 a[188:189], v232 offset:12672\n\t" "ds_read_b64_tr_b16 a[190:191], v232 offset:13440\n\t" "ds_read_b64_tr_b16 v[56:57], v232 offset:24832\n\t"
                     "ds_read_b64_tr_b16 v[58:59], v232 offset:25600\n\t" "ds_read_b64_tr_b16 v[60:61], v232 offset:37248\n\t" "ds_read_b64_tr_b16 v[62:63], v232 offset:38016\n\t"
                     "ds_read_b64_tr_b16 a[120:121], v232 offset:24960\n\t" "ds_read_b64_tr_b16 a[122:123], v232 offset:25728\n\t" "ds_read_b64_tr_b16 a[124:125], v232 offset:37376\n\t"
                     "ds_read_b64_tr_b16 a[126:127], v232 offset:38144\n\t" "ds_read_b64_tr_b16 a[200:201], v232 offset:25088\n\t" "ds_read_b64_tr_b16 a[202:203], v232 offset:25856\n\t"
                     "ds_read_b64_tr_b16 a[204:205], v232 offset:37504\n\t" "ds_read_b64_tr_b16 a[206:207], v232 offset:38272\n\t" "s_waitcnt vmcnt(12) lgkmcnt(0)\n\t" "s_barrier\n\t" "s_add_u32 s42, %21, 0x600000\n\t"
                     "s_add_u32 s42, s46, s42\n\t" "s_addc_u32 s43, s47, 0\n\t" "s_add_u32 m0, %18, 0xc200\n\t" "s_nop 0\n\t" "global_load_lds_dwordx4 v224, s[42:43]\n\t" "s_add_u32 m0, %18, 0xc600\n\t" "s_nop 0\n\t"
                     "global_load_lds_dwordx4 v225, s[42:43]\n\t" "s_add_u32 m0, %18, 0xca00\n\t" "s_nop 0\n\t" "global_load_lds_dwordx4 v226, s[42:43]\n\t" "s_add_u32 s42, %21, 0x618000\n\t" "s_add_u32 s42, s46, s42\n\t"
                     "s_addc_u32 s43, s47, 0\n\t" "s_add_u32 m0, %18, 0xce20\n\t" "s_nop 0\n\t" "global_load_lds_dwordx4 v224, s[42:43]\n\t" "s_add_u32 m0, %18, 0xd220\n\t" "s_nop 0\n\t"
                     "global_load_lds_dwordx4 v225, s[42:43]\n\t" "s_add_u32 m0, %18, 0xd620\n\t" "s_nop 0\n\t" "global_load_lds_dwordx4 v226, s[42:43]\n\t" "s_sub_u32 s42, 0x6c0000, %21\n\t" "s_add_u32 s42, s46, s42\n\t"
                     "s_addc_u32 s43, s47, 0\n\t" "s_add_u32 m0, %18, 0x12300\n\t" "s_nop 0\n\t" "global_load_lds_dwordx4 v224, s[42:43]\n\t" "s_add_u32 m0, %18, 0x12700\n\t" "s_nop 0\n\t"
                     "global_load_lds_dwordx4 v225, s[42:43]\n\t" "s_add_u32 m0, %18, 0x12b00\n\t" "s_nop 0\n\t" "global_load_lds_dwordx4 v226, s[42:43]\n\t" "s_sub_u32 s42, 0x6d8000, %21\n\t" "s_add_u32 s42, s46, s42\n\t"
                     "s_addc_u32 s43, s47, 0\n\t" "s_add_u32 m0, %18, 0x12f20\n\t" "s_nop 0\n\t" "global_load_lds_dwordx4 v224, s[42:43]\n\t" "s_add_u32 m0, %18, 0x13320\n\t" "s_nop 0\n\t"
                     "global_load_lds_dwordx4 v225, s[42:43]\n\t" "s_add_u32 m0, %18, 0x13720\n\t" "s_nop 0\n\t" "global_load_lds_dwordx4 v226, s[42:43]\n\t" "v_add_u32 v232, 0x18400, v229\n\t"
                     "ds_read_b64_tr_b16 v[72:73], v232\n\t" "ds_read_b64_tr_b16 v[74:75], v232 offset:768\n\t" "ds_read_b64_tr_b16 v[76:77], v232 offset:12416\n\t" "ds_read_b64_tr_b16 v[78:79], v232 offset:13184\n\t"
                     "ds_read_b64_tr_b16 a[128:129], v232 offset:128\n\t" "ds_read_b64_tr_b16 a[130:131], v232 offset:896\n\t" "ds_read_b64_tr_b16 a[132:133], v232 offset:12544\n\t"
                     "ds_read_b64_tr_b16 a[134:135], v232 offset:13312\n\t" "ds_read_b64_tr_b16 a[208:209], v232 offset:256\n\t" "ds_read_b64_tr_b16 a[210:211], v232 offset:1024\n\t"
                     "ds_read_b64_tr_b16 a[212:213], v232 offset:12672\n\t" "ds_read_b64_tr_b16 a[214:215], v232 offset:13440\n\t" "ds_read_b64_tr_b16 v[88:89], v232 offset:24832\n\t"
                     "ds_read_b64_tr_b16 v[90:91], v232 offset:25600\n\t" "ds_read_b64_tr_b16 v[92:93], v232 offset:37248\n\t" "ds_read_b64_tr_b16 v[94:95], v232 offset:38016\n\t"
                     "ds_read_b64_tr_b16 a[136:137], v232 offset:24960\n\t" "ds_read_b64_tr_b16 a[138:139], v232 offset:25728\n\t" "ds_read_b64_tr_b16 a[140:141], v232 offset:37376\n\t"
                     "ds_read_b64_tr_b16 a[142:143], v232 offset:38144\n\t" "ds_read_b64_tr_b16 a[216:217], v232 offset:25088\n\t" "ds_read_b64_tr_b16 a[218:219], v232 offset:25856\n\t"
                     "ds_read_b64_tr_b16 a[220:221], v232 offset:37504\n\t" "ds_read_b64_tr_b16 a[222:223], v232 offset:38272\n\t" "s_waitcnt vmcnt(12) lgkmcnt(0)\n\t" "s_barrier\n\t" "s_add_u32 s42, %21, 0x0\n\t"
                     "s_add_u32 s42, s44, s42\n\t" "s_addc_u32 s43, s45, 0\n\t" "s_add_u32 m0, %18, 0x18400\n\t" "s_nop 0\n\t" "global_load_lds_dwordx4 v224, s[42:43]\n\t" "s_add_u32 m0, %18, 0x18800\n\t" "s_nop 0\n\t"
                     "global_load_lds_dwordx4 v225, s[42:43]\n\t" "s_add_u32 m0, %18, 0x18c00\n\t" "s_nop 0\n\t" "global_load_lds_dwordx4 v226, s[42:43]\n\t" "s_add_u32 s42, %21, 0x18000\n\t" "s_add_u32 s42, s44, s42\n\t"
                     "s_addc_u32 s43, s45, 0\n\t" "s_add_u32 m0, %18, 0x19020\n\t" "s_nop 0\n\t" "global_load_lds_dwordx4 v224, s[42:43]\n\t" "s_add_u32 m0, %18, 0x19420\n\t" "s_nop 0\n\t"
                     "global_load_lds_dwordx4 v225, s[42:43]\n\t" "s_add_u32 m0, %18, 0x19820\n\t" "s_nop 0\n\t" "global_load_lds_dwordx4 v226, s[42:43]\n\t" "s_sub_u32 s42, 0xc0000, %21\n\t" "s_add_u32 s42, s44, s42\n\t"
                     "s_addc_u32 s43, s45, 0\n\t" "s_add_u32 m0, %18, 0x1e500\n\t" "s_nop 0\n\t" "global_load_lds_dwordx4 v224, s[42:43]\n\t" "s_add_u32 m0, %18, 0x1e900\n\t" "s_nop 0\n\t"
                     "global_load_lds_dwordx4 v225, s[42:43]\n\t" "s_add_u32 m0, %18, 0x1ed00\n\t" "s_nop 0\n\t" "global_load_lds_dwordx4 v226, s[42:43]\n\t" "s_sub_u32 s42, 0xd8000, %21\n\t" "s_add_u32 s42, s44, s42\n\t"
                     "s_addc_u32 s43, s45, 0\n\t" "s_add_u32 m0, %18, 0x1f120\n\t" "s_nop 0\n\t" "global_load_lds_dwordx4 v224, s[42:43]\n\t" "s_add_u32 m0, %18, 0x1f520\n\t" "s_nop 0\n\t"
                     "global_load_lds_dwordx4 v225, s[42:43]\n\t" "s_add_u32 m0, %18, 0x1f920\n\t" "s_nop 0\n\t" "global_load_lds_dwordx4 v226, s[42:43]\n\t" "v_add_u32 v232, 0x0, v229\n\t"
                     "ds_read_b64_tr_b16 v[104:105], v232\n\t" "ds_read_b64_tr_b16 v[106:107], v232 offset:768\n\t" "ds_read_b64_tr_b16 v[108:109], v232 offset:12416\n\t"
                     "ds_read_b64_tr_b16 v[110:111], v232 offset:13184\n\t" "ds_read_b64_tr_b16 a[144:145], v232 offset:128\n\t" "ds_read_b64_tr_b16 a[146:147], v232 offset:896\n\t"
                     "ds_read_b64_tr_b16 a[148:149], v232 offset:12544\n\t" "ds_read_b64_tr_b16 a[150:151], v232 offset:13312\n\t" "ds_read_b64_tr_b16 a[224:225], v232 offset:256\n\t"
                     "ds_read_b64_tr_b16 a[226:227], v232 offset:1024\n\t" "ds_read_b64_tr_b16 a[228:229], v232 offset:12672\n\t" "ds_read_b64_tr_b16 a[230:231], v232 offset:13440\n\t"
                     "ds_read_b64_tr_b16 v[120:121], v232 offset:24832\n\t" "ds_read_b64_tr_b16 v[122:123], v232 offset:25600\n\t" "ds_read_b64_tr_b16 v[124:125], v232 offset:37248\n\t"
                     "ds_read_b64_tr_b16 v[126:127], v232 offset:38016\n\t" "ds_read_b64_tr_b16 a[152:153], v232 offset:24960\n\t" "ds_read_b64_tr_b16 a[154:155], v232 offset:25728\n\t"
                     "ds_read_b64_tr_b16 a[156:157], v232 offset:37376\n\t" "ds_read_b64_tr_b16 a[158:159], v232 offset:38144\n\t" "ds_read_b64_tr_b16 a[232:233], v232 offset:25088\n\t"
                     "ds_read_b64_tr_b16 a[234:235], v232 offset:25856\n\t" "ds_read_b64_tr_b16 a[236:237], v232 offset:37504\n\t" "ds_read_b64_tr_b16 a[238:239], v232 offset:38272\n\t" "s_waitcnt vmcnt(12) lgkmcnt(0)\n\t"
                     "s_barrier\n\t" "s_add_u32 s42, %21, 0x180000\n\t" "s_add_u32 s42, s44, s42\n\t" "s_addc_u32 s43, s45, 0\n\t" "s_add_u32 m0, %18, 0x0\n\t" "s_nop 0\n\t" "global_load_lds_dwordx4 v224, s[42:43]\n\t"
                     "s_add_u32 m0, %18, 0x400\n\t" "s_nop 0\n\t" "global_load_lds_dwordx4 v225, s[42:43]\n\t" "s_add_u32 m0, %18, 0x800\n\t" "s_nop 0\n\t" "global_load_lds_dwordx4 v226, s[42:43]\n\t"
                     "s_add_u32 s42, %21, 0x198000\n\t" "s_add_u32 s42, s44, s42\n\t" "s_addc_u32 s43, s45, 0\n\t" "s_add_u32 m0, %18, 0xc20\n\t" "s_nop 0\n\t" "global_load_lds_dwordx4 v224, s[42:43]\n\t"
                     "s_add_u32 m0, %18, 0x1020\n\t" "s_nop 0\n\t" "global_load_lds_dwordx4 v225, s[42:43]\n\t" "s_add_u32 m0, %18, 0x1420\n\t" "s_nop 0\n\t" "global_load_lds_dwordx4 v226, s[42:43]\n\t"
                     "s_sub_u32 s42, 0x240000, %21\n\t" "s_add_u32 s42, s44, s42\n\t" "s_addc_u32 s43, s45, 0\n\t" "s_add_u32 m0, %18, 0x6100\n\t" "s_nop 0\n\t" "global_load_lds_dwordx4 v224, s[42:43]\n\t"
                     "s_add_u32 m0, %18, 0x6500\n\t" "s_nop 0\n\t" "global_load_lds_dwordx4 v225, s[42:43]\n\t" "s_add_u32 m0, %18, 0x6900\n\t" "s_nop 0\n\t" "global_load_lds_dwordx4 v226, s[42:43]\n\t"
                     "s_sub_u32 s42, 0x258000, %21\n\t" "s_add_u32 s42, s44, s42\n\t" "s_addc_u32 s43, s45, 0\n\t" "s_add_u32 m0, %18, 0x6d20\n\t" "s_nop 0\n\t" "global_load_lds_dwordx4 v224, s[42:43]\n\t"
                     "s_add_u32 m0, %18, 0x7120\n\t" "s_nop 0\n\t" "global_load_lds_dwordx4 v225, s[42:43]\n\t" "s_add_u32 m0, %18, 0x7520\n\t" "s_nop 0\n\t" "global_load_lds_dwordx4 v226, s[42:43]\n\t"
                     "v_add_u32 v232, 0xc200, v229\n\t" "ds_read_b64_tr_b16 a[56:57], v232\n\t" "ds_read_b64_tr_b16 a[58:59], v232 offset:768\n\t" "ds_read_b64_tr_b16 a[60:61], v232 offset:12416\n\t"
                     "ds_read_b64_tr_b16 a[62:63], v232 offset:13184\n\t" "ds_read_b64_tr_b16 a[160:161], v232 offset:128\n\t" "ds_read_b64_tr_b16 a[162:163], v232 offset:896\n\t"
                     "ds_read_b64_tr_b16 a[164:165], v232 offset:12544\n\t" "ds_read_b64_tr_b16 a[166:167], v232 offset:13312\n\t" "ds_read_b64_tr_b16 a[240:241], v232 offset:256\n\t"
                     "ds_read_b64_tr_b16 a[242:243], v232 offset:1024\n\t" "ds_read_b64_tr_b16 a[244:245], v232 offset:12672\n\t" "ds_read_b64_tr_b16 a[246:247], v232 offset:13440\n\t"
                     "ds_read_b64_tr_b16 a[88:89], v232 offset:24832\n\t" "ds_read_b64_tr_b16 a[90:91], v232 offset:25600\n\t" "ds_read_b64_tr_b16 a[92:93], v232 offset:37248\n\t"
                     "ds_read_b64_tr_b16 a[94:95], v232 offset:38016\n\t" "ds_read_b64_tr_b16 a[168:169], v232 offset:24960\n\t" "ds_read_b64_tr_b16 a[170:171], v232 offset:25728\n\t"
                     "ds_read_b64_tr_b16 a[172:173], v232 offset:37376\n\t" "ds_read_b64_tr_b16 a[174:175], v232 offset:38144\n\t" "ds_read_b64_tr_b16 a[248:249], v232 offset:25088\n\t"
                     "ds_read_b64_tr_b16 a[250:251], v232 offset:25856\n\t" "ds_read_b64_tr_b16 a[252:253], v232 offset:37504\n\t" "ds_read_b64_tr_b16 a[254:255], v232 offset:38272\n\t" "s_waitcnt vmcnt(12) lgkmcnt(0)\n\t"
                     "s_barrier\n\t" "s_add_u32 s42, %21, 0x300000\n\t" "s_add_u32 s42, s44, s42\n\t" "s_addc_u32 s43, s45, 0\n\t" "s_add_u32 m0, %18, 0xc200\n\t" "s_nop 0\n\t" "global_load_lds_dwordx4 v224, s[42:43]\n\t"
                     "s_add_u32 m0, %18, 0xc600\n\t" "s_nop 0\n\t" "global_load_lds_dwordx4 v225, s[42:43]\n\t" "s_add_u32 m0, %18, 0xca00\n\t" "s_nop 0\n\t" "global_load_lds_dwordx4 v226, s[42:43]\n\t"
                     "s_add_u32 s42, %21, 0x318000\n\t" "s_add_u32 s42, s44, s42\n\t" "s_addc_u32 s43, s45, 0\n\t" "s_add_u32 m0, %18, 0xce20\n\t" "s_nop 0\n\t" "global_load_lds_dwordx4 v224, s[42:43]\n\t"
                     "s_add_u32 m0, %18, 0xd220\n\t" "s_nop 0\n\t" "global_load_lds_dwordx4 v225, s[42:43]\n\t" "s_add_u32 m0, %18, 0xd620\n\t" "s_nop 0\n\t" "global_load_lds_dwordx4 v226, s[42:43]\n\t"
                     "s_sub_u32 s42, 0x3c0000, %21\n\t" "s_add_u32 s42, s44, s42\n\t" "s_addc_u32 s43, s45, 0\n\t" "s_add_u32 m0, %18, 0x12300\n\t" "s_nop 0\n\t" "global_load_lds_dwordx4 v224, s[42:43]\n\t"
                     "s_add_u32 m0, %18, 0x12700\n\t" "s_nop 0\n\t" "global_load_lds_dwordx4 v225, s[42:43]\n\t" "s_add_u32 m0, %18, 0x12b00\n\t" "s_nop 0\n\t" "global_load_lds_dwordx4 v226, s[42:43]\n\t"
                     "s_sub_u32 s42, 0x3d8000, %21\n\t" "s_add_u32 s42, s44, s42\n\t" "s_addc_u32 s43, s45, 0\n\t" "s_add_u32 m0, %18, 0x12f20\n\t" "s_nop 0\n\t" "global_load_lds_dwordx4 v224, s[42:43]\n\t"
                     "s_add_u32 m0, %18, 0x13320\n\t" "s_nop 0\n\t" "global_load_lds_dwordx4 v225, s[42:43]\n\t" "s_add_u32 m0, %18, 0x13720\n\t" "s_nop 0\n\t" "global_load_lds_dwordx4 v226, s[42:43]\n\t"
                     "v_add_u32 v232, 0x18400, v227\n\t" "v_add_u32 v233, 0x18400, v228\n\t" "ds_read_b64_tr_b16 v[128:129], v232\n\t" "ds_read_b64_tr_b16 v[130:131], v232 offset:768\n\t"
                     "ds_read_b64_tr_b16 v[132:133], v232 offset:12416\n\t" "ds_read_b64_tr_b16 v[134:135], v232 offset:13184\n\t" "ds_read_b64_tr_b16 v[0:1], v232 offset:32\n\t"
                     "ds_read_b64_tr_b16 v[2:3], v232 offset:800\n\t" "ds_read_b64_tr_b16 v[4:5], v232 offset:12448\n\t" "ds_read_b64_tr_b16 v[6:7], v232 offset:13216\n\t"
                     "ds_read_b64_tr_b16 v[136:137], v233 offset:64\n\t" "ds_read_b64_tr_b16 v[138:139], v233 offset:832\n\t" "ds_read_b64_tr_b16 v[140:141], v233 offset:12480\n\t"
                     "ds_read_b64_tr_b16 v[142:143], v233 offset:13248\n\t" "ds_read_b64_tr_b16 v[160:161], v232 offset:24832\n\t" "ds_read_b64_tr_b16 v[162:163], v232 offset:25600\n\t"
                     "ds_read_b64_tr_b16 v[164:165], v232 offset:37248\n\t" "ds_read_b64_tr_b16 v[166:167], v232 offset:38016\n\t" "ds_read_b64_tr_b16 v[32:33], v232 offset:24864\n\t"
                     "ds_read_b64_tr_b16 v[34:35], v232 offset:25632\n\t" "ds_read_b64_tr_b16 v[36:37], v232 offset:37280\n\t" "ds_read_b64_tr_b16 v[38:39], v232 offset:38048\n\t"
                     "ds_read_b64_tr_b16 v[168:169], v233 offset:24896\n\t" "ds_read_b64_tr_b16 v[170:171], v233 offset:25664\n\t" "ds_read_b64_tr_b16 v[172:173], v233 offset:37312\n\t"
                     "ds_read_b64_tr_b16 v[174:175], v233 offset:38080\n\t" "s_waitcnt vmcnt(12) lgkmcnt(0)\n\t" "s_barrier\n\t" "v_mov_b32 %14, 0\n\t" "global_load_dwordx2 %11, v231, %19 sc1\n\t"
                     "global_load_dwordx2 %12, %14, %19 offset:512 sc1\n\t" "global_load_dwordx2 %13, %14, %19 offset:1024 sc1\n\t" "s_add_u32 s42, %21, 0x480000\n\t" "s_add_u32 s42, s44, s42\n\t"
                     "s_addc_u32 s43, s45, 0\n\t" "s_add_u32 m0, %18, 0x18400\n\t" "s_nop 0\n\t" "global_load_lds_dwordx4 v224, s[42:43]\n\t" "s_add_u32 m0, %18, 0x18800\n\t" "s_nop 0\n\t"
                     "global_load_lds_dwordx4 v225, s[42:43]\n\t" "s_add_u32 m0, %18, 0x18c00\n\t" "s_nop 0\n\t" "global_load_lds_dwordx4 v226, s[42:43]\n\t" "s_add_u32 s42, %21, 0x498000\n\t" "s_add_u32 s42, s44, s42\n\t"
                     "s_addc_u32 s43, s45, 0\n\t" "s_add_u32 m0, %18, 0x19020\n\t" "s_nop 0\n\t" "global_load_lds_dwordx4 v224, s[42:43]\n\t" "s_add_u32 m0, %18, 0x19420\n\t" "s_nop 0\n\t"
                     "global_load_lds_dwordx4 v225, s[42:43]\n\t" "s_add_u32 m0, %18, 0x19820\n\t" "s_nop 0\n\t" "global_load_lds_dwordx4 v226, s[42:43]\n\t" "s_sub_u32 s42, 0x540000, %21\n\t" "s_add_u32 s42, s44, s42\n\t"
                     "s_addc_u32 s43, s45, 0\n\t" "s_add_u32 m0, %18, 0x1e500\n\t" "s_nop 0\n\t" "global_load_lds_dwordx4 v224, s[42:43]\n\t" "s_add_u32 m0, %18, 0x1e900\n\t" "s_nop 0\n\t"
                     "global_load_lds_dwordx4 v225, s[42:43]\n\t" "s_add_u32 m0, %18, 0x1ed00\n\t" "s_nop 0\n\t" "global_load_lds_dwordx4 v226, s[42:43]\n\t" "s_sub_u32 s42, 0x558000, %21\n\t" "s_add_u32 s42, s44, s42\n\t"
                     "s_addc_u32 s43, s45, 0\n\t" "s_add_u32 m0, %18, 0x1f120\n\t" "s_nop 0\n\t" "global_load_lds_dwordx4 v224, s[42:43]\n\t" "s_add_u32 m0, %18, 0x1f520\n\t" "s_nop 0\n\t"
                     "global_load_lds_dwordx4 v225, s[42:43]\n\t" "s_add_u32 m0, %18, 0x1f920\n\t" "s_nop 0\n\t" "global_load_lds_dwordx4 v226, s[42:43]\n\t" "v_add_u32 v232, 0x0, v227\n\t" "v_add_u32 v233, 0x0, v228\n\t"
                     "ds_read_b64_tr_b16 v[144:145], v232\n\t" "ds_read_b64_tr_b16 v[146:147], v232 offset:768\n\t" "ds_read_b64_tr_b16 v[148:149], v232 offset:12416\n\t"
                     "ds_read_b64_tr_b16 v[150:151], v232 offset:13184\n\t" "ds_read_b64_tr_b16 v[16:17], v232 offset:32\n\t" "ds_read_b64_tr_b16 v[18:19], v232 offset:800\n\t"
                     "ds_read_b64_tr_b16 v[20:21], v232 offset:12448\n\t" "ds_read_b64_tr_b16 v[22:23], v232 offset:13216\n\t" "ds_read_b64_tr_b16 v[152:153], v233 offset:64\n\t"
                     "ds_read_b64_tr_b16 v[154:155], v233 offset:832\n\t" "ds_read_b64_tr_b16 v[156:157], v233 offset:12480\n\t" "ds_read_b64_tr_b16 v[158:159], v233 offset:13248\n\t"
                     "ds_read_b64_tr_b16 v[176:177], v232 offset:24832\n\t" "ds_read_b64_tr_b16 v[178:179], v232 offset:25600\n\t" "ds_read_b64_tr_b16 v[180:181], v232 offset:37248\n\t"
                     "ds_read_b64_tr_b16 v[182:183], v232 offset:38016\n\t" "ds_read_b64_tr_b16 v[48:49], v232 offset:24864\n\t" "ds_read_b64_tr_b16 v[50:51], v232 offset:25632\n\t"
                     "ds_read_b64_tr_b16 v[52:53], v232 offset:37280\n\t" "ds_read_b64_tr_b16 v[54:55], v232 offset:38048\n\t" "ds_read_b64_tr_b16 v[184:185], v233 offset:24896\n\t"
                     "ds_read_b64_tr_b16 v[186:187], v233 offset:25664\n\t" "ds_read_b64_tr_b16 v[188:189], v233 offset:37312\n\t" "ds_read_b64_tr_b16 v[190:191], v233 offset:38080\n\t" "s_waitcnt vmcnt(12) lgkmcnt(0)\n\t"
                     "s_barrier\n\t" "s_add_u32 s42, %21, 0x600000\n\t" "s_add_u32 s42, s44, s42\n\t" "s_addc_u32 s43, s45, 0\n\t" "s_add_u32 m0, %18, 0x0\n\t" "s_nop 0\n\t" "global_load_lds_dwordx4 v224, s[42:43]\n\t"
                     "s_add_u32 m0, %18, 0x400\n\t" "s_nop 0\n\t" "global_load_lds_dwordx4 v225, s[42:43]\n\t" "s_add_u32 m0, %18, 0x800\n\t" "s_nop 0\n\t" "global_load_lds_dwordx4 v226, s[42:43]\n\t"
                     "s_add_u32 s42, %21, 0x618000\n\t" "s_add_u32 s42, s44, s42\n\t" "s_addc_u32 s43, s45, 0\n\t" "s_add_u32 m0, %18, 0xc20\n\t" "s_nop 0\n\t" "global_load_lds_dwordx4 v224, s[42:43]\n\t"
                     "s_add_u32 m0, %18, 0x1020\n\t" "s_nop 0\n\t" "global_load_lds_dwordx4 v225, s[42:43]\n\t" "s_add_u32 m0, %18, 0x1420\n\t" "s_nop 0\n\t" "global_load_lds_dwordx4 v226, s[42:43]\n\t"
                     "s_sub_u32 s42, 0x6c0000, %21\n\t" "s_add_u32 s42, s44, s42\n\t" "s_addc_u32 s43, s45, 0\n\t" "s_add_u32 m0, %18, 0x6100\n\t" "s_nop 0\n\t" "global_load_lds_dwordx4 v224, s[42:43]\n\t"
                     "s_add_u32 m0, %18, 0x6500\n\t" "s_nop 0\n\t" "global_load_lds_dwordx4 v225, s[42:43]\n\t" "s_add_u32 m0, %18, 0x6900\n\t" "s_nop 0\n\t" "global_load_lds_dwordx4 v226, s[42:43]\n\t"
                     "s_sub_u32 s42, 0x6d8000, %21\n\t" "s_add_u32 s42, s44, s42\n\t" "s_addc_u32 s43, s45, 0\n\t" "s_add_u32 m0, %18, 0x6d20\n\t" "s_nop 0\n\t" "global_load_lds_dwordx4 v224, s[42:43]\n\t"
                     "s_add_u32 m0, %18, 0x7120\n\t" "s_nop 0\n\t" "global_load_lds_dwordx4 v225, s[42:43]\n\t" "s_add_u32 m0, %18, 0x7520\n\t" "s_nop 0\n\t" "global_load_lds_dwordx4 v226, s[42:43]\n\t"
                     "global_load_dword %14, %14, %20 sc1\n\t" "v_add_u32 v232, 0xc200, v227\n\t" "v_add_u32 v233, 0xc200, v228\n\t" "ds_read_b64_tr_b16 v[192:193], v232\n\t"
                     "ds_read_b64_tr_b16 v[194:195], v232 offset:768\n\t" "ds_read_b64_tr_b16 v[196:197], v232 offset:12416\n\t" "ds_read_b64_tr_b16 v[198:199], v232 offset:13184\n\t"
                     "ds_read_b64_tr_b16 v[64:65], v232 offset:32\n\t" "ds_read_b64_tr_b16 v[66:67], v232 offset:800\n\t" "ds_read_b64_tr_b16 v[68:69], v232 offset:12448\n\t"
                     "ds_read_b64_tr_b16 v[70:71], v232 offset:13216\n\t" "ds_read_b64_tr_b16 v[200:201], v233 offset:64\n\t" "ds_read_b64_tr_b16 v[202:203], v233 offset:832\n\t"
                     "ds_read_b64_tr_b16 v[204:205], v233 offset:12480\n\t" "ds_read_b64_tr_b16 v[206:207], v233 offset:13248\n\t" "ds_read_b64_tr_b16 v[208:209], v232 offset:24832\n\t"
                     "ds_read_b64_tr_b16 v[210:211], v232 offset:25600\n\t" "ds_read_b64_tr_b16 v[212:213], v232 offset:37248\n\t" "ds_read_b64_tr_b16 v[214:215], v232 offset:38016\n\t"
                     "ds_read_b64_tr_b16 v[80:81], v232 offset:24864\n\t" "ds_read_b64_tr_b16 v[82:83], v232 offset:25632\n\t" "ds_read_b64_tr_b16 v[84:85], v232 offset:37280\n\t"
                     "ds_read_b64_tr_b16 v[86:87], v232 offset:38048\n\t" "ds_read_b64_tr_b16 v[216:217], v233 offset:24896\n\t" "ds_read_b64_tr_b16 v[218:219], v233 offset:25664\n\t"
                     "ds_read_b64_tr_b16 v[220:221], v233 offset:37312\n\t" "ds_read_b64_tr_b16 v[222:223], v233 offset:38080\n\t" "s_waitcnt vmcnt(13) lgkmcnt(0)\n\t" "s_barrier\n\t" "v_add_u32 v232, 0x18400, v227\n\t"
                     "v_add_u32 v233, 0x18400, v228\n\t" "ds_read_b64_tr_b16 a[0:1], v232\n\t" "ds_read_b64_tr_b16 a[2:3], v232 offset:768\n\t" "ds_read_b64_tr_b16 a[4:5], v232 offset:12416\n\t"
                     "ds_read_b64_tr_b16 a[6:7], v232 offset:13184\n\t" "ds_read_b64_tr_b16 v[96:97], v232 offset:32\n\t" "ds_read_b64_tr_b16 v[98:99], v232 offset:800\n\t"
                     "ds_read_b64_tr_b16 v[100:101], v232 offset:12448\n\t" "ds_read_b64_tr_b16 v[102:103], v232 offset:13216\n\t" "ds_read_b64_tr_b16 a[8:9], v233 offset:64\n\t"
                     "ds_read_b64_tr_b16 a[10:11], v233 offset:832\n\t" "ds_read_b64_tr_b16 a[12:13], v233 offset:12480\n\t" "ds_read_b64_tr_b16 a[14:15], v233 offset:13248\n\t"
                     "ds_read_b64_tr_b16 a[16:17], v232 offset:24832\n\t" "ds_read_b64_tr_b16 a[18:19], v232 offset:25600\n\t" "ds_read_b64_tr_b16 a[20:21], v232 offset:37248\n\t"
                     "ds_read_b64_tr_b16 a[22:23], v232 offset:38016\n\t" "ds_read_b64_tr_b16 v[112:113], v232 offset:24864\n\t" "ds_read_b64_tr_b16 v[114:115], v232 offset:25632\n\t"
                     "ds_read_b64_tr_b16 v[116:117], v232 offset:37280\n\t" "ds_read_b64_tr_b16 v[118:119], v232 offset:38048\n\t" "ds_read_b64_tr_b16 a[24:25], v233 offset:24896\n\t"
                     "ds_read_b64_tr_b16 a[26:27], v233 offset:25664\n\t" "ds_read_b64_tr_b16 a[28:29], v233 offset:37312\n\t" "ds_read_b64_tr_b16 a[30:31], v233 offset:38080\n\t" "s_waitcnt vmcnt(0) lgkmcnt(0)\n\t"
                     "s_barrier\n\t" "v_add_u32 v232, 0x0, v227\n\t" "v_add_u32 v233, 0x0, v228\n\t" "ds_read_b64_tr_b16 a[32:33], v232\n\t" "ds_read_b64_tr_b16 a[34:35], v232 offset:768\n\t"
                     "ds_read_b64_tr_b16 a[36:37], v232 offset:12416\n\t" "ds_read_b64_tr_b16 a[38:39], v232 offset:13184\n\t" "ds_read_b64_tr_b16 a[40:41], v232 offset:32\n\t"
                     "ds_read_b64_tr_b16 a[42:43], v232 offset:800\n\t" "ds_read_b64_tr_b16 a[44:45], v232 offset:12448\n\t" "ds_read_b64_tr_b16 a[46:47], v232 offset:13216\n\t"
                     "ds_read_b64_tr_b16 a[48:49], v233 offset:64\n\t" "ds_read_b64_tr_b16 a[50:51], v233 offset:832\n\t" "ds_read_b64_tr_b16 a[52:53], v233 offset:12480\n\t"
                     "ds_read_b64_tr_b16 a[54:55], v233 offset:13248\n\t" "ds_read_b64_tr_b16 a[64:65], v232 offset:24832\n\t" "ds_read_b64_tr_b16 a[66:67], v232 offset:25600\n\t"
                     "ds_read_b64_tr_b16 a[68:69], v232 offset:37248\n\t" "ds_read_b64_tr_b16 a[70:71], v232 offset:38016\n\t" "ds_read_b64_tr_b16 a[72:73], v232 offset:24864\n\t"
                     "ds_read_b64_tr_b16 a[74:75], v232 offset:25632\n\t" "ds_read_b64_tr_b16 a[76:77], v232 offset:37280\n\t" "ds_read_b64_tr_b16 a[78:79], v232 offset:38048\n\t"
                     "ds_read_b64_tr_b16 a[80:81], v233 offset:24896\n\t" "ds_read_b64_tr_b16 a[82:83], v233 offset:25664\n\t" "ds_read_b64_tr_b16 a[84:85], v233 offset:37312\n\t"
                     "ds_read_b64_tr_b16 a[86:87], v233 offset:38080\n\t" "s_waitcnt lgkmcnt(0)\n\t" "s_barrier\n\t" "ds_write_b128 v230, v[0:3]\n\t" "ds_write_b128 v230, v[4:7] offset:1024\n\t"
                     "ds_write_b128 v230, v[8:11] offset:2048\n\t" "ds_write_b128 v230, v[12:15] offset:3072\n\t" "ds_write_b128 v230, v[16:19] offset:4096\n\t" "ds_write_b128 v230, v[20:23] offset:5120\n\t"
                     "ds_write_b128 v230, v[24:27] offset:6144\n\t" "ds_write_b128 v230, v[28:31] offset:7168\n\t" "ds_write_b128 v230, v[32:35] offset:8192\n\t" "ds_write_b128 v230, v[36:39] offset:9216\n\t"
                     "ds_write_b128 v230, v[40:43] offset:10240\n\t" "ds_write_b128 v230, v[44:47] offset:11264\n\t" "ds_write_b128 v230, v[48:51] offset:12288\n\t" "ds_write_b128 v230, v[52:55] offset:13312\n\t"
                     "ds_write_b128 v230, v[56:59] offset:14336\n\t" "ds_write_b128 v230, v[60:63] offset:15360\n\t" "ds_write_b128 v230, v[64:67] offset:16384\n\t" "ds_write_b128 v230, v[68:71] offset:17408\n\t"
                     "ds_write_b128 v230, v[72:75] offset:18432\n\t" "ds_write_b128 v230, v[76:79] offset:19456\n\t" "ds_write_b128 v230, v[80:83] offset:20480\n\t" "ds_write_b128 v230, v[84:87] offset:21504\n\t"
                     "ds_write_b128 v230, v[88:91] offset:22528\n\t" "ds_write_b128 v230, v[92:95] offset:23552\n\t" "ds_write_b128 v230, v[96:99] offset:24576\n\t" "ds_write_b128 v230, v[100:103] offset:25600\n\t"
                     "ds_write_b128 v230, v[104:107] offset:26624\n\t" "ds_write_b128 v230, v[108:111] offset:27648\n\t" "ds_write_b128 v230, v[112:115] offset:28672\n\t" "ds_write_b128 v230, v[116:119] offset:29696\n\t"
                     "ds_write_b128 v230, v[120:123] offset:30720\n\t" "ds_write_b128 v230, v[124:127] offset:31744\n\t"
                     : "=&{a[0:31]}"(wa[0]), "=&{a[32:63]}"(wa[1]), "=&{a[64:95]}"(wa[2]), "=&{a[96:127]}"(wa[3]), "=&{a[128:159]}"(wa[4]), "=&{a[160:191]}"(wa[5]), "=&{a[192:223]}"(wa[6]), "=&{a[224:255]}"(wa[7]), "=&{v[128:159]}"(wv[0]), "=&{v[160:191]}"(wv[1]), "=&{v[192:223]}"(wv[2]), "=&v"(y.r), "=&v"(y.g), "=&v"(y.e), "=&v"(kfl)
                     : "{v[224:231]}"(pa), "{s[44:45]}"(p.wq + h * DH * 2 + (size_t)(16 * w) * (HD * 2)), "{s[46:47]}"(p.wk + h * DH * 2 + (size_t)(16 * w) * (HD * 2)),
                       "s"(lds + 2 * w * GS), "s"(p.ws + OFF_LAT), "s"(fl + (F_K + h) * 128), "s"((h & 4) * 16 * HD * 2)
                     : "v0", "v1", "v2", "v3", "v4", "v5", "v6", "v7", "v8", "v9", "v10", "v11", "v12", "v13", "v14", "v15", "v16", "v17", "v18", "v19", "v20", "v21", "v22", "v23", "v24", "v25", "v26", "v27", "v28", "v29", "v30", "v31", "v32", "v33", "v34", "v35", "v36", "v37", "v38", "v39", "v40", "v41", "v42", "v43", "v44", "v45", "v46", "v47", "v48", "v49", "v50", "v51", "v52", "v53", "v54", "v55", "v56", "v57", "v58", "v59", "v60", "v61", "v62", "v63", "v64", "v65", "v66", "v67", "v68", "v69", "v70", "v71", "v72", "v73", "v74", "v75", "v76", "v77", "v78", "v79", "v80", "v81", "v82", "v83", "v84", "v85", "v86", "v87", "v88", "v89", "v90", "v91", "v92", "v93", "v94", "v95", "v96", "v97", "v98", "v99", "v100", "v101", "v102", "v103", "v104", "v105", "v106", "v107", "v108", "v109", "v110", "v111", "v112", "v113", "v114", "v115", "v116", "v117", "v118", "v119", "v120", "v121", "v122", "v123", "v124", "v125", "v126", "v127", "v232", "v233", "s42", "s43", "scc", "memory");
        const u64 t = sync_tag(y.e);
        const bool rdy = __builtin_amdgcn_ballot_w64((y.r[0] & ~1u) != (u32)t || y.r[1] != (u32)(t >> 32) || y.g[0] != (u32)t) == 0 && uni((int)kfl) != -1;
        if (rdy) kxdma();
        sd = sync_end(y, p.ws, phi, w == 0);
        if (!rdy) { flag_wait(fl, F_K + h, 1); kxdma(); }
        kxp += NH * 12288;
    }
    const u32 sidx = (lane & 2) ? 0xEEEEu : 0x4444u;
    const u32 moff = lane * 16;
    u32 kxa = kxl - 16 + (n >> 3) * 6160 + (16 * g + 4 * (n & 3) + ((n >> 1) & 2)) * 16;
    const u64 mb0 = 0xffff0000ffff0000ull;
    const int tp = (3 * w + (w & 1)) >> 1, ts = 1 + 3 * (w >> 1);
    u32 qw0 = lds + SL_IMG + tp * 512 + lane * 8, qw1 = lds + SL_IMG + ts * 512 + lane * 8 + 4 * (w & 1);
    const u32 kw0 = lds + SL_IMG + 3072 + w * 288 + n * 16 + 4 * g;
    const u32 qr0 = lds + SL_IMG + (g >> 1) * 512 + w * 128 + 64 * (g & 1) + 8 * qq;
    const u32 kr0 = lds + SL_IMG + 3072 + (qq >> 1) * 288 + g * 64 + (qq & 1) * 16 + 8 * (pp & 1);
    const u32 xso = 32768 + (h >> 4) * 16384 + (h & 15) * 512 + (w >> 1) * 256 + (n >> 3) * 64 + (n & 4) * 32 + (w & 1) * 32 + (g & 1) * 16 + (n & 3) * 4 + (g >> 1) * 8192;
    const u32 pso = (n < 8 ? 20480 + (h >> 1) * 256 + (n & 2) * 16 + (h & 1) * 8 + (n & 4) : 18432 + (h >> 2) * 256 + (h & 1) * 8 + (h & 2) * 2) + w * 64 + (g & 1) * 16 + (g >> 1) * 8192;
    const u64 psm = 0x01aa01aa01aa01aaull;
    const u32 kmo = (u32)(((n >> 3) * 4 + g) * 128 + (n & 7) * 16);
    u32x16 ad = u32x16{moff, sidx, lfa, kxa, qw0, qw1, kw0, kr0 + 1152 * min(w, 2) + (pp >> 1) * 576, qr0, kr0, xso, pso, 0x3dd53b94u, kmo, kmo, kmo};
    asm volatile("" : "+{v[224:239]}"(ad));
    const char* memp = p.ws + mslot(sd, 0);
    char* kmp = p.ws + OFF_KM + h * 3072 + min(w, 2) * 1024;
    const float kscale = 0.07216878364870322f;
    int budget = 30000;
    Clock clk; clk.init();
    u32x32 wk[3];
    asm volatile("" : "={v[64:95]}"(wk[0]), "={v[96:127]}"(wk[1]), "={v[32:63]}"(wk[2]));
    const int kfo = (F_K + 32 + h) * 128;
    for (int s = 0; s < NSEG; ++s) {
        int tries = 0;
        u64 bad, tp8, en = ~0ull;
        u32 tt;
        int kf;
        while (true) {
            const u32 tgt = clk.sched(phi, 0, cfg.G, tries ? 2 : 0);
            asm volatile("s_cmp_lg_u32 %12, 0\n\t" "s_cbranch_scc1 .Lrt_s\n\t" "s_waitcnt vmcnt(0) lgkmcnt(0)\n\t" "s_barrier\n\t" "s_cmp_lg_u32 %13, 0\n\t" "s_cbranch_scc1 .Lsp_s\n\t" ".Lrt_s:\n\t"
                         "ds_read_b128 v[64:67], v226\n\t" "v_mov_b64 v[40:41], 0\n\t" "ds_read_b128 v[68:71], v226 offset:1024\n\t" "v_mov_b64 v[42:43], 0\n\t" "ds_read_b128 v[72:75], v226 offset:2048\n\t"
                         "v_mov_b64 v[44:45], 0\n\t" "ds_read_b128 v[76:79], v226 offset:3072\n\t" "v_mov_b64 v[46:47], 0\n\t" "ds_read_b128 v[80:83], v226 offset:4096\n\t" "v_mov_b64 v[48:49], 0\n\t"
                         "ds_read_b128 v[84:87], v226 offset:5120\n\t" "v_mov_b64 v[50:51], 0\n\t" "ds_read_b128 v[88:91], v226 offset:6144\n\t" "v_mov_b64 v[52:53], 0\n\t" "ds_read_b128 v[92:95], v226 offset:7168\n\t"
                         "v_mov_b64 v[54:55], 0\n\t" "ds_read_b128 v[96:99], v226 offset:8192\n\t" "v_mov_b64 v[56:57], 0\n\t" "ds_read_b128 v[100:103], v226 offset:9216\n\t" "v_mov_b64 v[58:59], 0\n\t"
                         "ds_read_b128 v[104:107], v226 offset:10240\n\t" "v_mov_b64 v[60:61], 0\n\t" "ds_read_b128 v[108:111], v226 offset:11264\n\t" "v_mov_b64 v[62:63], 0\n\t"
                         "ds_read_b128 v[112:115], v226 offset:12288\n\t" "ds_read_b128 v[116:119], v226 offset:13312\n\t" "ds_read_b128 v[120:123], v226 offset:14336\n\t" "ds_read_b128 v[124:127], v226 offset:15360\n\t"
                         ".Lsp_s:\n\t" "s_memtime vcc\n\t" "s_waitcnt lgkmcnt(0)\n\t" "s_sub_i32 s42, %4, vcc_lo\n\t" "s_cmp_gt_i32 s42, 80\n\t" "s_cbranch_scc1 .Lsp_s\n\t" "s_max_i32 s42, s42, 0\n\t"
                         "s_sub_i32 s42, 92, s42\n\t" "s_and_b32 s42, s42, -4\n\t" "s_getpc_b64 vcc\n\t" "s_add_u32 vcc_lo, vcc_lo, s42\n\t" "s_addc_u32 vcc_hi, vcc_hi, 0\n\t" "s_setpc_b64 vcc\n\t" "s_nop 0\n\t" "s_nop 0\n\t"
                         "s_nop 0\n\t" "s_nop 0\n\t" "s_nop 0\n\t" "s_nop 0\n\t" "s_nop 0\n\t" "s_nop 0\n\t" "s_nop 0\n\t" "s_nop 0\n\t" "s_nop 0\n\t" "s_nop 0\n\t" "s_nop 0\n\t" "s_nop 0\n\t" "s_nop 0\n\t" "s_nop 0\n\t"
                         "s_nop 0\n\t" "s_nop 0\n\t" "s_nop 0\n\t" "s_nop 0\n\t" "global_load_dwordx4 v[0:3], v224, %5 offset:-4096 sc1\n\t" "v_add_u32 v238, 0x4000, v224\n\t" "s_nop 9\n\t"
                         "global_load_dwordx4 v[4:7], v224, %5 offset:-2048 sc1\n\t" "s_nop 15\n\t" "global_load_dwordx4 v[8:11], v224, %6 offset:-3072 sc1\n\t" "s_nop 10\n\t"
                         "global_load_dwordx4 v[12:15], v224, %6 offset:-1024 sc1\n\t" "s_nop 10\n\t" "global_load_dwordx4 v[16:19], v224, %5 sc1\n\t" "s_nop 10\n\t" "global_load_dwordx4 v[20:23], v224, %6 offset:1024 sc1\n\t"
                         "s_nop 10\n\t" "global_load_dwordx4 v[24:27], v224, %5 offset:2048 sc1\n\t" "s_nop 10\n\t" "global_load_dwordx4 v[28:31], v224, %6 offset:3072 sc1\n\t" "s_nop 10\n\t"
                         "global_load_dwordx4 v[32:35], v238, %5 offset:-4096 sc1\n\t" "s_nop 10\n\t" "global_load_dwordx4 v[36:39], v238, %6 offset:-3072 sc1\n\t" "s_waitcnt vmcnt(9)\n\t"
                         "v_smfmac_f32_16x16x64_bf16 v[40:43], v[0:3], v[128:135], v225\n\t" "v_smfmac_f32_16x16x64_bf16 v[44:47], v[0:3], v[64:71], v225\n\t" "ds_read_b128 v[64:67], v226 offset:16384\n\t"
                         "v_smfmac_f32_16x16x64_bf16 v[48:51], v[0:3], v[136:143], v225\n\t" "ds_read_b128 v[68:71], v226 offset:17408\n\t" "v_smfmac_f32_16x16x64_bf16 v[52:55], v[0:3], v[72:79], v225\n\t"
                         "ds_read_b128 v[72:75], v226 offset:18432\n\t" "s_waitcnt vmcnt(8)\n\t" "v_smfmac_f32_16x16x64_bf16 v[40:43], v[4:7], v[144:151], v225\n\t" "ds_read_b128 v[76:79], v226 offset:19456\n\t"
                         "v_smfmac_f32_16x16x64_bf16 v[44:47], v[4:7], v[80:87], v225\n\t" "ds_read_b128 v[80:83], v226 offset:20480\n\t" "v_smfmac_f32_16x16x64_bf16 v[48:51], v[4:7], v[152:159], v225\n\t"
                         "ds_read_b128 v[84:87], v226 offset:21504\n\t" "v_smfmac_f32_16x16x64_bf16 v[52:55], v[4:7], v[88:95], v225\n\t" "ds_read_b128 v[88:91], v226 offset:22528\n\t" "s_waitcnt vmcnt(7)\n\t"
                         "v_smfmac_f32_16x16x64_bf16 v[40:43], v[8:11], v[160:167], v225\n\t" "ds_read_b128 v[92:95], v226 offset:23552\n\t" "v_max_u32 v238, v0, v4\n\t"
                         "v_smfmac_f32_16x16x64_bf16 v[44:47], v[8:11], v[96:103], v225\n\t" "ds_read_b128 v[96:99], v226 offset:24576\n\t" "v_cmp_eq_u32 vcc, -1, v238\n\t"
                         "v_smfmac_f32_16x16x64_bf16 v[48:51], v[8:11], v[168:175], v225\n\t" "ds_read_b128 v[100:103], v226 offset:25600\n\t" "v_smfmac_f32_16x16x64_bf16 v[52:55], v[8:11], v[104:111], v225\n\t"
                         "s_and_b64 vcc, vcc, %11\n\t" "s_cbranch_scc1 .Lsk_s\n\t" "ds_read_b128 v[104:107], v226 offset:26624\n\t" "s_waitcnt vmcnt(6)\n\t" "v_smfmac_f32_16x16x64_bf16 v[40:43], v[12:15], v[176:183], v225\n\t"
                         "ds_read_b128 v[108:111], v226 offset:27648\n\t" "v_smfmac_f32_16x16x64_bf16 v[44:47], v[12:15], v[112:119], v225\n\t" "ds_read_b128 v[112:115], v226 offset:28672\n\t"
                         "v_smfmac_f32_16x16x64_bf16 v[48:51], v[12:15], v[184:191], v225\n\t" "ds_read_b128 v[116:119], v226 offset:29696\n\t" "v_smfmac_f32_16x16x64_bf16 v[52:55], v[12:15], v[120:127], v225\n\t"
                         "ds_read_b128 v[120:123], v226 offset:30720\n\t" "s_waitcnt vmcnt(5)\n\t" "v_smfmac_f32_16x16x64_bf16 v[40:43], v[16:19], v[192:199], v225\n\t" "ds_read_b128 v[124:127], v226 offset:31744\n\t"
                         "s_waitcnt lgkmcnt(14)\n\t" "v_smfmac_f32_16x16x64_bf16 v[44:47], v[16:19], v[64:71], v225\n\t" "ds_read_b128 v[64:67], v227\n\t" "v_smfmac_f32_16x16x64_bf16 v[48:51], v[16:19], v[200:207], v225\n\t"
                         "ds_read_b128 v[68:71], v227 offset:1024\n\t" "s_waitcnt lgkmcnt(14)\n\t" "v_smfmac_f32_16x16x64_bf16 v[52:55], v[16:19], v[72:79], v225\n\t" "ds_read_b128 v[72:75], v227 offset:16\n\t"
                         "s_waitcnt vmcnt(4)\n\t" "v_smfmac_f32_16x16x64_bf16 v[40:43], v[20:23], v[208:215], v225\n\t" "ds_read_b128 v[76:79], v227 offset:1040\n\t" "s_waitcnt lgkmcnt(14)\n\t"
                         "v_smfmac_f32_16x16x64_bf16 v[44:47], v[20:23], v[80:87], v225\n\t" "ds_read_b128 v[80:83], v227 offset:2048\n\t" "v_smfmac_f32_16x16x64_bf16 v[48:51], v[20:23], v[216:223], v225\n\t"
                         "ds_read_b128 v[84:87], v227 offset:3072\n\t" "s_waitcnt lgkmcnt(14)\n\t" "v_smfmac_f32_16x16x64_bf16 v[52:55], v[20:23], v[88:95], v225\n\t" "ds_read_b128 v[88:91], v227 offset:2064\n\t"
                         "s_waitcnt vmcnt(3)\n\t" "v_smfmac_f32_16x16x64_bf16 v[40:43], v[24:27], a[0:7], v225\n\t" "ds_read_b128 v[92:95], v227 offset:3088\n\t" "s_waitcnt lgkmcnt(14)\n\t"
                         "v_smfmac_f32_16x16x64_bf16 v[44:47], v[24:27], v[96:103], v225\n\t" "ds_read_b128 v[96:99], v227 offset:4096\n\t" "v_smfmac_f32_16x16x64_bf16 v[48:51], v[24:27], a[8:15], v225\n\t"
                         "ds_read_b128 v[100:103], v227 offset:5120\n\t" "s_waitcnt lgkmcnt(14)\n\t" "v_smfmac_f32_16x16x64_bf16 v[52:55], v[24:27], v[104:111], v225\n\t" "ds_read_b128 v[104:107], v227 offset:4112\n\t"
                         "s_waitcnt vmcnt(2)\n\t" "v_smfmac_f32_16x16x64_bf16 v[40:43], v[28:31], a[16:23], v225\n\t" "ds_read_b128 v[108:111], v227 offset:5136\n\t" "s_waitcnt lgkmcnt(14)\n\t"
                         "v_smfmac_f32_16x16x64_bf16 v[44:47], v[28:31], v[112:119], v225\n\t" "v_smfmac_f32_16x16x64_bf16 v[48:51], v[28:31], a[24:31], v225\n\t" "s_waitcnt lgkmcnt(12)\n\t"
                         "v_smfmac_f32_16x16x64_bf16 v[52:55], v[28:31], v[120:127], v225\n\t" "s_waitcnt vmcnt(1)\n\t" "v_smfmac_f32_16x16x64_bf16 v[40:43], v[32:35], a[32:39], v225\n\t" "v_mov_b64 v[112:113], 0\n\t"
                         "v_smfmac_f32_16x16x64_bf16 v[44:47], v[32:35], a[40:47], v225\n\t" "v_mov_b64 v[114:115], 0\n\t" "v_smfmac_f32_16x16x64_bf16 v[48:51], v[32:35], a[48:55], v225\n\t" "v_mov_b64 v[116:117], 0\n\t"
                         "v_smfmac_f32_16x16x64_bf16 v[52:55], v[32:35], a[56:63], v225\n\t" "v_mov_b64 v[118:119], 0\n\t" "s_waitcnt vmcnt(0)\n\t" "v_smfmac_f32_16x16x64_bf16 v[40:43], v[36:39], a[64:71], v225\n\t"
                         "v_mov_b64 v[120:121], 0\n\t" "v_smfmac_f32_16x16x64_bf16 v[44:47], v[36:39], a[72:79], v225\n\t" "v_mov_b64 v[122:123], 0\n\t" "v_smfmac_f32_16x16x64_bf16 v[48:51], v[36:39], a[80:87], v225\n\t"
                         "v_mov_b64 v[124:125], 0\n\t" "v_smfmac_f32_16x16x64_bf16 v[52:55], v[36:39], a[88:95], v225\n\t" "v_add_f32 v40, v40, v42\n\t" "v_add_f32 v41, v41, v43\n\t"
                         "v_smfmac_f32_16x16x64_bf16 v[56:59], v[0:3], a[96:103], v225\n\t" "v_cmp_u_f32 vcc, v40, v41\n\t" "v_smfmac_f32_16x16x64_bf16 v[56:59], v[4:7], a[104:111], v225\n\t"
                         "v_cvt_pk_bf16_f32 v42, v40, v41\n\t" "v_add_f32 v44, v44, v46\n\t" "v_smfmac_f32_16x16x64_bf16 v[56:59], v[8:11], a[112:119], v225\n\t" "s_and_b64 vcc, vcc, %11\n\t" "s_cbranch_scc1 .Lsk_s\n\t"
                         "v_smfmac_f32_16x16x64_bf16 v[56:59], v[12:15], a[120:127], v225\n\t" "v_add_f32 v45, v45, v47\n\t" "v_cvt_pk_bf16_f32 v43, v44, v45\n\t"
                         "v_smfmac_f32_16x16x64_bf16 v[56:59], v[16:19], a[128:135], v225\n\t" "ds_write_b64 v228, v[42:43]\n\t" "v_add_f32 v48, v48, v50\n\t"
                         "v_smfmac_f32_16x16x64_bf16 v[56:59], v[20:23], a[136:143], v225\n\t" "v_add_f32 v49, v49, v51\n\t" "v_cvt_pk_bf16_f32 v46, v48, v49\n\t"
                         "v_smfmac_f32_16x16x64_bf16 v[56:59], v[24:27], a[144:151], v225\n\t" "ds_write_b32 v229, v46\n\t" "v_add_f32 v52, v52, v54\n\t" "v_smfmac_f32_16x16x64_bf16 v[56:59], v[28:31], a[152:159], v225\n\t"
                         "v_add_f32 v53, v53, v55\n\t" "v_cvt_pk_bf16_f32 v54, v52, v53\n\t" "v_smfmac_f32_16x16x64_bf16 v[56:59], v[32:35], a[160:167], v225\n\t" "ds_write_b32 v230, v54\n\t"
                         "v_smfmac_f32_16x16x64_bf16 v[56:59], v[36:39], a[168:175], v225\n\t" "v_smfmac_f32_16x16x64_bf16 v[60:63], v[0:3], a[176:183], v225\n\t"
                         "v_smfmac_f32_16x16x64_bf16 v[60:63], v[4:7], a[184:191], v225\n\t" "v_add_f32 v56, v56, v58\n\t" "v_smfmac_f32_16x16x64_bf16 v[60:63], v[8:11], a[192:199], v225\n\t" "s_waitcnt lgkmcnt(0)\n\t"
                         "s_barrier\n\t" "v_smfmac_f32_16x16x64_bf16 v[60:63], v[12:15], a[200:207], v225\n\t" "ds_read_b64_tr_b16 v[40:41], v232\n\t" "v_add_f32 v57, v57, v59\n\t"
                         "v_smfmac_f32_16x16x64_bf16 v[60:63], v[16:19], a[208:215], v225\n\t" "ds_read_b64_tr_b16 v[42:43], v232 offset:32\n\t" "v_cvt_pk_bf16_f32 v58, v56, v57\n\t"
                         "v_smfmac_f32_16x16x64_bf16 v[60:63], v[20:23], a[216:223], v225\n\t" "ds_write_b32 v230, v58 offset:1152\n\t" "v_smfmac_f32_16x16x64_bf16 v[60:63], v[24:27], a[224:231], v225\n\t"
                         "ds_read_b64_tr_b16 v[44:45], v232 offset:1024\n\t" "ds_read_b64_tr_b16 v[46:47], v232 offset:1056\n\t" "v_smfmac_f32_16x16x64_bf16 v[60:63], v[28:31], a[232:239], v225\n\t"
                         "ds_read_b64_tr_b16 v[48:49], v232 offset:2048\n\t" "ds_read_b64_tr_b16 v[50:51], v232 offset:2080\n\t" "v_smfmac_f32_16x16x64_bf16 v[60:63], v[32:35], a[240:247], v225\n\t"
                         "ds_read_b64_tr_b16 v[0:1], v233\n\t" "ds_read_b64_tr_b16 v[2:3], v233 offset:32\n\t" "v_smfmac_f32_16x16x64_bf16 v[60:63], v[36:39], a[248:255], v225\n\t"
                         "ds_read_b64_tr_b16 v[4:5], v233 offset:576\n\t" "ds_read_b64_tr_b16 v[6:7], v233 offset:608\n\t" "s_waitcnt lgkmcnt(9)\n\t" "v_smfmac_f32_16x16x64_bf16 v[112:115], v[40:43], v[64:71], v225\n\t"
                         "s_waitcnt lgkmcnt(8)\n\t" "s_barrier\n\t" "v_smfmac_f32_16x16x64_bf16 v[116:119], v[40:43], v[72:79], v225\n\t" "ds_read_b64_tr_b16 v[8:9], v233 offset:1152\n\t" "s_waitcnt lgkmcnt(7)\n\t"
                         "v_add_f32 v60, v60, v62\n\t" "v_add_f32 v61, v61, v63\n\t" "v_smfmac_f32_16x16x64_bf16 v[112:115], v[44:47], v[80:87], v225\n\t" "ds_read_b64_tr_b16 v[10:11], v233 offset:1184\n\t"
                         "v_cvt_pk_bf16_f32 v62, v60, v61\n\t" "v_smfmac_f32_16x16x64_bf16 v[116:119], v[44:47], v[88:95], v225\n\t" "ds_write_b32 v230, v62 offset:2304\n\t" "s_waitcnt lgkmcnt(7)\n\t"
                         "v_smfmac_f32_16x16x64_bf16 v[112:115], v[48:51], v[96:103], v225\n\t" "ds_read_b64_tr_b16 v[12:13], v233 offset:1728\n\t" "v_smfmac_f32_16x16x64_bf16 v[116:119], v[48:51], v[104:111], v225\n\t"
                         "ds_read_b64_tr_b16 v[14:15], v233 offset:1760\n\t" "s_waitcnt lgkmcnt(5)\n\t" "v_smfmac_f32_16x16x64_bf16 v[120:123], v[40:43], v[0:7], v225\n\t" "v_cndmask_b32 v24, v112, v113, %8\n\t"
                         "v_cndmask_b32 v25, v114, v115, %8\n\t" "v_add_f32 v24, v24, v25\n\t" "v_pk_add_f32 v[26:27], v[116:117], v[118:119]\n\t" "v_cndmask_b32 v25, v26, v27, %8\n\t" "v_max_f32 v26, v24, v25\n\t"
                         "s_waitcnt lgkmcnt(2)\n\t" "s_barrier\n\t" "ds_read_b64_tr_b16 v[16:17], v233 offset:2304\n\t" "v_max_f32_dpp v27, v26, v26 row_ror:4 row_mask:0xf bank_mask:0xf\n\t"
                         "ds_read_b64_tr_b16 v[18:19], v233 offset:2336\n\t" "v_max_f32_dpp v27, v26, v27 row_ror:8 row_mask:0xf bank_mask:0xf\n\t" "v_max_f32_dpp v27, v26, v27 row_ror:12 row_mask:0xf bank_mask:0xf\n\t"
                         "ds_read_b64_tr_b16 v[20:21], v233 offset:2880\n\t" "ds_read_b64_tr_b16 v[22:23], v233 offset:2912\n\t" "v_max_f32_dpp v30, v27, v27 row_ror:1 row_mask:0xf bank_mask:0xf\n\t"
                         "v_max_f32_dpp v30, v27, v30 row_ror:2 row_mask:0xf bank_mask:0xf\n\t" "v_max_f32_dpp v30, v27, v30 row_ror:3 row_mask:0xf bank_mask:0xf\n\t" "s_waitcnt lgkmcnt(4)\n\t"
                         "v_pk_add_f32 v[24:25], v[24:25], v[30:31] op_sel_hi:[1,0] neg_lo:[0,1] neg_hi:[0,1]\n\t" "v_pk_mul_f32 v[24:25], v[24:25], v[236:237] op_sel_hi:[1,0]\n\t" "v_exp_f32 v24, v24\n\t"
                         "v_exp_f32 v25, v25\n\t" "v_mul_f32 v31, %10, v30\n\t" "v_cvt_pk_bf16_f32 v28, |v24|, |v25|\n\t" "v_smfmac_f32_16x16x64_bf16 v[120:123], v[44:47], v[8:15], v225\n\t"
                         "global_store_dword v234, v28, %7 offset:-4096 sc1\n\t" "s_waitcnt lgkmcnt(0)\n\t" "v_add_f32 v26, v24, v25\n\t" "v_smfmac_f32_16x16x64_bf16 v[120:123], v[48:51], v[16:23], v225\n\t"
                         "s_nop 0\n\t" "v_add_f32_dpp v27, v26, v26 row_ror:4 row_mask:0xf bank_mask:0xf\n\t" "v_add_f32_dpp v27, v26, v27 row_ror:8 row_mask:0xf bank_mask:0xf\n\t"
                         "v_add_f32_dpp v27, v26, v27 row_ror:12 row_mask:0xf bank_mask:0xf\n\t" "s_nop 1\n\t" "v_add_f32_dpp v29, v27, v27 row_ror:1 row_mask:0xf bank_mask:0xf\n\t"
                         "v_pk_add_f32 v[24:25], v[120:121], v[122:123]\n\t" "v_cndmask_b32 v24, v24, v25, %8\n\t" "v_max_f32 v26, v24, v30\n\t" "v_add_f32_dpp v29, v27, v29 row_ror:2 row_mask:0xf bank_mask:0xf\n\t"
                         "v_add_f32_dpp v29, v27, v29 row_ror:3 row_mask:0xf bank_mask:0xf\n\t" "v_max_f32_dpp v27, v26, v26 row_ror:1 row_mask:0xf bank_mask:0xf\n\t"
                         "v_max_f32_dpp v27, v26, v27 row_ror:2 row_mask:0xf bank_mask:0xf\n\t" "v_max_f32_dpp v27, v26, v27 row_ror:3 row_mask:0xf bank_mask:0xf\n\t" "v_mul_f32 v24, %10, v24\n\t"
                         "v_mul_f32 v29, 0x3e000000, v29\n\t" "v_max_f32_dpp v26, v27, v27 row_ror:4 row_mask:0xf bank_mask:0xf\n\t" "v_fma_f32 v24, -v26, %10, v24\n\t" "v_fma_f32 v25, -v26, %10, v31\n\t"
                         "v_exp_f32 v24, v24\n\t" "v_exp_f32 v25, v25\n\t" "s_nop 0\n\t" "v_fma_f32 v26, v25, v29, v24\n\t" "v_mov_b32_dpp v124, v24 quad_perm:[0,1,2,3] row_mask:0xf bank_mask:0x3\n\t"
                         "v_mov_b32_dpp v125, v24 quad_perm:[1,0,3,2] row_mask:0xf bank_mask:0x3\n\t" "v_add_f32_dpp v27, v26, v26 row_ror:1 row_mask:0xf bank_mask:0xf\n\t"
                         "v_add_f32_dpp v27, v26, v27 row_ror:2 row_mask:0xf bank_mask:0xf\n\t" "v_add_f32_dpp v27, v26, v27 row_ror:3 row_mask:0xf bank_mask:0xf\n\t"
                         "v_mov_b32_dpp v124, v25 quad_perm:[0,1,2,3] row_mask:0x5 bank_mask:0x4\n\t" "v_mov_b32_dpp v125, v25 quad_perm:[0,1,2,3] row_mask:0xa bank_mask:0x4\n\t"
                         "v_add_f32_dpp v26, v27, v27 row_ror:4 row_mask:0xf bank_mask:0xf\n\t" "v_rcp_f32 v26, v26\n\t" "s_mov_b64 exec, %9\n\t" "v_pk_mul_f32 v[124:125], v[124:125], v[26:27] op_sel_hi:[1,0]\n\t"
                         "v_cvt_pk_bf16_f32 v24, |v124|, |v125|\n\t" "global_store_dword v235, v24, %7 offset:-4096 sc1\n\t" "s_mov_b64 exec, -1\n\t" "s_memrealtime %1\n\t" "s_load_dword %2, %14, %13 offset:%15\n\t"
                         "ds_read_b64_tr_b16 v[36:37], v231\n\t" "ds_read_b64_tr_b16 v[38:39], v231 offset:32\n\t" "v_readfirstlane_b32 %3, v237\n\t" "s_mov_b32 m0, %17\n\t" "v_mov_b64 v[40:41], 0\n\t"
                         "v_mov_b64 v[42:43], 0\n\t" "v_mov_b64 v[44:45], 0\n\t" "v_mov_b64 v[46:47], 0\n\t" "v_mov_b64 v[48:49], 0\n\t" "v_mov_b64 v[50:51], 0\n\t" "v_mov_b64 v[52:53], 0\n\t" "v_mov_b64 v[54:55], 0\n\t"
                         "v_mov_b64 v[56:57], 0\n\t" "v_mov_b64 v[58:59], 0\n\t" "v_mov_b64 v[60:61], 0\n\t" "v_mov_b64 v[62:63], 0\n\t" "v_mov_b32 v237, %18\n\t" "s_waitcnt lgkmcnt(0)\n\t" "ds_read_b128 v[64:67], v226\n\t"
                         "ds_read_b128 v[68:71], v226 offset:1024\n\t" "ds_read_b128 v[72:75], v226 offset:2048\n\t" "ds_read_b128 v[76:79], v226 offset:3072\n\t" "ds_read_b128 v[80:83], v226 offset:4096\n\t"
                         "ds_read_b128 v[84:87], v226 offset:5120\n\t" "s_cmp_eq_u32 %3, -1\n\t" "s_cbranch_scc1 .Lnk_s\n\t" "global_load_lds_dwordx4 v224, %16 sc1\n\t" "global_load_lds_dwordx4 v224, %16 offset:1024 sc1\n\t"
                         "global_load_lds_dwordx4 v224, %16 offset:2048 sc1\n\t" ".Lnk_s:\n\t" "global_store_dwordx4 v239, v[36:39], %21 sc1\n\t" "ds_read_b128 v[88:91], v226 offset:6144\n\t"
                         "ds_read_b128 v[92:95], v226 offset:7168\n\t" "ds_read_b128 v[96:99], v226 offset:8192\n\t" "ds_read_b128 v[100:103], v226 offset:9216\n\t" "ds_read_b128 v[104:107], v226 offset:10240\n\t"
                         "ds_read_b128 v[108:111], v226 offset:11264\n\t" "ds_read_b128 v[112:115], v226 offset:12288\n\t" "ds_read_b128 v[116:119], v226 offset:13312\n\t" "ds_read_b128 v[120:123], v226 offset:14336\n\t"
                         "ds_read_b128 v[124:127], v226 offset:15360\n\t" "s_and_b64 exec, %19, 1\n\t" "s_cbranch_execz .Lnf_s\n\t" "s_sleep 4\n\t" "global_load_dword v237, v237, %20 sc1\n\t" ".Lnf_s:\n\t"
                         "s_mov_b64 exec, -1\n\t" ".Lsk_s:\n\t" "s_mov_b64 %0, vcc\n\t"
                         : "=&s"(bad), "=&s"(tp8), "=&s"(tt), "=&s"(kf)
                         : "s"(uni((int)tgt)), "s"(memp + (h & 4) * 256), "s"(memp - (h & 4) * 256), "s"((u64)memp & ~8192ull), "s"(mb0), "s"(psm), "s"(kscale * 1.4426950408889634f), "s"(en), "s"(tries), "s"(4 * s),
                           "s"(__builtin_amdgcn_kernarg_segment_ptr()), "n"(TT_OFF), "s"(kxp), "s"(kxw), "s"(kfo + (s < 6 ? 0 : (s + 2) >> 3) * 4096), "s"(0x0040404040404044ull >> s), "s"(fl), "s"(kmp),
                           "{v[224:239]}"(ad), "{v[64:95]}"(wk[0]), "{v[96:127]}"(wk[1]), "{v[32:63]}"(wk[2]),
                           "{a[0:31]}"(wa[0]), "{a[32:63]}"(wa[1]), "{a[64:95]}"(wa[2]), "{a[96:127]}"(wa[3]), "{a[128:159]}"(wa[4]), "{a[160:191]}"(wa[5]), "{a[192:223]}"(wa[6]), "{a[224:255]}"(wa[7]), "{v[128:159]}"(wv[0]), "{v[160:191]}"(wv[1]), "{v[192:223]}"(wv[2])
                         : "v0", "v1", "v2", "v3", "v4", "v5", "v6", "v7", "v8", "v9", "v10", "v11", "v12", "v13", "v14", "v15", "v16", "v17", "v18", "v19", "v20", "v21", "v22", "v23", "v24", "v25", "v26", "v27", "v28", "v29", "v30", "v31", "vcc", "scc", "s42", "memory");
            if (__builtin_expect(bad == 0, 1)) break;
            waitvm<0>();
            if (((++tries & 15) == 0 && !unwritten(memp, ad[0])) || --budget < 0) en = 0;
            phi += (u32)cfg.G;
        }
        if (__builtin_expect(kf == -1, 0)) { flag_wait(fl, F_K + pblk(s + 1) * 32 + h, 1); kxdma(); }
        kxp += s < NSEG - 2 ? NH * 12288 : 0;
        clk.calib();
        if (__builtin_expect(s >= NSEG - 2, 0)) {
            waitvm<0>();
            barrier();
            if (w == 3) flag_add(fl, F_PROG + 8 * s + (h & 7), 1);
        }
        follow(phi, (int)(tt >> 16) - (cfg.dV >> 24), (u32)tp8, cfg.G);
        phi += tt & 0xffffu;
        memp = p.ws + mslot(sd, s + 1); kmp += NH * 3072;
    }
    if (h == 0 && w == 0) {
        flag_wait(fl, F_ACK, 256);
        Sync y;
        sync_ld(y, p.ws);
        asm volatile("s_waitcnt vmcnt(0)" : "+v"(y.r), "+v"(y.g), "+v"(y.e) : : "memory");
        const u64 t = ((u64)y.e[1] << 32 | y.e[0]) + 1;
        if (lanex() == 0) gst8(p.ws + OFF_LAT, 1024, u32x2{(u32)t, (u32)(t >> 32)});
    }
    asm volatile(".Lkm_last:\n\ts_waitcnt vmcnt(0)" : : : "memory");
}

DI void proj_role(const Ptrs& p, const ChainCfg cfg, int h, u32 lds, int w) {
    u32x32 wa[8], wv[4];
    u32 phi;
    int sd;
    {
        const int lane = lanex(), tid = 64 * w + lane;
        const int g = lane >> 4, qq = (lane >> 2) & 3, pp = lane & 3;
        constexpr int GS = 3104;
        auto dvo = [&](int i) { const int ix = i * 64 + lane; return (u32)((ix / 24) * (HD * 2) + (ix % 24) * 16); };
        auto ovo = [&](int i) { const int u = i * 64 + lane, r = u / 80; return (u32)(r * 1280 + (((u % 80) ^ (2 * (r ^ (w & 2)))) << 4)); };
        const u32 tra = lds + (2 * (g >> 1) + (qq >> 1)) * GS + (4 * (g & 1) + (qq & 1)) * 384 + 96 * w + 32 * (w & 1) + 8 * pp;
        const u32 tro = (4 * g + (qq & 1) + 16 * (qq >> 1)) * 1280 + 8 * pp + ((u32)(2 * w ^ 2 * ((4 * g + qq) & 7)) << 4);
        u32x16 pa = u32x16{dvo(0), dvo(1), dvo(2), tra, tra - 96 * (w & 1), tro, lds + (u32)((w & 1) * 24576 + (w >> 1) * 99328 + lane * 16), (u32)(lane & 31) * 8,
                           ovo(0), ovo(1), ovo(2), ovo(3), ovo(4), tro, tro, tro};
        Sync y;
        asm volatile("s_nop 4\n\t" "s_add_u32 s42, %22, 0x0\n\t" "s_add_u32 s42, s44, s42\n\t" "s_addc_u32 s43, s45, 0\n\t" "s_add_u32 m0, %18, 0x0\n\t" "s_nop 0\n\t" "global_load_lds_dwordx4 v224, s[42:43]\n\t"
                     "s_add_u32 m0, %18, 0x400\n\t" "s_nop 0\n\t" "global_load_lds_dwordx4 v225, s[42:43]\n\t" "s_add_u32 m0, %18, 0x800\n\t" "s_nop 0\n\t" "global_load_lds_dwordx4 v226, s[42:43]\n\t"
                     "s_add_u32 s42, %22, 0x18000\n\t" "s_add_u32 s42, s44, s42\n\t" "s_addc_u32 s43, s45, 0\n\t" "s_add_u32 m0, %18, 0xc20\n\t" "s_nop 0\n\t" "global_load_lds_dwordx4 v224, s[42:43]\n\t"
                     "s_add_u32 m0, %18, 0x1020\n\t" "s_nop 0\n\t" "global_load_lds_dwordx4 v225, s[42:43]\n\t" "s_add_u32 m0, %18, 0x1420\n\t" "s_nop 0\n\t" "global_load_lds_dwordx4 v226, s[42:43]\n\t"
                     "s_sub_u32 s42, 0xc0000, %22\n\t" "s_add_u32 s42, s44, s42\n\t" "s_addc_u32 s43, s45, 0\n\t" "s_add_u32 m0, %18, 0x6100\n\t" "s_nop 0\n\t" "global_load_lds_dwordx4 v224, s[42:43]\n\t"
                     "s_add_u32 m0, %18, 0x6500\n\t" "s_nop 0\n\t" "global_load_lds_dwordx4 v225, s[42:43]\n\t" "s_add_u32 m0, %18, 0x6900\n\t" "s_nop 0\n\t" "global_load_lds_dwordx4 v226, s[42:43]\n\t"
                     "s_sub_u32 s42, 0xd8000, %22\n\t" "s_add_u32 s42, s44, s42\n\t" "s_addc_u32 s43, s45, 0\n\t" "s_add_u32 m0, %18, 0x6d20\n\t" "s_nop 0\n\t" "global_load_lds_dwordx4 v224, s[42:43]\n\t"
                     "s_add_u32 m0, %18, 0x7120\n\t" "s_nop 0\n\t" "global_load_lds_dwordx4 v225, s[42:43]\n\t" "s_add_u32 m0, %18, 0x7520\n\t" "s_nop 0\n\t" "global_load_lds_dwordx4 v226, s[42:43]\n\t"
                     "s_add_u32 s42, %22, 0x180000\n\t" "s_add_u32 s42, s44, s42\n\t" "s_addc_u32 s43, s45, 0\n\t" "s_add_u32 m0, %18, 0xc200\n\t" "s_nop 0\n\t" "global_load_lds_dwordx4 v224, s[42:43]\n\t"
                     "s_add_u32 m0, %18, 0xc600\n\t" "s_nop 0\n\t" "global_load_lds_dwordx4 v225, s[42:43]\n\t" "s_add_u32 m0, %18, 0xca00\n\t" "s_nop 0\n\t" "global_load_lds_dwordx4 v226, s[42:43]\n\t"
                     "s_add_u32 s42, %22, 0x198000\n\t" "s_add_u32 s42, s44, s42\n\t" "s_addc_u32 s43, s45, 0\n\t" "s_add_u32 m0, %18, 0xce20\n\t" "s_nop 0\n\t" "global_load_lds_dwordx4 v224, s[42:43]\n\t"
                     "s_add_u32 m0, %18, 0xd220\n\t" "s_nop 0\n\t" "global_load_lds_dwordx4 v225, s[42:43]\n\t" "s_add_u32 m0, %18, 0xd620\n\t" "s_nop 0\n\t" "global_load_lds_dwordx4 v226, s[42:43]\n\t"
                     "s_sub_u32 s42, 0x240000, %22\n\t" "s_add_u32 s42, s44, s42\n\t" "s_addc_u32 s43, s45, 0\n\t" "s_add_u32 m0, %18, 0x12300\n\t" "s_nop 0\n\t" "global_load_lds_dwordx4 v224, s[42:43]\n\t"
                     "s_add_u32 m0, %18, 0x12700\n\t" "s_nop 0\n\t" "global_load_lds_dwordx4 v225, s[42:43]\n\t" "s_add_u32 m0, %18, 0x12b00\n\t" "s_nop 0\n\t" "global_load_lds_dwordx4 v226, s[42:43]\n\t"
                     "s_sub_u32 s42, 0x258000, %22\n\t" "s_add_u32 s42, s44, s42\n\t" "s_addc_u32 s43, s45, 0\n\t" "s_add_u32 m0, %18, 0x12f20\n\t" "s_nop 0\n\t" "global_load_lds_dwordx4 v224, s[42:43]\n\t"
                     "s_add_u32 m0, %18, 0x13320\n\t" "s_nop 0\n\t" "global_load_lds_dwordx4 v225, s[42:43]\n\t" "s_add_u32 m0, %18, 0x13720\n\t" "s_nop 0\n\t" "global_load_lds_dwordx4 v226, s[42:43]\n\t"
                     "s_waitcnt vmcnt(12) lgkmcnt(0)\n\t" "s_barrier\n\t" "s_add_u32 s42, %22, 0x300000\n\t" "s_add_u32 s42, s44, s42\n\t" "s_addc_u32 s43, s45, 0\n\t" "s_add_u32 m0, %18, 0x18400\n\t" "s_nop 0\n\t"
                     "global_load_lds_dwordx4 v224, s[42:43]\n\t" "s_add_u32 m0, %18, 0x18800\n\t" "s_nop 0\n\t" "global_load_lds_dwordx4 v225, s[42:43]\n\t" "s_add_u32 m0, %18, 0x18c00\n\t" "s_nop 0\n\t"
                     "global_load_lds_dwordx4 v226, s[42:43]\n\t" "s_add_u32 s42, %22, 0x318000\n\t" "s_add_u32 s42, s44, s42\n\t" "s_addc_u32 s43, s45, 0\n\t" "s_add_u32 m0, %18, 0x19020\n\t" "s_nop 0\n\t"
                     "global_load_lds_dwordx4 v224, s[42:43]\n\t" "s_add_u32 m0, %18, 0x19420\n\t" "s_nop 0\n\t" "global_load_lds_dwordx4 v225, s[42:43]\n\t" "s_add_u32 m0, %18, 0x19820\n\t" "s_nop 0\n\t"
                     "global_load_lds_dwordx4 v226, s[42:43]\n\t" "s_sub_u32 s42, 0x3c0000, %22\n\t" "s_add_u32 s42, s44, s42\n\t" "s_addc_u32 s43, s45, 0\n\t" "s_add_u32 m0, %18, 0x1e500\n\t" "s_nop 0\n\t"
                     "global_load_lds_dwordx4 v224, s[42:43]\n\t" "s_add_u32 m0, %18, 0x1e900\n\t" "s_nop 0\n\t" "global_load_lds_dwordx4 v225, s[42:43]\n\t" "s_add_u32 m0, %18, 0x1ed00\n\t" "s_nop 0\n\t"
                     "global_load_lds_dwordx4 v226, s[42:43]\n\t" "s_sub_u32 s42, 0x3d8000, %22\n\t" "s_add_u32 s42, s44, s42\n\t" "s_addc_u32 s43, s45, 0\n\t" "s_add_u32 m0, %18, 0x1f120\n\t" "s_nop 0\n\t"
                     "global_load_lds_dwordx4 v224, s[42:43]\n\t" "s_add_u32 m0, %18, 0x1f520\n\t" "s_nop 0\n\t" "global_load_lds_dwordx4 v225, s[42:43]\n\t" "s_add_u32 m0, %18, 0x1f920\n\t" "s_nop 0\n\t"
                     "global_load_lds_dwordx4 v226, s[42:43]\n\t" "v_add_u32 v237, 0x0, v227\n\t" "v_add_u32 v238, 0x0, v228\n\t" "ds_read_b64_tr_b16 a[0:1], v237\n\t" "ds_read_b64_tr_b16 a[2:3], v237 offset:768\n\t"
                     "ds_read_b64_tr_b16 a[4:5], v237 offset:12416\n\t" "ds_read_b64_tr_b16 a[6:7], v237 offset:13184\n\t" "ds_read_b64_tr_b16 v[0:1], v237 offset:32\n\t" "ds_read_b64_tr_b16 v[2:3], v237 offset:800\n\t"
                     "ds_read_b64_tr_b16 v[4:5], v237 offset:12448\n\t" "ds_read_b64_tr_b16 v[6:7], v237 offset:13216\n\t" "ds_read_b64_tr_b16 a[8:9], v238 offset:64\n\t" "ds_read_b64_tr_b16 a[10:11], v238 offset:832\n\t"
                     "ds_read_b64_tr_b16 a[12:13], v238 offset:12480\n\t" "ds_read_b64_tr_b16 a[14:15], v238 offset:13248\n\t" "ds_read_b64_tr_b16 a[32:33], v237 offset:24832\n\t"
                     "ds_read_b64_tr_b16 a[34:35], v237 offset:25600\n\t" "ds_read_b64_tr_b16 a[36:37], v237 offset:37248\n\t" "ds_read_b64_tr_b16 a[38:39], v237 offset:38016\n\t"
                     "ds_read_b64_tr_b16 v[16:17], v237 offset:24864\n\t" "ds_read_b64_tr_b16 v[18:19], v237 offset:25632\n\t" "ds_read_b64_tr_b16 v[20:21], v237 offset:37280\n\t"
                     "ds_read_b64_tr_b16 v[22:23], v237 offset:38048\n\t" "ds_read_b64_tr_b16 a[40:41], v238 offset:24896\n\t" "ds_read_b64_tr_b16 a[42:43], v238 offset:25664\n\t"
                     "ds_read_b64_tr_b16 a[44:45], v238 offset:37312\n\t" "ds_read_b64_tr_b16 a[46:47], v238 offset:38080\n\t" "s_waitcnt vmcnt(12) lgkmcnt(0)\n\t" "s_barrier\n\t" "s_add_u32 s42, %22, 0x480000\n\t"
                     "s_add_u32 s42, s44, s42\n\t" "s_addc_u32 s43, s45, 0\n\t" "s_add_u32 m0, %18, 0x0\n\t" "s_nop 0\n\t" "global_load_lds_dwordx4 v224, s[42:43]\n\t" "s_add_u32 m0, %18, 0x400\n\t" "s_nop 0\n\t"
                     "global_load_lds_dwordx4 v225, s[42:43]\n\t" "s_add_u32 m0, %18, 0x800\n\t" "s_nop 0\n\t" "global_load_lds_dwordx4 v226, s[42:43]\n\t" "s_add_u32 s42, %22, 0x498000\n\t" "s_add_u32 s42, s44, s42\n\t"
                     "s_addc_u32 s43, s45, 0\n\t" "s_add_u32 m0, %18, 0xc20\n\t" "s_nop 0\n\t" "global_load_lds_dwordx4 v224, s[42:43]\n\t" "s_add_u32 m0, %18, 0x1020\n\t" "s_nop 0\n\t"
                     "global_load_lds_dwordx4 v225, s[42:43]\n\t" "s_add_u32 m0, %18, 0x1420\n\t" "s_nop 0\n\t" "global_load_lds_dwordx4 v226, s[42:43]\n\t" "s_sub_u32 s42, 0x540000, %22\n\t" "s_add_u32 s42, s44, s42\n\t"
                     "s_addc_u32 s43, s45, 0\n\t" "s_add_u32 m0, %18, 0x6100\n\t" "s_nop 0\n\t" "global_load_lds_dwordx4 v224, s[42:43]\n\t" "s_add_u32 m0, %18, 0x6500\n\t" "s_nop 0\n\t"
                     "global_load_lds_dwordx4 v225, s[42:43]\n\t" "s_add_u32 m0, %18, 0x6900\n\t" "s_nop 0\n\t" "global_load_lds_dwordx4 v226, s[42:43]\n\t" "s_sub_u32 s42, 0x558000, %22\n\t" "s_add_u32 s42, s44, s42\n\t"
                     "s_addc_u32 s43, s45, 0\n\t" "s_add_u32 m0, %18, 0x6d20\n\t" "s_nop 0\n\t" "global_load_lds_dwordx4 v224, s[42:43]\n\t" "s_add_u32 m0, %18, 0x7120\n\t" "s_nop 0\n\t"
                     "global_load_lds_dwordx4 v225, s[42:43]\n\t" "s_add_u32 m0, %18, 0x7520\n\t" "s_nop 0\n\t" "global_load_lds_dwordx4 v226, s[42:43]\n\t" "v_add_u32 v237, 0xc200, v227\n\t"
                     "v_add_u32 v238, 0xc200, v228\n\t" "ds_read_b64_tr_b16 a[16:17], v237\n\t" "ds_read_b64_tr_b16 a[18:19], v237 offset:768\n\t" "ds_read_b64_tr_b16 a[20:21], v237 offset:12416\n\t"
                     "ds_read_b64_tr_b16 a[22:23], v237 offset:13184\n\t" "ds_read_b64_tr_b16 v[8:9], v237 offset:32\n\t" "ds_read_b64_tr_b16 v[10:11], v237 offset:800\n\t"
                     "ds_read_b64_tr_b16 v[12:13], v237 offset:12448\n\t" "ds_read_b64_tr_b16 v[14:15], v237 offset:13216\n\t" "ds_read_b64_tr_b16 a[24:25], v238 offset:64\n\t"
                     "ds_read_b64_tr_b16 a[26:27], v238 offset:832\n\t" "ds_read_b64_tr_b16 a[28:29], v238 offset:12480\n\t" "ds_read_b64_tr_b16 a[30:31], v238 offset:13248\n\t"
                     "ds_read_b64_tr_b16 a[48:49], v237 offset:24832\n\t" "ds_read_b64_tr_b16 a[50:51], v237 offset:25600\n\t" "ds_read_b64_tr_b16 a[52:53], v237 offset:37248\n\t"
                     "ds_read_b64_tr_b16 a[54:55], v237 offset:38016\n\t" "ds_read_b64_tr_b16 v[24:25], v237 offset:24864\n\t" "ds_read_b64_tr_b16 v[26:27], v237 offset:25632\n\t"
                     "ds_read_b64_tr_b16 v[28:29], v237 offset:37280\n\t" "ds_read_b64_tr_b16 v[30:31], v237 offset:38048\n\t" "ds_read_b64_tr_b16 a[56:57], v238 offset:24896\n\t"
                     "ds_read_b64_tr_b16 a[58:59], v238 offset:25664\n\t" "ds_read_b64_tr_b16 a[60:61], v238 offset:37312\n\t" "ds_read_b64_tr_b16 a[62:63], v238 offset:38080\n\t" "s_waitcnt vmcnt(12) lgkmcnt(0)\n\t"
                     "s_barrier\n\t" "s_add_u32 s42, %22, 0x600000\n\t" "s_add_u32 s42, s44, s42\n\t" "s_addc_u32 s43, s45, 0\n\t" "s_add_u32 m0, %18, 0xc200\n\t" "s_nop 0\n\t" "global_load_lds_dwordx4 v224, s[42:43]\n\t"
                     "s_add_u32 m0, %18, 0xc600\n\t" "s_nop 0\n\t" "global_load_lds_dwordx4 v225, s[42:43]\n\t" "s_add_u32 m0, %18, 0xca00\n\t" "s_nop 0\n\t" "global_load_lds_dwordx4 v226, s[42:43]\n\t"
                     "s_add_u32 s42, %22, 0x618000\n\t" "s_add_u32 s42, s44, s42\n\t" "s_addc_u32 s43, s45, 0\n\t" "s_add_u32 m0, %18, 0xce20\n\t" "s_nop 0\n\t" "global_load_lds_dwordx4 v224, s[42:43]\n\t"
                     "s_add_u32 m0, %18, 0xd220\n\t" "s_nop 0\n\t" "global_load_lds_dwordx4 v225, s[42:43]\n\t" "s_add_u32 m0, %18, 0xd620\n\t" "s_nop 0\n\t" "global_load_lds_dwordx4 v226, s[42:43]\n\t"
                     "s_sub_u32 s42, 0x6c0000, %22\n\t" "s_add_u32 s42, s44, s42\n\t" "s_addc_u32 s43, s45, 0\n\t" "s_add_u32 m0, %18, 0x12300\n\t" "s_nop 0\n\t" "global_load_lds_dwordx4 v224, s[42:43]\n\t"
                     "s_add_u32 m0, %18, 0x12700\n\t" "s_nop 0\n\t" "global_load_lds_dwordx4 v225, s[42:43]\n\t" "s_add_u32 m0, %18, 0x12b00\n\t" "s_nop 0\n\t" "global_load_lds_dwordx4 v226, s[42:43]\n\t"
                     "s_sub_u32 s42, 0x6d8000, %22\n\t" "s_add_u32 s42, s44, s42\n\t" "s_addc_u32 s43, s45, 0\n\t" "s_add_u32 m0, %18, 0x12f20\n\t" "s_nop 0\n\t" "global_load_lds_dwordx4 v224, s[42:43]\n\t"
                     "s_add_u32 m0, %18, 0x13320\n\t" "s_nop 0\n\t" "global_load_lds_dwordx4 v225, s[42:43]\n\t" "s_add_u32 m0, %18, 0x13720\n\t" "s_nop 0\n\t" "global_load_lds_dwordx4 v226, s[42:43]\n\t"
                     "v_add_u32 v237, 0x18400, v227\n\t" "v_add_u32 v238, 0x18400, v228\n\t" "ds_read_b64_tr_b16 a[64:65], v237\n\t" "ds_read_b64_tr_b16 a[66:67], v237 offset:768\n\t"
                     "ds_read_b64_tr_b16 a[68:69], v237 offset:12416\n\t" "ds_read_b64_tr_b16 a[70:71], v237 offset:13184\n\t" "ds_read_b64_tr_b16 v[32:33], v237 offset:32\n\t"
                     "ds_read_b64_tr_b16 v[34:35], v237 offset:800\n\t" "ds_read_b64_tr_b16 v[36:37], v237 offset:12448\n\t" "ds_read_b64_tr_b16 v[38:39], v237 offset:13216\n\t"
                     "ds_read_b64_tr_b16 a[72:73], v238 offset:64\n\t" "ds_read_b64_tr_b16 a[74:75], v238 offset:832\n\t" "ds_read_b64_tr_b16 a[76:77], v238 offset:12480\n\t"
                     "ds_read_b64_tr_b16 a[78:79], v238 offset:13248\n\t" "ds_read_b64_tr_b16 a[80:81], v237 offset:24832\n\t" "ds_read_b64_tr_b16 a[82:83], v237 offset:25600\n\t"
                     "ds_read_b64_tr_b16 a[84:85], v237 offset:37248\n\t" "ds_read_b64_tr_b16 a[86:87], v237 offset:38016\n\t" "ds_read_b64_tr_b16 v[40:41], v237 offset:24864\n\t"
                     "ds_read_b64_tr_b16 v[42:43], v237 offset:25632\n\t" "ds_read_b64_tr_b16 v[44:45], v237 offset:37280\n\t" "ds_read_b64_tr_b16 v[46:47], v237 offset:38048\n\t"
                     "ds_read_b64_tr_b16 a[88:89], v238 offset:24896\n\t" "ds_read_b64_tr_b16 a[90:91], v238 offset:25664\n\t" "ds_read_b64_tr_b16 a[92:93], v238 offset:37312\n\t"
                     "ds_read_b64_tr_b16 a[94:95], v238 offset:38080\n\t" "s_waitcnt vmcnt(12) lgkmcnt(0)\n\t" "s_barrier\n\t" "s_add_u32 s42, s46, 0x0\n\t" "s_addc_u32 s43, s47, 0\n\t" "s_add_u32 m0, %20, 0x18400\n\t"
                     "s_nop 0\n\t" "global_load_lds_dwordx4 v232, s[42:43]\n\t" "s_add_u32 m0, %20, 0x18800\n\t" "s_nop 0\n\t" "global_load_lds_dwordx4 v233, s[42:43]\n\t" "s_add_u32 m0, %20, 0x18c00\n\t" "s_nop 0\n\t"
                     "global_load_lds_dwordx4 v234, s[42:43]\n\t" "s_add_u32 m0, %20, 0x19000\n\t" "s_nop 0\n\t" "global_load_lds_dwordx4 v235, s[42:43]\n\t" "s_add_u32 m0, %20, 0x19400\n\t" "s_nop 0\n\t"
                     "global_load_lds_dwordx4 v236, s[42:43]\n\t" "s_add_u32 s42, s46, 0x1400\n\t" "s_addc_u32 s43, s47, 0\n\t" "s_add_u32 m0, %20, 0x19800\n\t" "v_xor_b32 v224, 0x80, v232\n\t"
                     "global_load_lds_dwordx4 v224, s[42:43]\n\t" "s_add_u32 m0, %20, 0x19c00\n\t" "v_xor_b32 v224, 0x80, v233\n\t" "global_load_lds_dwordx4 v224, s[42:43]\n\t" "s_add_u32 m0, %20, 0x1a000\n\t"
                     "v_xor_b32 v224, 0x80, v234\n\t" "global_load_lds_dwordx4 v224, s[42:43]\n\t" "s_add_u32 m0, %20, 0x1a400\n\t" "v_xor_b32 v224, 0x80, v235\n\t" "global_load_lds_dwordx4 v224, s[42:43]\n\t"
                     "s_add_u32 m0, %20, 0x1a800\n\t" "v_xor_b32 v224, 0x80, v236\n\t" "global_load_lds_dwordx4 v224, s[42:43]\n\t" "v_add_u32 v237, 0x0, v227\n\t" "v_add_u32 v238, 0x0, v228\n\t"
                     "ds_read_b64_tr_b16 a[96:97], v237\n\t" "ds_read_b64_tr_b16 a[98:99], v237 offset:768\n\t" "ds_read_b64_tr_b16 a[100:101], v237 offset:12416\n\t" "ds_read_b64_tr_b16 a[102:103], v237 offset:13184\n\t"
                     "ds_read_b64_tr_b16 v[48:49], v237 offset:32\n\t" "ds_read_b64_tr_b16 v[50:51], v237 offset:800\n\t" "ds_read_b64_tr_b16 v[52:53], v237 offset:12448\n\t"
                     "ds_read_b64_tr_b16 v[54:55], v237 offset:13216\n\t" "ds_read_b64_tr_b16 a[104:105], v238 offset:64\n\t" "ds_read_b64_tr_b16 a[106:107], v238 offset:832\n\t"
                     "ds_read_b64_tr_b16 a[108:109], v238 offset:12480\n\t" "ds_read_b64_tr_b16 a[110:111], v238 offset:13248\n\t" "ds_read_b64_tr_b16 a[112:113], v237 offset:24832\n\t"
                     "ds_read_b64_tr_b16 a[114:115], v237 offset:25600\n\t" "ds_read_b64_tr_b16 a[116:117], v237 offset:37248\n\t" "ds_read_b64_tr_b16 a[118:119], v237 offset:38016\n\t"
                     "ds_read_b64_tr_b16 v[56:57], v237 offset:24864\n\t" "ds_read_b64_tr_b16 v[58:59], v237 offset:25632\n\t" "ds_read_b64_tr_b16 v[60:61], v237 offset:37280\n\t"
                     "ds_read_b64_tr_b16 v[62:63], v237 offset:38048\n\t" "ds_read_b64_tr_b16 a[120:121], v238 offset:24896\n\t" "ds_read_b64_tr_b16 a[122:123], v238 offset:25664\n\t"
                     "ds_read_b64_tr_b16 a[124:125], v238 offset:37312\n\t" "ds_read_b64_tr_b16 a[126:127], v238 offset:38080\n\t" "s_waitcnt vmcnt(10) lgkmcnt(0)\n\t" "s_barrier\n\t" "s_add_u32 s42, s46, 0xa000\n\t"
                     "s_addc_u32 s43, s47, 0\n\t" "s_add_u32 m0, %20, 0x0\n\t" "s_nop 0\n\t" "global_load_lds_dwordx4 v232, s[42:43]\n\t" "s_add_u32 m0, %20, 0x400\n\t" "s_nop 0\n\t"
                     "global_load_lds_dwordx4 v233, s[42:43]\n\t" "s_add_u32 m0, %20, 0x800\n\t" "s_nop 0\n\t" "global_load_lds_dwordx4 v234, s[42:43]\n\t" "s_add_u32 m0, %20, 0xc00\n\t" "s_nop 0\n\t"
                     "global_load_lds_dwordx4 v235, s[42:43]\n\t" "s_add_u32 m0, %20, 0x1000\n\t" "s_nop 0\n\t" "global_load_lds_dwordx4 v236, s[42:43]\n\t" "s_add_u32 s42, s46, 0xb400\n\t" "s_addc_u32 s43, s47, 0\n\t"
                     "s_add_u32 m0, %20, 0x1400\n\t" "v_xor_b32 v224, 0x80, v232\n\t" "global_load_lds_dwordx4 v224, s[42:43]\n\t" "s_add_u32 m0, %20, 0x1800\n\t" "v_xor_b32 v224, 0x80, v233\n\t"
                     "global_load_lds_dwordx4 v224, s[42:43]\n\t" "s_add_u32 m0, %20, 0x1c00\n\t" "v_xor_b32 v224, 0x80, v234\n\t" "global_load_lds_dwordx4 v224, s[42:43]\n\t" "s_add_u32 m0, %20, 0x2000\n\t"
                     "v_xor_b32 v224, 0x80, v235\n\t" "global_load_lds_dwordx4 v224, s[42:43]\n\t" "s_add_u32 m0, %20, 0x2400\n\t" "v_xor_b32 v224, 0x80, v236\n\t" "global_load_lds_dwordx4 v224, s[42:43]\n\t"
                     "v_add_u32 v237, 0xc200, v227\n\t" "v_add_u32 v238, 0xc200, v228\n\t" "ds_read_b64_tr_b16 a[128:129], v237\n\t" "ds_read_b64_tr_b16 a[130:131], v237 offset:768\n\t"
                     "ds_read_b64_tr_b16 a[132:133], v237 offset:12416\n\t" "ds_read_b64_tr_b16 a[134:135], v237 offset:13184\n\t" "ds_read_b64_tr_b16 a[136:137], v237 offset:32\n\t"
                     "ds_read_b64_tr_b16 a[138:139], v237 offset:800\n\t" "ds_read_b64_tr_b16 a[140:141], v237 offset:12448\n\t" "ds_read_b64_tr_b16 a[142:143], v237 offset:13216\n\t"
                     "ds_read_b64_tr_b16 a[144:145], v238 offset:64\n\t" "ds_read_b64_tr_b16 a[146:147], v238 offset:832\n\t" "ds_read_b64_tr_b16 a[148:149], v238 offset:12480\n\t"
                     "ds_read_b64_tr_b16 a[150:151], v238 offset:13248\n\t" "ds_read_b64_tr_b16 a[152:153], v237 offset:24832\n\t" "ds_read_b64_tr_b16 a[154:155], v237 offset:25600\n\t"
                     "ds_read_b64_tr_b16 a[156:157], v237 offset:37248\n\t" "ds_read_b64_tr_b16 a[158:159], v237 offset:38016\n\t" "ds_read_b64_tr_b16 a[160:161], v237 offset:24864\n\t"
                     "ds_read_b64_tr_b16 a[162:163], v237 offset:25632\n\t" "ds_read_b64_tr_b16 a[164:165], v237 offset:37280\n\t" "ds_read_b64_tr_b16 a[166:167], v237 offset:38048\n\t"
                     "ds_read_b64_tr_b16 a[168:169], v238 offset:24896\n\t" "ds_read_b64_tr_b16 a[170:171], v238 offset:25664\n\t" "ds_read_b64_tr_b16 a[172:173], v238 offset:37312\n\t"
                     "ds_read_b64_tr_b16 a[174:175], v238 offset:38080\n\t" "s_waitcnt vmcnt(10) lgkmcnt(0)\n\t" "s_barrier\n\t" "s_add_u32 s42, s46, 0x14000\n\t" "s_addc_u32 s43, s47, 0\n\t"
                     "s_add_u32 m0, %20, 0xc200\n\t" "s_nop 0\n\t" "global_load_lds_dwordx4 v232, s[42:43]\n\t" "s_add_u32 m0, %20, 0xc600\n\t" "s_nop 0\n\t" "global_load_lds_dwordx4 v233, s[42:43]\n\t"
                     "s_add_u32 m0, %20, 0xca00\n\t" "s_nop 0\n\t" "global_load_lds_dwordx4 v234, s[42:43]\n\t" "s_add_u32 m0, %20, 0xce00\n\t" "s_nop 0\n\t" "global_load_lds_dwordx4 v235, s[42:43]\n\t"
                     "s_add_u32 m0, %20, 0xd200\n\t" "s_nop 0\n\t" "global_load_lds_dwordx4 v236, s[42:43]\n\t" "s_add_u32 s42, s46, 0x15400\n\t" "s_addc_u32 s43, s47, 0\n\t" "s_add_u32 m0, %20, 0xd600\n\t"
                     "v_xor_b32 v224, 0x80, v232\n\t" "global_load_lds_dwordx4 v224, s[42:43]\n\t" "s_add_u32 m0, %20, 0xda00\n\t" "v_xor_b32 v224, 0x80, v233\n\t" "global_load_lds_dwordx4 v224, s[42:43]\n\t"
                     "s_add_u32 m0, %20, 0xde00\n\t" "v_xor_b32 v224, 0x80, v234\n\t" "global_load_lds_dwordx4 v224, s[42:43]\n\t" "s_add_u32 m0, %20, 0xe200\n\t" "v_xor_b32 v224, 0x80, v235\n\t"
                     "global_load_lds_dwordx4 v224, s[42:43]\n\t" "s_add_u32 m0, %20, 0xe600\n\t" "v_xor_b32 v224, 0x80, v236\n\t" "global_load_lds_dwordx4 v224, s[42:43]\n\t" "s_add_u32 s42, %21, 0x18400\n\t"
                     "v_xor_b32 v237, 0x0, v229\n\t" "v_add_u32 v237, s42, v237\n\t" "v_xor_b32 v238, 0x40, v229\n\t" "v_add_u32 v238, s42, v238\n\t" "v_xor_b32 v239, 0x80, v229\n\t" "v_add_u32 v239, s42, v239\n\t"
                     "v_xor_b32 v226, 0xc0, v229\n\t" "v_add_u32 v226, s42, v226\n\t" "ds_read_b64_tr_b16 a[176:177], v237\n\t" "ds_read_b64_tr_b16 a[178:179], v238 offset:2560\n\t"
                     "ds_read_b64_tr_b16 a[200:201], v239\n\t" "ds_read_b64_tr_b16 a[202:203], v226 offset:2560\n\t" "ds_read_b64_tr_b16 a[224:225], v237 offset:256\n\t"
                     "ds_read_b64_tr_b16 a[226:227], v238 offset:2816\n\t" "ds_read_b64_tr_b16 a[248:249], v239 offset:256\n\t" "ds_read_b64_tr_b16 a[250:251], v226 offset:2816\n\t"
                     "ds_read_b64_tr_b16 v[112:113], v237 offset:512\n\t" "ds_read_b64_tr_b16 v[114:115], v238 offset:3072\n\t" "ds_read_b64_tr_b16 v[136:137], v239 offset:512\n\t"
                     "ds_read_b64_tr_b16 v[138:139], v226 offset:3072\n\t" "ds_read_b64_tr_b16 v[160:161], v237 offset:768\n\t" "ds_read_b64_tr_b16 v[162:163], v238 offset:3328\n\t"
                     "ds_read_b64_tr_b16 v[184:185], v239 offset:768\n\t" "ds_read_b64_tr_b16 v[186:187], v226 offset:3328\n\t" "ds_read_b64_tr_b16 v[208:209], v237 offset:1024\n\t"
                     "ds_read_b64_tr_b16 v[210:211], v238 offset:3584\n\t" "ds_read_b64_tr_b16 v[72:73], v239 offset:1024\n\t" "ds_read_b64_tr_b16 v[74:75], v226 offset:3584\n\t" "s_waitcnt vmcnt(10) lgkmcnt(0)\n\t"
                     "s_barrier\n\t" "s_add_u32 s42, s46, 0x1e000\n\t" "s_addc_u32 s43, s47, 0\n\t" "s_add_u32 m0, %20, 0x18400\n\t" "s_nop 0\n\t" "global_load_lds_dwordx4 v232, s[42:43]\n\t"
                     "s_add_u32 m0, %20, 0x18800\n\t" "s_nop 0\n\t" "global_load_lds_dwordx4 v233, s[42:43]\n\t" "s_add_u32 m0, %20, 0x18c00\n\t" "s_nop 0\n\t" "global_load_lds_dwordx4 v234, s[42:43]\n\t"
                     "s_add_u32 m0, %20, 0x19000\n\t" "s_nop 0\n\t" "global_load_lds_dwordx4 v235, s[42:43]\n\t" "s_add_u32 m0, %20, 0x19400\n\t" "s_nop 0\n\t" "global_load_lds_dwordx4 v236, s[42:43]\n\t"
                     "s_add_u32 s42, s46, 0x1f400\n\t" "s_addc_u32 s43, s47, 0\n\t" "s_add_u32 m0, %20, 0x19800\n\t" "v_xor_b32 v224, 0x80, v232\n\t" "global_load_lds_dwordx4 v224, s[42:43]\n\t"
                     "s_add_u32 m0, %20, 0x19c00\n\t" "v_xor_b32 v224, 0x80, v233\n\t" "global_load_lds_dwordx4 v224, s[42:43]\n\t" "s_add_u32 m0, %20, 0x1a000\n\t" "v_xor_b32 v224, 0x80, v234\n\t"
                     "global_load_lds_dwordx4 v224, s[42:43]\n\t" "s_add_u32 m0, %20, 0x1a400\n\t" "v_xor_b32 v224, 0x80, v235\n\t" "global_load_lds_dwordx4 v224, s[42:43]\n\t" "s_add_u32 m0, %20, 0x1a800\n\t"
                     "v_xor_b32 v224, 0x80, v236\n\t" "global_load_lds_dwordx4 v224, s[42:43]\n\t" "s_add_u32 s42, %21, 0x0\n\t" "v_xor_b32 v237, 0x0, v229\n\t" "v_add_u32 v237, s42, v237\n\t"
                     "v_xor_b32 v238, 0x40, v229\n\t" "v_add_u32 v238, s42, v238\n\t" "v_xor_b32 v239, 0x80, v229\n\t" "v_add_u32 v239, s42, v239\n\t" "v_xor_b32 v226, 0xc0, v229\n\t" "v_add_u32 v226, s42, v226\n\t"
                     "ds_read_b64_tr_b16 a[180:181], v237\n\t" "ds_read_b64_tr_b16 a[182:183], v238 offset:2560\n\t" "ds_read_b64_tr_b16 a[204:205], v239\n\t" "ds_read_b64_tr_b16 a[206:207], v226 offset:2560\n\t"
                     "ds_read_b64_tr_b16 a[228:229], v237 offset:256\n\t" "ds_read_b64_tr_b16 a[230:231], v238 offset:2816\n\t" "ds_read_b64_tr_b16 a[252:253], v239 offset:256\n\t"
                     "ds_read_b64_tr_b16 a[254:255], v226 offset:2816\n\t" "ds_read_b64_tr_b16 v[116:117], v237 offset:512\n\t" "ds_read_b64_tr_b16 v[118:119], v238 offset:3072\n\t"
                     "ds_read_b64_tr_b16 v[140:141], v239 offset:512\n\t" "ds_read_b64_tr_b16 v[142:143], v226 offset:3072\n\t" "ds_read_b64_tr_b16 v[164:165], v237 offset:768\n\t"
                     "ds_read_b64_tr_b16 v[166:167], v238 offset:3328\n\t" "ds_read_b64_tr_b16 v[188:189], v239 offset:768\n\t" "ds_read_b64_tr_b16 v[190:191], v226 offset:3328\n\t"
                     "ds_read_b64_tr_b16 v[212:213], v237 offset:1024\n\t" "ds_read_b64_tr_b16 v[214:215], v238 offset:3584\n\t" "ds_read_b64_tr_b16 v[76:77], v239 offset:1024\n\t"
                     "ds_read_b64_tr_b16 v[78:79], v226 offset:3584\n\t" "s_waitcnt vmcnt(10) lgkmcnt(0)\n\t" "s_barrier\n\t" "v_mov_b32 v225, 0\n\t" "global_load_dwordx2 %12, v231, %19 sc1\n\t"
                     "global_load_dwordx2 %13, v225, %19 offset:512 sc1\n\t" "global_load_dwordx2 %14, v225, %19 offset:1024 sc1\n\t" "s_add_u32 s42, s46, 0x28000\n\t" "s_addc_u32 s43, s47, 0\n\t"
                     "s_add_u32 m0, %20, 0x0\n\t" "s_nop 0\n\t" "global_load_lds_dwordx4 v232, s[42:43]\n\t" "s_add_u32 m0, %20, 0x400\n\t" "s_nop 0\n\t" "global_load_lds_dwordx4 v233, s[42:43]\n\t"
                     "s_add_u32 m0, %20, 0x800\n\t" "s_nop 0\n\t" "global_load_lds_dwordx4 v234, s[42:43]\n\t" "s_add_u32 m0, %20, 0xc00\n\t" "s_nop 0\n\t" "global_load_lds_dwordx4 v235, s[42:43]\n\t"
                     "s_add_u32 m0, %20, 0x1000\n\t" "s_nop 0\n\t" "global_load_lds_dwordx4 v236, s[42:43]\n\t" "s_add_u32 s42, s46, 0x29400\n\t" "s_addc_u32 s43, s47, 0\n\t" "s_add_u32 m0, %20, 0x1400\n\t"
                     "v_xor_b32 v224, 0x80, v232\n\t" "global_load_lds_dwordx4 v224, s[42:43]\n\t" "s_add_u32 m0, %20, 0x1800\n\t" "v_xor_b32 v224, 0x80, v233\n\t" "global_load_lds_dwordx4 v224, s[42:43]\n\t"
                     "s_add_u32 m0, %20, 0x1c00\n\t" "v_xor_b32 v224, 0x80, v234\n\t" "global_load_lds_dwordx4 v224, s[42:43]\n\t" "s_add_u32 m0, %20, 0x2000\n\t" "v_xor_b32 v224, 0x80, v235\n\t"
                     "global_load_lds_dwordx4 v224, s[42:43]\n\t" "s_add_u32 m0, %20, 0x2400\n\t" "v_xor_b32 v224, 0x80, v236\n\t" "global_load_lds_dwordx4 v224, s[42:43]\n\t" "s_add_u32 s42, %21, 0xc200\n\t"
                     "v_xor_b32 v237, 0x0, v229\n\t" "v_add_u32 v237, s42, v237\n\t" "v_xor_b32 v238, 0x40, v229\n\t" "v_add_u32 v238, s42, v238\n\t" "v_xor_b32 v239, 0x80, v229\n\t" "v_add_u32 v239, s42, v239\n\t"
                     "v_xor_b32 v226, 0xc0, v229\n\t" "v_add_u32 v226, s42, v226\n\t" "ds_read_b64_tr_b16 a[184:185], v237\n\t" "ds_read_b64_tr_b16 a[186:187], v238 offset:2560\n\t"
                     "ds_read_b64_tr_b16 a[208:209], v239\n\t" "ds_read_b64_tr_b16 a[210:211], v226 offset:2560\n\t" "ds_read_b64_tr_b16 a[232:233], v237 offset:256\n\t"
                     "ds_read_b64_tr_b16 a[234:235], v238 offset:2816\n\t" "ds_read_b64_tr_b16 v[96:97], v239 offset:256\n\t" "ds_read_b64_tr_b16 v[98:99], v226 offset:2816\n\t"
                     "ds_read_b64_tr_b16 v[120:121], v237 offset:512\n\t" "ds_read_b64_tr_b16 v[122:123], v238 offset:3072\n\t" "ds_read_b64_tr_b16 v[144:145], v239 offset:512\n\t"
                     "ds_read_b64_tr_b16 v[146:147], v226 offset:3072\n\t" "ds_read_b64_tr_b16 v[168:169], v237 offset:768\n\t" "ds_read_b64_tr_b16 v[170:171], v238 offset:3328\n\t"
                     "ds_read_b64_tr_b16 v[192:193], v239 offset:768\n\t" "ds_read_b64_tr_b16 v[194:195], v226 offset:3328\n\t" "ds_read_b64_tr_b16 v[216:217], v237 offset:1024\n\t"
                     "ds_read_b64_tr_b16 v[218:219], v238 offset:3584\n\t" "ds_read_b64_tr_b16 v[80:81], v239 offset:1024\n\t" "ds_read_b64_tr_b16 v[82:83], v226 offset:3584\n\t" "s_waitcnt vmcnt(10) lgkmcnt(0)\n\t"
                     "s_barrier\n\t" "s_add_u32 s42, s46, 0x32000\n\t" "s_addc_u32 s43, s47, 0\n\t" "s_add_u32 m0, %20, 0xc200\n\t" "s_nop 0\n\t" "global_load_lds_dwordx4 v232, s[42:43]\n\t" "s_add_u32 m0, %20, 0xc600\n\t"
                     "s_nop 0\n\t" "global_load_lds_dwordx4 v233, s[42:43]\n\t" "s_add_u32 m0, %20, 0xca00\n\t" "s_nop 0\n\t" "global_load_lds_dwordx4 v234, s[42:43]\n\t" "s_add_u32 m0, %20, 0xce00\n\t" "s_nop 0\n\t"
                     "global_load_lds_dwordx4 v235, s[42:43]\n\t" "s_add_u32 m0, %20, 0xd200\n\t" "s_nop 0\n\t" "global_load_lds_dwordx4 v236, s[42:43]\n\t" "s_add_u32 s42, s46, 0x33400\n\t" "s_addc_u32 s43, s47, 0\n\t"
                     "s_add_u32 m0, %20, 0xd600\n\t" "v_xor_b32 v224, 0x80, v232\n\t" "global_load_lds_dwordx4 v224, s[42:43]\n\t" "s_add_u32 m0, %20, 0xda00\n\t" "v_xor_b32 v224, 0x80, v233\n\t"
                     "global_load_lds_dwordx4 v224, s[42:43]\n\t" "s_add_u32 m0, %20, 0xde00\n\t" "v_xor_b32 v224, 0x80, v234\n\t" "global_load_lds_dwordx4 v224, s[42:43]\n\t" "s_add_u32 m0, %20, 0xe200\n\t"
                     "v_xor_b32 v224, 0x80, v235\n\t" "global_load_lds_dwordx4 v224, s[42:43]\n\t" "s_add_u32 m0, %20, 0xe600\n\t" "v_xor_b32 v224, 0x80, v236\n\t" "global_load_lds_dwordx4 v224, s[42:43]\n\t"
                     "s_add_u32 s42, %21, 0x18400\n\t" "v_xor_b32 v237, 0x0, v229\n\t" "v_add_u32 v237, s42, v237\n\t" "v_xor_b32 v238, 0x40, v229\n\t" "v_add_u32 v238, s42, v238\n\t" "v_xor_b32 v239, 0x80, v229\n\t"
                     "v_add_u32 v239, s42, v239\n\t" "v_xor_b32 v226, 0xc0, v229\n\t" "v_add_u32 v226, s42, v226\n\t" "ds_read_b64_tr_b16 a[188:189], v237\n\t" "ds_read_b64_tr_b16 a[190:191], v238 offset:2560\n\t"
                     "ds_read_b64_tr_b16 a[212:213], v239\n\t" "ds_read_b64_tr_b16 a[214:215], v226 offset:2560\n\t" "ds_read_b64_tr_b16 a[236:237], v237 offset:256\n\t"
                     "ds_read_b64_tr_b16 a[238:239], v238 offset:2816\n\t" "ds_read_b64_tr_b16 v[100:101], v239 offset:256\n\t" "ds_read_b64_tr_b16 v[102:103], v226 offset:2816\n\t"
                     "ds_read_b64_tr_b16 v[124:125], v237 offset:512\n\t" "ds_read_b64_tr_b16 v[126:127], v238 offset:3072\n\t" "ds_read_b64_tr_b16 v[148:149], v239 offset:512\n\t"
                     "ds_read_b64_tr_b16 v[150:151], v226 offset:3072\n\t" "ds_read_b64_tr_b16 v[172:173], v237 offset:768\n\t" "ds_read_b64_tr_b16 v[174:175], v238 offset:3328\n\t"
                     "ds_read_b64_tr_b16 v[196:197], v239 offset:768\n\t" "ds_read_b64_tr_b16 v[198:199], v226 offset:3328\n\t" "ds_read_b64_tr_b16 v[220:221], v237 offset:1024\n\t"
                     "ds_read_b64_tr_b16 v[222:223], v238 offset:3584\n\t" "ds_read_b64_tr_b16 v[84:85], v239 offset:1024\n\t" "ds_read_b64_tr_b16 v[86:87], v226 offset:3584\n\t" "s_waitcnt vmcnt(10) lgkmcnt(0)\n\t"
                     "s_barrier\n\t" "s_add_u32 s42, %21, 0x0\n\t" "v_xor_b32 v237, 0x0, v229\n\t" "v_add_u32 v237, s42, v237\n\t" "v_xor_b32 v238, 0x40, v229\n\t" "v_add_u32 v238, s42, v238\n\t"
                     "v_xor_b32 v239, 0x80, v229\n\t" "v_add_u32 v239, s42, v239\n\t" "v_xor_b32 v226, 0xc0, v229\n\t" "v_add_u32 v226, s42, v226\n\t" "ds_read_b64_tr_b16 a[192:193], v237\n\t"
                     "ds_read_b64_tr_b16 a[194:195], v238 offset:2560\n\t" "ds_read_b64_tr_b16 a[216:217], v239\n\t" "ds_read_b64_tr_b16 a[218:219], v226 offset:2560\n\t"
                     "ds_read_b64_tr_b16 a[240:241], v237 offset:256\n\t" "ds_read_b64_tr_b16 a[242:243], v238 offset:2816\n\t" "ds_read_b64_tr_b16 v[104:105], v239 offset:256\n\t"
                     "ds_read_b64_tr_b16 v[106:107], v226 offset:2816\n\t" "ds_read_b64_tr_b16 v[128:129], v237 offset:512\n\t" "ds_read_b64_tr_b16 v[130:131], v238 offset:3072\n\t"
                     "ds_read_b64_tr_b16 v[152:153], v239 offset:512\n\t" "ds_read_b64_tr_b16 v[154:155], v226 offset:3072\n\t" "ds_read_b64_tr_b16 v[176:177], v237 offset:768\n\t"
                     "ds_read_b64_tr_b16 v[178:179], v238 offset:3328\n\t" "ds_read_b64_tr_b16 v[200:201], v239 offset:768\n\t" "ds_read_b64_tr_b16 v[202:203], v226 offset:3328\n\t"
                     "ds_read_b64_tr_b16 v[64:65], v237 offset:1024\n\t" "ds_read_b64_tr_b16 v[66:67], v238 offset:3584\n\t" "ds_read_b64_tr_b16 v[88:89], v239 offset:1024\n\t"
                     "ds_read_b64_tr_b16 v[90:91], v226 offset:3584\n\t" "s_waitcnt vmcnt(0) lgkmcnt(0)\n\t" "s_barrier\n\t" "s_add_u32 s42, %21, 0xc200\n\t" "v_xor_b32 v237, 0x0, v229\n\t" "v_add_u32 v237, s42, v237\n\t"
                     "v_xor_b32 v238, 0x40, v229\n\t" "v_add_u32 v238, s42, v238\n\t" "v_xor_b32 v239, 0x80, v229\n\t" "v_add_u32 v239, s42, v239\n\t" "v_xor_b32 v226, 0xc0, v229\n\t" "v_add_u32 v226, s42, v226\n\t"
                     "ds_read_b64_tr_b16 a[196:197], v237\n\t" "ds_read_b64_tr_b16 a[198:199], v238 offset:2560\n\t" "ds_read_b64_tr_b16 a[220:221], v239\n\t" "ds_read_b64_tr_b16 a[222:223], v226 offset:2560\n\t"
                     "ds_read_b64_tr_b16 a[244:245], v237 offset:256\n\t" "ds_read_b64_tr_b16 a[246:247], v238 offset:2816\n\t" "ds_read_b64_tr_b16 v[108:109], v239 offset:256\n\t"
                     "ds_read_b64_tr_b16 v[110:111], v226 offset:2816\n\t" "ds_read_b64_tr_b16 v[132:133], v237 offset:512\n\t" "ds_read_b64_tr_b16 v[134:135], v238 offset:3072\n\t"
                     "ds_read_b64_tr_b16 v[156:157], v239 offset:512\n\t" "ds_read_b64_tr_b16 v[158:159], v226 offset:3072\n\t" "ds_read_b64_tr_b16 v[180:181], v237 offset:768\n\t"
                     "ds_read_b64_tr_b16 v[182:183], v238 offset:3328\n\t" "ds_read_b64_tr_b16 v[204:205], v239 offset:768\n\t" "ds_read_b64_tr_b16 v[206:207], v226 offset:3328\n\t"
                     "ds_read_b64_tr_b16 v[68:69], v237 offset:1024\n\t" "ds_read_b64_tr_b16 v[70:71], v238 offset:3584\n\t" "ds_read_b64_tr_b16 v[92:93], v239 offset:1024\n\t"
                     "ds_read_b64_tr_b16 v[94:95], v226 offset:3584\n\t" "s_waitcnt lgkmcnt(0)\n\t" "ds_write_b128 v230, v[0:3]\n\t" "ds_write_b128 v230, v[4:7] offset:1024\n\t"
                     "ds_write_b128 v230, v[8:11] offset:2048\n\t" "ds_write_b128 v230, v[12:15] offset:3072\n\t" "ds_write_b128 v230, v[16:19] offset:4096\n\t" "ds_write_b128 v230, v[20:23] offset:5120\n\t"
                     "ds_write_b128 v230, v[24:27] offset:6144\n\t" "ds_write_b128 v230, v[28:31] offset:7168\n\t" "ds_write_b128 v230, v[32:35] offset:8192\n\t" "ds_write_b128 v230, v[36:39] offset:9216\n\t"
                     "ds_write_b128 v230, v[40:43] offset:10240\n\t" "ds_write_b128 v230, v[44:47] offset:11264\n\t" "ds_write_b128 v230, v[48:51] offset:12288\n\t" "ds_write_b128 v230, v[52:55] offset:13312\n\t"
                     "ds_write_b128 v230, v[56:59] offset:14336\n\t" "ds_write_b128 v230, v[60:63] offset:15360\n\t" "ds_write_b128 v230, v[64:67] offset:16384\n\t" "ds_write_b128 v230, v[68:71] offset:17408\n\t"
                     "ds_write_b128 v230, v[72:75] offset:18432\n\t" "ds_write_b128 v230, v[76:79] offset:19456\n\t" "ds_write_b128 v230, v[80:83] offset:20480\n\t" "ds_write_b128 v230, v[84:87] offset:21504\n\t"
                     "ds_write_b128 v230, v[88:91] offset:22528\n\t" "ds_write_b128 v230, v[92:95] offset:23552\n\t"
                     : "=&{a[0:31]}"(wa[0]), "=&{a[32:63]}"(wa[1]), "=&{a[64:95]}"(wa[2]), "=&{a[96:127]}"(wa[3]), "=&{a[128:159]}"(wa[4]), "=&{a[160:191]}"(wa[5]), "=&{a[192:223]}"(wa[6]), "=&{a[224:255]}"(wa[7]), "=&{v[96:127]}"(wv[0]), "=&{v[128:159]}"(wv[1]), "=&{v[160:191]}"(wv[2]), "=&{v[192:223]}"(wv[3]), "=&v"(y.r), "=&v"(y.g), "=&v"(y.e), "+{v[224:239]}"(pa)
                     : "{s[44:45]}"(p.wv + h * DH * 2 + (size_t)(16 * w) * (HD * 2)), "{s[46:47]}"(p.wo + (size_t)(h * DH + 8 * w) * 1280),
                       "s"(lds + 2 * w * GS), "s"(p.ws + OFF_LAT), "s"(lds + w * 10240), "s"(lds), "s"((h & 4) * 16 * HD * 2)
                     : "v0", "v1", "v2", "v3", "v4", "v5", "v6", "v7", "v8", "v9", "v10", "v11", "v12", "v13", "v14", "v15", "v16", "v17", "v18", "v19", "v20", "v21", "v22", "v23", "v24", "v25", "v26", "v27", "v28", "v29", "v30", "v31", "v32", "v33", "v34", "v35", "v36", "v37", "v38", "v39", "v40", "v41", "v42", "v43", "v44", "v45", "v46", "v47", "v48", "v49", "v50", "v51", "v52", "v53", "v54", "v55", "v56", "v57", "v58", "v59", "v60", "v61", "v62", "v63", "v64", "v65", "v66", "v67", "v68", "v69", "v70", "v71", "v72", "v73", "v74", "v75", "v76", "v77", "v78", "v79", "v80", "v81", "v82", "v83", "v84", "v85", "v86", "v87", "v88", "v89", "v90", "v91", "v92", "v93", "v94", "v95", "s42", "s43", "scc", "memory");
#pragma unroll
        for (int i = UPF; i < 8; ++i) {
            const int l = 64 * min(i * 4 + w, 29) + lanex();
            pfl(p.wq + h * 384, (u32)((l / 3) * 12288 + (l % 3) * 128), lds);
        }
        if (BPF) {
#pragma unroll
            for (int i = 0; i < 3; ++i) pfl(p.wqkvb, (u32)min(h * 768 + i * 256 + w * 64 + lanex(), 19199) * 128, lds);
            pfl(p.wob, (u32)min(h * 256 + w * 64 + lanex(), 6399) * 128, lds);
            if (h == 31) pfl(p.gw, (u32)min(lanex(), 9) * 128, lds);
        }
        sd = sync_end(y, p.ws, phi, w == 0);
        barrier();
    }
    const int lane = lanex(), tid = 64 * w + lane;
    const int n = lane & 15, g = lane >> 4, qq = (lane >> 2) & 3, pp = lane & 3;
    char* const fl = p.ws + OFF_ST;
    const u32 iw = lds + 65536 + ((lane * 8) ^ (lane & 32));
    const u32 qr0 = lds + 65536 + (g >> 1) * 512 + pp * 128 + 64 * (g & 1) + 8 * qq + 32 * (pp >> 1);
    const u32 uo = (u32)((4 * w + (n >> 3)) * 4096 + h * 128 + (n & 7) * 16);
    u32x16 ad = u32x16{(u32)lane * 16 + 1024 + (h & 4) * 256, (lane & 2) ? 0xEEEEu : 0x4444u, lds + (u32)((w & 1) * 24576 + (w >> 1) * 99328 + lane * 16), iw + ((3 * w + (w & 1)) >> 1) * 512,
                       iw + (1 + 3 * (w >> 1)) * 512 + 4 * (w & 1), qr0, qr0 ^ 32, uo + (g & 1) * 8192 + (1 + (g >> 1)) * 65536, uo + (g & 1) * 8192 + (g >> 1) * 8, uo, (u32)lane * 16 + 1024 - (h & 4) * 256, uo, uo, uo, uo, uo};
    asm volatile("" : "+{v[224:239]}"(ad));
    const char* memp = p.ws + mslot(sd, 0);
    int budget = 30000;
    Clock clk; clk.init();
    for (int s = 0; s < NSEG; ++s) {
        int tries = 0;
        u64 bad, tp8, en = ~0ull;
        while (true) {
            const u32 tgt = clk.sched(phi, 0, cfg.G, tries ? 2 : 0);
            char* up = p.ws + uslot(sd, s);
            asm volatile("ds_read_b128 v[64:67], v226\n\t" "ds_read_b128 v[68:71], v226 offset:1024\n\t" "ds_read_b128 v[72:75], v226 offset:2048\n\t" "ds_read_b128 v[76:79], v226 offset:3072\n\t"
                         "ds_read_b128 v[80:83], v226 offset:4096\n\t" "ds_read_b128 v[84:87], v226 offset:5120\n\t" "ds_read_b128 v[88:91], v226 offset:6144\n\t" "ds_read_b128 v[92:95], v226 offset:7168\n\t"
                         "v_mov_b64 v[40:41], 0\n\t" "v_mov_b64 v[42:43], 0\n\t" "v_mov_b64 v[44:45], 0\n\t" "v_mov_b64 v[46:47], 0\n\t" "v_mov_b64 v[48:49], 0\n\t" "v_mov_b64 v[50:51], 0\n\t" ".Lsp_u:\n\t" "s_memtime vcc\n\t"
                         "s_waitcnt lgkmcnt(0)\n\t" "s_sub_i32 s42, %3, vcc_lo\n\t" "s_cmp_gt_i32 s42, 80\n\t" "s_cbranch_scc1 .Lsp_u\n\t" "s_max_i32 s42, s42, 0\n\t" "s_sub_i32 s42, 92, s42\n\t" "s_and_b32 s42, s42, -4\n\t"
                         "s_getpc_b64 vcc\n\t" "s_add_u32 vcc_lo, vcc_lo, s42\n\t" "s_addc_u32 vcc_hi, vcc_hi, 0\n\t" "s_setpc_b64 vcc\n\t" "s_nop 0\n\t" "s_nop 0\n\t" "s_nop 0\n\t" "s_nop 0\n\t" "s_nop 0\n\t" "s_nop 0\n\t"
                         "s_nop 0\n\t" "s_nop 0\n\t" "s_nop 0\n\t" "s_nop 0\n\t" "s_nop 0\n\t" "s_nop 0\n\t" "s_nop 0\n\t" "s_nop 0\n\t" "s_nop 0\n\t" "s_nop 0\n\t" "s_nop 0\n\t" "s_nop 0\n\t" "s_nop 0\n\t" "s_nop 0\n\t"
                         "global_load_dwordx4 v[0:3], v224, %4 offset:-4096 sc1\n\t" "s_nop 10\n\t" "global_load_dwordx4 v[4:7], v224, %4 offset:-2048 sc1\n\t" "s_nop 15\n\t"
                         "global_load_dwordx4 v[8:11], v234, %4 offset:-3072 sc1\n\t" "s_nop 10\n\t" "global_load_dwordx4 v[12:15], v234, %4 offset:-1024 sc1\n\t" "s_nop 10\n\t" "global_load_dwordx4 v[16:19], v224, %4 sc1\n\t"
                         "s_nop 10\n\t" "global_load_dwordx4 v[20:23], v234, %4 offset:1024 sc1\n\t" "s_nop 10\n\t" "global_load_dwordx4 v[24:27], v224, %4 offset:2048 sc1\n\t" "s_nop 10\n\t"
                         "global_load_dwordx4 v[28:31], v234, %4 offset:3072 sc1\n\t" "s_nop 10\n\t" "global_load_dwordx4 v[32:35], v224, %5 offset:-4096 sc1\n\t" "s_nop 10\n\t"
                         "global_load_dwordx4 v[36:39], v234, %5 offset:-3072 sc1\n\t" "s_waitcnt vmcnt(9)\n\t" "v_smfmac_f32_16x16x64_bf16 v[40:43], v[0:3], a[0:7], v225\n\t"
                         "v_smfmac_f32_16x16x64_bf16 v[44:47], v[0:3], v[64:71], v225\n\t" "ds_read_b128 v[64:67], v226 offset:8192\n\t" "v_smfmac_f32_16x16x64_bf16 v[48:51], v[0:3], a[8:15], v225\n\t"
                         "ds_read_b128 v[68:71], v226 offset:9216\n\t" "v_cmp_eq_u32 vcc, -1, v0\n\t" "s_waitcnt vmcnt(8)\n\t" "v_smfmac_f32_16x16x64_bf16 v[40:43], v[4:7], a[16:23], v225\n\t" "v_mov_b64 v[0:1], 0\n\t"
                         "v_smfmac_f32_16x16x64_bf16 v[44:47], v[4:7], v[72:79], v225\n\t" "s_and_b64 vcc, vcc, %6\n\t" "s_cbranch_scc1 .Lsk_u\n\t" "ds_read_b128 v[72:75], v226 offset:10240\n\t" "v_mov_b64 v[2:3], 0\n\t"
                         "v_smfmac_f32_16x16x64_bf16 v[48:51], v[4:7], a[24:31], v225\n\t" "ds_read_b128 v[76:79], v226 offset:11264\n\t" "s_waitcnt vmcnt(7)\n\t"
                         "v_smfmac_f32_16x16x64_bf16 v[40:43], v[8:11], a[32:39], v225\n\t" "v_mov_b64 v[4:5], 0\n\t" "v_smfmac_f32_16x16x64_bf16 v[44:47], v[8:11], v[80:87], v225\n\t"
                         "ds_read_b128 v[80:83], v226 offset:12288\n\t" "v_mov_b64 v[6:7], 0\n\t" "v_smfmac_f32_16x16x64_bf16 v[48:51], v[8:11], a[40:47], v225\n\t" "ds_read_b128 v[84:87], v226 offset:13312\n\t"
                         "s_waitcnt vmcnt(6)\n\t" "v_smfmac_f32_16x16x64_bf16 v[40:43], v[12:15], a[48:55], v225\n\t" "v_mov_b64 v[8:9], 0\n\t" "v_smfmac_f32_16x16x64_bf16 v[44:47], v[12:15], v[88:95], v225\n\t"
                         "ds_read_b128 v[88:91], v226 offset:14336\n\t" "v_mov_b64 v[10:11], 0\n\t" "v_smfmac_f32_16x16x64_bf16 v[48:51], v[12:15], a[56:63], v225\n\t" "ds_read_b128 v[92:95], v226 offset:15360\n\t"
                         "s_waitcnt vmcnt(5)\n\t" "v_smfmac_f32_16x16x64_bf16 v[40:43], v[16:19], a[64:71], v225\n\t" "v_mov_b64 v[12:13], 0\n\t" "s_waitcnt lgkmcnt(6)\n\t"
                         "v_smfmac_f32_16x16x64_bf16 v[44:47], v[16:19], v[64:71], v225\n\t" "ds_read_b128 v[64:67], v226 offset:16384\n\t" "v_mov_b64 v[14:15], 0\n\t"
                         "v_smfmac_f32_16x16x64_bf16 v[48:51], v[16:19], a[72:79], v225\n\t" "ds_read_b128 v[68:71], v226 offset:17408\n\t" "s_waitcnt vmcnt(4)\n\t"
                         "v_smfmac_f32_16x16x64_bf16 v[40:43], v[20:23], a[80:87], v225\n\t" "v_mov_b64 v[16:17], 0\n\t" "s_waitcnt lgkmcnt(6)\n\t" "v_smfmac_f32_16x16x64_bf16 v[44:47], v[20:23], v[72:79], v225\n\t"
                         "ds_read_b128 v[72:75], v226 offset:18432\n\t" "v_mov_b64 v[18:19], 0\n\t" "v_smfmac_f32_16x16x64_bf16 v[48:51], v[20:23], a[88:95], v225\n\t" "ds_read_b128 v[76:79], v226 offset:19456\n\t"
                         "s_waitcnt vmcnt(3)\n\t" "v_smfmac_f32_16x16x64_bf16 v[40:43], v[24:27], a[96:103], v225\n\t" "v_mov_b64 v[20:21], 0\n\t" "s_waitcnt lgkmcnt(6)\n\t"
                         "v_smfmac_f32_16x16x64_bf16 v[44:47], v[24:27], v[80:87], v225\n\t" "ds_read_b128 v[80:83], v226 offset:20480\n\t" "v_mov_b64 v[22:23], 0\n\t"
                         "v_smfmac_f32_16x16x64_bf16 v[48:51], v[24:27], a[104:111], v225\n\t" "ds_read_b128 v[84:87], v226 offset:21504\n\t" "s_waitcnt vmcnt(2)\n\t"
                         "v_smfmac_f32_16x16x64_bf16 v[40:43], v[28:31], a[112:119], v225\n\t" "v_mov_b64 v[24:25], 0\n\t" "s_waitcnt lgkmcnt(6)\n\t" "v_smfmac_f32_16x16x64_bf16 v[44:47], v[28:31], v[88:95], v225\n\t"
                         "ds_read_b128 v[88:91], v226 offset:22528\n\t" "v_mov_b64 v[26:27], 0\n\t" "v_smfmac_f32_16x16x64_bf16 v[48:51], v[28:31], a[120:127], v225\n\t" "ds_read_b128 v[92:95], v226 offset:23552\n\t"
                         "s_waitcnt vmcnt(1)\n\t" "v_smfmac_f32_16x16x64_bf16 v[40:43], v[32:35], a[128:135], v225\n\t" "v_mov_b64 v[28:29], 0\n\t" "v_smfmac_f32_16x16x64_bf16 v[44:47], v[32:35], a[136:143], v225\n\t"
                         "v_mov_b64 v[30:31], 0\n\t" "v_smfmac_f32_16x16x64_bf16 v[48:51], v[32:35], a[144:151], v225\n\t" "s_waitcnt vmcnt(0)\n\t" "v_smfmac_f32_16x16x64_bf16 v[40:43], v[36:39], a[152:159], v225\n\t"
                         "v_mov_b64 v[32:33], 0\n\t" "v_smfmac_f32_16x16x64_bf16 v[44:47], v[36:39], a[160:167], v225\n\t" "v_mov_b64 v[34:35], 0\n\t" "v_smfmac_f32_16x16x64_bf16 v[48:51], v[36:39], a[168:175], v225\n\t"
                         "v_add_f32 v52, v40, v42\n\t" "v_add_f32 v53, v41, v43\n\t" "v_cmp_u_f32 vcc, v52, v53\n\t" "v_add_f32 v54, v44, v46\n\t" "v_add_f32 v55, v45, v47\n\t" "v_cvt_pk_bf16_f32 v58, v52, v53\n\t"
                         "v_pk_add_f32 v[56:57], v[48:49], v[50:51]\n\t" "v_cvt_pk_bf16_f32 v59, v54, v55\n\t" "v_cvt_pk_bf16_f32 v60, v56, v57\n\t" "ds_write_b64 v227, v[58:59]\n\t" "ds_write_b32 v228, v60\n\t"
                         "v_mov_b64 v[36:37], 0\n\t" "v_mov_b64 v[38:39], 0\n\t" "s_and_b64 vcc, vcc, %6\n\t" "v_mov_b32 v233, 0\n\t" "s_cbranch_scc1 .Lsk_u\n\t" "v_mov_b32 v233, 0\n\t" "v_mov_b32 v233, 0\n\t"
                         "v_mov_b32 v233, 0\n\t" "s_waitcnt lgkmcnt(0)\n\t" "s_barrier\n\t" "ds_read_b64_tr_b16 v[40:41], v229\n\t" "ds_read_b64_tr_b16 v[42:43], v230\n\t" "v_mov_b32 v233, 0\n\t"
                         "ds_read_b64_tr_b16 v[44:45], v229 offset:1024\n\t" "v_mov_b32 v233, 0\n\t" "ds_read_b64_tr_b16 v[46:47], v230 offset:1024\n\t" "v_mov_b32 v233, 0\n\t"
                         "ds_read_b64_tr_b16 v[48:49], v229 offset:2048\n\t" "v_mov_b32 v233, 0\n\t" "ds_read_b64_tr_b16 v[50:51], v230 offset:2048\n\t" "v_mov_b32 v233, 0\n\t" "s_waitcnt lgkmcnt(4)\n\t"
                         "v_smfmac_f32_16x16x64_bf16 v[0:3], v[40:43], a[176:183], v225\n\t" "v_smfmac_f32_16x16x64_bf16 v[4:7], v[40:43], a[200:207], v225\n\t" "s_waitcnt lgkmcnt(2)\n\t"
                         "v_smfmac_f32_16x16x64_bf16 v[0:3], v[44:47], a[184:191], v225\n\t" "v_smfmac_f32_16x16x64_bf16 v[4:7], v[44:47], a[208:215], v225\n\t" "s_waitcnt lgkmcnt(0)\n\t"
                         "v_smfmac_f32_16x16x64_bf16 v[0:3], v[48:51], a[192:199], v225\n\t" "v_smfmac_f32_16x16x64_bf16 v[4:7], v[48:51], a[216:223], v225\n\t"
                         "v_smfmac_f32_16x16x64_bf16 v[8:11], v[40:43], a[224:231], v225\n\t" "v_add_f32 v52, v0, v2\n\t" "v_add_f32 v53, v1, v3\n\t" "v_cvt_pk_bf16_f32 v54, v53, v52\n\t"
                         "v_smfmac_f32_16x16x64_bf16 v[8:11], v[44:47], a[232:239], v225\n\t" "v_add_f32 v52, v4, v6\n\t" "v_add_f32 v53, v5, v7\n\t" "v_cvt_pk_bf16_f32 v55, v53, v52\n\t" "s_nop 1\n\t"
                         "v_permlane16_swap_b32 v54, v55\n\t" "v_smfmac_f32_16x16x64_bf16 v[8:11], v[48:51], a[240:247], v225\n\t" "global_store_dwordx2 v232, v[54:55], %2 sc1\n\t" "s_add_u32 s40, s40, 0x10000\n\t"
                         "s_addc_u32 s41, s41, 0\n\t" "v_smfmac_f32_16x16x64_bf16 v[12:15], v[40:43], a[248:255], v225\n\t" "v_smfmac_f32_16x16x64_bf16 v[12:15], v[44:47], v[96:103], v225\n\t" "v_add_f32 v52, v8, v10\n\t"
                         "v_smfmac_f32_16x16x64_bf16 v[12:15], v[48:51], v[104:111], v225\n\t" "v_add_f32 v53, v9, v11\n\t" "v_smfmac_f32_16x16x64_bf16 v[16:19], v[40:43], v[112:119], v225\n\t"
                         "v_cvt_pk_bf16_f32 v56, v53, v52\n\t" "v_smfmac_f32_16x16x64_bf16 v[16:19], v[44:47], v[120:127], v225\n\t" "v_add_f32 v52, v12, v14\n\t" "v_add_f32 v53, v13, v15\n\t"
                         "v_cvt_pk_bf16_f32 v57, v53, v52\n\t" "s_nop 1\n\t" "v_permlane16_swap_b32 v56, v57\n\t" "v_smfmac_f32_16x16x64_bf16 v[16:19], v[48:51], v[128:135], v225\n\t"
                         "global_store_dwordx2 v232, v[56:57], %2 sc1\n\t" "s_add_u32 s40, s40, 0x10000\n\t" "s_addc_u32 s41, s41, 0\n\t" "v_smfmac_f32_16x16x64_bf16 v[20:23], v[40:43], v[136:143], v225\n\t"
                         "v_smfmac_f32_16x16x64_bf16 v[20:23], v[44:47], v[144:151], v225\n\t" "v_add_f32 v52, v16, v18\n\t" "v_smfmac_f32_16x16x64_bf16 v[20:23], v[48:51], v[152:159], v225\n\t" "v_add_f32 v53, v17, v19\n\t"
                         "v_smfmac_f32_16x16x64_bf16 v[24:27], v[40:43], v[160:167], v225\n\t" "v_cvt_pk_bf16_f32 v58, v53, v52\n\t" "v_smfmac_f32_16x16x64_bf16 v[24:27], v[44:47], v[168:175], v225\n\t"
                         "v_add_f32 v52, v20, v22\n\t" "v_add_f32 v53, v21, v23\n\t" "v_smfmac_f32_16x16x64_bf16 v[24:27], v[48:51], v[176:183], v225\n\t" "v_cvt_pk_bf16_f32 v59, v53, v52\n\t"
                         "v_smfmac_f32_16x16x64_bf16 v[28:31], v[40:43], v[184:191], v225\n\t" "s_nop 0\n\t" "v_permlane16_swap_b32 v58, v59\n\t" "v_smfmac_f32_16x16x64_bf16 v[28:31], v[44:47], v[192:199], v225\n\t"
                         "global_store_dwordx2 v232, v[58:59], %2 sc1\n\t" "v_add_f32 v52, v24, v26\n\t" "v_smfmac_f32_16x16x64_bf16 v[28:31], v[48:51], v[200:207], v225\n\t" "v_add_f32 v53, v25, v27\n\t"
                         "v_smfmac_f32_16x16x64_bf16 v[32:35], v[40:43], v[208:215], v225\n\t" "v_cvt_pk_bf16_f32 v60, v53, v52\n\t" "v_smfmac_f32_16x16x64_bf16 v[32:35], v[44:47], v[216:223], v225\n\t"
                         "v_add_f32 v52, v28, v30\n\t" "v_smfmac_f32_16x16x64_bf16 v[32:35], v[48:51], v[64:71], v225\n\t" "v_add_f32 v53, v29, v31\n\t" "v_smfmac_f32_16x16x64_bf16 v[36:39], v[40:43], v[72:79], v225\n\t"
                         "v_cvt_pk_bf16_f32 v61, v53, v52\n\t" "v_smfmac_f32_16x16x64_bf16 v[36:39], v[44:47], v[80:87], v225\n\t" "v_add_f32 v52, v32, v34\n\t"
                         "v_smfmac_f32_16x16x64_bf16 v[36:39], v[48:51], v[88:95], v225\n\t" "v_add_f32 v53, v33, v35\n\t" "v_cvt_pk_bf16_f32 v62, v53, v52\n\t" "v_mov_b32 v233, 0\n\t" "v_mov_b32 v233, 0\n\t"
                         "v_mov_b32 v233, 0\n\t" "v_mov_b32 v233, 0\n\t" "v_mov_b32 v233, 0\n\t" "v_pk_add_f32 v[52:53], v[36:37], v[38:39]\n\t" "v_cvt_pk_bf16_f32 v63, v53, v52\n\t" "v_permlane32_swap_b32 v60, v62\n\t"
                         "s_nop 0\n\t" "v_permlane32_swap_b32 v61, v63\n\t" "s_nop 1\n\t" "v_permlane16_swap_b32 v60, v61\n\t" "v_permlane16_swap_b32 v62, v63\n\t" "global_store_dwordx4 v231, v[60:63], %2 sc1\n\t"
                         ".Lsk_u:\n\t" "s_memrealtime %1\n\t" "s_mov_b64 %0, vcc\n\t" "s_waitcnt lgkmcnt(0)\n\t"
                         : "=&s"(bad), "=&s"(tp8), "+{s[40:41]}"(up)
                         : "s"(uni((int)tgt)), "s"(memp - 1024), "s"(memp + 15360), "s"(en), "{v[224:239]}"(ad),
                           "{a[0:31]}"(wa[0]), "{a[32:63]}"(wa[1]), "{a[64:95]}"(wa[2]), "{a[96:127]}"(wa[3]), "{a[128:159]}"(wa[4]), "{a[160:191]}"(wa[5]), "{a[192:223]}"(wa[6]), "{a[224:255]}"(wa[7]), "{v[96:127]}"(wv[0]), "{v[128:159]}"(wv[1]), "{v[160:191]}"(wv[2]), "{v[192:223]}"(wv[3])
                         : "v0", "v1", "v2", "v3", "v4", "v5", "v6", "v7", "v8", "v9", "v10", "v11", "v12", "v13", "v14", "v15", "v16", "v17", "v18", "v19", "v20", "v21", "v22", "v23", "v24", "v25", "v26", "v27", "v28", "v29", "v30", "v31", "v32", "v33", "v34", "v35", "v36", "v37", "v38", "v39", "v40", "v41", "v42", "v43", "v44", "v45", "v46", "v47", "v48", "v49", "v50", "v51", "v52", "v53", "v54", "v55", "v56", "v57", "v58", "v59", "v60", "v61", "v62", "v63", "v64", "v65", "v66", "v67", "v68", "v69", "v70", "v71", "v72", "v73", "v74", "v75", "v76", "v77", "v78", "v79", "v80", "v81", "v82", "v83", "v84", "v85", "v86", "v87", "v88", "v89", "v90", "v91", "v92", "v93", "v94", "v95", "vcc", "scc", "s42", "memory");
            if (__builtin_expect(bad == 0, 1)) break;
            waitvm<0>();
            if (--budget < 0 || ((++tries & 15) == 0 && !unwritten(memp, lanex() * 16))) en = 0;
            phi += (u32)cfg.G;
        }
        clk.calib();
        const u32 tt = sched_ld0(s);
        if (s >= NSEG - 2) waitvm<0>();
        barrier();
        if (s >= NSEG - 2 && w == 3) flag_add(fl, F_PROG + 8 * s + (h & 7), 1);
        if (s >= NSEG - 2 && w == 2) flag_add(fl, F_UD + 2 * (s & 1) + (h & 1), 1);
        follow(phi, (int)(tt >> 16) + 4 + cfg.T3 - VISS, (u32)tp8, cfg.G);
        phi += tt & 0xffffu;
        memp = p.ws + mslot(sd, s + 1);
    }
    asm volatile(".Lkm_lu:\n\ts_waitcnt vmcnt(0)" : : : "memory");
}

#define VALL "{v[0:3]}"(o), "{v[16:47]}"(rv[0]), "{v[48:79]}"(rv[1]), "{v[80:111]}"(rv[2]), "{v[112:143]}"(rv[3]), "{v[144:175]}"(rv[4]), "{v[176:207]}"(rv[5]), "{v[208:239]}"(rv[6]), "{a[0:31]}"(ra[0]), "{a[32:63]}"(ra[1]), "{a[64:95]}"(ra[2]), "{a[96:127]}"(ra[3])
DI void value_role(const Ptrs& p, const ChainCfg cfg, int vb, int sd, u32 phi, u32 lds, int w) {
    const int lane = lanex(), tid = 64 * w + lane, n = lane & 15, g = lane >> 4;
    const int v = vb;
    const u32 moff = lane * 16;
    char* const fl = p.ws + OFF_ST;
    char* const ub = p.ws + uchunk(v);
    const int xh = (vb >> 2) & 1;
    if (w == 3) {
        const u32x4 ff = u32x4{~0u, ~0u, ~0u, ~0u};
        if (SPL) barrier();
#pragma nounroll
        for (int s = 0; s < NSEG - 2; ++s) {
            dly(cfg.cR3 >> 8 & 255);
            char* const mk = ub + uslot(sd, s + 2);
#pragma unroll
            for (int i = 0; i < 8; ++i) { gst16(mk + i * 1024, moff, ff); gap<HQ>(-1); }
            if (vb < 32 && s) {
                gst16(p.ws + (size_t)(s + 2) * HO_STRIDE + vb * 2048, moff, ff);
                gst16(p.ws + (size_t)(s + 2) * HO_STRIDE + vb * 2048 + 1024, moff, ff);
            }
            waitvm<0>();
            if (SPL && s) barrier();
            barrier();
        }
        return;
    }
    if (w) {
        const u32 dof = (u32)((n >> 3) * 16384 + g * 128 + (n & 7) * 16);
        const char* dp = p.ws + OFF_D + (size_t)v * 32768 - 4096 + w * 8192;
        if (SPL) barrier();
        flag_wait(fl, F_VD + (vb & 3) + (v < 20 ? 0 : 4), 32);
#pragma nounroll
        for (int s = 0; s < NSEG - 1; ++s) {
            u32x4 df[16];
            u32 dfl;
            const int on = uni(1 - s);
            sfor<0, SPL>([&](auto I) { gld16c<I.v * 512 - 4096>(df[I.v], dp, dof); gap<HP0>(on); });
            if (SPL) asm volatile("s_cmp_ge_i32 %0, 0\n\ts_cbranch_scc1 .Lhb\n\ts_barrier\n.Lhb:" : : "s"(on) : "memory", "scc");
            dly(cfg.cR3 & 255);
            sfor<SPL, 16>([&](auto I) { gld16c<I.v * 512 - 4096>(df[I.v], dp, dof); gap<HP>(on); });
            dp += D_STRIDE;
            flag_ldm(dfl, fl, F_VD + pblk(s + 1) * 8 + (vb & RM), pblk(s + 1) != pblk(s));
            const u32 la = lds + moff + (s & 1) * 32768 + (w - 1) * 16384;
            sfor<0, 8>([&](auto I) {
                constexpr int i = I.v;
                waitv<15 - 2 * i>(df[2 * i]); waitv<15 - 2 * i>(df[2 * i + 1]);
                const u32x4 x = df[2 * i], y = df[2 * i + 1];
                const u32 lb = i & 2 ? la - xh * 4096 : la + xh * 4096;
                *LDSP(u32x4, lb + 2048 * i) = u32x4{x[0], y[0], x[2], y[2]};
                *LDSP(u32x4, lb + 2048 * i + 1024) = u32x4{x[1], y[1], x[3], y[3]};
                PIN();
            });
            waitvm<0>();
            asm volatile("" : "+v"(dfl));
            if (uni((int)dfl) < 31) {
                dly(FRD);
                flag_ld(dfl, fl, F_VD + pblk(s + 1) * 8 + (vb & RM));
                asm volatile("s_waitcnt vmcnt(0)" : "+v"(dfl) : : "memory");
            }
            barrier();
            if (uni((int)dfl) < 31) flag_wait(fl, F_VD + pblk(s + 1) * 8 + (vb & RM), 32);
        }
        return;
    }
    u32x32 rv[7], ra[4];
    u32x4 o;
    asm volatile("v_accvgpr_write_b32 a64, 0\n\t" "v_accvgpr_write_b32 a65, 0\n\t" "v_accvgpr_write_b32 a66, 0\n\t" "v_accvgpr_write_b32 a67, 0\n\t" "v_accvgpr_write_b32 a68, 0\n\t" "v_accvgpr_write_b32 a69, 0\n\t"
                 "v_accvgpr_write_b32 a70, 0\n\t" "v_accvgpr_write_b32 a71, 0\n\t" "v_accvgpr_write_b32 a72, 0\n\t" "v_accvgpr_write_b32 a73, 0\n\t" "v_accvgpr_write_b32 a74, 0\n\t" "v_accvgpr_write_b32 a75, 0\n\t"
                 "v_accvgpr_write_b32 a76, 0\n\t" "v_accvgpr_write_b32 a77, 0\n\t" "v_accvgpr_write_b32 a78, 0\n\t" "v_accvgpr_write_b32 a79, 0\n\t" "v_mov_b64 v[112:113], -1\n\t" "v_mov_b64 v[120:121], -1\n\t"
                 "v_mov_b64 v[128:129], -1\n\t" "v_mov_b64 v[136:137], -1\n\t" "v_mov_b64 v[144:145], -1\n\t" "v_mov_b64 v[152:153], -1\n\t" "v_mov_b64 v[160:161], -1\n\t" "v_mov_b64 v[168:169], -1\n\t"
                 "v_mov_b64 v[176:177], -1\n\t" "v_mov_b64 v[184:185], -1\n\t" "v_mov_b64 v[192:193], -1\n\t" "v_mov_b64 v[200:201], -1\n\t" "v_mov_b64 v[208:209], -1\n\t" "v_mov_b64 v[216:217], -1\n\t"
                 "v_mov_b64 v[224:225], -1\n\t" "v_mov_b64 v[232:233], -1\n\t"
                 : "={v[0:3]}"(o), "={v[16:47]}"(rv[0]), "={v[48:79]}"(rv[1]), "={v[80:111]}"(rv[2]), "={v[112:143]}"(rv[3]), "={v[144:175]}"(rv[4]), "={v[176:207]}"(rv[5]), "={v[208:239]}"(rv[6]), "={a[0:31]}"(ra[0]), "={a[32:63]}"(ra[1]), "={a[64:95]}"(ra[2]), "={a[96:127]}"(ra[3]));
    const u32 ho = (u32)(min(lane, 39) * 256 + (lane >> 5) * 8192);
    const u32 pof = (u32)((n & 4) * 128 + (n >> 3) * 256 + g * 64 + (n & 3) * 16 + 2048);
    const u32 uof = (u32)((n >> 3) * 4096 + g * 128 + (n & 7) * 16 + 512);
    u32x8 ad = u32x8{uof + xh * 512, pof + xh * 2048, (u32)(((g >> 1) ^ xh) * 256 + (n >> 2) * 64 + (n & 1) * 16), moff,
                     (lane & 2) ? 0xEEEEu : 0x4444u, (lane & 4) ? 0xEEEEu : 0x4444u, (u32)(v * 256 + (v >> 5) * 8192 + g * 64 + (n >> 3) * 32 + (n & 1) * 16 + (n & 6) * 2),
                     (lane & 1) ? 0x07060302u : 0x01000504u};
    u32 pob = pof - xh * 2048, uob = uof - xh * 512, pmo = moff ^ xh * 512;
    asm volatile("" : "+{v[4:11]}"(ad), "+v"(pob), "+v"(uob), "+v"(pmo));
    auto zero = [&]() {
        asm volatile("v_mov_b64 v[0:1], 0\n\t" "v_mov_b64 v[2:3], 0\n\t" "v_mov_b64 v[48:49], 0\n\t" "v_mov_b64 v[50:51], 0\n\t" "v_mov_b64 v[52:53], 0\n\t" "v_mov_b64 v[54:55], 0\n\t" "v_mov_b64 v[56:57], 0\n\t"
                     "v_mov_b64 v[58:59], 0\n\t" "v_mov_b64 v[60:61], 0\n\t" "v_mov_b64 v[62:63], 0\n\t" "v_mov_b64 v[64:65], 0\n\t" "v_mov_b64 v[66:67], 0\n\t" "v_mov_b64 v[68:69], 0\n\t" "v_mov_b64 v[70:71], 0\n\t"
                     "v_mov_b64 v[72:73], 0\n\t" "v_mov_b64 v[74:75], 0\n\t" "v_mov_b64 v[76:77], 0\n\t" "v_mov_b64 v[78:79], 0\n\t" "v_mov_b64 v[80:81], 0\n\t" "v_mov_b64 v[82:83], 0\n\t" "v_mov_b64 v[84:85], 0\n\t"
                     "v_mov_b64 v[86:87], 0\n\t" "v_mov_b64 v[88:89], 0\n\t" "v_mov_b64 v[90:91], 0\n\t" "v_mov_b64 v[92:93], 0\n\t" "v_mov_b64 v[94:95], 0\n\t" "v_mov_b64 v[96:97], 0\n\t" "v_mov_b64 v[98:99], 0\n\t"
                     "v_mov_b64 v[100:101], 0\n\t" "v_mov_b64 v[102:103], 0\n\t" "v_mov_b64 v[104:105], 0\n\t" "v_mov_b64 v[106:107], 0\n\t" "v_mov_b64 v[108:109], 0\n\t" "v_mov_b64 v[110:111], 0\n\t"
                     : : VALL : "memory");
    };
    const char* memp = p.ws + OFF_XF + 4096;
    char* up = ub + OFF_XF;
    int budget = 30000;
    Clock clk; clk.init();
    u32 tt = 0;
    for (int s = -1; s < NSEG - 1; ++s) {
        const char* const mnx = p.ws + mslot(sd, s + 1);
        bool all = false;
        int tries = 0;
        u64 tp8;
        while (true) {
            const u32 tgt = clk.sched(phi, (int)(tt >> 16), cfg.G, tries ? 2 : s > 0) - (s < 0 ? 1u << 20 : 0u);
            u64 ok;
            asm volatile("s_cmp_lg_u32 %11, 0\n\t" "s_cbranch_scc0 .Lsp_v0\n\t" ".Lsp_v3:\n\t" "s_memtime vcc\n\t" "s_waitcnt lgkmcnt(0)\n\t" "s_sub_i32 s42, %10, vcc_lo\n\t" "s_cmp_gt_i32 s42, 80\n\t"
                         "s_cbranch_scc1 .Lsp_v3\n\t" "s_max_i32 s42, s42, 0\n\t" "s_sub_i32 s42, 92, s42\n\t" "s_and_b32 s42, s42, -4\n\t" "s_getpc_b64 vcc\n\t" "s_add_u32 vcc_lo, vcc_lo, s42\n\t"
                         "s_addc_u32 vcc_hi, vcc_hi, 0\n\t" "s_setpc_b64 vcc\n\t" "s_nop 0\n\t" "s_nop 0\n\t" "s_nop 0\n\t" "s_nop 0\n\t" "s_nop 0\n\t" "s_nop 0\n\t" "s_nop 0\n\t" "s_nop 0\n\t" "s_nop 0\n\t" "s_nop 0\n\t"
                         "s_nop 0\n\t" "s_nop 0\n\t" "s_nop 0\n\t" "s_nop 0\n\t" "s_nop 0\n\t" "s_nop 0\n\t" "s_nop 0\n\t" "s_nop 0\n\t" "s_nop 0\n\t" "s_nop 0\n\t"
                         "global_load_dwordx4 a[0:3], v5, %7 offset:-4096 sc1\n\t" "global_load_dwordx4 a[4:7], v5, %7 offset:-3072 sc1\n\t" "global_load_dwordx4 a[16:19], v5, %7 sc1\n\t"
                         "global_load_dwordx4 a[20:23], v5, %7 offset:1024 sc1\n\t" "global_load_dwordx4 a[32:35], v5, %8 offset:-4096 sc1\n\t" "global_load_dwordx4 a[36:39], v5, %8 offset:-3072 sc1\n\t"
                         "global_load_dwordx4 a[48:51], v5, %8 sc1\n\t" "global_load_dwordx4 a[52:55], v5, %8 offset:1024 sc1\n\t" "s_nop 14\n\t" "global_load_dwordx4 a[8:11], %12, %7 offset:-2048 sc1\n\t"
                         "global_load_dwordx4 a[12:15], %12, %7 offset:-1024 sc1\n\t" "global_load_dwordx4 a[24:27], %12, %7 offset:2048 sc1\n\t" "global_load_dwordx4 a[28:31], %12, %7 offset:3072 sc1\n\t"
                         "global_load_dwordx4 a[40:43], %12, %8 offset:-2048 sc1\n\t" "global_load_dwordx4 a[44:47], %12, %8 offset:-1024 sc1\n\t" "global_load_dwordx4 a[56:59], %12, %8 offset:2048 sc1\n\t"
                         "global_load_dwordx4 a[60:63], %12, %8 offset:3072 sc1\n\t" "s_nop 15\n\t" "s_nop 15\n\t" "s_nop 14\n\t" "global_load_dwordx4 a[80:83], %14, %6 sc1\n\t"
                         "global_load_dwordx4 a[84:87], %14, %6 offset:1024 sc1\n\t" "global_load_dwordx4 a[88:91], %14, %6 offset:2048 sc1\n\t" "global_load_dwordx4 a[92:95], %14, %6 offset:3072 sc1\n\t"
                         "s_mov_b64 exec, %9\n\t" "global_load_dwordx4 a[64:67], v6, %6 offset:-2048 sc1\n\t" "global_load_dwordx4 a[68:71], v6, %6 offset:-1536 sc1\n\t"
                         "global_load_dwordx4 a[72:75], v6, %6 offset:-1024 sc1\n\t" "global_load_dwordx4 a[76:79], v6, %6 offset:-512 sc1\n\t" "s_mov_b64 exec, -1\n\t" "s_mov_b32 s42, %11\n\t" ".Lsp_v5:\n\t"
                         "s_sub_u32 s42, s42, 1\n\t" "s_cmp_gt_i32 s42, 0\n\t" "s_cbranch_scc1 .Lsp_v5\n\t" "global_load_dwordx4 a[96:99], v4, %3 offset:-512 sc1\n\t" "global_load_dwordx4 a[100:103], %13, %3 sc1\n\t"
                         "global_load_dwordx4 a[104:107], v4, %3 offset:512 sc1\n\t" "global_load_dwordx4 a[108:111], %13, %3 offset:1024 sc1\n\t" "global_load_dwordx4 a[112:115], v4, %3 offset:1536 sc1\n\t"
                         "global_load_dwordx4 a[116:119], %13, %3 offset:2048 sc1\n\t" "global_load_dwordx4 a[120:123], v4, %3 offset:2560 sc1\n\t" "global_load_dwordx4 a[124:127], %13, %3 offset:3072 sc1\n\t"
                         "s_waitcnt vmcnt(31)\n\t" "v_smfmac_f32_16x16x64_bf16 v[48:51], a[0:3], v[112:119], v9\n\t" "s_waitcnt vmcnt(30)\n\t" "v_smfmac_f32_16x16x64_bf16 v[52:55], a[4:7], v[120:127], v9\n\t"
                         "s_waitcnt vmcnt(29)\n\t" "v_smfmac_f32_16x16x64_bf16 v[64:67], a[16:19], v[144:151], v9\n\t" "s_waitcnt vmcnt(28)\n\t" "v_cvt_pk_bf16_f32 v16, v48, v49\n\t" "v_cvt_pk_bf16_f32 v17, v50, v51\n\t"
                         "v_smfmac_f32_16x16x64_bf16 v[68:71], a[20:23], v[152:159], v9\n\t" "s_waitcnt vmcnt(27)\n\t" "v_cvt_pk_bf16_f32 v18, v52, v53\n\t" "v_cvt_pk_bf16_f32 v19, v54, v55\n\t"
                         "v_smfmac_f32_16x16x64_bf16 v[80:83], a[32:35], v[176:183], v9\n\t" "s_waitcnt vmcnt(26)\n\t" "v_cvt_pk_bf16_f32 v24, v64, v65\n\t" "v_cvt_pk_bf16_f32 v25, v66, v67\n\t"
                         "v_smfmac_f32_16x16x64_bf16 v[84:87], a[36:39], v[184:191], v9\n\t" "s_waitcnt vmcnt(25)\n\t" "v_cvt_pk_bf16_f32 v26, v68, v69\n\t" "v_cvt_pk_bf16_f32 v27, v70, v71\n\t"
                         "v_smfmac_f32_16x16x64_bf16 v[96:99], a[48:51], v[208:215], v9\n\t" "s_waitcnt vmcnt(24)\n\t" "v_cvt_pk_bf16_f32 v32, v80, v81\n\t" "v_cvt_pk_bf16_f32 v33, v82, v83\n\t"
                         "v_smfmac_f32_16x16x64_bf16 v[100:103], a[52:55], v[216:223], v9\n\t" "s_waitcnt vmcnt(23)\n\t" "v_cvt_pk_bf16_f32 v34, v84, v85\n\t" "v_cvt_pk_bf16_f32 v35, v86, v87\n\t"
                         "v_smfmac_f32_16x16x64_bf16 v[56:59], a[8:11], v[128:135], v9\n\t" "s_waitcnt vmcnt(22)\n\t" "v_cvt_pk_bf16_f32 v40, v96, v97\n\t" "v_cvt_pk_bf16_f32 v41, v98, v99\n\t"
                         "v_smfmac_f32_16x16x64_bf16 v[60:63], a[12:15], v[136:143], v9\n\t" "s_waitcnt vmcnt(21)\n\t" "v_cvt_pk_bf16_f32 v42, v100, v101\n\t" "v_cvt_pk_bf16_f32 v43, v102, v103\n\t"
                         "v_smfmac_f32_16x16x64_bf16 v[72:75], a[24:27], v[160:167], v9\n\t" "s_waitcnt vmcnt(20)\n\t" "v_cvt_pk_bf16_f32 v20, v56, v57\n\t" "v_cvt_pk_bf16_f32 v21, v58, v59\n\t"
                         "v_smfmac_f32_16x16x64_bf16 v[76:79], a[28:31], v[168:175], v9\n\t" "s_waitcnt vmcnt(19)\n\t" "v_cvt_pk_bf16_f32 v22, v60, v61\n\t" "v_cvt_pk_bf16_f32 v23, v62, v63\n\t"
                         "v_smfmac_f32_16x16x64_bf16 v[88:91], a[40:43], v[192:199], v9\n\t" "s_waitcnt vmcnt(18)\n\t" "v_cvt_pk_bf16_f32 v28, v72, v73\n\t" "v_cvt_pk_bf16_f32 v29, v74, v75\n\t"
                         "v_smfmac_f32_16x16x64_bf16 v[92:95], a[44:47], v[200:207], v9\n\t" "s_waitcnt vmcnt(17)\n\t" "v_cvt_pk_bf16_f32 v30, v76, v77\n\t" "v_cvt_pk_bf16_f32 v31, v78, v79\n\t"
                         "v_smfmac_f32_16x16x64_bf16 v[104:107], a[56:59], v[224:231], v9\n\t" "s_waitcnt vmcnt(16)\n\t" "v_cvt_pk_bf16_f32 v36, v88, v89\n\t" "v_cvt_pk_bf16_f32 v37, v90, v91\n\t"
                         "v_smfmac_f32_16x16x64_bf16 v[108:111], a[60:63], v[232:239], v9\n\t" "s_waitcnt vmcnt(11)\n\t" "v_cvt_pk_bf16_f32 v38, v92, v93\n\t" "v_cvt_pk_bf16_f32 v39, v94, v95\n\t"
                         "v_smfmac_f32_16x16x64_bf16 v[0:3], a[64:67], v[16:23], v8\n\t" "s_waitcnt vmcnt(10)\n\t" "v_cvt_pk_bf16_f32 v44, v104, v105\n\t" "v_cvt_pk_bf16_f32 v45, v106, v107\n\t"
                         "v_smfmac_f32_16x16x64_bf16 v[0:3], a[68:71], v[24:31], v8\n\t" "s_waitcnt vmcnt(9)\n\t" "v_cvt_pk_bf16_f32 v46, v108, v109\n\t" "v_cvt_pk_bf16_f32 v47, v110, v111\n\t"
                         "v_smfmac_f32_16x16x64_bf16 v[0:3], a[72:75], v[32:39], v8\n\t" "s_waitcnt vmcnt(8)\n\t" "v_smfmac_f32_16x16x64_bf16 v[0:3], a[76:79], v[40:47], v8\n\t" "s_waitcnt vmcnt(6)\n\t"
                         "v_smfmac_f32_16x16x64_bf16 v[0:3], a[80:83], a[96:103], v8\n\t" "s_waitcnt vmcnt(4)\n\t" "v_smfmac_f32_16x16x64_bf16 v[0:3], a[84:87], a[104:111], v8\n\t" "s_waitcnt vmcnt(2)\n\t"
                         "v_smfmac_f32_16x16x64_bf16 v[0:3], a[88:91], a[112:119], v8\n\t" "s_waitcnt vmcnt(0)\n\t" "v_smfmac_f32_16x16x64_bf16 v[0:3], a[92:95], a[120:127], v8\n\t" "s_branch .Lv_end\n\t" ".Lsp_v0:\n\t"
                         "s_memtime vcc\n\t" "s_waitcnt lgkmcnt(0)\n\t" "s_sub_i32 s42, %10, vcc_lo\n\t" "s_cmp_gt_i32 s42, 80\n\t" "s_cbranch_scc1 .Lsp_v0\n\t" "s_max_i32 s42, s42, 0\n\t" "s_sub_i32 s42, 92, s42\n\t"
                         "s_and_b32 s42, s42, -4\n\t" "s_getpc_b64 vcc\n\t" "s_add_u32 vcc_lo, vcc_lo, s42\n\t" "s_addc_u32 vcc_hi, vcc_hi, 0\n\t" "s_setpc_b64 vcc\n\t" "s_nop 0\n\t" "s_nop 0\n\t" "s_nop 0\n\t" "s_nop 0\n\t"
                         "s_nop 0\n\t" "s_nop 0\n\t" "s_nop 0\n\t" "s_nop 0\n\t" "s_nop 0\n\t" "s_nop 0\n\t" "s_nop 0\n\t" "s_nop 0\n\t" "s_nop 0\n\t" "s_nop 0\n\t" "s_nop 0\n\t" "s_nop 0\n\t" "s_nop 0\n\t" "s_nop 0\n\t"
                         "s_nop 0\n\t" "s_nop 0\n\t" "global_load_dwordx4 a[96:99], v4, %3 offset:-512 sc1\n\t" "global_load_dwordx4 a[104:107], v4, %3 offset:512 sc1\n\t"
                         "global_load_dwordx4 a[112:115], v4, %3 offset:1536 sc1\n\t" "global_load_dwordx4 a[120:123], v4, %3 offset:2560 sc1\n\t" "global_load_dwordx4 a[100:103], %13, %3 sc1\n\t"
                         "global_load_dwordx4 a[108:111], %13, %3 offset:1024 sc1\n\t" "global_load_dwordx4 a[116:119], %13, %3 offset:2048 sc1\n\t" "global_load_dwordx4 a[124:127], %13, %3 offset:3072 sc1\n\t" "s_nop 9\n\t"
                         "global_load_dwordx4 a[0:3], v5, %7 offset:-4096 sc1\n\t" "global_load_dwordx4 a[4:7], v5, %7 offset:-3072 sc1\n\t" "global_load_dwordx4 a[16:19], v5, %7 sc1\n\t"
                         "global_load_dwordx4 a[20:23], v5, %7 offset:1024 sc1\n\t" "global_load_dwordx4 a[32:35], v5, %8 offset:-4096 sc1\n\t" "global_load_dwordx4 a[36:39], v5, %8 offset:-3072 sc1\n\t"
                         "global_load_dwordx4 a[48:51], v5, %8 sc1\n\t" "global_load_dwordx4 a[52:55], v5, %8 offset:1024 sc1\n\t" "s_nop 14\n\t" "global_load_dwordx4 a[8:11], %12, %7 offset:-2048 sc1\n\t"
                         "global_load_dwordx4 a[12:15], %12, %7 offset:-1024 sc1\n\t" "global_load_dwordx4 a[24:27], %12, %7 offset:2048 sc1\n\t" "global_load_dwordx4 a[28:31], %12, %7 offset:3072 sc1\n\t"
                         "global_load_dwordx4 a[40:43], %12, %8 offset:-2048 sc1\n\t" "global_load_dwordx4 a[44:47], %12, %8 offset:-1024 sc1\n\t" "global_load_dwordx4 a[56:59], %12, %8 offset:2048 sc1\n\t"
                         "global_load_dwordx4 a[60:63], %12, %8 offset:3072 sc1\n\t" "s_nop 15\n\t" "s_nop 15\n\t" "s_nop 14\n\t" "global_load_dwordx4 a[80:83], %14, %6 sc1\n\t"
                         "global_load_dwordx4 a[84:87], %14, %6 offset:1024 sc1\n\t" "global_load_dwordx4 a[88:91], %14, %6 offset:2048 sc1\n\t" "global_load_dwordx4 a[92:95], %14, %6 offset:3072 sc1\n\t"
                         "s_mov_b64 exec, %9\n\t" "global_load_dwordx4 a[64:67], v6, %6 offset:-2048 sc1\n\t" "global_load_dwordx4 a[68:71], v6, %6 offset:-1536 sc1\n\t"
                         "global_load_dwordx4 a[72:75], v6, %6 offset:-1024 sc1\n\t" "global_load_dwordx4 a[76:79], v6, %6 offset:-512 sc1\n\t" "s_mov_b64 exec, -1\n\t" "s_waitcnt vmcnt(23)\n\t"
                         "v_smfmac_f32_16x16x64_bf16 v[48:51], a[0:3], v[112:119], v9\n\t" "s_waitcnt vmcnt(22)\n\t" "v_smfmac_f32_16x16x64_bf16 v[52:55], a[4:7], v[120:127], v9\n\t" "s_waitcnt vmcnt(21)\n\t"
                         "v_smfmac_f32_16x16x64_bf16 v[64:67], a[16:19], v[144:151], v9\n\t" "s_waitcnt vmcnt(20)\n\t" "v_cvt_pk_bf16_f32 v16, v48, v49\n\t" "v_cvt_pk_bf16_f32 v17, v50, v51\n\t"
                         "v_smfmac_f32_16x16x64_bf16 v[68:71], a[20:23], v[152:159], v9\n\t" "s_waitcnt vmcnt(19)\n\t" "v_cvt_pk_bf16_f32 v18, v52, v53\n\t" "v_cvt_pk_bf16_f32 v19, v54, v55\n\t"
                         "v_smfmac_f32_16x16x64_bf16 v[80:83], a[32:35], v[176:183], v9\n\t" "s_waitcnt vmcnt(18)\n\t" "v_cvt_pk_bf16_f32 v24, v64, v65\n\t" "v_cvt_pk_bf16_f32 v25, v66, v67\n\t"
                         "v_smfmac_f32_16x16x64_bf16 v[84:87], a[36:39], v[184:191], v9\n\t" "s_waitcnt vmcnt(17)\n\t" "v_cvt_pk_bf16_f32 v26, v68, v69\n\t" "v_cvt_pk_bf16_f32 v27, v70, v71\n\t"
                         "v_smfmac_f32_16x16x64_bf16 v[96:99], a[48:51], v[208:215], v9\n\t" "s_waitcnt vmcnt(16)\n\t" "v_cvt_pk_bf16_f32 v32, v80, v81\n\t" "v_cvt_pk_bf16_f32 v33, v82, v83\n\t"
                         "v_smfmac_f32_16x16x64_bf16 v[100:103], a[52:55], v[216:223], v9\n\t" "s_waitcnt vmcnt(15)\n\t" "v_cvt_pk_bf16_f32 v34, v84, v85\n\t" "v_cvt_pk_bf16_f32 v35, v86, v87\n\t"
                         "v_smfmac_f32_16x16x64_bf16 v[56:59], a[8:11], v[128:135], v9\n\t" "s_waitcnt vmcnt(14)\n\t" "v_cvt_pk_bf16_f32 v40, v96, v97\n\t" "v_cvt_pk_bf16_f32 v41, v98, v99\n\t"
                         "v_smfmac_f32_16x16x64_bf16 v[60:63], a[12:15], v[136:143], v9\n\t" "s_waitcnt vmcnt(13)\n\t" "v_cvt_pk_bf16_f32 v42, v100, v101\n\t" "v_cvt_pk_bf16_f32 v43, v102, v103\n\t"
                         "v_smfmac_f32_16x16x64_bf16 v[72:75], a[24:27], v[160:167], v9\n\t" "s_waitcnt vmcnt(12)\n\t" "v_cvt_pk_bf16_f32 v20, v56, v57\n\t" "v_cvt_pk_bf16_f32 v21, v58, v59\n\t"
                         "v_smfmac_f32_16x16x64_bf16 v[76:79], a[28:31], v[168:175], v9\n\t" "s_waitcnt vmcnt(11)\n\t" "v_cvt_pk_bf16_f32 v22, v60, v61\n\t" "v_cvt_pk_bf16_f32 v23, v62, v63\n\t"
                         "v_smfmac_f32_16x16x64_bf16 v[88:91], a[40:43], v[192:199], v9\n\t" "s_waitcnt vmcnt(10)\n\t" "v_cvt_pk_bf16_f32 v28, v72, v73\n\t" "v_cvt_pk_bf16_f32 v29, v74, v75\n\t"
                         "v_smfmac_f32_16x16x64_bf16 v[92:95], a[44:47], v[200:207], v9\n\t" "s_waitcnt vmcnt(9)\n\t" "v_cvt_pk_bf16_f32 v30, v76, v77\n\t" "v_cvt_pk_bf16_f32 v31, v78, v79\n\t"
                         "v_smfmac_f32_16x16x64_bf16 v[104:107], a[56:59], v[224:231], v9\n\t" "s_waitcnt vmcnt(8)\n\t" "v_cvt_pk_bf16_f32 v36, v88, v89\n\t"
                         "v_smfmac_f32_16x16x64_bf16 v[108:111], a[60:63], v[232:239], v9\n\t" "s_waitcnt vmcnt(7)\n\t" "v_cvt_pk_bf16_f32 v37, v90, v91\n\t"
                         "v_smfmac_f32_16x16x64_bf16 v[0:3], a[80:83], a[96:103], v8\n\t" "s_waitcnt vmcnt(6)\n\t" "v_cvt_pk_bf16_f32 v38, v92, v93\n\t" "v_smfmac_f32_16x16x64_bf16 v[0:3], a[84:87], a[104:111], v8\n\t"
                         "s_waitcnt vmcnt(5)\n\t" "v_cvt_pk_bf16_f32 v39, v94, v95\n\t" "v_smfmac_f32_16x16x64_bf16 v[0:3], a[88:91], a[112:119], v8\n\t" "s_waitcnt vmcnt(4)\n\t" "v_cvt_pk_bf16_f32 v44, v104, v105\n\t"
                         "v_smfmac_f32_16x16x64_bf16 v[0:3], a[92:95], a[120:127], v8\n\t" "s_waitcnt vmcnt(3)\n\t" "v_cvt_pk_bf16_f32 v45, v106, v107\n\t" "v_smfmac_f32_16x16x64_bf16 v[0:3], a[64:67], v[16:23], v8\n\t"
                         "s_waitcnt vmcnt(2)\n\t" "v_cvt_pk_bf16_f32 v46, v108, v109\n\t" "v_smfmac_f32_16x16x64_bf16 v[0:3], a[68:71], v[24:31], v8\n\t" "s_waitcnt vmcnt(1)\n\t" "v_cvt_pk_bf16_f32 v47, v110, v111\n\t"
                         "v_smfmac_f32_16x16x64_bf16 v[0:3], a[72:75], v[32:39], v8\n\t" "s_waitcnt vmcnt(0)\n\t" "v_smfmac_f32_16x16x64_bf16 v[0:3], a[76:79], v[40:47], v8\n\t" ".Lv_end:\n\t" "s_nop 7\n\t"
                         "v_pk_add_f32 v[0:1], v[0:1], v[2:3]\n\t" "v_cvt_pk_bf16_f32 v2, v0, v1\n\t" "v_cmp_u_f32 vcc, v0, v1\n\t" "s_cmp_eq_u64 vcc, 0\n\t"
                         "v_mov_b32_dpp v3, v2 quad_perm:[1,0,3,2] row_mask:0xf bank_mask:0xf\n\t" "s_cselect_b64 %0, -1, 0\n\t" "v_perm_b32 v0, v2, v3, v11\n\t" "s_mov_b64 exec, %0\n\t"
                         "global_store_dword v10, v0, %5 offset:-4096 sc1\n\t" "global_store_dword v10, v0, %4 offset:-4096 sc1\n\t" "s_memrealtime %1\n\t" "s_mov_b64 exec, -1\n\t" "s_waitcnt lgkmcnt(0)\n\t"
                         : "=&s"(ok), "=&s"(tp8)
                         : "{v[4:11]}"(ad), "s"(up), "s"(mnx), "s"((u64)mnx ^ 8192), "s"(memp + 16384), "s"(memp + 32768 - 2048), "s"(memp + 49152 - 2048), "s"(0xc30000c3c30000c3ull),
                           "s"(uni((int)(tgt - (u32)(v < 16 ? cfg.dU - (v >> 3) * cfg.s2 : cfg.cR2)))), "s"(v < 16 ? 0 : (v < 24 ? 1 : cfg.T3)), "v"(pob), "v"(uob), "v"(pmo), "s"(tries), VALL : "memory", "vcc", "scc", "s42");
            if (__builtin_expect(ok != 0, 1) || s < 0) break;
            if (all || --budget < 0) {
                asm volatile("v_min_u32 v0, -2, v0\n\tglobal_store_dword v10, v0, %0 offset:-4096 sc1\n\tglobal_store_dword v10, v0, %1 offset:-4096 sc1"
                             : : "s"(mnx), "s"((u64)mnx ^ 8192), "{v[4:11]}"(ad), VALL : "memory");
                break;
            }
            if ((++tries & 15) == 0) {
                u32 m = 0;
#pragma nounroll
                for (int i = 0; i < 32; ++i) {
                    u32x4 t;
                    gld16c<0>(t, i < 24 ? memp + 12288 : up, i < 20 ? (4 + i + ((4 + i) & 24)) * 1024 + ad[3] : (i < 24 ? i * 512 - 8192 + ad[2] : (i & 7) * 512 - 512 + (i & 1 ? uob : ad[0]))); waitv<0>(t);
                    m = max3u(max3u(m, t[0], t[1]), t[2], t[3]);
                }
                all = __builtin_amdgcn_ballot_w64(m == 0xffffffffu) == 0;
            }
            phi += (u32)cfg.G;
            zero();
        }
        asm volatile("s_barrier\n\t" "ds_read_b128 v[112:115], %0\n\t" "ds_read_b128 v[116:119], %0 offset:1024\n\t" "ds_read_b128 v[120:123], %0 offset:2048\n\t" "ds_read_b128 v[124:127], %0 offset:3072\n\t"
                     "ds_read_b128 v[128:131], %0 offset:4096\n\t" "ds_read_b128 v[132:135], %0 offset:5120\n\t" "ds_read_b128 v[136:139], %0 offset:6144\n\t" "ds_read_b128 v[140:143], %0 offset:7168\n\t"
                     "ds_read_b128 v[144:147], %0 offset:8192\n\t" "ds_read_b128 v[148:151], %0 offset:9216\n\t" "ds_read_b128 v[152:155], %0 offset:10240\n\t" "ds_read_b128 v[156:159], %0 offset:11264\n\t"
                     "ds_read_b128 v[160:163], %0 offset:12288\n\t" "ds_read_b128 v[164:167], %0 offset:13312\n\t" "ds_read_b128 v[168:171], %0 offset:14336\n\t" "ds_read_b128 v[172:175], %0 offset:15360\n\t"
                     "ds_read_b128 v[176:179], %0 offset:16384\n\t" "ds_read_b128 v[180:183], %0 offset:17408\n\t" "ds_read_b128 v[184:187], %0 offset:18432\n\t" "ds_read_b128 v[188:191], %0 offset:19456\n\t"
                     "ds_read_b128 v[192:195], %0 offset:20480\n\t" "ds_read_b128 v[196:199], %0 offset:21504\n\t" "ds_read_b128 v[200:203], %0 offset:22528\n\t" "ds_read_b128 v[204:207], %0 offset:23552\n\t"
                     "ds_read_b128 v[208:211], %0 offset:24576\n\t" "ds_read_b128 v[212:215], %0 offset:25600\n\t" "ds_read_b128 v[216:219], %0 offset:26624\n\t" "ds_read_b128 v[220:223], %0 offset:27648\n\t"
                     "ds_read_b128 v[224:227], %0 offset:28672\n\t" "ds_read_b128 v[228:231], %0 offset:29696\n\t" "ds_read_b128 v[232:235], %0 offset:30720\n\t" "ds_read_b128 v[236:239], %0 offset:31744\n\t"
                     : : "v"(lds + ad[3] + ((s + 1) & 1) * 32768), VALL : "memory");
        if (s >= 0) {
            follow(phi, (int)(tt & 0xffffu) + 5 * (v >> 2) / 2 - (cfg.dV >> 16 & 255), (u32)tp8, cfg.G);
            if (vb == 0 && lane < 8) gst8(fl, (F_CUR + lane) * 128, u32x2{(u32)s - 1, phi + (tt & 0xffffu)});
        }
        phi += tt & 0xffffu;
        memp = mnx; up = ub + uslot(sd, s + 1);
        zero();
        tt = sched_ld0(s + 1);
        clk.calib();
        while (s >= 0) {
            u32 h0, h1;
            spin_to(clk.at(phi + (u32)(cfg.dV >> 8 & 255)));
            asm volatile("global_load_dword %0, %1, %2 offset:-4096 sc1" : "=&v"(h1) : "v"(ho & 0xfff), "s"(memp) : "memory");
            spin_to(clk.at(phi + (u32)(cfg.dV & 255)));
            asm volatile("global_load_dword %0, %1, %2 offset:-4096 sc1\n\ts_sleep 5" : "=&v"(h0) : "v"(ho), "s"(memp) : "memory");
            edge(clk.r1, clk.m1);
            asm volatile("s_waitcnt vmcnt(0)" : "+v"(h0), "+v"(h1) : : "memory");
            h0 = max(h0, h1);
            if (__builtin_amdgcn_ballot_w64(h0 == 0xffffffffu) == 0 || --budget < 0) break;
            phi += (u32)cfg.G;
        }
        asm volatile(".Lkm_lv:");
    }
}

DI void xcopy_task(const Ptrs& p, int j, int w) {
    const int tid = 64 * w + lanex();
    Sync y;
    sync_ld(y, p.ws);
    u32x4 xv[10];
    sfor<0, 10>([&](auto I) { gld16<I.v * 128>(xv[I.v], p.x + (size_t)j * 40960, (u32)((tid & 31) * 1280 + (tid >> 5) * 16)); });
    sfor<0, 10>([&](auto I) { waitv<9 - I.v>(xv[I.v]); });
    asm volatile("" : "+v"(y.r), "+v"(y.g), "+v"(y.e));
    sfor<0, 10>([&](auto I) { gst16(p.ws + OFF_XF + (size_t)j * 40960 + I.v * 4096, (u32)tid * 16, xv[I.v]); });
    waitvm<0>();
    barrier();
    const u64 t = sync_tag(y.e);
    if (tid == 0) gst8(p.ws + OFF_LAT, 1536 + (u32)j * 8, u32x2{(u32)t, (u32)(t >> 32)});
}
DI void xcopy_wait(const char* ws, u64 t, int lo = 0, int msk = 63) {
#pragma nounroll
    for (int it = 0; it < (1 << 16); ++it) {
        u32x2 r;
        asm volatile("s_nop 4\n\tglobal_load_dwordx2 %0, %1, %2 offset:1536 sc1\n\ts_waitcnt vmcnt(0)" : "=&v"(r) : "v"((lo + (lanex() & msk)) * 8), "s"(ws + OFF_LAT) : "memory");
        if (__builtin_amdgcn_ballot_w64(r[0] != (u32)t || r[1] != (u32)(t >> 32)) == 0) break;
        __builtin_amdgcn_s_sleep(XSL);
    }
}
DI void init_task(const Ptrs& p, int j, u32 lds, int w) {
    const int lane = lanex(), tid = 64 * w + lane;
    Sync y;
    sync_ld(y, p.ws);
    const u32x4 ff = u32x4{~0u, ~0u, ~0u, ~0u};
    const int i = j * 256 + tid, r = j < 8 ? j & 1 : min(j / 2 - 2, NREG - 1);
    char* const vb = p.ws + (r < 2 ? 0 : OFF_U - (4u << 20)) + ((size_t)r << 21) + 1984 * 1024 + (j < 8 ? j & 6 : (j & 1) * 2 + (j >> 1 == 15) * 4) * 8192;
    const u32 vo = (u32)((w >> 1) * 8192 + (w & 1) * 1024);
    gst16(vb, vo + lane * 16, ff);
    if (i < F_N * 8) gst16(p.ws + OFF_ST, (u32)i * 16, u32x4{i == F_ONE * 8 ? 1000u : ~0u, ~0u, ~0u, ~0u});
    if (i < 512) gst16(p.ws + OFF_GATE, (u32)i * 16, ff);
    gst16(p.ws + 65536, (u32)i * 16, ff);
    if (i < 4096) {
        const int k = (i & 511) + ((i >> 1) & 0x600);
        u32x4 m;
        gld16<0>(m, p.mem0, (u32)((2 * ((k >> 2) & 3) + (k & 1)) * 1280 + min(2 * (k >> 4) + ((k >> 1) & 1), 79) * 16));
        waitv<0>(m);
        gst16(p.ws, (u32)i * 16, k < 640 ? u32x4{min(m[0], 0xfffffffeu), min(m[1], 0xfffffffeu), min(m[2], 0xfffffffeu), min(m[3], 0xfffffffeu)} : ff);
    }
#pragma unroll
    for (int c = 0; c < 6; ++c) gst16(p.ws + OFF_U + c * 131072 - (c == 5) * 122880, (u32)i * 16, ff);
    waitvm<0>();
    asm volatile("" : "+v"(y.r), "+v"(y.g), "+v"(y.e));
    barrier();
    if (w) return;
    u32 dt[2] = {0xffffffu, 0xffffffu};
#pragma unroll
    for (int c = 0; c < 2 * NSAMP; ++c) {
        u32 v;
        const u32 t0 = mt();
        asm volatile("global_load_dword %0, %1, %2 sc1\n\ts_waitcnt vmcnt(0)" : "=&v"(v) : "v"((c & 1) * 8192 + (c >> 1) * 256), "s"(vb) : "memory");
        dt[c & 1] = min(dt[c & 1], mt() - t0);
    }
    const u64 t = sync_tag(y.e);
    const u32 vote = (u32)((dt[0] > dt[1]) ^ xhalf() ^ (int)((vb - p.ws) >> 21)) & 1u;
    if (tid == 0) {
        if (j == 0) gst8(p.ws + OFF_LAT, 512, u32x2{(u32)t, rt()});
        gst8(p.ws + OFF_LAT, (u32)j * 8, u32x2{(u32)t | vote, (u32)(t >> 32)});
    }
}

DI float xmax(float v) {
    const auto a = __builtin_amdgcn_permlane32_swap(__float_as_uint(v), __float_as_uint(v), false, false);
    v = fmaxf(__uint_as_float(a[0]), __uint_as_float(a[1]));
    const auto b = __builtin_amdgcn_permlane16_swap(__float_as_uint(v), __float_as_uint(v), false, false);
    return fmaxf(__uint_as_float(b[0]), __uint_as_float(b[1]));
}
DI float xsum(float v) {
    const auto a = __builtin_amdgcn_permlane32_swap(__float_as_uint(v), __float_as_uint(v), false, false);
    v = __uint_as_float(a[0]) + __uint_as_float(a[1]);
    const auto b = __builtin_amdgcn_permlane16_swap(__float_as_uint(v), __float_as_uint(v), false, false);
    return __uint_as_float(b[0]) + __uint_as_float(b[1]);
}
DI void gst16c(char* sbase, u32 voff, u32x4 v) {
    asm volatile("global_store_dwordx4 %0, %1, %2 sc1\n\ts_nop 1" : : "v"(voff), "v"(v), "s"(sbase) : "memory");
}
#define WAIT6(N, r, o) asm volatile("s_waitcnt vmcnt(" #N ")" : "+v"(r[o]), "+v"(r[o + 1]), "+v"(r[o + 2]), "+v"(r[o + 3]), "+v"(r[o + 4]), "+v"(r[o + 5]) : : "memory")
#define WSLEEP 90
DI void wg_wait(const char* fl, int w, int i0, int t0, int i1, int t1, int i2, int t2, int i3, int t3, int dt, bool acq) {
    if (w == 0) {
        const int lane = lanex(), tg = lane == 1 ? t1 : (lane == 2 ? t2 : (lane == 3 ? t3 : t0));
        const int ix = tg <= 0 ? i0 : (lane == 1 ? i1 : (lane == 2 ? i2 : (lane == 3 ? i3 : i0)));
#pragma nounroll
        for (int it = 0; it < (1 << 14); ++it) {
            u32 v;
            flag_ld(v, fl, ix);
            asm volatile("s_waitcnt vmcnt(0)" : "+v"(v) : : "memory");
            if (__builtin_amdgcn_ballot_w64((int)(v + 1u) < tg) == 0) break;
            const u32 wake = rt() + (u32)(dt + 180 * min(max(t0 - 2 - uni((int)v), 0), 64));
            while ((int)(rt() - wake) < 0) __builtin_amdgcn_s_sleep(4);
        }
        if (acq) acquire();
    }
    barrier();
}
#define TEST 172
DI void until(u32 t) { while ((int)(rt() - t) < 0) __builtin_amdgcn_s_sleep(1); }
DI u32 step_wait(const char* fl, int w, int gr, int t, u32 lds) {
    if (w == 0) {
        u32x2 v;
#pragma nounroll
        for (int it = 0; it < (1 << 14); ++it) {
            asm volatile("s_nop 4\n\tglobal_load_dwordx2 %0, %1, %2 sc1\n\ts_waitcnt vmcnt(0)" : "=&v"(v) : "v"((F_CUR + gr) * 128), "s"(fl) : "memory");
            const int d = t - 1 - uni((int)v[0]);
            if (d <= 0) { v[1] += (u32)(d * TEST); break; }
            const u32 now = rt();
            nap(min(max((int)((u32)uni((int)v[1]) - now) + d * TEST + 10, 40), 2000));
        }
        *LDSP(u32, lds + GRING * GBUF + 4) = v[1];
    }
    barrier();
    return (u32)uni((int)*LDSP(u32, lds + GRING * GBUF + 4));
}
#define PDL 135
#define ODL 280
#define TDT 35
template <bool LATE>
DI void px_wave(const Ptrs& p, const ChainCfg& cfg, int s, int h, int slow, int w, u32 lds, bool keep) {
    const int lane = lanex(), n = lane & 15, g = lane >> 4;
    const char* qb = p.ws + OFF_QX + (size_t)(s * NH + h) * 12288;
    const char* kb = p.ws + OFF_KX + (size_t)(s * NH + h) * 12288;
    const char* mb = p.ws + OFF_KM + (size_t)(s * NH + h) * 3072;
    const u32 ko = (u32)((n >> 3) * 6144 + (8 * ((n >> 2) & 1) + (n & 3) + 16 * g) * 16);
    const u32 mo = (u32)(g * 128 + (n & 7) * 16);
    u32x4 qf[12], kf[12], mf[6];
    sfor<0, 12>([&](auto I) { gld16c<(I.v % 3) * 1024>(qf[I.v], qb + (I.v / 3) * 3072, lane * 16); gap<SMP>(slow); });
    if (!SMP) pace(slow);
    sfor<0, 12>([&](auto I) { gld16c<(I.v % 3) * 1024 + (I.v / 6) * 64>(kf[I.v], kb + ((I.v / 3) & 1) * 3072, ko); gap<SMP>(slow); });
    if (!SMP) pace(slow);
    if constexpr (LATE) { WAIT6(0, qf, 0); WAIT6(0, qf, 6); WAIT6(0, kf, 0); }
    else {
        sfor<0, 6>([&](auto I) { gld16c<I.v * 512>(mf[I.v], mb, mo); });
        WAIT6(24, qf, 0); WAIT6(18, qf, 6); WAIT6(12, kf, 0);
    }
    f32x4 sc[2][3];
#pragma unroll
    for (int tt = 0; tt < 2; ++tt)
#pragma unroll
        for (int T = 0; T < 3; ++T) sc[tt][T] = f32x4{0.f, 0.f, 0.f, 0.f};
#pragma unroll
    for (int ks = 0; ks < 6; ++ks) { sc[0][0] = mfma16(kf[ks], qf[ks], sc[0][0]); sc[1][0] = mfma16(kf[ks], qf[6 + ks], sc[1][0]); }
    WAIT6(6, kf, 6);
#pragma unroll
    for (int ks = 0; ks < 6; ++ks) { sc[0][1] = mfma16(kf[6 + ks], qf[ks], sc[0][1]); sc[1][1] = mfma16(kf[6 + ks], qf[6 + ks], sc[1][1]); }
    if constexpr (LATE) {
        until(step_wait(p.ws + OFF_ST, w, h & 7, s - 1, lds) + (u32)(s == NSEG - 1 ? cfg.T : cfg.T + PDX));
        bool ok = false;
#pragma nounroll
        for (int it = 0; it < (1 << 14); ++it) {
            u32 v;
            sfor<0, 6>([&](auto I) { gld16c<I.v * 512>(mf[I.v], mb, mo); });
            flag_ldm(v, p.ws + OFF_ST, (s < NSEG - 2 ? F_CUR : F_PROG + 8 * s) + (h & 7), it > 3);
            WAIT6(0, mf, 0);
            asm volatile("" : "+v"(v));
            u32 m = 0;
#pragma unroll
            for (int i = 0; i < 24; ++i) m = max(m, mf[i >> 2][i & 3]);
            if (ok || __builtin_amdgcn_ballot_w64(m == ~0u) == 0) break;
            ok = it > 3 && uni((int)v) >= (s < NSEG - 2 ? s : 7);
            if (s < NSEG - 1) nap(60);
        }
    }
    WAIT6(0, mf, 0);
#pragma unroll
    for (int ks = 0; ks < 6; ++ks) { sc[0][2] = mfma16(mf[ks], qf[ks], sc[0][2]); sc[1][2] = mfma16(mf[ks], qf[6 + ks], sc[1][2]); }
    const float kscale = 0.07216878364870322f;
    char* pb = p.ws + OFF_PX + (size_t)s * 81920;
#pragma unroll
    for (int tt = 0; tt < 2; ++tt) {
        float v[12], mx = -3.0e38f;
#pragma unroll
        for (int i = 0; i < 12; ++i) {
            const float t = rbf(rbf(sc[tt][i >> 2][i & 3]) * kscale);
            v[i] = (i >= 8 || 8 * g + i <= 16 * tt + n) ? t : -3.0e38f;
            mx = fmaxf(mx, v[i]);
        }
        mx = xmax(mx);
        float sx = 0.f, sm = 0.f;
#pragma unroll
        for (int i = 0; i < 12; ++i) {
            v[i] = __builtin_amdgcn_exp2f((v[i] - mx) * 1.4426950408889634f);
            if (i < 8) sx += v[i]; else sm += v[i];
        }
        const float inv = __builtin_amdgcn_rcpf(xsum(sx + 0.5f * sm));
        const u32x4 px = u32x4{cvtpk(v[0] * inv, v[1] * inv), cvtpk(v[2] * inv, v[3] * inv), cvtpk(v[4] * inv, v[5] * inv), cvtpk(v[6] * inv, v[7] * inv)};
        const u32x2 pm = u32x2{cvtpk(v[9] * inv, v[8] * inv), cvtpk(v[11] * inv, v[10] * inv)};
        const int ox = h * 2048 + tt * 1024, om = 65536 + (h >> 2) * 2048 + (h & 3) * 256 + tt * 1024;
        if (!LATE && keep) {
            *LDSP(u32x4, lds + ox + lane * 16) = px;
            *LDSP(u32x2, lds + om + n * 16 + (g & 1) * 8) = pm;
        } else {
            gst16c(pb + ox, lane * 16, px);
            const auto a = __builtin_amdgcn_permlane16_swap(pm[0], pm[0], false, false), b = __builtin_amdgcn_permlane16_swap(pm[1], pm[1], false, false);
            if (g == 0) gst16c(pb + om, (u32)(n * 16), u32x4{a[0], b[0], a[1], b[1]});
        }
    }
}

#define WAIT10(N, r) asm volatile("s_waitcnt vmcnt(" #N ")" : "+v"(r[0]), "+v"(r[1]), "+v"(r[2]), "+v"(r[3]), "+v"(r[4]), "+v"(r[5]), "+v"(r[6]), "+v"(r[7]), "+v"(r[8]), "+v"(r[9]) : : "memory")
DI void out_task(const Ptrs& p, int s, int cg0, int cg1, u32 lds, int slow, int w, int sd) {
    u32x4 pf[2][10];
    if (cg1 - cg0 > 1) {
        const u32 la = lds + lanex() * 16;
#pragma unroll
        for (int i = 0; i < 20; ++i) pf[i & 1][i >> 1] = ldsr16(la + (i < 16 ? w * 16384 + i * 1024 : 65536 + w * 4096 + (i - 16) * 1024));
    } else {
        const char* pb = p.ws + OFF_PX + (size_t)s * 81920;
        const u32 l16 = lanex() * 16;
        sfor<0, 20>([&](auto I) { gld16c<(I.v & 3) * 1024>(pf[I.v & 1][I.v >> 1], pb + (I.v < 16 ? w * 16384 + (I.v >> 2) * 4096 : 65536 + w * 4096), l16); gap<SMO>(slow); if (!SMO && I.v == 9) pace(slow); });
        WAIT10(0, pf[0]); WAIT10(0, pf[1]);
    }
#pragma nounroll
    for (int cg = cg0; cg < cg1; ++cg) {
    const int lane = lanex(), tid = 64 * w + lane, n = lane & 15, g = lane >> 4;
    const char* db = p.ws + OFF_D + s * D_STRIDE + (size_t)(16 * cg) * 16384 + w * 4096;
    const char* ub = p.ws + uslot(sd, s) + (size_t)(16 * cg) * 4096 + w * 1024;
    const u32 dof = (u32)((n >> 3) * 16384 + g * 128 + (n & 7) * 16), uof = (u32)((n >> 3) * 4096 + g * 128 + (n & 7) * 16);
    u32x4 af[3][10];
    auto aload = [&](u32x4 (&r)[10], int ct) {
        if (!SMO) pace(slow);
        sfor<0, 8>([&](auto I) { gld16c<I.v * 512>(r[I.v], db + ct * 32768, dof); gap<SMO>(slow); });
        gld16c<0>(r[8], ub + uchunk(ct), uof); gap<SMO>(slow); gld16c<512>(r[9], ub + uchunk(ct), uof); gap<SMO>(slow);
    };
    aload(af[0], 0);
    aload(af[1], 1);
    aload(af[2], 2);
    const int tok = 32 * s + (tid >> 3), col = 128 * cg + 8 * (tid & 7);
    u32x4 bb[2]; u32 gv;
    gld16c<0>(bb[0], p.ws + OFF_BOUT, (u32)(s * 40960 + col * 64 + (tid >> 3) * 16)); gld16c<0>(bb[1], p.ws + OFF_BOUT + 4096, (u32)(s * 40960 + col * 64 + (tid >> 3) * 16));
    asm volatile("global_load_dword %0, %1, %2 sc1" : "=v"(gv) : "v"(tok * 4), "s"(p.ws + OFF_GATE) : "memory");
    barrier();
    const u32 wa = lds + (u32)w * 16896 + n * 528 + g * 16;
    sfor<0, 8>([&](auto I) {
        constexpr int ct = I.v;
        u32x4 (&r)[10] = af[ct % 3];
        if constexpr (ct == 0) WAIT10(23, r);
        else if constexpr (ct < 3) WAIT10(23, r);
        else if constexpr (ct < 6) WAIT10(20, r);
        else if constexpr (ct == 6) WAIT10(10, r);
        else WAIT10(0, r);
        f32x4 c0 = f32x4{0.f, 0.f, 0.f, 0.f}, c1 = c0;
#pragma unroll
        for (int i = 0; i < 10; ++i) { c0 = mfma16(r[i], pf[0][i], c0); c1 = mfma16(r[i], pf[1][i], c1); }
        *LDSP(f32x4, wa + ct * 64) = c0;
        *LDSP(f32x4, wa + ct * 64 + 16 * 528) = c1;
        PIN();
        if constexpr (ct + 3 < 8) aload(r, ct + 3);
        PIN();
    });
    asm volatile("" : "+v"(bb[0]), "+v"(bb[1]), "+v"(gv));
    barrier();
    const u32 ra = lds + (u32)(tid >> 3) * 528 + (tid & 7) * 32;
    const float gt = __uint_as_float(gv), g1 = rbf(1.f - gt);
    u32 ov[8];
#pragma unroll
    for (int i = 0; i < 4; ++i) {
        const u32 ri = ra + 16 * (i & 1) + 256 * (i >> 1);
        const f32x4 z = (*LDSP(f32x4, ri) + *LDSP(f32x4, ri + 16896)) + (*LDSP(f32x4, ri + 2 * 16896) + *LDSP(f32x4, ri + 3 * 16896));
#pragma unroll
        for (int j = 0; j < 2; ++j) {
            const u32 b = bb[i >> 1][2 * (i & 1) + j];
            const u32 a = cvtpk(z[2 * j], z[2 * j + 1]);
            const u32 ga = cvtpk(gt * __uint_as_float(a << 16), gt * __uint_as_float(a & 0xffff0000u));
            const u32 gb = cvtpk(g1 * __uint_as_float(b << 16), g1 * __uint_as_float(b & 0xffff0000u));
            ov[2 * i + j] = cvtpk(__uint_as_float(ga << 16) + __uint_as_float(gb << 16), __uint_as_float(ga & 0xffff0000u) + __uint_as_float(gb & 0xffff0000u));
        }
    }
    gst16p(p.out, (u32)(tok * 1280 + col * 2), u32x4{ov[0], ov[1], ov[2], ov[3]});
    gst16p(p.out, (u32)(tok * 1280 + col * 2 + 128), u32x4{ov[4], ov[5], ov[6], ov[7]});
    }
}

DI void out_tail(const Ptrs& p, const ChainCfg& cfg, int s, int cg, u32 lds, int w, int sd, int gr) {
    const int lane = lanex(), tid = 64 * w + lane, n = lane & 15, g = lane >> 4;
    const char* db = p.ws + OFF_D + s * D_STRIDE + (size_t)(4 * cg) * 16384 + w * 4096;
    const char* ub = p.ws + uslot(sd, s) + uchunk(2 * cg) + w * 1024;
    const char* pb = p.ws + OFF_PX + (size_t)s * 81920;
    const u32 dof = (u32)((n >> 3) * 16384 + g * 128 + (n & 7) * 16), uof = (u32)((n >> 3) * 4096 + g * 128 + (n & 7) * 16);
    u32x4 pf[2][10], af[2][10];
    sfor<0, 16>([&](auto I) { gld16c<(I.v & 7) * 512>(af[I.v >> 3][I.v & 7], db + (I.v >> 3) * 32768, dof); });
    const int tok = 32 * s + (tid >> 3), col = 32 * cg + 4 * (tid & 7);
    u32x2 bb; u32 gv;
    asm volatile("global_load_dwordx2 %0, %2, %3 sc1\n\tglobal_load_dword %1, %4, %5 sc1\n\ts_waitcnt vmcnt(0)" : "=&v"(bb), "=&v"(gv)
                 : "v"(s * 40960 + (col >> 3) * 512 + (tid >> 3) * 16 + (col & 7) * 2), "s"(p.ws + OFF_BOUT), "v"(tok * 4), "s"(p.ws + OFF_GATE) : "memory");
    sfor<0, 16>([&](auto I) { waitv<0>(af[I.v >> 3][I.v & 7]); });
    until(step_wait(p.ws + OFF_ST, w, gr, s - 1, lds) + (u32)(s == NSEG - 1 ? cfg.cR : cfg.cR + 2 * PDX));
    const u32 wa = lds + (u32)w * 4608 + n * 144 + g * 16;
#pragma nounroll
    for (int it = 0; it < (1 << 14); ++it) {
        if (it == 4) {
            flag_wait(p.ws + OFF_ST, F_PX + s, 8);
            flag_wait(p.ws + OFF_ST, s < NSEG - 2 ? F_CUR + gr : F_UD + 2 * (s & 1), s < NSEG - 2 ? s + 1 : 16);
            flag_wait(p.ws + OFF_ST, s < NSEG - 2 ? F_ONE : F_UD + 2 * (s & 1) + 1, 16);
        }
        sfor<0, 4>([&](auto I) { gld16c<(I.v & 1) * 512>(af[I.v >> 1][8 + (I.v & 1)], ub + (I.v >> 1) * 16384, uof); });
        sfor<0, 8>([&](auto I) { gld16c<(I.v & 1) * 2048>(pf[0][I.v], pb + w * 16384 + (I.v >> 1) * 4096, lane * 16); });
        gld16c<0>(pf[0][8], pb + 65536 + w * 4096, lane * 16); gld16c<2048>(pf[0][9], pb + 65536 + w * 4096, lane * 16);
        sfor<0, 8>([&](auto I) { gld16c<(I.v & 1) * 2048 + 1024>(pf[1][I.v], pb + w * 16384 + (I.v >> 1) * 4096, lane * 16); });
        gld16c<1024>(pf[1][8], pb + 65536 + w * 4096, lane * 16); gld16c<3072>(pf[1][9], pb + 65536 + w * 4096, lane * 16);
        WAIT10(10, pf[0]); WAIT10(10, af[0]); WAIT10(10, af[1]);
        float chk = 0.f;
#pragma unroll
        for (int tt = 0; tt < 2; ++tt) {
            if (tt) WAIT10(0, pf[1]);
            f32x4 c0 = f32x4{0.f, 0.f, 0.f, 0.f}, c1 = c0;
#pragma unroll
            for (int i = 0; i < 10; ++i) { c0 = mfma16(af[0][i], pf[tt][i], c0); c1 = mfma16(af[1][i], pf[tt][i], c1); }
            *LDSP(f32x4, wa + tt * 2304) = c0;
            *LDSP(f32x4, wa + tt * 2304 + 64) = c1;
            const f32x4 c = c0 + c1;
            chk += (c[0] + c[1]) + (c[2] + c[3]);
            PIN();
        }
        if (it >= 4 || __builtin_amdgcn_ballot_w64((__float_as_uint(chk) & 0x7fffffffu) > 0x7f800000u) == 0) break;
        if (s < NSEG - 1) nap(60);
    }
    barrier();
    const u32 ra = lds + (u32)(tid >> 3) * 144 + (tid & 7) * 16;
    const float gt = __uint_as_float(gv), g1 = rbf(1.f - gt);
    const f32x4 z = (*LDSP(f32x4, ra) + *LDSP(f32x4, ra + 4608)) + (*LDSP(f32x4, ra + 2 * 4608) + *LDSP(f32x4, ra + 3 * 4608));
    u32x2 ov;
#pragma unroll
    for (int j = 0; j < 2; ++j) {
        const u32 b = bb[j], a = cvtpk(z[2 * j], z[2 * j + 1]);
        const u32 ga = cvtpk(gt * __uint_as_float(a << 16), gt * __uint_as_float(a & 0xffff0000u));
        const u32 gb = cvtpk(g1 * __uint_as_float(b << 16), g1 * __uint_as_float(b & 0xffff0000u));
        ov[j] = cvtpk(__uint_as_float(ga << 16) + __uint_as_float(gb << 16), __uint_as_float(ga & 0xffff0000u) + __uint_as_float(gb & 0xffff0000u));
    }
    *(u32x2*)(p.out + tok * 1280 + col * 2) = ov;
}

DI float dot2bf(u32 a, u32 b, float c) { return __builtin_amdgcn_fdot2_f32_bf16(__builtin_bit_cast(bf16x2, a), __builtin_bit_cast(bf16x2, b), c, false); }
DI void battn_task(const Ptrs& p, int t0, u32 lds, int w) {
    const int lane = lanex(), tid = 64 * w + lane, tl = tid & 31, t = t0 + tl;
    const char* const qkv = p.ws + OFF_QKVB + (size_t)(t0 >> 5) * 122880;
    {
        u32 so[3];
#pragma unroll
        for (int i = 0; i < 3; ++i) {
            const int e = 64 * i + lane, sl = e % 48;
            so[i] = (u32)((e / 48) * 512 + (sl < 16 ? (t0 ? 0 : 122880) + 256 + sl * 16 : 122880 - 256 + sl * 16));
        }
#pragma unroll
        for (int i = 0; i < 30; ++i) { glds16c(qkv - 122880 + 40960 + (10 * w + i / 3) * 2048, so[i % 3], lds + (30 * w + i) * 1024); gap<SMB>(-1); }
    }
    u32x4 q[2][5], xv[10], gv[10];
    sfor<0, 10>([&](auto I) { gld16c<(I.v % 5) * 512>(q[I.v / 5][I.v % 5], qkv + (I.v / 5) * 20480, (u32)((tid >> 5) * 2560 + tl * 16)); gap<SMB>(-1); });
    sfor<0, 10>([&](auto I) { gld16<I.v * 128>(xv[I.v], p.x + (size_t)t0 * 1280, (u32)((tid >> 3) * 1280 + (tid & 7) * 16)); gld16<I.v * 128>(gv[I.v], p.gw, (u32)((tid & 7) * 16)); gap<SMB>(-1); });
    sfor<0, 10>([&](auto I) { waitv<0>(q[I.v / 5][I.v % 5]); waitv<0>(xv[I.v]); waitv<0>(gv[I.v]); });
    barrier();
    {
        float a = 0.f;
#pragma unroll
        for (int i = 0; i < 10; ++i)
#pragma unroll
            for (int j = 0; j < 4; ++j) {
                const u32 r = cvtpk(__uint_as_float(xv[i][j] << 16) * __uint_as_float(gv[i][j] << 16), __uint_as_float(xv[i][j] & 0xffff0000u) * __uint_as_float(gv[i][j] & 0xffff0000u));
                a += __uint_as_float(r << 16) + __uint_as_float(r & 0xffff0000u);
            }
        a += __shfl_xor(a, 1); a += __shfl_xor(a, 2); a += __shfl_xor(a, 4);
        const float z = rbf(a);
        if ((tid & 7) == 0) gst4c(p.ws + OFF_GATE, (u32)((t0 + (tid >> 3)) * 4), __float_as_uint(rbf(1.0f / (1.0f + __expf(-z)))));
    }
#pragma unroll
    for (int it = 0; it < 2; ++it) {
        const int m = (tid >> 5) + 8 * it;
        u32 ka = lds + (u32)(m * 3840 + (2 + tl) * 16), va = ka + 61440;
        asm volatile("" : "+v"(ka), "+v"(va));
        u32x4 k[5][8];
#pragma unroll
        for (int i = 0; i < 40; ++i) k[i >> 3][i & 7] = ldsr16(ka + (i >> 3) * 768 + (7 - (i & 7)) * 32);
        PIN();
        float lg[2][8], mx[2] = {-3.0e38f, -3.0e38f};
#pragma unroll
        for (int i = 0; i < 16; ++i) lg[i >> 3][i & 7] = 0.f;
#pragma unroll
        for (int i = 0; i < 5; ++i) {
#pragma unroll
            for (int j = 0; j < 4; ++j)
#pragma unroll
                for (int wv = 0; wv < 8; ++wv) lg[4 * i + j >= 10][wv] = dot2bf(q[it][i][j], k[i][wv][j], lg[4 * i + j >= 10][wv]);
            PIN();
#pragma unroll
            for (int wv = 0; wv < 8; ++wv) k[i][wv] = ldsr16(va + i * 768 + (7 - wv) * 32);
            PIN();
        }
#pragma unroll
        for (int i = 0; i < 16; ++i) {
            float& l = lg[i >> 3][i & 7];
            l = rbf(rbf(l) * 0.22360679774997896f);
            asm volatile("" : "+v"(l));
            l = t - 2 * (i & 7) >= 0 ? l : -3.0e38f;
            mx[i >> 3] = fmaxf(mx[i >> 3], l);
        }
        u32 pk[2][4];
#pragma unroll
        for (int u = 0; u < 2; ++u) {
            float sum = 0.f;
#pragma unroll
            for (int wv = 0; wv < 8; ++wv) { lg[u][wv] = __builtin_amdgcn_exp2f((lg[u][wv] - mx[u]) * 1.4426950408889634f); sum += lg[u][wv]; }
            const float inv = __builtin_amdgcn_rcpf(sum);
#pragma unroll
            for (int wv = 0; wv < 4; ++wv) pk[u][wv] = cvtpk(lg[u][2 * wv] * inv, lg[u][2 * wv + 1] * inv);
        }
#pragma unroll
        for (int i = 0; i < 5; ++i) {
            float o[8];
#pragma unroll
            for (int d = 0; d < 8; ++d) o[d] = 0.f;
#pragma unroll
            for (int wp = 0; wp < 4; ++wp) {
                const u32x4 v0 = k[i][2 * wp], v1 = k[i][2 * wp + 1];
#pragma unroll
                for (int j = 0; j < 4; ++j) {
                    const u32 pw = pk[4 * i + j >= 10][wp];
                    o[2 * j] = dot2bf(__builtin_amdgcn_perm(v1[j], v0[j], 0x05040100u), pw, o[2 * j]);
                    o[2 * j + 1] = dot2bf(__builtin_amdgcn_perm(v1[j], v0[j], 0x07060302u), pw, o[2 * j + 1]);
                }
            }
            gst16c(p.ws + OFF_OB + (size_t)(t0 >> 5) * 40960 + 512 * i, (u32)(m * 2560 + tl * 16), u32x4{cvtpk(o[0], o[1]), cvtpk(o[2], o[3]), cvtpk(o[4], o[5]), cvtpk(o[6], o[7])});
        }
    }
}

#define XPF 1
#define PFQ 0
#define PFN 8
#define OLAG 3
constexpr int QN = 176, QF = 19;
struct QTab { u16 e[QN]; int n, nf; u32 tt[64]; };
typedef const __attribute__((address_space(4))) int* KI;
constexpr int QT_OFF = (88 + 48) / 4;
DI int pick_task(char* fl, KI k, int gr) {
    u32 r;
    asm volatile("s_mov_b64 exec, 1\n\ts_nop 3\n\tglobal_atomic_add %0, %1, %2, %3 sc0 sc1\n\ts_mov_b64 exec, -1\n\ts_waitcnt vmcnt(0)"
                 : "=&v"(r) : "v"((F_QH + gr) * 128), "v"(1), "s"(fl) : "memory");
    const int x = uni((int)r) + 1 + QF;
    if (x >= k[QT_OFF + QN / 2]) return -1;
    const u32 e2 = (u32)k[QT_OFF + (x >> 1)];
    return (int)(x & 1 ? e2 >> 16 : e2 & 0xffffu);
}
__global__ __launch_bounds__(256, 1) void k_main(Ptrs p0, ChainCfg cfg, QTab tab) {
    extern __shared__ __attribute__((aligned(16))) char smem[];
    u32 lds = (u32)(size_t)LDSP(char, smem);
#define HALF 0
    int n = HALF ? (blockIdx.x & 4) * 32 + (int)(blockIdx.x >> 3) * 4 + (blockIdx.x & 3) : blockIdx.x, gr = n & 7;
    int w = uni(threadIdx.x >> 6);
    {
        u64 pc; int len;
        asm volatile("s_getpc_b64 %0\n.Lkm_first:\n\ts_mov_b32 %1, .Lkm_last-.Lkm_first\n\ts_max_i32 %1, %1, .Lkm_lu-.Lkm_first\n\ts_max_i32 %1, %1, .Lkm_lv-.Lkm_first\n\t"
                     "s_max_i32 %1, %1, .Lkm_g2-.Lkm_first\n\ts_max_i32 %1, %1, .Lkm_g4-.Lkm_first" : "=s"(pc), "=s"(len) : : "scc");
        if (w == 3) pfl((const char*)pc, (u32)min((int)(blockIdx.x >> 3) * 8192 + (int)(threadIdx.x & 63) * 128, max(len, 0)), lds);
    }
    karg_warm();
    if (n < NH) { score_role(p0, cfg, n, lds, w); return; }
    if (n < NS) { proj_role(p0, cfg, n - NH, lds, w); return; }
    const bool isv = n < NS + NVB;
#define qslot (lds + GRING * GBUF)
    int sdw = -1;
    int en = isv ? Q_VD + (n & 24) : tab.e[(n - NS - NVB) >> 3] & 511;
    u32 phi0 = 0;
    Sync y;
    if (XPF && n < 160 && w >= 2) pfl(p0.x, (u32)min(((n - NS) >> 3) * 128 + (w & 1) * 64 + lanex(), 1279) * 128, lds);
    if (n >= 160) {
        const int lane = lanex();
        if (n >= 192 && (n & 8)) init_task(p0, (n & 7) + ((n - 192) >> 4) * 8, lds, w);
        else {
            const int h = n < 192 ? n & 31 : (n & 7) + ((n - 192) >> 4) * 8;
            xcopy_task(p0, h + (n < 192 ? 0 : 32), w);
            if (n < 192) {
#pragma unroll
                for (int i = 0; i < PFN; ++i) {
                    const int l = 64 * min(i * 4 + w, 29) + lane;
                    pfl(p0.wo + (size_t)h * 245760, (u32)(l * 128), lds);
                    if (PFQ) pfl(p0.wq + h * 384, (u32)((l / 3) * 12288 + (l % 3) * 128), lds);
                }
            }
        }
        if (w == 1) {
            sync_ld(y, p0.ws);
            waitvm<0>();
            asm volatile("" : "+v"(y.r), "+v"(y.g), "+v"(y.e));
            if (n < 192) xcopy_wait(p0.ws, sync_tag(y.e));
            else xcopy_wait(p0.ws, sync_tag(y.e), n & 8 ? 4 : 8, n & 8 ? 3 : 7);
        }
        if (w == 0 && n < 192) {
            sync_ld(y, p0.ws);
            waitvm<0>();
            *LDSP(u32, qslot) = (u32)sync_end(y, p0.ws, phi0, false);
        }
        barrier();
        if (w == 0 && n < 192) flag_add(p0.ws + OFF_ST, F_ACK, 1);
        if (n < 192) sdw = uni((int)*LDSP(u32, qslot));
        barrier();
    }
    while (true) {
        typedef char* const __attribute__((address_space(4)))* KP;
        KP kp = (KP)__builtin_amdgcn_kernarg_segment_ptr();
        asm volatile("" : "+s"(kp), "+s"(lds), "+s"(w), "+s"(gr), "+s"(n));
        __builtin_assume(w >= 0 && w < 4 && gr >= 0 && gr < 8);
        const Ptrs p{kp[0], kp[1], kp[2], kp[3], kp[4], kp[5], kp[6], kp[7], kp[8], kp[9], kp[10]};
        const int ty = en & 7, gate = en >> 9;
        const bool fu = ty == Q_PX && (en >> 3 & 63) < 8;
        const int ix = fu ? (en & 504) + gr : en >> 3 & 63, sf = ((KI)kp)[QT_OFF + QN / 2 + 1] & 255;
        char* const fl = p.ws + OFF_ST;
        const bool raw = sdw < 0;
        int t0 = gate - 1, f1 = F_ONE, t1 = 0, f2 = F_ONE, t2 = 0, f3 = F_ONE, t3 = 0, dt = cfg.T2;
        const int os = ix >= 40 ? 44 + (8 * ix + gr) / 20 : (8 * ix + gr) / 5;
        const int bb = ty == Q_BO ? (8 * ix + gr) >> 2 : os;
        if (ty == Q_BA) { f1 = F_B + (gr & RM) | ix << 16; t1 = 5; f2 = F_B + (gr & RM) | max(ix - 1, 0) << 16; t2 = 5; }
        if (ty == Q_BO) { f1 = F_B + (gr & RM) | (8 + bb) << 16; t1 = 8; }
        if (ty == Q_PX) { f1 = F_Q + pblk(ix) * 8 + (fu ? gr & RM : gr) | fu << 16; t1 = fu ? 32 : 4; if (fu) t0 += gr * (((KI)kp)[QT_OFF + QN / 2 + 1] >> 8) >> 3; if (ix < PXT) t0 = max(t0, ix + 1); }
        if (ty == Q_OUT) { f3 = F_B + (gr & RM) | (16 + (os >> 3)) << 16; t3 = 4; if (os < 60) { f1 = F_PX + os; t1 = 8; t0 = max(t0, os + (os < PXT ? OLAG : 1)); if (os >= PXT) dt = cfg.s1; } else { f1 = F_VD + 64 + (gr & RM); t1 = 32; } }
        const bool skip = fu ? ix >= sf : ty == Q_OUT && ix < 40 && (os < sf || os >= 60);
        if ((gate || ty >= Q_BA) && !skip) wg_wait(fl, w, F_CUR + gr, t0, f1, t1, f2, t2, f3, t3, dt, false);
        int sig = F_END, nrep = 1;
        if (skip) {}
        else if (ty <= Q_QKVB || ty == Q_BO) {
            const bool ab = ty <= Q_KQ;
            sig = ty == Q_VD ? F_VD + (ix >> 2) * 8 : (ty == Q_KQ ? F_Q + (ix >> 2) * 8 : (ix & 4 ? F_Q : F_K + 8 * ix)) + gr;
            if (!ab) sig = F_B | (bb + (ty == Q_BO ? 16 : 0)) << 16;
            nrep = ty == Q_VD || !ab ? RM + 1 : 1;
            if (ty == Q_VD && ix < 4) { sig += n & 32 ? 4 : 0; nrep = 4; }
            run_gemm(p, ty, ab ? ix >> 2 : bb, ab ? gr + 8 * (ix & 3) : 8 * ix + gr - (ty == Q_BO ? 4 : 5) * bb, (n >> 5) & 1, raw && n < 160, y, lds, w);
            if (raw) sdw = sync_end(y, p.ws, phi0, w == 0);
        } else {
            if (ty == Q_BA) {
                battn_task(p, (8 * ix + gr) * 32, lds, w);
                sig = F_B | (8 + ix) << 16; nrep = RM + 1;
            } else if (ty == Q_PX) {
                if (ix < PXT) {
#pragma nounroll
                    for (int i = fu ? 0 : gr; i < (fu ? 8 : gr + 1); ++i) px_wave<false>(p, cfg, ix, i + 8 * w, ix - cfg.fast, w, lds, fu);
                } else px_wave<true>(p, cfg, ix, gr + 8 * w, -1, w, lds, false);
                sig = F_PX + ix;
                if (fu) wg_wait(fl, w, F_ONE, 0, F_B + (gr & RM) | (16 + (ix >> 3)) << 16, 4, F_ONE, 0, F_ONE, 0, dt, false);
            }
            if (ty == Q_OUT || fu) {
                const int cg = 8 * ix + gr - 5 * os;
                if (ix >= 40 && !fu) out_tail(p, cfg, os, 8 * ix + gr - 20 * (os - 44), lds, w, sdw, gr);
                else out_task(p, fu ? ix : os, fu ? 0 : cg, fu ? 5 : cg + 1, lds, (fu ? ix : os) + 1 - cfg.fast, w, sdw);
                sig = F_END;
            }
            waitvm<0>();
        }
        barrier();
        if (w == 0 && sig != F_END) flag_add(fl, sig, nrep);
        if (w == 1 && sig >= F_Q && sig < F_N) flag_add(fl, sig - gr | 1 << 16, RM + 1);
        if (raw && !isv && w == 0) xcopy_wait(p.ws, sync_tag(y.e));
        if (isv) break;
        if (w == 0) *LDSP(int, qslot) = pick_task(fl, (KI)kp, gr);
        barrier();
        en = uni(*LDSP(int, qslot));
        if (en < 0) return;
        barrier();
    }
    value_role(p0, cfg, n - NS, sdw, phi0, lds, w);
}

#define QCFG 14, 11, 3, 5, 0, 50, 36
#define LVDEF "00?>>===<<<<<<<<<;:99::;;<<;;:::;;<<;;<<;:99988899::;;;;::999996"
constexpr int DX = 340, DUA = 430, DUB = 0, DUC = 4;
static ChainCfg dev_cfg(ChainCfg c) { c.T = PDL; c.cR = ODL; c.s1 = TDT; c.T2 = WSLEEP; c.cR2 = DX; c.dU = DUA; c.s2 = DUB; c.T3 = DUC; c.dV |= 4 << 8 | 8 << 16 | 0 << 24; c.cR3 = 4 | 4 << 8 | 0 << 16; return c; }
static QTab set_sched(QTab t, const ChainCfg c, const char* lv) {
    for (int s = 0; s < 64; ++s) {
        const int d = lv[s] - '0';
        t.tt[s] = (u32)((s < c.s1 ? c.T : (s < c.s2 ? c.T2 : c.T3)) + 4 * d) | (u32)((s < c.s1 ? c.cR : (s < c.s2 ? c.cR2 : c.cR3)) + 5 * d / 2) << 16;
    }
    t.tt[0] = (u32)270 | (u32)165 << 16;
    t.tt[1] = (u32)210 | (u32)144 << 16;
    return t;
}
#define QORD "bbV3qbK3m44bm55bm66m77m88"
#define GATES "11 13 15 17 22 24 32 36 19 22 29 42 13 17 21 26 33 41"
static QTab make_tab(int lv, int lk, int b0, int d1, int d2, int lg, int nf) {
    QTab t{};
    int n = 0, dl[QN], nq = 0;
    auto add = [&](int ty, int ix) { t.e[n] = (u16)(ty | ix << 3); dl[n++] = ty == Q_VD || ty == Q_KQ ? 8 * (ix / 4 - 1) - (ty == Q_VD ? lv : lk) - (ix >= 32 ? 0 : 0) : 99; };
    for (int i = 1; i < 4; ++i) add(Q_VD, i);
    for (int i = 0; i < 16; ++i) add(i < 4 ? Q_H : ((i & 8) ? Q_VD : Q_KQ), i < 8 ? i : 8 - (i & 1) * 4 + ((i - 8) >> 1));
    for (int i = 0; i < 4; ++i) add(Q_KQ, 8 + i);
    for (const char* c = QORD; *c; ++c) {
        if (*c == 'b') add(Q_QKVB, nq++);
        else if (*c == 'q') for (int i = 4; i < 8; ++i) add(Q_H, i);
        else if (*c == '.') dl[n - 1] |= 1 << 20;
        else if (*c == 'w') { const int b = *++c - '0'; for (int i = 0; i < 16; ++i) add(i & 2 ? Q_KQ : Q_VD, 4 * (b + (i & 1)) + (i >> 2)); }
        else if (*c == 'n') { const int b = *++c - '0'; for (int i = 0; i < 8; ++i) add(i & 1 ? Q_KQ : Q_VD, 4 * b + (i >> 1) - (i & 1) * 2); }
        else if (*c == 'h' || *c == 'e') { const int b = 4 * (c[1] - '0') + (*c == 'e') * 2; ++c; add(Q_KQ, b); add(Q_KQ, b + 1); }
        else if (*c == 'm') { const int b = c[1] - '0', b2 = c[2] - '0'; c += 2; for (int i = 0; i < 8; ++i) add(i & 1 ? Q_KQ : Q_VD, 4 * (i & 1 ? b2 : b) + ((i >> 1) ^ (i & 2 ? 0 : 0))); }
        else {
            const int ty = (*c | 32) == 'v' ? Q_VD : Q_KQ, two = *c >> 5 & 1, b = *++c - '0';
            for (int i = 0; i < (4 << two); ++i) add(ty, 4 * (b + (i & two)) + (i >> two));
        }
    }
    const int gap = 8;
    for (int i = n - 1, m = 999; i >= 0; --i) { const int pz = dl[i] > 999; m = min(8 * (pz ? dl[i] - (1 << 20) : dl[i]), m - gap - 8 * pz); dl[i] = max(m, 0); }
    auto put = [&](int ty, int ix, int g, int pos) {
        int i = n++;
        for (; dl[i - 1] > pos; --i) { t.e[i] = t.e[i - 1]; dl[i] = dl[i - 1]; }
        t.e[i] = (u16)(ty | ix << 3 | (g + 1) << 9); dl[i] = pos;
    };
    for (int i = 0; i < n; ++i) if (dl[i] >= 8) t.e[i] |= (u16)((dl[i] / 8 + 1) << 9);
    const char* gs = GATES;
    auto gt = [&](int i) { return 10 * (gs[3 * i] - '0') + gs[3 * i + 1] - '0'; };
    for (int i = 0; i < 8; ++i) put(Q_BA, i, gt(i), 8 * gt(i));
    for (int i = 0; i < 4; ++i) put(Q_BO, i, gt(8 + i), 8 * gt(8 + i));
    for (int i = 0; 8 * i < nf; ++i) put(Q_PX, i, gt(12 + i), 8 * (max(gt(12 + i), 8 * i + 1) + b0));
    auto pg = [&](int s) { return max(8 * s + 8, 8 * 48 + (s - nf) * 3); };
    for (int s = nf; s < NSEG; ++s) { const int g = s < PXT ? pg(s) : 8 * max(s - 3, lg); put(Q_PX, s, g / 8, g); }
    for (int x = 5 * nf / 8; x < 38; ++x) { const int os = (8 * x + 4) / 5, g = pg(min((8 * x + 7) / 5, PXT - 1)); put(Q_OUT, x, g > 8 * ((8 * x + 7) / 5) + 8 ? g / 8 + OLAG - 1 : 0, os < PXT ? pg(os) + 8 * (OLAG - 1) : 8 * (os + 1)); }
    for (int x = 40; x < 50; ++x) { const int g = max(44 + 8 * x / 20 - 3, lg); put(Q_OUT, x, g, 8 * g + 4); }
    t.n = n; t.nf = nf | d1 << 8;
    return t;
}

extern "C" void kernel_launch(void* const* d_in, const int* in_sizes, int n_in, void* d_out, int out_size, void* d_ws,
                              size_t ws_size, hipStream_t stream) {
    Ptrs p{(const char*)d_in[0], (const char*)d_in[1], (const char*)d_in[2], (const char*)d_in[3], (const char*)d_in[4], (const char*)d_in[5],
           (const char*)d_in[8], (const char*)d_in[9], (const char*)d_in[10], (char*)d_out, (char*)(((size_t)d_ws + 0x1fffff) & ~(size_t)0x1fffff)};
    (void)hipFuncSetAttribute((const void*)k_main, hipFuncAttributeMaxDynamicSharedMemorySize, S_LDS);
    k_main<<<256, 256, S_LDS, stream>>>(p, dev_cfg(ChainCfg{CFG}), set_sched(make_tab(QCFG), ChainCfg{CFG}, LVDEF));
    static_assert(sizeof(Ptrs) + sizeof(ChainCfg) + sizeof(QTab) == TT_OFF + 256, "");
}
```

```cpp
#include <hip/hip_runtime.h>
#pragma clang diagnostic ignored "-Wint-to-pointer-cast"

typedef unsigned short u16;
typedef unsigned int u32;
typedef unsigned long long u64;
typedef __bf16 bf16x8 __attribute__((ext_vector_type(8)));
typedef __bf16 bf16x16 __attribute__((ext_vector_type(16)));
typedef short s16x4 __attribute__((ext_vector_type(4)));
typedef float f32x4 __attribute__((ext_vector_type(4)));
typedef unsigned u32x4 __attribute__((ext_vector_type(4)));
typedef unsigned u32x2 __attribute__((ext_vector_type(2)));
typedef unsigned u32x8 __attribute__((ext_vector_type(8)));
typedef unsigned u32x16 __attribute__((ext_vector_type(16)));
typedef unsigned u32x32 __attribute__((ext_vector_type(32)));
#define DI __device__ __forceinline__
#define LDSP(T, a) ((__attribute__((address_space(3))) T*)(a))

constexpr int S_LEN = 2048, DM = 640, NH = 32, DH = 192, NSEG = 64, HD = NH * DH;
#define NSAMP 4
constexpr int NREG = 13;
constexpr int NV = 80, NVB = 40;
constexpr int NS = 64;
#define CFG 147, 105, 104, 20, 57, 16, 147, 105, 52, 147, 105, 6

constexpr size_t U_STRIDE = NV * 4096, D_STRIDE = NV * 16384;
constexpr size_t HO_STRIDE = 65536;
constexpr size_t OFF_ST = NSEG * HO_STRIDE;
enum { F_ACK = 0, F_ONE = 3, F_VD = 16, F_QH = 96, F_B = 104, F_CUR = 128, F_PROG = 136 - 8 * 62, F_UD = 152, F_PX = 160, F_K = 224, F_END = 512, F_Q = 520, F_N = 592 };
constexpr size_t OFF_LAT = OFF_ST + F_N * 128;
constexpr size_t OFF_KX = 5u << 20;
constexpr size_t OFF_QX = OFF_KX + (size_t)S_LEN * HD * 2;
constexpr size_t OFF_D = OFF_QX + (size_t)S_LEN * HD * 2;
constexpr size_t OFF_U = (OFF_D + NSEG * D_STRIDE + 0x1fffff) & ~(size_t)0x1fffff;
constexpr size_t OFF_KM = OFF_U + (22u << 20);
constexpr size_t OFF_PX = OFF_KM + (size_t)NSEG * NH * 3072;
constexpr size_t OFF_QKVB = OFF_PX + (size_t)NSEG * 81920;
constexpr size_t OFF_OB = OFF_QKVB + (size_t)S_LEN * 1920 * 2;
constexpr size_t OFF_BOUT = OFF_OB + (size_t)S_LEN * DM * 2;
constexpr size_t OFF_GATE = OFF_BOUT + (size_t)S_LEN * DM * 2;
constexpr size_t OFF_XF = OFF_GATE + S_LEN * 4;
constexpr size_t WS_END = OFF_XF + (size_t)S_LEN * DM * 2;
static_assert(WS_END + (2u << 20) <= (256u << 20), "workspace");

typedef __bf16 bf16x2 __attribute__((ext_vector_type(2)));
typedef float f32x2 __attribute__((ext_vector_type(2)));
DI u32 cvtpk(float lo, float hi) { return __builtin_bit_cast(u32, __builtin_convertvector(f32x2{lo, hi}, bf16x2)); }
DI float rbf(float f) { return __uint_as_float(cvtpk(f, f) << 16); }
DI f32x4 mfma16(u32x4 a, u32x4 b, f32x4 c) {
    return __builtin_amdgcn_mfma_f32_16x16x32_bf16(__builtin_bit_cast(bf16x8, a), __builtin_bit_cast(bf16x8, b), c, 0, 0, 0);
}
DI u32x2 trread(u32 a) {
    return __builtin_bit_cast(u32x2, __builtin_amdgcn_ds_read_tr16_b64_v4i16(LDSP(s16x4, a)));
}
DI void glds16c(const char* sbase, u32 voff, u32 lds) {
    u32 sv;
    asm volatile("s_mov_b32 %0, m0\n\ts_mov_b32 m0, %3\n\ts_nop 0\n\tglobal_load_lds_dwordx4 %1, %2 sc1\n\ts_mov_b32 m0, %0"
                 : "=&s"(sv) : "v"(voff), "s"(sbase), "s"(lds) : "memory");
}
template <int IMM>
DI void gld16(u32x4& r, const char* sbase, u32 voff) {
    asm volatile("s_nop 4\n\tglobal_load_dwordx4 %0, %1, %2 offset:%3" : "=v"(r) : "v"(voff), "s"(sbase), "i"(IMM) : "memory");
}
DI void gst16(char* sbase, u32 voff, u32x4 v) {
    asm volatile("global_store_dwordx4 %0, %1, %2 sc1\n\ts_nop 1" : : "v"(voff), "v"(v), "s"(sbase) : "memory");
}
#define OST " nt"
DI void gst16p(char* sbase, u32 voff, u32x4 v) {
    asm volatile("s_nop 4\n\tglobal_store_dwordx4 %0, %1, %2" OST "\n\ts_nop 1" : : "v"(voff), "v"(v), "s"(sbase) : "memory");
}
DI void gst8(char* sbase, u32 voff, u32x2 v) {
    asm volatile("global_store_dwordx2 %0, %1, %2 sc1\n\ts_nop 1" : : "v"(voff), "v"(v), "s"(sbase) : "memory");
}
template <int N>
DI void waitvm() { asm volatile("s_waitcnt vmcnt(%0)" : : "i"(N) : "memory"); }
DI void barrier() { asm volatile("s_waitcnt lgkmcnt(0)\n\ts_barrier" : : : "memory"); }
#define PIN() __builtin_amdgcn_sched_barrier(0)
DI int uni(int v) { return __builtin_amdgcn_readfirstlane(v); }
DI int lanex() {
    int t;
    asm volatile("v_mbcnt_lo_u32_b32 %0, -1, 0\n\tv_mbcnt_hi_u32_b32 %0, -1, %0" : "=v"(t));
    __builtin_assume(t >= 0 && t < 64);
    return t;
}
DI void flag_ld(u32& r, const char* fl, int idx) {
    asm volatile("s_nop 4\n\tglobal_load_dword %0, %1, %2 sc1" : "=v"(r) : "v"((idx & 0xffff) * 128 + (idx >> 14)), "s"(fl) : "memory");
}
DI void flag_ldm(u32& r, const char* fl, int idx, bool on) {
    asm volatile("v_mov_b32 %0, 1000\n\ts_mov_b32 exec_lo, %3\n\ts_mov_b32 exec_hi, 0\n\tglobal_load_dword %0, %1, %2 sc1\n\ts_mov_b64 exec, -1" : "=&v"(r) : "v"(idx * 128), "s"(fl), "s"(uni(on ? 1 : 0)) : "memory");
}
DI void flag_wait(const char* fl, int idx, int target) {
#pragma nounroll
    for (int it = 0; it < (1 << 16); ++it) {
        u32 v;
        flag_ld(v, fl, idx);
        asm volatile("s_waitcnt vmcnt(0)" : "+v"(v) : : "memory");
        if (uni((int)(v + 1u)) >= target) break;
        __builtin_amdgcn_s_sleep(6);
    }
}
DI void flag_add(char* fl, int idx, int nrep) {
    const int lane = lanex();
    if (lane < nrep) asm volatile("s_nop 4\n\tglobal_atomic_add %0, %1, %2 sc1" : : "v"(((idx & 0xffff) + lane) * 128 + (idx >> 14)), "v"(1), "s"(fl) : "memory");
}
DI void acquire() { asm volatile("buffer_inv sc1\n\ts_waitcnt vmcnt(0)" : : : "memory"); }

template <int V> struct IC { static constexpr int v = V; };
template <int B, int E, class Fn>
DI void sfor(Fn&& fn) { if constexpr (B < E) { fn(IC<B>{}); sfor<B + 1, E>(fn); } }
enum { T_QK = 0, T_VD = 1, T_BL = 3 };
constexpr int GBUF = 24576, GRING = 6;
struct GemmArgs {
    const char *w1, *w1b;
    const char* w2;
    const char* x;
    char *out, *outb;
    int ldw;
    char* sig;
    int type, np1, tn;
    int sV, sP, sQ;
    int xb;
};
DI const char* unip(const char* p) {
    const u64 v = (u64)p;
    return (const char*)(((u64)(u32)uni((int)(v >> 32)) << 32) | (u32)uni((int)v));
}
template <int Z>
DI void mf1(f32x4& c, const u32x4& a, const u32x4& b) {
    if constexpr (Z) asm volatile("v_mfma_f32_16x16x32_bf16 %0, %1, %2, 0" : "=&v"(c) : "v"(a), "a"(b));
    else asm volatile("v_mfma_f32_16x16x32_bf16 %0, %1, %2, %0" : "+v"(c) : "v"(a), "a"(b));
}
template <int Z>
DI void mf2(f32x4& c, const u32x4& a, const u32x4& b) {
    if constexpr (Z) asm volatile("v_mfma_f32_16x16x32_bf16 %0, %1, %2, 0" : "=&v"(c) : "v"(a), "v"(b));
    else asm volatile("v_mfma_f32_16x16x32_bf16 %0, %1, %2, %0" : "+v"(c) : "v"(a), "v"(b));
}
template <int IMM>
DI void gld16a(u32x4& r, const char* sbase, u32 voff) {
    asm volatile("global_load_dwordx4 %0, %1, %2 offset:%3 sc1" : "+a"(r) : "v"(voff), "s"(sbase), "i"(IMM) : "memory");
}
DI void setm0(u32 v) { asm volatile("s_mov_b32 m0, %0" : : "s"(v) : "memory"); }
template <int IMM>
DI void gldsm(const char* sbase, u32 voff) {
    asm volatile("global_load_lds_dwordx4 %0, %1 offset:%2" : : "v"(voff), "s"(sbase), "i"(IMM) : "memory");
}
DI void gst16n(char* sbase, u32 voff, u32x4 v) {
    asm volatile("global_store_dwordx4 %0, %1, %2 sc1" : : "v"(voff), "v"(v), "s"(sbase) : "memory");
}
template <int N>
DI void waitbar() { asm volatile("s_waitcnt vmcnt(%0)\n\ts_barrier" : : "i"(N) : "memory"); }
#define LGKM(n) __builtin_amdgcn_s_waitcnt(0xC07F | ((n) << 8))

#define XSL 10
DI int xhalf() { u32 x; asm volatile("s_getreg_b32 %0, hwreg(HW_REG_XCC_ID)" : "=s"(x)); return (int)((x >> 2) & 1); }
struct Sync { u32x2 r, g, e; };
DI void sync_ld(Sync& y, const char* ws) {
    asm volatile("s_nop 4\n\tglobal_load_dwordx2 %0, %3, %5 sc1\n\tglobal_load_dwordx2 %1, %4, %5 offset:512 sc1\n\tglobal_load_dwordx2 %2, %4, %5 offset:1024 sc1"
                 : "=&v"(y.r), "=&v"(y.g), "=&v"(y.e) : "v"((lanex() & 31) * 8), "v"(0), "s"(ws + OFF_LAT) : "memory");
}
DI u64 sync_tag(u32x2 e) { return (((u64)e[1] << 32 | e[0]) + 1) << 1; }
DI int sync_end(Sync& y, const char* ws, u32& phi, bool ack) {
    asm volatile("" : "+v"(y.r), "+v"(y.g), "+v"(y.e));
#pragma nounroll
    for (int it = 0; it < (1 << 16); ++it) {
        const u64 t = sync_tag(y.e);
        if (__builtin_amdgcn_ballot_w64((y.r[0] & ~1u) != (u32)t || y.r[1] != (u32)(t >> 32) || y.g[0] != (u32)t) == 0) break;
        __builtin_amdgcn_s_sleep(XSL);
        sync_ld(y, ws);
        asm volatile("s_waitcnt vmcnt(0)" : "+v"(y.r), "+v"(y.g), "+v"(y.e) : : "memory");
    }
    if (ack) flag_add((char*)ws + OFF_ST, F_ACK, 1);
    phi = (u32)uni((int)y.g[1]);
    const u32 b = (u32)__builtin_amdgcn_ballot_w64(y.r[0] & 1), all = __builtin_popcount(b) > 16;
    u32 q = 0;
#pragma unroll
    for (int r = 0; r < NREG; ++r) {
        const u32 c = r < 2 ? __builtin_popcount(b & (0x55u << r)) + all : 2 * __builtin_popcount((b >> (4 + 2 * r)) & 3) + all;
        q |= ((c > 2) ^ (u32)((r < 2 ? r : r + (OFF_U >> 21)) & 1)) << r;
    }
    return uni((int)(q ^ 3u * xhalf()));
}
DI void pace(int on) {
    asm volatile("s_cmp_ge_i32 %0, 0\n\ts_cbranch_scc1 .Lp%=\n\ts_waitcnt vmcnt(0)\n.Lp%=:" : : "s"(on) : "memory", "scc");
}
#define SMO 48
#define RM 7
#define BLS 1
#define EPG 1
#define PXT 58
#define PDX 40
#define SPL 0
#define HP0 48
#define UPF 5
#define BPF 1
#define FRD 25
#define KNT 1
#define BAS 3
#define SMB 48
#define SMP SMO
template <int C>
DI void gap(int on) {
    if constexpr (C > 0) asm volatile("s_cmp_ge_i32 %0, 0\n\ts_cbranch_scc1 .Lg%=\n\ts_nop %1\n\ts_nop %2\n\ts_nop %3\n.Lg%=:" : : "s"(on), "n"(C / 12 - 1), "n"(C / 12 - 1), "n"(C / 12 - 1) : "memory", "scc");
}
DI void early_add(char* ctr, int c) {
    asm volatile("s_cmp_lg_u32 %1, 1\n\ts_cbranch_scc1 .Le%=\n\ts_mov_b64 exec, 1\n\ts_nop 1\n\tglobal_atomic_add %2, %3, %0 sc1\n\ts_mov_b64 exec, -1\n.Le%=:"
                 : : "s"(ctr), "s"(c), "v"(0), "v"(1) : "memory", "scc");
}
#define XL 2
template <int NT>
DI void gemm_task(const GemmArgs& g, Sync& y, const char* ws, u32 lds, int w) {
    constexpr int FD = 5, NX = 2 * NT, OPS = NX + 6, BI = 24 - FD;
    const int lane = lanex();
    const int n = lane & 15, q = lane >> 4, qq = (lane >> 2) & 3, pp = lane & 3;
    const bool p2 = g.type == T_VD;
    const int rw = lane >> 3, sp = (lane >> 1) & 3;
    const u32 v2b = (u32)(rw * 1280 + 16 * (lane & 1) + 5120);
    auto v2o = [&](int i) { return v2b + (u32)(((sp ^ (rw >> 1)) << 5) + i * 9216); };
    u32 wv[6];
#pragma unroll
    for (int i = 0; i < 6; ++i)
        wv[i] = (u32)((rw + 8 * (i / 3)) * g.ldw + ((sp ^ (((rw >> 1) & 1) + 2 * (i / 3))) << 5) + 16 * (lane & 1) + (i % 3) * 128 + 5120 - 1024 * i);
    const char* const wp2 = g.w2 + (size_t)(48 * w) * 1280 - 2048;
    const char* wp = unip(g.w1 + (size_t)(16 * w) * g.ldw - 2048);
    int wstep = uni(64 * g.ldw);
    u32 wdst = lds + (u32)w * 6144 + 3072;
    const u32 wdlast = wdst + (GRING - 1) * GBUF;
    auto wring = [&]() { wdst = wdst == wdlast ? wdst - (GRING - 1) * GBUF : wdst + GBUF; };
    auto wload = [&](int i) {
        setm0(wdst);
        wring();
        asm volatile("s_nop 0");
        sfor<0, 6>([&](auto I) { gldsm<1024 * (I.v - 3)>(wp, wv[I.v]); });
        wp += wstep;
    };
    u32x4 vf[NT][6];
    u32x4 fr[FD + 1];
    u32x4 xr[5][NX];
    auto xtie = [&](u32x4 (&x)[NX]) {
#pragma unroll
        for (int i = 0; i < NX; i += 4) asm volatile("" : "+a"(x[i]), "+a"(x[i + 1]), "+a"(x[i + 2]), "+a"(x[i + 3]));
    };
#pragma unroll
    for (int i = 0; i < 5 * NX; ++i) asm volatile("" : "=a"(xr[i / NX][i % NX]));
    u32x2 flo;
    u32 c[4], slot = 0;
    int sd = GBUF;
    auto sring = [&]() { sd = slot == (GRING - 1) * GBUF ? -(GRING - 1) * GBUF : GBUF; slot += (u32)sd; };
    {
        u32 xo[NT];
#pragma unroll
        for (int tt = 0; tt < NT; ++tt) {
            const int tok = 16 * (tt & 1) + n;
            xo[tt] = (u32)((NT / 2 * w + (tt >> 1)) * 40960 + (g.xb ? q * 512 + tok * 16 : q * 16 + tok * 1280));
        }
        f32x4 acc[12][NT];
        const bool rm = NT == 4 && g.type == T_BL;
        const int sw = (qq >> 1) + 2 * (q & 1);
#pragma unroll
        for (int t = 0; t < 4; ++t)
            c[t] = lds + q * 3072 + qq * 128 + (rm ? ((((t & 2) + (pp >> 1)) ^ sw) << 5) + 16 * (pp & 1) + 8 * ((pp >> 1) ^ (t & 1)) : ((t ^ sw) << 5) + 8 * pp);
        auto rlo = [&](int f) { flo = trread(c[f & 3] + (f / 12) * 12288 + ((f % 12) >> 2) * 1024); };
        auto rhi = [&](int f) {
            const u32x2 hi = trread(c[f & 3] + (f / 12) * 12288 + ((f % 12) >> 2) * 1024 + 512);
            fr[f % (FD + 1)] = u32x4{flo[0], flo[1], hi[0], hi[1]};
        };
        const int xk = uni(g.xb ? 2048 : 64);
        const char *xp = g.x, *xq = xp + xk;
        sfor<0, 4>([&](auto J) {
            if constexpr (J.v < XL) { sfor<0, NX>([&](auto I) { gld16a<0>(xr[J.v][I.v], I.v & 1 ? xq : xp, xo[I.v >> 1]); }); xp += 2 * xk; xq += 2 * xk; }
            wload(J.v);
        });
        waitbar<18 + (XL - 1) * NX>();
        xtie(xr[0]);
#pragma unroll
        for (int i = 0; i < FD; ++i) { rlo(i); rhi(i); }
        char* po = g.out;
#pragma nounroll
        for (int ph = g.np1; ph > 0; --ph) {
            sfor<0, 10>([&](auto J) {
                constexpr int j = J.v;
                u32x4 (&xc)[NX] = xr[j % 5];
                u32x4 (&xn)[NX] = xr[(j + XL) % 5];
                sfor<0, 24>([&](auto I) {
                    constexpr int idx = I.v, kk = idx / 12, T = idx % 12, z = j == 0 && kk == 0, f = (idx + FD) % 24;
                    constexpr int mo = NT == 4 ? (idx < BI && idx % 4 != 2 ? idx - (idx + 1) / 4 : -1) : (idx < BI && idx % 2 == 0 ? idx / 2 : -1);
                    auto mem = [&]() {
                        if constexpr (mo >= NX) gldsm<1024 * (mo - NX - 3)>(wp, wv[mo - NX]);
                        else if constexpr (mo >= 0) gld16a<0>(xn[mo], mo & 1 ? xq : xp, xo[mo >> 1]);
                    };
                    auto misc = [&]() {
                        if constexpr (idx == 1) wring();
                        if constexpr (idx == 13) sring();
                        if constexpr (j == 1 && idx == 22) early_add(g.sig, ph + w);
                        if constexpr (idx >= 15 && idx < BI) { c[idx - 15] += (u32)sd; asm volatile("" : "+v"(c[idx - 15])); }
                        const bool more = ph > 1;
                        if constexpr (j != 5) {
                            if constexpr (idx == 20) wp += wstep;
                        } else {
                            if constexpr (idx == 19) { wp = more ? g.w1b + (size_t)(16 * w) * g.ldw - 2048 : wp2; wstep = more ? wstep : (p2 ? 128 : 0); }
                            if constexpr (idx >= 20 && idx < 23) {
#pragma unroll
                                for (int i = 2 * (idx - 20); i < 2 * (idx - 19); ++i) { wv[i] = more ? wv[i] : (p2 ? v2o(i) : 5120u); asm volatile("" : "+v"(wv[i])); }
                            }
                        }
                        if constexpr (j != 9 - XL) {
                            if constexpr (idx == 21) xp += 2 * xk;
                            if constexpr (idx == 22) xq += 2 * xk;
                        } else if constexpr (idx == 23) {
                            xp = g.x; xq = xp + xk;
#pragma unroll
                            for (int i = 0; i < NT; ++i) { xo[i] = more ? xo[i] : 0u; asm volatile("" : "+v"(xo[i])); }
                        }
                    };
                    if constexpr (idx == BI) { waitbar<6 + (XL - 1) * OPS>(); xtie(xr[(j + 1) % 5]); }
                    if constexpr (idx % 2 == 0) LGKM(6);
                    PIN();
                    const u32x4 a = fr[idx % (FD + 1)];
                    mf1<z>(acc[T][0], a, xc[kk]); PIN();
                    rlo(f);
                    if constexpr (idx == 0) setm0(wdst);
                    if constexpr (NT == 2) rhi(f);
                    PIN();
                    mf1<z>(acc[T][1], a, xc[2 + kk]); PIN();
                    if constexpr (NT == 4) {
                        rhi(f); PIN();
                        mf1<z>(acc[T][2], a, xc[4 + kk]); PIN();
                        mem(); PIN();
                        mf1<z>(acc[T][3], a, xc[6 + kk]); PIN();
                    } else { mem(); PIN(); }
                    misc();
                    PIN();
                });
            });
            asm volatile("s_nop 7\n\ts_nop 1");
            PIN();
#pragma unroll
            for (int tt = 0; tt < NT; ++tt)
#pragma unroll
                for (int ks = 0; ks < 6; ++ks)
                    vf[tt][ks] = u32x4{cvtpk(acc[2 * ks][tt][0], acc[2 * ks][tt][1]), cvtpk(acc[2 * ks][tt][2], acc[2 * ks][tt][3]),
                                       cvtpk(acc[2 * ks + 1][tt][0], acc[2 * ks + 1][tt][1]), cvtpk(acc[2 * ks + 1][tt][2], acc[2 * ks + 1][tt][3])};
            if (g.type == T_QK) {
#pragma unroll
                for (int tt = 0; tt < NT; ++tt)
#pragma unroll
                    for (int ks = 0; ks < 6; ++ks) {
                        gst16(po + (size_t)(NT / 2 * w + (tt >> 1)) * (NH * 12288) + (tt & 1) * 6144 + ks * 1024, lane * 16,
                              u32x4{vf[tt][ks][0], vf[tt][ks][2], vf[tt][ks][1], vf[tt][ks][3]});
                        if (NT == 4 && EPG) asm volatile("s_sleep %0" : : "n"(EPG));
                    }
            } else if (rm) {
                const bool sx = q >= 2;
#pragma unroll
                for (int i = 0; i < 3 * NT; ++i) {
                    const int tp = i / 6, ks = i % 6;
                    u32x4 o[2];
#pragma unroll
                    for (int j = 0; j < 4; ++j) {
                        const u32x4 &v = vf[2 * tp][ks], &u = vf[2 * tp + 1][ks];
                        if (BLS) {
                            const auto r = __builtin_amdgcn_permlane32_swap(v[j], u[j], false, false);
                            o[0][j] = r[0]; o[1][j ^ 2] = r[1];
                        } else { o[0][j] = sx ? v[j ^ 2] : v[j]; o[1][j] = sx ? u[j ^ 2] : u[j]; }
                    }
#pragma unroll
                    for (int j = 0; j < 2; ++j) {
                        gst16(po + (size_t)(2 * w + tp) * g.sV + j * (BLS ? 1024 : 256) + ks * 2048, BLS ? (u32)((q & 1) * 512 + (q >> 1) * 256 + n * 16) : (u32)(q * 512 + n * 16), o[j]);
                        if (EPG) asm volatile("s_sleep %0" : : "n"(EPG));
                    }
                }
            }
            po = g.outb;
            PIN();
        }
    }
    if (p2) {
        constexpr int HI = 2048;
        const int r = 4 * (q & 1) + qq, sw = r >> 1;
#pragma unroll
        for (int t = 0; t < 4; ++t) c[t] = lds + slot + (q >> 1) * 1024 + r * 128 + ((t ^ sw) << 5) + 8 * pp;
        sring();
        u32 so = (u32)((n >> 3) * g.sV + q * g.sQ + (n & 7) * 16);
        char* const o0 = g.out + (size_t)(NT / 2 * w) * g.sP;
        char* const o1 = o0 + g.sP;
        const int sv2 = 2 * g.sV;
        auto rlo = [&](int f) { flo = trread(c[f / 6] + (f % 6) * 4096); };
        auto rhi = [&](int f) {
            const u32x2 hi = trread(c[f / 6] + (f % 6) * 4096 + HI);
            fr[f % (FD + 1)] = u32x4{flo[0], flo[1], hi[0], hi[1]};
        };
        f32x4 a2[2][NT];
        u32 e[NT / 2][4];
        auto cv = [&](f32x4 (&a)[NT], int i) { e[i >> 2][i & 3] = cvtpk(a[i >> 1][2 * (i & 1)], a[i >> 1][2 * (i & 1) + 1]); asm volatile("" : "+v"(e[i >> 2][i & 3])); };
        auto st = [&](int pl) { gst16n(pl ? o1 : o0, so, u32x4{e[pl][0], e[pl][1], e[pl][2], e[pl][3]}); };
        PIN();
#pragma unroll
        for (int i = 0; i < FD; ++i) { rlo(i); rhi(i); }
#pragma unroll
        for (int tt = 0; tt < NT; ++tt) a2[1][tt] = f32x4{0.f, 0.f, 0.f, 0.f};
#pragma nounroll
        for (int t = 0; t < g.tn; ++t) {
            sfor<0, 24>([&](auto I) {
                constexpr int idx = I.v, cb = idx / 6, ks = idx % 6, f = (idx + FD) % 24;
                f32x4 (&ac)[NT] = a2[cb & 1];
                f32x4 (&ap)[NT] = a2[(cb & 1) ^ 1];
                constexpr int mo = cb < 2 ? (ks == 0 ? 3 * cb : (ks >= 4 ? 3 * cb + ks - 3 : -1)) : -1;
                auto mem = [&]() {
                    if constexpr (mo >= 0) gldsm<1024 * (mo - 3)>(wp, wv[mo]);
                    if constexpr (ks == 3 || (NT == 4 && ks == 2)) { if (cb > 0 || t > 0) st(NT == 4 ? ks - 2 : 0); }
                };
                auto misc = [&]() {
                    if constexpr (idx == 12) wring();
                    if constexpr (idx == 23) sring();
                    if constexpr (idx == 16) { wp += t < g.tn - 5 ? 128 : 0; }
                    if constexpr (ks == 0) { c[cb] += (u32)sd; asm volatile("" : "+v"(c[cb])); }
                    if constexpr (ks == 4) { so += (cb > 0 || t > 0) ? (u32)sv2 : 0u; asm volatile("" : "+v"(so)); }
                };
                if constexpr (idx == BI) waitbar<NT == 4 ? 38 : 28>();
                if constexpr (idx % 2 == 0) LGKM(6);
                PIN();
                const u32x4 b = fr[idx % (FD + 1)];
                mf2<ks == 0>(ac[0], vf[0][ks], b); PIN();
                rlo(f);
                if constexpr (idx == 0) setm0(wdst);
                if constexpr (NT == 2) rhi(f);
                if constexpr (NT == 4 && ks >= 1 && ks <= 3) cv(ap, ks == 1 ? 0 : (ks == 2 ? 4 : 7));
                PIN();
                mf2<ks == 0>(ac[1], vf[1][ks], b); PIN();
                if constexpr (NT == 4) {
                    rhi(f);
                    if constexpr (ks == 1 || ks == 2) cv(ap, ks == 1 ? 1 : 5);
                    PIN();
                    mf2<ks == 0>(ac[2], vf[2][ks], b); PIN();
                    mem();
                    if constexpr (ks == 1) { cv(ap, 2); cv(ap, 3); }
                    PIN();
                    mf2<ks == 0>(ac[3], vf[3][ks], b); PIN();
                    if constexpr (ks == 2) cv(ap, 6);
                } else {
                    if constexpr (ks == 1 || ks == 2) { cv(ap, 2 * ks - 2); cv(ap, 2 * ks - 1); }
                    mem(); PIN();
                }
                misc();
                PIN();
            });
            asm volatile("s_nop 7\n\ts_nop 1");
        }
        PIN();
#pragma unroll
        for (int i = 0; i < 2 * NT; ++i) cv(a2[1], i);
#pragma unroll
        for (int pl = 0; pl < NT / 2; ++pl) st(pl);
        asm volatile("s_nop 1");
    }
    if constexpr (NT == 2) asm volatile(".Lkm_g2:"); else asm volatile(".Lkm_g4:");
    sync_ld(y, ws);
    waitvm<0>();
    asm volatile("" : "+v"(y.r), "+v"(y.g), "+v"(y.e));
    sfor<0, 5>([&](auto I) { xtie(xr[I.v]); });
}

struct Ptrs {
    const char *x, *mem0, *wq, *wk, *wv, *wo, *wqkvb, *wob, *gw;
    char* out;
    char* ws;
};

enum { Q_H = 0, Q_VD = 1, Q_KQ = 2, Q_QKVB = 3, Q_BA = 4, Q_BO = 5, Q_PX = 6, Q_OUT = 7 };
DI void run_gemm(const Ptrs& p, int ty, int b, int i, int hf, bool raw, Sync& y, u32 lds, int w) {
    GemmArgs g;
    g.sV = g.sP = g.sQ = 0; g.w2 = p.wo; g.ldw = HD * 2; g.np1 = 1; g.tn = 10;
    g.sig = p.ws + OFF_ST + F_END * 128;
    const char* xf = raw ? p.x : p.ws + OFF_XF;
    g.xb = !raw;
    int s0 = 8 * b;
    if (ty == Q_VD) {
        s0 = b < 2 ? 4 * b : s0 - 8;
        g.type = T_VD; g.w1 = g.w1b = p.wv + i * DH * 2;
        g.w2 = p.wo + (size_t)i * DH * 1280;
        g.out = g.outb = p.ws + OFF_D + (size_t)s0 * D_STRIDE + i * 512;
        g.sV = 16384; g.sP = (int)D_STRIDE; g.sQ = 128;
        if (b == 0) { g.tn = 5; g.w2 += hf * 640; g.out += hf * 40 * 16384; }
    } else if (ty == Q_H) {
        s0 = 0;
        g.type = T_QK; g.w1 = g.w1b = (b ? p.wq : p.wk) + i * DH * 2;
        g.out = g.outb = p.ws + (b ? OFF_QX : OFF_KX) + (size_t)i * 12288;
        b = 0;
    } else if (ty == Q_KQ) {
        s0 = b < 2 ? 4 * b : s0 - 8;
        g.type = T_QK; g.w1 = p.wk + i * DH * 2; g.w1b = p.wq + i * DH * 2;
        g.out = p.ws + OFF_KX + (size_t)(s0 * NH + i) * 12288;
        g.outb = g.out + (OFF_QX - OFF_KX);
        g.np1 = 2;
        g.sig = p.ws + OFF_ST + (F_K + b * 32 + i) * 128;
    } else if (ty == Q_QKVB) {
        g.type = T_BL; g.w1 = p.wqkvb + i * 768; g.w1b = g.w1 + 384; g.ldw = 3840;
        g.sV = 1920 * 64; g.np1 = 2;
        g.out = p.ws + OFF_QKVB + (size_t)s0 * g.sV + i * 48 * 512; g.outb = g.out + 24 * 512;
    } else {
        const int c0 = min(i * DH, DM - DH);
        g.type = T_BL; g.w1 = g.w1b = p.wob + c0 * 2; g.ldw = 1280;
        xf = p.ws + OFF_OB;
        g.sV = 40960;
        g.out = g.outb = p.ws + OFF_BOUT + (size_t)s0 * 40960 + c0 * 64;
    }
    g.x = xf + (size_t)s0 * 40960;
    if (ty <= Q_KQ && b < 2) gemm_task<2>(g, y, p.ws, lds, w);
    else gemm_task<4>(g, y, p.ws, lds, w);
}
DI int pblk(int s) { return s < 8 ? s >> 2 : min(s >> 3, 7) + 1; }

template <int IMM>
DI void gld16c(u32x4& r, const char* sbase, u32 voff) {
    asm volatile("global_load_dwordx4 %0, %1, %2 offset:%3 sc1" : "=v"(r) : "v"(voff), "s"(sbase), "i"(IMM) : "memory");
}
#define XP1 " nt"
template <int IMM>
DI void gld16x(u32x4& r, const char* sbase, u32 voff) {
    asm volatile("global_load_dwordx4 %0, %1, %2 offset:%3 sc1" XP1 : "=v"(r) : "v"(voff), "s"(sbase), "i"(IMM) : "memory");
}
DI void gst4c(char* sbase, u32 voff, u32 v) {
    asm volatile("global_store_dword %0, %1, %2 sc1" : : "v"(voff), "v"(v), "s"(sbase) : "memory");
}
template <int N>
DI void waitv(u32x4& r) { asm volatile("s_waitcnt vmcnt(%1)" : "+v"(r) : "i"(N) : "memory"); }
DI u32 mt() { return (u32)__builtin_amdgcn_s_memtime(); }
DI u32 rt() { return (u32)__builtin_amdgcn_s_memrealtime(); }
DI void spin_to(u32 tgt) { while ((int)(mt() - tgt) < 0) {} }
DI u32 max3u(u32 a, u32 b, u32 c) { return max(max(a, b), c); }
DI u32x4 ldsr16(u32 a) { return *LDSP(u32x4, a); }
DI void clocks(u32& r, u32& m) {
    u64 a, b;
    asm volatile("s_memrealtime %0\n\ts_memtime %1\n\ts_waitcnt lgkmcnt(0)" : "=s"(a), "=s"(b) : : "memory");
    r = (u32)a; m = (u32)b;
}
#define SLATE 20
DI void edge(u32& r, u32& m) {
    u64 a, t[9];
    u32 n = 0;
#define RT(i) "s_memrealtime %" #i "\n\ts_nop 1\n\t"
#define RC(i) "\n\ts_cmp_eq_u64 %" #i ", %1\n\ts_addc_u32 %10, %10, 0"
    asm volatile("s_memtime %0\n\t" RT(1) RT(2) RT(3) RT(4) RT(5) RT(6) RT(7) RT(8) "s_memrealtime %9\n\ts_waitcnt lgkmcnt(0)" RC(2) RC(3) RC(4) RC(5) RC(6) RC(7) RC(8) RC(9)
                 : "=&s"(a), "=&s"(t[0]), "=&s"(t[1]), "=&s"(t[2]), "=&s"(t[3]), "=&s"(t[4]), "=&s"(t[5]), "=&s"(t[6]), "=&s"(t[7]), "=&s"(t[8]), "+s"(n) : : "scc", "memory");
    m = (u32)a + 12 * n;
    r = (u32)t[0] + 4;
}
#define SPB 90
struct Clock {
    u32 r0, m0, r1, m1; int cpt;
    DI void init() { edge(r0, m0); r1 = r0; m1 = m0; cpt = 24 * 64; }
    DI u32 sched(u32& phi, int ofs, int G, int mode) {
        if (mode == 0) edge(r1, m1);
        u32 r = r1, m = m1;
        if (mode == 2) clocks(r, m);
        int rem = (int)(phi + (u32)ofs - r);
        if (__builtin_expect(rem < -SLATE, 0)) { const int k = (-rem / G + 1) * G; phi += (u32)k; rem += k; }
        return m + (u32)((min(rem, 3000) * cpt) >> 6) - SPB;
    }
    DI u32 at(u32 t) { u32 r, m; clocks(r, m); return m + (u32)((min(max((int)(t - r), 0), 3000) * cpt) >> 6); }
    DI void calib() {
        const int dr = (int)(r1 - r0);
        if (dr >= 60) { int c = (int)fminf(fmaxf((float)(int)(m1 - m0) * 64.f * __builtin_amdgcn_rcpf((float)dr), 256.f), 2560.f); asm volatile("" : "+v"(c)); cpt = uni(c); r0 = r1; m0 = m1; }
    }
};
#define VISS 0
DI void follow(u32& phi, int ofs, u32 t, int G) { const int late = (int)(t - phi) - ofs; if (__builtin_expect(late > 0, 0)) phi += (u32)((late / G + 1) * G); }
DI bool unwritten(const char* base, u32 off) {
#pragma nounroll
    for (int i = 0; i < 10; ++i) {
        u32x4 t;
        gld16c<-4096>(t, base + (i + (i & 8)) * 1024, off); waitv<0>(t);
        if (__builtin_amdgcn_ballot_w64(max3u(max(t[0], t[1]), t[2], t[3]) == 0xffffffffu) != 0) return true;
    }
    return false;
}

DI size_t uslot(int sd, int s) { const int r = (s * 43) >> 8; return OFF_U + (u32)uni(s * (int)U_STRIDE + r * (int)((2u << 20) - 6 * U_STRIDE) + ((sd >> (2 + r)) & 1) * 8192); }
DI int uchunk(int v) { return (2 * (v & 3) + ((v >> 2) & 1) + (v & ~7)) * 8192; }
DI size_t mslot(int sd, int s) { return (u32)uni(s * (int)HO_STRIDE + ((sd >> (s >> 5)) & 1) * 8192 + 4096); }
struct ChainCfg { int T, cR, G, dV, fast, s1, T2, cR2, s2, T3, cR3, dU; };
constexpr int TT_OFF = 88 + 48 + 360;
DI void karg_warm() {
    u32 a[13];
    asm volatile("s_load_dword %0, %13, 0\n\t" "s_load_dword %1, %13, 64\n\t" "s_load_dword %2, %13, 128\n\t" "s_load_dword %3, %13, 192\n\t" "s_load_dword %4, %13, 256\n\t" "s_load_dword %5, %13, 320\n\t" "s_load_dword %6, %13, 384\n\t" "s_load_dword %7, %13, 448\n\t" "s_load_dword %8, %13, 512\n\t" "s_load_dword %9, %13, 576\n\t" "s_load_dword %10, %13, 640\n\t" "s_load_dword %11, %13, 704\n\t" "s_load_dword %12, %13, 748\n\t" "s_waitcnt lgkmcnt(0)"
                 : "=&s"(a[0]), "=&s"(a[1]), "=&s"(a[2]), "=&s"(a[3]), "=&s"(a[4]), "=&s"(a[5]), "=&s"(a[6]), "=&s"(a[7]), "=&s"(a[8]), "=&s"(a[9]), "=&s"(a[10]), "=&s"(a[11]), "=&s"(a[12])
                 : "s"(__builtin_amdgcn_kernarg_segment_ptr()) : "memory");
}
DI u32 sched_ld0(int s) {
    u32 r;
    asm volatile("s_load_dword %0, %1, %2 offset:%3\n\ts_waitcnt lgkmcnt(0)" : "=s"(r) : "s"(__builtin_amdgcn_kernarg_segment_ptr()), "s"(4 * s), "n"(TT_OFF) : "memory");
    return r;
}
#define HP 60
#define HQ 96

constexpr int SL_W = 0;
constexpr int SL_IMG = 131072;
constexpr int SL_KX = SL_IMG + 6656;
constexpr int SL_PF = SL_KX + 12288 + 1024;
constexpr int S_LDS = SL_PF + 256;
DI void nap(int t) { const u32 wake = rt() + (u32)t; while ((int)(rt() - wake) < 0) __builtin_amdgcn_s_sleep(4); }
DI void dly(int n) {
    asm volatile(".Ld%=:\n\ts_sub_u32 %0, %0, 1\n\ts_nop 0\n\ts_cbranch_scc0 .Ld%=" : "+s"(n) : : "scc", "memory");
}
DI void pfl(const char* sbase, u32 voff, u32 lds) {
    asm volatile("s_mov_b32 m0, %2\n\ts_nop 3\n\tglobal_load_lds_dword %0, %1" : : "v"(voff), "s"(sbase), "s"(lds + SL_PF) : "memory");
}
DI void score_role(const Ptrs& p, const ChainCfg cfg, int h, u32 lds, int w) {
    const int lane = lanex(), tid = 64 * w + lane;
    const int n = lane & 15, g = lane >> 4, qq = (lane >> 2) & 3, pp = lane & 3;
    u32x32 wa[8], wv[3];
    const u32 wreg = lds + SL_W + (u32)w * 32768;
    const u32 lfa = wreg + lane * 16;
    const char* kxp = p.ws + OFF_KX + (size_t)h * 12288 + w * 3072;
    const u32 kxl = lds + SL_KX;
    char* const fl = p.ws + OFF_ST;
    const u32 kxw = kxl + w * 3072 - (w < 2 ? 16 : 0);
    auto kxdma = [&]() {
        asm volatile("s_mov_b32 m0, %2\n\ts_nop 3\n\tglobal_load_lds_dwordx4 %0, %1 sc1\n\tglobal_load_lds_dwordx4 %0, %1 offset:1024 sc1\n\tglobal_load_lds_dwordx4 %0, %1 offset:2048 sc1"
                     : : "v"(lanex() * 16), "s"(kxp), "s"(kxw) : "memory");
    };
    u32 kfl, phi;
    int sd;
    {
        constexpr int GS = 3104;
        Sync y;
        if (w < 3) {
#pragma unroll
            for (int i = PXT; i < NSEG; ++i) gst16(p.ws + OFF_KM + (i * NH + h) * 3072 + w * 1024, lane * 16, u32x4{~0u, ~0u, ~0u, ~0u});
#pragma unroll
            for (int i = NSEG - 4; i < NSEG; ++i) gst16(p.ws + OFF_PX + i * 81920 + (w < 2 ? h * 2048 + w * 1024 : 65536 + h * 512), w < 2 ? lane * 16 : (lane * 16) & 511, u32x4{~0u, ~0u, ~0u, ~0u});
        }
        auto dvo = [&](int i) { const int ix = i * 64 + lane; return (u32)((ix / 24) * (HD * 2) + (ix % 24) * 16); };
        const u32 tra = lds + (2 * (g >> 1) + (qq >> 1)) * GS + (4 * (g & 1) + (qq & 1)) * 384 + 8 * pp;
        u32x8 pa = u32x8{dvo(0), dvo(1), dvo(2), tra + 96 * w + 32 * (w & 1), tra + 96 * w - 64 * (w & 1), tra + 32 * w, lfa, (u32)(lane & 31) * 8};
        asm volatile("s_nop 4\n\t" "s_add_u32 s42, %21, 0x0\n\t" "s_add_u32 s42, s46, s42\n\t" "s_addc_u32 s43, s47, 0\n\t" "s_add_u32 m0, %18, 0x0\n\t" "s_nop 0\n\t" "global_load_lds_dwordx4 v224, s[42:43]\n\t"
                     "s_add_u32 m0, %18, 0x400\n\t" "s_nop 0\n\t" "global_load_lds_dwordx4 v225, s[42:43]\n\t" "s_add_u32 m0, %18, 0x800\n\t" "s_nop 0\n\t" "global_load_lds_dwordx4 v226, s[42:43]\n\t"
                     "s_add_u32 s42, %21, 0x18000\n\t" "s_add_u32 s42, s46, s42\n\t" "s_addc_u32 s43, s47, 0\n\t" "s_add_u32 m0, %18, 0xc20\n\t" "s_nop 0\n\t" "global_load_lds_dwordx4 v224, s[42:43]\n\t"
                     "s_add_u32 m0, %18, 0x1020\n\t" "s_nop 0\n\t" "global_load_lds_dwordx4 v225, s[42:43]\n\t" "s_add_u32 m0, %18, 0x1420\n\t" "s_nop 0\n\t" "global_load_lds_dwordx4 v226, s[42:43]\n\t"
                     "s_sub_u32 s42, 0xc0000, %21\n\t" "s_add_u32 s42, s46, s42\n\t" "s_addc_u32 s43, s47, 0\n\t" "s_add_u32 m0, %18, 0x6100\n\t" "s_nop 0\n\t" "global_load_lds_dwordx4 v224, s[42:43]\n\t"
                     "s_add_u32 m0, %18, 0x6500\n\t" "s_nop 0\n\t" "global_load_lds_dwordx4 v225, s[42:43]\n\t" "s_add_u32 m0, %18, 0x6900\n\t" "s_nop 0\n\t" "global_load_lds_dwordx4 v226, s[42:43]\n\t"
                     "s_sub_u32 s42, 0xd8000, %21\n\t" "s_add_u32 s42, s46, s42\n\t" "s_addc_u32 s43, s47, 0\n\t" "s_add_u32 m0, %18, 0x6d20\n\t" "s_nop 0\n\t" "global_load_lds_dwordx4 v224, s[42:43]\n\t"
                     "s_add_u32 m0, %18, 0x7120\n\t" "s_nop 0\n\t" "global_load_lds_dwordx4 v225, s[42:43]\n\t" "s_add_u32 m0, %18, 0x7520\n\t" "s_nop 0\n\t" "global_load_lds_dwordx4 v226, s[42:43]\n\t"
                     "s_add_u32 s42, %21, 0x180000\n\t" "s_add_u32 s42, s46, s42\n\t" "s_addc_u32 s43, s47, 0\n\t" "s_add_u32 m0, %18, 0xc200\n\t" "s_nop 0\n\t" "global_load_lds_dwordx4 v224, s[42:43]\n\t"
                     "s_add_u32 m0, %18, 0xc600\n\t" "s_nop 0\n\t" "global_load_lds_dwordx4 v225, s[42:43]\n\t" "s_add_u32 m0, %18, 0xca00\n\t" "s_nop 0\n\t" "global_load_lds_dwordx4 v226, s[42:43]\n\t"
                     "s_add_u32 s42, %21, 0x198000\n\t" "s_add_u32 s42, s46, s42\n\t" "s_addc_u32 s43, s47, 0\n\t" "s_add_u32 m0, %18, 0xce20\n\t" "s_nop 0\n\t" "global_load_lds_dwordx4 v224, s[42:43]\n\t"
                     "s_add_u32 m0, %18, 0xd220\n\t" "s_nop 0\n\t" "global_load_lds_dwordx4 v225, s[42:43]\n\t" "s_add_u32 m0, %18, 0xd620\n\t" "s_nop 0\n\t" "global_load_lds_dwordx4 v226, s[42:43]\n\t"
                     "s_sub_u32 s42, 0x240000, %21\n\t" "s_add_u32 s42, s46, s42\n\t" "s_addc_u32 s43, s47, 0\n\t" "s_add_u32 m0, %18, 0x12300\n\t" "s_nop 0\n\t" "global_load_lds_dwordx4 v224, s[42:43]\n\t"
                     "s_add_u32 m0, %18, 0x12700\n\t" "s_nop 0\n\t" "global_load_lds_dwordx4 v225, s[42:43]\n\t" "s_add_u32 m0, %18, 0x12b00\n\t" "s_nop 0\n\t" "global_load_lds_dwordx4 v226, s[42:43]\n\t"
                     "s_sub_u32 s42, 0x258000, %21\n\t" "s_add_u32 s42, s46, s42\n\t" "s_addc_u32 s43, s47, 0\n\t" "s_add_u32 m0, %18, 0x12f20\n\t" "s_nop 0\n\t" "global_load_lds_dwordx4 v224, s[42:43]\n\t"
                     "s_add_u32 m0, %18, 0x13320\n\t" "s_nop 0\n\t" "global_load_lds_dwordx4 v225, s[42:43]\n\t" "s_add_u32 m0, %18, 0x13720\n\t" "s_nop 0\n\t" "global_load_lds_dwordx4 v226, s[42:43]\n\t"
                     "s_waitcnt vmcnt(12) lgkmcnt(0)\n\t" "s_barrier\n\t" "s_add_u32 s42, %21, 0x300000\n\t" "s_add_u32 s42, s46, s42\n\t" "s_addc_u32 s43, s47, 0\n\t" "s_add_u32 m0, %18, 0x18400\n\t" "s_nop 0\n\t"
                     "global_load_lds_dwordx4 v224, s[42:43]\n\t" "s_add_u32 m0, %18, 0x18800\n\t" "s_nop 0\n\t" "global_load_lds_dwordx4 v225, s[42:43]\n\t" "s_add_u32 m0, %18, 0x18c00\n\t" "s_nop 0\n\t"
                     "global_load_lds_dwordx4 v226, s[42:43]\n\t" "s_add_u32 s42, %21, 0x318000\n\t" "s_add_u32 s42, s46, s42\n\t" "s_addc_u32 s43, s47, 0\n\t" "s_add_u32 m0, %18, 0x19020\n\t" "s_nop 0\n\t"
                     "global_load_lds_dwordx4 v224, s[42:43]\n\t" "s_add_u32 m0, %18, 0x19420\n\t" "s_nop 0\n\t" "global_load_lds_dwordx4 v225, s[42:43]\n\t" "s_add_u32 m0, %18, 0x19820\n\t" "s_nop 0\n\t"
                     "global_load_lds_dwordx4 v226, s[42:43]\n\t" "s_sub_u32 s42, 0x3c0000, %21\n\t" "s_add_u32 s42, s46, s42\n\t" "s_addc_u32 s43, s47, 0\n\t" "s_add_u32 m0, %18, 0x1e500\n\t" "s_nop 0\n\t"
                     "global_load_lds_dwordx4 v224, s[42:43]\n\t" "s_add_u32 m0, %18, 0x1e900\n\t" "s_nop 0\n\t" "global_load_lds_dwordx4 v225, s[42:43]\n\t" "s_add_u32 m0, %18, 0x1ed00\n\t" "s_nop 0\n\t"
                     "global_load_lds_dwordx4 v226, s[42:43]\n\t" "s_sub_u32 s42, 0x3d8000, %21\n\t" "s_add_u32 s42, s46, s42\n\t" "s_addc_u32 s43, s47, 0\n\t" "s_add_u32 m0, %18, 0x1f120\n\t" "s_nop 0\n\t"
                     "global_load_lds_dwordx4 v224, s[42:43]\n\t" "s_add_u32 m0, %18, 0x1f520\n\t" "s_nop 0\n\t" "global_load_lds_dwordx4 v225, s[42:43]\n\t" "s_add_u32 m0, %18, 0x1f920\n\t" "s_nop 0\n\t"
                     "global_load_lds_dwordx4 v226, s[42:43]\n\t" "v_add_u32 v232, 0x0, v229\n\t" "ds_read_b64_tr_b16 v[8:9], v232\n\t" "ds_read_b64_tr_b16 v[10:11], v232 offset:768\n\t"
                     "ds_read_b64_tr_b16 v[12:13], v232 offset:12416\n\t" "ds_read_b64_tr_b16 v[14:15], v232 offset:13184\n\t" "ds_read_b64_tr_b16 a[96:97], v232 offset:128\n\t"
                     "ds_read_b64_tr_b16 a[98:99], v232 offset:896\n\t" "ds_read_b64_tr_b16 a[100:101], v232 offset:12544\n\t" "ds_read_b64_tr_b16 a[102:103], v232 offset:13312\n\t"
                     "ds_read_b64_tr_b16 a[176:177], v232 offset:256\n\t" "ds_read_b64_tr_b16 a[178:179], v232 offset:1024\n\t" "ds_read_b64_tr_b16 a[180:181], v232 offset:12672\n\t"
                     "ds_read_b64_tr_b16 a[182:183], v232 offset:13440\n\t" "ds_read_b64_tr_b16 v[40:41], v232 offset:24832\n\t" "ds_read_b64_tr_b16 v[42:43], v232 offset:25600\n\t"
                     "ds_read_b64_tr_b16 v[44:45], v232 offset:37248\n\t" "ds_read_b64_tr_b16 v[46:47], v232 offset:38016\n\t" "ds_read_b64_tr_b16 a[112:113], v232 offset:24960\n\t"
                     "ds_read_b64_tr_b16 a[114:115], v232 offset:25728\n\t" "ds_read_b64_tr_b16 a[116:117], v232 offset:37376\n\t" "ds_read_b64_tr_b16 a[118:119], v232 offset:38144\n\t"
                     "ds_read_b64_tr_b16 a[192:193], v232 offset:25088\n\t" "ds_read_b64_tr_b16 a[194:195], v232 offset:25856\n\t" "ds_read_b64_tr_b16 a[196:197], v232 offset:37504\n\t"
                     "ds_read_b64_tr_b16 a[198:199], v232 offset:38272\n\t" "s_waitcnt vmcnt(12) lgkmcnt(0)\n\t" "s_barrier\n\t" "s_add_u32 s42, %21, 0x480000\n\t" "s_add_u32 s42, s46, s42\n\t" "s_addc_u32 s43, s47, 0\n\t"
                     "s_add_u32 m0, %18, 0x0\n\t" "s_nop 0\n\t" "global_load_lds_dwordx4 v224, s[42:43]\n\t" "s_add_u32 m0, %18, 0x400\n\t" "s_nop 0\n\t" "global_load_lds_dwordx4 v225, s[42:43]\n\t"
                     "s_add_u32 m0, %18, 0x800\n\t" "s_nop 0\n\t" "global_load_lds_dwordx4 v226, s[42:43]\n\t" "s_add_u32 s42, %21, 0x498000\n\t" "s_add_u32 s42, s46, s42\n\t" "s_addc_u32 s43, s47, 0\n\t"
                     "s_add_u32 m0, %18, 0xc20\n\t" "s_nop 0\n\t" "global_load_lds_dwordx4 v224, s[42:43]\n\t" "s_add_u32 m0, %18, 0x1020\n\t" "s_nop 0\n\t" "global_load_lds_dwordx4 v225, s[42:43]\n\t"
                     "s_add_u32 m0, %18, 0x1420\n\t" "s_nop 0\n\t" "global_load_lds_dwordx4 v226, s[42:43]\n\t" "s_sub_u32 s42, 0x540000, %21\n\t" "s_add_u32 s42, s46, s42\n\t" "s_addc_u32 s43, s47, 0\n\t"
                     "s_add_u32 m0, %18, 0x6100\n\t" "s_nop 0\n\t" "global_load_lds_dwordx4 v224, s[42:43]\n\t" "s_add_u32 m0, %18, 0x6500\n\t" "s_nop 0\n\t" "global_load_lds_dwordx4 v225, s[42:43]\n\t"
                     "s_add_u32 m0, %18, 0x6900\n\t" "s_nop 0\n\t" "global_load_lds_dwordx4 v226, s[42:43]\n\t" "s_sub_u32 s42, 0x558000, %21\n\t" "s_add_u32 s42, s46, s42\n\t" "s_addc_u32 s43, s47, 0\n\t"
                     "s_add_u32 m0, %18, 0x6d20\n\t" "s_nop 0\n\t" "global_load_lds_dwordx4 v224, s[42:43]\n\t" "s_add_u32 m0, %18, 0x7120\n\t" "s_nop 0\n\t" "global_load_lds_dwordx4 v225, s[42:43]\n\t"
                     "s_add_u32 m0, %18, 0x7520\n\t" "s_nop 0\n\t" "global_load_lds_dwordx4 v226, s[42:43]\n\t" "v_add_u32 v232, 0xc200, v229\n\t" "ds_read_b64_tr_b16 v[24:25], v232\n\t"
                     "ds_read_b64_tr_b16 v[26:27], v232 offset:768\n\t" "ds_read_b64_tr_b16 v[28:29], v232 offset:12416\n\t" "ds_read_b64_tr_b16 v[30:31], v232 offset:13184\n\t"
                     "ds_read_b64_tr_b16 a[104:105], v232 offset:128\n\t" "ds_read_b64_tr_b16 a[106:107], v232 offset:896\n\t" "ds_read_b64_tr_b16 a[108:109], v232 offset:12544\n\t"
                     "ds_read_b64_tr_b16 a[110:111], v232 offset:13312\n\t" "ds_read_b64_tr_b16 a[184:185], v232 offset:256\n\t" "ds_read_b64_tr_b16 a[186:187], v232 offset:1024\n\t"
                     "ds_read_b64_tr_b16 a[188:189], v232 offset:12672\n\t" "ds_read_b64_tr_b16 a[190:191], v232 offset:13440\n\t" "ds_read_b64_tr_b16 v[56:57], v232 offset:24832\n\t"
                     "ds_read_b64_tr_b16 v[58:59], v232 offset:25600\n\t" "ds_read_b64_tr_b16 v[60:61], v232 offset:37248\n\t" "ds_read_b64_tr_b16 v[62:63], v232 offset:38016\n\t"
                     "ds_read_b64_tr_b16 a[120:121], v232 offset:24960\n\t" "ds_read_b64_tr_b16 a[122:123], v232 offset:25728\n\t" "ds_read_b64_tr_b16 a[124:125], v232 offset:37376\n\t"
                     "ds_read_b64_tr_b16 a[126:127], v232 offset:38144\n\t" "ds_read_b64_tr_b16 a[200:201], v232 offset:25088\n\t" "ds_read_b64_tr_b16 a[202:203], v232 offset:25856\n\t"
                     "ds_read_b64_tr_b16 a[204:205], v232 offset:37504\n\t" "ds_read_b64_tr_b16 a[206:207], v232 offset:38272\n\t" "s_waitcnt vmcnt(12) lgkmcnt(0)\n\t" "s_barrier\n\t" "s_add_u32 s42, %21, 0x600000\n\t"
                     "s_add_u32 s42, s46, s42\n\t" "s_addc_u32 s43, s47, 0\n\t" "s_add_u32 m0, %18, 0xc200\n\t" "s_nop 0\n\t" "global_load_lds_dwordx4 v224, s[42:43]\n\t" "s_add_u32 m0, %18, 0xc600\n\t" "s_nop 0\n\t"
                     "global_load_lds_dwordx4 v225, s[42:43]\n\t" "s_add_u32 m0, %18, 0xca00\n\t" "s_nop 0\n\t" "global_load_lds_dwordx4 v226, s[42:43]\n\t" "s_add_u32 s42, %21, 0x618000\n\t" "s_add_u32 s42, s46, s42\n\t"
                     "s_addc_u32 s43, s47, 0\n\t" "s_add_u32 m0, %18, 0xce20\n\t" "s_nop 0\n\t" "global_load_lds_dwordx4 v224, s[42:43]\n\t" "s_add_u32 m0, %18, 0xd220\n\t" "s_nop 0\n\t"
                     "global_load_lds_dwordx4 v225, s[42:43]\n\t" "s_add_u32 m0, %18, 0xd620\n\t" "s_nop 0\n\t" "global_load_lds_dwordx4 v226, s[42:43]\n\t" "s_sub_u32 s42, 0x6c0000, %21\n\t" "s_add_u32 s42, s46, s42\n\t"
                     "s_addc_u32 s43, s47, 0\n\t" "s_add_u32 m0, %18, 0x12300\n\t" "s_nop 0\n\t" "global_load_lds_dwordx4 v224, s[42:43]\n\t" "s_add_u32 m0, %18, 0x12700\n\t" "s_nop 0\n\t"
                     "global_load_lds_dwordx4 v225, s[42:43]\n\t" "s_add_u32 m0, %18, 0x12b00\n\t" "s_nop 0\n\t" "global_load_lds_dwordx4 v226, s[42:43]\n\t" "s_sub_u32 s42, 0x6d8000, %21\n\t" "s_add_u32 s42, s46, s42\n\t"
                     "s_addc_u32 s43, s47, 0\n\t" "s_add_u32 m0, %18, 0x12f20\n\t" "s_nop 0\n\t" "global_load_lds_dwordx4 v224, s[42:43]\n\t" "s_add_u32 m0, %18, 0x13320\n\t" "s_nop 0\n\t"
                     "global_load_lds_dwordx4 v225, s[42:43]\n\t" "s_add_u32 m0, %18, 0x13720\n\t" "s_nop 0\n\t" "global_load_lds_dwordx4 v226, s[42:43]\n\t" "v_add_u32 v232, 0x18400, v229\n\t"
                     "ds_read_b64_tr_b16 v[72:73], v232\n\t" "ds_read_b64_tr_b16 v[74:75], v232 offset:768\n\t" "ds_read_b64_tr_b16 v[76:77], v232 offset:12416\n\t" "ds_read_b64_tr_b16 v[78:79], v232 offset:13184\n\t"
                     "ds_read_b64_tr_b16 a[128:129], v232 offset:128\n\t" "ds_read_b64_tr_b16 a[130:131], v232 offset:896\n\t" "ds_read_b64_tr_b16 a[132:133], v232 offset:12544\n\t"
                     "ds_read_b64_tr_b16 a[134:135], v232 offset:13312\n\t" "ds_read_b64_tr_b16 a[208:209], v232 offset:256\n\t" "ds_read_b64_tr_b16 a[210:211], v232 offset:1024\n\t"
                     "ds_read_b64_tr_b16 a[212:213], v232 offset:12672\n\t" "ds_read_b64_tr_b16 a[214:215], v232 offset:13440\n\t" "ds_read_b64_tr_b16 v[88:89], v232 offset:24832\n\t"
                     "ds_read_b64_tr_b16 v[90:91], v232 offset:25600\n\t" "ds_read_b64_tr_b16 v[92:93], v232 offset:37248\n\t" "ds_read_b64_tr_b16 v[94:95], v232 offset:38016\n\t"
                     "ds_read_b64_tr_b16 a[136:137], v232 offset:24960\n\t" "ds_read_b64_tr_b16 a[138:139], v232 offset:25728\n\t" "ds_read_b64_tr_b16 a[140:141], v232 offset:37376\n\t"
                     "ds_read_b64_tr_b16 a[142:143], v232 offset:38144\n\t" "ds_read_b64_tr_b16 a[216:217], v232 offset:25088\n\t" "ds_read_b64_tr_b16 a[218:219], v232 offset:25856\n\t"
                     "ds_read_b64_tr_b16 a[220:221], v232 offset:37504\n\t" "ds_read_b64_tr_b16 a[222:223], v232 offset:38272\n\t" "s_waitcnt vmcnt(12) lgkmcnt(0)\n\t" "s_barrier\n\t" "s_add_u32 s42, %21, 0x0\n\t"
                     "s_add_u32 s42, s44, s42\n\t" "s_addc_u32 s43, s45, 0\n\t" "s_add_u32 m0, %18, 0x18400\n\t" "s_nop 0\n\t" "global_load_lds_dwordx4 v224, s[42:43]\n\t" "s_add_u32 m0, %18, 0x18800\n\t" "s_nop 0\n\t"
                     "global_load_lds_dwordx4 v225, s[42:43]\n\t" "s_add_u32 m0, %18, 0x18c00\n\t" "s_nop 0\n\t" "global_load_lds_dwordx4 v226, s[42:43]\n\t" "s_add_u32 s42, %21, 0x18000\n\t" "s_add_u32 s42, s44, s42\n\t"
                     "s_addc_u32 s43, s45, 0\n\t" "s_add_u32 m0, %18, 0x19020\n\t" "s_nop 0\n\t" "global_load_lds_dwordx4 v224, s[42:43]\n\t" "s_add_u32 m0, %18, 0x19420\n\t" "s_nop 0\n\t"
                     "global_load_lds_dwordx4 v225, s[42:43]\n\t" "s_add_u32 m0, %18, 0x19820\n\t" "s_nop 0\n\t" "global_load_lds_dwordx4 v226, s[42:43]\n\t" "s_sub_u32 s42, 0xc0000, %21\n\t" "s_add_u32 s42, s44, s42\n\t"
                     "s_addc_u32 s43, s45, 0\n\t" "s_add_u32 m0, %18, 0x1e500\n\t" "s_nop 0\n\t" "global_load_lds_dwordx4 v224, s[42:43]\n\t" "s_add_u32 m0, %18, 0x1e900\n\t" "s_nop 0\n\t"
                     "global_load_lds_dwordx4 v225, s[42:43]\n\t" "s_add_u32 m0, %18, 0x1ed00\n\t" "s_nop 0\n\t" "global_load_lds_dwordx4 v226, s[42:43]\n\t" "s_sub_u32 s42, 0xd8000, %21\n\t" "s_add_u32 s42, s44, s42\n\t"
                     "s_addc_u32 s43, s45, 0\n\t" "s_add_u32 m0, %18, 0x1f120\n\t" "s_nop 0\n\t" "global_load_lds_dwordx4 v224, s[42:43]\n\t" "s_add_u32 m0, %18, 0x1f520\n\t" "s_nop 0\n\t"
                     "global_load_lds_dwordx4 v225, s[42:43]\n\t" "s_add_u32 m0, %18, 0x1f920\n\t" "s_nop 0\n\t" "global_load_lds_dwordx4 v226, s[42:43]\n\t" "v_add_u32 v232, 0x0, v229\n\t"
                     "ds_read_b64_tr_b16 v[104:105], v232\n\t" "ds_read_b64_tr_b16 v[106:107], v232 offset:768\n\t" "ds_read_b64_tr_b16 v[108:109], v232 offset:12416\n\t"
                     "ds_read_b64_tr_b16 v[110:111], v232 offset:13184\n\t" "ds_read_b64_tr_b16 a[144:145], v232 offset:128\n\t" "ds_read_b64_tr_b16 a[146:147], v232 offset:896\n\t"
                     "ds_read_b64_tr_b16 a[148:149], v232 offset:12544\n\t" "ds_read_b64_tr_b16 a[150:151], v232 offset:13312\n\t" "ds_read_b64_tr_b16 a[224:225], v232 offset:256\n\t"
                     "ds_read_b64_tr_b16 a[226:227], v232 offset:1024\n\t" "ds_read_b64_tr_b16 a[228:229], v232 offset:12672\n\t" "ds_read_b64_tr_b16 a[230:231], v232 offset:13440\n\t"
                     "ds_read_b64_tr_b16 v[120:121], v232 offset:24832\n\t" "ds_read_b64_tr_b16 v[122:123], v232 offset:25600\n\t" "ds_read_b64_tr_b16 v[124:125], v232 offset:37248\n\t"
                     "ds_read_b64_tr_b16 v[126:127], v232 offset:38016\n\t" "ds_read_b64_tr_b16 a[152:153], v232 offset:24960\n\t" "ds_read_b64_tr_b16 a[154:155], v232 offset:25728\n\t"
                     "ds_read_b64_tr_b16 a[156:157], v232 offset:37376\n\t" "ds_read_b64_tr_b16 a[158:159], v232 offset:38144\n\t" "ds_read_b64_tr_b16 a[232:233], v232 offset:25088\n\t"
                     "ds_read_b64_tr_b16 a[234:235], v232 offset:25856\n\t" "ds_read_b64_tr_b16 a[236:237], v232 offset:37504\n\t" "ds_read_b64_tr_b16 a[238:239], v232 offset:38272\n\t" "s_waitcnt vmcnt(12) lgkmcnt(0)\n\t"
                     "s_barrier\n\t" "s_add_u32 s42, %21, 0x180000\n\t" "s_add_u32 s42, s44, s42\n\t" "s_addc_u32 s43, s45, 0\n\t" "s_add_u32 m0, %18, 0x0\n\t" "s_nop 0\n\t" "global_load_lds_dwordx4 v224, s[42:43]\n\t"
                     "s_add_u32 m0, %18, 0x400\n\t" "s_nop 0\n\t" "global_load_lds_dwordx4 v225, s[42:43]\n\t" "s_add_u32 m0, %18, 0x800\n\t" "s_nop 0\n\t" "global_load_lds_dwordx4 v226, s[42:43]\n\t"
                     "s_add_u32 s42, %21, 0x198000\n\t" "s_add_u32 s42, s44, s42\n\t" "s_addc_u32 s43, s45, 0\n\t" "s_add_u32 m0, %18, 0xc20\n\t" "s_nop 0\n\t" "global_load_lds_dwordx4 v224, s[42:43]\n\t"
                     "s_add_u32 m0, %18, 0x1020\n\t" "s_nop 0\n\t" "global_load_lds_dwordx4 v225, s[42:43]\n\t" "s_add_u32 m0, %18, 0x1420\n\t" "s_nop 0\n\t" "global_load_lds_dwordx4 v226, s[42:43]\n\t"
                     "s_sub_u32 s42, 0x240000, %21\n\t" "s_add_u32 s42, s44, s42\n\t" "s_addc_u32 s43, s45, 0\n\t" "s_add_u32 m0, %18, 0x6100\n\t" "s_nop 0\n\t" "global_load_lds_dwordx4 v224, s[42:43]\n\t"
                     "s_add_u32 m0, %18, 0x6500\n\t" "s_nop 0\n\t" "global_load_lds_dwordx4 v225, s[42:43]\n\t" "s_add_u32 m0, %18, 0x6900\n\t" "s_nop 0\n\t" "global_load_lds_dwordx4 v226, s[42:43]\n\t"
                     "s_sub_u32 s42, 0x258000, %21\n\t" "s_add_u32 s42, s44, s42\n\t" "s_addc_u32 s43, s45, 0\n\t" "s_add_u32 m0, %18, 0x6d20\n\t" "s_nop 0\n\t" "global_load_lds_dwordx4 v224, s[42:43]\n\t"
                     "s_add_u32 m0, %18, 0x7120\n\t" "s_nop 0\n\t" "global_load_lds_dwordx4 v225, s[42:43]\n\t" "s_add_u32 m0, %18, 0x7520\n\t" "s_nop 0\n\t" "global_load_lds_dwordx4 v226, s[42:43]\n\t"
                     "v_add_u32 v232, 0xc200, v229\n\t" "ds_read_b64_tr_b16 a[56:57], v232\n\t" "ds_read_b64_tr_b16 a[58:59], v232 offset:768\n\t" "ds_read_b64_tr_b16 a[60:61], v232 offset:12416\n\t"
                     "ds_read_b64_tr_b16 a[62:63], v232 offset:13184\n\t" "ds_read_b64_tr_b16 a[160:161], v232 offset:128\n\t" "ds_read_b64_tr_b16 a[162:163], v232 offset:896\n\t"
                     "ds_read_b64_tr_b16 a[164:165], v232 offset:12544\n\t" "ds_read_b64_tr_b16 a[166:167], v232 offset:13312\n\t" "ds_read_b64_tr_b16 a[240:241], v232 offset:256\n\t"
                     "ds_read_b64_tr_b16 a[242:243], v232 offset:1024\n\t" "ds_read_b64_tr_b16 a[244:245], v232 offset:12672\n\t" "ds_read_b64_tr_b16 a[246:247], v232 offset:13440\n\t"
                     "ds_read_b64_tr_b16 a[88:89], v232 offset:24832\n\t" "ds_read_b64_tr_b16 a[90:91], v232 offset:25600\n\t" "ds_read_b64_tr_b16 a[92:93], v232 offset:37248\n\t"
                     "ds_read_b64_tr_b16 a[94:95], v232 offset:38016\n\t" "ds_read_b64_tr_b16 a[168:169], v232 offset:24960\n\t" "ds_read_b64_tr_b16 a[170:171], v232 offset:25728\n\t"
                     "ds_read_b64_tr_b16 a[172:173], v232 offset:37376\n\t" "ds_read_b64_tr_b16 a[174:175], v232 offset:38144\n\t" "ds_read_b64_tr_b16 a[248:249], v232 offset:25088\n\t"
                     "ds_read_b64_tr_b16 a[250:251], v232 offset:25856\n\t" "ds_read_b64_tr_b16 a[252:253], v232 offset:37504\n\t" "ds_read_b64_tr_b16 a[254:255], v232 offset:38272\n\t" "s_waitcnt vmcnt(12) lgkmcnt(0)\n\t"
                     "s_barrier\n\t" "s_add_u32 s42, %21, 0x300000\n\t" "s_add_u32 s42, s44, s42\n\t" "s_addc_u32 s43, s45, 0\n\t" "s_add_u32 m0, %18, 0xc200\n\t" "s_nop 0\n\t" "global_load_lds_dwordx4 v224, s[42:43]\n\t"
                     "s_add_u32 m0, %18, 0xc600\n\t" "s_nop 0\n\t" "global_load_lds_dwordx4 v225, s[42:43]\n\t" "s_add_u32 m0, %18, 0xca00\n\t" "s_nop 0\n\t" "global_load_lds_dwordx4 v226, s[42:43]\n\t"
                     "s_add_u32 s42, %21, 0x318000\n\t" "s_add_u32 s42, s44, s42\n\t" "s_addc_u32 s43, s45, 0\n\t" "s_add_u32 m0, %18, 0xce20\n\t" "s_nop 0\n\t" "global_load_lds_dwordx4 v224, s[42:43]\n\t"
                     "s_add_u32 m0, %18, 0xd220\n\t" "s_nop 0\n\t" "global_load_lds_dwordx4 v225, s[42:43]\n\t" "s_add_u32 m0, %18, 0xd620\n\t" "s_nop 0\n\t" "global_load_lds_dwordx4 v226, s[42:43]\n\t"
                     "s_sub_u32 s42, 0x3c0000, %21\n\t" "s_add_u32 s42, s44, s42\n\t" "s_addc_u32 s43, s45, 0\n\t" "s_add_u32 m0, %18, 0x12300\n\t" "s_nop 0\n\t" "global_load_lds_dwordx4 v224, s[42:43]\n\t"
                     "s_add_u32 m0, %18, 0x12700\n\t" "s_nop 0\n\t" "global_load_lds_dwordx4 v225, s[42:43]\n\t" "s_add_u32 m0, %18, 0x12b00\n\t" "s_nop 0\n\t" "global_load_lds_dwordx4 v226, s[42:43]\n\t"
                     "s_sub_u32 s42, 0x3d8000, %21\n\t" "s_add_u32 s42, s44, s42\n\t" "s_addc_u32 s43, s45, 0\n\t" "s_add_u32 m0, %18, 0x12f20\n\t" "s_nop 0\n\t" "global_load_lds_dwordx4 v224, s[42:43]\n\t"
                     "s_add_u32 m0, %18, 0x13320\n\t" "s_nop 0\n\t" "global_load_lds_dwordx4 v225, s[42:43]\n\t" "s_add_u32 m0, %18, 0x13720\n\t" "s_nop 0\n\t" "global_load_lds_dwordx4 v226, s[42:43]\n\t"
                     "v_add_u32 v232, 0x18400, v227\n\t" "v_add_u32 v233, 0x18400, v228\n\t" "ds_read_b64_tr_b16 v[128:129], v232\n\t" "ds_read_b64_tr_b16 v[130:131], v232 offset:768\n\t"
                     "ds_read_b64_tr_b16 v[132:133], v232 offset:12416\n\t" "ds_read_b64_tr_b16 v[134:135], v232 offset:13184\n\t" "ds_read_b64_tr_b16 v[0:1], v232 offset:32\n\t"
                     "ds_read_b64_tr_b16 v[2:3], v232 offset:800\n\t" "ds_read_b64_tr_b16 v[4:5], v232 offset:12448\n\t" "ds_read_b64_tr_b16 v[6:7], v232 offset:13216\n\t"
                     "ds_read_b64_tr_b16 v[136:137], v233 offset:64\n\t" "ds_read_b64_tr_b16 v[138:139], v233 offset:832\n\t" "ds_read_b64_tr_b16 v[140:141], v233 offset:12480\n\t"
                     "ds_read_b64_tr_b16 v[142:143], v233 offset:13248\n\t" "ds_read_b64_tr_b16 v[160:161], v232 offset:24832\n\t" "ds_read_b64_tr_b16 v[162:163], v232 offset:25600\n\t"
                     "ds_read_b64_tr_b16 v[164:165], v232 offset:37248\n\t" "ds_read_b64_tr_b16 v[166:167], v232 offset:38016\n\t" "ds_read_b64_tr_b16 v[32:33], v232 offset:24864\n\t"
                     "ds_read_b64_tr_b16 v[34:35], v232 offset:25632\n\t" "ds_read_b64_tr_b16 v[36:37], v232 offset:37280\n\t" "ds_read_b64_tr_b16 v[38:39], v232 offset:38048\n\t"
                     "ds_read_b64_tr_b16 v[168:169], v233 offset:24896\n\t" "ds_read_b64_tr_b16 v[170:171], v233 offset:25664\n\t" "ds_read_b64_tr_b16 v[172:173], v233 offset:37312\n\t"
                     "ds_read_b64_tr_b16 v[174:175], v233 offset:38080\n\t" "s_waitcnt vmcnt(12) lgkmcnt(0)\n\t" "s_barrier\n\t" "v_mov_b32 %14, 0\n\t" "global_load_dwordx2 %11, v231, %19 sc1\n\t"
                     "global_load_dwordx2 %12, %14, %19 offset:512 sc1\n\t" "global_load_dwordx2 %13, %14, %19 offset:1024 sc1\n\t" "s_add_u32 s42, %21, 0x480000\n\t" "s_add_u32 s42, s44, s42\n\t"
                     "s_addc_u32 s43, s45, 0\n\t" "s_add_u32 m0, %18, 0x18400\n\t" "s_nop 0\n\t" "global_load_lds_dwordx4 v224, s[42:43]\n\t" "s_add_u32 m0, %18, 0x18800\n\t" "s_nop 0\n\t"
                     "global_load_lds_dwordx4 v225, s[42:43]\n\t" "s_add_u32 m0, %18, 0x18c00\n\t" "s_nop 0\n\t" "global_load_lds_dwordx4 v226, s[42:43]\n\t" "s_add_u32 s42, %21, 0x498000\n\t" "s_add_u32 s42, s44, s42\n\t"
                     "s_addc_u32 s43, s45, 0\n\t" "s_add_u32 m0, %18, 0x19020\n\t" "s_nop 0\n\t" "global_load_lds_dwordx4 v224, s[42:43]\n\t" "s_add_u32 m0, %18, 0x19420\n\t" "s_nop 0\n\t"
                     "global_load_lds_dwordx4 v225, s[42:43]\n\t" "s_add_u32 m0, %18, 0x19820\n\t" "s_nop 0\n\t" "global_load_lds_dwordx4 v226, s[42:43]\n\t" "s_sub_u32 s42, 0x540000, %21\n\t" "s_add_u32 s42, s44, s42\n\t"
                     "s_addc_u32 s43, s45, 0\n\t" "s_add_u32 m0, %18, 0x1e500\n\t" "s_nop 0\n\t" "global_load_lds_dwordx4 v224, s[42:43]\n\t" "s_add_u32 m0, %18, 0x1e900\n\t" "s_nop 0\n\t"
                     "global_load_lds_dwordx4 v225, s[42:43]\n\t" "s_add_u32 m0, %18, 0x1ed00\n\t" "s_nop 0\n\t" "global_load_lds_dwordx4 v226, s[42:43]\n\t" "s_sub_u32 s42, 0x558000, %21\n\t" "s_add_u32 s42, s44, s42\n\t"
                     "s_addc_u32 s43, s45, 0\n\t" "s_add_u32 m0, %18, 0x1f120\n\t" "s_nop 0\n\t" "global_load_lds_dwordx4 v224, s[42:43]\n\t" "s_add_u32 m0, %18, 0x1f520\n\t" "s_nop 0\n\t"
                     "global_load_lds_dwordx4 v225, s[42:43]\n\t" "s_add_u32 m0, %18, 0x1f920\n\t" "s_nop 0\n\t" "global_load_lds_dwordx4 v226, s[42:43]\n\t" "v_add_u32 v232, 0x0, v227\n\t" "v_add_u32 v233, 0x0, v228\n\t"
                     "ds_read_b64_tr_b16 v[144:145], v232\n\t" "ds_read_b64_tr_b16 v[146:147], v232 offset:768\n\t" "ds_read_b64_tr_b16 v[148:149], v232 offset:12416\n\t"
                     "ds_read_b64_tr_b16 v[150:151], v232 offset:13184\n\t" "ds_read_b64_tr_b16 v[16:17], v232 offset:32\n\t" "ds_read_b64_tr_b16 v[18:19], v232 offset:800\n\t"
                     "ds_read_b64_tr_b16 v[20:21], v232 offset:12448\n\t" "ds_read_b64_tr_b16 v[22:23], v232 offset:13216\n\t" "ds_read_b64_tr_b16 v[152:153], v233 offset:64\n\t"
                     "ds_read_b64_tr_b16 v[154:155], v233 offset:832\n\t" "ds_read_b64_tr_b16 v[156:157], v233 offset:12480\n\t" "ds_read_b64_tr_b16 v[158:159], v233 offset:13248\n\t"
                     "ds_read_b64_tr_b16 v[176:177], v232 offset:24832\n\t" "ds_read_b64_tr_b16 v[178:179], v232 offset:25600\n\t" "ds_read_b64_tr_b16 v[180:181], v232 offset:37248\n\t"
                     "ds_read_b64_tr_b16 v[182:183], v232 offset:38016\n\t" "ds_read_b64_tr_b16 v[48:49], v232 offset:24864\n\t" "ds_read_b64_tr_b16 v[50:51], v232 offset:25632\n\t"
                     "ds_read_b64_tr_b16 v[52:53], v232 offset:37280\n\t" "ds_read_b64_tr_b16 v[54:55], v232 offset:38048\n\t" "ds_read_b64_tr_b16 v[184:185], v233 offset:24896\n\t"
                     "ds_read_b64_tr_b16 v[186:187], v233 offset:25664\n\t" "ds_read_b64_tr_b16 v[188:189], v233 offset:37312\n\t" "ds_read_b64_tr_b16 v[190:191], v233 offset:38080\n\t" "s_waitcnt vmcnt(12) lgkmcnt(0)\n\t"
                     "s_barrier\n\t" "s_add_u32 s42, %21, 0x600000\n\t" "s_add_u32 s42, s44, s42\n\t" "s_addc_u32 s43, s45, 0\n\t" "s_add_u32 m0, %18, 0x0\n\t" "s_nop 0\n\t" "global_load_lds_dwordx4 v224, s[42:43]\n\t"
                     "s_add_u32 m0, %18, 0x400\n\t" "s_nop 0\n\t" "global_load_lds_dwordx4 v225, s[42:43]\n\t" "s_add_u32 m0, %18, 0x800\n\t" "s_nop 0\n\t" "global_load_lds_dwordx4 v226, s[42:43]\n\t"
                     "s_add_u32 s42, %21, 0x618000\n\t" "s_add_u32 s42, s44, s42\n\t" "s_addc_u32 s43, s45, 0\n\t" "s_add_u32 m0, %18, 0xc20\n\t" "s_nop 0\n\t" "global_load_lds_dwordx4 v224, s[42:43]\n\t"
                     "s_add_u32 m0, %18, 0x1020\n\t" "s_nop 0\n\t" "global_load_lds_dwordx4 v225, s[42:43]\n\t" "s_add_u32 m0, %18, 0x1420\n\t" "s_nop 0\n\t" "global_load_lds_dwordx4 v226, s[42:43]\n\t"
                     "s_sub_u32 s42, 0x6c0000, %21\n\t" "s_add_u32 s42, s44, s42\n\t" "s_addc_u32 s43, s45, 0\n\t" "s_add_u32 m0, %18, 0x6100\n\t" "s_nop 0\n\t" "global_load_lds_dwordx4 v224, s[42:43]\n\t"
                     "s_add_u32 m0, %18, 0x6500\n\t" "s_nop 0\n\t" "global_load_lds_dwordx4 v225, s[42:43]\n\t" "s_add_u32 m0, %18, 0x6900\n\t" "s_nop 0\n\t" "global_load_lds_dwordx4 v226, s[42:43]\n\t"
                     "s_sub_u32 s42, 0x6d8000, %21\n\t" "s_add_u32 s42, s44, s42\n\t" "s_addc_u32 s43, s45, 0\n\t" "s_add_u32 m0, %18, 0x6d20\n\t" "s_nop 0\n\t" "global_load_lds_dwordx4 v224, s[42:43]\n\t"
                     "s_add_u32 m0, %18, 0x7120\n\t" "s_nop 0\n\t" "global_load_lds_dwordx4 v225, s[42:43]\n\t" "s_add_u32 m0, %18, 0x7520\n\t" "s_nop 0\n\t" "global_load_lds_dwordx4 v226, s[42:43]\n\t"
                     "global_load_dword %14, %14, %20 sc1\n\t" "v_add_u32 v232, 0xc200, v227\n\t" "v_add_u32 v233, 0xc200, v228\n\t" "ds_read_b64_tr_b16 v[192:193], v232\n\t"
                     "ds_read_b64_tr_b16 v[194:195], v232 offset:768\n\t" "ds_read_b64_tr_b16 v[196:197], v232 offset:12416\n\t" "ds_read_b64_tr_b16 v[198:199], v232 offset:13184\n\t"
                     "ds_read_b64_tr_b16 v[64:65], v232 offset:32\n\t" "ds_read_b64_tr_b16 v[66:67], v232 offset:800\n\t" "ds_read_b64_tr_b16 v[68:69], v232 offset:12448\n\t"
                     "ds_read_b64_tr_b16 v[70:71], v232 offset:13216\n\t" "ds_read_b64_tr_b16 v[200:201], v233 offset:64\n\t" "ds_read_b64_tr_b16 v[202:203], v233 offset:832\n\t"
                     "ds_read_b64_tr_b16 v[204:205], v233 offset:12480\n\t" "ds_read_b64_tr_b16 v[206:207], v233 offset:13248\n\t" "ds_read_b64_tr_b16 v[208:209], v232 offset:24832\n\t"
                     "ds_read_b64_tr_b16 v[210:211], v232 offset:25600\n\t" "ds_read_b64_tr_b16 v[212:213], v232 offset:37248\n\t" "ds_read_b64_tr_b16 v[214:215], v232 offset:38016\n\t"
                     "ds_read_b64_tr_b16 v[80:81], v232 offset:24864\n\t" "ds_read_b64_tr_b16 v[82:83], v232 offset:25632\n\t" "ds_read_b64_tr_b16 v[84:85], v232 offset:37280\n\t"
                     "ds_read_b64_tr_b16 v[86:87], v232 offset:38048\n\t" "ds_read_b64_tr_b16 v[216:217], v233 offset:24896\n\t" "ds_read_b64_tr_b16 v[218:219], v233 offset:25664\n\t"
                     "ds_read_b64_tr_b16 v[220:221], v233 offset:37312\n\t" "ds_read_b64_tr_b16 v[222:223], v233 offset:38080\n\t" "s_waitcnt vmcnt(13) lgkmcnt(0)\n\t" "s_barrier\n\t" "v_add_u32 v232, 0x18400, v227\n\t"
                     "v_add_u32 v233, 0x18400, v228\n\t" "ds_read_b64_tr_b16 a[0:1], v232\n\t" "ds_read_b64_tr_b16 a[2:3], v232 offset:768\n\t" "ds_read_b64_tr_b16 a[4:5], v232 offset:12416\n\t"
                     "ds_read_b64_tr_b16 a[6:7], v232 offset:13184\n\t" "ds_read_b64_tr_b16 v[96:97], v232 offset:32\n\t" "ds_read_b64_tr_b16 v[98:99], v232 offset:800\n\t"
                     "ds_read_b64_tr_b16 v[100:101], v232 offset:12448\n\t" "ds_read_b64_tr_b16 v[102:103], v232 offset:13216\n\t" "ds_read_b64_tr_b16 a[8:9], v233 offset:64\n\t"
                     "ds_read_b64_tr_b16 a[10:11], v233 offset:832\n\t" "ds_read_b64_tr_b16 a[12:13], v233 offset:12480\n\t" "ds_read_b64_tr_b16 a[14:15], v233 offset:13248\n\t"
                     "ds_read_b64_tr_b16 a[16:17], v232 offset:24832\n\t" "ds_read_b64_tr_b16 a[18:19], v232 offset:25600\n\t" "ds_read_b64_tr_b16 a[20:21], v232 offset:37248\n\t"
                     "ds_read_b64_tr_b16 a[22:23], v232 offset:38016\n\t" "ds_read_b64_tr_b16 v[112:113], v232 offset:24864\n\t" "ds_read_b64_tr_b16 v[114:115], v232 offset:25632\n\t"
                     "ds_read_b64_tr_b16 v[116:117], v232 offset:37280\n\t" "ds_read_b64_tr_b16 v[118:119], v232 offset:38048\n\t" "ds_read_b64_tr_b16 a[24:25], v233 offset:24896\n\t"
                     "ds_read_b64_tr_b16 a[26:27], v233 offset:25664\n\t" "ds_read_b64_tr_b16 a[28:29], v233 offset:37312\n\t" "ds_read_b64_tr_b16 a[30:31], v233 offset:38080\n\t" "s_waitcnt vmcnt(0) lgkmcnt(0)\n\t"
                     "s_barrier\n\t" "v_add_u32 v232, 0x0, v227\n\t" "v_add_u32 v233, 0x0, v228\n\t" "ds_read_b64_tr_b16 a[32:33], v232\n\t" "ds_read_b64_tr_b16 a[34:35], v232 offset:768\n\t"
                     "ds_read_b64_tr_b16 a[36:37], v232 offset:12416\n\t" "ds_read_b64_tr_b16 a[38:39], v232 offset:13184\n\t" "ds_read_b64_tr_b16 a[40:41], v232 offset:32\n\t"
                     "ds_read_b64_tr_b16 a[42:43], v232 offset:800\n\t" "ds_read_b64_tr_b16 a[44:45], v232 offset:12448\n\t" "ds_read_b64_tr_b16 a[46:47], v232 offset:13216\n\t"
                     "ds_read_b64_tr_b16 a[48:49], v233 offset:64\n\t" "ds_read_b64_tr_b16 a[50:51], v233 offset:832\n\t" "ds_read_b64_tr_b16 a[52:53], v233 offset:12480\n\t"
                     "ds_read_b64_tr_b16 a[54:55], v233 offset:13248\n\t" "ds_read_b64_tr_b16 a[64:65], v232 offset:24832\n\t" "ds_read_b64_tr_b16 a[66:67], v232 offset:25600\n\t"
                     "ds_read_b64_tr_b16 a[68:69], v232 offset:37248\n\t" "ds_read_b64_tr_b16 a[70:71], v232 offset:38016\n\t" "ds_read_b64_tr_b16 a[72:73], v232 offset:24864\n\t"
                     "ds_read_b64_tr_b16 a[74:75], v232 offset:25632\n\t" "ds_read_b64_tr_b16 a[76:77], v232 offset:37280\n\t" "ds_read_b64_tr_b16 a[78:79], v232 offset:38048\n\t"
                     "ds_read_b64_tr_b16 a[80:81], v233 offset:24896\n\t" "ds_read_b64_tr_b16 a[82:83], v233 offset:25664\n\t" "ds_read_b64_tr_b16 a[84:85], v233 offset:37312\n\t"
                     "ds_read_b64_tr_b16 a[86:87], v233 offset:38080\n\t" "s_waitcnt lgkmcnt(0)\n\t" "s_barrier\n\t" "ds_write_b128 v230, v[0:3]\n\t" "ds_write_b128 v230, v[4:7] offset:1024\n\t"
                     "ds_write_b128 v230, v[8:11] offset:2048\n\t" "ds_write_b128 v230, v[12:15] offset:3072\n\t" "ds_write_b128 v230, v[16:19] offset:4096\n\t" "ds_write_b128 v230, v[20:23] offset:5120\n\t"
                     "ds_write_b128 v230, v[24:27] offset:6144\n\t" "ds_write_b128 v230, v[28:31] offset:7168\n\t" "ds_write_b128 v230, v[32:35] offset:8192\n\t" "ds_write_b128 v230, v[36:39] offset:9216\n\t"
                     "ds_write_b128 v230, v[40:43] offset:10240\n\t" "ds_write_b128 v230, v[44:47] offset:11264\n\t" "ds_write_b128 v230, v[48:51] offset:12288\n\t" "ds_write_b128 v230, v[52:55] offset:13312\n\t"
                     "ds_write_b128 v230, v[56:59] offset:14336\n\t" "ds_write_b128 v230, v[60:63] offset:15360\n\t" "ds_write_b128 v230, v[64:67] offset:16384\n\t" "ds_write_b128 v230, v[68:71] offset:17408\n\t"
                     "ds_write_b128 v230, v[72:75] offset:18432\n\t" "ds_write_b128 v230, v[76:79] offset:19456\n\t" "ds_write_b128 v230, v[80:83] offset:20480\n\t" "ds_write_b128 v230, v[84:87] offset:21504\n\t"
                     "ds_write_b128 v230, v[88:91] offset:22528\n\t" "ds_write_b128 v230, v[92:95] offset:23552\n\t" "ds_write_b128 v230, v[96:99] offset:24576\n\t" "ds_write_b128 v230, v[100:103] offset:25600\n\t"
                     "ds_write_b128 v230, v[104:107] offset:26624\n\t" "ds_write_b128 v230, v[108:111] offset:27648\n\t" "ds_write_b128 v230, v[112:115] offset:28672\n\t" "ds_write_b128 v230, v[116:119] offset:29696\n\t"
                     "ds_write_b128 v230, v[120:123] offset:30720\n\t" "ds_write_b128 v230, v[124:127] offset:31744\n\t"
                     : "=&{a[0:31]}"(wa[0]), "=&{a[32:63]}"(wa[1]), "=&{a[64:95]}"(wa[2]), "=&{a[96:127]}"(wa[3]), "=&{a[128:159]}"(wa[4]), "=&{a[160:191]}"(wa[5]), "=&{a[192:223]}"(wa[6]), "=&{a[224:255]}"(wa[7]), "=&{v[128:159]}"(wv[0]), "=&{v[160:191]}"(wv[1]), "=&{v[192:223]}"(wv[2]), "=&v"(y.r), "=&v"(y.g), "=&v"(y.e), "=&v"(kfl)
                     : "{v[224:231]}"(pa), "{s[44:45]}"(p.wq + h * DH * 2 + (size_t)(16 * w) * (HD * 2)), "{s[46:47]}"(p.wk + h * DH * 2 + (size_t)(16 * w) * (HD * 2)),
                       "s"(lds + 2 * w * GS), "s"(p.ws + OFF_LAT), "s"(fl + (F_K + h) * 128), "s"((h & 4) * 16 * HD * 2)
                     : "v0", "v1", "v2", "v3", "v4", "v5", "v6", "v7", "v8", "v9", "v10", "v11", "v12", "v13", "v14", "v15", "v16", "v17", "v18", "v19", "v20", "v21", "v22", "v23", "v24", "v25", "v26", "v27", "v28", "v29", "v30", "v31", "v32", "v33", "v34", "v35", "v36", "v37", "v38", "v39", "v40", "v41", "v42", "v43", "v44", "v45", "v46", "v47", "v48", "v49", "v50", "v51", "v52", "v53", "v54", "v55", "v56", "v57", "v58", "v59", "v60", "v61", "v62", "v63", "v64", "v65", "v66", "v67", "v68", "v69", "v70", "v71", "v72", "v73", "v74", "v75", "v76", "v77", "v78", "v79", "v80", "v81", "v82", "v83", "v84", "v85", "v86", "v87", "v88", "v89", "v90", "v91", "v92", "v93", "v94", "v95", "v96", "v97", "v98", "v99", "v100", "v101", "v102", "v103", "v104", "v105", "v106", "v107", "v108", "v109", "v110", "v111", "v112", "v113", "v114", "v115", "v116", "v117", "v118", "v119", "v120", "v121", "v122", "v123", "v124", "v125", "v126", "v127", "v232", "v233", "s42", "s43", "scc", "memory");
        const u64 t = sync_tag(y.e);
        const bool rdy = __builtin_amdgcn_ballot_w64((y.r[0] & ~1u) != (u32)t || y.r[1] != (u32)(t >> 32) || y.g[0] != (u32)t) == 0 && uni((int)kfl) != -1;
        if (rdy) kxdma();
        sd = sync_end(y, p.ws, phi, w == 0);
        if (!rdy) { flag_wait(fl, F_K + h, 1); kxdma(); }
        kxp += NH * 12288;
    }
    const u32 sidx = (lane & 2) ? 0xEEEEu : 0x4444u;
    const u32 moff = lane * 16;
    u32 kxa = kxl - 16 + (n & 1) * 6160 + (16 * g + (n & 8) + 2 * (n & 2) + ((n >> 1) & 2)) * 16;
    const u64 mb0 = 0xffff0000ffff0000ull;
    const int tp = (3 * w + (w & 1)) >> 1, ts = 1 + 3 * (w >> 1);
    u32 qw0 = lds + SL_IMG + tp * 512 + lane * 8, qw1 = lds + SL_IMG + ts * 512 + lane * 8 + 4 * (w & 1);
    const u32 kw0 = lds + SL_IMG + 3072 + w * 288 + n * 16 + 4 * g;
    const u32 qr0 = lds + SL_IMG + (g >> 1) * 512 + w * 128 + 64 * (g & 1) + 8 * qq;
    const u32 kr0 = lds + SL_IMG + 3072 + (qq >> 1) * 288 + g * 64 + (qq & 1) * 16 + 8 * (pp & 1);
    const u32 xso = 32768 + (h >> 4) * 16384 + (h & 15) * 512 + (w >> 1) * 256 + (n >> 3) * 64 + (n & 4) * 32 + (w & 1) * 32 + (g & 1) * 16 + (n & 3) * 4 + (g >> 1) * 8192;
    const u32 pso = (n < 8 ? 20480 + (h >> 1) * 256 + (n & 2) * 16 + (h & 1) * 8 + (n & 4) : 18432 + (h >> 2) * 256 + (h & 1) * 8 + (h & 2) * 2) + w * 64 + (g & 1) * 16 + (g >> 1) * 8192;
    const u64 psm = 0x01aa01aa01aa01aaull;
    const u32 kmo = (u32)(((n >> 3) * 4 + g) * 128 + (n & 7) * 16);
    u32x16 ad = u32x16{moff, sidx, lfa, kxa, qw0, qw1, kw0, kr0 + 1152 * min(w, 2) + (pp >> 1) * 576, qr0, kr0, xso, pso, 0x3dd53b94u, kmo, kmo, kmo};
    asm volatile("" : "+{v[224:239]}"(ad));
    const char* memp = p.ws + mslot(sd, 0);
    char* kmp = p.ws + OFF_KM + h * 3072 + min(w, 2) * 1024;
    const float kscale = 0.07216878364870322f;
    int budget = 30000;
    Clock clk; clk.init();
    u32x32 wk[3];
    asm volatile("" : "={v[64:95]}"(wk[0]), "={v[96:127]}"(wk[1]), "={v[32:63]}"(wk[2]));
    const int kfo = (F_K + 32 + h) * 128;
    for (int s = 0; s < NSEG; ++s) {
        int tries = 0;
        u64 bad, tp8, en = ~0ull;
        u32 tt;
        int kf;
        while (true) {
            const u32 tgt = clk.sched(phi, 0, cfg.G, tries ? 2 : 0);
            asm volatile("s_cmp_lg_u32 %12, 0\n\t" "s_cbranch_scc1 .Lrt_s\n\t" "s_waitcnt vmcnt(0) lgkmcnt(0)\n\t" "s_barrier\n\t" "s_cmp_lg_u32 %13, 0\n\t" "s_cbranch_scc1 .Lsp_s\n\t" ".Lrt_s:\n\t"
                         "ds_read_b128 v[64:67], v226\n\t" "v_mov_b64 v[40:41], 0\n\t" "ds_read_b128 v[68:71], v226 offset:1024\n\t" "v_mov_b64 v[42:43], 0\n\t" "ds_read_b128 v[72:75], v226 offset:2048\n\t"
                         "v_mov_b64 v[44:45], 0\n\t" "ds_read_b128 v[76:79], v226 offset:3072\n\t" "v_mov_b64 v[46:47], 0\n\t" "ds_read_b128 v[80:83], v226 offset:4096\n\t" "v_mov_b64 v[48:49], 0\n\t"
                         "ds_read_b128 v[84:87], v226 offset:5120\n\t" "v_mov_b64 v[50:51], 0\n\t" "ds_read_b128 v[88:91], v226 offset:6144\n\t" "v_mov_b64 v[52:53], 0\n\t" "ds_read_b128 v[92:95], v226 offset:7168\n\t"
                         "v_mov_b64 v[54:55], 0\n\t" "ds_read_b128 v[96:99], v226 offset:8192\n\t" "v_mov_b64 v[56:57], 0\n\t" "ds_read_b128 v[100:103], v226 offset:9216\n\t" "v_mov_b64 v[58:59], 0\n\t"
                         "ds_read_b128 v[104:107], v226 offset:10240\n\t" "v_mov_b64 v[60:61], 0\n\t" "ds_read_b128 v[108:111], v226 offset:11264\n\t" "v_mov_b64 v[62:63], 0\n\t"
                         "ds_read_b128 v[112:115], v226 offset:12288\n\t" "ds_read_b128 v[116:119], v226 offset:13312\n\t" "ds_read_b128 v[120:123], v226 offset:14336\n\t" "ds_read_b128 v[124:127], v226 offset:15360\n\t"
                         ".Lsp_s:\n\t" "s_memtime vcc\n\t" "s_waitcnt lgkmcnt(0)\n\t" "s_sub_i32 s42, %4, vcc_lo\n\t" "s_cmp_gt_i32 s42, 80\n\t" "s_cbranch_scc1 .Lsp_s\n\t" "s_max_i32 s42, s42, 0\n\t"
                         "s_sub_i32 s42, 92, s42\n\t" "s_and_b32 s42, s42, -4\n\t" "s_getpc_b64 vcc\n\t" "s_add_u32 vcc_lo, vcc_lo, s42\n\t" "s_addc_u32 vcc_hi, vcc_hi, 0\n\t" "s_setpc_b64 vcc\n\t" "s_nop 0\n\t" "s_nop 0\n\t"
                         "s_nop 0\n\t" "s_nop 0\n\t" "s_nop 0\n\t" "s_nop 0\n\t" "s_nop 0\n\t" "s_nop 0\n\t" "s_nop 0\n\t" "s_nop 0\n\t" "s_nop 0\n\t" "s_nop 0\n\t" "s_nop 0\n\t" "s_nop 0\n\t" "s_nop 0\n\t" "s_nop 0\n\t"
                         "s_nop 0\n\t" "s_nop 0\n\t" "s_nop 0\n\t" "s_nop 0\n\t" "global_load_dwordx4 v[0:3], v224, %5 offset:-4096 sc1\n\t" "v_add_u32 v238, 0x4000, v224\n\t" "s_nop 9\n\t"
                         "global_load_dwordx4 v[4:7], v224, %5 offset:-2048 sc1\n\t" "s_nop 15\n\t" "global_load_dwordx4 v[8:11], v224, %6 offset:-3072 sc1\n\t" "s_nop 10\n\t"
                         "global_load_dwordx4 v[12:15], v224, %6 offset:-1024 sc1\n\t" "s_nop 10\n\t" "global_load_dwordx4 v[16:19], v224, %5 sc1\n\t" "s_nop 10\n\t" "global_load_dwordx4 v[20:23], v224, %6 offset:1024 sc1\n\t"
                         "s_nop 10\n\t" "global_load_dwordx4 v[24:27], v224, %5 offset:2048 sc1\n\t" "s_nop 10\n\t" "global_load_dwordx4 v[28:31], v224, %6 offset:3072 sc1\n\t" "s_nop 10\n\t"
                         "global_load_dwordx4 v[32:35], v238, %5 offset:-4096 sc1\n\t" "s_nop 10\n\t" "global_load_dwordx4 v[36:39], v238, %6 offset:-3072 sc1\n\t" "s_waitcnt vmcnt(9)\n\t"
                         "v_smfmac_f32_16x16x64_bf16 v[40:43], v[0:3], v[128:135], v225\n\t" "v_smfmac_f32_16x16x64_bf16 v[44:47], v[0:3], v[64:71], v225\n\t" "ds_read_b128 v[64:67], v226 offset:16384\n\t"
                         "v_smfmac_f32_16x16x64_bf16 v[48:51], v[0:3], v[136:143], v225\n\t" "ds_read_b128 v[68:71], v226 offset:17408\n\t" "v_smfmac_f32_16x16x64_bf16 v[52:55], v[0:3], v[72:79], v225\n\t"
                         "ds_read_b128 v[72:75], v226 offset:18432\n\t" "s_waitcnt vmcnt(8)\n\t" "v_smfmac_f32_16x16x64_bf16 v[40:43], v[4:7], v[144:151], v225\n\t" "ds_read_b128 v[76:79], v226 offset:19456\n\t"
                         "v_smfmac_f32_16x16x64_bf16 v[44:47], v[4:7], v[80:87], v225\n\t" "ds_read_b128 v[80:83], v226 offset:20480\n\t" "v_smfmac_f32_16x16x64_bf16 v[48:51], v[4:7], v[152:159], v225\n\t"
                         "ds_read_b128 v[84:87], v226 offset:21504\n\t" "v_smfmac_f32_16x16x64_bf16 v[52:55], v[4:7], v[88:95], v225\n\t" "ds_read_b128 v[88:91], v226 offset:22528\n\t" "s_waitcnt vmcnt(7)\n\t"
                         "v_smfmac_f32_16x16x64_bf16 v[40:43], v[8:11], v[160:167], v225\n\t" "ds_read_b128 v[92:95], v226 offset:23552\n\t" "v_max_u32 v238, v0, v4\n\t"
                         "v_smfmac_f32_16x16x64_bf16 v[44:47], v[8:11], v[96:103], v225\n\t" "ds_read_b128 v[96:99], v226 offset:24576\n\t" "v_cmp_eq_u32 vcc, -1, v238\n\t"
                         "v_smfmac_f32_16x16x64_bf16 v[48:51], v[8:11], v[168:175], v225\n\t" "ds_read_b128 v[100:103], v226 offset:25600\n\t" "v_smfmac_f32_16x16x64_bf16 v[52:55], v[8:11], v[104:111], v225\n\t"
                         "s_and_b64 vcc, vcc, %11\n\t" "s_cbranch_scc1 .Lsk_s\n\t" "ds_read_b128 v[104:107], v226 offset:26624\n\t" "s_waitcnt vmcnt(6)\n\t" "v_smfmac_f32_16x16x64_bf16 v[40:43], v[12:15], v[176:183], v225\n\t"
                         "ds_read_b128 v[108:111], v226 offset:27648\n\t" "v_smfmac_f32_16x16x64_bf16 v[44:47], v[12:15], v[112:119], v225\n\t" "ds_read_b128 v[112:115], v226 offset:28672\n\t"
                         "v_smfmac_f32_16x16x64_bf16 v[48:51], v[12:15], v[184:191], v225\n\t" "ds_read_b128 v[116:119], v226 offset:29696\n\t" "v_smfmac_f32_16x16x64_bf16 v[52:55], v[12:15], v[120:127], v225\n\t"
                         "ds_read_b128 v[120:123], v226 offset:30720\n\t" "s_waitcnt vmcnt(5)\n\t" "v_smfmac_f32_16x16x64_bf16 v[40:43], v[16:19], v[192:199], v225\n\t" "ds_read_b128 v[124:127], v226 offset:31744\n\t"
                         "s_waitcnt lgkmcnt(14)\n\t" "v_smfmac_f32_16x16x64_bf16 v[44:47], v[16:19], v[64:71], v225\n\t" "ds_read_b128 v[64:67], v227\n\t" "v_smfmac_f32_16x16x64_bf16 v[48:51], v[16:19], v[200:207], v225\n\t"
                         "ds_read_b128 v[68:71], v227 offset:1024\n\t" "s_waitcnt lgkmcnt(14)\n\t" "v_smfmac_f32_16x16x64_bf16 v[52:55], v[16:19], v[72:79], v225\n\t" "ds_read_b128 v[72:75], v227 offset:16\n\t"
                         "s_waitcnt vmcnt(4)\n\t" "v_smfmac_f32_16x16x64_bf16 v[40:43], v[20:23], v[208:215], v225\n\t" "ds_read_b128 v[76:79], v227 offset:1040\n\t" "s_waitcnt lgkmcnt(14)\n\t"
                         "v_smfmac_f32_16x16x64_bf16 v[44:47], v[20:23], v[80:87], v225\n\t" "ds_read_b128 v[80:83], v227 offset:2048\n\t" "v_smfmac_f32_16x16x64_bf16 v[48:51], v[20:23], v[216:223], v225\n\t"
                         "ds_read_b128 v[84:87], v227 offset:3072\n\t" "s_waitcnt lgkmcnt(14)\n\t" "v_smfmac_f32_16x16x64_bf16 v[52:55], v[20:23], v[88:95], v225\n\t" "ds_read_b128 v[88:91], v227 offset:2064\n\t"
                         "s_waitcnt vmcnt(3)\n\t" "v_smfmac_f32_16x16x64_bf16 v[40:43], v[24:27], a[0:7], v225\n\t" "ds_read_b128 v[92:95], v227 offset:3088\n\t" "s_waitcnt lgkmcnt(14)\n\t"
                         "v_smfmac_f32_16x16x64_bf16 v[44:47], v[24:27], v[96:103], v225\n\t" "ds_read_b128 v[96:99], v227 offset:4096\n\t" "v_smfmac_f32_16x16x64_bf16 v[48:51], v[24:27], a[8:15], v225\n\t"
                         "ds_read_b128 v[100:103], v227 offset:5120\n\t" "s_waitcnt lgkmcnt(14)\n\t" "v_smfmac_f32_16x16x64_bf16 v[52:55], v[24:27], v[104:111], v225\n\t" "ds_read_b128 v[104:107], v227 offset:4112\n\t"
                         "s_waitcnt vmcnt(2)\n\t" "v_smfmac_f32_16x16x64_bf16 v[40:43], v[28:31], a[16:23], v225\n\t" "ds_read_b128 v[108:111], v227 offset:5136\n\t" "s_waitcnt lgkmcnt(14)\n\t"
                         "v_smfmac_f32_16x16x64_bf16 v[44:47], v[28:31], v[112:119], v225\n\t" "v_smfmac_f32_16x16x64_bf16 v[48:51], v[28:31], a[24:31], v225\n\t" "s_waitcnt lgkmcnt(12)\n\t"
                         "v_smfmac_f32_16x16x64_bf16 v[52:55], v[28:31], v[120:127], v225\n\t" "s_waitcnt vmcnt(1)\n\t" "v_smfmac_f32_16x16x64_bf16 v[40:43], v[32:35], a[32:39], v225\n\t" "v_mov_b64 v[112:113], 0\n\t"
                         "v_smfmac_f32_16x16x64_bf16 v[44:47], v[32:35], a[40:47], v225\n\t" "v_mov_b64 v[114:115], 0\n\t" "v_smfmac_f32_16x16x64_bf16 v[48:51], v[32:35], a[48:55], v225\n\t" "v_mov_b64 v[116:117], 0\n\t"
                         "v_smfmac_f32_16x16x64_bf16 v[52:55], v[32:35], a[56:63], v225\n\t" "v_mov_b64 v[118:119], 0\n\t" "s_waitcnt vmcnt(0)\n\t" "v_smfmac_f32_16x16x64_bf16 v[40:43], v[36:39], a[64:71], v225\n\t"
                         "v_mov_b64 v[120:121], 0\n\t" "v_smfmac_f32_16x16x64_bf16 v[44:47], v[36:39], a[72:79], v225\n\t" "v_mov_b64 v[122:123], 0\n\t" "v_smfmac_f32_16x16x64_bf16 v[48:51], v[36:39], a[80:87], v225\n\t"
                         "v_mov_b64 v[124:125], 0\n\t" "v_smfmac_f32_16x16x64_bf16 v[52:55], v[36:39], a[88:95], v225\n\t" "v_add_f32 v40, v40, v42\n\t" "v_add_f32 v41, v41, v43\n\t"
                         "v_smfmac_f32_16x16x64_bf16 v[56:59], v[0:3], a[96:103], v225\n\t" "v_cmp_u_f32 vcc, v40, v41\n\t" "v_smfmac_f32_16x16x64_bf16 v[56:59], v[4:7], a[104:111], v225\n\t"
                         "v_cvt_pk_bf16_f32 v42, v40, v41\n\t" "v_add_f32 v44, v44, v46\n\t" "v_smfmac_f32_16x16x64_bf16 v[56:59], v[8:11], a[112:119], v225\n\t" "s_and_b64 vcc, vcc, %11\n\t" "s_cbranch_scc1 .Lsk_s\n\t"
                         "v_smfmac_f32_16x16x64_bf16 v[56:59], v[12:15], a[120:127], v225\n\t" "v_add_f32 v45, v45, v47\n\t" "v_cvt_pk_bf16_f32 v43, v44, v45\n\t"
                         "v_smfmac_f32_16x16x64_bf16 v[56:59], v[16:19], a[128:135], v225\n\t" "ds_write_b64 v228, v[42:43]\n\t" "v_add_f32 v48, v48, v50\n\t"
                         "v_smfmac_f32_16x16x64_bf16 v[56:59], v[20:23], a[136:143], v225\n\t" "v_add_f32 v49, v49, v51\n\t" "v_cvt_pk_bf16_f32 v46, v48, v49\n\t"
                         "v_smfmac_f32_16x16x64_bf16 v[56:59], v[24:27], a[144:151], v225\n\t" "ds_write_b32 v229, v46\n\t" "v_add_f32 v52, v52, v54\n\t" "v_smfmac_f32_16x16x64_bf16 v[56:59], v[28:31], a[152:159], v225\n\t"
                         "v_add_f32 v53, v53, v55\n\t" "v_cvt_pk_bf16_f32 v54, v52, v53\n\t" "v_smfmac_f32_16x16x64_bf16 v[56:59], v[32:35], a[160:167], v225\n\t" "ds_write_b32 v230, v54\n\t"
                         "v_smfmac_f32_16x16x64_bf16 v[56:59], v[36:39], a[168:175], v225\n\t" "v_smfmac_f32_16x16x64_bf16 v[60:63], v[0:3], a[176:183], v225\n\t"
                         "v_smfmac_f32_16x16x64_bf16 v[60:63], v[4:7], a[184:191], v225\n\t" "v_add_f32 v56, v56, v58\n\t" "v_smfmac_f32_16x16x64_bf16 v[60:63], v[8:11], a[192:199], v225\n\t" "s_waitcnt lgkmcnt(0)\n\t"
                         "s_barrier\n\t" "v_smfmac_f32_16x16x64_bf16 v[60:63], v[12:15], a[200:207], v225\n\t" "ds_read_b64_tr_b16 v[40:41], v232\n\t" "v_add_f32 v57, v57, v59\n\t"
                         "v_smfmac_f32_16x16x64_bf16 v[60:63], v[16:19], a[208:215], v225\n\t" "ds_read_b64_tr_b16 v[42:43], v232 offset:32\n\t" "v_cvt_pk_bf16_f32 v58, v56, v57\n\t"
                         "v_smfmac_f32_16x16x64_bf16 v[60:63], v[20:23], a[216:223], v225\n\t" "ds_write_b32 v230, v58 offset:1152\n\t" "v_smfmac_f32_16x16x64_bf16 v[60:63], v[24:27], a[224:231], v225\n\t"
                         "ds_read_b64_tr_b16 v[44:45], v232 offset:1024\n\t" "ds_read_b64_tr_b16 v[46:47], v232 offset:1056\n\t" "v_smfmac_f32_16x16x64_bf16 v[60:63], v[28:31], a[232:239], v225\n\t"
                         "ds_read_b64_tr_b16 v[48:49], v232 offset:2048\n\t" "ds_read_b64_tr_b16 v[50:51], v232 offset:2080\n\t" "v_smfmac_f32_16x16x64_bf16 v[60:63], v[32:35], a[240:247], v225\n\t"
                         "ds_read_b64_tr_b16 v[0:1], v233\n\t" "ds_read_b64_tr_b16 v[2:3], v233 offset:32\n\t" "v_smfmac_f32_16x16x64_bf16 v[60:63], v[36:39], a[248:255], v225\n\t"
                         "ds_read_b64_tr_b16 v[4:5], v233 offset:576\n\t" "ds_read_b64_tr_b16 v[6:7], v233 offset:608\n\t" "s_waitcnt lgkmcnt(9)\n\t" "v_smfmac_f32_16x16x64_bf16 v[112:115], v[40:43], v[64:71], v225\n\t"
                         "s_waitcnt lgkmcnt(8)\n\t" "s_barrier\n\t" "v_smfmac_f32_16x16x64_bf16 v[116:119], v[40:43], v[72:79], v225\n\t" "ds_read_b64_tr_b16 v[8:9], v233 offset:1152\n\t" "s_waitcnt lgkmcnt(7)\n\t"
                         "v_add_f32 v60, v60, v62\n\t" "v_add_f32 v61, v61, v63\n\t" "v_smfmac_f32_16x16x64_bf16 v[112:115], v[44:47], v[80:87], v225\n\t" "ds_read_b64_tr_b16 v[10:11], v233 offset:1184\n\t"
                         "v_cvt_pk_bf16_f32 v62, v60, v61\n\t" "v_smfmac_f32_16x16x64_bf16 v[116:119], v[44:47], v[88:95], v225\n\t" "ds_write_b32 v230, v62 offset:2304\n\t" "s_waitcnt lgkmcnt(7)\n\t"
                         "v_smfmac_f32_16x16x64_bf16 v[112:115], v[48:51], v[96:103], v225\n\t" "ds_read_b64_tr_b16 v[12:13], v233 offset:1728\n\t" "v_smfmac_f32_16x16x64_bf16 v[116:119], v[48:51], v[104:111], v225\n\t"
                         "ds_read_b64_tr_b16 v[14:15], v233 offset:1760\n\t" "s_waitcnt lgkmcnt(5)\n\t" "v_smfmac_f32_16x16x64_bf16 v[120:123], v[40:43], v[0:7], v225\n\t" "v_cndmask_b32 v24, v112, v113, %8\n\t"
                         "v_cndmask_b32 v25, v114, v115, %8\n\t" "v_add_f32 v24, v24, v25\n\t" "v_pk_add_f32 v[26:27], v[116:117], v[118:119]\n\t" "v_cndmask_b32 v25, v26, v27, %8\n\t" "v_max_f32 v26, v24, v25\n\t"
                         "s_waitcnt lgkmcnt(2)\n\t" "s_barrier\n\t" "ds_read_b64_tr_b16 v[16:17], v233 offset:2304\n\t" "v_max_f32_dpp v27, v26, v26 row_ror:4 row_mask:0xf bank_mask:0xf\n\t"
                         "ds_read_b64_tr_b16 v[18:19], v233 offset:2336\n\t" "v_max_f32_dpp v27, v26, v27 row_ror:8 row_mask:0xf bank_mask:0xf\n\t" "v_max_f32_dpp v27, v26, v27 row_ror:12 row_mask:0xf bank_mask:0xf\n\t"
                         "ds_read_b64_tr_b16 v[20:21], v233 offset:2880\n\t" "ds_read_b64_tr_b16 v[22:23], v233 offset:2912\n\t" "v_max_f32_dpp v30, v27, v27 row_ror:1 row_mask:0xf bank_mask:0xf\n\t"
                         "v_max_f32_dpp v30, v27, v30 row_ror:2 row_mask:0xf bank_mask:0xf\n\t" "v_max_f32_dpp v30, v27, v30 row_ror:3 row_mask:0xf bank_mask:0xf\n\t" "s_waitcnt lgkmcnt(4)\n\t"
                         "v_pk_add_f32 v[24:25], v[24:25], v[30:31] op_sel_hi:[1,0] neg_lo:[0,1] neg_hi:[0,1]\n\t" "v_pk_mul_f32 v[24:25], v[24:25], v[236:237] op_sel_hi:[1,0]\n\t" "v_exp_f32 v24, v24\n\t"
                         "v_exp_f32 v25, v25\n\t" "v_mul_f32 v31, %10, v30\n\t" "v_cvt_pk_bf16_f32 v28, |v24|, |v25|\n\t" "v_smfmac_f32_16x16x64_bf16 v[120:123], v[44:47], v[8:15], v225\n\t"
                         "global_store_dword v234, v28, %7 offset:-4096 sc1\n\t" "s_waitcnt lgkmcnt(0)\n\t" "v_add_f32 v26, v24, v25\n\t" "v_smfmac_f32_16x16x64_bf16 v[120:123], v[48:51], v[16:23], v225\n\t"
                         "s_nop 0\n\t" "v_add_f32_dpp v27, v26, v26 row_ror:4 row_mask:0xf bank_mask:0xf\n\t" "v_add_f32_dpp v27, v26, v27 row_ror:8 row_mask:0xf bank_mask:0xf\n\t"
                         "v_add_f32_dpp v27, v26, v27 row_ror:12 row_mask:0xf bank_mask:0xf\n\t" "s_nop 1\n\t" "v_add_f32_dpp v29, v27, v27 row_ror:1 row_mask:0xf bank_mask:0xf\n\t"
                         "v_pk_add_f32 v[24:25], v[120:121], v[122:123]\n\t" "v_cndmask_b32 v24, v24, v25, %8\n\t" "v_max_f32 v26, v24, v30\n\t" "v_add_f32_dpp v29, v27, v29 row_ror:2 row_mask:0xf bank_mask:0xf\n\t"
                         "v_add_f32_dpp v29, v27, v29 row_ror:3 row_mask:0xf bank_mask:0xf\n\t" "v_max_f32_dpp v27, v26, v26 row_ror:1 row_mask:0xf bank_mask:0xf\n\t"
                         "v_max_f32_dpp v27, v26, v27 row_ror:2 row_mask:0xf bank_mask:0xf\n\t" "v_max_f32_dpp v27, v26, v27 row_ror:3 row_mask:0xf bank_mask:0xf\n\t" "v_mul_f32 v24, %10, v24\n\t"
                         "v_mul_f32 v29, 0x3e000000, v29\n\t" "v_max_f32_dpp v26, v27, v27 row_ror:4 row_mask:0xf bank_mask:0xf\n\t" "v_fma_f32 v24, -v26, %10, v24\n\t" "v_fma_f32 v25, -v26, %10, v31\n\t"
                         "v_exp_f32 v24, v24\n\t" "v_exp_f32 v25, v25\n\t" "s_nop 0\n\t" "v_fma_f32 v26, v25, v29, v24\n\t" "v_mov_b32_dpp v124, v24 quad_perm:[0,1,2,3] row_mask:0xf bank_mask:0x3\n\t"
                         "v_mov_b32_dpp v125, v24 quad_perm:[1,0,3,2] row_mask:0xf bank_mask:0x3\n\t" "v_add_f32_dpp v27, v26, v26 row_ror:1 row_mask:0xf bank_mask:0xf\n\t"
                         "v_add_f32_dpp v27, v26, v27 row_ror:2 row_mask:0xf bank_mask:0xf\n\t" "v_add_f32_dpp v27, v26, v27 row_ror:3 row_mask:0xf bank_mask:0xf\n\t"
                         "v_mov_b32_dpp v124, v25 quad_perm:[0,1,2,3] row_mask:0x5 bank_mask:0x4\n\t" "v_mov_b32_dpp v125, v25 quad_perm:[0,1,2,3] row_mask:0xa bank_mask:0x4\n\t"
                         "v_add_f32_dpp v26, v27, v27 row_ror:4 row_mask:0xf bank_mask:0xf\n\t" "v_rcp_f32 v26, v26\n\t" "s_mov_b64 exec, %9\n\t" "v_pk_mul_f32 v[124:125], v[124:125], v[26:27] op_sel_hi:[1,0]\n\t"
                         "v_cvt_pk_bf16_f32 v24, |v124|, |v125|\n\t" "global_store_dword v235, v24, %7 offset:-4096 sc1\n\t" "s_mov_b64 exec, -1\n\t" "s_memrealtime %1\n\t" "s_load_dword %2, %14, %13 offset:%15\n\t"
                         "ds_read_b64_tr_b16 v[36:37], v231\n\t" "ds_read_b64_tr_b16 v[38:39], v231 offset:32\n\t" "v_readfirstlane_b32 %3, v237\n\t" "s_mov_b32 m0, %17\n\t" "v_mov_b64 v[40:41], 0\n\t"
                         "v_mov_b64 v[42:43], 0\n\t" "v_mov_b64 v[44:45], 0\n\t" "v_mov_b64 v[46:47], 0\n\t" "v_mov_b64 v[48:49], 0\n\t" "v_mov_b64 v[50:51], 0\n\t" "v_mov_b64 v[52:53], 0\n\t" "v_mov_b64 v[54:55], 0\n\t"
                         "v_mov_b64 v[56:57], 0\n\t" "v_mov_b64 v[58:59], 0\n\t" "v_mov_b64 v[60:61], 0\n\t" "v_mov_b64 v[62:63], 0\n\t" "v_mov_b32 v237, %18\n\t" "s_waitcnt lgkmcnt(0)\n\t" "ds_read_b128 v[64:67], v226\n\t"
                         "ds_read_b128 v[68:71], v226 offset:1024\n\t" "ds_read_b128 v[72:75], v226 offset:2048\n\t" "ds_read_b128 v[76:79], v226 offset:3072\n\t" "ds_read_b128 v[80:83], v226 offset:4096\n\t"
                         "ds_read_b128 v[84:87], v226 offset:5120\n\t" "s_cmp_eq_u32 %3, -1\n\t" "s_cbranch_scc1 .Lnk_s\n\t" "global_load_lds_dwordx4 v224, %16 sc1\n\t" "global_load_lds_dwordx4 v224, %16 offset:1024 sc1\n\t"
                         "global_load_lds_dwordx4 v224, %16 offset:2048 sc1\n\t" ".Lnk_s:\n\t" "global_store_dwordx4 v239, v[36:39], %21 sc1\n\t" "ds_read_b128 v[88:91], v226 offset:6144\n\t"
                         "ds_read_b128 v[92:95], v226 offset:7168\n\t" "ds_read_b128 v[96:99], v226 offset:8192\n\t" "ds_read_b128 v[100:103], v226 offset:9216\n\t" "ds_read_b128 v[104:107], v226 offset:10240\n\t"
                         "ds_read_b128 v[108:111], v226 offset:11264\n\t" "ds_read_b128 v[112:115], v226 offset:12288\n\t" "ds_read_b128 v[116:119], v226 offset:13312\n\t" "ds_read_b128 v[120:123], v226 offset:14336\n\t"
                         "ds_read_b128 v[124:127], v226 offset:15360\n\t" "s_and_b64 exec, %19, 1\n\t" "s_cbranch_execz .Lnf_s\n\t" "s_sleep 4\n\t" "global_load_dword v237, v237, %20 sc1\n\t" ".Lnf_s:\n\t"
                         "s_mov_b64 exec, -1\n\t" ".Lsk_s:\n\t" "s_mov_b64 %0, vcc\n\t"
                         : "=&s"(bad), "=&s"(tp8), "=&s"(tt), "=&s"(kf)
                         : "s"(uni((int)tgt)), "s"(memp + (h & 4) * 256), "s"(memp - (h & 4) * 256), "s"((u64)memp & ~8192ull), "s"(mb0), "s"(psm), "s"(kscale * 1.4426950408889634f), "s"(en), "s"(tries), "s"(4 * s),
                           "s"(__builtin_amdgcn_kernarg_segment_ptr()), "n"(TT_OFF), "s"(kxp), "s"(kxw), "s"(kfo + (s < 6 ? 0 : (s + 2) >> 3) * 4096), "s"(0x0040404040404044ull >> s), "s"(fl), "s"(kmp),
                           "{v[224:239]}"(ad), "{v[64:95]}"(wk[0]), "{v[96:127]}"(wk[1]), "{v[32:63]}"(wk[2]),
                           "{a[0:31]}"(wa[0]), "{a[32:63]}"(wa[1]), "{a[64:95]}"(wa[2]), "{a[96:127]}"(wa[3]), "{a[128:159]}"(wa[4]), "{a[160:191]}"(wa[5]), "{a[192:223]}"(wa[6]), "{a[224:255]}"(wa[7]), "{v[128:159]}"(wv[0]), "{v[160:191]}"(wv[1]), "{v[192:223]}"(wv[2])
                         : "v0", "v1", "v2", "v3", "v4", "v5", "v6", "v7", "v8", "v9", "v10", "v11", "v12", "v13", "v14", "v15", "v16", "v17", "v18", "v19", "v20", "v21", "v22", "v23", "v24", "v25", "v26", "v27", "v28", "v29", "v30", "v31", "vcc", "scc", "s42", "memory");
            if (__builtin_expect(bad == 0, 1)) break;
            waitvm<0>();
            if (((++tries & 15) == 0 && !unwritten(memp, ad[0])) || --budget < 0) en = 0;
            phi += (u32)cfg.G;
        }
        if (__builtin_expect(kf == -1, 0)) { flag_wait(fl, F_K + pblk(s + 1) * 32 + h, 1); kxdma(); }
        kxp += s < NSEG - 2 ? NH * 12288 : 0;
        clk.calib();
        if (__builtin_expect(s >= NSEG - 2, 0)) {
            waitvm<0>();
            barrier();
            if (w == 3) flag_add(fl, F_PROG + 8 * s + (h & 7), 1);
        }
        follow(phi, (int)(tt >> 16) - (cfg.dV >> 24), (u32)tp8, cfg.G);
        phi += tt & 0xffffu;
        memp = p.ws + mslot(sd, s + 1); kmp += NH * 3072;
    }
    if (h == 0 && w == 0) {
        flag_wait(fl, F_ACK, 256);
        Sync y;
        sync_ld(y, p.ws);
        asm volatile("s_waitcnt vmcnt(0)" : "+v"(y.r), "+v"(y.g), "+v"(y.e) : : "memory");
        const u64 t = ((u64)y.e[1] << 32 | y.e[0]) + 1;
        if (lanex() == 0) gst8(p.ws + OFF_LAT, 1024, u32x2{(u32)t, (u32)(t >> 32)});
    }
    asm volatile(".Lkm_last:\n\ts_waitcnt vmcnt(0)" : : : "memory");
}

DI void proj_role(const Ptrs& p, const ChainCfg cfg, int h, u32 lds, int w) {
    u32x32 wa[8], wv[4];
    u32 phi;
    int sd;
    {
        const int lane = lanex(), tid = 64 * w + lane;
        const int g = lane >> 4, qq = (lane >> 2) & 3, pp = lane & 3;
        constexpr int GS = 3104;
        auto dvo = [&](int i) { const int ix = i * 64 + lane; return (u32)((ix / 24) * (HD * 2) + (ix % 24) * 16); };
        auto ovo = [&](int i) { const int u = i * 64 + lane, r = u / 80; return (u32)(r * 1280 + (((u % 80) ^ (2 * (r ^ (w & 2)))) << 4)); };
        const u32 tra = lds + (2 * (g >> 1) + (qq >> 1)) * GS + (4 * (g & 1) + (qq & 1)) * 384 + 96 * w + 32 * (w & 1) + 8 * pp;
        const u32 tro = (4 * g + (qq & 1) + 16 * (qq >> 1)) * 1280 + 8 * pp + ((u32)(2 * w ^ 2 * ((4 * g + qq) & 7)) << 4);
        u32x16 pa = u32x16{dvo(0), dvo(1), dvo(2), tra, tra - 96 * (w & 1), tro, lds + (u32)((w & 1) * 24576 + (w >> 1) * 99328 + lane * 16), (u32)(lane & 31) * 8,
                           ovo(0), ovo(1), ovo(2), ovo(3), ovo(4), tro, tro, tro};
        Sync y;
        asm volatile("s_nop 4\n\t" "s_add_u32 s42, %22, 0x0\n\t" "s_add_u32 s42, s44, s42\n\t" "s_addc_u32 s43, s45, 0\n\t" "s_add_u32 m0, %18, 0x0\n\t" "s_nop 0\n\t" "global_load_lds_dwordx4 v224, s[42:43]\n\t"
                     "s_add_u32 m0, %18, 0x400\n\t" "s_nop 0\n\t" "global_load_lds_dwordx4 v225, s[42:43]\n\t" "s_add_u32 m0, %18, 0x800\n\t" "s_nop 0\n\t" "global_load_lds_dwordx4 v226, s[42:43]\n\t"
                     "s_add_u32 s42, %22, 0x18000\n\t" "s_add_u32 s42, s44, s42\n\t" "s_addc_u32 s43, s45, 0\n\t" "s_add_u32 m0, %18, 0xc20\n\t" "s_nop 0\n\t" "global_load_lds_dwordx4 v224, s[42:43]\n\t"
                     "s_add_u32 m0, %18, 0x1020\n\t" "s_nop 0\n\t" "global_load_lds_dwordx4 v225, s[42:43]\n\t" "s_add_u32 m0, %18, 0x1420\n\t" "s_nop 0\n\t" "global_load_lds_dwordx4 v226, s[42:43]\n\t"
                     "s_sub_u32 s42, 0xc0000, %22\n\t" "s_add_u32 s42, s44, s42\n\t" "s_addc_u32 s43, s45, 0\n\t" "s_add_u32 m0, %18, 0x6100\n\t" "s_nop 0\n\t" "global_load_lds_dwordx4 v224, s[42:43]\n\t"
                     "s_add_u32 m0, %18, 0x6500\n\t" "s_nop 0\n\t" "global_load_lds_dwordx4 v225, s[42:43]\n\t" "s_add_u32 m0, %18, 0x6900\n\t" "s_nop 0\n\t" "global_load_lds_dwordx4 v226, s[42:43]\n\t"
                     "s_sub_u32 s42, 0xd8000, %22\n\t" "s_add_u32 s42, s44, s42\n\t" "s_addc_u32 s43, s45, 0\n\t" "s_add_u32 m0, %18, 0x6d20\n\t" "s_nop 0\n\t" "global_load_lds_dwordx4 v224, s[42:43]\n\t"
                     "s_add_u32 m0, %18, 0x7120\n\t" "s_nop 0\n\t" "global_load_lds_dwordx4 v225, s[42:43]\n\t" "s_add_u32 m0, %18, 0x7520\n\t" "s_nop 0\n\t" "global_load_lds_dwordx4 v226, s[42:43]\n\t"
                     "s_add_u32 s42, %22, 0x180000\n\t" "s_add_u32 s42, s44, s42\n\t" "s_addc_u32 s43, s45, 0\n\t" "s_add_u32 m0, %18, 0xc200\n\t" "s_nop 0\n\t" "global_load_lds_dwordx4 v224, s[42:43]\n\t"
                     "s_add_u32 m0, %18, 0xc600\n\t" "s_nop 0\n\t" "global_load_lds_dwordx4 v225, s[42:43]\n\t" "s_add_u32 m0, %18, 0xca00\n\t" "s_nop 0\n\t" "global_load_lds_dwordx4 v226, s[42:43]\n\t"
                     "s_add_u32 s42, %22, 0x198000\n\t" "s_add_u32 s42, s44, s42\n\t" "s_addc_u32 s43, s45, 0\n\t" "s_add_u32 m0, %18, 0xce20\n\t" "s_nop 0\n\t" "global_load_lds_dwordx4 v224, s[42:43]\n\t"
                     "s_add_u32 m0, %18, 0xd220\n\t" "s_nop 0\n\t" "global_load_lds_dwordx4 v225, s[42:43]\n\t" "s_add_u32 m0, %18, 0xd620\n\t" "s_nop 0\n\t" "global_load_lds_dwordx4 v226, s[42:43]\n\t"
                     "s_sub_u32 s42, 0x240000, %22\n\t" "s_add_u32 s42, s44, s42\n\t" "s_addc_u32 s43, s45, 0\n\t" "s_add_u32 m0, %18, 0x12300\n\t" "s_nop 0\n\t" "global_load_lds_dwordx4 v224, s[42:43]\n\t"
                     "s_add_u32 m0, %18, 0x12700\n\t" "s_nop 0\n\t" "global_load_lds_dwordx4 v225, s[42:43]\n\t" "s_add_u32 m0, %18, 0x12b00\n\t" "s_nop 0\n\t" "global_load_lds_dwordx4 v226, s[42:43]\n\t"
                     "s_sub_u32 s42, 0x258000, %22\n\t" "s_add_u32 s42, s44, s42\n\t" "s_addc_u32 s43, s45, 0\n\t" "s_add_u32 m0, %18, 0x12f20\n\t" "s_nop 0\n\t" "global_load_lds_dwordx4 v224, s[42:43]\n\t"
                     "s_add_u32 m0, %18, 0x13320\n\t" "s_nop 0\n\t" "global_load_lds_dwordx4 v225, s[42:43]\n\t" "s_add_u32 m0, %18, 0x13720\n\t" "s_nop 0\n\t" "global_load_lds_dwordx4 v226, s[42:43]\n\t"
                     "s_waitcnt vmcnt(12) lgkmcnt(0)\n\t" "s_barrier\n\t" "s_add_u32 s42, %22, 0x300000\n\t" "s_add_u32 s42, s44, s42\n\t" "s_addc_u32 s43, s45, 0\n\t" "s_add_u32 m0, %18, 0x18400\n\t" "s_nop 0\n\t"
                     "global_load_lds_dwordx4 v224, s[42:43]\n\t" "s_add_u32 m0, %18, 0x18800\n\t" "s_nop 0\n\t" "global_load_lds_dwordx4 v225, s[42:43]\n\t" "s_add_u32 m0, %18, 0x18c00\n\t" "s_nop 0\n\t"
                     "global_load_lds_dwordx4 v226, s[42:43]\n\t" "s_add_u32 s42, %22, 0x318000\n\t" "s_add_u32 s42, s44, s42\n\t" "s_addc_u32 s43, s45, 0\n\t" "s_add_u32 m0, %18, 0x19020\n\t" "s_nop 0\n\t"
                     "global_load_lds_dwordx4 v224, s[42:43]\n\t" "s_add_u32 m0, %18, 0x19420\n\t" "s_nop 0\n\t" "global_load_lds_dwordx4 v225, s[42:43]\n\t" "s_add_u32 m0, %18, 0x19820\n\t" "s_nop 0\n\t"
                     "global_load_lds_dwordx4 v226, s[42:43]\n\t" "s_sub_u32 s42, 0x3c0000, %22\n\t" "s_add_u32 s42, s44, s42\n\t" "s_addc_u32 s43, s45, 0\n\t" "s_add_u32 m0, %18, 0x1e500\n\t" "s_nop 0\n\t"
                     "global_load_lds_dwordx4 v224, s[42:43]\n\t" "s_add_u32 m0, %18, 0x1e900\n\t" "s_nop 0\n\t" "global_load_lds_dwordx4 v225, s[42:43]\n\t" "s_add_u32 m0, %18, 0x1ed00\n\t" "s_nop 0\n\t"
                     "global_load_lds_dwordx4 v226, s[42:43]\n\t" "s_sub_u32 s42, 0x3d8000, %22\n\t" "s_add_u32 s42, s44, s42\n\t" "s_addc_u32 s43, s45, 0\n\t" "s_add_u32 m0, %18, 0x1f120\n\t" "s_nop 0\n\t"
                     "global_load_lds_dwordx4 v224, s[42:43]\n\t" "s_add_u32 m0, %18, 0x1f520\n\t" "s_nop 0\n\t" "global_load_lds_dwordx4 v225, s[42:43]\n\t" "s_add_u32 m0, %18, 0x1f920\n\t" "s_nop 0\n\t"
                     "global_load_lds_dwordx4 v226, s[42:43]\n\t" "v_add_u32 v237, 0x0, v227\n\t" "v_add_u32 v238, 0x0, v228\n\t" "ds_read_b64_tr_b16 a[0:1], v237\n\t" "ds_read_b64_tr_b16 a[2:3], v237 offset:768\n\t"
                     "ds_read_b64_tr_b16 a[4:5], v237 offset:12416\n\t" "ds_read_b64_tr_b16 a[6:7], v237 offset:13184\n\t" "ds_read_b64_tr_b16 v[0:1], v237 offset:32\n\t" "ds_read_b64_tr_b16 v[2:3], v237 offset:800\n\t"
                     "ds_read_b64_tr_b16 v[4:5], v237 offset:12448\n\t" "ds_read_b64_tr_b16 v[6:7], v237 offset:13216\n\t" "ds_read_b64_tr_b16 a[8:9], v238 offset:64\n\t" "ds_read_b64_tr_b16 a[10:11], v238 offset:832\n\t"
                     "ds_read_b64_tr_b16 a[12:13], v238 offset:12480\n\t" "ds_read_b64_tr_b16 a[14:15], v238 offset:13248\n\t" "ds_read_b64_tr_b16 a[32:33], v237 offset:24832\n\t"
                     "ds_read_b64_tr_b16 a[34:35], v237 offset:25600\n\t" "ds_read_b64_tr_b16 a[36:37], v237 offset:37248\n\t" "ds_read_b64_tr_b16 a[38:39], v237 offset:38016\n\t"
                     "ds_read_b64_tr_b16 v[16:17], v237 offset:24864\n\t" "ds_read_b64_tr_b16 v[18:19], v237 offset:25632\n\t" "ds_read_b64_tr_b16 v[20:21], v237 offset:37280\n\t"
                     "ds_read_b64_tr_b16 v[22:23], v237 offset:38048\n\t" "ds_read_b64_tr_b16 a[40:41], v238 offset:24896\n\t" "ds_read_b64_tr_b16 a[42:43], v238 offset:25664\n\t"
                     "ds_read_b64_tr_b16 a[44:45], v238 offset:37312\n\t" "ds_read_b64_tr_b16 a[46:47], v238 offset:38080\n\t" "s_waitcnt vmcnt(12) lgkmcnt(0)\n\t" "s_barrier\n\t" "s_add_u32 s42, %22, 0x480000\n\t"
                     "s_add_u32 s42, s44, s42\n\t" "s_addc_u32 s43, s45, 0\n\t" "s_add_u32 m0, %18, 0x0\n\t" "s_nop 0\n\t" "global_load_lds_dwordx4 v224, s[42:43]\n\t" "s_add_u32 m0, %18, 0x400\n\t" "s_nop 0\n\t"
                     "global_load_lds_dwordx4 v225, s[42:43]\n\t" "s_add_u32 m0, %18, 0x800\n\t" "s_nop 0\n\t" "global_load_lds_dwordx4 v226, s[42:43]\n\t" "s_add_u32 s42, %22, 0x498000\n\t" "s_add_u32 s42, s44, s42\n\t"
                     "s_addc_u32 s43, s45, 0\n\t" "s_add_u32 m0, %18, 0xc20\n\t" "s_nop 0\n\t" "global_load_lds_dwordx4 v224, s[42:43]\n\t" "s_add_u32 m0, %18, 0x1020\n\t" "s_nop 0\n\t"
                     "global_load_lds_dwordx4 v225, s[42:43]\n\t" "s_add_u32 m0, %18, 0x1420\n\t" "s_nop 0\n\t" "global_load_lds_dwordx4 v226, s[42:43]\n\t" "s_sub_u32 s42, 0x540000, %22\n\t" "s_add_u32 s42, s44, s42\n\t"
                     "s_addc_u32 s43, s45, 0\n\t" "s_add_u32 m0, %18, 0x6100\n\t" "s_nop 0\n\t" "global_load_lds_dwordx4 v224, s[42:43]\n\t" "s_add_u32 m0, %18, 0x6500\n\t" "s_nop 0\n\t"
                     "global_load_lds_dwordx4 v225, s[42:43]\n\t" "s_add_u32 m0, %18, 0x6900\n\t" "s_nop 0\n\t" "global_load_lds_dwordx4 v226, s[42:43]\n\t" "s_sub_u32 s42, 0x558000, %22\n\t" "s_add_u32 s42, s44, s42\n\t"
                     "s_addc_u32 s43, s45, 0\n\t" "s_add_u32 m0, %18, 0x6d20\n\t" "s_nop 0\n\t" "global_load_lds_dwordx4 v224, s[42:43]\n\t" "s_add_u32 m0, %18, 0x7120\n\t" "s_nop 0\n\t"
                     "global_load_lds_dwordx4 v225, s[42:43]\n\t" "s_add_u32 m0, %18, 0x7520\n\t" "s_nop 0\n\t" "global_load_lds_dwordx4 v226, s[42:43]\n\t" "v_add_u32 v237, 0xc200, v227\n\t"
                     "v_add_u32 v238, 0xc200, v228\n\t" "ds_read_b64_tr_b16 a[16:17], v237\n\t" "ds_read_b64_tr_b16 a[18:19], v237 offset:768\n\t" "ds_read_b64_tr_b16 a[20:21], v237 offset:12416\n\t"
                     "ds_read_b64_tr_b16 a[22:23], v237 offset:13184\n\t" "ds_read_b64_tr_b16 v[8:9], v237 offset:32\n\t" "ds_read_b64_tr_b16 v[10:11], v237 offset:800\n\t"
                     "ds_read_b64_tr_b16 v[12:13], v237 offset:12448\n\t" "ds_read_b64_tr_b16 v[14:15], v237 offset:13216\n\t" "ds_read_b64_tr_b16 a[24:25], v238 offset:64\n\t"
                     "ds_read_b64_tr_b16 a[26:27], v238 offset:832\n\t" "ds_read_b64_tr_b16 a[28:29], v238 offset:12480\n\t" "ds_read_b64_tr_b16 a[30:31], v238 offset:13248\n\t"
                     "ds_read_b64_tr_b16 a[48:49], v237 offset:24832\n\t" "ds_read_b64_tr_b16 a[50:51], v237 offset:25600\n\t" "ds_read_b64_tr_b16 a[52:53], v237 offset:37248\n\t"
                     "ds_read_b64_tr_b16 a[54:55], v237 offset:38016\n\t" "ds_read_b64_tr_b16 v[24:25], v237 offset:24864\n\t" "ds_read_b64_tr_b16 v[26:27], v237 offset:25632\n\t"
                     "ds_read_b64_tr_b16 v[28:29], v237 offset:37280\n\t" "ds_read_b64_tr_b16 v[30:31], v237 offset:38048\n\t" "ds_read_b64_tr_b16 a[56:57], v238 offset:24896\n\t"
                     "ds_read_b64_tr_b16 a[58:59], v238 offset:25664\n\t" "ds_read_b64_tr_b16 a[60:61], v238 offset:37312\n\t" "ds_read_b64_tr_b16 a[62:63], v238 offset:38080\n\t" "s_waitcnt vmcnt(12) lgkmcnt(0)\n\t"
                     "s_barrier\n\t" "s_add_u32 s42, %22, 0x600000\n\t" "s_add_u32 s42, s44, s42\n\t" "s_addc_u32 s43, s45, 0\n\t" "s_add_u32 m0, %18, 0xc200\n\t" "s_nop 0\n\t" "global_load_lds_dwordx4 v224, s[42:43]\n\t"
                     "s_add_u32 m0, %18, 0xc600\n\t" "s_nop 0\n\t" "global_load_lds_dwordx4 v225, s[42:43]\n\t" "s_add_u32 m0, %18, 0xca00\n\t" "s_nop 0\n\t" "global_load_lds_dwordx4 v226, s[42:43]\n\t"
                     "s_add_u32 s42, %22, 0x618000\n\t" "s_add_u32 s42, s44, s42\n\t" "s_addc_u32 s43, s45, 0\n\t" "s_add_u32 m0, %18, 0xce20\n\t" "s_nop 0\n\t" "global_load_lds_dwordx4 v224, s[42:43]\n\t"
                     "s_add_u32 m0, %18, 0xd220\n\t" "s_nop 0\n\t" "global_load_lds_dwordx4 v225, s[42:43]\n\t" "s_add_u32 m0, %18, 0xd620\n\t" "s_nop 0\n\t" "global_load_lds_dwordx4 v226, s[42:43]\n\t"
                     "s_sub_u32 s42, 0x6c0000, %22\n\t" "s_add_u32 s42, s44, s42\n\t" "s_addc_u32 s43, s45, 0\n\t" "s_add_u32 m0, %18, 0x12300\n\t" "s_nop 0\n\t" "global_load_lds_dwordx4 v224, s[42:43]\n\t"
                     "s_add_u32 m0, %18, 0x12700\n\t" "s_nop 0\n\t" "global_load_lds_dwordx4 v225, s[42:43]\n\t" "s_add_u32 m0, %18, 0x12b00\n\t" "s_nop 0\n\t" "global_load_lds_dwordx4 v226, s[42:43]\n\t"
                     "s_sub_u32 s42, 0x6d8000, %22\n\t" "s_add_u32 s42, s44, s42\n\t" "s_addc_u32 s43, s45, 0\n\t" "s_add_u32 m0, %18, 0x12f20\n\t" "s_nop 0\n\t" "global_load_lds_dwordx4 v224, s[42:43]\n\t"
                     "s_add_u32 m0, %18, 0x13320\n\t" "s_nop 0\n\t" "global_load_lds_dwordx4 v225, s[42:43]\n\t" "s_add_u32 m0, %18, 0x13720\n\t" "s_nop 0\n\t" "global_load_lds_dwordx4 v226, s[42:43]\n\t"
                     "v_add_u32 v237, 0x18400, v227\n\t" "v_add_u32 v238, 0x18400, v228\n\t" "ds_read_b64_tr_b16 a[64:65], v237\n\t" "ds_read_b64_tr_b16 a[66:67], v237 offset:768\n\t"
                     "ds_read_b64_tr_b16 a[68:69], v237 offset:12416\n\t" "ds_read_b64_tr_b16 a[70:71], v237 offset:13184\n\t" "ds_read_b64_tr_b16 v[32:33], v237 offset:32\n\t"
                     "ds_read_b64_tr_b16 v[34:35], v237 offset:800\n\t" "ds_read_b64_tr_b16 v[36:37], v237 offset:12448\n\t" "ds_read_b64_tr_b16 v[38:39], v237 offset:13216\n\t"
                     "ds_read_b64_tr_b16 a[72:73], v238 offset:64\n\t" "ds_read_b64_tr_b16 a[74:75], v238 offset:832\n\t" "ds_read_b64_tr_b16 a[76:77], v238 offset:12480\n\t"
                     "ds_read_b64_tr_b16 a[78:79], v238 offset:13248\n\t" "ds_read_b64_tr_b16 a[80:81], v237 offset:24832\n\t" "ds_read_b64_tr_b16 a[82:83], v237 offset:25600\n\t"
                     "ds_read_b64_tr_b16 a[84:85], v237 offset:37248\n\t" "ds_read_b64_tr_b16 a[86:87], v237 offset:38016\n\t" "ds_read_b64_tr_b16 v[40:41], v237 offset:24864\n\t"
                     "ds_read_b64_tr_b16 v[42:43], v237 offset:25632\n\t" "ds_read_b64_tr_b16 v[44:45], v237 offset:37280\n\t" "ds_read_b64_tr_b16 v[46:47], v237 offset:38048\n\t"
                     "ds_read_b64_tr_b16 a[88:89], v238 offset:24896\n\t" "ds_read_b64_tr_b16 a[90:91], v238 offset:25664\n\t" "ds_read_b64_tr_b16 a[92:93], v238 offset:37312\n\t"
                     "ds_read_b64_tr_b16 a[94:95], v238 offset:38080\n\t" "s_waitcnt vmcnt(12) lgkmcnt(0)\n\t" "s_barrier\n\t" "s_add_u32 s42, s46, 0x0\n\t" "s_addc_u32 s43, s47, 0\n\t" "s_add_u32 m0, %20, 0x18400\n\t"
                     "s_nop 0\n\t" "global_load_lds_dwordx4 v232, s[42:43]\n\t" "s_add_u32 m0, %20, 0x18800\n\t" "s_nop 0\n\t" "global_load_lds_dwordx4 v233, s[42:43]\n\t" "s_add_u32 m0, %20, 0x18c00\n\t" "s_nop 0\n\t"
                     "global_load_lds_dwordx4 v234, s[42:43]\n\t" "s_add_u32 m0, %20, 0x19000\n\t" "s_nop 0\n\t" "global_load_lds_dwordx4 v235, s[42:43]\n\t" "s_add_u32 m0, %20, 0x19400\n\t" "s_nop 0\n\t"
                     "global_load_lds_dwordx4 v236, s[42:43]\n\t" "s_add_u32 s42, s46, 0x1400\n\t" "s_addc_u32 s43, s47, 0\n\t" "s_add_u32 m0, %20, 0x19800\n\t" "v_xor_b32 v224, 0x80, v232\n\t"
                     "global_load_lds_dwordx4 v224, s[42:43]\n\t" "s_add_u32 m0, %20, 0x19c00\n\t" "v_xor_b32 v224, 0x80, v233\n\t" "global_load_lds_dwordx4 v224, s[42:43]\n\t" "s_add_u32 m0, %20, 0x1a000\n\t"
                     "v_xor_b32 v224, 0x80, v234\n\t" "global_load_lds_dwordx4 v224, s[42:43]\n\t" "s_add_u32 m0, %20, 0x1a400\n\t" "v_xor_b32 v224, 0x80, v235\n\t" "global_load_lds_dwordx4 v224, s[42:43]\n\t"
                     "s_add_u32 m0, %20, 0x1a800\n\t" "v_xor_b32 v224, 0x80, v236\n\t" "global_load_lds_dwordx4 v224, s[42:43]\n\t" "v_add_u32 v237, 0x0, v227\n\t" "v_add_u32 v238, 0x0, v228\n\t"
                     "ds_read_b64_tr_b16 a[96:97], v237\n\t" "ds_read_b64_tr_b16 a[98:99], v237 offset:768\n\t" "ds_read_b64_tr_b16 a[100:101], v237 offset:12416\n\t" "ds_read_b64_tr_b16 a[102:103], v237 offset:13184\n\t"
                     "ds_read_b64_tr_b16 v[48:49], v237 offset:32\n\t" "ds_read_b64_tr_b16 v[50:51], v237 offset:800\n\t" "ds_read_b64_tr_b16 v[52:53], v237 offset:12448\n\t"
                     "ds_read_b64_tr_b16 v[54:55], v237 offset:13216\n\t" "ds_read_b64_tr_b16 a[104:105], v238 offset:64\n\t" "ds_read_b64_tr_b16 a[106:107], v238 offset:832\n\t"
                     "ds_read_b64_tr_b16 a[108:109], v238 offset:12480\n\t" "ds_read_b64_tr_b16 a[110:111], v238 offset:13248\n\t" "ds_read_b64_tr_b16 a[112:113], v237 offset:24832\n\t"
                     "ds_read_b64_tr_b16 a[114:115], v237 offset:25600\n\t" "ds_read_b64_tr_b16 a[116:117], v237 offset:37248\n\t" "ds_read_b64_tr_b16 a[118:119], v237 offset:38016\n\t"
                     "ds_read_b64_tr_b16 v[56:57], v237 offset:24864\n\t" "ds_read_b64_tr_b16 v[58:59], v237 offset:25632\n\t" "ds_read_b64_tr_b16 v[60:61], v237 offset:37280\n\t"
                     "ds_read_b64_tr_b16 v[62:63], v237 offset:38048\n\t" "ds_read_b64_tr_b16 a[120:121], v238 offset:24896\n\t" "ds_read_b64_tr_b16 a[122:123], v238 offset:25664\n\t"
                     "ds_read_b64_tr_b16 a[124:125], v238 offset:37312\n\t" "ds_read_b64_tr_b16 a[126:127], v238 offset:38080\n\t" "s_waitcnt vmcnt(10) lgkmcnt(0)\n\t" "s_barrier\n\t" "s_add_u32 s42, s46, 0xa000\n\t"
                     "s_addc_u32 s43, s47, 0\n\t" "s_add_u32 m0, %20, 0x0\n\t" "s_nop 0\n\t" "global_load_lds_dwordx4 v232, s[42:43]\n\t" "s_add_u32 m0, %20, 0x400\n\t" "s_nop 0\n\t"
                     "global_load_lds_dwordx4 v233, s[42:43]\n\t" "s_add_u32 m0, %20, 0x800\n\t" "s_nop 0\n\t" "global_load_lds_dwordx4 v234, s[42:43]\n\t" "s_add_u32 m0, %20, 0xc00\n\t" "s_nop 0\n\t"
                     "global_load_lds_dwordx4 v235, s[42:43]\n\t" "s_add_u32 m0, %20, 0x1000\n\t" "s_nop 0\n\t" "global_load_lds_dwordx4 v236, s[42:43]\n\t" "s_add_u32 s42, s46, 0xb400\n\t" "s_addc_u32 s43, s47, 0\n\t"
                     "s_add_u32 m0, %20, 0x1400\n\t" "v_xor_b32 v224, 0x80, v232\n\t" "global_load_lds_dwordx4 v224, s[42:43]\n\t" "s_add_u32 m0, %20, 0x1800\n\t" "v_xor_b32 v224, 0x80, v233\n\t"
                     "global_load_lds_dwordx4 v224, s[42:43]\n\t" "s_add_u32 m0, %20, 0x1c00\n\t" "v_xor_b32 v224, 0x80, v234\n\t" "global_load_lds_dwordx4 v224, s[42:43]\n\t" "s_add_u32 m0, %20, 0x2000\n\t"
                     "v_xor_b32 v224, 0x80, v235\n\t" "global_load_lds_dwordx4 v224, s[42:43]\n\t" "s_add_u32 m0, %20, 0x2400\n\t" "v_xor_b32 v224, 0x80, v236\n\t" "global_load_lds_dwordx4 v224, s[42:43]\n\t"
                     "v_add_u32 v237, 0xc200, v227\n\t" "v_add_u32 v238, 0xc200, v228\n\t" "ds_read_b64_tr_b16 a[128:129], v237\n\t" "ds_read_b64_tr_b16 a[130:131], v237 offset:768\n\t"
                     "ds_read_b64_tr_b16 a[132:133], v237 offset:12416\n\t" "ds_read_b64_tr_b16 a[134:135], v237 offset:13184\n\t" "ds_read_b64_tr_b16 a[136:137], v237 offset:32\n\t"
                     "ds_read_b64_tr_b16 a[138:139], v237 offset:800\n\t" "ds_read_b64_tr_b16 a[140:141], v237 offset:12448\n\t" "ds_read_b64_tr_b16 a[142:143], v237 offset:13216\n\t"
                     "ds_read_b64_tr_b16 a[144:145], v238 offset:64\n\t" "ds_read_b64_tr_b16 a[146:147], v238 offset:832\n\t" "ds_read_b64_tr_b16 a[148:149], v238 offset:12480\n\t"
                     "ds_read_b64_tr_b16 a[150:151], v238 offset:13248\n\t" "ds_read_b64_tr_b16 a[152:153], v237 offset:24832\n\t" "ds_read_b64_tr_b16 a[154:155], v237 offset:25600\n\t"
                     "ds_read_b64_tr_b16 a[156:157], v237 offset:37248\n\t" "ds_read_b64_tr_b16 a[158:159], v237 offset:38016\n\t" "ds_read_b64_tr_b16 a[160:161], v237 offset:24864\n\t"
                     "ds_read_b64_tr_b16 a[162:163], v237 offset:25632\n\t" "ds_read_b64_tr_b16 a[164:165], v237 offset:37280\n\t" "ds_read_b64_tr_b16 a[166:167], v237 offset:38048\n\t"
                     "ds_read_b64_tr_b16 a[168:169], v238 offset:24896\n\t" "ds_read_b64_tr_b16 a[170:171], v238 offset:25664\n\t" "ds_read_b64_tr_b16 a[172:173], v238 offset:37312\n\t"
                     "ds_read_b64_tr_b16 a[174:175], v238 offset:38080\n\t" "s_waitcnt vmcnt(10) lgkmcnt(0)\n\t" "s_barrier\n\t" "s_add_u32 s42, s46, 0x14000\n\t" "s_addc_u32 s43, s47, 0\n\t"
                     "s_add_u32 m0, %20, 0xc200\n\t" "s_nop 0\n\t" "global_load_lds_dwordx4 v232, s[42:43]\n\t" "s_add_u32 m0, %20, 0xc600\n\t" "s_nop 0\n\t" "global_load_lds_dwordx4 v233, s[42:43]\n\t"
                     "s_add_u32 m0, %20, 0xca00\n\t" "s_nop 0\n\t" "global_load_lds_dwordx4 v234, s[42:43]\n\t" "s_add_u32 m0, %20, 0xce00\n\t" "s_nop 0\n\t" "global_load_lds_dwordx4 v235, s[42:43]\n\t"
                     "s_add_u32 m0, %20, 0xd200\n\t" "s_nop 0\n\t" "global_load_lds_dwordx4 v236, s[42:43]\n\t" "s_add_u32 s42, s46, 0x15400\n\t" "s_addc_u32 s43, s47, 0\n\t" "s_add_u32 m0, %20, 0xd600\n\t"
                     "v_xor_b32 v224, 0x80, v232\n\t" "global_load_lds_dwordx4 v224, s[42:43]\n\t" "s_add_u32 m0, %20, 0xda00\n\t" "v_xor_b32 v224, 0x80, v233\n\t" "global_load_lds_dwordx4 v224, s[42:43]\n\t"
                     "s_add_u32 m0, %20, 0xde00\n\t" "v_xor_b32 v224, 0x80, v234\n\t" "global_load_lds_dwordx4 v224, s[42:43]\n\t" "s_add_u32 m0, %20, 0xe200\n\t" "v_xor_b32 v224, 0x80, v235\n\t"
                     "global_load_lds_dwordx4 v224, s[42:43]\n\t" "s_add_u32 m0, %20, 0xe600\n\t" "v_xor_b32 v224, 0x80, v236\n\t" "global_load_lds_dwordx4 v224, s[42:43]\n\t" "s_add_u32 s42, %21, 0x18400\n\t"
                     "v_xor_b32 v237, 0x0, v229\n\t" "v_add_u32 v237, s42, v237\n\t" "v_xor_b32 v238, 0x40, v229\n\t" "v_add_u32 v238, s42, v238\n\t" "v_xor_b32 v239, 0x80, v229\n\t" "v_add_u32 v239, s42, v239\n\t"
                     "v_xor_b32 v226, 0xc0, v229\n\t" "v_add_u32 v226, s42, v226\n\t" "ds_read_b64_tr_b16 a[176:177], v237\n\t" "ds_read_b64_tr_b16 a[178:179], v238 offset:2560\n\t"
                     "ds_read_b64_tr_b16 a[200:201], v239\n\t" "ds_read_b64_tr_b16 a[202:203], v226 offset:2560\n\t" "ds_read_b64_tr_b16 a[224:225], v237 offset:256\n\t"
                     "ds_read_b64_tr_b16 a[226:227], v238 offset:2816\n\t" "ds_read_b64_tr_b16 a[248:249], v239 offset:256\n\t" "ds_read_b64_tr_b16 a[250:251], v226 offset:2816\n\t"
                     "ds_read_b64_tr_b16 v[112:113], v237 offset:512\n\t" "ds_read_b64_tr_b16 v[114:115], v238 offset:3072\n\t" "ds_read_b64_tr_b16 v[136:137], v239 offset:512\n\t"
                     "ds_read_b64_tr_b16 v[138:139], v226 offset:3072\n\t" "ds_read_b64_tr_b16 v[160:161], v237 offset:768\n\t" "ds_read_b64_tr_b16 v[162:163], v238 offset:3328\n\t"
                     "ds_read_b64_tr_b16 v[184:185], v239 offset:768\n\t" "ds_read_b64_tr_b16 v[186:187], v226 offset:3328\n\t" "ds_read_b64_tr_b16 v[208:209], v237 offset:1024\n\t"
                     "ds_read_b64_tr_b16 v[210:211], v238 offset:3584\n\t" "ds_read_b64_tr_b16 v[72:73], v239 offset:1024\n\t" "ds_read_b64_tr_b16 v[74:75], v226 offset:3584\n\t" "s_waitcnt vmcnt(10) lgkmcnt(0)\n\t"
                     "s_barrier\n\t" "s_add_u32 s42, s46, 0x1e000\n\t" "s_addc_u32 s43, s47, 0\n\t" "s_add_u32 m0, %20, 0x18400\n\t" "s_nop 0\n\t" "global_load_lds_dwordx4 v232, s[42:43]\n\t"
                     "s_add_u32 m0, %20, 0x18800\n\t" "s_nop 0\n\t" "global_load_lds_dwordx4 v233, s[42:43]\n\t" "s_add_u32 m0, %20, 0x18c00\n\t" "s_nop 0\n\t" "global_load_lds_dwordx4 v234, s[42:43]\n\t"
                     "s_add_u32 m0, %20, 0x19000\n\t" "s_nop 0\n\t" "global_load_lds_dwordx4 v235, s[42:43]\n\t" "s_add_u32 m0, %20, 0x19400\n\t" "s_nop 0\n\t" "global_load_lds_dwordx4 v236, s[42:43]\n\t"
                     "s_add_u32 s42, s46, 0x1f400\n\t" "s_addc_u32 s43, s47, 0\n\t" "s_add_u32 m0, %20, 0x19800\n\t" "v_xor_b32 v224, 0x80, v232\n\t" "global_load_lds_dwordx4 v224, s[42:43]\n\t"
                     "s_add_u32 m0, %20, 0x19c00\n\t" "v_xor_b32 v224, 0x80, v233\n\t" "global_load_lds_dwordx4 v224, s[42:43]\n\t" "s_add_u32 m0, %20, 0x1a000\n\t" "v_xor_b32 v224, 0x80, v234\n\t"
                     "global_load_lds_dwordx4 v224, s[42:43]\n\t" "s_add_u32 m0, %20, 0x1a400\n\t" "v_xor_b32 v224, 0x80, v235\n\t" "global_load_lds_dwordx4 v224, s[42:43]\n\t" "s_add_u32 m0, %20, 0x1a800\n\t"
                     "v_xor_b32 v224, 0x80, v236\n\t" "global_load_lds_dwordx4 v224, s[42:43]\n\t" "s_add_u32 s42, %21, 0x0\n\t" "v_xor_b32 v237, 0x0, v229\n\t" "v_add_u32 v237, s42, v237\n\t"
                     "v_xor_b32 v238, 0x40, v229\n\t" "v_add_u32 v238, s42, v238\n\t" "v_xor_b32 v239, 0x80, v229\n\t" "v_add_u32 v239, s42, v239\n\t" "v_xor_b32 v226, 0xc0, v229\n\t" "v_add_u32 v226, s42, v226\n\t"
                     "ds_read_b64_tr_b16 a[180:181], v237\n\t" "ds_read_b64_tr_b16 a[182:183], v238 offset:2560\n\t" "ds_read_b64_tr_b16 a[204:205], v239\n\t" "ds_read_b64_tr_b16 a[206:207], v226 offset:2560\n\t"
                     "ds_read_b64_tr_b16 a[228:229], v237 offset:256\n\t" "ds_read_b64_tr_b16 a[230:231], v238 offset:2816\n\t" "ds_read_b64_tr_b16 a[252:253], v239 offset:256\n\t"
                     "ds_read_b64_tr_b16 a[254:255], v226 offset:2816\n\t" "ds_read_b64_tr_b16 v[116:117], v237 offset:512\n\t" "ds_read_b64_tr_b16 v[118:119], v238 offset:3072\n\t"
                     "ds_read_b64_tr_b16 v[140:141], v239 offset:512\n\t" "ds_read_b64_tr_b16 v[142:143], v226 offset:3072\n\t" "ds_read_b64_tr_b16 v[164:165], v237 offset:768\n\t"
                     "ds_read_b64_tr_b16 v[166:167], v238 offset:3328\n\t" "ds_read_b64_tr_b16 v[188:189], v239 offset:768\n\t" "ds_read_b64_tr_b16 v[190:191], v226 offset:3328\n\t"
                     "ds_read_b64_tr_b16 v[212:213], v237 offset:1024\n\t" "ds_read_b64_tr_b16 v[214:215], v238 offset:3584\n\t" "ds_read_b64_tr_b16 v[76:77], v239 offset:1024\n\t"
                     "ds_read_b64_tr_b16 v[78:79], v226 offset:3584\n\t" "s_waitcnt vmcnt(10) lgkmcnt(0)\n\t" "s_barrier\n\t" "v_mov_b32 v225, 0\n\t" "global_load_dwordx2 %12, v231, %19 sc1\n\t"
                     "global_load_dwordx2 %13, v225, %19 offset:512 sc1\n\t" "global_load_dwordx2 %14, v225, %19 offset:1024 sc1\n\t" "s_add_u32 s42, s46, 0x28000\n\t" "s_addc_u32 s43, s47, 0\n\t"
                     "s_add_u32 m0, %20, 0x0\n\t" "s_nop 0\n\t" "global_load_lds_dwordx4 v232, s[42:43]\n\t" "s_add_u32 m0, %20, 0x400\n\t" "s_nop 0\n\t" "global_load_lds_dwordx4 v233, s[42:43]\n\t"
                     "s_add_u32 m0, %20, 0x800\n\t" "s_nop 0\n\t" "global_load_lds_dwordx4 v234, s[42:43]\n\t" "s_add_u32 m0, %20, 0xc00\n\t" "s_nop 0\n\t" "global_load_lds_dwordx4 v235, s[42:43]\n\t"
                     "s_add_u32 m0, %20, 0x1000\n\t" "s_nop 0\n\t" "global_load_lds_dwordx4 v236, s[42:43]\n\t" "s_add_u32 s42, s46, 0x29400\n\t" "s_addc_u32 s43, s47, 0\n\t" "s_add_u32 m0, %20, 0x1400\n\t"
                     "v_xor_b32 v224, 0x80, v232\n\t" "global_load_lds_dwordx4 v224, s[42:43]\n\t" "s_add_u32 m0, %20, 0x1800\n\t" "v_xor_b32 v224, 0x80, v233\n\t" "global_load_lds_dwordx4 v224, s[42:43]\n\t"
                     "s_add_u32 m0, %20, 0x1c00\n\t" "v_xor_b32 v224, 0x80, v234\n\t" "global_load_lds_dwordx4 v224, s[42:43]\n\t" "s_add_u32 m0, %20, 0x2000\n\t" "v_xor_b32 v224, 0x80, v235\n\t"
                     "global_load_lds_dwordx4 v224, s[42:43]\n\t" "s_add_u32 m0, %20, 0x2400\n\t" "v_xor_b32 v224, 0x80, v236\n\t" "global_load_lds_dwordx4 v224, s[42:43]\n\t" "s_add_u32 s42, %21, 0xc200\n\t"
                     "v_xor_b32 v237, 0x0, v229\n\t" "v_add_u32 v237, s42, v237\n\t" "v_xor_b32 v238, 0x40, v229\n\t" "v_add_u32 v238, s42, v238\n\t" "v_xor_b32 v239, 0x80, v229\n\t" "v_add_u32 v239, s42, v239\n\t"
                     "v_xor_b32 v226, 0xc0, v229\n\t" "v_add_u32 v226, s42, v226\n\t" "ds_read_b64_tr_b16 a[184:185], v237\n\t" "ds_read_b64_tr_b16 a[186:187], v238 offset:2560\n\t"
                     "ds_read_b64_tr_b16 a[208:209], v239\n\t" "ds_read_b64_tr_b16 a[210:211], v226 offset:2560\n\t" "ds_read_b64_tr_b16 a[232:233], v237 offset:256\n\t"
                     "ds_read_b64_tr_b16 a[234:235], v238 offset:2816\n\t" "ds_read_b64_tr_b16 v[96:97], v239 offset:256\n\t" "ds_read_b64_tr_b16 v[98:99], v226 offset:2816\n\t"
                     "ds_read_b64_tr_b16 v[120:121], v237 offset:512\n\t" "ds_read_b64_tr_b16 v[122:123], v238 offset:3072\n\t" "ds_read_b64_tr_b16 v[144:145], v239 offset:512\n\t"
                     "ds_read_b64_tr_b16 v[146:147], v226 offset:3072\n\t" "ds_read_b64_tr_b16 v[168:169], v237 offset:768\n\t" "ds_read_b64_tr_b16 v[170:171], v238 offset:3328\n\t"
                     "ds_read_b64_tr_b16 v[192:193], v239 offset:768\n\t" "ds_read_b64_tr_b16 v[194:195], v226 offset:3328\n\t" "ds_read_b64_tr_b16 v[216:217], v237 offset:1024\n\t"
                     "ds_read_b64_tr_b16 v[218:219], v238 offset:3584\n\t" "ds_read_b64_tr_b16 v[80:81], v239 offset:1024\n\t" "ds_read_b64_tr_b16 v[82:83], v226 offset:3584\n\t" "s_waitcnt vmcnt(10) lgkmcnt(0)\n\t"
                     "s_barrier\n\t" "s_add_u32 s42, s46, 0x32000\n\t" "s_addc_u32 s43, s47, 0\n\t" "s_add_u32 m0, %20, 0xc200\n\t" "s_nop 0\n\t" "global_load_lds_dwordx4 v232, s[42:43]\n\t" "s_add_u32 m0, %20, 0xc600\n\t"
                     "s_nop 0\n\t" "global_load_lds_dwordx4 v233, s[42:43]\n\t" "s_add_u32 m0, %20, 0xca00\n\t" "s_nop 0\n\t" "global_load_lds_dwordx4 v234, s[42:43]\n\t" "s_add_u32 m0, %20, 0xce00\n\t" "s_nop 0\n\t"
                     "global_load_lds_dwordx4 v235, s[42:43]\n\t" "s_add_u32 m0, %20, 0xd200\n\t" "s_nop 0\n\t" "global_load_lds_dwordx4 v236, s[42:43]\n\t" "s_add_u32 s42, s46, 0x33400\n\t" "s_addc_u32 s43, s47, 0\n\t"
                     "s_add_u32 m0, %20, 0xd600\n\t" "v_xor_b32 v224, 0x80, v232\n\t" "global_load_lds_dwordx4 v224, s[42:43]\n\t" "s_add_u32 m0, %20, 0xda00\n\t" "v_xor_b32 v224, 0x80, v233\n\t"
                     "global_load_lds_dwordx4 v224, s[42:43]\n\t" "s_add_u32 m0, %20, 0xde00\n\t" "v_xor_b32 v224, 0x80, v234\n\t" "global_load_lds_dwordx4 v224, s[42:43]\n\t" "s_add_u32 m0, %20, 0xe200\n\t"
                     "v_xor_b32 v224, 0x80, v235\n\t" "global_load_lds_dwordx4 v224, s[42:43]\n\t" "s_add_u32 m0, %20, 0xe600\n\t" "v_xor_b32 v224, 0x80, v236\n\t" "global_load_lds_dwordx4 v224, s[42:43]\n\t"
                     "s_add_u32 s42, %21, 0x18400\n\t" "v_xor_b32 v237, 0x0, v229\n\t" "v_add_u32 v237, s42, v237\n\t" "v_xor_b32 v238, 0x40, v229\n\t" "v_add_u32 v238, s42, v238\n\t" "v_xor_b32 v239, 0x80, v229\n\t"
                     "v_add_u32 v239, s42, v239\n\t" "v_xor_b32 v226, 0xc0, v229\n\t" "v_add_u32 v226, s42, v226\n\t" "ds_read_b64_tr_b16 a[188:189], v237\n\t" "ds_read_b64_tr_b16 a[190:191], v238 offset:2560\n\t"
                     "ds_read_b64_tr_b16 a[212:213], v239\n\t" "ds_read_b64_tr_b16 a[214:215], v226 offset:2560\n\t" "ds_read_b64_tr_b16 a[236:237], v237 offset:256\n\t"
                     "ds_read_b64_tr_b16 a[238:239], v238 offset:2816\n\t" "ds_read_b64_tr_b16 v[100:101], v239 offset:256\n\t" "ds_read_b64_tr_b16 v[102:103], v226 offset:2816\n\t"
                     "ds_read_b64_tr_b16 v[124:125], v237 offset:512\n\t" "ds_read_b64_tr_b16 v[126:127], v238 offset:3072\n\t" "ds_read_b64_tr_b16 v[148:149], v239 offset:512\n\t"
                     "ds_read_b64_tr_b16 v[150:151], v226 offset:3072\n\t" "ds_read_b64_tr_b16 v[172:173], v237 offset:768\n\t" "ds_read_b64_tr_b16 v[174:175], v238 offset:3328\n\t"
                     "ds_read_b64_tr_b16 v[196:197], v239 offset:768\n\t" "ds_read_b64_tr_b16 v[198:199], v226 offset:3328\n\t" "ds_read_b64_tr_b16 v[220:221], v237 offset:1024\n\t"
                     "ds_read_b64_tr_b16 v[222:223], v238 offset:3584\n\t" "ds_read_b64_tr_b16 v[84:85], v239 offset:1024\n\t" "ds_read_b64_tr_b16 v[86:87], v226 offset:3584\n\t" "s_waitcnt vmcnt(10) lgkmcnt(0)\n\t"
                     "s_barrier\n\t" "s_add_u32 s42, %21, 0x0\n\t" "v_xor_b32 v237, 0x0, v229\n\t" "v_add_u32 v237, s42, v237\n\t" "v_xor_b32 v238, 0x40, v229\n\t" "v_add_u32 v238, s42, v238\n\t"
                     "v_xor_b32 v239, 0x80, v229\n\t" "v_add_u32 v239, s42, v239\n\t" "v_xor_b32 v226, 0xc0, v229\n\t" "v_add_u32 v226, s42, v226\n\t" "ds_read_b64_tr_b16 a[192:193], v237\n\t"
                     "ds_read_b64_tr_b16 a[194:195], v238 offset:2560\n\t" "ds_read_b64_tr_b16 a[216:217], v239\n\t" "ds_read_b64_tr_b16 a[218:219], v226 offset:2560\n\t"
                     "ds_read_b64_tr_b16 a[240:241], v237 offset:256\n\t" "ds_read_b64_tr_b16 a[242:243], v238 offset:2816\n\t" "ds_read_b64_tr_b16 v[104:105], v239 offset:256\n\t"
                     "ds_read_b64_tr_b16 v[106:107], v226 offset:2816\n\t" "ds_read_b64_tr_b16 v[128:129], v237 offset:512\n\t" "ds_read_b64_tr_b16 v[130:131], v238 offset:3072\n\t"
                     "ds_read_b64_tr_b16 v[152:153], v239 offset:512\n\t" "ds_read_b64_tr_b16 v[154:155], v226 offset:3072\n\t" "ds_read_b64_tr_b16 v[176:177], v237 offset:768\n\t"
                     "ds_read_b64_tr_b16 v[178:179], v238 offset:3328\n\t" "ds_read_b64_tr_b16 v[200:201], v239 offset:768\n\t" "ds_read_b64_tr_b16 v[202:203], v226 offset:3328\n\t"
                     "ds_read_b64_tr_b16 v[64:65], v237 offset:1024\n\t" "ds_read_b64_tr_b16 v[66:67], v238 offset:3584\n\t" "ds_read_b64_tr_b16 v[88:89], v239 offset:1024\n\t"
                     "ds_read_b64_tr_b16 v[90:91], v226 offset:3584\n\t" "s_waitcnt vmcnt(0) lgkmcnt(0)\n\t" "s_barrier\n\t" "s_add_u32 s42, %21, 0xc200\n\t" "v_xor_b32 v237, 0x0, v229\n\t" "v_add_u32 v237, s42, v237\n\t"
                     "v_xor_b32 v238, 0x40, v229\n\t" "v_add_u32 v238, s42, v238\n\t" "v_xor_b32 v239, 0x80, v229\n\t" "v_add_u32 v239, s42, v239\n\t" "v_xor_b32 v226, 0xc0, v229\n\t" "v_add_u32 v226, s42, v226\n\t"
                     "ds_read_b64_tr_b16 a[196:197], v237\n\t" "ds_read_b64_tr_b16 a[198:199], v238 offset:2560\n\t" "ds_read_b64_tr_b16 a[220:221], v239\n\t" "ds_read_b64_tr_b16 a[222:223], v226 offset:2560\n\t"
                     "ds_read_b64_tr_b16 a[244:245], v237 offset:256\n\t" "ds_read_b64_tr_b16 a[246:247], v238 offset:2816\n\t" "ds_read_b64_tr_b16 v[108:109], v239 offset:256\n\t"
                     "ds_read_b64_tr_b16 v[110:111], v226 offset:2816\n\t" "ds_read_b64_tr_b16 v[132:133], v237 offset:512\n\t" "ds_read_b64_tr_b16 v[134:135], v238 offset:3072\n\t"
                     "ds_read_b64_tr_b16 v[156:157], v239 offset:512\n\t" "ds_read_b64_tr_b16 v[158:159], v226 offset:3072\n\t" "ds_read_b64_tr_b16 v[180:181], v237 offset:768\n\t"
                     "ds_read_b64_tr_b16 v[182:183], v238 offset:3328\n\t" "ds_read_b64_tr_b16 v[204:205], v239 offset:768\n\t" "ds_read_b64_tr_b16 v[206:207], v226 offset:3328\n\t"
                     "ds_read_b64_tr_b16 v[68:69], v237 offset:1024\n\t" "ds_read_b64_tr_b16 v[70:71], v238 offset:3584\n\t" "ds_read_b64_tr_b16 v[92:93], v239 offset:1024\n\t"
                     "ds_read_b64_tr_b16 v[94:95], v226 offset:3584\n\t" "s_waitcnt lgkmcnt(0)\n\t" "ds_write_b128 v230, v[0:3]\n\t" "ds_write_b128 v230, v[4:7] offset:1024\n\t"
                     "ds_write_b128 v230, v[8:11] offset:2048\n\t" "ds_write_b128 v230, v[12:15] offset:3072\n\t" "ds_write_b128 v230, v[16:19] offset:4096\n\t" "ds_write_b128 v230, v[20:23] offset:5120\n\t"
                     "ds_write_b128 v230, v[24:27] offset:6144\n\t" "ds_write_b128 v230, v[28:31] offset:7168\n\t" "ds_write_b128 v230, v[32:35] offset:8192\n\t" "ds_write_b128 v230, v[36:39] offset:9216\n\t"
                     "ds_write_b128 v230, v[40:43] offset:10240\n\t" "ds_write_b128 v230, v[44:47] offset:11264\n\t" "ds_write_b128 v230, v[48:51] offset:12288\n\t" "ds_write_b128 v230, v[52:55] offset:13312\n\t"
                     "ds_write_b128 v230, v[56:59] offset:14336\n\t" "ds_write_b128 v230, v[60:63] offset:15360\n\t" "ds_write_b128 v230, v[64:67] offset:16384\n\t" "ds_write_b128 v230, v[68:71] offset:17408\n\t"
                     "ds_write_b128 v230, v[72:75] offset:18432\n\t" "ds_write_b128 v230, v[76:79] offset:19456\n\t" "ds_write_b128 v230, v[80:83] offset:20480\n\t" "ds_write_b128 v230, v[84:87] offset:21504\n\t"
                     "ds_write_b128 v230, v[88:91] offset:22528\n\t" "ds_write_b128 v230, v[92:95] offset:23552\n\t"
                     : "=&{a[0:31]}"(wa[0]), "=&{a[32:63]}"(wa[1]), "=&{a[64:95]}"(wa[2]), "=&{a[96:127]}"(wa[3]), "=&{a[128:159]}"(wa[4]), "=&{a[160:191]}"(wa[5]), "=&{a[192:223]}"(wa[6]), "=&{a[224:255]}"(wa[7]), "=&{v[96:127]}"(wv[0]), "=&{v[128:159]}"(wv[1]), "=&{v[160:191]}"(wv[2]), "=&{v[192:223]}"(wv[3]), "=&v"(y.r), "=&v"(y.g), "=&v"(y.e), "+{v[224:239]}"(pa)
                     : "{s[44:45]}"(p.wv + h * DH * 2 + (size_t)(16 * w) * (HD * 2)), "{s[46:47]}"(p.wo + (size_t)(h * DH + 8 * w) * 1280),
                       "s"(lds + 2 * w * GS), "s"(p.ws + OFF_LAT), "s"(lds + w * 10240), "s"(lds), "s"((h & 4) * 16 * HD * 2)
                     : "v0", "v1", "v2", "v3", "v4", "v5", "v6", "v7", "v8", "v9", "v10", "v11", "v12", "v13", "v14", "v15", "v16", "v17", "v18", "v19", "v20", "v21", "v22", "v23", "v24", "v25", "v26", "v27", "v28", "v29", "v30", "v31", "v32", "v33", "v34", "v35", "v36", "v37", "v38", "v39", "v40", "v41", "v42", "v43", "v44", "v45", "v46", "v47", "v48", "v49", "v50", "v51", "v52", "v53", "v54", "v55", "v56", "v57", "v58", "v59", "v60", "v61", "v62", "v63", "v64", "v65", "v66", "v67", "v68", "v69", "v70", "v71", "v72", "v73", "v74", "v75", "v76", "v77", "v78", "v79", "v80", "v81", "v82", "v83", "v84", "v85", "v86", "v87", "v88", "v89", "v90", "v91", "v92", "v93", "v94", "v95", "s42", "s43", "scc", "memory");
#pragma unroll
        for (int i = UPF; i < 8; ++i) {
            const int l = 64 * min(i * 4 + w, 29) + lanex();
            pfl(p.wq + h * 384, (u32)((l / 3) * 12288 + (l % 3) * 128), lds);
        }
        if (BPF) {
#pragma unroll
            for (int i = 0; i < 3; ++i) pfl(p.wqkvb, (u32)min(h * 768 + i * 256 + w * 64 + lanex(), 19199) * 128, lds);
            pfl(p.wob, (u32)min(h * 256 + w * 64 + lanex(), 6399) * 128, lds);
            if (h == 31) pfl(p.gw, (u32)min(lanex(), 9) * 128, lds);
        }
        sd = sync_end(y, p.ws, phi, w == 0);
        barrier();
    }
    const int lane = lanex(), tid = 64 * w + lane;
    const int n = lane & 15, g = lane >> 4, qq = (lane >> 2) & 3, pp = lane & 3;
    char* const fl = p.ws + OFF_ST;
    const u32 iw = lds + 65536 + ((lane * 8) ^ (lane & 32));
    const u32 qr0 = lds + 65536 + (g >> 1) * 512 + pp * 128 + 64 * (g & 1) + 8 * qq + 32 * (pp >> 1);
    const u32 uo = (u32)((4 * w + (n >> 3)) * 4096 + h * 128 + (n & 7) * 16);
    u32x16 ad = u32x16{(u32)lane * 16 + 1024 + (h & 4) * 256, (lane & 2) ? 0xEEEEu : 0x4444u, lds + (u32)((w & 1) * 24576 + (w >> 1) * 99328 + lane * 16), iw + ((3 * w + (w & 1)) >> 1) * 512,
                       iw + (1 + 3 * (w >> 1)) * 512 + 4 * (w & 1), qr0, qr0 ^ 32, uo + (g & 1) * 8192 + (1 + (g >> 1)) * 65536, uo + (g & 1) * 8192 + (g >> 1) * 8, uo, (u32)lane * 16 + 1024 - (h & 4) * 256, uo, uo, uo, uo, uo};
    asm volatile("" : "+{v[224:239]}"(ad));
    const char* memp = p.ws + mslot(sd, 0);
    int budget = 30000;
    Clock clk; clk.init();
    for (int s = 0; s < NSEG; ++s) {
        int tries = 0;
        u64 bad, tp8, en = ~0ull;
        while (true) {
            const u32 tgt = clk.sched(phi, 0, cfg.G, tries ? 2 : 0);
            char* up = p.ws + uslot(sd, s);
            asm volatile("ds_read_b128 v[64:67], v226\n\t" "ds_read_b128 v[68:71], v226 offset:1024\n\t" "ds_read_b128 v[72:75], v226 offset:2048\n\t" "ds_read_b128 v[76:79], v226 offset:3072\n\t"
                         "ds_read_b128 v[80:83], v226 offset:4096\n\t" "ds_read_b128 v[84:87], v226 offset:5120\n\t" "ds_read_b128 v[88:91], v226 offset:6144\n\t" "ds_read_b128 v[92:95], v226 offset:7168\n\t"
                         "v_mov_b64 v[40:41], 0\n\t" "v_mov_b64 v[42:43], 0\n\t" "v_mov_b64 v[44:45], 0\n\t" "v_mov_b64 v[46:47], 0\n\t" "v_mov_b64 v[48:49], 0\n\t" "v_mov_b64 v[50:51], 0\n\t" ".Lsp_u:\n\t" "s_memtime vcc\n\t"
                         "s_waitcnt lgkmcnt(0)\n\t" "s_sub_i32 s42, %3, vcc_lo\n\t" "s_cmp_gt_i32 s42, 80\n\t" "s_cbranch_scc1 .Lsp_u\n\t" "s_max_i32 s42, s42, 0\n\t" "s_sub_i32 s42, 92, s42\n\t" "s_and_b32 s42, s42, -4\n\t"
                         "s_getpc_b64 vcc\n\t" "s_add_u32 vcc_lo, vcc_lo, s42\n\t" "s_addc_u32 vcc_hi, vcc_hi, 0\n\t" "s_setpc_b64 vcc\n\t" "s_nop 0\n\t" "s_nop 0\n\t" "s_nop 0\n\t" "s_nop 0\n\t" "s_nop 0\n\t" "s_nop 0\n\t"
                         "s_nop 0\n\t" "s_nop 0\n\t" "s_nop 0\n\t" "s_nop 0\n\t" "s_nop 0\n\t" "s_nop 0\n\t" "s_nop 0\n\t" "s_nop 0\n\t" "s_nop 0\n\t" "s_nop 0\n\t" "s_nop 0\n\t" "s_nop 0\n\t" "s_nop 0\n\t" "s_nop 0\n\t"
                         "global_load_dwordx4 v[0:3], v224, %4 offset:-4096 sc1\n\t" "s_nop 10\n\t" "global_load_dwordx4 v[4:7], v224, %4 offset:-2048 sc1\n\t" "s_nop 15\n\t"
                         "global_load_dwordx4 v[8:11], v234, %4 offset:-3072 sc1\n\t" "s_nop 10\n\t" "global_load_dwordx4 v[12:15], v234, %4 offset:-1024 sc1\n\t" "s_nop 10\n\t" "global_load_dwordx4 v[16:19], v224, %4 sc1\n\t"
                         "s_nop 10\n\t" "global_load_dwordx4 v[20:23], v234, %4 offset:1024 sc1\n\t" "s_nop 10\n\t" "global_load_dwordx4 v[24:27], v224, %4 offset:2048 sc1\n\t" "s_nop 10\n\t"
                         "global_load_dwordx4 v[28:31], v234, %4 offset:3072 sc1\n\t" "s_nop 10\n\t" "global_load_dwordx4 v[32:35], v224, %5 offset:-4096 sc1\n\t" "s_nop 10\n\t"
                         "global_load_dwordx4 v[36:39], v234, %5 offset:-3072 sc1\n\t" "s_waitcnt vmcnt(9)\n\t" "v_smfmac_f32_16x16x64_bf16 v[40:43], v[0:3], a[0:7], v225\n\t"
                         "v_smfmac_f32_16x16x64_bf16 v[44:47], v[0:3], v[64:71], v225\n\t" "ds_read_b128 v[64:67], v226 offset:8192\n\t" "v_smfmac_f32_16x16x64_bf16 v[48:51], v[0:3], a[8:15], v225\n\t"
                         "ds_read_b128 v[68:71], v226 offset:9216\n\t" "v_cmp_eq_u32 vcc, -1, v0\n\t" "s_waitcnt vmcnt(8)\n\t" "v_smfmac_f32_16x16x64_bf16 v[40:43], v[4:7], a[16:23], v225\n\t" "v_mov_b64 v[0:1], 0\n\t"
                         "v_smfmac_f32_16x16x64_bf16 v[44:47], v[4:7], v[72:79], v225\n\t" "s_and_b64 vcc, vcc, %6\n\t" "s_cbranch_scc1 .Lsk_u\n\t" "ds_read_b128 v[72:75], v226 offset:10240\n\t" "v_mov_b64 v[2:3], 0\n\t"
                         "v_smfmac_f32_16x16x64_bf16 v[48:51], v[4:7], a[24:31], v225\n\t" "ds_read_b128 v[76:79], v226 offset:11264\n\t" "s_waitcnt vmcnt(7)\n\t"
                         "v_smfmac_f32_16x16x64_bf16 v[40:43], v[8:11], a[32:39], v225\n\t" "v_mov_b64 v[4:5], 0\n\t" "v_smfmac_f32_16x16x64_bf16 v[44:47], v[8:11], v[80:87], v225\n\t"
                         "ds_read_b128 v[80:83], v226 offset:12288\n\t" "v_mov_b64 v[6:7], 0\n\t" "v_smfmac_f32_16x16x64_bf16 v[48:51], v[8:11], a[40:47], v225\n\t" "ds_read_b128 v[84:87], v226 offset:13312\n\t"
                         "s_waitcnt vmcnt(6)\n\t" "v_smfmac_f32_16x16x64_bf16 v[40:43], v[12:15], a[48:55], v225\n\t" "v_mov_b64 v[8:9], 0\n\t" "v_smfmac_f32_16x16x64_bf16 v[44:47], v[12:15], v[88:95], v225\n\t"
                         "ds_read_b128 v[88:91], v226 offset:14336\n\t" "v_mov_b64 v[10:11], 0\n\t" "v_smfmac_f32_16x16x64_bf16 v[48:51], v[12:15], a[56:63], v225\n\t" "ds_read_b128 v[92:95], v226 offset:15360\n\t"
                         "s_waitcnt vmcnt(5)\n\t" "v_smfmac_f32_16x16x64_bf16 v[40:43], v[16:19], a[64:71], v225\n\t" "v_mov_b64 v[12:13], 0\n\t" "s_waitcnt lgkmcnt(6)\n\t"
                         "v_smfmac_f32_16x16x64_bf16 v[44:47], v[16:19], v[64:71], v225\n\t" "ds_read_b128 v[64:67], v226 offset:16384\n\t" "v_mov_b64 v[14:15], 0\n\t"
                         "v_smfmac_f32_16x16x64_bf16 v[48:51], v[16:19], a[72:79], v225\n\t" "ds_read_b128 v[68:71], v226 offset:17408\n\t" "s_waitcnt vmcnt(4)\n\t"
                         "v_smfmac_f32_16x16x64_bf16 v[40:43], v[20:23], a[80:87], v225\n\t" "v_mov_b64 v[16:17], 0\n\t" "s_waitcnt lgkmcnt(6)\n\t" "v_smfmac_f32_16x16x64_bf16 v[44:47], v[20:23], v[72:79], v225\n\t"
                         "ds_read_b128 v[72:75], v226 offset:18432\n\t" "v_mov_b64 v[18:19], 0\n\t" "v_smfmac_f32_16x16x64_bf16 v[48:51], v[20:23], a[88:95], v225\n\t" "ds_read_b128 v[76:79], v226 offset:19456\n\t"
                         "s_waitcnt vmcnt(3)\n\t" "v_smfmac_f32_16x16x64_bf16 v[40:43], v[24:27], a[96:103], v225\n\t" "v_mov_b64 v[20:21], 0\n\t" "s_waitcnt lgkmcnt(6)\n\t"
                         "v_smfmac_f32_16x16x64_bf16 v[44:47], v[24:27], v[80:87], v225\n\t" "ds_read_b128 v[80:83], v226 offset:20480\n\t" "v_mov_b64 v[22:23], 0\n\t"
                         "v_smfmac_f32_16x16x64_bf16 v[48:51], v[24:27], a[104:111], v225\n\t" "ds_read_b128 v[84:87], v226 offset:21504\n\t" "s_waitcnt vmcnt(2)\n\t"
                         "v_smfmac_f32_16x16x64_bf16 v[40:43], v[28:31], a[112:119], v225\n\t" "v_mov_b64 v[24:25], 0\n\t" "s_waitcnt lgkmcnt(6)\n\t" "v_smfmac_f32_16x16x64_bf16 v[44:47], v[28:31], v[88:95], v225\n\t"
                         "ds_read_b128 v[88:91], v226 offset:22528\n\t" "v_mov_b64 v[26:27], 0\n\t" "v_smfmac_f32_16x16x64_bf16 v[48:51], v[28:31], a[120:127], v225\n\t" "ds_read_b128 v[92:95], v226 offset:23552\n\t"
                         "s_waitcnt vmcnt(1)\n\t" "v_smfmac_f32_16x16x64_bf16 v[40:43], v[32:35], a[128:135], v225\n\t" "v_mov_b64 v[28:29], 0\n\t" "v_smfmac_f32_16x16x64_bf16 v[44:47], v[32:35], a[136:143], v225\n\t"
                         "v_mov_b64 v[30:31], 0\n\t" "v_smfmac_f32_16x16x64_bf16 v[48:51], v[32:35], a[144:151], v225\n\t" "s_waitcnt vmcnt(0)\n\t" "v_smfmac_f32_16x16x64_bf16 v[40:43], v[36:39], a[152:159], v225\n\t"
                         "v_mov_b64 v[32:33], 0\n\t" "v_smfmac_f32_16x16x64_bf16 v[44:47], v[36:39], a[160:167], v225\n\t" "v_mov_b64 v[34:35], 0\n\t" "v_smfmac_f32_16x16x64_bf16 v[48:51], v[36:39], a[168:175], v225\n\t"
                         "v_add_f32 v52, v40, v42\n\t" "v_add_f32 v53, v41, v43\n\t" "v_cmp_u_f32 vcc, v52, v53\n\t" "v_add_f32 v54, v44, v46\n\t" "v_add_f32 v55, v45, v47\n\t" "v_cvt_pk_bf16_f32 v58, v52, v53\n\t"
                         "v_pk_add_f32 v[56:57], v[48:49], v[50:51]\n\t" "v_cvt_pk_bf16_f32 v59, v54, v55\n\t" "v_cvt_pk_bf16_f32 v60, v56, v57\n\t" "ds_write_b64 v227, v[58:59]\n\t" "ds_write_b32 v228, v60\n\t"
                         "v_mov_b64 v[36:37], 0\n\t" "v_mov_b64 v[38:39], 0\n\t" "s_and_b64 vcc, vcc, %6\n\t" "v_mov_b32 v233, 0\n\t" "s_cbranch_scc1 .Lsk_u\n\t" "v_mov_b32 v233, 0\n\t" "v_mov_b32 v233, 0\n\t"
                         "v_mov_b32 v233, 0\n\t" "s_waitcnt lgkmcnt(0)\n\t" "s_barrier\n\t" "ds_read_b64_tr_b16 v[40:41], v229\n\t" "ds_read_b64_tr_b16 v[42:43], v230\n\t" "v_mov_b32 v233, 0\n\t"
                         "ds_read_b64_tr_b16 v[44:45], v229 offset:1024\n\t" "v_mov_b32 v233, 0\n\t" "ds_read_b64_tr_b16 v[46:47], v230 offset:1024\n\t" "v_mov_b32 v233, 0\n\t"
                         "ds_read_b64_tr_b16 v[48:49], v229 offset:2048\n\t" "v_mov_b32 v233, 0\n\t" "ds_read_b64_tr_b16 v[50:51], v230 offset:2048\n\t" "v_mov_b32 v233, 0\n\t" "s_waitcnt lgkmcnt(4)\n\t"
                         "v_smfmac_f32_16x16x64_bf16 v[0:3], v[40:43], a[176:183], v225\n\t" "v_smfmac_f32_16x16x64_bf16 v[4:7], v[40:43], a[200:207], v225\n\t" "s_waitcnt lgkmcnt(2)\n\t"
                         "v_smfmac_f32_16x16x64_bf16 v[0:3], v[44:47], a[184:191], v225\n\t" "v_smfmac_f32_16x16x64_bf16 v[4:7], v[44:47], a[208:215], v225\n\t" "s_waitcnt lgkmcnt(0)\n\t"
                         "v_smfmac_f32_16x16x64_bf16 v[0:3], v[48:51], a[192:199], v225\n\t" "v_smfmac_f32_16x16x64_bf16 v[4:7], v[48:51], a[216:223], v225\n\t"
                         "v_smfmac_f32_16x16x64_bf16 v[8:11], v[40:43], a[224:231], v225\n\t" "v_add_f32 v52, v0, v2\n\t" "v_add_f32 v53, v1, v3\n\t" "v_cvt_pk_bf16_f32 v54, v53, v52\n\t"
                         "v_smfmac_f32_16x16x64_bf16 v[8:11], v[44:47], a[232:239], v225\n\t" "v_add_f32 v52, v4, v6\n\t" "v_add_f32 v53, v5, v7\n\t" "v_cvt_pk_bf16_f32 v55, v53, v52\n\t" "s_nop 1\n\t"
                         "v_permlane16_swap_b32 v54, v55\n\t" "v_smfmac_f32_16x16x64_bf16 v[8:11], v[48:51], a[240:247], v225\n\t" "global_store_dwordx2 v232, v[54:55], %2 sc1\n\t" "s_add_u32 s40, s40, 0x10000\n\t"
                         "s_addc_u32 s41, s41, 0\n\t" "v_smfmac_f32_16x16x64_bf16 v[12:15], v[40:43], a[248:255], v225\n\t" "v_smfmac_f32_16x16x64_bf16 v[12:15], v[44:47], v[96:103], v225\n\t" "v_add_f32 v52, v8, v10\n\t"
                         "v_smfmac_f32_16x16x64_bf16 v[12:15], v[48:51], v[104:111], v225\n\t" "v_add_f32 v53, v9, v11\n\t" "v_smfmac_f32_16x16x64_bf16 v[16:19], v[40:43], v[112:119], v225\n\t"
                         "v_cvt_pk_bf16_f32 v56, v53, v52\n\t" "v_smfmac_f32_16x16x64_bf16 v[16:19], v[44:47], v[120:127], v225\n\t" "v_add_f32 v52, v12, v14\n\t" "v_add_f32 v53, v13, v15\n\t"
                         "v_cvt_pk_bf16_f32 v57, v53, v52\n\t" "s_nop 1\n\t" "v_permlane16_swap_b32 v56, v57\n\t" "v_smfmac_f32_16x16x64_bf16 v[16:19], v[48:51], v[128:135], v225\n\t"
                         "global_store_dwordx2 v232, v[56:57], %2 sc1\n\t" "s_add_u32 s40, s40, 0x10000\n\t" "s_addc_u32 s41, s41, 0\n\t" "v_smfmac_f32_16x16x64_bf16 v[20:23], v[40:43], v[136:143], v225\n\t"
                         "v_smfmac_f32_16x16x64_bf16 v[20:23], v[44:47], v[144:151], v225\n\t" "v_add_f32 v52, v16, v18\n\t" "v_smfmac_f32_16x16x64_bf16 v[20:23], v[48:51], v[152:159], v225\n\t" "v_add_f32 v53, v17, v19\n\t"
                         "v_smfmac_f32_16x16x64_bf16 v[24:27], v[40:43], v[160:167], v225\n\t" "v_cvt_pk_bf16_f32 v58, v53, v52\n\t" "v_smfmac_f32_16x16x64_bf16 v[24:27], v[44:47], v[168:175], v225\n\t"
                         "v_add_f32 v52, v20, v22\n\t" "v_add_f32 v53, v21, v23\n\t" "v_smfmac_f32_16x16x64_bf16 v[24:27], v[48:51], v[176:183], v225\n\t" "v_cvt_pk_bf16_f32 v59, v53, v52\n\t"
                         "v_smfmac_f32_16x16x64_bf16 v[28:31], v[40:43], v[184:191], v225\n\t" "s_nop 0\n\t" "v_permlane16_swap_b32 v58, v59\n\t" "v_smfmac_f32_16x16x64_bf16 v[28:31], v[44:47], v[192:199], v225\n\t"
                         "global_store_dwordx2 v232, v[58:59], %2 sc1\n\t" "v_add_f32 v52, v24, v26\n\t" "v_smfmac_f32_16x16x64_bf16 v[28:31], v[48:51], v[200:207], v225\n\t" "v_add_f32 v53, v25, v27\n\t"
                         "v_smfmac_f32_16x16x64_bf16 v[32:35], v[40:43], v[208:215], v225\n\t" "v_cvt_pk_bf16_f32 v60, v53, v52\n\t" "v_smfmac_f32_16x16x64_bf16 v[32:35], v[44:47], v[216:223], v225\n\t"
                         "v_add_f32 v52, v28, v30\n\t" "v_smfmac_f32_16x16x64_bf16 v[32:35], v[48:51], v[64:71], v225\n\t" "v_add_f32 v53, v29, v31\n\t" "v_smfmac_f32_16x16x64_bf16 v[36:39], v[40:43], v[72:79], v225\n\t"
                         "v_cvt_pk_bf16_f32 v61, v53, v52\n\t" "v_smfmac_f32_16x16x64_bf16 v[36:39], v[44:47], v[80:87], v225\n\t" "v_add_f32 v52, v32, v34\n\t"
                         "v_smfmac_f32_16x16x64_bf16 v[36:39], v[48:51], v[88:95], v225\n\t" "v_add_f32 v53, v33, v35\n\t" "v_cvt_pk_bf16_f32 v62, v53, v52\n\t" "v_mov_b32 v233, 0\n\t" "v_mov_b32 v233, 0\n\t"
                         "v_mov_b32 v233, 0\n\t" "v_mov_b32 v233, 0\n\t" "v_mov_b32 v233, 0\n\t" "v_pk_add_f32 v[52:53], v[36:37], v[38:39]\n\t" "v_cvt_pk_bf16_f32 v63, v53, v52\n\t" "v_permlane32_swap_b32 v60, v62\n\t"
                         "s_nop 0\n\t" "v_permlane32_swap_b32 v61, v63\n\t" "s_nop 1\n\t" "v_permlane16_swap_b32 v60, v61\n\t" "v_permlane16_swap_b32 v62, v63\n\t" "global_store_dwordx4 v231, v[60:63], %2 sc1\n\t"
                         ".Lsk_u:\n\t" "s_memrealtime %1\n\t" "s_mov_b64 %0, vcc\n\t" "s_waitcnt lgkmcnt(0)\n\t"
                         : "=&s"(bad), "=&s"(tp8), "+{s[40:41]}"(up)
                         : "s"(uni((int)tgt)), "s"(memp - 1024), "s"(memp + 15360), "s"(en), "{v[224:239]}"(ad),
                           "{a[0:31]}"(wa[0]), "{a[32:63]}"(wa[1]), "{a[64:95]}"(wa[2]), "{a[96:127]}"(wa[3]), "{a[128:159]}"(wa[4]), "{a[160:191]}"(wa[5]), "{a[192:223]}"(wa[6]), "{a[224:255]}"(wa[7]), "{v[96:127]}"(wv[0]), "{v[128:159]}"(wv[1]), "{v[160:191]}"(wv[2]), "{v[192:223]}"(wv[3])
                         : "v0", "v1", "v2", "v3", "v4", "v5", "v6", "v7", "v8", "v9", "v10", "v11", "v12", "v13", "v14", "v15", "v16", "v17", "v18", "v19", "v20", "v21", "v22", "v23", "v24", "v25", "v26", "v27", "v28", "v29", "v30", "v31", "v32", "v33", "v34", "v35", "v36", "v37", "v38", "v39", "v40", "v41", "v42", "v43", "v44", "v45", "v46", "v47", "v48", "v49", "v50", "v51", "v52", "v53", "v54", "v55", "v56", "v57", "v58", "v59", "v60", "v61", "v62", "v63", "v64", "v65", "v66", "v67", "v68", "v69", "v70", "v71", "v72", "v73", "v74", "v75", "v76", "v77", "v78", "v79", "v80", "v81", "v82", "v83", "v84", "v85", "v86", "v87", "v88", "v89", "v90", "v91", "v92", "v93", "v94", "v95", "vcc", "scc", "s42", "memory");
            if (__builtin_expect(bad == 0, 1)) break;
            waitvm<0>();
            if (--budget < 0 || ((++tries & 15) == 0 && !unwritten(memp, lanex() * 16))) en = 0;
            phi += (u32)cfg.G;
        }
        clk.calib();
        const u32 tt = sched_ld0(s);
        if (s >= NSEG - 2) waitvm<0>();
        barrier();
        if (s >= NSEG - 2 && w == 3) flag_add(fl, F_PROG + 8 * s + (h & 7), 1);
        if (s >= NSEG - 2 && w == 2) flag_add(fl, F_UD + 2 * (s & 1) + (h & 1), 1);
        follow(phi, (int)(tt >> 16) + 4 + cfg.T3 - VISS, (u32)tp8, cfg.G);
        phi += tt & 0xffffu;
        memp = p.ws + mslot(sd, s + 1);
    }
    asm volatile(".Lkm_lu:\n\ts_waitcnt vmcnt(0)" : : : "memory");
}

#define VALL "{v[0:3]}"(o), "{v[16:47]}"(rv[0]), "{v[48:79]}"(rv[1]), "{v[80:111]}"(rv[2]), "{v[112:143]}"(rv[3]), "{v[144:175]}"(rv[4]), "{v[176:207]}"(rv[5]), "{v[208:239]}"(rv[6]), "{a[0:31]}"(ra[0]), "{a[32:63]}"(ra[1]), "{a[64:95]}"(ra[2]), "{a[96:127]}"(ra[3])
DI void value_role(const Ptrs& p, const ChainCfg cfg, int vb, int sd, u32 phi, u32 lds, int w) {
    const int lane = lanex(), tid = 64 * w + lane, n = lane & 15, g = lane >> 4;
    const int v = vb;
    const u32 moff = lane * 16;
    char* const fl = p.ws + OFF_ST;
    char* const ub = p.ws + uchunk(v);
    const int xh = (vb >> 2) & 1;
    if (w == 3) {
        const u32x4 ff = u32x4{~0u, ~0u, ~0u, ~0u};
        if (SPL) barrier();
#pragma nounroll
        for (int s = 0; s < NSEG - 2; ++s) {
            dly(cfg.cR3 >> 8 & 255);
            char* const mk = ub + uslot(sd, s + 2);
#pragma unroll
            for (int i = 0; i < 8; ++i) { gst16(mk + i * 1024, moff, ff); gap<HQ>(-1); }
            if (vb < 32 && s) {
                gst16(p.ws + (size_t)(s + 2) * HO_STRIDE + vb * 2048, moff, ff);
                gst16(p.ws + (size_t)(s + 2) * HO_STRIDE + vb * 2048 + 1024, moff, ff);
            }
            waitvm<0>();
            if (SPL && s) barrier();
            barrier();
        }
        return;
    }
    if (w) {
        const u32 dof = (u32)((n >> 3) * 16384 + g * 128 + (n & 7) * 16);
        const char* dp = p.ws + OFF_D + (size_t)v * 32768 - 4096 + w * 8192;
        if (SPL) barrier();
        flag_wait(fl, F_VD + (vb & 3) + (v < 20 ? 0 : 4), 32);
#pragma nounroll
        for (int s = 0; s < NSEG - 1; ++s) {
            u32x4 df[16];
            u32 dfl;
            const int on = uni(1 - s);
            sfor<0, SPL>([&](auto I) { gld16c<I.v * 512 - 4096>(df[I.v], dp, dof); gap<HP0>(on); });
            if (SPL) asm volatile("s_cmp_ge_i32 %0, 0\n\ts_cbranch_scc1 .Lhb\n\ts_barrier\n.Lhb:" : : "s"(on) : "memory", "scc");
            dly(cfg.cR3 & 255);
            sfor<SPL, 16>([&](auto I) { gld16c<I.v * 512 - 4096>(df[I.v], dp, dof); gap<HP>(on); });
            dp += D_STRIDE;
            flag_ldm(dfl, fl, F_VD + pblk(s + 1) * 8 + (vb & RM), pblk(s + 1) != pblk(s));
            const u32 la = lds + moff + (s & 1) * 32768 + (w - 1) * 16384;
            sfor<0, 8>([&](auto I) {
                constexpr int i = I.v;
                waitv<15 - 2 * i>(df[2 * i]); waitv<15 - 2 * i>(df[2 * i + 1]);
                const u32x4 x = df[2 * i], y = df[2 * i + 1];
                const u32 lb = i & 2 ? la - xh * 4096 : la + xh * 4096;
                *LDSP(u32x4, lb + 2048 * i) = u32x4{x[0], y[0], x[2], y[2]};
                *LDSP(u32x4, lb + 2048 * i + 1024) = u32x4{x[1], y[1], x[3], y[3]};
                PIN();
            });
            waitvm<0>();
            asm volatile("" : "+v"(dfl));
            if (uni((int)dfl) < 31) {
                dly(FRD);
                flag_ld(dfl, fl, F_VD + pblk(s + 1) * 8 + (vb & RM));
                asm volatile("s_waitcnt vmcnt(0)" : "+v"(dfl) : : "memory");
            }
            barrier();
            if (uni((int)dfl) < 31) flag_wait(fl, F_VD + pblk(s + 1) * 8 + (vb & RM), 32);
        }
        return;
    }
    u32x32 rv[7], ra[4];
    u32x4 o;
    asm volatile("v_accvgpr_write_b32 a64, 0\n\t" "v_accvgpr_write_b32 a65, 0\n\t" "v_accvgpr_write_b32 a66, 0\n\t" "v_accvgpr_write_b32 a67, 0\n\t" "v_accvgpr_write_b32 a68, 0\n\t" "v_accvgpr_write_b32 a69, 0\n\t"
                 "v_accvgpr_write_b32 a70, 0\n\t" "v_accvgpr_write_b32 a71, 0\n\t" "v_accvgpr_write_b32 a72, 0\n\t" "v_accvgpr_write_b32 a73, 0\n\t" "v_accvgpr_write_b32 a74, 0\n\t" "v_accvgpr_write_b32 a75, 0\n\t"
                 "v_accvgpr_write_b32 a76, 0\n\t" "v_accvgpr_write_b32 a77, 0\n\t" "v_accvgpr_write_b32 a78, 0\n\t" "v_accvgpr_write_b32 a79, 0\n\t" "v_mov_b64 v[112:113], -1\n\t" "v_mov_b64 v[120:121], -1\n\t"
                 "v_mov_b64 v[128:129], -1\n\t" "v_mov_b64 v[136:137], -1\n\t" "v_mov_b64 v[144:145], -1\n\t" "v_mov_b64 v[152:153], -1\n\t" "v_mov_b64 v[160:161], -1\n\t" "v_mov_b64 v[168:169], -1\n\t"
                 "v_mov_b64 v[176:177], -1\n\t" "v_mov_b64 v[184:185], -1\n\t" "v_mov_b64 v[192:193], -1\n\t" "v_mov_b64 v[200:201], -1\n\t" "v_mov_b64 v[208:209], -1\n\t" "v_mov_b64 v[216:217], -1\n\t"
                 "v_mov_b64 v[224:225], -1\n\t" "v_mov_b64 v[232:233], -1\n\t"
                 : "={v[0:3]}"(o), "={v[16:47]}"(rv[0]), "={v[48:79]}"(rv[1]), "={v[80:111]}"(rv[2]), "={v[112:143]}"(rv[3]), "={v[144:175]}"(rv[4]), "={v[176:207]}"(rv[5]), "={v[208:239]}"(rv[6]), "={a[0:31]}"(ra[0]), "={a[32:63]}"(ra[1]), "={a[64:95]}"(ra[2]), "={a[96:127]}"(ra[3]));
    const u32 ho = (u32)(min(lane, 39) * 256 + (lane >> 5) * 8192);
    const u32 pof = (u32)((n & 4) * 128 + (n >> 3) * 256 + g * 64 + (n & 3) * 16 + 2048);
    const u32 uof = (u32)((n >> 3) * 4096 + g * 128 + (n & 7) * 16 + 512);
    u32x8 ad = u32x8{uof + xh * 512, pof + xh * 2048, (u32)(((g >> 1) ^ xh) * 256 + (n >> 2) * 64 + (n & 1) * 16), moff,
                     (lane & 2) ? 0xEEEEu : 0x4444u, (lane & 4) ? 0xEEEEu : 0x4444u, (u32)(v * 256 + (v >> 5) * 8192 + g * 64 + (n >> 3) * 32 + (n & 1) * 16 + (n & 6) * 2),
                     (lane & 1) ? 0x07060302u : 0x01000504u};
    u32 pob = pof - xh * 2048, uob = uof - xh * 512, pmo = moff ^ xh * 512;
    asm volatile("" : "+{v[4:11]}"(ad), "+v"(pob), "+v"(uob), "+v"(pmo));
    auto zero = [&]() {
        asm volatile("v_mov_b64 v[0:1], 0\n\t" "v_mov_b64 v[2:3], 0\n\t" "v_mov_b64 v[48:49], 0\n\t" "v_mov_b64 v[50:51], 0\n\t" "v_mov_b64 v[52:53], 0\n\t" "v_mov_b64 v[54:55], 0\n\t" "v_mov_b64 v[56:57], 0\n\t"
                     "v_mov_b64 v[58:59], 0\n\t" "v_mov_b64 v[60:61], 0\n\t" "v_mov_b64 v[62:63], 0\n\t" "v_mov_b64 v[64:65], 0\n\t" "v_mov_b64 v[66:67], 0\n\t" "v_mov_b64 v[68:69], 0\n\t" "v_mov_b64 v[70:71], 0\n\t"
                     "v_mov_b64 v[72:73], 0\n\t" "v_mov_b64 v[74:75], 0\n\t" "v_mov_b64 v[76:77], 0\n\t" "v_mov_b64 v[78:79], 0\n\t" "v_mov_b64 v[80:81], 0\n\t" "v_mov_b64 v[82:83], 0\n\t" "v_mov_b64 v[84:85], 0\n\t"
                     "v_mov_b64 v[86:87], 0\n\t" "v_mov_b64 v[88:89], 0\n\t" "v_mov_b64 v[90:91], 0\n\t" "v_mov_b64 v[92:93], 0\n\t" "v_mov_b64 v[94:95], 0\n\t" "v_mov_b64 v[96:97], 0\n\t" "v_mov_b64 v[98:99], 0\n\t"
                     "v_mov_b64 v[100:101], 0\n\t" "v_mov_b64 v[102:103], 0\n\t" "v_mov_b64 v[104:105], 0\n\t" "v_mov_b64 v[106:107], 0\n\t" "v_mov_b64 v[108:109], 0\n\t" "v_mov_b64 v[110:111], 0\n\t"
                     : : VALL : "memory");
    };
    const char* memp = p.ws + OFF_XF + 4096;
    char* up = ub + OFF_XF;
    int budget = 30000;
    Clock clk; clk.init();
    u32 tt = 0;
    for (int s = -1; s < NSEG - 1; ++s) {
        const char* const mnx = p.ws + mslot(sd, s + 1);
        bool all = false;
        int tries = 0;
        u64 tp8;
        while (true) {
            const u32 tgt = clk.sched(phi, (int)(tt >> 16), cfg.G, tries ? 2 : s > 0) - (s < 0 ? 1u << 20 : 0u);
            u64 ok;
            asm volatile("s_cmp_lg_u32 %11, 0\n\t" "s_cbranch_scc0 .Lsp_v0\n\t" ".Lsp_v3:\n\t" "s_memtime vcc\n\t" "s_waitcnt lgkmcnt(0)\n\t" "s_sub_i32 s42, %10, vcc_lo\n\t" "s_cmp_gt_i32 s42, 80\n\t"
                         "s_cbranch_scc1 .Lsp_v3\n\t" "s_max_i32 s42, s42, 0\n\t" "s_sub_i32 s42, 92, s42\n\t" "s_and_b32 s42, s42, -4\n\t" "s_getpc_b64 vcc\n\t" "s_add_u32 vcc_lo, vcc_lo, s42\n\t"
                         "s_addc_u32 vcc_hi, vcc_hi, 0\n\t" "s_setpc_b64 vcc\n\t" "s_nop 0\n\t" "s_nop 0\n\t" "s_nop 0\n\t" "s_nop 0\n\t" "s_nop 0\n\t" "s_nop 0\n\t" "s_nop 0\n\t" "s_nop 0\n\t" "s_nop 0\n\t" "s_nop 0\n\t"
                         "s_nop 0\n\t" "s_nop 0\n\t" "s_nop 0\n\t" "s_nop 0\n\t" "s_nop 0\n\t" "s_nop 0\n\t" "s_nop 0\n\t" "s_nop 0\n\t" "s_nop 0\n\t" "s_nop 0\n\t"
                         "global_load_dwordx4 a[0:3], v5, %7 offset:-4096 sc1\n\t" "global_load_dwordx4 a[4:7], v5, %7 offset:-3072 sc1\n\t" "global_load_dwordx4 a[16:19], v5, %7 sc1\n\t"
                         "global_load_dwordx4 a[20:23], v5, %7 offset:1024 sc1\n\t" "global_load_dwordx4 a[32:35], v5, %8 offset:-4096 sc1\n\t" "global_load_dwordx4 a[36:39], v5, %8 offset:-3072 sc1\n\t"
                         "global_load_dwordx4 a[48:51], v5, %8 sc1\n\t" "global_load_dwordx4 a[52:55], v5, %8 offset:1024 sc1\n\t" "s_nop 14\n\t" "global_load_dwordx4 a[8:11], %12, %7 offset:-2048 sc1\n\t"
                         "global_load_dwordx4 a[12:15], %12, %7 offset:-1024 sc1\n\t" "global_load_dwordx4 a[24:27], %12, %7 offset:2048 sc1\n\t" "global_load_dwordx4 a[28:31], %12, %7 offset:3072 sc1\n\t"
                         "global_load_dwordx4 a[40:43], %12, %8 offset:-2048 sc1\n\t" "global_load_dwordx4 a[44:47], %12, %8 offset:-1024 sc1\n\t" "global_load_dwordx4 a[56:59], %12, %8 offset:2048 sc1\n\t"
                         "global_load_dwordx4 a[60:63], %12, %8 offset:3072 sc1\n\t" "s_nop 15\n\t" "s_nop 15\n\t" "s_nop 14\n\t" "global_load_dwordx4 a[80:83], %14, %6 sc1\n\t"
                         "global_load_dwordx4 a[84:87], %14, %6 offset:1024 sc1\n\t" "global_load_dwordx4 a[88:91], %14, %6 offset:2048 sc1\n\t" "global_load_dwordx4 a[92:95], %14, %6 offset:3072 sc1\n\t"
                         "s_mov_b64 exec, %9\n\t" "global_load_dwordx4 a[64:67], v6, %6 offset:-2048 sc1\n\t" "global_load_dwordx4 a[68:71], v6, %6 offset:-1536 sc1\n\t"
                         "global_load_dwordx4 a[72:75], v6, %6 offset:-1024 sc1\n\t" "global_load_dwordx4 a[76:79], v6, %6 offset:-512 sc1\n\t" "s_mov_b64 exec, -1\n\t" "s_mov_b32 s42, %11\n\t" ".Lsp_v5:\n\t"
                         "s_sub_u32 s42, s42, 1\n\t" "s_cmp_gt_i32 s42, 0\n\t" "s_cbranch_scc1 .Lsp_v5\n\t" "global_load_dwordx4 a[96:99], v4, %3 offset:-512 sc1\n\t" "global_load_dwordx4 a[100:103], %13, %3 sc1\n\t"
                         "global_load_dwordx4 a[104:107], v4, %3 offset:512 sc1\n\t" "global_load_dwordx4 a[108:111], %13, %3 offset:1024 sc1\n\t" "global_load_dwordx4 a[112:115], v4, %3 offset:1536 sc1\n\t"
                         "global_load_dwordx4 a[116:119], %13, %3 offset:2048 sc1\n\t" "global_load_dwordx4 a[120:123], v4, %3 offset:2560 sc1\n\t" "global_load_dwordx4 a[124:127], %13, %3 offset:3072 sc1\n\t"
                         "s_waitcnt vmcnt(31)\n\t" "v_smfmac_f32_16x16x64_bf16 v[48:51], a[0:3], v[112:119], v9\n\t" "s_waitcnt vmcnt(30)\n\t" "v_smfmac_f32_16x16x64_bf16 v[52:55], a[4:7], v[120:127], v9\n\t"
                         "s_waitcnt vmcnt(29)\n\t" "v_smfmac_f32_16x16x64_bf16 v[64:67], a[16:19], v[144:151], v9\n\t" "s_waitcnt vmcnt(28)\n\t" "v_cvt_pk_bf16_f32 v16, v48, v49\n\t" "v_cvt_pk_bf16_f32 v17, v50, v51\n\t"
                         "v_smfmac_f32_16x16x64_bf16 v[68:71], a[20:23], v[152:159], v9\n\t" "s_waitcnt vmcnt(27)\n\t" "v_cvt_pk_bf16_f32 v18, v52, v53\n\t" "v_cvt_pk_bf16_f32 v19, v54, v55\n\t"
                         "v_smfmac_f32_16x16x64_bf16 v[80:83], a[32:35], v[176:183], v9\n\t" "s_waitcnt vmcnt(26)\n\t" "v_cvt_pk_bf16_f32 v24, v64, v65\n\t" "v_cvt_pk_bf16_f32 v25, v66, v67\n\t"
                         "v_smfmac_f32_16x16x64_bf16 v[84:87], a[36:39], v[184:191], v9\n\t" "s_waitcnt vmcnt(25)\n\t" "v_cvt_pk_bf16_f32 v26, v68, v69\n\t" "v_cvt_pk_bf16_f32 v27, v70, v71\n\t"
                         "v_smfmac_f32_16x16x64_bf16 v[96:99], a[48:51], v[208:215], v9\n\t" "s_waitcnt vmcnt(24)\n\t" "v_cvt_pk_bf16_f32 v32, v80, v81\n\t" "v_cvt_pk_bf16_f32 v33, v82, v83\n\t"
                         "v_smfmac_f32_16x16x64_bf16 v[100:103], a[52:55], v[216:223], v9\n\t" "s_waitcnt vmcnt(23)\n\t" "v_cvt_pk_bf16_f32 v34, v84, v85\n\t" "v_cvt_pk_bf16_f32 v35, v86, v87\n\t"
                         "v_smfmac_f32_16x16x64_bf16 v[56:59], a[8:11], v[128:135], v9\n\t" "s_waitcnt vmcnt(22)\n\t" "v_cvt_pk_bf16_f32 v40, v96, v97\n\t" "v_cvt_pk_bf16_f32 v41, v98, v99\n\t"
                         "v_smfmac_f32_16x16x64_bf16 v[60:63], a[12:15], v[136:143], v9\n\t" "s_waitcnt vmcnt(21)\n\t" "v_cvt_pk_bf16_f32 v42, v100, v101\n\t" "v_cvt_pk_bf16_f32 v43, v102, v103\n\t"
                         "v_smfmac_f32_16x16x64_bf16 v[72:75], a[24:27], v[160:167], v9\n\t" "s_waitcnt vmcnt(20)\n\t" "v_cvt_pk_bf16_f32 v20, v56, v57\n\t" "v_cvt_pk_bf16_f32 v21, v58, v59\n\t"
                         "v_smfmac_f32_16x16x64_bf16 v[76:79], a[28:31], v[168:175], v9\n\t" "s_waitcnt vmcnt(19)\n\t" "v_cvt_pk_bf16_f32 v22, v60, v61\n\t" "v_cvt_pk_bf16_f32 v23, v62, v63\n\t"
                         "v_smfmac_f32_16x16x64_bf16 v[88:91], a[40:43], v[192:199], v9\n\t" "s_waitcnt vmcnt(18)\n\t" "v_cvt_pk_bf16_f32 v28, v72, v73\n\t" "v_cvt_pk_bf16_f32 v29, v74, v75\n\t"
                         "v_smfmac_f32_16x16x64_bf16 v[92:95], a[44:47], v[200:207], v9\n\t" "s_waitcnt vmcnt(17)\n\t" "v_cvt_pk_bf16_f32 v30, v76, v77\n\t" "v_cvt_pk_bf16_f32 v31, v78, v79\n\t"
                         "v_smfmac_f32_16x16x64_bf16 v[104:107], a[56:59], v[224:231], v9\n\t" "s_waitcnt vmcnt(16)\n\t" "v_cvt_pk_bf16_f32 v36, v88, v89\n\t" "v_cvt_pk_bf16_f32 v37, v90, v91\n\t"
                         "v_smfmac_f32_16x16x64_bf16 v[108:111], a[60:63], v[232:239], v9\n\t" "s_waitcnt vmcnt(11)\n\t" "v_cvt_pk_bf16_f32 v38, v92, v93\n\t" "v_cvt_pk_bf16_f32 v39, v94, v95\n\t"
                         "v_smfmac_f32_16x16x64_bf16 v[0:3], a[64:67], v[16:23], v8\n\t" "s_waitcnt vmcnt(10)\n\t" "v_cvt_pk_bf16_f32 v44, v104, v105\n\t" "v_cvt_pk_bf16_f32 v45, v106, v107\n\t"
                         "v_smfmac_f32_16x16x64_bf16 v[0:3], a[68:71], v[24:31], v8\n\t" "s_waitcnt vmcnt(9)\n\t" "v_cvt_pk_bf16_f32 v46, v108, v109\n\t" "v_cvt_pk_bf16_f32 v47, v110, v111\n\t"
                         "v_smfmac_f32_16x16x64_bf16 v[0:3], a[72:75], v[32:39], v8\n\t" "s_waitcnt vmcnt(8)\n\t" "v_smfmac_f32_16x16x64_bf16 v[0:3], a[76:79], v[40:47], v8\n\t" "s_waitcnt vmcnt(6)\n\t"
                         "v_smfmac_f32_16x16x64_bf16 v[0:3], a[80:83], a[96:103], v8\n\t" "s_waitcnt vmcnt(4)\n\t" "v_smfmac_f32_16x16x64_bf16 v[0:3], a[84:87], a[104:111], v8\n\t" "s_waitcnt vmcnt(2)\n\t"
                         "v_smfmac_f32_16x16x64_bf16 v[0:3], a[88:91], a[112:119], v8\n\t" "s_waitcnt vmcnt(0)\n\t" "v_smfmac_f32_16x16x64_bf16 v[0:3], a[92:95], a[120:127], v8\n\t" "s_branch .Lv_end\n\t" ".Lsp_v0:\n\t"
                         "s_memtime vcc\n\t" "s_waitcnt lgkmcnt(0)\n\t" "s_sub_i32 s42, %10, vcc_lo\n\t" "s_cmp_gt_i32 s42, 80\n\t" "s_cbranch_scc1 .Lsp_v0\n\t" "s_max_i32 s42, s42, 0\n\t" "s_sub_i32 s42, 92, s42\n\t"
                         "s_and_b32 s42, s42, -4\n\t" "s_getpc_b64 vcc\n\t" "s_add_u32 vcc_lo, vcc_lo, s42\n\t" "s_addc_u32 vcc_hi, vcc_hi, 0\n\t" "s_setpc_b64 vcc\n\t" "s_nop 0\n\t" "s_nop 0\n\t" "s_nop 0\n\t" "s_nop 0\n\t"
                         "s_nop 0\n\t" "s_nop 0\n\t" "s_nop 0\n\t" "s_nop 0\n\t" "s_nop 0\n\t" "s_nop 0\n\t" "s_nop 0\n\t" "s_nop 0\n\t" "s_nop 0\n\t" "s_nop 0\n\t" "s_nop 0\n\t" "s_nop 0\n\t" "s_nop 0\n\t" "s_nop 0\n\t"
                         "s_nop 0\n\t" "s_nop 0\n\t" "global_load_dwordx4 a[96:99], v4, %3 offset:-512 sc1\n\t" "global_load_dwordx4 a[104:107], v4, %3 offset:512 sc1\n\t"
                         "global_load_dwordx4 a[112:115], v4, %3 offset:1536 sc1\n\t" "global_load_dwordx4 a[120:123], v4, %3 offset:2560 sc1\n\t" "global_load_dwordx4 a[100:103], %13, %3 sc1\n\t"
                         "global_load_dwordx4 a[108:111], %13, %3 offset:1024 sc1\n\t" "global_load_dwordx4 a[116:119], %13, %3 offset:2048 sc1\n\t" "global_load_dwordx4 a[124:127], %13, %3 offset:3072 sc1\n\t" "s_nop 9\n\t"
                         "global_load_dwordx4 a[0:3], v5, %7 offset:-4096 sc1\n\t" "global_load_dwordx4 a[4:7], v5, %7 offset:-3072 sc1\n\t" "global_load_dwordx4 a[16:19], v5, %7 sc1\n\t"
                         "global_load_dwordx4 a[20:23], v5, %7 offset:1024 sc1\n\t" "global_load_dwordx4 a[32:35], v5, %8 offset:-4096 sc1\n\t" "global_load_dwordx4 a[36:39], v5, %8 offset:-3072 sc1\n\t"
                         "global_load_dwordx4 a[48:51], v5, %8 sc1\n\t" "global_load_dwordx4 a[52:55], v5, %8 offset:1024 sc1\n\t" "s_nop 14\n\t" "global_load_dwordx4 a[8:11], %12, %7 offset:-2048 sc1\n\t"
                         "global_load_dwordx4 a[12:15], %12, %7 offset:-1024 sc1\n\t" "global_load_dwordx4 a[24:27], %12, %7 offset:2048 sc1\n\t" "global_load_dwordx4 a[28:31], %12, %7 offset:3072 sc1\n\t"
                         "global_load_dwordx4 a[40:43], %12, %8 offset:-2048 sc1\n\t" "global_load_dwordx4 a[44:47], %12, %8 offset:-1024 sc1\n\t" "global_load_dwordx4 a[56:59], %12, %8 offset:2048 sc1\n\t"
                         "global_load_dwordx4 a[60:63], %12, %8 offset:3072 sc1\n\t" "s_nop 15\n\t" "s_nop 15\n\t" "s_nop 14\n\t" "global_load_dwordx4 a[80:83], %14, %6 sc1\n\t"
                         "global_load_dwordx4 a[84:87], %14, %6 offset:1024 sc1\n\t" "global_load_dwordx4 a[88:91], %14, %6 offset:2048 sc1\n\t" "global_load_dwordx4 a[92:95], %14, %6 offset:3072 sc1\n\t"
                         "s_mov_b64 exec, %9\n\t" "global_load_dwordx4 a[64:67], v6, %6 offset:-2048 sc1\n\t" "global_load_dwordx4 a[68:71], v6, %6 offset:-1536 sc1\n\t"
                         "global_load_dwordx4 a[72:75], v6, %6 offset:-1024 sc1\n\t" "global_load_dwordx4 a[76:79], v6, %6 offset:-512 sc1\n\t" "s_mov_b64 exec, -1\n\t" "s_waitcnt vmcnt(23)\n\t"
                         "v_smfmac_f32_16x16x64_bf16 v[48:51], a[0:3], v[112:119], v9\n\t" "s_waitcnt vmcnt(22)\n\t" "v_smfmac_f32_16x16x64_bf16 v[52:55], a[4:7], v[120:127], v9\n\t" "s_waitcnt vmcnt(21)\n\t"
                         "v_smfmac_f32_16x16x64_bf16 v[64:67], a[16:19], v[144:151], v9\n\t" "s_waitcnt vmcnt(20)\n\t" "v_cvt_pk_bf16_f32 v16, v48, v49\n\t" "v_cvt_pk_bf16_f32 v17, v50, v51\n\t"
                         "v_smfmac_f32_16x16x64_bf16 v[68:71], a[20:23], v[152:159], v9\n\t" "s_waitcnt vmcnt(19)\n\t" "v_cvt_pk_bf16_f32 v18, v52, v53\n\t" "v_cvt_pk_bf16_f32 v19, v54, v55\n\t"
                         "v_smfmac_f32_16x16x64_bf16 v[80:83], a[32:35], v[176:183], v9\n\t" "s_waitcnt vmcnt(18)\n\t" "v_cvt_pk_bf16_f32 v24, v64, v65\n\t" "v_cvt_pk_bf16_f32 v25, v66, v67\n\t"
                         "v_smfmac_f32_16x16x64_bf16 v[84:87], a[36:39], v[184:191], v9\n\t" "s_waitcnt vmcnt(17)\n\t" "v_cvt_pk_bf16_f32 v26, v68, v69\n\t" "v_cvt_pk_bf16_f32 v27, v70, v71\n\t"
                         "v_smfmac_f32_16x16x64_bf16 v[96:99], a[48:51], v[208:215], v9\n\t" "s_waitcnt vmcnt(16)\n\t" "v_cvt_pk_bf16_f32 v32, v80, v81\n\t" "v_cvt_pk_bf16_f32 v33, v82, v83\n\t"
                         "v_smfmac_f32_16x16x64_bf16 v[100:103], a[52:55], v[216:223], v9\n\t" "s_waitcnt vmcnt(15)\n\t" "v_cvt_pk_bf16_f32 v34, v84, v85\n\t" "v_cvt_pk_bf16_f32 v35, v86, v87\n\t"
                         "v_smfmac_f32_16x16x64_bf16 v[56:59], a[8:11], v[128:135], v9\n\t" "s_waitcnt vmcnt(14)\n\t" "v_cvt_pk_bf16_f32 v40, v96, v97\n\t" "v_cvt_pk_bf16_f32 v41, v98, v99\n\t"
                         "v_smfmac_f32_16x16x64_bf16 v[60:63], a[12:15], v[136:143], v9\n\t" "s_waitcnt vmcnt(13)\n\t" "v_cvt_pk_bf16_f32 v42, v100, v101\n\t" "v_cvt_pk_bf16_f32 v43, v102, v103\n\t"
                         "v_smfmac_f32_16x16x64_bf16 v[72:75], a[24:27], v[160:167], v9\n\t" "s_waitcnt vmcnt(12)\n\t" "v_cvt_pk_bf16_f32 v20, v56, v57\n\t" "v_cvt_pk_bf16_f32 v21, v58, v59\n\t"
                         "v_smfmac_f32_16x16x64_bf16 v[76:79], a[28:31], v[168:175], v9\n\t" "s_waitcnt vmcnt(11)\n\t" "v_cvt_pk_bf16_f32 v22, v60, v61\n\t" "v_cvt_pk_bf16_f32 v23, v62, v63\n\t"
                         "v_smfmac_f32_16x16x64_bf16 v[88:91], a[40:43], v[192:199], v9\n\t" "s_waitcnt vmcnt(10)\n\t" "v_cvt_pk_bf16_f32 v28, v72, v73\n\t" "v_cvt_pk_bf16_f32 v29, v74, v75\n\t"
                         "v_smfmac_f32_16x16x64_bf16 v[92:95], a[44:47], v[200:207], v9\n\t" "s_waitcnt vmcnt(9)\n\t" "v_cvt_pk_bf16_f32 v30, v76, v77\n\t" "v_cvt_pk_bf16_f32 v31, v78, v79\n\t"
                         "v_smfmac_f32_16x16x64_bf16 v[104:107], a[56:59], v[224:231], v9\n\t" "s_waitcnt vmcnt(8)\n\t" "v_cvt_pk_bf16_f32 v36, v88, v89\n\t"
                         "v_smfmac_f32_16x16x64_bf16 v[108:111], a[60:63], v[232:239], v9\n\t" "s_waitcnt vmcnt(7)\n\t" "v_cvt_pk_bf16_f32 v37, v90, v91\n\t"
                         "v_smfmac_f32_16x16x64_bf16 v[0:3], a[80:83], a[96:103], v8\n\t" "s_waitcnt vmcnt(6)\n\t" "v_cvt_pk_bf16_f32 v38, v92, v93\n\t" "v_smfmac_f32_16x16x64_bf16 v[0:3], a[84:87], a[104:111], v8\n\t"
                         "s_waitcnt vmcnt(5)\n\t" "v_cvt_pk_bf16_f32 v39, v94, v95\n\t" "v_smfmac_f32_16x16x64_bf16 v[0:3], a[88:91], a[112:119], v8\n\t" "s_waitcnt vmcnt(4)\n\t" "v_cvt_pk_bf16_f32 v44, v104, v105\n\t"
                         "v_smfmac_f32_16x16x64_bf16 v[0:3], a[92:95], a[120:127], v8\n\t" "s_waitcnt vmcnt(3)\n\t" "v_cvt_pk_bf16_f32 v45, v106, v107\n\t" "v_smfmac_f32_16x16x64_bf16 v[0:3], a[64:67], v[16:23], v8\n\t"
                         "s_waitcnt vmcnt(2)\n\t" "v_cvt_pk_bf16_f32 v46, v108, v109\n\t" "v_smfmac_f32_16x16x64_bf16 v[0:3], a[68:71], v[24:31], v8\n\t" "s_waitcnt vmcnt(1)\n\t" "v_cvt_pk_bf16_f32 v47, v110, v111\n\t"
                         "v_smfmac_f32_16x16x64_bf16 v[0:3], a[72:75], v[32:39], v8\n\t" "s_waitcnt vmcnt(0)\n\t" "v_smfmac_f32_16x16x64_bf16 v[0:3], a[76:79], v[40:47], v8\n\t" ".Lv_end:\n\t" "s_nop 7\n\t"
                         "v_pk_add_f32 v[0:1], v[0:1], v[2:3]\n\t" "v_cvt_pk_bf16_f32 v2, v0, v1\n\t" "v_cmp_u_f32 vcc, v0, v1\n\t" "s_cmp_eq_u64 vcc, 0\n\t"
                         "v_mov_b32_dpp v3, v2 quad_perm:[1,0,3,2] row_mask:0xf bank_mask:0xf\n\t" "s_cselect_b64 %0, -1, 0\n\t" "v_perm_b32 v0, v2, v3, v11\n\t" "s_mov_b64 exec, %0\n\t"
                         "global_store_dword v10, v0, %5 offset:-4096 sc1\n\t" "global_store_dword v10, v0, %4 offset:-4096 sc1\n\t" "s_memrealtime %1\n\t" "s_mov_b64 exec, -1\n\t" "s_waitcnt lgkmcnt(0)\n\t"
                         : "=&s"(ok), "=&s"(tp8)
                         : "{v[4:11]}"(ad), "s"(up), "s"(mnx), "s"((u64)mnx ^ 8192), "s"(memp + 16384), "s"(memp + 32768 - 2048), "s"(memp + 49152 - 2048), "s"(0xc30000c3c30000c3ull),
                           "s"(uni((int)(tgt - (u32)(v < 16 ? cfg.dU - (v >> 3) * cfg.s2 : cfg.cR2)))), "s"(v < 16 ? 0 : (v < 24 ? 1 : cfg.T3)), "v"(pob), "v"(uob), "v"(pmo), "s"(tries), VALL : "memory", "vcc", "scc", "s42");
            if (__builtin_expect(ok != 0, 1) || s < 0) break;
            if (all || --budget < 0) {
                asm volatile("v_min_u32 v0, -2, v0\n\tglobal_store_dword v10, v0, %0 offset:-4096 sc1\n\tglobal_store_dword v10, v0, %1 offset:-4096 sc1"
                             : : "s"(mnx), "s"((u64)mnx ^ 8192), "{v[4:11]}"(ad), VALL : "memory");
                break;
            }
            if ((++tries & 15) == 0) {
                u32 m = 0;
#pragma nounroll
                for (int i = 0; i < 32; ++i) {
                    u32x4 t;
                    gld16c<0>(t, i < 24 ? memp + 12288 : up, i < 20 ? (4 + i + ((4 + i) & 24)) * 1024 + ad[3] : (i < 24 ? i * 512 - 8192 + ad[2] : (i & 7) * 512 - 512 + (i & 1 ? uob : ad[0]))); waitv<0>(t);
                    m = max3u(max3u(m, t[0], t[1]), t[2], t[3]);
                }
                all = __builtin_amdgcn_ballot_w64(m == 0xffffffffu) == 0;
            }
            phi += (u32)cfg.G;
            zero();
        }
        asm volatile("s_barrier\n\t" "ds_read_b128 v[112:115], %0\n\t" "ds_read_b128 v[116:119], %0 offset:1024\n\t" "ds_read_b128 v[120:123], %0 offset:2048\n\t" "ds_read_b128 v[124:127], %0 offset:3072\n\t"
                     "ds_read_b128 v[128:131], %0 offset:4096\n\t" "ds_read_b128 v[132:135], %0 offset:5120\n\t" "ds_read_b128 v[136:139], %0 offset:6144\n\t" "ds_read_b128 v[140:143], %0 offset:7168\n\t"
                     "ds_read_b128 v[144:147], %0 offset:8192\n\t" "ds_read_b128 v[148:151], %0 offset:9216\n\t" "ds_read_b128 v[152:155], %0 offset:10240\n\t" "ds_read_b128 v[156:159], %0 offset:11264\n\t"
                     "ds_read_b128 v[160:163], %0 offset:12288\n\t" "ds_read_b128 v[164:167], %0 offset:13312\n\t" "ds_read_b128 v[168:171], %0 offset:14336\n\t" "ds_read_b128 v[172:175], %0 offset:15360\n\t"
                     "ds_read_b128 v[176:179], %0 offset:16384\n\t" "ds_read_b128 v[180:183], %0 offset:17408\n\t" "ds_read_b128 v[184:187], %0 offset:18432\n\t" "ds_read_b128 v[188:191], %0 offset:19456\n\t"
                     "ds_read_b128 v[192:195], %0 offset:20480\n\t" "ds_read_b128 v[196:199], %0 offset:21504\n\t" "ds_read_b128 v[200:203], %0 offset:22528\n\t" "ds_read_b128 v[204:207], %0 offset:23552\n\t"
                     "ds_read_b128 v[208:211], %0 offset:24576\n\t" "ds_read_b128 v[212:215], %0 offset:25600\n\t" "ds_read_b128 v[216:219], %0 offset:26624\n\t" "ds_read_b128 v[220:223], %0 offset:27648\n\t"
                     "ds_read_b128 v[224:227], %0 offset:28672\n\t" "ds_read_b128 v[228:231], %0 offset:29696\n\t" "ds_read_b128 v[232:235], %0 offset:30720\n\t" "ds_read_b128 v[236:239], %0 offset:31744\n\t"
                     : : "v"(lds + ad[3] + ((s + 1) & 1) * 32768), VALL : "memory");
        if (s >= 0) {
            follow(phi, (int)(tt & 0xffffu) + 5 * (v >> 2) / 2 - (cfg.dV >> 16 & 255), (u32)tp8, cfg.G);
            if (vb == 0 && lane < 8) gst8(fl, (F_CUR + lane) * 128, u32x2{(u32)s - 1, phi + (tt & 0xffffu)});
        }
        phi += tt & 0xffffu;
        memp = mnx; up = ub + uslot(sd, s + 1);
        zero();
        tt = sched_ld0(s + 1);
        clk.calib();
        while (s >= 0) {
            u32 h0, h1;
            spin_to(clk.at(phi + (u32)(cfg.dV >> 8 & 255)));
            asm volatile("global_load_dword %0, %1, %2 offset:-4096 sc1" : "=&v"(h1) : "v"(ho & 0xfff), "s"(memp) : "memory");
            spin_to(clk.at(phi + (u32)(cfg.dV & 255)));
            asm volatile("global_load_dword %0, %1, %2 offset:-4096 sc1\n\ts_sleep 5" : "=&v"(h0) : "v"(ho), "s"(memp) : "memory");
            edge(clk.r1, clk.m1);
            asm volatile("s_waitcnt vmcnt(0)" : "+v"(h0), "+v"(h1) : : "memory");
            h0 = max(h0, h1);
            if (__builtin_amdgcn_ballot_w64(h0 == 0xffffffffu) == 0 || --budget < 0) break;
            phi += (u32)cfg.G;
        }
        asm volatile(".Lkm_lv:");
    }
}

DI void xcopy_task(const Ptrs& p, int j, int w) {
    const int tid = 64 * w + lanex();
    Sync y;
    sync_ld(y, p.ws);
    u32x4 xv[10];
    sfor<0, 10>([&](auto I) { gld16<I.v * 128>(xv[I.v], p.x + (size_t)j * 40960, (u32)((tid & 31) * 1280 + (tid >> 5) * 16)); });
    sfor<0, 10>([&](auto I) { waitv<9 - I.v>(xv[I.v]); });
    asm volatile("" : "+v"(y.r), "+v"(y.g), "+v"(y.e));
    sfor<0, 10>([&](auto I) { gst16(p.ws + OFF_XF + (size_t)j * 40960 + I.v * 4096, (u32)tid * 16, xv[I.v]); });
    waitvm<0>();
    barrier();
    const u64 t = sync_tag(y.e);
    if (tid == 0) gst8(p.ws + OFF_LAT, 1536 + (u32)j * 8, u32x2{(u32)t, (u32)(t >> 32)});
}
DI void xcopy_wait(const char* ws, u64 t, int lo = 0, int msk = 63) {
#pragma nounroll
    for (int it = 0; it < (1 << 16); ++it) {
        u32x2 r;
        asm volatile("s_nop 4\n\tglobal_load_dwordx2 %0, %1, %2 offset:1536 sc1\n\ts_waitcnt vmcnt(0)" : "=&v"(r) : "v"((lo + (lanex() & msk)) * 8), "s"(ws + OFF_LAT) : "memory");
        if (__builtin_amdgcn_ballot_w64(r[0] != (u32)t || r[1] != (u32)(t >> 32)) == 0) break;
        __builtin_amdgcn_s_sleep(XSL);
    }
}
DI void init_task(const Ptrs& p, int j, u32 lds, int w) {
    const int lane = lanex(), tid = 64 * w + lane;
    Sync y;
    sync_ld(y, p.ws);
    const u32x4 ff = u32x4{~0u, ~0u, ~0u, ~0u};
    const int i = j * 256 + tid, r = j < 8 ? j & 1 : min(j / 2 - 2, NREG - 1);
    char* const vb = p.ws + (r < 2 ? 0 : OFF_U - (4u << 20)) + ((size_t)r << 21) + 1984 * 1024 + (j < 8 ? j & 6 : (j & 1) * 2 + (j >> 1 == 15) * 4) * 8192;
    const u32 vo = (u32)((w >> 1) * 8192 + (w & 1) * 1024);
    gst16(vb, vo + lane * 16, ff);
    if (i < F_N * 8) gst16(p.ws + OFF_ST, (u32)i * 16, u32x4{i == F_ONE * 8 ? 1000u : ~0u, ~0u, ~0u, ~0u});
    if (i < 512) gst16(p.ws + OFF_GATE, (u32)i * 16, ff);
    gst16(p.ws + 65536, (u32)i * 16, ff);
    if (i < 4096) {
        const int k = (i & 511) + ((i >> 1) & 0x600);
        u32x4 m;
        gld16<0>(m, p.mem0, (u32)((2 * ((k >> 2) & 3) + (k & 1)) * 1280 + min(2 * (k >> 4) + ((k >> 1) & 1), 79) * 16));
        waitv<0>(m);
        gst16(p.ws, (u32)i * 16, k < 640 ? u32x4{min(m[0], 0xfffffffeu), min(m[1], 0xfffffffeu), min(m[2], 0xfffffffeu), min(m[3], 0xfffffffeu)} : ff);
    }
#pragma unroll
    for (int c = 0; c < 6; ++c) gst16(p.ws + OFF_U + c * 131072 - (c == 5) * 122880, (u32)i * 16, ff);
    waitvm<0>();
    asm volatile("" : "+v"(y.r), "+v"(y.g), "+v"(y.e));
    barrier();
    if (w) return;
    u32 dt[2] = {0xffffffu, 0xffffffu};
#pragma unroll
    for (int c = 0; c < 2 * NSAMP; ++c) {
        u32 v;
        const u32 t0 = mt();
        asm volatile("global_load_dword %0, %1, %2 sc1\n\ts_waitcnt vmcnt(0)" : "=&v"(v) : "v"((c & 1) * 8192 + (c >> 1) * 256), "s"(vb) : "memory");
        dt[c & 1] = min(dt[c & 1], mt() - t0);
    }
    const u64 t = sync_tag(y.e);
    const u32 vote = (u32)((dt[0] > dt[1]) ^ xhalf() ^ (int)((vb - p.ws) >> 21)) & 1u;
    if (tid == 0) {
        if (j == 0) gst8(p.ws + OFF_LAT, 512, u32x2{(u32)t, rt()});
        gst8(p.ws + OFF_LAT, (u32)j * 8, u32x2{(u32)t | vote, (u32)(t >> 32)});
    }
}

DI float xmax(float v) {
    const auto a = __builtin_amdgcn_permlane32_swap(__float_as_uint(v), __float_as_uint(v), false, false);
    v = fmaxf(__uint_as_float(a[0]), __uint_as_float(a[1]));
    const auto b = __builtin_amdgcn_permlane16_swap(__float_as_uint(v), __float_as_uint(v), false, false);
    return fmaxf(__uint_as_float(b[0]), __uint_as_float(b[1]));
}
DI float xsum(float v) {
    const auto a = __builtin_amdgcn_permlane32_swap(__float_as_uint(v), __float_as_uint(v), false, false);
    v = __uint_as_float(a[0]) + __uint_as_float(a[1]);
    const auto b = __builtin_amdgcn_permlane16_swap(__float_as_uint(v), __float_as_uint(v), false, false);
    return __uint_as_float(b[0]) + __uint_as_float(b[1]);
}
DI void gst16c(char* sbase, u32 voff, u32x4 v) {
    asm volatile("global_store_dwordx4 %0, %1, %2 sc1\n\ts_nop 1" : : "v"(voff), "v"(v), "s"(sbase) : "memory");
}
#define WAIT6(N, r, o) asm volatile("s_waitcnt vmcnt(" #N ")" : "+v"(r[o]), "+v"(r[o + 1]), "+v"(r[o + 2]), "+v"(r[o + 3]), "+v"(r[o + 4]), "+v"(r[o + 5]) : : "memory")
#define WSLEEP 90
DI void wg_wait(const char* fl, int w, int i0, int t0, int i1, int t1, int i2, int t2, int i3, int t3, int dt, bool acq) {
    if (w == 0) {
        const int lane = lanex(), tg = lane == 1 ? t1 : (lane == 2 ? t2 : (lane == 3 ? t3 : t0));
        const int ix = tg <= 0 ? i0 : (lane == 1 ? i1 : (lane == 2 ? i2 : (lane == 3 ? i3 : i0)));
#pragma nounroll
        for (int it = 0; it < (1 << 14); ++it) {
            u32 v;
            flag_ld(v, fl, ix);
            asm volatile("s_waitcnt vmcnt(0)" : "+v"(v) : : "memory");
            if (__builtin_amdgcn_ballot_w64((int)(v + 1u) < tg) == 0) break;
            const u32 wake = rt() + (u32)(dt + 180 * min(max(t0 - 2 - uni((int)v), 0), 64));
            while ((int)(rt() - wake) < 0) __builtin_amdgcn_s_sleep(4);
        }
        if (acq) acquire();
    }
    barrier();
}
#define TEST 172
DI void until(u32 t) { while ((int)(rt() - t) < 0) __builtin_amdgcn_s_sleep(1); }
DI u32 step_wait(const char* fl, int w, int gr, int t, u32 lds) {
    if (w == 0) {
        u32x2 v;
#pragma nounroll
        for (int it = 0; it < (1 << 14); ++it) {
            asm volatile("s_nop 4\n\tglobal_load_dwordx2 %0, %1, %2 sc1\n\ts_waitcnt vmcnt(0)" : "=&v"(v) : "v"((F_CUR + gr) * 128), "s"(fl) : "memory");
            const int d = t - 1 - uni((int)v[0]);
            if (d <= 0) { v[1] += (u32)(d * TEST); break; }
            const u32 now = rt();
            nap(min(max((int)((u32)uni((int)v[1]) - now) + d * TEST + 10, 40), 2000));
        }
        *LDSP(u32, lds + GRING * GBUF + 4) = v[1];
    }
    barrier();
    return (u32)uni((int)*LDSP(u32, lds + GRING * GBUF + 4));
}
#define PDL 115
#define ODL 240
#define TDT 35
template <bool LATE>
DI void px_wave(const Ptrs& p, const ChainCfg& cfg, int s, int h, int slow, int w, u32 lds, bool keep) {
    const int lane = lanex(), n = lane & 15, g = lane >> 4;
    const char* qb = p.ws + OFF_QX + (size_t)(s * NH + h) * 12288;
    const char* kb = p.ws + OFF_KX + (size_t)(s * NH + h) * 12288;
    const char* mb = p.ws + OFF_KM + (size_t)(s * NH + h) * 3072;
    const u32 mo = (u32)(g * 128 + (n & 7) * 16);
    u32x4 qf[12], kf[12], mf[6];
    sfor<0, 12>([&](auto I) { gld16x<(I.v % 3) * 1024>(qf[I.v], qb + (I.v / 3) * 3072, lane * 16); gap<SMP>(slow); });
    if (!SMP) pace(slow);
    sfor<0, 12>([&](auto I) { if constexpr (LATE || !KNT) gld16c<(I.v % 3) * 1024>(kf[I.v], kb + (I.v / 3) * 3072, lane * 16); else gld16x<(I.v % 3) * 1024>(kf[I.v], kb + (I.v / 3) * 3072, lane * 16); gap<SMP>(slow); });
    if (!SMP) pace(slow);
    if constexpr (LATE) { WAIT6(0, qf, 0); WAIT6(0, qf, 6); WAIT6(0, kf, 0); }
    else {
        sfor<0, 6>([&](auto I) { gld16x<I.v * 512>(mf[I.v], mb, mo); });
        WAIT6(24, qf, 0); WAIT6(18, qf, 6); WAIT6(12, kf, 0);
    }
    f32x4 sc[2][3];
#pragma unroll
    for (int tt = 0; tt < 2; ++tt)
#pragma unroll
        for (int T = 0; T < 3; ++T) sc[tt][T] = f32x4{0.f, 0.f, 0.f, 0.f};
#pragma unroll
    for (int ks = 0; ks < 6; ++ks) { sc[0][0] = mfma16(kf[ks], qf[ks], sc[0][0]); sc[1][0] = mfma16(kf[ks], qf[6 + ks], sc[1][0]); }
    WAIT6(6, kf, 6);
#pragma unroll
    for (int ks = 0; ks < 6; ++ks) sc[1][1] = mfma16(kf[6 + ks], qf[6 + ks], sc[1][1]);
    if constexpr (LATE) {
        until(step_wait(p.ws + OFF_ST, w, h & 7, s - 1, lds) + (u32)(s == NSEG - 1 ? cfg.T : cfg.T + PDX));
        bool ok = false;
#pragma nounroll
        for (int it = 0; it < (1 << 14); ++it) {
            u32 v;
            sfor<0, 6>([&](auto I) { gld16c<I.v * 512>(mf[I.v], mb, mo); });
            flag_ldm(v, p.ws + OFF_ST, (s < NSEG - 2 ? F_CUR : F_PROG + 8 * s) + (h & 7), it > 3);
            WAIT6(0, mf, 0);
            asm volatile("" : "+v"(v));
            u32 m = 0;
#pragma unroll
            for (int i = 0; i < 24; ++i) m = max(m, mf[i >> 2][i & 3]);
            if (ok || __builtin_amdgcn_ballot_w64(m == ~0u) == 0) break;
            ok = it > 3 && uni((int)v) >= (s < NSEG - 2 ? s : 7);
            if (s < NSEG - 1) nap(60);
        }
    }
    WAIT6(0, mf, 0);
#pragma unroll
    for (int ks = 0; ks < 6; ++ks) { sc[0][2] = mfma16(mf[ks], qf[ks], sc[0][2]); sc[1][2] = mfma16(mf[ks], qf[6 + ks], sc[1][2]); }
    const float kscale = 0.07216878364870322f;
    char* pb = p.ws + OFF_PX + (size_t)s * 81920;
#pragma unroll
    for (int tt = 0; tt < 2; ++tt) {
        float v[12], mx = -3.0e38f;
#pragma unroll
        for (int i = 0; i < 12; ++i) {
            const float t = rbf(rbf(sc[tt][i >> 2][i & 3]) * kscale);
            v[i] = (i >= 8 || 4 * g + (i & 3) + 16 * (i >> 2) <= 16 * tt + n) ? t : -3.0e38f;
            mx = fmaxf(mx, v[i]);
        }
        mx = xmax(mx);
        float sx = 0.f, sm = 0.f;
#pragma unroll
        for (int i = 0; i < 12; ++i) {
            v[i] = __builtin_amdgcn_exp2f((v[i] - mx) * 1.4426950408889634f);
            if (i < 8) sx += v[i]; else sm += v[i];
        }
        const float inv = __builtin_amdgcn_rcpf(xsum(sx + 0.5f * sm));
        const u32x4 px = u32x4{cvtpk(v[0] * inv, v[1] * inv), cvtpk(v[2] * inv, v[3] * inv), cvtpk(v[4] * inv, v[5] * inv), cvtpk(v[6] * inv, v[7] * inv)};
        const u32x2 pm = u32x2{cvtpk(v[9] * inv, v[8] * inv), cvtpk(v[11] * inv, v[10] * inv)};
        const int ox = h * 2048 + tt * 1024, om = 65536 + (h >> 2) * 2048 + (h & 3) * 256 + tt * 1024;
        if (!LATE && keep) {
            *LDSP(u32x4, lds + ox + lane * 16) = px;
            *LDSP(u32x2, lds + om + n * 16 + (g & 1) * 8) = pm;
        } else {
            gst16c(pb + ox, lane * 16, px);
            const auto a = __builtin_amdgcn_permlane16_swap(pm[0], pm[0], false, false), b = __builtin_amdgcn_permlane16_swap(pm[1], pm[1], false, false);
            if (g == 0) gst16c(pb + om, (u32)(n * 16), u32x4{a[0], b[0], a[1], b[1]});
        }
    }
}

#define WAIT10(N, r) asm volatile("s_waitcnt vmcnt(" #N ")" : "+v"(r[0]), "+v"(r[1]), "+v"(r[2]), "+v"(r[3]), "+v"(r[4]), "+v"(r[5]), "+v"(r[6]), "+v"(r[7]), "+v"(r[8]), "+v"(r[9]) : : "memory")
DI void out_task(const Ptrs& p, int s, int cg0, int cg1, u32 lds, int slow, int w, int sd) {
    u32x4 pf[2][10];
    if (cg1 - cg0 > 1) {
        const u32 la = lds + lanex() * 16;
#pragma unroll
        for (int i = 0; i < 20; ++i) pf[i & 1][i >> 1] = ldsr16(la + (i < 16 ? w * 16384 + i * 1024 : 65536 + w * 4096 + (i - 16) * 1024));
    } else {
        const char* pb = p.ws + OFF_PX + (size_t)s * 81920;
        const u32 l16 = lanex() * 16;
        sfor<0, 20>([&](auto I) { gld16c<(I.v & 3) * 1024>(pf[I.v & 1][I.v >> 1], pb + (I.v < 16 ? w * 16384 + (I.v >> 2) * 4096 : 65536 + w * 4096), l16); gap<SMO>(slow); if (!SMO && I.v == 9) pace(slow); });
        WAIT10(0, pf[0]); WAIT10(0, pf[1]);
    }
#pragma nounroll
    for (int cg = cg0; cg < cg1; ++cg) {
    const int lane = lanex(), tid = 64 * w + lane, n = lane & 15, g = lane >> 4;
    const char* db = p.ws + OFF_D + s * D_STRIDE + (size_t)(16 * cg) * 16384 + w * 4096;
    const char* ub = p.ws + uslot(sd, s) + (size_t)(16 * cg) * 4096 + w * 1024;
    const u32 dof = (u32)((n >> 3) * 16384 + g * 128 + (n & 7) * 16), uof = (u32)((n >> 3) * 4096 + g * 128 + (n & 7) * 16);
    u32x4 af[3][10];
    auto aload = [&](u32x4 (&r)[10], int ct) {
        if (!SMO) pace(slow);
        sfor<0, 8>([&](auto I) { gld16x<I.v * 512>(r[I.v], db + ct * 32768, dof); gap<SMO>(slow); });
        gld16x<0>(r[8], ub + uchunk(ct), uof); gap<SMO>(slow); gld16x<512>(r[9], ub + uchunk(ct), uof); gap<SMO>(slow);
    };
    aload(af[0], 0);
    aload(af[1], 1);
    aload(af[2], 2);
    const int tok = 32 * s + (tid >> 3), col = 128 * cg + 8 * (tid & 7);
    u32x4 bb[2]; u32 gv;
    gld16c<0>(bb[0], p.ws + OFF_BOUT, (u32)(s * 40960 + col * 64 + (tid >> 3) * 16)); gld16c<0>(bb[1], p.ws + OFF_BOUT + 4096, (u32)(s * 40960 + col * 64 + (tid >> 3) * 16));
    asm volatile("global_load_dword %0, %1, %2 sc1" : "=v"(gv) : "v"(tok * 4), "s"(p.ws + OFF_GATE) : "memory");
    barrier();
    const u32 wa = lds + (u32)w * 16896 + n * 528 + g * 16;
    sfor<0, 8>([&](auto I) {
        constexpr int ct = I.v;
        u32x4 (&r)[10] = af[ct % 3];
        if constexpr (ct == 0) WAIT10(23, r);
        else if constexpr (ct < 3) WAIT10(23, r);
        else if constexpr (ct < 6) WAIT10(20, r);
        else if constexpr (ct == 6) WAIT10(10, r);
        else WAIT10(0, r);
        f32x4 c0 = f32x4{0.f, 0.f, 0.f, 0.f}, c1 = c0;
#pragma unroll
        for (int i = 0; i < 10; ++i) { c0 = mfma16(r[i], pf[0][i], c0); c1 = mfma16(r[i], pf[1][i], c1); }
        *LDSP(f32x4, wa + ct * 64) = c0;
        *LDSP(f32x4, wa + ct * 64 + 16 * 528) = c1;
        PIN();
        if constexpr (ct + 3 < 8) aload(r, ct + 3);
        PIN();
    });
    asm volatile("" : "+v"(bb[0]), "+v"(bb[1]), "+v"(gv));
    barrier();
    const u32 ra = lds + (u32)(tid >> 3) * 528 + (tid & 7) * 32;
    const float gt = __uint_as_float(gv), g1 = rbf(1.f - gt);
    u32 ov[8];
#pragma unroll
    for (int i = 0; i < 4; ++i) {
        const u32 ri = ra + 16 * (i & 1) + 256 * (i >> 1);
        const f32x4 z = (*LDSP(f32x4, ri) + *LDSP(f32x4, ri + 16896)) + (*LDSP(f32x4, ri + 2 * 16896) + *LDSP(f32x4, ri + 3 * 16896));
#pragma unroll
        for (int j = 0; j < 2; ++j) {
            const u32 b = bb[i >> 1][2 * (i & 1) + j];
            const u32 a = cvtpk(z[2 * j], z[2 * j + 1]);
            const u32 ga = cvtpk(gt * __uint_as_float(a << 16), gt * __uint_as_float(a & 0xffff0000u));
            const u32 gb = cvtpk(g1 * __uint_as_float(b << 16), g1 * __uint_as_float(b & 0xffff0000u));
            ov[2 * i + j] = cvtpk(__uint_as_float(ga << 16) + __uint_as_float(gb << 16), __uint_as_float(ga & 0xffff0000u) + __uint_as_float(gb & 0xffff0000u));
        }
    }
    gst16p(p.out, (u32)(tok * 1280 + col * 2), u32x4{ov[0], ov[1], ov[2], ov[3]});
    gst16p(p.out, (u32)(tok * 1280 + col * 2 + 128), u32x4{ov[4], ov[5], ov[6], ov[7]});
    }
}

DI void out_tail(const Ptrs& p, const ChainCfg& cfg, int s, int cg, u32 lds, int w, int sd, int gr) {
    const int lane = lanex(), tid = 64 * w + lane, n = lane & 15, g = lane >> 4;
    const char* db = p.ws + OFF_D + s * D_STRIDE + (size_t)(4 * cg) * 16384 + w * 4096;
    const char* ub = p.ws + uslot(sd, s) + uchunk(2 * cg) + w * 1024;
    const char* pb = p.ws + OFF_PX + (size_t)s * 81920;
    const u32 dof = (u32)((n >> 3) * 16384 + g * 128 + (n & 7) * 16), uof = (u32)((n >> 3) * 4096 + g * 128 + (n & 7) * 16);
    u32x4 pf[2][10], af[2][10];
    sfor<0, 16>([&](auto I) { gld16c<(I.v & 7) * 512>(af[I.v >> 3][I.v & 7], db + (I.v >> 3) * 32768, dof); });
    const int tok = 32 * s + (tid >> 3), col = 32 * cg + 4 * (tid & 7);
    u32x2 bb; u32 gv;
    asm volatile("global_load_dwordx2 %0, %2, %3 sc1\n\tglobal_load_dword %1, %4, %5 sc1\n\ts_waitcnt vmcnt(0)" : "=&v"(bb), "=&v"(gv)
                 : "v"(s * 40960 + (col >> 3) * 512 + (tid >> 3) * 16 + (col & 7) * 2), "s"(p.ws + OFF_BOUT), "v"(tok * 4), "s"(p.ws + OFF_GATE) : "memory");
    sfor<0, 16>([&](auto I) { waitv<0>(af[I.v >> 3][I.v & 7]); });
    until(step_wait(p.ws + OFF_ST, w, gr, s - 1, lds) + (u32)(s == NSEG - 1 ? cfg.cR : cfg.cR + 2 * PDX));
    const u32 wa = lds + (u32)w * 4608 + n * 144 + g * 16;
#pragma nounroll
    for (int it = 0; it < (1 << 14); ++it) {
        if (it == 4) {
            flag_wait(p.ws + OFF_ST, F_PX + s, 8);
            flag_wait(p.ws + OFF_ST, s < NSEG - 2 ? F_CUR + gr : F_UD + 2 * (s & 1), s < NSEG - 2 ? s + 1 : 16);
            flag_wait(p.ws + OFF_ST, s < NSEG - 2 ? F_ONE : F_UD + 2 * (s & 1) + 1, 16);
        }
        sfor<0, 4>([&](auto I) { gld16c<(I.v & 1) * 512>(af[I.v >> 1][8 + (I.v & 1)], ub + (I.v >> 1) * 16384, uof); });
        sfor<0, 8>([&](auto I) { gld16c<(I.v & 1) * 2048>(pf[0][I.v], pb + w * 16384 + (I.v >> 1) * 4096, lane * 16); });
        gld16c<0>(pf[0][8], pb + 65536 + w * 4096, lane * 16); gld16c<2048>(pf[0][9], pb + 65536 + w * 4096, lane * 16);
        sfor<0, 8>([&](auto I) { gld16c<(I.v & 1) * 2048 + 1024>(pf[1][I.v], pb + w * 16384 + (I.v >> 1) * 4096, lane * 16); });
        gld16c<1024>(pf[1][8], pb + 65536 + w * 4096, lane * 16); gld16c<3072>(pf[1][9], pb + 65536 + w * 4096, lane * 16);
        WAIT10(10, pf[0]); WAIT10(10, af[0]); WAIT10(10, af[1]);
        float chk = 0.f;
#pragma unroll
        for (int tt = 0; tt < 2; ++tt) {
            if (tt) WAIT10(0, pf[1]);
            f32x4 c0 = f32x4{0.f, 0.f, 0.f, 0.f}, c1 = c0;
#pragma unroll
            for (int i = 0; i < 10; ++i) { c0 = mfma16(af[0][i], pf[tt][i], c0); c1 = mfma16(af[1][i], pf[tt][i], c1); }
            *LDSP(f32x4, wa + tt * 2304) = c0;
            *LDSP(f32x4, wa + tt * 2304 + 64) = c1;
            const f32x4 c = c0 + c1;
            chk += (c[0] + c[1]) + (c[2] + c[3]);
            PIN();
        }
        if (it >= 4 || __builtin_amdgcn_ballot_w64((__float_as_uint(chk) & 0x7fffffffu) > 0x7f800000u) == 0) break;
        if (s < NSEG - 1) nap(60);
    }
    barrier();
    const u32 ra = lds + (u32)(tid >> 3) * 144 + (tid & 7) * 16;
    const float gt = __uint_as_float(gv), g1 = rbf(1.f - gt);
    const f32x4 z = (*LDSP(f32x4, ra) + *LDSP(f32x4, ra + 4608)) + (*LDSP(f32x4, ra + 2 * 4608) + *LDSP(f32x4, ra + 3 * 4608));
    u32x2 ov;
#pragma unroll
    for (int j = 0; j < 2; ++j) {
        const u32 b = bb[j], a = cvtpk(z[2 * j], z[2 * j + 1]);
        const u32 ga = cvtpk(gt * __uint_as_float(a << 16), gt * __uint_as_float(a & 0xffff0000u));
        const u32 gb = cvtpk(g1 * __uint_as_float(b << 16), g1 * __uint_as_float(b & 0xffff0000u));
        ov[j] = cvtpk(__uint_as_float(ga << 16) + __uint_as_float(gb << 16), __uint_as_float(ga & 0xffff0000u) + __uint_as_float(gb & 0xffff0000u));
    }
    *(u32x2*)(p.out + tok * 1280 + col * 2) = ov;
}

DI float dot2bf(u32 a, u32 b, float c) { return __builtin_amdgcn_fdot2_f32_bf16(__builtin_bit_cast(bf16x2, a), __builtin_bit_cast(bf16x2, b), c, false); }
DI void battn_task(const Ptrs& p, int t0, u32 lds, int w) {
    const int lane = lanex(), tid = 64 * w + lane, tl = tid & 31, t = t0 + tl;
    const char* const qkv = p.ws + OFF_QKVB + (size_t)(t0 >> 5) * 122880;
    {
        u32 so[3];
#pragma unroll
        for (int i = 0; i < 3; ++i) {
            const int e = 64 * i + lane, sl = e % 48;
            so[i] = (u32)((e / 48) * 512 + (sl < 16 ? (t0 ? 0 : 122880) + 256 + sl * 16 : 122880 - 256 + sl * 16));
        }
#pragma unroll
        for (int i = 0; i < 30; ++i) { glds16c(qkv - 122880 + 40960 + (10 * w + i / 3) * 2048, so[i % 3], lds + (30 * w + i) * 1024); gap<SMB>(-1); }
    }
    u32x4 q[2][5], xv[10], gv[10];
    sfor<0, 10>([&](auto I) { gld16x<(I.v % 5) * 512>(q[I.v / 5][I.v % 5], qkv + (I.v / 5) * 20480, (u32)((tid >> 5) * 2560 + tl * 16)); gap<SMB>(-1); });
    sfor<0, 10>([&](auto I) { gld16<I.v * 128>(xv[I.v], p.x + (size_t)t0 * 1280, (u32)((tid >> 3) * 1280 + (tid & 7) * 16)); gld16<I.v * 128>(gv[I.v], p.gw, (u32)((tid & 7) * 16)); gap<SMB>(-1); });
    sfor<0, 10>([&](auto I) { waitv<0>(q[I.v / 5][I.v % 5]); waitv<0>(xv[I.v]); waitv<0>(gv[I.v]); });
    barrier();
    {
        float a = 0.f;
#pragma unroll
        for (int i = 0; i < 10; ++i)
#pragma unroll
            for (int j = 0; j < 4; ++j) {
                const u32 r = cvtpk(__uint_as_float(xv[i][j] << 16) * __uint_as_float(gv[i][j] << 16), __uint_as_float(xv[i][j] & 0xffff0000u) * __uint_as_float(gv[i][j] & 0xffff0000u));
                a += __uint_as_float(r << 16) + __uint_as_float(r & 0xffff0000u);
            }
        a += __shfl_xor(a, 1); a += __shfl_xor(a, 2); a += __shfl_xor(a, 4);
        const float z = rbf(a);
        if ((tid & 7) == 0) gst4c(p.ws + OFF_GATE, (u32)((t0 + (tid >> 3)) * 4), __float_as_uint(rbf(1.0f / (1.0f + __expf(-z)))));
    }
#pragma unroll
    for (int it = 0; it < 2; ++it) {
        const int m = (tid >> 5) + 8 * it;
        u32 ka = lds + (u32)(m * 3840 + (2 + tl) * 16), va = ka + 61440;
        asm volatile("" : "+v"(ka), "+v"(va));
        u32x4 k[5][8];
#pragma unroll
        for (int i = 0; i < 40; ++i) k[i >> 3][i & 7] = ldsr16(ka + (i >> 3) * 768 + (7 - (i & 7)) * 32);
        PIN();
        float lg[2][8], mx[2] = {-3.0e38f, -3.0e38f};
#pragma unroll
        for (int i = 0; i < 16; ++i) lg[i >> 3][i & 7] = 0.f;
#pragma unroll
        for (int i = 0; i < 5; ++i) {
#pragma unroll
            for (int j = 0; j < 4; ++j)
#pragma unroll
                for (int wv = 0; wv < 8; ++wv) lg[4 * i + j >= 10][wv] = dot2bf(q[it][i][j], k[i][wv][j], lg[4 * i + j >= 10][wv]);
            PIN();
#pragma unroll
            for (int wv = 0; wv < 8; ++wv) k[i][wv] = ldsr16(va + i * 768 + (7 - wv) * 32);
            PIN();
        }
#pragma unroll
        for (int i = 0; i < 16; ++i) {
            float& l = lg[i >> 3][i & 7];
            l = rbf(rbf(l) * 0.22360679774997896f);
            asm volatile("" : "+v"(l));
            l = t - 2 * (i & 7) >= 0 ? l : -3.0e38f;
            mx[i >> 3] = fmaxf(mx[i >> 3], l);
        }
        u32 pk[2][4];
#pragma unroll
        for (int u = 0; u < 2; ++u) {
            float sum = 0.f;
#pragma unroll
            for (int wv = 0; wv < 8; ++wv) { lg[u][wv] = __builtin_amdgcn_exp2f((lg[u][wv] - mx[u]) * 1.4426950408889634f); sum += lg[u][wv]; }
            const float inv = __builtin_amdgcn_rcpf(sum);
#pragma unroll
            for (int wv = 0; wv < 4; ++wv) pk[u][wv] = cvtpk(lg[u][2 * wv] * inv, lg[u][2 * wv + 1] * inv);
        }
#pragma unroll
        for (int i = 0; i < 5; ++i) {
            float o[8];
#pragma unroll
            for (int d = 0; d < 8; ++d) o[d] = 0.f;
#pragma unroll
            for (int wp = 0; wp < 4; ++wp) {
                const u32x4 v0 = k[i][2 * wp], v1 = k[i][2 * wp + 1];
#pragma unroll
                for (int j = 0; j < 4; ++j) {
                    const u32 pw = pk[4 * i + j >= 10][wp];
                    o[2 * j] = dot2bf(__builtin_amdgcn_perm(v1[j], v0[j], 0x05040100u), pw, o[2 * j]);
                    o[2 * j + 1] = dot2bf(__builtin_amdgcn_perm(v1[j], v0[j], 0x07060302u), pw, o[2 * j + 1]);
                }
            }
            gst16c(p.ws + OFF_OB + (size_t)(t0 >> 5) * 40960 + 512 * i, (u32)(m * 2560 + tl * 16), u32x4{cvtpk(o[0], o[1]), cvtpk(o[2], o[3]), cvtpk(o[4], o[5]), cvtpk(o[6], o[7])});
        }
    }
}

#define XPF 1
#define PFQ 0
#define PFN 8
#define OLAG 3
constexpr int QN = 176, QF = 19;
struct QTab { u16 e[QN]; int n, nf; u32 tt[64]; };
typedef const __attribute__((address_space(4))) int* KI;
constexpr int QT_OFF = (88 + 48) / 4;
DI int pick_task(char* fl, KI k, int gr) {
    u32 r;
    asm volatile("s_mov_b64 exec, 1\n\ts_nop 3\n\tglobal_atomic_add %0, %1, %2, %3 sc0 sc1\n\ts_mov_b64 exec, -1\n\ts_waitcnt vmcnt(0)"
                 : "=&v"(r) : "v"((F_QH + gr) * 128), "v"(1), "s"(fl) : "memory");
    const int x = uni((int)r) + 1 + QF;
    if (x >= k[QT_OFF + QN / 2]) return -1;
    const u32 e2 = (u32)k[QT_OFF + (x >> 1)];
    return (int)(x & 1 ? e2 >> 16 : e2 & 0xffffu);
}
__global__ __launch_bounds__(256, 1) void k_main(Ptrs p0, ChainCfg cfg, QTab tab) {
    extern __shared__ __attribute__((aligned(16))) char smem[];
    u32 lds = (u32)(size_t)LDSP(char, smem);
#define HALF 0
    int n = HALF ? (blockIdx.x & 4) * 32 + (int)(blockIdx.x >> 3) * 4 + (blockIdx.x & 3) : blockIdx.x, gr = n & 7;
    int w = uni(threadIdx.x >> 6);
    {
        u64 pc; int len;
        asm volatile("s_getpc_b64 %0\n.Lkm_first:\n\ts_mov_b32 %1, .Lkm_last-.Lkm_first\n\ts_max_i32 %1, %1, .Lkm_lu-.Lkm_first\n\ts_max_i32 %1, %1, .Lkm_lv-.Lkm_first\n\t"
                     "s_max_i32 %1, %1, .Lkm_g2-.Lkm_first\n\ts_max_i32 %1, %1, .Lkm_g4-.Lkm_first" : "=s"(pc), "=s"(len) : : "scc");
        if (w == 3) pfl((const char*)pc, (u32)min((int)(blockIdx.x >> 3) * 8192 + (int)(threadIdx.x & 63) * 128, max(len, 0)), lds);
    }
    karg_warm();
    if (n < NH) { score_role(p0, cfg, n, lds, w); return; }
    if (n < NS) { proj_role(p0, cfg, n - NH, lds, w); return; }
    const bool isv = n < NS + NVB;
#define qslot (lds + GRING * GBUF)
    int sdw = -1;
    int en = isv ? Q_VD + (n & 24) : tab.e[(n - NS - NVB) >> 3] & 511;
    u32 phi0 = 0;
    Sync y;
    if (XPF && n < 160 && w >= 2) pfl(p0.x, (u32)min(((n - NS) >> 3) * 128 + (w & 1) * 64 + lanex(), 1279) * 128, lds);
    if (n >= 160) {
        const int lane = lanex();
        if (n >= 192 && (n & 8)) init_task(p0, (n & 7) + ((n - 192) >> 4) * 8, lds, w);
        else {
            const int h = n < 192 ? n & 31 : (n & 7) + ((n - 192) >> 4) * 8;
            xcopy_task(p0, h + (n < 192 ? 0 : 32), w);
            if (n < 192) {
#pragma unroll
                for (int i = 0; i < PFN; ++i) {
                    const int l = 64 * min(i * 4 + w, 29) + lane;
                    pfl(p0.wo + (size_t)h * 245760, (u32)(l * 128), lds);
                    if (PFQ) pfl(p0.wq + h * 384, (u32)((l / 3) * 12288 + (l % 3) * 128), lds);
                }
            }
        }
        if (w == 1) {
            sync_ld(y, p0.ws);
            waitvm<0>();
            asm volatile("" : "+v"(y.r), "+v"(y.g), "+v"(y.e));
            if (n < 192) xcopy_wait(p0.ws, sync_tag(y.e));
            else xcopy_wait(p0.ws, sync_tag(y.e), n & 8 ? 4 : 8, n & 8 ? 3 : 7);
        }
        if (w == 0 && n < 192) {
            sync_ld(y, p0.ws);
            waitvm<0>();
            *LDSP(u32, qslot) = (u32)sync_end(y, p0.ws, phi0, false);
        }
        barrier();
        if (w == 0 && n < 192) flag_add(p0.ws + OFF_ST, F_ACK, 1);
        if (n < 192) sdw = uni((int)*LDSP(u32, qslot));
        barrier();
    }
    while (true) {
        typedef char* const __attribute__((address_space(4)))* KP;
        KP kp = (KP)__builtin_amdgcn_kernarg_segment_ptr();
        asm volatile("" : "+s"(kp), "+s"(lds), "+s"(w), "+s"(gr), "+s"(n));
        __builtin_assume(w >= 0 && w < 4 && gr >= 0 && gr < 8);
        const Ptrs p{kp[0], kp[1], kp[2], kp[3], kp[4], kp[5], kp[6], kp[7], kp[8], kp[9], kp[10]};
        const int ty = en & 7, gate = en >> 9;
        const bool fu = ty == Q_PX && (en >> 3 & 63) < 8;
        const int ix = fu ? (en & 504) + gr : en >> 3 & 63, sf = ((KI)kp)[QT_OFF + QN / 2 + 1] & 255;
        char* const fl = p.ws + OFF_ST;
        const bool raw = sdw < 0;
        int t0 = gate - 1, f1 = F_ONE, t1 = 0, f2 = F_ONE, t2 = 0, f3 = F_ONE, t3 = 0, dt = cfg.T2;
        const int os = ix >= 40 ? 44 + (8 * ix + gr) / 20 : (8 * ix + gr) / 5;
        const int bb = ty == Q_BO ? (8 * ix + gr) >> 2 : os;
        if (ty == Q_BA) { t0 += gr * BAS >> 3; f1 = F_B + (gr & RM) | ix << 16; t1 = 5; f2 = F_B + (gr & RM) | max(ix - 1, 0) << 16; t2 = 5; }
        if (ty == Q_BO) { f1 = F_B + (gr & RM) | (8 + bb) << 16; t1 = 8; }
        if (ty == Q_PX) { f1 = F_Q + pblk(ix) * 8 + (fu ? gr & RM : gr) | fu << 16; t1 = fu ? 32 : 4; if (fu) t0 += gr * (((KI)kp)[QT_OFF + QN / 2 + 1] >> 8) >> 3; if (ix < PXT) t0 = max(t0, ix + 1); }
        if (ty == Q_OUT) { f3 = F_B + (gr & RM) | (16 + (os >> 3)) << 16; t3 = 4; if (os < 60) { f1 = F_PX + os; t1 = 8; t0 = max(t0, os + (os < PXT ? OLAG : 1)); if (os >= PXT) dt = cfg.s1; } else { f1 = F_VD + 64 + (gr & RM); t1 = 32; } }
        const bool skip = fu ? ix >= sf : ty == Q_OUT && ix < 40 && (os < sf || os >= 60);
        if ((gate || ty >= Q_BA) && !skip) wg_wait(fl, w, F_CUR + gr, t0, f1, t1, f2, t2, f3, t3, dt, false);
        int sig = F_END, nrep = 1;
        if (skip) {}
        else if (ty <= Q_QKVB || ty == Q_BO) {
            const bool ab = ty <= Q_KQ;
            sig = ty == Q_VD ? F_VD + (ix >> 2) * 8 : (ty == Q_KQ ? F_Q + (ix >> 2) * 8 : (ix & 4 ? F_Q : F_K + 8 * ix)) + gr;
            if (!ab) sig = F_B | (bb + (ty == Q_BO ? 16 : 0)) << 16;
            nrep = ty == Q_VD || !ab ? RM + 1 : 1;
            if (ty == Q_VD && ix < 4) { sig += n & 32 ? 4 : 0; nrep = 4; }
            run_gemm(p, ty, ab ? ix >> 2 : bb, ab ? gr + 8 * (ix & 3) : 8 * ix + gr - (ty == Q_BO ? 4 : 5) * bb, (n >> 5) & 1, raw && n < 160, y, lds, w);
            if (raw) sdw = sync_end(y, p.ws, phi0, w == 0);
        } else {
            if (ty == Q_BA) {
                battn_task(p, (8 * ix + gr) * 32, lds, w);
                sig = F_B | (8 + ix) << 16; nrep = RM + 1;
            } else if (ty == Q_PX) {
                if (ix < PXT) {
#pragma nounroll
                    for (int i = fu ? 0 : gr; i < (fu ? 8 : gr + 1); ++i) px_wave<false>(p, cfg, ix, i + 8 * w, ix - cfg.fast, w, lds, fu);
                } else px_wave<true>(p, cfg, ix, gr + 8 * w, -1, w, lds, false);
                sig = F_PX + ix;
                if (fu) wg_wait(fl, w, F_ONE, 0, F_B + (gr & RM) | (16 + (ix >> 3)) << 16, 4, F_ONE, 0, F_ONE, 0, dt, false);
            }
            if (ty == Q_OUT || fu) {
                const int cg = 8 * ix + gr - 5 * os;
                if (ix >= 40 && !fu) out_tail(p, cfg, os, 8 * ix + gr - 20 * (os - 44), lds, w, sdw, gr);
                else out_task(p, fu ? ix : os, fu ? 0 : cg, fu ? 5 : cg + 1, lds, (fu ? ix : os) + 1 - cfg.fast, w, sdw);
                sig = F_END;
            }
            waitvm<0>();
        }
        barrier();
        if (w == 0 && sig != F_END) flag_add(fl, sig, nrep);
        if (w == 1 && sig >= F_Q && sig < F_N) flag_add(fl, sig - gr | 1 << 16, RM + 1);
        if (raw && !isv && w == 0) xcopy_wait(p.ws, sync_tag(y.e));
        if (isv) break;
        if (w == 0) *LDSP(int, qslot) = pick_task(fl, (KI)kp, gr);
        barrier();
        en = uni(*LDSP(int, qslot));
        if (en < 0) return;
        barrier();
    }
    value_role(p0, cfg, n - NS, sdw, phi0, lds, w);
}

#define QCFG 14, 11, 3, 5, 0, 50, 36
#define LVDEF "00@?>===<<<<<<<<<;:99::;;;;;;;;;;;;;;;<<;:9988888899999999888886"
constexpr int DX = 340, DUA = 430, DUB = 0, DUC = 4;
static ChainCfg dev_cfg(ChainCfg c) { c.T = PDL; c.cR = ODL; c.s1 = TDT; c.T2 = WSLEEP; c.cR2 = DX; c.dU = DUA; c.s2 = DUB; c.T3 = DUC; c.dV |= 4 << 8 | 8 << 16 | 0 << 24; c.cR3 = 4 | 4 << 8 | 0 << 16; return c; }
static QTab set_sched(QTab t, const ChainCfg c, const char* lv) {
    for (int s = 0; s < 64; ++s) {
        const int d = lv[s] - '0';
        t.tt[s] = (u32)((s < c.s1 ? c.T : (s < c.s2 ? c.T2 : c.T3)) + 4 * d) | (u32)((s < c.s1 ? c.cR : (s < c.s2 ? c.cR2 : c.cR3)) + 5 * d / 2) << 16;
    }
    t.tt[0] = (u32)270 | (u32)165 << 16;
    t.tt[1] = (u32)210 | (u32)144 << 16;
    return t;
}
#define QORD "bbV3qbK3m44bm55bm66m77m88"
#define GATES "11 13 15 17 22 24 32 36 21 25 32 44 14 18 22 27 33 41"
static QTab make_tab(int lv, int lk, int b0, int d1, int d2, int lg, int nf) {
    QTab t{};
    int n = 0, dl[QN], nq = 0;
    auto add = [&](int ty, int ix) { t.e[n] = (u16)(ty | ix << 3); dl[n++] = ty == Q_VD || ty == Q_KQ ? 8 * (ix / 4 - 1) - (ty == Q_VD ? lv : lk) - (ix >= 32 ? 0 : 0) : 99; };
    for (int i = 1; i < 4; ++i) add(Q_VD, i);
    for (int i = 0; i < 16; ++i) add(i < 4 ? Q_H : ((i & 8) ? Q_VD : Q_KQ), i < 8 ? i : 8 - (i & 1) * 4 + ((i - 8) >> 1));
    for (int i = 0; i < 4; ++i) add(Q_KQ, 8 + i);
    for (const char* c = QORD; *c; ++c) {
        if (*c == 'b') add(Q_QKVB, nq++);
        else if (*c == 'q') for (int i = 4; i < 8; ++i) add(Q_H, i);
        else if (*c == '.') dl[n - 1] |= 1 << 20;
        else if (*c == 'w') { const int b = *++c - '0'; for (int i = 0; i < 16; ++i) add(i & 2 ? Q_KQ : Q_VD, 4 * (b + (i & 1)) + (i >> 2)); }
        else if (*c == 'n') { const int b = *++c - '0'; for (int i = 0; i < 8; ++i) add(i & 1 ? Q_KQ : Q_VD, 4 * b + (i >> 1) - (i & 1) * 2); }
        else if (*c == 'h' || *c == 'e') { const int b = 4 * (c[1] - '0') + (*c == 'e') * 2; ++c; add(Q_KQ, b); add(Q_KQ, b + 1); }
        else if (*c == 'm') { const int b = c[1] - '0', b2 = c[2] - '0'; c += 2; for (int i = 0; i < 8; ++i) add(i & 1 ? Q_KQ : Q_VD, 4 * (i & 1 ? b2 : b) + ((i >> 1) ^ (i & 2 ? 0 : 0))); }
        else {
            const int ty = (*c | 32) == 'v' ? Q_VD : Q_KQ, two = *c >> 5 & 1, b = *++c - '0';
            for (int i = 0; i < (4 << two); ++i) add(ty, 4 * (b + (i & two)) + (i >> two));
        }
    }
    const int gap = 8;
    for (int i = n - 1, m = 999; i >= 0; --i) { const int pz = dl[i] > 999; m = min(8 * (pz ? dl[i] - (1 << 20) : dl[i]), m - gap - 8 * pz); dl[i] = max(m, 0); }
    auto put = [&](int ty, int ix, int g, int pos) {
        int i = n++;
        for (; dl[i - 1] > pos; --i) { t.e[i] = t.e[i - 1]; dl[i] = dl[i - 1]; }
        t.e[i] = (u16)(ty | ix << 3 | (g + 1) << 9); dl[i] = pos;
    };
    for (int i = 0; i < n; ++i) if (dl[i] >= 8) t.e[i] |= (u16)((dl[i] / 8 + 1) << 9);
    const char* gs = GATES;
    auto gt = [&](int i) { return 10 * (gs[3 * i] - '0') + gs[3 * i + 1] - '0'; };
    for (int i = 0; i < 8; ++i) put(Q_BA, i, gt(i), 8 * gt(i));
    for (int i = 0; i < 4; ++i) put(Q_BO, i, gt(8 + i), 8 * gt(8 + i));
    for (int i = 0; 8 * i < nf; ++i) put(Q_PX, i, gt(12 + i), 8 * (max(gt(12 + i), 8 * i + 1) + b0));
    auto pg = [&](int s) { return max(8 * s + 8, 8 * 48 + (s - nf) * 3); };
    for (int s = nf; s < NSEG; ++s) { const int g = s < PXT ? pg(s) : 8 * max(s - 3, lg); put(Q_PX, s, g / 8, g); }
    for (int x = 5 * nf / 8; x < 38; ++x) { const int os = (8 * x + 4) / 5, g = pg(min((8 * x + 7) / 5, PXT - 1)); put(Q_OUT, x, g > 8 * ((8 * x + 7) / 5) + 8 ? g / 8 + OLAG - 1 : 0, os < PXT ? pg(os) + 8 * (OLAG - 1) : 8 * (os + 1)); }
    for (int x = 40; x < 50; ++x) { const int g = max(44 + 8 * x / 20 - 3, lg); put(Q_OUT, x, g, 8 * g + 4); }
    t.n = n; t.nf = nf | d1 << 8;
    return t;
}

extern "C" void kernel_launch(void* const* d_in, const int* in_sizes, int n_in, void* d_out, int out_size, void* d_ws,
                              size_t ws_size, hipStream_t stream) {
    Ptrs p{(const char*)d_in[0], (const char*)d_in[1], (const char*)d_in[2], (const char*)d_in[3], (const char*)d_in[4], (const char*)d_in[5],
           (const char*)d_in[8], (const char*)d_in[9], (const char*)d_in[10], (char*)d_out, (char*)(((size_t)d_ws + 0x1fffff) & ~(size_t)0x1fffff)};
    (void)hipFuncSetAttribute((const void*)k_main, hipFuncAttributeMaxDynamicSharedMemorySize, S_LDS);
    k_main<<<256, 256, S_LDS, stream>>>(p, dev_cfg(ChainCfg{CFG}), set_sched(make_tab(QCFG), ChainCfg{CFG}, LVDEF));
    static_assert(sizeof(Ptrs) + sizeof(ChainCfg) + sizeof(QTab) == TT_OFF + 256, "");
}
```

```cpp
#include <hip/hip_runtime.h>
#pragma clang diagnostic ignored "-Wint-to-pointer-cast"

typedef unsigned short u16;
typedef unsigned int u32;
typedef unsigned long long u64;
typedef __bf16 bf16x8 __attribute__((ext_vector_type(8)));
typedef __bf16 bf16x16 __attribute__((ext_vector_type(16)));
typedef short s16x4 __attribute__((ext_vector_type(4)));
typedef float f32x4 __attribute__((ext_vector_type(4)));
typedef unsigned u32x4 __attribute__((ext_vector_type(4)));
typedef unsigned u32x2 __attribute__((ext_vector_type(2)));
typedef unsigned u32x8 __attribute__((ext_vector_type(8)));
typedef unsigned u32x16 __attribute__((ext_vector_type(16)));
typedef unsigned u32x32 __attribute__((ext_vector_type(32)));
#define DI __device__ __forceinline__
#define LDSP(T, a) ((__attribute__((address_space(3))) T*)(a))

constexpr int S_LEN = 2048, DM = 640, NH = 32, DH = 192, NSEG = 64, HD = NH * DH;
#define NSAMP 4
constexpr int NREG = 13;
constexpr int NV = 80, NVB = 40;
constexpr int NS = 64;
#define CFG 147, 105, 104, 20, 57, 16, 147, 105, 52, 147, 105, 6

constexpr size_t U_STRIDE = NV * 4096, D_STRIDE = NV * 16384;
constexpr size_t HO_STRIDE = 65536;
constexpr size_t OFF_ST = NSEG * HO_STRIDE;
enum { F_ACK = 0, F_RDY = 1, F_ONE = 3, F_GO = 8, F_VD = 16, F_QH = 96, F_B = 104, F_CUR = 128, F_PROG = 136 - 8 * 62, F_UD = 152, F_PX = 160, F_K = 224, F_END = 512, F_Q = 520, F_N = 592 };
constexpr size_t OFF_LAT = OFF_ST + F_N * 128;
constexpr size_t OFF_KX = 5u << 20;
constexpr size_t OFF_QX = OFF_KX + (size_t)S_LEN * HD * 2;
constexpr size_t OFF_D = OFF_QX + (size_t)S_LEN * HD * 2;
constexpr size_t OFF_U = (OFF_D + NSEG * D_STRIDE + 0x1fffff) & ~(size_t)0x1fffff;
constexpr size_t OFF_KM = OFF_U + (22u << 20);
constexpr size_t OFF_PX = OFF_KM + (size_t)NSEG * NH * 3072;
constexpr size_t OFF_QKVB = OFF_PX + (size_t)NSEG * 81920;
constexpr size_t OFF_OB = OFF_QKVB + (size_t)S_LEN * 1920 * 2;
constexpr size_t OFF_BOUT = OFF_OB + (size_t)S_LEN * DM * 2;
constexpr size_t OFF_GATE = OFF_BOUT + (size_t)S_LEN * DM * 2;
constexpr size_t OFF_XF = OFF_GATE + S_LEN * 4;
constexpr size_t WS_END = OFF_XF + (size_t)S_LEN * DM * 2;
static_assert(WS_END + (2u << 20) <= (256u << 20), "workspace");

typedef __bf16 bf16x2 __attribute__((ext_vector_type(2)));
typedef float f32x2 __attribute__((ext_vector_type(2)));
DI u32 cvtpk(float lo, float hi) { return __builtin_bit_cast(u32, __builtin_convertvector(f32x2{lo, hi}, bf16x2)); }
DI float rbf(float f) { return __uint_as_float(cvtpk(f, f) << 16); }
DI f32x4 mfma16(u32x4 a, u32x4 b, f32x4 c) {
    return __builtin_amdgcn_mfma_f32_16x16x32_bf16(__builtin_bit_cast(bf16x8, a), __builtin_bit_cast(bf16x8, b), c, 0, 0, 0);
}
DI u32x2 trread(u32 a) {
    return __builtin_bit_cast(u32x2, __builtin_amdgcn_ds_read_tr16_b64_v4i16(LDSP(s16x4, a)));
}
DI void glds16c(const char* sbase, u32 voff, u32 lds) {
    u32 sv;
    asm volatile("s_mov_b32 %0, m0\n\ts_mov_b32 m0, %3\n\ts_nop 0\n\tglobal_load_lds_dwordx4 %1, %2 sc1\n\ts_mov_b32 m0, %0"
                 : "=&s"(sv) : "v"(voff), "s"(sbase), "s"(lds) : "memory");
}
template <int IMM>
DI void gld16(u32x4& r, const char* sbase, u32 voff) {
    asm volatile("s_nop 4\n\tglobal_load_dwordx4 %0, %1, %2 offset:%3" : "=v"(r) : "v"(voff), "s"(sbase), "i"(IMM) : "memory");
}
DI void gst16(char* sbase, u32 voff, u32x4 v) {
    asm volatile("global_store_dwordx4 %0, %1, %2 sc1\n\ts_nop 1" : : "v"(voff), "v"(v), "s"(sbase) : "memory");
}
#define OST " nt"
DI void gst16p(char* sbase, u32 voff, u32x4 v) {
    asm volatile("s_nop 4\n\tglobal_store_dwordx4 %0, %1, %2" OST "\n\ts_nop 1" : : "v"(voff), "v"(v), "s"(sbase) : "memory");
}
DI void gst8(char* sbase, u32 voff, u32x2 v) {
    asm volatile("global_store_dwordx2 %0, %1, %2 sc1\n\ts_nop 1" : : "v"(voff), "v"(v), "s"(sbase) : "memory");
}
template <int N>
DI void waitvm() { asm volatile("s_waitcnt vmcnt(%0)" : : "i"(N) : "memory"); }
DI void barrier() { asm volatile("s_waitcnt lgkmcnt(0)\n\ts_barrier" : : : "memory"); }
#define PIN() __builtin_amdgcn_sched_barrier(0)
DI int uni(int v) { return __builtin_amdgcn_readfirstlane(v); }
DI int lanex() {
    int t;
    asm volatile("v_mbcnt_lo_u32_b32 %0, -1, 0\n\tv_mbcnt_hi_u32_b32 %0, -1, %0" : "=v"(t));
    __builtin_assume(t >= 0 && t < 64);
    return t;
}
DI void flag_ld(u32& r, const char* fl, int idx) {
    asm volatile("s_nop 4\n\tglobal_load_dword %0, %1, %2 sc1" : "=v"(r) : "v"((idx & 0xffff) * 128 + (idx >> 14)), "s"(fl) : "memory");
}
DI void flag_ldm(u32& r, const char* fl, int idx, bool on) {
    asm volatile("v_mov_b32 %0, 1000\n\ts_mov_b32 exec_lo, %3\n\ts_mov_b32 exec_hi, 0\n\tglobal_load_dword %0, %1, %2 sc1\n\ts_mov_b64 exec, -1" : "=&v"(r) : "v"(idx * 128), "s"(fl), "s"(uni(on ? 1 : 0)) : "memory");
}
DI void flag_wait(const char* fl, int idx, int target) {
#pragma nounroll
    for (int it = 0; it < (1 << 16); ++it) {
        u32 v;
        flag_ld(v, fl, idx);
        asm volatile("s_waitcnt vmcnt(0)" : "+v"(v) : : "memory");
        if (uni((int)(v + 1u)) >= target) break;
        __builtin_amdgcn_s_sleep(6);
    }
}
DI void flag_add(char* fl, int idx, int nrep) {
    const int lane = lanex();
    if (lane < nrep) asm volatile("s_nop 4\n\tglobal_atomic_add %0, %1, %2 sc1" : : "v"(((idx & 0xffff) + lane) * 128 + (idx >> 14)), "v"(1), "s"(fl) : "memory");
}
DI void acquire() { asm volatile("buffer_inv sc1\n\ts_waitcnt vmcnt(0)" : : : "memory"); }

template <int V> struct IC { static constexpr int v = V; };
template <int B, int E, class Fn>
DI void sfor(Fn&& fn) { if constexpr (B < E) { fn(IC<B>{}); sfor<B + 1, E>(fn); } }
enum { T_QK = 0, T_VD = 1, T_BL = 3 };
constexpr int GBUF = 24576, GRING = 6;
struct GemmArgs {
    const char *w1, *w1b;
    const char* w2;
    const char* x;
    char *out, *outb;
    int ldw;
    char* sig;
    int type, np1, tn;
    int sV, sP, sQ;
    int xb;
};
DI const char* unip(const char* p) {
    const u64 v = (u64)p;
    return (const char*)(((u64)(u32)uni((int)(v >> 32)) << 32) | (u32)uni((int)v));
}
template <int Z>
DI void mf1(f32x4& c, const u32x4& a, const u32x4& b) {
    if constexpr (Z) asm volatile("v_mfma_f32_16x16x32_bf16 %0, %1, %2, 0" : "=&v"(c) : "v"(a), "a"(b));
    else asm volatile("v_mfma_f32_16x16x32_bf16 %0, %1, %2, %0" : "+v"(c) : "v"(a), "a"(b));
}
template <int Z>
DI void mf2(f32x4& c, const u32x4& a, const u32x4& b) {
    if constexpr (Z) asm volatile("v_mfma_f32_16x16x32_bf16 %0, %1, %2, 0" : "=&v"(c) : "v"(a), "v"(b));
    else asm volatile("v_mfma_f32_16x16x32_bf16 %0, %1, %2, %0" : "+v"(c) : "v"(a), "v"(b));
}
template <int IMM>
DI void gld16a(u32x4& r, const char* sbase, u32 voff) {
    asm volatile("global_load_dwordx4 %0, %1, %2 offset:%3 sc1" : "+a"(r) : "v"(voff), "s"(sbase), "i"(IMM) : "memory");
}
DI void setm0(u32 v) { asm volatile("s_mov_b32 m0, %0" : : "s"(v) : "memory"); }
template <int IMM>
DI void gldsm(const char* sbase, u32 voff) {
    asm volatile("global_load_lds_dwordx4 %0, %1 offset:%2" : : "v"(voff), "s"(sbase), "i"(IMM) : "memory");
}
DI void gst16n(char* sbase, u32 voff, u32x4 v) {
    asm volatile("global_store_dwordx4 %0, %1, %2 sc1" : : "v"(voff), "v"(v), "s"(sbase) : "memory");
}
template <int N>
DI void waitbar() { asm volatile("s_waitcnt vmcnt(%0)\n\ts_barrier" : : "i"(N) : "memory"); }
#define LGKM(n) __builtin_amdgcn_s_waitcnt(0xC07F | ((n) << 8))

#define XSL 10
DI int xhalf() { u32 x; asm volatile("s_getreg_b32 %0, hwreg(HW_REG_XCC_ID)" : "=s"(x)); return (int)((x >> 2) & 1); }
struct Sync { u32x2 r, g, e; };
DI void sync_ld(Sync& y, const char* ws) {
    asm volatile("s_nop 4\n\tglobal_load_dwordx2 %0, %3, %5 sc1\n\tglobal_load_dwordx2 %1, %4, %5 offset:512 sc1\n\tglobal_load_dwordx2 %2, %4, %5 offset:1024 sc1"
                 : "=&v"(y.r), "=&v"(y.g), "=&v"(y.e) : "v"((lanex() & 31) * 8), "v"(0), "s"(ws + OFF_LAT) : "memory");
}
DI u64 sync_tag(u32x2 e) { return (((u64)e[1] << 32 | e[0]) + 1) << 1; }
DI int sync_end(Sync& y, const char* ws, u32& phi, bool ack) {
    asm volatile("" : "+v"(y.r), "+v"(y.g), "+v"(y.e));
#pragma nounroll
    for (int it = 0; it < (1 << 16); ++it) {
        const u64 t = sync_tag(y.e);
        if (__builtin_amdgcn_ballot_w64((y.r[0] & ~1u) != (u32)t || y.r[1] != (u32)(t >> 32) || y.g[0] != (u32)t) == 0) break;
        __builtin_amdgcn_s_sleep(XSL);
        sync_ld(y, ws);
        asm volatile("s_waitcnt vmcnt(0)" : "+v"(y.r), "+v"(y.g), "+v"(y.e) : : "memory");
    }
    if (ack) flag_add((char*)ws + OFF_ST, F_ACK, 1);
    phi = (u32)uni((int)y.g[1]);
    const u32 b = (u32)__builtin_amdgcn_ballot_w64(y.r[0] & 1), all = __builtin_popcount(b) > 16;
    u32 q = 0;
#pragma unroll
    for (int r = 0; r < NREG; ++r) {
        const u32 c = r < 2 ? __builtin_popcount(b & (0x55u << r)) + all : 2 * __builtin_popcount((b >> (4 + 2 * r)) & 3) + all;
        q |= ((c > 2) ^ (u32)((r < 2 ? r : r + (OFF_U >> 21)) & 1)) << r;
    }
    return uni((int)(q ^ 3u * xhalf()));
}
DI void pace(int on) {
    asm volatile("s_cmp_ge_i32 %0, 0\n\ts_cbranch_scc1 .Lp%=\n\ts_waitcnt vmcnt(0)\n.Lp%=:" : : "s"(on) : "memory", "scc");
}
#define SMO 48
#define RM 7
#define BLS 1
#define EPG 1
#define PXT 58
#define PDX 40
#define SPL 0
#define HP0 48
#define UPF 5
#define FRD 25
#define KNT 1
#define BAS 3
#define NO2 2
#define OW 2
#define GSL 6
#define UNAP 700
#define HD1 40
#define SMB 48
#define SMP SMO
template <int C>
DI void gap(int on) {
    if constexpr (C > 0) asm volatile("s_cmp_ge_i32 %0, 0\n\ts_cbranch_scc1 .Lg%=\n\ts_nop %1\n\ts_nop %2\n\ts_nop %3\n.Lg%=:" : : "s"(on), "n"(C / 12 - 1), "n"(C / 12 - 1), "n"(C / 12 - 1) : "memory", "scc");
}
DI void early_add(char* ctr, int c) {
    asm volatile("s_cmp_lg_u32 %1, 1\n\ts_cbranch_scc1 .Le%=\n\ts_mov_b64 exec, 1\n\ts_nop 1\n\tglobal_atomic_add %2, %3, %0 sc1\n\ts_mov_b64 exec, -1\n.Le%=:"
                 : : "s"(ctr), "s"(c), "v"(0), "v"(1) : "memory", "scc");
}
#define XL 2
template <int NT>
DI void gemm_task(const GemmArgs& g, Sync& y, const char* ws, u32 lds, int w) {
    constexpr int FD = 5, NX = 2 * NT, OPS = NX + 6, BI = 24 - FD;
    const int lane = lanex();
    const int n = lane & 15, q = lane >> 4, qq = (lane >> 2) & 3, pp = lane & 3;
    const bool p2 = g.type == T_VD;
    const int rw = lane >> 3, sp = (lane >> 1) & 3;
    const u32 v2b = (u32)(rw * 1280 + 16 * (lane & 1) + 5120);
    auto v2o = [&](int i) { return v2b + (u32)(((sp ^ (rw >> 1)) << 5) + i * 9216); };
    u32 wv[6];
#pragma unroll
    for (int i = 0; i < 6; ++i)
        wv[i] = (u32)((rw + 8 * (i / 3)) * g.ldw + ((sp ^ (((rw >> 1) & 1) + 2 * (i / 3))) << 5) + 16 * (lane & 1) + (i % 3) * 128 + 5120 - 1024 * i);
    const char* const wp2 = g.w2 + (size_t)(48 * w) * 1280 - 2048;
    const char* wp = unip(g.w1 + (size_t)(16 * w) * g.ldw - 2048);
    int wstep = uni(64 * g.ldw);
    u32 wdst = lds + (u32)w * 6144 + 3072;
    const u32 wdlast = wdst + (GRING - 1) * GBUF;
    auto wring = [&]() { wdst = wdst == wdlast ? wdst - (GRING - 1) * GBUF : wdst + GBUF; };
    auto wload = [&](int i) {
        setm0(wdst);
        wring();
        asm volatile("s_nop 0");
        sfor<0, 6>([&](auto I) { gldsm<1024 * (I.v - 3)>(wp, wv[I.v]); });
        wp += wstep;
    };
    u32x4 vf[NT][6];
    u32x4 fr[FD + 1];
    u32x4 xr[5][NX];
    auto xtie = [&](u32x4 (&x)[NX]) {
#pragma unroll
        for (int i = 0; i < NX; i += 4) asm volatile("" : "+a"(x[i]), "+a"(x[i + 1]), "+a"(x[i + 2]), "+a"(x[i + 3]));
    };
#pragma unroll
    for (int i = 0; i < 5 * NX; ++i) asm volatile("" : "=a"(xr[i / NX][i % NX]));
    u32x2 flo;
    u32 c[4], slot = 0;
    int sd = GBUF;
    auto sring = [&]() { sd = slot == (GRING - 1) * GBUF ? -(GRING - 1) * GBUF : GBUF; slot += (u32)sd; };
    {
        u32 xo[NT];
#pragma unroll
        for (int tt = 0; tt < NT; ++tt) {
            const int tok = 16 * (tt & 1) + n;
            xo[tt] = (u32)((NT / 2 * w + (tt >> 1)) * 40960 + (g.xb ? q * 512 + tok * 16 : q * 16 + tok * 1280));
        }
        f32x4 acc[12][NT];
        const bool rm = NT == 4 && g.type == T_BL;
        const int sw = (qq >> 1) + 2 * (q & 1);
#pragma unroll
        for (int t = 0; t < 4; ++t)
            c[t] = lds + q * 3072 + qq * 128 + (rm ? ((((t & 2) + (pp >> 1)) ^ sw) << 5) + 16 * (pp & 1) + 8 * ((pp >> 1) ^ (t & 1)) : ((t ^ sw) << 5) + 8 * pp);
        auto rlo = [&](int f) { flo = trread(c[f & 3] + (f / 12) * 12288 + ((f % 12) >> 2) * 1024); };
        auto rhi = [&](int f) {
            const u32x2 hi = trread(c[f & 3] + (f / 12) * 12288 + ((f % 12) >> 2) * 1024 + 512);
            fr[f % (FD + 1)] = u32x4{flo[0], flo[1], hi[0], hi[1]};
        };
        const int xk = uni(g.xb ? 2048 : 64);
        const char *xp = g.x, *xq = xp + xk;
        sfor<0, 4>([&](auto J) {
            if constexpr (J.v < XL) { sfor<0, NX>([&](auto I) { gld16a<0>(xr[J.v][I.v], I.v & 1 ? xq : xp, xo[I.v >> 1]); }); xp += 2 * xk; xq += 2 * xk; }
            wload(J.v);
        });
        waitbar<18 + (XL - 1) * NX>();
        xtie(xr[0]);
#pragma unroll
        for (int i = 0; i < FD; ++i) { rlo(i); rhi(i); }
        char* po = g.out;
#pragma nounroll
        for (int ph = g.np1; ph > 0; --ph) {
            sfor<0, 10>([&](auto J) {
                constexpr int j = J.v;
                u32x4 (&xc)[NX] = xr[j % 5];
                u32x4 (&xn)[NX] = xr[(j + XL) % 5];
                sfor<0, 24>([&](auto I) {
                    constexpr int idx = I.v, kk = idx / 12, T = idx % 12, z = j == 0 && kk == 0, f = (idx + FD) % 24;
                    constexpr int mo = NT == 4 ? (idx < BI && idx % 4 != 2 ? idx - (idx + 1) / 4 : -1) : (idx < BI && idx % 2 == 0 ? idx / 2 : -1);
                    auto mem = [&]() {
                        if constexpr (mo >= NX) gldsm<1024 * (mo - NX - 3)>(wp, wv[mo - NX]);
                        else if constexpr (mo >= 0) gld16a<0>(xn[mo], mo & 1 ? xq : xp, xo[mo >> 1]);
                    };
                    auto misc = [&]() {
                        if constexpr (idx == 1) wring();
                        if constexpr (idx == 13) sring();
                        if constexpr (j == 1 && idx == 22) early_add(g.sig, ph + w);
                        if constexpr (idx >= 15 && idx < BI) { c[idx - 15] += (u32)sd; asm volatile("" : "+v"(c[idx - 15])); }
                        const bool more = ph > 1;
                        if constexpr (j != 5) {
                            if constexpr (idx == 20) wp += wstep;
                        } else {
                            if constexpr (idx == 19) { wp = more ? g.w1b + (size_t)(16 * w) * g.ldw - 2048 : wp2; wstep = more ? wstep : (p2 ? 128 : 0); }
                            if constexpr (idx >= 20 && idx < 23) {
#pragma unroll
                                for (int i = 2 * (idx - 20); i < 2 * (idx - 19); ++i) { wv[i] = more ? wv[i] : (p2 ? v2o(i) : 5120u); asm volatile("" : "+v"(wv[i])); }
                            }
                        }
                        if constexpr (j != 9 - XL) {
                            if constexpr (idx == 21) xp += 2 * xk;
                            if constexpr (idx == 22) xq += 2 * xk;
                        } else if constexpr (idx == 23) {
                            xp = g.x; xq = xp + xk;
#pragma unroll
                            for (int i = 0; i < NT; ++i) { xo[i] = more ? xo[i] : 0u; asm volatile("" : "+v"(xo[i])); }
                        }
                    };
                    if constexpr (idx == BI) { waitbar<6 + (XL - 1) * OPS>(); xtie(xr[(j + 1) % 5]); }
                    if constexpr (idx % 2 == 0) LGKM(6);
                    PIN();
                    const u32x4 a = fr[idx % (FD + 1)];
                    mf1<z>(acc[T][0], a, xc[kk]); PIN();
                    rlo(f);
                    if constexpr (idx == 0) setm0(wdst);
                    if constexpr (NT == 2) rhi(f);
                    PIN();
                    mf1<z>(acc[T][1], a, xc[2 + kk]); PIN();
                    if constexpr (NT == 4) {
                        rhi(f); PIN();
                        mf1<z>(acc[T][2], a, xc[4 + kk]); PIN();
                        mem(); PIN();
                        mf1<z>(acc[T][3], a, xc[6 + kk]); PIN();
                    } else { mem(); PIN(); }
                    misc();
                    PIN();
                });
            });
            asm volatile("s_nop 7\n\ts_nop 1");
            PIN();
#pragma unroll
            for (int tt = 0; tt < NT; ++tt)
#pragma unroll
                for (int ks = 0; ks < 6; ++ks)
                    vf[tt][ks] = u32x4{cvtpk(acc[2 * ks][tt][0], acc[2 * ks][tt][1]), cvtpk(acc[2 * ks][tt][2], acc[2 * ks][tt][3]),
                                       cvtpk(acc[2 * ks + 1][tt][0], acc[2 * ks + 1][tt][1]), cvtpk(acc[2 * ks + 1][tt][2], acc[2 * ks + 1][tt][3])};
            if (g.type == T_QK) {
#pragma unroll
                for (int tt = 0; tt < NT; ++tt)
#pragma unroll
                    for (int ks = 0; ks < 6; ++ks) {
                        gst16(po + (size_t)(NT / 2 * w + (tt >> 1)) * (NH * 12288) + (tt & 1) * 6144 + ks * 1024, lane * 16,
                              u32x4{vf[tt][ks][0], vf[tt][ks][2], vf[tt][ks][1], vf[tt][ks][3]});
                        if (NT == 4 && EPG) asm volatile("s_sleep %0" : : "n"(EPG));
                    }
            } else if (rm) {
                const bool sx = q >= 2;
#pragma unroll
                for (int i = 0; i < 3 * NT; ++i) {
                    const int tp = i / 6, ks = i % 6;
                    u32x4 o[2];
#pragma unroll
                    for (int j = 0; j < 4; ++j) {
                        const u32x4 &v = vf[2 * tp][ks], &u = vf[2 * tp + 1][ks];
                        if (BLS) {
                            const auto r = __builtin_amdgcn_permlane32_swap(v[j], u[j], false, false);
                            o[0][j] = r[0]; o[1][j ^ 2] = r[1];
                        } else { o[0][j] = sx ? v[j ^ 2] : v[j]; o[1][j] = sx ? u[j ^ 2] : u[j]; }
                    }
#pragma unroll
                    for (int j = 0; j < 2; ++j) {
                        gst16(po + (size_t)(2 * w + tp) * g.sV + j * (BLS ? 1024 : 256) + ks * 2048, BLS ? (u32)((q & 1) * 512 + (q >> 1) * 256 + n * 16) : (u32)(q * 512 + n * 16), o[j]);
                        if (EPG) asm volatile("s_sleep %0" : : "n"(EPG));
                    }
                }
            }
            po = g.outb;
            PIN();
        }
    }
    if (p2) {
        constexpr int HI = 2048;
        const int r = 4 * (q & 1) + qq, sw = r >> 1;
#pragma unroll
        for (int t = 0; t < 4; ++t) c[t] = lds + slot + (q >> 1) * 1024 + r * 128 + ((t ^ sw) << 5) + 8 * pp;
        sring();
        u32 so = (u32)((n >> 3) * g.sV + q * g.sQ + (n & 7) * 16);
        char* const o0 = g.out + (size_t)(NT / 2 * w) * g.sP;
        char* const o1 = o0 + g.sP;
        const int sv2 = 2 * g.sV;
        auto rlo = [&](int f) { flo = trread(c[f / 6] + (f % 6) * 4096); };
        auto rhi = [&](int f) {
            const u32x2 hi = trread(c[f / 6] + (f % 6) * 4096 + HI);
            fr[f % (FD + 1)] = u32x4{flo[0], flo[1], hi[0], hi[1]};
        };
        f32x4 a2[2][NT];
        u32 e[NT / 2][4];
        auto cv = [&](f32x4 (&a)[NT], int i) { e[i >> 2][i & 3] = cvtpk(a[i >> 1][2 * (i & 1)], a[i >> 1][2 * (i & 1) + 1]); asm volatile("" : "+v"(e[i >> 2][i & 3])); };
        auto st = [&](int pl) { gst16n(pl ? o1 : o0, so, u32x4{e[pl][0], e[pl][1], e[pl][2], e[pl][3]}); };
        PIN();
#pragma unroll
        for (int i = 0; i < FD; ++i) { rlo(i); rhi(i); }
#pragma unroll
        for (int tt = 0; tt < NT; ++tt) a2[1][tt] = f32x4{0.f, 0.f, 0.f, 0.f};
#pragma nounroll
        for (int t = 0; t < g.tn; ++t) {
            sfor<0, 24>([&](auto I) {
                constexpr int idx = I.v, cb = idx / 6, ks = idx % 6, f = (idx + FD) % 24;
                f32x4 (&ac)[NT] = a2[cb & 1];
                f32x4 (&ap)[NT] = a2[(cb & 1) ^ 1];
                constexpr int mo = cb < 2 ? (ks == 0 ? 3 * cb : (ks >= 4 ? 3 * cb + ks - 3 : -1)) : -1;
                auto mem = [&]() {
                    if constexpr (mo >= 0) gldsm<1024 * (mo - 3)>(wp, wv[mo]);
                    if constexpr (ks == 3 || (NT == 4 && ks == 2)) { if (cb > 0 || t > 0) st(NT == 4 ? ks - 2 : 0); }
                };
                auto misc = [&]() {
                    if constexpr (idx == 12) wring();
                    if constexpr (idx == 23) sring();
                    if constexpr (idx == 16) { wp += t < g.tn - 5 ? 128 : 0; }
                    if constexpr (ks == 0) { c[cb] += (u32)sd; asm volatile("" : "+v"(c[cb])); }
                    if constexpr (ks == 4) { so += (cb > 0 || t > 0) ? (u32)sv2 : 0u; asm volatile("" : "+v"(so)); }
                };
                if constexpr (idx == BI) waitbar<NT == 4 ? 38 : 28>();
                if constexpr (idx % 2 == 0) LGKM(6);
                PIN();
                const u32x4 b = fr[idx % (FD + 1)];
                mf2<ks == 0>(ac[0], vf[0][ks], b); PIN();
                rlo(f);
                if constexpr (idx == 0) setm0(wdst);
                if constexpr (NT == 2) rhi(f);
                if constexpr (NT == 4 && ks >= 1 && ks <= 3) cv(ap, ks == 1 ? 0 : (ks == 2 ? 4 : 7));
                PIN();
                mf2<ks == 0>(ac[1], vf[1][ks], b); PIN();
                if constexpr (NT == 4) {
                    rhi(f);
                    if constexpr (ks == 1 || ks == 2) cv(ap, ks == 1 ? 1 : 5);
                    PIN();
                    mf2<ks == 0>(ac[2], vf[2][ks], b); PIN();
                    mem();
                    if constexpr (ks == 1) { cv(ap, 2); cv(ap, 3); }
                    PIN();
                    mf2<ks == 0>(ac[3], vf[3][ks], b); PIN();
                    if constexpr (ks == 2) cv(ap, 6);
                } else {
                    if constexpr (ks == 1 || ks == 2) { cv(ap, 2 * ks - 2); cv(ap, 2 * ks - 1); }
                    mem(); PIN();
                }
                misc();
                PIN();
            });
            asm volatile("s_nop 7\n\ts_nop 1");
        }
        PIN();
#pragma unroll
        for (int i = 0; i < 2 * NT; ++i) cv(a2[1], i);
#pragma unroll
        for (int pl = 0; pl < NT / 2; ++pl) st(pl);
        asm volatile("s_nop 1");
    }
    if constexpr (NT == 2) asm volatile(".Lkm_g2:"); else asm volatile(".Lkm_g4:");
    sync_ld(y, ws);
    waitvm<0>();
    asm volatile("" : "+v"(y.r), "+v"(y.g), "+v"(y.e));
    sfor<0, 5>([&](auto I) { xtie(xr[I.v]); });
}

struct Ptrs {
    const char *x, *mem0, *wq, *wk, *wv, *wo, *wqkvb, *wob, *gw;
    char* out;
    char* ws;
};

enum { Q_H = 0, Q_VD = 1, Q_KQ = 2, Q_QKVB = 3, Q_BA = 4, Q_BO = 5, Q_PX = 6, Q_OUT = 7 };
DI void run_gemm(const Ptrs& p, int ty, int b, int i, int hf, bool raw, Sync& y, u32 lds, int w) {
    GemmArgs g;
    g.sV = g.sP = g.sQ = 0; g.w2 = p.wo; g.ldw = HD * 2; g.np1 = 1; g.tn = 10;
    g.sig = p.ws + OFF_ST + F_END * 128;
    const char* xf = raw ? p.x : p.ws + OFF_XF;
    g.xb = !raw;
    int s0 = 8 * b;
    if (ty == Q_VD) {
        s0 = b < 2 ? 4 * b : s0 - 8;
        g.type = T_VD; g.w1 = g.w1b = p.wv + i * DH * 2;
        g.w2 = p.wo + (size_t)i * DH * 1280;
        g.out = g.outb = p.ws + OFF_D + (size_t)s0 * D_STRIDE + i * 512;
        g.sV = 16384; g.sP = (int)D_STRIDE; g.sQ = 128;
        if (b == 0) { g.tn = 5; g.w2 += hf * 640; g.out += hf * 40 * 16384; }
    } else if (ty == Q_H) {
        s0 = 0;
        g.type = T_QK; g.w1 = g.w1b = (b ? p.wq : p.wk) + i * DH * 2;
        g.out = g.outb = p.ws + (b ? OFF_QX : OFF_KX) + (size_t)i * 12288;
        b = 0;
    } else if (ty == Q_KQ) {
        s0 = b < 2 ? 4 * b : s0 - 8;
        g.type = T_QK; g.w1 = p.wk + i * DH * 2; g.w1b = p.wq + i * DH * 2;
        g.out = p.ws + OFF_KX + (size_t)(s0 * NH + i) * 12288;
        g.outb = g.out + (OFF_QX - OFF_KX);
        g.np1 = 2;
        g.sig = p.ws + OFF_ST + (F_K + b * 32 + i) * 128;
    } else if (ty == Q_QKVB) {
        g.type = T_BL; g.w1 = p.wqkvb + i * 768; g.w1b = g.w1 + 384; g.ldw = 3840;
        g.sV = 1920 * 64; g.np1 = 2;
        g.out = p.ws + OFF_QKVB + (size_t)s0 * g.sV + i * 48 * 512; g.outb = g.out + 24 * 512;
    } else {
        const int c0 = min(i * DH, DM - DH);
        g.type = T_BL; g.w1 = g.w1b = p.wob + c0 * 2; g.ldw = 1280;
        xf = p.ws + OFF_OB;
        g.sV = 40960;
        g.out = g.outb = p.ws + OFF_BOUT + (size_t)s0 * 40960 + c0 * 64;
    }
    g.x = xf + (size_t)s0 * 40960;
    if (ty <= Q_KQ && b < 2) gemm_task<2>(g, y, p.ws, lds, w);
    else gemm_task<4>(g, y, p.ws, lds, w);
}
DI int pblk(int s) { return s < 8 ? s >> 2 : min(s >> 3, 7) + 1; }

template <int IMM>
DI void gld16c(u32x4& r, const char* sbase, u32 voff) {
    asm volatile("global_load_dwordx4 %0, %1, %2 offset:%3 sc1" : "=v"(r) : "v"(voff), "s"(sbase), "i"(IMM) : "memory");
}
#define XP1 " nt"
template <int IMM>
DI void gld16x(u32x4& r, const char* sbase, u32 voff) {
    asm volatile("global_load_dwordx4 %0, %1, %2 offset:%3 sc1" XP1 : "=v"(r) : "v"(voff), "s"(sbase), "i"(IMM) : "memory");
}
DI void gst4c(char* sbase, u32 voff, u32 v) {
    asm volatile("global_store_dword %0, %1, %2 sc1" : : "v"(voff), "v"(v), "s"(sbase) : "memory");
}
template <int N>
DI void waitv(u32x4& r) { asm volatile("s_waitcnt vmcnt(%1)" : "+v"(r) : "i"(N) : "memory"); }
DI u32 mt() { return (u32)__builtin_amdgcn_s_memtime(); }
DI u32 rt() { return (u32)__builtin_amdgcn_s_memrealtime(); }
DI void spin_to(u32 tgt) { while ((int)(mt() - tgt) < 0) {} }
DI u32 max3u(u32 a, u32 b, u32 c) { return max(max(a, b), c); }
DI u32x4 ldsr16(u32 a) { return *LDSP(u32x4, a); }
DI void clocks(u32& r, u32& m) {
    u64 a, b;
    asm volatile("s_memrealtime %0\n\ts_memtime %1\n\ts_waitcnt lgkmcnt(0)" : "=s"(a), "=s"(b) : : "memory");
    r = (u32)a; m = (u32)b;
}
#define SLATE 20
DI void edge(u32& r, u32& m) {
    u64 a, t[9];
    u32 n = 0;
#define RT(i) "s_memrealtime %" #i "\n\ts_nop 1\n\t"
#define RC(i) "\n\ts_cmp_eq_u64 %" #i ", %1\n\ts_addc_u32 %10, %10, 0"
    asm volatile("s_memtime %0\n\t" RT(1) RT(2) RT(3) RT(4) RT(5) RT(6) RT(7) RT(8) "s_memrealtime %9\n\ts_waitcnt lgkmcnt(0)" RC(2) RC(3) RC(4) RC(5) RC(6) RC(7) RC(8) RC(9)
                 : "=&s"(a), "=&s"(t[0]), "=&s"(t[1]), "=&s"(t[2]), "=&s"(t[3]), "=&s"(t[4]), "=&s"(t[5]), "=&s"(t[6]), "=&s"(t[7]), "=&s"(t[8]), "+s"(n) : : "scc", "memory");
    m = (u32)a + 12 * n;
    r = (u32)t[0] + 4;
}
#define SPB 90
struct Clock {
    u32 r0, m0, r1, m1; int cpt;
    DI void init() { edge(r0, m0); r1 = r0; m1 = m0; cpt = 24 * 64; }
    DI u32 sched(u32& phi, int ofs, int G, int mode) {
        if (mode == 0) edge(r1, m1);
        u32 r = r1, m = m1;
        if (mode == 2) clocks(r, m);
        int rem = (int)(phi + (u32)ofs - r);
        if (__builtin_expect(rem < -SLATE, 0)) { const int k = (-rem / G + 1) * G; phi += (u32)k; rem += k; }
        return m + (u32)((min(rem, 3000) * cpt) >> 6) - SPB;
    }
    DI u32 at(u32 t) { u32 r, m; clocks(r, m); return m + (u32)((min(max((int)(t - r), 0), 3000) * cpt) >> 6); }
    DI void calib() {
        const int dr = (int)(r1 - r0);
        if (dr >= 60) { int c = (int)fminf(fmaxf((float)(int)(m1 - m0) * 64.f * __builtin_amdgcn_rcpf((float)dr), 256.f), 2560.f); asm volatile("" : "+v"(c)); cpt = uni(c); r0 = r1; m0 = m1; }
    }
};
#define VISS 0
DI void follow(u32& phi, int ofs, u32 t, int G) { const int late = (int)(t - phi) - ofs; if (__builtin_expect(late > 0, 0)) phi += (u32)((late / G + 1) * G); }
DI bool unwritten(const char* base, u32 off) {
#pragma nounroll
    for (int i = 0; i < 10; ++i) {
        u32x4 t;
        gld16c<-4096>(t, base + (i + (i & 8)) * 1024, off); waitv<0>(t);
        if (__builtin_amdgcn_ballot_w64(max3u(max(t[0], t[1]), t[2], t[3]) == 0xffffffffu) != 0) return true;
    }
    return false;
}

DI size_t uslot(int sd, int s) { const int r = (s * 43) >> 8; return OFF_U + (u32)uni(s * (int)U_STRIDE + r * (int)((2u << 20) - 6 * U_STRIDE) + ((sd >> (2 + r)) & 1) * 8192); }
DI int uchunk(int v) { return (2 * (v & 3) + ((v >> 2) & 1) + (v & ~7)) * 8192; }
DI size_t mslot(int sd, int s) { return (u32)uni(s * (int)HO_STRIDE + ((sd >> (s >> 5)) & 1) * 8192 + 4096); }
struct ChainCfg { int T, cR, G, dV, fast, s1, T2, cR2, s2, T3, cR3, dU; };
constexpr int TT_OFF = 88 + 48 + 360;
DI void karg_warm() {
    u32 a[13];
    asm volatile("s_load_dword %0, %13, 0\n\t" "s_load_dword %1, %13, 64\n\t" "s_load_dword %2, %13, 128\n\t" "s_load_dword %3, %13, 192\n\t" "s_load_dword %4, %13, 256\n\t" "s_load_dword %5, %13, 320\n\t" "s_load_dword %6, %13, 384\n\t" "s_load_dword %7, %13, 448\n\t" "s_load_dword %8, %13, 512\n\t" "s_load_dword %9, %13, 576\n\t" "s_load_dword %10, %13, 640\n\t" "s_load_dword %11, %13, 704\n\t" "s_load_dword %12, %13, 748\n\t" "s_waitcnt lgkmcnt(0)"
                 : "=&s"(a[0]), "=&s"(a[1]), "=&s"(a[2]), "=&s"(a[3]), "=&s"(a[4]), "=&s"(a[5]), "=&s"(a[6]), "=&s"(a[7]), "=&s"(a[8]), "=&s"(a[9]), "=&s"(a[10]), "=&s"(a[11]), "=&s"(a[12])
                 : "s"(__builtin_amdgcn_kernarg_segment_ptr()) : "memory");
}
DI u32 sched_ld0(int s) {
    u32 r;
    asm volatile("s_load_dword %0, %1, %2 offset:%3\n\ts_waitcnt lgkmcnt(0)" : "=s"(r) : "s"(__builtin_amdgcn_kernarg_segment_ptr()), "s"(4 * s), "n"(TT_OFF) : "memory");
    return r;
}
#define HP 60
#define HQ 96

constexpr int SL_W = 0;
constexpr int SL_IMG = 131072;
constexpr int SL_KX = SL_IMG + 6656;
constexpr int SL_PF = SL_KX + 12288 + 1024;
constexpr int S_LDS = SL_PF + 256;
DI void nap(int t) { const u32 wake = rt() + (u32)t; while ((int)(rt() - wake) < 0) __builtin_amdgcn_s_sleep(4); }
DI void until(u32 t) { while ((int)(rt() - t) < 0) __builtin_amdgcn_s_sleep(1); }
DI void dly(int n) {
    asm volatile(".Ld%=:\n\ts_sub_u32 %0, %0, 1\n\ts_nop 0\n\ts_cbranch_scc0 .Ld%=" : "+s"(n) : : "scc", "memory");
}
DI void rdy_add(u32& r, char* fl) {
    asm volatile("s_mov_b64 exec, 1\n\ts_nop 3\n\tglobal_atomic_add %0, %1, %2, %3 sc0 sc1\n\ts_mov_b64 exec, -1" : "=&v"(r) : "v"(F_RDY * 128), "v"(1), "s"(fl) : "memory");
}
DI void rdy_end(u32 r, char* fl, int dt) {
    if (uni((int)r) == NS + NVB - 2 && lanex() < 8) gst4c(fl, (F_GO + lanex()) * 128, (rt() + (u32)dt) & ~1u);
}
DI u32 go_wait(const char* fl, int i) {
    u32 v;
#pragma nounroll
    for (int it = 0; it < (1 << 16); ++it) {
        flag_ld(v, fl, F_GO + (i & 7));
        asm volatile("s_waitcnt vmcnt(0)" : "+v"(v) : : "memory");
        if (uni((int)v) != -1) break;
        __builtin_amdgcn_s_sleep(GSL);
    }
    return (u32)uni((int)v);
}
DI u32 go_wg(const char* fl, int i, int w, u32 la) {
    if (w == 0) *LDSP(u32, la) = go_wait(fl, i);
    barrier();
    return (u32)uni((int)*LDSP(u32, la));
}
DI void pfl(const char* sbase, u32 voff, u32 lds) {
    asm volatile("s_mov_b32 m0, %2\n\ts_nop 3\n\tglobal_load_lds_dword %0, %1" : : "v"(voff), "s"(sbase), "s"(lds + SL_PF) : "memory");
}
DI void score_role(const Ptrs& p, const ChainCfg cfg, int h, u32 lds, int w) {
    const int lane = lanex(), tid = 64 * w + lane;
    const int n = lane & 15, g = lane >> 4, qq = (lane >> 2) & 3, pp = lane & 3;
    u32x32 wa[8], wv[3];
    const u32 wreg = lds + SL_W + (u32)w * 32768;
    const u32 lfa = wreg + lane * 16;
    const char* kxp = p.ws + OFF_KX + (size_t)h * 12288 + w * 3072;
    const u32 kxl = lds + SL_KX;
    char* const fl = p.ws + OFF_ST;
    const u32 kxw = kxl + w * 3072 - (w < 2 ? 16 : 0);
    auto kxdma = [&]() {
        asm volatile("s_mov_b32 m0, %2\n\ts_nop 3\n\tglobal_load_lds_dwordx4 %0, %1 sc1\n\tglobal_load_lds_dwordx4 %0, %1 offset:1024 sc1\n\tglobal_load_lds_dwordx4 %0, %1 offset:2048 sc1"
                     : : "v"(lanex() * 16), "s"(kxp), "s"(kxw) : "memory");
    };
    u32 kfl, phi;
    int sd;
    {
        constexpr int GS = 3104;
        Sync y;
        if (w < 3) {
#pragma unroll
            for (int i = PXT; i < NSEG; ++i) gst16(p.ws + OFF_KM + (i * NH + h) * 3072 + w * 1024, lane * 16, u32x4{~0u, ~0u, ~0u, ~0u});
#pragma unroll
            for (int i = NSEG - 4; i < NSEG; ++i) gst16(p.ws + OFF_PX + i * 81920 + (w < 2 ? h * 2048 + w * 1024 : 65536 + h * 512), w < 2 ? lane * 16 : (lane * 16) & 511, u32x4{~0u, ~0u, ~0u, ~0u});
        }
        auto dvo = [&](int i) { const int ix = i * 64 + lane; return (u32)((ix / 24) * (HD * 2) + (ix % 24) * 16); };
        const u32 tra = lds + (2 * (g >> 1) + (qq >> 1)) * GS + (4 * (g & 1) + (qq & 1)) * 384 + 8 * pp;
        u32x8 pa = u32x8{dvo(0), dvo(1), dvo(2), tra + 96 * w + 32 * (w & 1), tra + 96 * w - 64 * (w & 1), tra + 32 * w, lfa, (u32)(lane & 31) * 8};
        asm volatile("s_nop 4\n\t" "s_add_u32 s42, %21, 0x0\n\t" "s_add_u32 s42, s46, s42\n\t" "s_addc_u32 s43, s47, 0\n\t" "s_add_u32 m0, %18, 0x0\n\t" "s_nop 0\n\t" "global_load_lds_dwordx4 v224, s[42:43]\n\t"
                     "s_add_u32 m0, %18, 0x400\n\t" "s_nop 0\n\t" "global_load_lds_dwordx4 v225, s[42:43]\n\t" "s_add_u32 m0, %18, 0x800\n\t" "s_nop 0\n\t" "global_load_lds_dwordx4 v226, s[42:43]\n\t"
                     "s_add_u32 s42, %21, 0x18000\n\t" "s_add_u32 s42, s46, s42\n\t" "s_addc_u32 s43, s47, 0\n\t" "s_add_u32 m0, %18, 0xc20\n\t" "s_nop 0\n\t" "global_load_lds_dwordx4 v224, s[42:43]\n\t"
                     "s_add_u32 m0, %18, 0x1020\n\t" "s_nop 0\n\t" "global_load_lds_dwordx4 v225, s[42:43]\n\t" "s_add_u32 m0, %18, 0x1420\n\t" "s_nop 0\n\t" "global_load_lds_dwordx4 v226, s[42:43]\n\t"
                     "s_sub_u32 s42, 0xc0000, %21\n\t" "s_add_u32 s42, s46, s42\n\t" "s_addc_u32 s43, s47, 0\n\t" "s_add_u32 m0, %18, 0x6100\n\t" "s_nop 0\n\t" "global_load_lds_dwordx4 v224, s[42:43]\n\t"
                     "s_add_u32 m0, %18, 0x6500\n\t" "s_nop 0\n\t" "global_load_lds_dwordx4 v225, s[42:43]\n\t" "s_add_u32 m0, %18, 0x6900\n\t" "s_nop 0\n\t" "global_load_lds_dwordx4 v226, s[42:43]\n\t"
                     "s_sub_u32 s42, 0xd8000, %21\n\t" "s_add_u32 s42, s46, s42\n\t" "s_addc_u32 s43, s47, 0\n\t" "s_add_u32 m0, %18, 0x6d20\n\t" "s_nop 0\n\t" "global_load_lds_dwordx4 v224, s[42:43]\n\t"
                     "s_add_u32 m0, %18, 0x7120\n\t" "s_nop 0\n\t" "global_load_lds_dwordx4 v225, s[42:43]\n\t" "s_add_u32 m0, %18, 0x7520\n\t" "s_nop 0\n\t" "global_load_lds_dwordx4 v226, s[42:43]\n\t"
                     "s_add_u32 s42, %21, 0x180000\n\t" "s_add_u32 s42, s46, s42\n\t" "s_addc_u32 s43, s47, 0\n\t" "s_add_u32 m0, %18, 0xc200\n\t" "s_nop 0\n\t" "global_load_lds_dwordx4 v224, s[42:43]\n\t"
                     "s_add_u32 m0, %18, 0xc600\n\t" "s_nop 0\n\t" "global_load_lds_dwordx4 v225, s[42:43]\n\t" "s_add_u32 m0, %18, 0xca00\n\t" "s_nop 0\n\t" "global_load_lds_dwordx4 v226, s[42:43]\n\t"
                     "s_add_u32 s42, %21, 0x198000\n\t" "s_add_u32 s42, s46, s42\n\t" "s_addc_u32 s43, s47, 0\n\t" "s_add_u32 m0, %18, 0xce20\n\t" "s_nop 0\n\t" "global_load_lds_dwordx4 v224, s[42:43]\n\t"
                     "s_add_u32 m0, %18, 0xd220\n\t" "s_nop 0\n\t" "global_load_lds_dwordx4 v225, s[42:43]\n\t" "s_add_u32 m0, %18, 0xd620\n\t" "s_nop 0\n\t" "global_load_lds_dwordx4 v226, s[42:43]\n\t"
                     "s_sub_u32 s42, 0x240000, %21\n\t" "s_add_u32 s42, s46, s42\n\t" "s_addc_u32 s43, s47, 0\n\t" "s_add_u32 m0, %18, 0x12300\n\t" "s_nop 0\n\t" "global_load_lds_dwordx4 v224, s[42:43]\n\t"
                     "s_add_u32 m0, %18, 0x12700\n\t" "s_nop 0\n\t" "global_load_lds_dwordx4 v225, s[42:43]\n\t" "s_add_u32 m0, %18, 0x12b00\n\t" "s_nop 0\n\t" "global_load_lds_dwordx4 v226, s[42:43]\n\t"
                     "s_sub_u32 s42, 0x258000, %21\n\t" "s_add_u32 s42, s46, s42\n\t" "s_addc_u32 s43, s47, 0\n\t" "s_add_u32 m0, %18, 0x12f20\n\t" "s_nop 0\n\t" "global_load_lds_dwordx4 v224, s[42:43]\n\t"
                     "s_add_u32 m0, %18, 0x13320\n\t" "s_nop 0\n\t" "global_load_lds_dwordx4 v225, s[42:43]\n\t" "s_add_u32 m0, %18, 0x13720\n\t" "s_nop 0\n\t" "global_load_lds_dwordx4 v226, s[42:43]\n\t"
                     "s_waitcnt vmcnt(12) lgkmcnt(0)\n\t" "s_barrier\n\t" "s_add_u32 s42, %21, 0x300000\n\t" "s_add_u32 s42, s46, s42\n\t" "s_addc_u32 s43, s47, 0\n\t" "s_add_u32 m0, %18, 0x18400\n\t" "s_nop 0\n\t"
                     "global_load_lds_dwordx4 v224, s[42:43]\n\t" "s_add_u32 m0, %18, 0x18800\n\t" "s_nop 0\n\t" "global_load_lds_dwordx4 v225, s[42:43]\n\t" "s_add_u32 m0, %18, 0x18c00\n\t" "s_nop 0\n\t"
                     "global_load_lds_dwordx4 v226, s[42:43]\n\t" "s_add_u32 s42, %21, 0x318000\n\t" "s_add_u32 s42, s46, s42\n\t" "s_addc_u32 s43, s47, 0\n\t" "s_add_u32 m0, %18, 0x19020\n\t" "s_nop 0\n\t"
                     "global_load_lds_dwordx4 v224, s[42:43]\n\t" "s_add_u32 m0, %18, 0x19420\n\t" "s_nop 0\n\t" "global_load_lds_dwordx4 v225, s[42:43]\n\t" "s_add_u32 m0, %18, 0x19820\n\t" "s_nop 0\n\t"
                     "global_load_lds_dwordx4 v226, s[42:43]\n\t" "s_sub_u32 s42, 0x3c0000, %21\n\t" "s_add_u32 s42, s46, s42\n\t" "s_addc_u32 s43, s47, 0\n\t" "s_add_u32 m0, %18, 0x1e500\n\t" "s_nop 0\n\t"
                     "global_load_lds_dwordx4 v224, s[42:43]\n\t" "s_add_u32 m0, %18, 0x1e900\n\t" "s_nop 0\n\t" "global_load_lds_dwordx4 v225, s[42:43]\n\t" "s_add_u32 m0, %18, 0x1ed00\n\t" "s_nop 0\n\t"
                     "global_load_lds_dwordx4 v226, s[42:43]\n\t" "s_sub_u32 s42, 0x3d8000, %21\n\t" "s_add_u32 s42, s46, s42\n\t" "s_addc_u32 s43, s47, 0\n\t" "s_add_u32 m0, %18, 0x1f120\n\t" "s_nop 0\n\t"
                     "global_load_lds_dwordx4 v224, s[42:43]\n\t" "s_add_u32 m0, %18, 0x1f520\n\t" "s_nop 0\n\t" "global_load_lds_dwordx4 v225, s[42:43]\n\t" "s_add_u32 m0, %18, 0x1f920\n\t" "s_nop 0\n\t"
                     "global_load_lds_dwordx4 v226, s[42:43]\n\t" "v_add_u32 v232, 0x0, v229\n\t" "ds_read_b64_tr_b16 v[8:9], v232\n\t" "ds_read_b64_tr_b16 v[10:11], v232 offset:768\n\t"
                     "ds_read_b64_tr_b16 v[12:13], v232 offset:12416\n\t" "ds_read_b64_tr_b16 v[14:15], v232 offset:13184\n\t" "ds_read_b64_tr_b16 a[96:97], v232 offset:128\n\t"
                     "ds_read_b64_tr_b16 a[98:99], v232 offset:896\n\t" "ds_read_b64_tr_b16 a[100:101], v232 offset:12544\n\t" "ds_read_b64_tr_b16 a[102:103], v232 offset:13312\n\t"
                     "ds_read_b64_tr_b16 a[176:177], v232 offset:256\n\t" "ds_read_b64_tr_b16 a[178:179], v232 offset:1024\n\t" "ds_read_b64_tr_b16 a[180:181], v232 offset:12672\n\t"
                     "ds_read_b64_tr_b16 a[182:183], v232 offset:13440\n\t" "ds_read_b64_tr_b16 v[40:41], v232 offset:24832\n\t" "ds_read_b64_tr_b16 v[42:43], v232 offset:25600\n\t"
                     "ds_read_b64_tr_b16 v[44:45], v232 offset:37248\n\t" "ds_read_b64_tr_b16 v[46:47], v232 offset:38016\n\t" "ds_read_b64_tr_b16 a[112:113], v232 offset:24960\n\t"
                     "ds_read_b64_tr_b16 a[114:115], v232 offset:25728\n\t" "ds_read_b64_tr_b16 a[116:117], v232 offset:37376\n\t" "ds_read_b64_tr_b16 a[118:119], v232 offset:38144\n\t"
                     "ds_read_b64_tr_b16 a[192:193], v232 offset:25088\n\t" "ds_read_b64_tr_b16 a[194:195], v232 offset:25856\n\t" "ds_read_b64_tr_b16 a[196:197], v232 offset:37504\n\t"
                     "ds_read_b64_tr_b16 a[198:199], v232 offset:38272\n\t" "s_waitcnt vmcnt(12) lgkmcnt(0)\n\t" "s_barrier\n\t" "s_add_u32 s42, %21, 0x480000\n\t" "s_add_u32 s42, s46, s42\n\t" "s_addc_u32 s43, s47, 0\n\t"
                     "s_add_u32 m0, %18, 0x0\n\t" "s_nop 0\n\t" "global_load_lds_dwordx4 v224, s[42:43]\n\t" "s_add_u32 m0, %18, 0x400\n\t" "s_nop 0\n\t" "global_load_lds_dwordx4 v225, s[42:43]\n\t"
                     "s_add_u32 m0, %18, 0x800\n\t" "s_nop 0\n\t" "global_load_lds_dwordx4 v226, s[42:43]\n\t" "s_add_u32 s42, %21, 0x498000\n\t" "s_add_u32 s42, s46, s42\n\t" "s_addc_u32 s43, s47, 0\n\t"
                     "s_add_u32 m0, %18, 0xc20\n\t" "s_nop 0\n\t" "global_load_lds_dwordx4 v224, s[42:43]\n\t" "s_add_u32 m0, %18, 0x1020\n\t" "s_nop 0\n\t" "global_load_lds_dwordx4 v225, s[42:43]\n\t"
                     "s_add_u32 m0, %18, 0x1420\n\t" "s_nop 0\n\t" "global_load_lds_dwordx4 v226, s[42:43]\n\t" "s_sub_u32 s42, 0x540000, %21\n\t" "s_add_u32 s42, s46, s42\n\t" "s_addc_u32 s43, s47, 0\n\t"
                     "s_add_u32 m0, %18, 0x6100\n\t" "s_nop 0\n\t" "global_load_lds_dwordx4 v224, s[42:43]\n\t" "s_add_u32 m0, %18, 0x6500\n\t" "s_nop 0\n\t" "global_load_lds_dwordx4 v225, s[42:43]\n\t"
                     "s_add_u32 m0, %18, 0x6900\n\t" "s_nop 0\n\t" "global_load_lds_dwordx4 v226, s[42:43]\n\t" "s_sub_u32 s42, 0x558000, %21\n\t" "s_add_u32 s42, s46, s42\n\t" "s_addc_u32 s43, s47, 0\n\t"
                     "s_add_u32 m0, %18, 0x6d20\n\t" "s_nop 0\n\t" "global_load_lds_dwordx4 v224, s[42:43]\n\t" "s_add_u32 m0, %18, 0x7120\n\t" "s_nop 0\n\t" "global_load_lds_dwordx4 v225, s[42:43]\n\t"
                     "s_add_u32 m0, %18, 0x7520\n\t" "s_nop 0\n\t" "global_load_lds_dwordx4 v226, s[42:43]\n\t" "v_add_u32 v232, 0xc200, v229\n\t" "ds_read_b64_tr_b16 v[24:25], v232\n\t"
                     "ds_read_b64_tr_b16 v[26:27], v232 offset:768\n\t" "ds_read_b64_tr_b16 v[28:29], v232 offset:12416\n\t" "ds_read_b64_tr_b16 v[30:31], v232 offset:13184\n\t"
                     "ds_read_b64_tr_b16 a[104:105], v232 offset:128\n\t" "ds_read_b64_tr_b16 a[106:107], v232 offset:896\n\t" "ds_read_b64_tr_b16 a[108:109], v232 offset:12544\n\t"
                     "ds_read_b64_tr_b16 a[110:111], v232 offset:13312\n\t" "ds_read_b64_tr_b16 a[184:185], v232 offset:256\n\t" "ds_read_b64_tr_b16 a[186:187], v232 offset:1024\n\t"
                     "ds_read_b64_tr_b16 a[188:189], v232 offset:12672\n\t" "ds_read_b64_tr_b16 a[190:191], v232 offset:13440\n\t" "ds_read_b64_tr_b16 v[56:57], v232 offset:24832\n\t"
                     "ds_read_b64_tr_b16 v[58:59], v232 offset:25600\n\t" "ds_read_b64_tr_b16 v[60:61], v232 offset:37248\n\t" "ds_read_b64_tr_b16 v[62:63], v232 offset:38016\n\t"
                     "ds_read_b64_tr_b16 a[120:121], v232 offset:24960\n\t" "ds_read_b64_tr_b16 a[122:123], v232 offset:25728\n\t" "ds_read_b64_tr_b16 a[124:125], v232 offset:37376\n\t"
                     "ds_read_b64_tr_b16 a[126:127], v232 offset:38144\n\t" "ds_read_b64_tr_b16 a[200:201], v232 offset:25088\n\t" "ds_read_b64_tr_b16 a[202:203], v232 offset:25856\n\t"
                     "ds_read_b64_tr_b16 a[204:205], v232 offset:37504\n\t" "ds_read_b64_tr_b16 a[206:207], v232 offset:38272\n\t" "s_waitcnt vmcnt(12) lgkmcnt(0)\n\t" "s_barrier\n\t" "s_add_u32 s42, %21, 0x600000\n\t"
                     "s_add_u32 s42, s46, s42\n\t" "s_addc_u32 s43, s47, 0\n\t" "s_add_u32 m0, %18, 0xc200\n\t" "s_nop 0\n\t" "global_load_lds_dwordx4 v224, s[42:43]\n\t" "s_add_u32 m0, %18, 0xc600\n\t" "s_nop 0\n\t"
                     "global_load_lds_dwordx4 v225, s[42:43]\n\t" "s_add_u32 m0, %18, 0xca00\n\t" "s_nop 0\n\t" "global_load_lds_dwordx4 v226, s[42:43]\n\t" "s_add_u32 s42, %21, 0x618000\n\t" "s_add_u32 s42, s46, s42\n\t"
                     "s_addc_u32 s43, s47, 0\n\t" "s_add_u32 m0, %18, 0xce20\n\t" "s_nop 0\n\t" "global_load_lds_dwordx4 v224, s[42:43]\n\t" "s_add_u32 m0, %18, 0xd220\n\t" "s_nop 0\n\t"
                     "global_load_lds_dwordx4 v225, s[42:43]\n\t" "s_add_u32 m0, %18, 0xd620\n\t" "s_nop 0\n\t" "global_load_lds_dwordx4 v226, s[42:43]\n\t" "s_sub_u32 s42, 0x6c0000, %21\n\t" "s_add_u32 s42, s46, s42\n\t"
                     "s_addc_u32 s43, s47, 0\n\t" "s_add_u32 m0, %18, 0x12300\n\t" "s_nop 0\n\t" "global_load_lds_dwordx4 v224, s[42:43]\n\t" "s_add_u32 m0, %18, 0x12700\n\t" "s_nop 0\n\t"
                     "global_load_lds_dwordx4 v225, s[42:43]\n\t" "s_add_u32 m0, %18, 0x12b00\n\t" "s_nop 0\n\t" "global_load_lds_dwordx4 v226, s[42:43]\n\t" "s_sub_u32 s42, 0x6d8000, %21\n\t" "s_add_u32 s42, s46, s42\n\t"
                     "s_addc_u32 s43, s47, 0\n\t" "s_add_u32 m0, %18, 0x12f20\n\t" "s_nop 0\n\t" "global_load_lds_dwordx4 v224, s[42:43]\n\t" "s_add_u32 m0, %18, 0x13320\n\t" "s_nop 0\n\t"
                     "global_load_lds_dwordx4 v225, s[42:43]\n\t" "s_add_u32 m0, %18, 0x13720\n\t" "s_nop 0\n\t" "global_load_lds_dwordx4 v226, s[42:43]\n\t" "v_add_u32 v232, 0x18400, v229\n\t"
                     "ds_read_b64_tr_b16 v[72:73], v232\n\t" "ds_read_b64_tr_b16 v[74:75], v232 offset:768\n\t" "ds_read_b64_tr_b16 v[76:77], v232 offset:12416\n\t" "ds_read_b64_tr_b16 v[78:79], v232 offset:13184\n\t"
                     "ds_read_b64_tr_b16 a[128:129], v232 offset:128\n\t" "ds_read_b64_tr_b16 a[130:131], v232 offset:896\n\t" "ds_read_b64_tr_b16 a[132:133], v232 offset:12544\n\t"
                     "ds_read_b64_tr_b16 a[134:135], v232 offset:13312\n\t" "ds_read_b64_tr_b16 a[208:209], v232 offset:256\n\t" "ds_read_b64_tr_b16 a[210:211], v232 offset:1024\n\t"
                     "ds_read_b64_tr_b16 a[212:213], v232 offset:12672\n\t" "ds_read_b64_tr_b16 a[214:215], v232 offset:13440\n\t" "ds_read_b64_tr_b16 v[88:89], v232 offset:24832\n\t"
                     "ds_read_b64_tr_b16 v[90:91], v232 offset:25600\n\t" "ds_read_b64_tr_b16 v[92:93], v232 offset:37248\n\t" "ds_read_b64_tr_b16 v[94:95], v232 offset:38016\n\t"
                     "ds_read_b64_tr_b16 a[136:137], v232 offset:24960\n\t" "ds_read_b64_tr_b16 a[138:139], v232 offset:25728\n\t" "ds_read_b64_tr_b16 a[140:141], v232 offset:37376\n\t"
                     "ds_read_b64_tr_b16 a[142:143], v232 offset:38144\n\t" "ds_read_b64_tr_b16 a[216:217], v232 offset:25088\n\t" "ds_read_b64_tr_b16 a[218:219], v232 offset:25856\n\t"
                     "ds_read_b64_tr_b16 a[220:221], v232 offset:37504\n\t" "ds_read_b64_tr_b16 a[222:223], v232 offset:38272\n\t" "s_waitcnt vmcnt(12) lgkmcnt(0)\n\t" "s_barrier\n\t" "s_add_u32 s42, %21, 0x0\n\t"
                     "s_add_u32 s42, s44, s42\n\t" "s_addc_u32 s43, s45, 0\n\t" "s_add_u32 m0, %18, 0x18400\n\t" "s_nop 0\n\t" "global_load_lds_dwordx4 v224, s[42:43]\n\t" "s_add_u32 m0, %18, 0x18800\n\t" "s_nop 0\n\t"
                     "global_load_lds_dwordx4 v225, s[42:43]\n\t" "s_add_u32 m0, %18, 0x18c00\n\t" "s_nop 0\n\t" "global_load_lds_dwordx4 v226, s[42:43]\n\t" "s_add_u32 s42, %21, 0x18000\n\t" "s_add_u32 s42, s44, s42\n\t"
                     "s_addc_u32 s43, s45, 0\n\t" "s_add_u32 m0, %18, 0x19020\n\t" "s_nop 0\n\t" "global_load_lds_dwordx4 v224, s[42:43]\n\t" "s_add_u32 m0, %18, 0x19420\n\t" "s_nop 0\n\t"
                     "global_load_lds_dwordx4 v225, s[42:43]\n\t" "s_add_u32 m0, %18, 0x19820\n\t" "s_nop 0\n\t" "global_load_lds_dwordx4 v226, s[42:43]\n\t" "s_sub_u32 s42, 0xc0000, %21\n\t" "s_add_u32 s42, s44, s42\n\t"
                     "s_addc_u32 s43, s45, 0\n\t" "s_add_u32 m0, %18, 0x1e500\n\t" "s_nop 0\n\t" "global_load_lds_dwordx4 v224, s[42:43]\n\t" "s_add_u32 m0, %18, 0x1e900\n\t" "s_nop 0\n\t"
                     "global_load_lds_dwordx4 v225, s[42:43]\n\t" "s_add_u32 m0, %18, 0x1ed00\n\t" "s_nop 0\n\t" "global_load_lds_dwordx4 v226, s[42:43]\n\t" "s_sub_u32 s42, 0xd8000, %21\n\t" "s_add_u32 s42, s44, s42\n\t"
                     "s_addc_u32 s43, s45, 0\n\t" "s_add_u32 m0, %18, 0x1f120\n\t" "s_nop 0\n\t" "global_load_lds_dwordx4 v224, s[42:43]\n\t" "s_add_u32 m0, %18, 0x1f520\n\t" "s_nop 0\n\t"
                     "global_load_lds_dwordx4 v225, s[42:43]\n\t" "s_add_u32 m0, %18, 0x1f920\n\t" "s_nop 0\n\t" "global_load_lds_dwordx4 v226, s[42:43]\n\t" "v_add_u32 v232, 0x0, v229\n\t"
                     "ds_read_b64_tr_b16 v[104:105], v232\n\t" "ds_read_b64_tr_b16 v[106:107], v232 offset:768\n\t" "ds_read_b64_tr_b16 v[108:109], v232 offset:12416\n\t"
                     "ds_read_b64_tr_b16 v[110:111], v232 offset:13184\n\t" "ds_read_b64_tr_b16 a[144:145], v232 offset:128\n\t" "ds_read_b64_tr_b16 a[146:147], v232 offset:896\n\t"
                     "ds_read_b64_tr_b16 a[148:149], v232 offset:12544\n\t" "ds_read_b64_tr_b16 a[150:151], v232 offset:13312\n\t" "ds_read_b64_tr_b16 a[224:225], v232 offset:256\n\t"
                     "ds_read_b64_tr_b16 a[226:227], v232 offset:1024\n\t" "ds_read_b64_tr_b16 a[228:229], v232 offset:12672\n\t" "ds_read_b64_tr_b16 a[230:231], v232 offset:13440\n\t"
                     "ds_read_b64_tr_b16 v[120:121], v232 offset:24832\n\t" "ds_read_b64_tr_b16 v[122:123], v232 offset:25600\n\t" "ds_read_b64_tr_b16 v[124:125], v232 offset:37248\n\t"
                     "ds_read_b64_tr_b16 v[126:127], v232 offset:38016\n\t" "ds_read_b64_tr_b16 a[152:153], v232 offset:24960\n\t" "ds_read_b64_tr_b16 a[154:155], v232 offset:25728\n\t"
                     "ds_read_b64_tr_b16 a[156:157], v232 offset:37376\n\t" "ds_read_b64_tr_b16 a[158:159], v232 offset:38144\n\t" "ds_read_b64_tr_b16 a[232:233], v232 offset:25088\n\t"
                     "ds_read_b64_tr_b16 a[234:235], v232 offset:25856\n\t" "ds_read_b64_tr_b16 a[236:237], v232 offset:37504\n\t" "ds_read_b64_tr_b16 a[238:239], v232 offset:38272\n\t" "s_waitcnt vmcnt(12) lgkmcnt(0)\n\t"
                     "s_barrier\n\t" "s_add_u32 s42, %21, 0x180000\n\t" "s_add_u32 s42, s44, s42\n\t" "s_addc_u32 s43, s45, 0\n\t" "s_add_u32 m0, %18, 0x0\n\t" "s_nop 0\n\t" "global_load_lds_dwordx4 v224, s[42:43]\n\t"
                     "s_add_u32 m0, %18, 0x400\n\t" "s_nop 0\n\t" "global_load_lds_dwordx4 v225, s[42:43]\n\t" "s_add_u32 m0, %18, 0x800\n\t" "s_nop 0\n\t" "global_load_lds_dwordx4 v226, s[42:43]\n\t"
                     "s_add_u32 s42, %21, 0x198000\n\t" "s_add_u32 s42, s44, s42\n\t" "s_addc_u32 s43, s45, 0\n\t" "s_add_u32 m0, %18, 0xc20\n\t" "s_nop 0\n\t" "global_load_lds_dwordx4 v224, s[42:43]\n\t"
                     "s_add_u32 m0, %18, 0x1020\n\t" "s_nop 0\n\t" "global_load_lds_dwordx4 v225, s[42:43]\n\t" "s_add_u32 m0, %18, 0x1420\n\t" "s_nop 0\n\t" "global_load_lds_dwordx4 v226, s[42:43]\n\t"
                     "s_sub_u32 s42, 0x240000, %21\n\t" "s_add_u32 s42, s44, s42\n\t" "s_addc_u32 s43, s45, 0\n\t" "s_add_u32 m0, %18, 0x6100\n\t" "s_nop 0\n\t" "global_load_lds_dwordx4 v224, s[42:43]\n\t"
                     "s_add_u32 m0, %18, 0x6500\n\t" "s_nop 0\n\t" "global_load_lds_dwordx4 v225, s[42:43]\n\t" "s_add_u32 m0, %18, 0x6900\n\t" "s_nop 0\n\t" "global_load_lds_dwordx4 v226, s[42:43]\n\t"
                     "s_sub_u32 s42, 0x258000, %21\n\t" "s_add_u32 s42, s44, s42\n\t" "s_addc_u32 s43, s45, 0\n\t" "s_add_u32 m0, %18, 0x6d20\n\t" "s_nop 0\n\t" "global_load_lds_dwordx4 v224, s[42:43]\n\t"
                     "s_add_u32 m0, %18, 0x7120\n\t" "s_nop 0\n\t" "global_load_lds_dwordx4 v225, s[42:43]\n\t" "s_add_u32 m0, %18, 0x7520\n\t" "s_nop 0\n\t" "global_load_lds_dwordx4 v226, s[42:43]\n\t"
                     "v_add_u32 v232, 0xc200, v229\n\t" "ds_read_b64_tr_b16 a[56:57], v232\n\t" "ds_read_b64_tr_b16 a[58:59], v232 offset:768\n\t" "ds_read_b64_tr_b16 a[60:61], v232 offset:12416\n\t"
                     "ds_read_b64_tr_b16 a[62:63], v232 offset:13184\n\t" "ds_read_b64_tr_b16 a[160:161], v232 offset:128\n\t" "ds_read_b64_tr_b16 a[162:163], v232 offset:896\n\t"
                     "ds_read_b64_tr_b16 a[164:165], v232 offset:12544\n\t" "ds_read_b64_tr_b16 a[166:167], v232 offset:13312\n\t" "ds_read_b64_tr_b16 a[240:241], v232 offset:256\n\t"
                     "ds_read_b64_tr_b16 a[242:243], v232 offset:1024\n\t" "ds_read_b64_tr_b16 a[244:245], v232 offset:12672\n\t" "ds_read_b64_tr_b16 a[246:247], v232 offset:13440\n\t"
                     "ds_read_b64_tr_b16 a[88:89], v232 offset:24832\n\t" "ds_read_b64_tr_b16 a[90:91], v232 offset:25600\n\t" "ds_read_b64_tr_b16 a[92:93], v232 offset:37248\n\t"
                     "ds_read_b64_tr_b16 a[94:95], v232 offset:38016\n\t" "ds_read_b64_tr_b16 a[168:169], v232 offset:24960\n\t" "ds_read_b64_tr_b16 a[170:171], v232 offset:25728\n\t"
                     "ds_read_b64_tr_b16 a[172:173], v232 offset:37376\n\t" "ds_read_b64_tr_b16 a[174:175], v232 offset:38144\n\t" "ds_read_b64_tr_b16 a[248:249], v232 offset:25088\n\t"
                     "ds_read_b64_tr_b16 a[250:251], v232 offset:25856\n\t" "ds_read_b64_tr_b16 a[252:253], v232 offset:37504\n\t" "ds_read_b64_tr_b16 a[254:255], v232 offset:38272\n\t" "s_waitcnt vmcnt(12) lgkmcnt(0)\n\t"
                     "s_barrier\n\t" "s_add_u32 s42, %21, 0x300000\n\t" "s_add_u32 s42, s44, s42\n\t" "s_addc_u32 s43, s45, 0\n\t" "s_add_u32 m0, %18, 0xc200\n\t" "s_nop 0\n\t" "global_load_lds_dwordx4 v224, s[42:43]\n\t"
                     "s_add_u32 m0, %18, 0xc600\n\t" "s_nop 0\n\t" "global_load_lds_dwordx4 v225, s[42:43]\n\t" "s_add_u32 m0, %18, 0xca00\n\t" "s_nop 0\n\t" "global_load_lds_dwordx4 v226, s[42:43]\n\t"
                     "s_add_u32 s42, %21, 0x318000\n\t" "s_add_u32 s42, s44, s42\n\t" "s_addc_u32 s43, s45, 0\n\t" "s_add_u32 m0, %18, 0xce20\n\t" "s_nop 0\n\t" "global_load_lds_dwordx4 v224, s[42:43]\n\t"
                     "s_add_u32 m0, %18, 0xd220\n\t" "s_nop 0\n\t" "global_load_lds_dwordx4 v225, s[42:43]\n\t" "s_add_u32 m0, %18, 0xd620\n\t" "s_nop 0\n\t" "global_load_lds_dwordx4 v226, s[42:43]\n\t"
                     "s_sub_u32 s42, 0x3c0000, %21\n\t" "s_add_u32 s42, s44, s42\n\t" "s_addc_u32 s43, s45, 0\n\t" "s_add_u32 m0, %18, 0x12300\n\t" "s_nop 0\n\t" "global_load_lds_dwordx4 v224, s[42:43]\n\t"
                     "s_add_u32 m0, %18, 0x12700\n\t" "s_nop 0\n\t" "global_load_lds_dwordx4 v225, s[42:43]\n\t" "s_add_u32 m0, %18, 0x12b00\n\t" "s_nop 0\n\t" "global_load_lds_dwordx4 v226, s[42:43]\n\t"
                     "s_sub_u32 s42, 0x3d8000, %21\n\t" "s_add_u32 s42, s44, s42\n\t" "s_addc_u32 s43, s45, 0\n\t" "s_add_u32 m0, %18, 0x12f20\n\t" "s_nop 0\n\t" "global_load_lds_dwordx4 v224, s[42:43]\n\t"
                     "s_add_u32 m0, %18, 0x13320\n\t" "s_nop 0\n\t" "global_load_lds_dwordx4 v225, s[42:43]\n\t" "s_add_u32 m0, %18, 0x13720\n\t" "s_nop 0\n\t" "global_load_lds_dwordx4 v226, s[42:43]\n\t"
                     "v_add_u32 v232, 0x18400, v227\n\t" "v_add_u32 v233, 0x18400, v228\n\t" "ds_read_b64_tr_b16 v[128:129], v232\n\t" "ds_read_b64_tr_b16 v[130:131], v232 offset:768\n\t"
                     "ds_read_b64_tr_b16 v[132:133], v232 offset:12416\n\t" "ds_read_b64_tr_b16 v[134:135], v232 offset:13184\n\t" "ds_read_b64_tr_b16 v[0:1], v232 offset:32\n\t"
                     "ds_read_b64_tr_b16 v[2:3], v232 offset:800\n\t" "ds_read_b64_tr_b16 v[4:5], v232 offset:12448\n\t" "ds_read_b64_tr_b16 v[6:7], v232 offset:13216\n\t"
                     "ds_read_b64_tr_b16 v[136:137], v233 offset:64\n\t" "ds_read_b64_tr_b16 v[138:139], v233 offset:832\n\t" "ds_read_b64_tr_b16 v[140:141], v233 offset:12480\n\t"
                     "ds_read_b64_tr_b16 v[142:143], v233 offset:13248\n\t" "ds_read_b64_tr_b16 v[160:161], v232 offset:24832\n\t" "ds_read_b64_tr_b16 v[162:163], v232 offset:25600\n\t"
                     "ds_read_b64_tr_b16 v[164:165], v232 offset:37248\n\t" "ds_read_b64_tr_b16 v[166:167], v232 offset:38016\n\t" "ds_read_b64_tr_b16 v[32:33], v232 offset:24864\n\t"
                     "ds_read_b64_tr_b16 v[34:35], v232 offset:25632\n\t" "ds_read_b64_tr_b16 v[36:37], v232 offset:37280\n\t" "ds_read_b64_tr_b16 v[38:39], v232 offset:38048\n\t"
                     "ds_read_b64_tr_b16 v[168:169], v233 offset:24896\n\t" "ds_read_b64_tr_b16 v[170:171], v233 offset:25664\n\t" "ds_read_b64_tr_b16 v[172:173], v233 offset:37312\n\t"
                     "ds_read_b64_tr_b16 v[174:175], v233 offset:38080\n\t" "s_waitcnt vmcnt(12) lgkmcnt(0)\n\t" "s_barrier\n\t" "v_mov_b32 %14, 0\n\t" "global_load_dwordx2 %11, v231, %19 sc1\n\t"
                     "global_load_dwordx2 %12, %14, %19 offset:512 sc1\n\t" "global_load_dwordx2 %13, %14, %19 offset:1024 sc1\n\t" "s_add_u32 s42, %21, 0x480000\n\t" "s_add_u32 s42, s44, s42\n\t"
                     "s_addc_u32 s43, s45, 0\n\t" "s_add_u32 m0, %18, 0x18400\n\t" "s_nop 0\n\t" "global_load_lds_dwordx4 v224, s[42:43]\n\t" "s_add_u32 m0, %18, 0x18800\n\t" "s_nop 0\n\t"
                     "global_load_lds_dwordx4 v225, s[42:43]\n\t" "s_add_u32 m0, %18, 0x18c00\n\t" "s_nop 0\n\t" "global_load_lds_dwordx4 v226, s[42:43]\n\t" "s_add_u32 s42, %21, 0x498000\n\t" "s_add_u32 s42, s44, s42\n\t"
                     "s_addc_u32 s43, s45, 0\n\t" "s_add_u32 m0, %18, 0x19020\n\t" "s_nop 0\n\t" "global_load_lds_dwordx4 v224, s[42:43]\n\t" "s_add_u32 m0, %18, 0x19420\n\t" "s_nop 0\n\t"
                     "global_load_lds_dwordx4 v225, s[42:43]\n\t" "s_add_u32 m0, %18, 0x19820\n\t" "s_nop 0\n\t" "global_load_lds_dwordx4 v226, s[42:43]\n\t" "s_sub_u32 s42, 0x540000, %21\n\t" "s_add_u32 s42, s44, s42\n\t"
                     "s_addc_u32 s43, s45, 0\n\t" "s_add_u32 m0, %18, 0x1e500\n\t" "s_nop 0\n\t" "global_load_lds_dwordx4 v224, s[42:43]\n\t" "s_add_u32 m0, %18, 0x1e900\n\t" "s_nop 0\n\t"
                     "global_load_lds_dwordx4 v225, s[42:43]\n\t" "s_add_u32 m0, %18, 0x1ed00\n\t" "s_nop 0\n\t" "global_load_lds_dwordx4 v226, s[42:43]\n\t" "s_sub_u32 s42, 0x558000, %21\n\t" "s_add_u32 s42, s44, s42\n\t"
                     "s_addc_u32 s43, s45, 0\n\t" "s_add_u32 m0, %18, 0x1f120\n\t" "s_nop 0\n\t" "global_load_lds_dwordx4 v224, s[42:43]\n\t" "s_add_u32 m0, %18, 0x1f520\n\t" "s_nop 0\n\t"
                     "global_load_lds_dwordx4 v225, s[42:43]\n\t" "s_add_u32 m0, %18, 0x1f920\n\t" "s_nop 0\n\t" "global_load_lds_dwordx4 v226, s[42:43]\n\t" "v_add_u32 v232, 0x0, v227\n\t" "v_add_u32 v233, 0x0, v228\n\t"
                     "ds_read_b64_tr_b16 v[144:145], v232\n\t" "ds_read_b64_tr_b16 v[146:147], v232 offset:768\n\t" "ds_read_b64_tr_b16 v[148:149], v232 offset:12416\n\t"
                     "ds_read_b64_tr_b16 v[150:151], v232 offset:13184\n\t" "ds_read_b64_tr_b16 v[16:17], v232 offset:32\n\t" "ds_read_b64_tr_b16 v[18:19], v232 offset:800\n\t"
                     "ds_read_b64_tr_b16 v[20:21], v232 offset:12448\n\t" "ds_read_b64_tr_b16 v[22:23], v232 offset:13216\n\t" "ds_read_b64_tr_b16 v[152:153], v233 offset:64\n\t"
                     "ds_read_b64_tr_b16 v[154:155], v233 offset:832\n\t" "ds_read_b64_tr_b16 v[156:157], v233 offset:12480\n\t" "ds_read_b64_tr_b16 v[158:159], v233 offset:13248\n\t"
                     "ds_read_b64_tr_b16 v[176:177], v232 offset:24832\n\t" "ds_read_b64_tr_b16 v[178:179], v232 offset:25600\n\t" "ds_read_b64_tr_b16 v[180:181], v232 offset:37248\n\t"
                     "ds_read_b64_tr_b16 v[182:183], v232 offset:38016\n\t" "ds_read_b64_tr_b16 v[48:49], v232 offset:24864\n\t" "ds_read_b64_tr_b16 v[50:51], v232 offset:25632\n\t"
                     "ds_read_b64_tr_b16 v[52:53], v232 offset:37280\n\t" "ds_read_b64_tr_b16 v[54:55], v232 offset:38048\n\t" "ds_read_b64_tr_b16 v[184:185], v233 offset:24896\n\t"
                     "ds_read_b64_tr_b16 v[186:187], v233 offset:25664\n\t" "ds_read_b64_tr_b16 v[188:189], v233 offset:37312\n\t" "ds_read_b64_tr_b16 v[190:191], v233 offset:38080\n\t" "s_waitcnt vmcnt(12) lgkmcnt(0)\n\t"
                     "s_barrier\n\t" "s_add_u32 s42, %21, 0x600000\n\t" "s_add_u32 s42, s44, s42\n\t" "s_addc_u32 s43, s45, 0\n\t" "s_add_u32 m0, %18, 0x0\n\t" "s_nop 0\n\t" "global_load_lds_dwordx4 v224, s[42:43]\n\t"
                     "s_add_u32 m0, %18, 0x400\n\t" "s_nop 0\n\t" "global_load_lds_dwordx4 v225, s[42:43]\n\t" "s_add_u32 m0, %18, 0x800\n\t" "s_nop 0\n\t" "global_load_lds_dwordx4 v226, s[42:43]\n\t"
                     "s_add_u32 s42, %21, 0x618000\n\t" "s_add_u32 s42, s44, s42\n\t" "s_addc_u32 s43, s45, 0\n\t" "s_add_u32 m0, %18, 0xc20\n\t" "s_nop 0\n\t" "global_load_lds_dwordx4 v224, s[42:43]\n\t"
                     "s_add_u32 m0, %18, 0x1020\n\t" "s_nop 0\n\t" "global_load_lds_dwordx4 v225, s[42:43]\n\t" "s_add_u32 m0, %18, 0x1420\n\t" "s_nop 0\n\t" "global_load_lds_dwordx4 v226, s[42:43]\n\t"
                     "s_sub_u32 s42, 0x6c0000, %21\n\t" "s_add_u32 s42, s44, s42\n\t" "s_addc_u32 s43, s45, 0\n\t" "s_add_u32 m0, %18, 0x6100\n\t" "s_nop 0\n\t" "global_load_lds_dwordx4 v224, s[42:43]\n\t"
                     "s_add_u32 m0, %18, 0x6500\n\t" "s_nop 0\n\t" "global_load_lds_dwordx4 v225, s[42:43]\n\t" "s_add_u32 m0, %18, 0x6900\n\t" "s_nop 0\n\t" "global_load_lds_dwordx4 v226, s[42:43]\n\t"
                     "s_sub_u32 s42, 0x6d8000, %21\n\t" "s_add_u32 s42, s44, s42\n\t" "s_addc_u32 s43, s45, 0\n\t" "s_add_u32 m0, %18, 0x6d20\n\t" "s_nop 0\n\t" "global_load_lds_dwordx4 v224, s[42:43]\n\t"
                     "s_add_u32 m0, %18, 0x7120\n\t" "s_nop 0\n\t" "global_load_lds_dwordx4 v225, s[42:43]\n\t" "s_add_u32 m0, %18, 0x7520\n\t" "s_nop 0\n\t" "global_load_lds_dwordx4 v226, s[42:43]\n\t"
                     "global_load_dword %14, %14, %20 sc1\n\t" "v_add_u32 v232, 0xc200, v227\n\t" "v_add_u32 v233, 0xc200, v228\n\t" "ds_read_b64_tr_b16 v[192:193], v232\n\t"
                     "ds_read_b64_tr_b16 v[194:195], v232 offset:768\n\t" "ds_read_b64_tr_b16 v[196:197], v232 offset:12416\n\t" "ds_read_b64_tr_b16 v[198:199], v232 offset:13184\n\t"
                     "ds_read_b64_tr_b16 v[64:65], v232 offset:32\n\t" "ds_read_b64_tr_b16 v[66:67], v232 offset:800\n\t" "ds_read_b64_tr_b16 v[68:69], v232 offset:12448\n\t"
                     "ds_read_b64_tr_b16 v[70:71], v232 offset:13216\n\t" "ds_read_b64_tr_b16 v[200:201], v233 offset:64\n\t" "ds_read_b64_tr_b16 v[202:203], v233 offset:832\n\t"
                     "ds_read_b64_tr_b16 v[204:205], v233 offset:12480\n\t" "ds_read_b64_tr_b16 v[206:207], v233 offset:13248\n\t" "ds_read_b64_tr_b16 v[208:209], v232 offset:24832\n\t"
                     "ds_read_b64_tr_b16 v[210:211], v232 offset:25600\n\t" "ds_read_b64_tr_b16 v[212:213], v232 offset:37248\n\t" "ds_read_b64_tr_b16 v[214:215], v232 offset:38016\n\t"
                     "ds_read_b64_tr_b16 v[80:81], v232 offset:24864\n\t" "ds_read_b64_tr_b16 v[82:83], v232 offset:25632\n\t" "ds_read_b64_tr_b16 v[84:85], v232 offset:37280\n\t"
                     "ds_read_b64_tr_b16 v[86:87], v232 offset:38048\n\t" "ds_read_b64_tr_b16 v[216:217], v233 offset:24896\n\t" "ds_read_b64_tr_b16 v[218:219], v233 offset:25664\n\t"
                     "ds_read_b64_tr_b16 v[220:221], v233 offset:37312\n\t" "ds_read_b64_tr_b16 v[222:223], v233 offset:38080\n\t" "s_waitcnt vmcnt(13) lgkmcnt(0)\n\t" "s_barrier\n\t" "v_add_u32 v232, 0x18400, v227\n\t"
                     "v_add_u32 v233, 0x18400, v228\n\t" "ds_read_b64_tr_b16 a[0:1], v232\n\t" "ds_read_b64_tr_b16 a[2:3], v232 offset:768\n\t" "ds_read_b64_tr_b16 a[4:5], v232 offset:12416\n\t"
                     "ds_read_b64_tr_b16 a[6:7], v232 offset:13184\n\t" "ds_read_b64_tr_b16 v[96:97], v232 offset:32\n\t" "ds_read_b64_tr_b16 v[98:99], v232 offset:800\n\t"
                     "ds_read_b64_tr_b16 v[100:101], v232 offset:12448\n\t" "ds_read_b64_tr_b16 v[102:103], v232 offset:13216\n\t" "ds_read_b64_tr_b16 a[8:9], v233 offset:64\n\t"
                     "ds_read_b64_tr_b16 a[10:11], v233 offset:832\n\t" "ds_read_b64_tr_b16 a[12:13], v233 offset:12480\n\t" "ds_read_b64_tr_b16 a[14:15], v233 offset:13248\n\t"
                     "ds_read_b64_tr_b16 a[16:17], v232 offset:24832\n\t" "ds_read_b64_tr_b16 a[18:19], v232 offset:25600\n\t" "ds_read_b64_tr_b16 a[20:21], v232 offset:37248\n\t"
                     "ds_read_b64_tr_b16 a[22:23], v232 offset:38016\n\t" "ds_read_b64_tr_b16 v[112:113], v232 offset:24864\n\t" "ds_read_b64_tr_b16 v[114:115], v232 offset:25632\n\t"
                     "ds_read_b64_tr_b16 v[116:117], v232 offset:37280\n\t" "ds_read_b64_tr_b16 v[118:119], v232 offset:38048\n\t" "ds_read_b64_tr_b16 a[24:25], v233 offset:24896\n\t"
                     "ds_read_b64_tr_b16 a[26:27], v233 offset:25664\n\t" "ds_read_b64_tr_b16 a[28:29], v233 offset:37312\n\t" "ds_read_b64_tr_b16 a[30:31], v233 offset:38080\n\t" "s_waitcnt vmcnt(0) lgkmcnt(0)\n\t"
                     "s_barrier\n\t" "v_add_u32 v232, 0x0, v227\n\t" "v_add_u32 v233, 0x0, v228\n\t" "ds_read_b64_tr_b16 a[32:33], v232\n\t" "ds_read_b64_tr_b16 a[34:35], v232 offset:768\n\t"
                     "ds_read_b64_tr_b16 a[36:37], v232 offset:12416\n\t" "ds_read_b64_tr_b16 a[38:39], v232 offset:13184\n\t" "ds_read_b64_tr_b16 a[40:41], v232 offset:32\n\t"
                     "ds_read_b64_tr_b16 a[42:43], v232 offset:800\n\t" "ds_read_b64_tr_b16 a[44:45], v232 offset:12448\n\t" "ds_read_b64_tr_b16 a[46:47], v232 offset:13216\n\t"
                     "ds_read_b64_tr_b16 a[48:49], v233 offset:64\n\t" "ds_read_b64_tr_b16 a[50:51], v233 offset:832\n\t" "ds_read_b64_tr_b16 a[52:53], v233 offset:12480\n\t"
                     "ds_read_b64_tr_b16 a[54:55], v233 offset:13248\n\t" "ds_read_b64_tr_b16 a[64:65], v232 offset:24832\n\t" "ds_read_b64_tr_b16 a[66:67], v232 offset:25600\n\t"
                     "ds_read_b64_tr_b16 a[68:69], v232 offset:37248\n\t" "ds_read_b64_tr_b16 a[70:71], v232 offset:38016\n\t" "ds_read_b64_tr_b16 a[72:73], v232 offset:24864\n\t"
                     "ds_read_b64_tr_b16 a[74:75], v232 offset:25632\n\t" "ds_read_b64_tr_b16 a[76:77], v232 offset:37280\n\t" "ds_read_b64_tr_b16 a[78:79], v232 offset:38048\n\t"
                     "ds_read_b64_tr_b16 a[80:81], v233 offset:24896\n\t" "ds_read_b64_tr_b16 a[82:83], v233 offset:25664\n\t" "ds_read_b64_tr_b16 a[84:85], v233 offset:37312\n\t"
                     "ds_read_b64_tr_b16 a[86:87], v233 offset:38080\n\t" "s_waitcnt lgkmcnt(0)\n\t" "s_barrier\n\t" "ds_write_b128 v230, v[0:3]\n\t" "ds_write_b128 v230, v[4:7] offset:1024\n\t"
                     "ds_write_b128 v230, v[8:11] offset:2048\n\t" "ds_write_b128 v230, v[12:15] offset:3072\n\t" "ds_write_b128 v230, v[16:19] offset:4096\n\t" "ds_write_b128 v230, v[20:23] offset:5120\n\t"
                     "ds_write_b128 v230, v[24:27] offset:6144\n\t" "ds_write_b128 v230, v[28:31] offset:7168\n\t" "ds_write_b128 v230, v[32:35] offset:8192\n\t" "ds_write_b128 v230, v[36:39] offset:9216\n\t"
                     "ds_write_b128 v230, v[40:43] offset:10240\n\t" "ds_write_b128 v230, v[44:47] offset:11264\n\t" "ds_write_b128 v230, v[48:51] offset:12288\n\t" "ds_write_b128 v230, v[52:55] offset:13312\n\t"
                     "ds_write_b128 v230, v[56:59] offset:14336\n\t" "ds_write_b128 v230, v[60:63] offset:15360\n\t" "ds_write_b128 v230, v[64:67] offset:16384\n\t" "ds_write_b128 v230, v[68:71] offset:17408\n\t"
                     "ds_write_b128 v230, v[72:75] offset:18432\n\t" "ds_write_b128 v230, v[76:79] offset:19456\n\t" "ds_write_b128 v230, v[80:83] offset:20480\n\t" "ds_write_b128 v230, v[84:87] offset:21504\n\t"
                     "ds_write_b128 v230, v[88:91] offset:22528\n\t" "ds_write_b128 v230, v[92:95] offset:23552\n\t" "ds_write_b128 v230, v[96:99] offset:24576\n\t" "ds_write_b128 v230, v[100:103] offset:25600\n\t"
                     "ds_write_b128 v230, v[104:107] offset:26624\n\t" "ds_write_b128 v230, v[108:111] offset:27648\n\t" "ds_write_b128 v230, v[112:115] offset:28672\n\t" "ds_write_b128 v230, v[116:119] offset:29696\n\t"
                     "ds_write_b128 v230, v[120:123] offset:30720\n\t" "ds_write_b128 v230, v[124:127] offset:31744\n\t"
                     : "=&{a[0:31]}"(wa[0]), "=&{a[32:63]}"(wa[1]), "=&{a[64:95]}"(wa[2]), "=&{a[96:127]}"(wa[3]), "=&{a[128:159]}"(wa[4]), "=&{a[160:191]}"(wa[5]), "=&{a[192:223]}"(wa[6]), "=&{a[224:255]}"(wa[7]), "=&{v[128:159]}"(wv[0]), "=&{v[160:191]}"(wv[1]), "=&{v[192:223]}"(wv[2]), "=&v"(y.r), "=&v"(y.g), "=&v"(y.e), "=&v"(kfl)
                     : "{v[224:231]}"(pa), "{s[44:45]}"(p.wq + h * DH * 2 + (size_t)(16 * w) * (HD * 2)), "{s[46:47]}"(p.wk + h * DH * 2 + (size_t)(16 * w) * (HD * 2)),
                       "s"(lds + 2 * w * GS), "s"(p.ws + OFF_LAT), "s"(fl + (F_K + h) * 128), "s"((h & 4) * 16 * HD * 2)
                     : "v0", "v1", "v2", "v3", "v4", "v5", "v6", "v7", "v8", "v9", "v10", "v11", "v12", "v13", "v14", "v15", "v16", "v17", "v18", "v19", "v20", "v21", "v22", "v23", "v24", "v25", "v26", "v27", "v28", "v29", "v30", "v31", "v32", "v33", "v34", "v35", "v36", "v37", "v38", "v39", "v40", "v41", "v42", "v43", "v44", "v45", "v46", "v47", "v48", "v49", "v50", "v51", "v52", "v53", "v54", "v55", "v56", "v57", "v58", "v59", "v60", "v61", "v62", "v63", "v64", "v65", "v66", "v67", "v68", "v69", "v70", "v71", "v72", "v73", "v74", "v75", "v76", "v77", "v78", "v79", "v80", "v81", "v82", "v83", "v84", "v85", "v86", "v87", "v88", "v89", "v90", "v91", "v92", "v93", "v94", "v95", "v96", "v97", "v98", "v99", "v100", "v101", "v102", "v103", "v104", "v105", "v106", "v107", "v108", "v109", "v110", "v111", "v112", "v113", "v114", "v115", "v116", "v117", "v118", "v119", "v120", "v121", "v122", "v123", "v124", "v125", "v126", "v127", "v232", "v233", "s42", "s43", "scc", "memory");
        const u64 t = sync_tag(y.e);
        const bool rdy = __builtin_amdgcn_ballot_w64((y.r[0] & ~1u) != (u32)t || y.r[1] != (u32)(t >> 32) || y.g[0] != (u32)t) == 0 && uni((int)kfl) != -1;
        if (rdy) kxdma();
        sd = sync_end(y, p.ws, phi, w == 0);
        if (!rdy) { flag_wait(fl, F_K + h, 1); kxdma(); }
        if (w == 3) rdy_add(kfl, fl);
        kxp += NH * 12288;
    }
    const u32 sidx = (lane & 2) ? 0xEEEEu : 0x4444u;
    const u32 moff = lane * 16;
    u32 kxa = kxl - 16 + (n & 1) * 6160 + (16 * g + (n & 8) + 2 * (n & 2) + ((n >> 1) & 2)) * 16;
    const u64 mb0 = 0xffff0000ffff0000ull;
    const int tp = (3 * w + (w & 1)) >> 1, ts = 1 + 3 * (w >> 1);
    u32 qw0 = lds + SL_IMG + tp * 512 + lane * 8, qw1 = lds + SL_IMG + ts * 512 + lane * 8 + 4 * (w & 1);
    const u32 kw0 = lds + SL_IMG + 3072 + w * 288 + n * 16 + 4 * g;
    const u32 qr0 = lds + SL_IMG + (g >> 1) * 512 + w * 128 + 64 * (g & 1) + 8 * qq;
    const u32 kr0 = lds + SL_IMG + 3072 + (qq >> 1) * 288 + g * 64 + (qq & 1) * 16 + 8 * (pp & 1);
    const u32 xso = 32768 + (h >> 4) * 16384 + (h & 15) * 512 + (w >> 1) * 256 + (n >> 3) * 64 + (n & 4) * 32 + (w & 1) * 32 + (g & 1) * 16 + (n & 3) * 4 + (g >> 1) * 8192;
    const u32 pso = (n < 8 ? 20480 + (h >> 1) * 256 + (n & 2) * 16 + (h & 1) * 8 + (n & 4) : 18432 + (h >> 2) * 256 + (h & 1) * 8 + (h & 2) * 2) + w * 64 + (g & 1) * 16 + (g >> 1) * 8192;
    const u64 psm = 0x01aa01aa01aa01aaull;
    const u32 kmo = (u32)(((n >> 3) * 4 + g) * 128 + (n & 7) * 16);
    u32x16 ad = u32x16{moff, sidx, lfa, kxa, qw0, qw1, kw0, kr0 + 1152 * min(w, 2) + (pp >> 1) * 576, qr0, kr0, xso, pso, 0x3dd53b94u, kmo, kmo, kmo};
    asm volatile("" : "+{v[224:239]}"(ad));
    const char* memp = p.ws + mslot(sd, 0);
    char* kmp = p.ws + OFF_KM + h * 3072 + min(w, 2) * 1024;
    const float kscale = 0.07216878364870322f;
    int budget = 30000;
    Clock clk; clk.init();
    if (w == 3) { asm volatile("s_waitcnt vmcnt(0)" : "+v"(kfl) : : "memory"); rdy_end(kfl, fl, cfg.s1 >> 8 & 255); }
    u32x32 wk[3];
    asm volatile("" : "={v[64:95]}"(wk[0]), "={v[96:127]}"(wk[1]), "={v[32:63]}"(wk[2]));
    const int kfo = (F_K + 32 + h) * 128;
    for (int s = 0; s < NSEG; ++s) {
        int tries = 0;
        u64 bad, tp8, en = ~0ull;
        u32 tt;
        int kf;
        while (true) {
            const u32 tgt = s ? clk.sched(phi, 0, cfg.G, tries ? 2 : 0) : mt();
            asm volatile("s_cmp_lg_u32 %12, 0\n\t" "s_cbranch_scc1 .Lrt_s\n\t" "s_waitcnt vmcnt(0) lgkmcnt(0)\n\t" "s_barrier\n\t" "s_cmp_lg_u32 %13, 0\n\t" "s_cbranch_scc1 .Lsp_s\n\t" ".Lrt_s:\n\t"
                         "ds_read_b128 v[64:67], v226\n\t" "v_mov_b64 v[40:41], 0\n\t" "ds_read_b128 v[68:71], v226 offset:1024\n\t" "v_mov_b64 v[42:43], 0\n\t" "ds_read_b128 v[72:75], v226 offset:2048\n\t"
                         "v_mov_b64 v[44:45], 0\n\t" "ds_read_b128 v[76:79], v226 offset:3072\n\t" "v_mov_b64 v[46:47], 0\n\t" "ds_read_b128 v[80:83], v226 offset:4096\n\t" "v_mov_b64 v[48:49], 0\n\t"
                         "ds_read_b128 v[84:87], v226 offset:5120\n\t" "v_mov_b64 v[50:51], 0\n\t" "ds_read_b128 v[88:91], v226 offset:6144\n\t" "v_mov_b64 v[52:53], 0\n\t" "ds_read_b128 v[92:95], v226 offset:7168\n\t"
                         "v_mov_b64 v[54:55], 0\n\t" "ds_read_b128 v[96:99], v226 offset:8192\n\t" "v_mov_b64 v[56:57], 0\n\t" "ds_read_b128 v[100:103], v226 offset:9216\n\t" "v_mov_b64 v[58:59], 0\n\t"
                         "ds_read_b128 v[104:107], v226 offset:10240\n\t" "v_mov_b64 v[60:61], 0\n\t" "ds_read_b128 v[108:111], v226 offset:11264\n\t" "v_mov_b64 v[62:63], 0\n\t"
                         "ds_read_b128 v[112:115], v226 offset:12288\n\t" "ds_read_b128 v[116:119], v226 offset:13312\n\t" "ds_read_b128 v[120:123], v226 offset:14336\n\t" "ds_read_b128 v[124:127], v226 offset:15360\n\t"
                         ".Lsp_s:\n\t" "s_memtime vcc\n\t" "s_waitcnt lgkmcnt(0)\n\t" "s_sub_i32 s42, %4, vcc_lo\n\t" "s_cmp_gt_i32 s42, 80\n\t" "s_cbranch_scc1 .Lsp_s\n\t" "s_max_i32 s42, s42, 0\n\t"
                         "s_sub_i32 s42, 92, s42\n\t" "s_and_b32 s42, s42, -4\n\t" "s_getpc_b64 vcc\n\t" "s_add_u32 vcc_lo, vcc_lo, s42\n\t" "s_addc_u32 vcc_hi, vcc_hi, 0\n\t" "s_setpc_b64 vcc\n\t" "s_nop 0\n\t" "s_nop 0\n\t"
                         "s_nop 0\n\t" "s_nop 0\n\t" "s_nop 0\n\t" "s_nop 0\n\t" "s_nop 0\n\t" "s_nop 0\n\t" "s_nop 0\n\t" "s_nop 0\n\t" "s_nop 0\n\t" "s_nop 0\n\t" "s_nop 0\n\t" "s_nop 0\n\t" "s_nop 0\n\t" "s_nop 0\n\t"
                         "s_nop 0\n\t" "s_nop 0\n\t" "s_nop 0\n\t" "s_nop 0\n\t" "global_load_dwordx4 v[0:3], v224, %5 offset:-4096 sc1\n\t" "v_add_u32 v238, 0x4000, v224\n\t" "s_nop 9\n\t"
                         "global_load_dwordx4 v[4:7], v224, %5 offset:-2048 sc1\n\t" "s_nop 15\n\t" "global_load_dwordx4 v[8:11], v224, %6 offset:-3072 sc1\n\t" "s_nop 10\n\t"
                         "global_load_dwordx4 v[12:15], v224, %6 offset:-1024 sc1\n\t" "s_nop 10\n\t" "global_load_dwordx4 v[16:19], v224, %5 sc1\n\t" "s_nop 10\n\t" "global_load_dwordx4 v[20:23], v224, %6 offset:1024 sc1\n\t"
                         "s_nop 10\n\t" "global_load_dwordx4 v[24:27], v224, %5 offset:2048 sc1\n\t" "s_nop 10\n\t" "global_load_dwordx4 v[28:31], v224, %6 offset:3072 sc1\n\t" "s_nop 10\n\t"
                         "global_load_dwordx4 v[32:35], v238, %5 offset:-4096 sc1\n\t" "s_nop 10\n\t" "global_load_dwordx4 v[36:39], v238, %6 offset:-3072 sc1\n\t" "s_waitcnt vmcnt(9)\n\t"
                         "v_smfmac_f32_16x16x64_bf16 v[40:43], v[0:3], v[128:135], v225\n\t" "v_smfmac_f32_16x16x64_bf16 v[44:47], v[0:3], v[64:71], v225\n\t" "ds_read_b128 v[64:67], v226 offset:16384\n\t"
                         "v_smfmac_f32_16x16x64_bf16 v[48:51], v[0:3], v[136:143], v225\n\t" "ds_read_b128 v[68:71], v226 offset:17408\n\t" "v_smfmac_f32_16x16x64_bf16 v[52:55], v[0:3], v[72:79], v225\n\t"
                         "ds_read_b128 v[72:75], v226 offset:18432\n\t" "s_waitcnt vmcnt(8)\n\t" "v_smfmac_f32_16x16x64_bf16 v[40:43], v[4:7], v[144:151], v225\n\t" "ds_read_b128 v[76:79], v226 offset:19456\n\t"
                         "v_smfmac_f32_16x16x64_bf16 v[44:47], v[4:7], v[80:87], v225\n\t" "ds_read_b128 v[80:83], v226 offset:20480\n\t" "v_smfmac_f32_16x16x64_bf16 v[48:51], v[4:7], v[152:159], v225\n\t"
                         "ds_read_b128 v[84:87], v226 offset:21504\n\t" "v_smfmac_f32_16x16x64_bf16 v[52:55], v[4:7], v[88:95], v225\n\t" "ds_read_b128 v[88:91], v226 offset:22528\n\t" "s_waitcnt vmcnt(7)\n\t"
                         "v_smfmac_f32_16x16x64_bf16 v[40:43], v[8:11], v[160:167], v225\n\t" "ds_read_b128 v[92:95], v226 offset:23552\n\t" "v_max_u32 v238, v0, v4\n\t"
                         "v_smfmac_f32_16x16x64_bf16 v[44:47], v[8:11], v[96:103], v225\n\t" "ds_read_b128 v[96:99], v226 offset:24576\n\t" "v_cmp_eq_u32 vcc, -1, v238\n\t"
                         "v_smfmac_f32_16x16x64_bf16 v[48:51], v[8:11], v[168:175], v225\n\t" "ds_read_b128 v[100:103], v226 offset:25600\n\t" "v_smfmac_f32_16x16x64_bf16 v[52:55], v[8:11], v[104:111], v225\n\t"
                         "s_and_b64 vcc, vcc, %11\n\t" "s_cbranch_scc1 .Lsk_s\n\t" "ds_read_b128 v[104:107], v226 offset:26624\n\t" "s_waitcnt vmcnt(6)\n\t" "v_smfmac_f32_16x16x64_bf16 v[40:43], v[12:15], v[176:183], v225\n\t"
                         "ds_read_b128 v[108:111], v226 offset:27648\n\t" "v_smfmac_f32_16x16x64_bf16 v[44:47], v[12:15], v[112:119], v225\n\t" "ds_read_b128 v[112:115], v226 offset:28672\n\t"
                         "v_smfmac_f32_16x16x64_bf16 v[48:51], v[12:15], v[184:191], v225\n\t" "ds_read_b128 v[116:119], v226 offset:29696\n\t" "v_smfmac_f32_16x16x64_bf16 v[52:55], v[12:15], v[120:127], v225\n\t"
                         "ds_read_b128 v[120:123], v226 offset:30720\n\t" "s_waitcnt vmcnt(5)\n\t" "v_smfmac_f32_16x16x64_bf16 v[40:43], v[16:19], v[192:199], v225\n\t" "ds_read_b128 v[124:127], v226 offset:31744\n\t"
                         "s_waitcnt lgkmcnt(14)\n\t" "v_smfmac_f32_16x16x64_bf16 v[44:47], v[16:19], v[64:71], v225\n\t" "ds_read_b128 v[64:67], v227\n\t" "v_smfmac_f32_16x16x64_bf16 v[48:51], v[16:19], v[200:207], v225\n\t"
                         "ds_read_b128 v[68:71], v227 offset:1024\n\t" "s_waitcnt lgkmcnt(14)\n\t" "v_smfmac_f32_16x16x64_bf16 v[52:55], v[16:19], v[72:79], v225\n\t" "ds_read_b128 v[72:75], v227 offset:16\n\t"
                         "s_waitcnt vmcnt(4)\n\t" "v_smfmac_f32_16x16x64_bf16 v[40:43], v[20:23], v[208:215], v225\n\t" "ds_read_b128 v[76:79], v227 offset:1040\n\t" "s_waitcnt lgkmcnt(14)\n\t"
                         "v_smfmac_f32_16x16x64_bf16 v[44:47], v[20:23], v[80:87], v225\n\t" "ds_read_b128 v[80:83], v227 offset:2048\n\t" "v_smfmac_f32_16x16x64_bf16 v[48:51], v[20:23], v[216:223], v225\n\t"
                         "ds_read_b128 v[84:87], v227 offset:3072\n\t" "s_waitcnt lgkmcnt(14)\n\t" "v_smfmac_f32_16x16x64_bf16 v[52:55], v[20:23], v[88:95], v225\n\t" "ds_read_b128 v[88:91], v227 offset:2064\n\t"
                         "s_waitcnt vmcnt(3)\n\t" "v_smfmac_f32_16x16x64_bf16 v[40:43], v[24:27], a[0:7], v225\n\t" "ds_read_b128 v[92:95], v227 offset:3088\n\t" "s_waitcnt lgkmcnt(14)\n\t"
                         "v_smfmac_f32_16x16x64_bf16 v[44:47], v[24:27], v[96:103], v225\n\t" "ds_read_b128 v[96:99], v227 offset:4096\n\t" "v_smfmac_f32_16x16x64_bf16 v[48:51], v[24:27], a[8:15], v225\n\t"
                         "ds_read_b128 v[100:103], v227 offset:5120\n\t" "s_waitcnt lgkmcnt(14)\n\t" "v_smfmac_f32_16x16x64_bf16 v[52:55], v[24:27], v[104:111], v225\n\t" "ds_read_b128 v[104:107], v227 offset:4112\n\t"
                         "s_waitcnt vmcnt(2)\n\t" "v_smfmac_f32_16x16x64_bf16 v[40:43], v[28:31], a[16:23], v225\n\t" "ds_read_b128 v[108:111], v227 offset:5136\n\t" "s_waitcnt lgkmcnt(14)\n\t"
                         "v_smfmac_f32_16x16x64_bf16 v[44:47], v[28:31], v[112:119], v225\n\t" "v_smfmac_f32_16x16x64_bf16 v[48:51], v[28:31], a[24:31], v225\n\t" "s_waitcnt lgkmcnt(12)\n\t"
                         "v_smfmac_f32_16x16x64_bf16 v[52:55], v[28:31], v[120:127], v225\n\t" "s_waitcnt vmcnt(1)\n\t" "v_smfmac_f32_16x16x64_bf16 v[40:43], v[32:35], a[32:39], v225\n\t" "v_mov_b64 v[112:113], 0\n\t"
                         "v_smfmac_f32_16x16x64_bf16 v[44:47], v[32:35], a[40:47], v225\n\t" "v_mov_b64 v[114:115], 0\n\t" "v_smfmac_f32_16x16x64_bf16 v[48:51], v[32:35], a[48:55], v225\n\t" "v_mov_b64 v[116:117], 0\n\t"
                         "v_smfmac_f32_16x16x64_bf16 v[52:55], v[32:35], a[56:63], v225\n\t" "v_mov_b64 v[118:119], 0\n\t" "s_waitcnt vmcnt(0)\n\t" "v_smfmac_f32_16x16x64_bf16 v[40:43], v[36:39], a[64:71], v225\n\t"
                         "v_mov_b64 v[120:121], 0\n\t" "v_smfmac_f32_16x16x64_bf16 v[44:47], v[36:39], a[72:79], v225\n\t" "v_mov_b64 v[122:123], 0\n\t" "v_smfmac_f32_16x16x64_bf16 v[48:51], v[36:39], a[80:87], v225\n\t"
                         "v_mov_b64 v[124:125], 0\n\t" "v_smfmac_f32_16x16x64_bf16 v[52:55], v[36:39], a[88:95], v225\n\t" "v_add_f32 v40, v40, v42\n\t" "v_add_f32 v41, v41, v43\n\t"
                         "v_smfmac_f32_16x16x64_bf16 v[56:59], v[0:3], a[96:103], v225\n\t" "v_cmp_u_f32 vcc, v40, v41\n\t" "v_smfmac_f32_16x16x64_bf16 v[56:59], v[4:7], a[104:111], v225\n\t"
                         "v_cvt_pk_bf16_f32 v42, v40, v41\n\t" "v_add_f32 v44, v44, v46\n\t" "v_smfmac_f32_16x16x64_bf16 v[56:59], v[8:11], a[112:119], v225\n\t" "s_and_b64 vcc, vcc, %11\n\t" "s_cbranch_scc1 .Lsk_s\n\t"
                         "v_smfmac_f32_16x16x64_bf16 v[56:59], v[12:15], a[120:127], v225\n\t" "v_add_f32 v45, v45, v47\n\t" "v_cvt_pk_bf16_f32 v43, v44, v45\n\t"
                         "v_smfmac_f32_16x16x64_bf16 v[56:59], v[16:19], a[128:135], v225\n\t" "ds_write_b64 v228, v[42:43]\n\t" "v_add_f32 v48, v48, v50\n\t"
                         "v_smfmac_f32_16x16x64_bf16 v[56:59], v[20:23], a[136:143], v225\n\t" "v_add_f32 v49, v49, v51\n\t" "v_cvt_pk_bf16_f32 v46, v48, v49\n\t"
                         "v_smfmac_f32_16x16x64_bf16 v[56:59], v[24:27], a[144:151], v225\n\t" "ds_write_b32 v229, v46\n\t" "v_add_f32 v52, v52, v54\n\t" "v_smfmac_f32_16x16x64_bf16 v[56:59], v[28:31], a[152:159], v225\n\t"
                         "v_add_f32 v53, v53, v55\n\t" "v_cvt_pk_bf16_f32 v54, v52, v53\n\t" "v_smfmac_f32_16x16x64_bf16 v[56:59], v[32:35], a[160:167], v225\n\t" "ds_write_b32 v230, v54\n\t"
                         "v_smfmac_f32_16x16x64_bf16 v[56:59], v[36:39], a[168:175], v225\n\t" "v_smfmac_f32_16x16x64_bf16 v[60:63], v[0:3], a[176:183], v225\n\t"
                         "v_smfmac_f32_16x16x64_bf16 v[60:63], v[4:7], a[184:191], v225\n\t" "v_add_f32 v56, v56, v58\n\t" "v_smfmac_f32_16x16x64_bf16 v[60:63], v[8:11], a[192:199], v225\n\t" "s_waitcnt lgkmcnt(0)\n\t"
                         "s_barrier\n\t" "v_smfmac_f32_16x16x64_bf16 v[60:63], v[12:15], a[200:207], v225\n\t" "ds_read_b64_tr_b16 v[40:41], v232\n\t" "v_add_f32 v57, v57, v59\n\t"
                         "v_smfmac_f32_16x16x64_bf16 v[60:63], v[16:19], a[208:215], v225\n\t" "ds_read_b64_tr_b16 v[42:43], v232 offset:32\n\t" "v_cvt_pk_bf16_f32 v58, v56, v57\n\t"
                         "v_smfmac_f32_16x16x64_bf16 v[60:63], v[20:23], a[216:223], v225\n\t" "ds_write_b32 v230, v58 offset:1152\n\t" "v_smfmac_f32_16x16x64_bf16 v[60:63], v[24:27], a[224:231], v225\n\t"
                         "ds_read_b64_tr_b16 v[44:45], v232 offset:1024\n\t" "ds_read_b64_tr_b16 v[46:47], v232 offset:1056\n\t" "v_smfmac_f32_16x16x64_bf16 v[60:63], v[28:31], a[232:239], v225\n\t"
                         "ds_read_b64_tr_b16 v[48:49], v232 offset:2048\n\t" "ds_read_b64_tr_b16 v[50:51], v232 offset:2080\n\t" "v_smfmac_f32_16x16x64_bf16 v[60:63], v[32:35], a[240:247], v225\n\t"
                         "ds_read_b64_tr_b16 v[0:1], v233\n\t" "ds_read_b64_tr_b16 v[2:3], v233 offset:32\n\t" "v_smfmac_f32_16x16x64_bf16 v[60:63], v[36:39], a[248:255], v225\n\t"
                         "ds_read_b64_tr_b16 v[4:5], v233 offset:576\n\t" "ds_read_b64_tr_b16 v[6:7], v233 offset:608\n\t" "s_waitcnt lgkmcnt(9)\n\t" "v_smfmac_f32_16x16x64_bf16 v[112:115], v[40:43], v[64:71], v225\n\t"
                         "s_waitcnt lgkmcnt(8)\n\t" "s_barrier\n\t" "v_smfmac_f32_16x16x64_bf16 v[116:119], v[40:43], v[72:79], v225\n\t" "ds_read_b64_tr_b16 v[8:9], v233 offset:1152\n\t" "s_waitcnt lgkmcnt(7)\n\t"
                         "v_add_f32 v60, v60, v62\n\t" "v_add_f32 v61, v61, v63\n\t" "v_smfmac_f32_16x16x64_bf16 v[112:115], v[44:47], v[80:87], v225\n\t" "ds_read_b64_tr_b16 v[10:11], v233 offset:1184\n\t"
                         "v_cvt_pk_bf16_f32 v62, v60, v61\n\t" "v_smfmac_f32_16x16x64_bf16 v[116:119], v[44:47], v[88:95], v225\n\t" "ds_write_b32 v230, v62 offset:2304\n\t" "s_waitcnt lgkmcnt(7)\n\t"
                         "v_smfmac_f32_16x16x64_bf16 v[112:115], v[48:51], v[96:103], v225\n\t" "ds_read_b64_tr_b16 v[12:13], v233 offset:1728\n\t" "v_smfmac_f32_16x16x64_bf16 v[116:119], v[48:51], v[104:111], v225\n\t"
                         "ds_read_b64_tr_b16 v[14:15], v233 offset:1760\n\t" "s_waitcnt lgkmcnt(5)\n\t" "v_smfmac_f32_16x16x64_bf16 v[120:123], v[40:43], v[0:7], v225\n\t" "v_cndmask_b32 v24, v112, v113, %8\n\t"
                         "v_cndmask_b32 v25, v114, v115, %8\n\t" "v_add_f32 v24, v24, v25\n\t" "v_pk_add_f32 v[26:27], v[116:117], v[118:119]\n\t" "v_cndmask_b32 v25, v26, v27, %8\n\t" "v_max_f32 v26, v24, v25\n\t"
                         "s_waitcnt lgkmcnt(2)\n\t" "s_barrier\n\t" "ds_read_b64_tr_b16 v[16:17], v233 offset:2304\n\t" "v_max_f32_dpp v27, v26, v26 row_ror:4 row_mask:0xf bank_mask:0xf\n\t"
                         "ds_read_b64_tr_b16 v[18:19], v233 offset:2336\n\t" "v_max_f32_dpp v27, v26, v27 row_ror:8 row_mask:0xf bank_mask:0xf\n\t" "v_max_f32_dpp v27, v26, v27 row_ror:12 row_mask:0xf bank_mask:0xf\n\t"
                         "ds_read_b64_tr_b16 v[20:21], v233 offset:2880\n\t" "ds_read_b64_tr_b16 v[22:23], v233 offset:2912\n\t" "v_max_f32_dpp v30, v27, v27 row_ror:1 row_mask:0xf bank_mask:0xf\n\t"
                         "v_max_f32_dpp v30, v27, v30 row_ror:2 row_mask:0xf bank_mask:0xf\n\t" "v_max_f32_dpp v30, v27, v30 row_ror:3 row_mask:0xf bank_mask:0xf\n\t" "s_waitcnt lgkmcnt(4)\n\t"
                         "v_pk_add_f32 v[24:25], v[24:25], v[30:31] op_sel_hi:[1,0] neg_lo:[0,1] neg_hi:[0,1]\n\t" "v_pk_mul_f32 v[24:25], v[24:25], v[236:237] op_sel_hi:[1,0]\n\t" "v_exp_f32 v24, v24\n\t"
                         "v_exp_f32 v25, v25\n\t" "v_mul_f32 v31, %10, v30\n\t" "v_cvt_pk_bf16_f32 v28, |v24|, |v25|\n\t" "v_smfmac_f32_16x16x64_bf16 v[120:123], v[44:47], v[8:15], v225\n\t"
                         "global_store_dword v234, v28, %7 offset:-4096 sc1\n\t" "s_waitcnt lgkmcnt(0)\n\t" "v_add_f32 v26, v24, v25\n\t" "v_smfmac_f32_16x16x64_bf16 v[120:123], v[48:51], v[16:23], v225\n\t"
                         "s_nop 0\n\t" "v_add_f32_dpp v27, v26, v26 row_ror:4 row_mask:0xf bank_mask:0xf\n\t" "v_add_f32_dpp v27, v26, v27 row_ror:8 row_mask:0xf bank_mask:0xf\n\t"
                         "v_add_f32_dpp v27, v26, v27 row_ror:12 row_mask:0xf bank_mask:0xf\n\t" "s_nop 1\n\t" "v_add_f32_dpp v29, v27, v27 row_ror:1 row_mask:0xf bank_mask:0xf\n\t"
                         "v_pk_add_f32 v[24:25], v[120:121], v[122:123]\n\t" "v_cndmask_b32 v24, v24, v25, %8\n\t" "v_max_f32 v26, v24, v30\n\t" "v_add_f32_dpp v29, v27, v29 row_ror:2 row_mask:0xf bank_mask:0xf\n\t"
                         "v_add_f32_dpp v29, v27, v29 row_ror:3 row_mask:0xf bank_mask:0xf\n\t" "v_max_f32_dpp v27, v26, v26 row_ror:1 row_mask:0xf bank_mask:0xf\n\t"
                         "v_max_f32_dpp v27, v26, v27 row_ror:2 row_mask:0xf bank_mask:0xf\n\t" "v_max_f32_dpp v27, v26, v27 row_ror:3 row_mask:0xf bank_mask:0xf\n\t" "v_mul_f32 v24, %10, v24\n\t"
                         "v_mul_f32 v29, 0x3e000000, v29\n\t" "v_max_f32_dpp v26, v27, v27 row_ror:4 row_mask:0xf bank_mask:0xf\n\t" "v_fma_f32 v24, -v26, %10, v24\n\t" "v_fma_f32 v25, -v26, %10, v31\n\t"
                         "v_exp_f32 v24, v24\n\t" "v_exp_f32 v25, v25\n\t" "s_nop 0\n\t" "v_fma_f32 v26, v25, v29, v24\n\t" "v_mov_b32_dpp v124, v24 quad_perm:[0,1,2,3] row_mask:0xf bank_mask:0x3\n\t"
                         "v_mov_b32_dpp v125, v24 quad_perm:[1,0,3,2] row_mask:0xf bank_mask:0x3\n\t" "v_add_f32_dpp v27, v26, v26 row_ror:1 row_mask:0xf bank_mask:0xf\n\t"
                         "v_add_f32_dpp v27, v26, v27 row_ror:2 row_mask:0xf bank_mask:0xf\n\t" "v_add_f32_dpp v27, v26, v27 row_ror:3 row_mask:0xf bank_mask:0xf\n\t"
                         "v_mov_b32_dpp v124, v25 quad_perm:[0,1,2,3] row_mask:0x5 bank_mask:0x4\n\t" "v_mov_b32_dpp v125, v25 quad_perm:[0,1,2,3] row_mask:0xa bank_mask:0x4\n\t"
                         "v_add_f32_dpp v26, v27, v27 row_ror:4 row_mask:0xf bank_mask:0xf\n\t" "v_rcp_f32 v26, v26\n\t" "s_mov_b64 exec, %9\n\t" "v_pk_mul_f32 v[124:125], v[124:125], v[26:27] op_sel_hi:[1,0]\n\t"
                         "v_cvt_pk_bf16_f32 v24, |v124|, |v125|\n\t" "global_store_dword v235, v24, %7 offset:-4096 sc1\n\t" "s_mov_b64 exec, -1\n\t" "s_memrealtime %1\n\t" "s_load_dword %2, %14, %13 offset:%15\n\t"
                         "ds_read_b64_tr_b16 v[36:37], v231\n\t" "ds_read_b64_tr_b16 v[38:39], v231 offset:32\n\t" "v_readfirstlane_b32 %3, v237\n\t" "s_mov_b32 m0, %17\n\t" "v_mov_b64 v[40:41], 0\n\t"
                         "v_mov_b64 v[42:43], 0\n\t" "v_mov_b64 v[44:45], 0\n\t" "v_mov_b64 v[46:47], 0\n\t" "v_mov_b64 v[48:49], 0\n\t" "v_mov_b64 v[50:51], 0\n\t" "v_mov_b64 v[52:53], 0\n\t" "v_mov_b64 v[54:55], 0\n\t"
                         "v_mov_b64 v[56:57], 0\n\t" "v_mov_b64 v[58:59], 0\n\t" "v_mov_b64 v[60:61], 0\n\t" "v_mov_b64 v[62:63], 0\n\t" "v_mov_b32 v237, %18\n\t" "s_waitcnt lgkmcnt(0)\n\t" "ds_read_b128 v[64:67], v226\n\t"
                         "ds_read_b128 v[68:71], v226 offset:1024\n\t" "ds_read_b128 v[72:75], v226 offset:2048\n\t" "ds_read_b128 v[76:79], v226 offset:3072\n\t" "ds_read_b128 v[80:83], v226 offset:4096\n\t"
                         "ds_read_b128 v[84:87], v226 offset:5120\n\t" "s_cmp_eq_u32 %3, -1\n\t" "s_cbranch_scc1 .Lnk_s\n\t" "global_load_lds_dwordx4 v224, %16 sc1\n\t" "global_load_lds_dwordx4 v224, %16 offset:1024 sc1\n\t"
                         "global_load_lds_dwordx4 v224, %16 offset:2048 sc1\n\t" ".Lnk_s:\n\t" "global_store_dwordx4 v239, v[36:39], %21 sc1\n\t" "ds_read_b128 v[88:91], v226 offset:6144\n\t"
                         "ds_read_b128 v[92:95], v226 offset:7168\n\t" "ds_read_b128 v[96:99], v226 offset:8192\n\t" "ds_read_b128 v[100:103], v226 offset:9216\n\t" "ds_read_b128 v[104:107], v226 offset:10240\n\t"
                         "ds_read_b128 v[108:111], v226 offset:11264\n\t" "ds_read_b128 v[112:115], v226 offset:12288\n\t" "ds_read_b128 v[116:119], v226 offset:13312\n\t" "ds_read_b128 v[120:123], v226 offset:14336\n\t"
                         "ds_read_b128 v[124:127], v226 offset:15360\n\t" "s_and_b64 exec, %19, 1\n\t" "s_cbranch_execz .Lnf_s\n\t" "s_sleep 4\n\t" "global_load_dword v237, v237, %20 sc1\n\t" ".Lnf_s:\n\t"
                         "s_mov_b64 exec, -1\n\t" ".Lsk_s:\n\t" "s_mov_b64 %0, vcc\n\t"
                         : "=&s"(bad), "=&s"(tp8), "=&s"(tt), "=&s"(kf)
                         : "s"(uni((int)tgt)), "s"(memp + (h & 4) * 256), "s"(memp - (h & 4) * 256), "s"((u64)memp & ~8192ull), "s"(mb0), "s"(psm), "s"(kscale * 1.4426950408889634f), "s"(en), "s"(tries), "s"(4 * s),
                           "s"(__builtin_amdgcn_kernarg_segment_ptr()), "n"(TT_OFF), "s"(kxp), "s"(kxw), "s"(kfo + (s < 6 ? 0 : (s + 2) >> 3) * 4096), "s"(0x0040404040404044ull >> s), "s"(fl), "s"(kmp),
                           "{v[224:239]}"(ad), "{v[64:95]}"(wk[0]), "{v[96:127]}"(wk[1]), "{v[32:63]}"(wk[2]),
                           "{a[0:31]}"(wa[0]), "{a[32:63]}"(wa[1]), "{a[64:95]}"(wa[2]), "{a[96:127]}"(wa[3]), "{a[128:159]}"(wa[4]), "{a[160:191]}"(wa[5]), "{a[192:223]}"(wa[6]), "{a[224:255]}"(wa[7]), "{v[128:159]}"(wv[0]), "{v[160:191]}"(wv[1]), "{v[192:223]}"(wv[2])
                         : "v0", "v1", "v2", "v3", "v4", "v5", "v6", "v7", "v8", "v9", "v10", "v11", "v12", "v13", "v14", "v15", "v16", "v17", "v18", "v19", "v20", "v21", "v22", "v23", "v24", "v25", "v26", "v27", "v28", "v29", "v30", "v31", "vcc", "scc", "s42", "memory");
            if (__builtin_expect(bad == 0, 1)) break;
            waitvm<0>();
            if (((++tries & 15) == 0 && !unwritten(memp, ad[0])) || --budget < 0) en = 0;
            phi += (u32)cfg.G;
        }
        if (__builtin_expect(kf == -1, 0)) { flag_wait(fl, F_K + pblk(s + 1) * 32 + h, 1); kxdma(); }
        kxp += s < NSEG - 2 ? NH * 12288 : 0;
        clk.calib();
        if (__builtin_expect(s >= NSEG - 2, 0)) {
            waitvm<0>();
            barrier();
            if (w == 3) flag_add(fl, F_PROG + 8 * s + (h & 7), 1);
        }
        if (s == 0) { phi = go_wg(fl, h, w, lds + SL_PF - 16) - (tt >> 16); edge(clk.r1, clk.m1); clk.calib(); }
        follow(phi, (int)(tt >> 16) - (cfg.dV >> 24), (u32)tp8, cfg.G);
        phi += tt & 0xffffu;
        memp = p.ws + mslot(sd, s + 1); kmp += NH * 3072;
    }
    if (h == 0 && w == 0) {
        flag_wait(fl, F_ACK, 256);
        Sync y;
        sync_ld(y, p.ws);
        asm volatile("s_waitcnt vmcnt(0)" : "+v"(y.r), "+v"(y.g), "+v"(y.e) : : "memory");
        const u64 t = ((u64)y.e[1] << 32 | y.e[0]) + 1;
        if (lanex() == 0) gst8(p.ws + OFF_LAT, 1024, u32x2{(u32)t, (u32)(t >> 32)});
    }
    asm volatile(".Lkm_last:\n\ts_waitcnt vmcnt(0)" : : : "memory");
}

DI void proj_role(const Ptrs& p, const ChainCfg cfg, int h, u32 lds, int w) {
    u32x32 wa[8], wv[4];
    u32 phi;
    int sd;
    {
        const int lane = lanex(), tid = 64 * w + lane;
        const int g = lane >> 4, qq = (lane >> 2) & 3, pp = lane & 3;
        constexpr int GS = 3104;
        auto dvo = [&](int i) { const int ix = i * 64 + lane; return (u32)((ix / 24) * (HD * 2) + (ix % 24) * 16); };
        auto ovo = [&](int i) { const int u = i * 64 + lane, r = u / 80; return (u32)(r * 1280 + (((u % 80) ^ (2 * (r ^ (w & 2)))) << 4)); };
        const u32 tra = lds + (2 * (g >> 1) + (qq >> 1)) * GS + (4 * (g & 1) + (qq & 1)) * 384 + 96 * w + 32 * (w & 1) + 8 * pp;
        const u32 tro = (4 * g + (qq & 1) + 16 * (qq >> 1)) * 1280 + 8 * pp + ((u32)(2 * w ^ 2 * ((4 * g + qq) & 7)) << 4);
        u32x16 pa = u32x16{dvo(0), dvo(1), dvo(2), tra, tra - 96 * (w & 1), tro, lds + (u32)((w & 1) * 24576 + (w >> 1) * 99328 + lane * 16), (u32)(lane & 31) * 8,
                           ovo(0), ovo(1), ovo(2), ovo(3), ovo(4), tro, tro, tro};
        Sync y;
        asm volatile("s_nop 4\n\t" "s_add_u32 s42, %22, 0x0\n\t" "s_add_u32 s42, s44, s42\n\t" "s_addc_u32 s43, s45, 0\n\t" "s_add_u32 m0, %18, 0x0\n\t" "s_nop 0\n\t" "global_load_lds_dwordx4 v224, s[42:43]\n\t"
                     "s_add_u32 m0, %18, 0x400\n\t" "s_nop 0\n\t" "global_load_lds_dwordx4 v225, s[42:43]\n\t" "s_add_u32 m0, %18, 0x800\n\t" "s_nop 0\n\t" "global_load_lds_dwordx4 v226, s[42:43]\n\t"
                     "s_add_u32 s42, %22, 0x18000\n\t" "s_add_u32 s42, s44, s42\n\t" "s_addc_u32 s43, s45, 0\n\t" "s_add_u32 m0, %18, 0xc20\n\t" "s_nop 0\n\t" "global_load_lds_dwordx4 v224, s[42:43]\n\t"
                     "s_add_u32 m0, %18, 0x1020\n\t" "s_nop 0\n\t" "global_load_lds_dwordx4 v225, s[42:43]\n\t" "s_add_u32 m0, %18, 0x1420\n\t" "s_nop 0\n\t" "global_load_lds_dwordx4 v226, s[42:43]\n\t"
                     "s_sub_u32 s42, 0xc0000, %22\n\t" "s_add_u32 s42, s44, s42\n\t" "s_addc_u32 s43, s45, 0\n\t" "s_add_u32 m0, %18, 0x6100\n\t" "s_nop 0\n\t" "global_load_lds_dwordx4 v224, s[42:43]\n\t"
                     "s_add_u32 m0, %18, 0x6500\n\t" "s_nop 0\n\t" "global_load_lds_dwordx4 v225, s[42:43]\n\t" "s_add_u32 m0, %18, 0x6900\n\t" "s_nop 0\n\t" "global_load_lds_dwordx4 v226, s[42:43]\n\t"
                     "s_sub_u32 s42, 0xd8000, %22\n\t" "s_add_u32 s42, s44, s42\n\t" "s_addc_u32 s43, s45, 0\n\t" "s_add_u32 m0, %18, 0x6d20\n\t" "s_nop 0\n\t" "global_load_lds_dwordx4 v224, s[42:43]\n\t"
                     "s_add_u32 m0, %18, 0x7120\n\t" "s_nop 0\n\t" "global_load_lds_dwordx4 v225, s[42:43]\n\t" "s_add_u32 m0, %18, 0x7520\n\t" "s_nop 0\n\t" "global_load_lds_dwordx4 v226, s[42:43]\n\t"
                     "s_add_u32 s42, %22, 0x180000\n\t" "s_add_u32 s42, s44, s42\n\t" "s_addc_u32 s43, s45, 0\n\t" "s_add_u32 m0, %18, 0xc200\n\t" "s_nop 0\n\t" "global_load_lds_dwordx4 v224, s[42:43]\n\t"
                     "s_add_u32 m0, %18, 0xc600\n\t" "s_nop 0\n\t" "global_load_lds_dwordx4 v225, s[42:43]\n\t" "s_add_u32 m0, %18, 0xca00\n\t" "s_nop 0\n\t" "global_load_lds_dwordx4 v226, s[42:43]\n\t"
                     "s_add_u32 s42, %22, 0x198000\n\t" "s_add_u32 s42, s44, s42\n\t" "s_addc_u32 s43, s45, 0\n\t" "s_add_u32 m0, %18, 0xce20\n\t" "s_nop 0\n\t" "global_load_lds_dwordx4 v224, s[42:43]\n\t"
                     "s_add_u32 m0, %18, 0xd220\n\t" "s_nop 0\n\t" "global_load_lds_dwordx4 v225, s[42:43]\n\t" "s_add_u32 m0, %18, 0xd620\n\t" "s_nop 0\n\t" "global_load_lds_dwordx4 v226, s[42:43]\n\t"
                     "s_sub_u32 s42, 0x240000, %22\n\t" "s_add_u32 s42, s44, s42\n\t" "s_addc_u32 s43, s45, 0\n\t" "s_add_u32 m0, %18, 0x12300\n\t" "s_nop 0\n\t" "global_load_lds_dwordx4 v224, s[42:43]\n\t"
                     "s_add_u32 m0, %18, 0x12700\n\t" "s_nop 0\n\t" "global_load_lds_dwordx4 v225, s[42:43]\n\t" "s_add_u32 m0, %18, 0x12b00\n\t" "s_nop 0\n\t" "global_load_lds_dwordx4 v226, s[42:43]\n\t"
                     "s_sub_u32 s42, 0x258000, %22\n\t" "s_add_u32 s42, s44, s42\n\t" "s_addc_u32 s43, s45, 0\n\t" "s_add_u32 m0, %18, 0x12f20\n\t" "s_nop 0\n\t" "global_load_lds_dwordx4 v224, s[42:43]\n\t"
                     "s_add_u32 m0, %18, 0x13320\n\t" "s_nop 0\n\t" "global_load_lds_dwordx4 v225, s[42:43]\n\t" "s_add_u32 m0, %18, 0x13720\n\t" "s_nop 0\n\t" "global_load_lds_dwordx4 v226, s[42:43]\n\t"
                     "s_waitcnt vmcnt(12) lgkmcnt(0)\n\t" "s_barrier\n\t" "s_add_u32 s42, %22, 0x300000\n\t" "s_add_u32 s42, s44, s42\n\t" "s_addc_u32 s43, s45, 0\n\t" "s_add_u32 m0, %18, 0x18400\n\t" "s_nop 0\n\t"
                     "global_load_lds_dwordx4 v224, s[42:43]\n\t" "s_add_u32 m0, %18, 0x18800\n\t" "s_nop 0\n\t" "global_load_lds_dwordx4 v225, s[42:43]\n\t" "s_add_u32 m0, %18, 0x18c00\n\t" "s_nop 0\n\t"
                     "global_load_lds_dwordx4 v226, s[42:43]\n\t" "s_add_u32 s42, %22, 0x318000\n\t" "s_add_u32 s42, s44, s42\n\t" "s_addc_u32 s43, s45, 0\n\t" "s_add_u32 m0, %18, 0x19020\n\t" "s_nop 0\n\t"
                     "global_load_lds_dwordx4 v224, s[42:43]\n\t" "s_add_u32 m0, %18, 0x19420\n\t" "s_nop 0\n\t" "global_load_lds_dwordx4 v225, s[42:43]\n\t" "s_add_u32 m0, %18, 0x19820\n\t" "s_nop 0\n\t"
                     "global_load_lds_dwordx4 v226, s[42:43]\n\t" "s_sub_u32 s42, 0x3c0000, %22\n\t" "s_add_u32 s42, s44, s42\n\t" "s_addc_u32 s43, s45, 0\n\t" "s_add_u32 m0, %18, 0x1e500\n\t" "s_nop 0\n\t"
                     "global_load_lds_dwordx4 v224, s[42:43]\n\t" "s_add_u32 m0, %18, 0x1e900\n\t" "s_nop 0\n\t" "global_load_lds_dwordx4 v225, s[42:43]\n\t" "s_add_u32 m0, %18, 0x1ed00\n\t" "s_nop 0\n\t"
                     "global_load_lds_dwordx4 v226, s[42:43]\n\t" "s_sub_u32 s42, 0x3d8000, %22\n\t" "s_add_u32 s42, s44, s42\n\t" "s_addc_u32 s43, s45, 0\n\t" "s_add_u32 m0, %18, 0x1f120\n\t" "s_nop 0\n\t"
                     "global_load_lds_dwordx4 v224, s[42:43]\n\t" "s_add_u32 m0, %18, 0x1f520\n\t" "s_nop 0\n\t" "global_load_lds_dwordx4 v225, s[42:43]\n\t" "s_add_u32 m0, %18, 0x1f920\n\t" "s_nop 0\n\t"
                     "global_load_lds_dwordx4 v226, s[42:43]\n\t" "v_add_u32 v237, 0x0, v227\n\t" "v_add_u32 v238, 0x0, v228\n\t" "ds_read_b64_tr_b16 a[0:1], v237\n\t" "ds_read_b64_tr_b16 a[2:3], v237 offset:768\n\t"
                     "ds_read_b64_tr_b16 a[4:5], v237 offset:12416\n\t" "ds_read_b64_tr_b16 a[6:7], v237 offset:13184\n\t" "ds_read_b64_tr_b16 v[0:1], v237 offset:32\n\t" "ds_read_b64_tr_b16 v[2:3], v237 offset:800\n\t"
                     "ds_read_b64_tr_b16 v[4:5], v237 offset:12448\n\t" "ds_read_b64_tr_b16 v[6:7], v237 offset:13216\n\t" "ds_read_b64_tr_b16 a[8:9], v238 offset:64\n\t" "ds_read_b64_tr_b16 a[10:11], v238 offset:832\n\t"
                     "ds_read_b64_tr_b16 a[12:13], v238 offset:12480\n\t" "ds_read_b64_tr_b16 a[14:15], v238 offset:13248\n\t" "ds_read_b64_tr_b16 a[32:33], v237 offset:24832\n\t"
                     "ds_read_b64_tr_b16 a[34:35], v237 offset:25600\n\t" "ds_read_b64_tr_b16 a[36:37], v237 offset:37248\n\t" "ds_read_b64_tr_b16 a[38:39], v237 offset:38016\n\t"
                     "ds_read_b64_tr_b16 v[16:17], v237 offset:24864\n\t" "ds_read_b64_tr_b16 v[18:19], v237 offset:25632\n\t" "ds_read_b64_tr_b16 v[20:21], v237 offset:37280\n\t"
                     "ds_read_b64_tr_b16 v[22:23], v237 offset:38048\n\t" "ds_read_b64_tr_b16 a[40:41], v238 offset:24896\n\t" "ds_read_b64_tr_b16 a[42:43], v238 offset:25664\n\t"
                     "ds_read_b64_tr_b16 a[44:45], v238 offset:37312\n\t" "ds_read_b64_tr_b16 a[46:47], v238 offset:38080\n\t" "s_waitcnt vmcnt(12) lgkmcnt(0)\n\t" "s_barrier\n\t" "s_add_u32 s42, %22, 0x480000\n\t"
                     "s_add_u32 s42, s44, s42\n\t" "s_addc_u32 s43, s45, 0\n\t" "s_add_u32 m0, %18, 0x0\n\t" "s_nop 0\n\t" "global_load_lds_dwordx4 v224, s[42:43]\n\t" "s_add_u32 m0, %18, 0x400\n\t" "s_nop 0\n\t"
                     "global_load_lds_dwordx4 v225, s[42:43]\n\t" "s_add_u32 m0, %18, 0x800\n\t" "s_nop 0\n\t" "global_load_lds_dwordx4 v226, s[42:43]\n\t" "s_add_u32 s42, %22, 0x498000\n\t" "s_add_u32 s42, s44, s42\n\t"
                     "s_addc_u32 s43, s45, 0\n\t" "s_add_u32 m0, %18, 0xc20\n\t" "s_nop 0\n\t" "global_load_lds_dwordx4 v224, s[42:43]\n\t" "s_add_u32 m0, %18, 0x1020\n\t" "s_nop 0\n\t"
                     "global_load_lds_dwordx4 v225, s[42:43]\n\t" "s_add_u32 m0, %18, 0x1420\n\t" "s_nop 0\n\t" "global_load_lds_dwordx4 v226, s[42:43]\n\t" "s_sub_u32 s42, 0x540000, %22\n\t" "s_add_u32 s42, s44, s42\n\t"
                     "s_addc_u32 s43, s45, 0\n\t" "s_add_u32 m0, %18, 0x6100\n\t" "s_nop 0\n\t" "global_load_lds_dwordx4 v224, s[42:43]\n\t" "s_add_u32 m0, %18, 0x6500\n\t" "s_nop 0\n\t"
                     "global_load_lds_dwordx4 v225, s[42:43]\n\t" "s_add_u32 m0, %18, 0x6900\n\t" "s_nop 0\n\t" "global_load_lds_dwordx4 v226, s[42:43]\n\t" "s_sub_u32 s42, 0x558000, %22\n\t" "s_add_u32 s42, s44, s42\n\t"
                     "s_addc_u32 s43, s45, 0\n\t" "s_add_u32 m0, %18, 0x6d20\n\t" "s_nop 0\n\t" "global_load_lds_dwordx4 v224, s[42:43]\n\t" "s_add_u32 m0, %18, 0x7120\n\t" "s_nop 0\n\t"
                     "global_load_lds_dwordx4 v225, s[42:43]\n\t" "s_add_u32 m0, %18, 0x7520\n\t" "s_nop 0\n\t" "global_load_lds_dwordx4 v226, s[42:43]\n\t" "v_add_u32 v237, 0xc200, v227\n\t"
                     "v_add_u32 v238, 0xc200, v228\n\t" "ds_read_b64_tr_b16 a[16:17], v237\n\t" "ds_read_b64_tr_b16 a[18:19], v237 offset:768\n\t" "ds_read_b64_tr_b16 a[20:21], v237 offset:12416\n\t"
                     "ds_read_b64_tr_b16 a[22:23], v237 offset:13184\n\t" "ds_read_b64_tr_b16 v[8:9], v237 offset:32\n\t" "ds_read_b64_tr_b16 v[10:11], v237 offset:800\n\t"
                     "ds_read_b64_tr_b16 v[12:13], v237 offset:12448\n\t" "ds_read_b64_tr_b16 v[14:15], v237 offset:13216\n\t" "ds_read_b64_tr_b16 a[24:25], v238 offset:64\n\t"
                     "ds_read_b64_tr_b16 a[26:27], v238 offset:832\n\t" "ds_read_b64_tr_b16 a[28:29], v238 offset:12480\n\t" "ds_read_b64_tr_b16 a[30:31], v238 offset:13248\n\t"
                     "ds_read_b64_tr_b16 a[48:49], v237 offset:24832\n\t" "ds_read_b64_tr_b16 a[50:51], v237 offset:25600\n\t" "ds_read_b64_tr_b16 a[52:53], v237 offset:37248\n\t"
                     "ds_read_b64_tr_b16 a[54:55], v237 offset:38016\n\t" "ds_read_b64_tr_b16 v[24:25], v237 offset:24864\n\t" "ds_read_b64_tr_b16 v[26:27], v237 offset:25632\n\t"
                     "ds_read_b64_tr_b16 v[28:29], v237 offset:37280\n\t" "ds_read_b64_tr_b16 v[30:31], v237 offset:38048\n\t" "ds_read_b64_tr_b16 a[56:57], v238 offset:24896\n\t"
                     "ds_read_b64_tr_b16 a[58:59], v238 offset:25664\n\t" "ds_read_b64_tr_b16 a[60:61], v238 offset:37312\n\t" "ds_read_b64_tr_b16 a[62:63], v238 offset:38080\n\t" "s_waitcnt vmcnt(12) lgkmcnt(0)\n\t"
                     "s_barrier\n\t" "s_add_u32 s42, %22, 0x600000\n\t" "s_add_u32 s42, s44, s42\n\t" "s_addc_u32 s43, s45, 0\n\t" "s_add_u32 m0, %18, 0xc200\n\t" "s_nop 0\n\t" "global_load_lds_dwordx4 v224, s[42:43]\n\t"
                     "s_add_u32 m0, %18, 0xc600\n\t" "s_nop 0\n\t" "global_load_lds_dwordx4 v225, s[42:43]\n\t" "s_add_u32 m0, %18, 0xca00\n\t" "s_nop 0\n\t" "global_load_lds_dwordx4 v226, s[42:43]\n\t"
                     "s_add_u32 s42, %22, 0x618000\n\t" "s_add_u32 s42, s44, s42\n\t" "s_addc_u32 s43, s45, 0\n\t" "s_add_u32 m0, %18, 0xce20\n\t" "s_nop 0\n\t" "global_load_lds_dwordx4 v224, s[42:43]\n\t"
                     "s_add_u32 m0, %18, 0xd220\n\t" "s_nop 0\n\t" "global_load_lds_dwordx4 v225, s[42:43]\n\t" "s_add_u32 m0, %18, 0xd620\n\t" "s_nop 0\n\t" "global_load_lds_dwordx4 v226, s[42:43]\n\t"
                     "s_sub_u32 s42, 0x6c0000, %22\n\t" "s_add_u32 s42, s44, s42\n\t" "s_addc_u32 s43, s45, 0\n\t" "s_add_u32 m0, %18, 0x12300\n\t" "s_nop 0\n\t" "global_load_lds_dwordx4 v224, s[42:43]\n\t"
                     "s_add_u32 m0, %18, 0x12700\n\t" "s_nop 0\n\t" "global_load_lds_dwordx4 v225, s[42:43]\n\t" "s_add_u32 m0, %18, 0x12b00\n\t" "s_nop 0\n\t" "global_load_lds_dwordx4 v226, s[42:43]\n\t"
                     "s_sub_u32 s42, 0x6d8000, %22\n\t" "s_add_u32 s42, s44, s42\n\t" "s_addc_u32 s43, s45, 0\n\t" "s_add_u32 m0, %18, 0x12f20\n\t" "s_nop 0\n\t" "global_load_lds_dwordx4 v224, s[42:43]\n\t"
                     "s_add_u32 m0, %18, 0x13320\n\t" "s_nop 0\n\t" "global_load_lds_dwordx4 v225, s[42:43]\n\t" "s_add_u32 m0, %18, 0x13720\n\t" "s_nop 0\n\t" "global_load_lds_dwordx4 v226, s[42:43]\n\t"
                     "v_add_u32 v237, 0x18400, v227\n\t" "v_add_u32 v238, 0x18400, v228\n\t" "ds_read_b64_tr_b16 a[64:65], v237\n\t" "ds_read_b64_tr_b16 a[66:67], v237 offset:768\n\t"
                     "ds_read_b64_tr_b16 a[68:69], v237 offset:12416\n\t" "ds_read_b64_tr_b16 a[70:71], v237 offset:13184\n\t" "ds_read_b64_tr_b16 v[32:33], v237 offset:32\n\t"
                     "ds_read_b64_tr_b16 v[34:35], v237 offset:800\n\t" "ds_read_b64_tr_b16 v[36:37], v237 offset:12448\n\t" "ds_read_b64_tr_b16 v[38:39], v237 offset:13216\n\t"
                     "ds_read_b64_tr_b16 a[72:73], v238 offset:64\n\t" "ds_read_b64_tr_b16 a[74:75], v238 offset:832\n\t" "ds_read_b64_tr_b16 a[76:77], v238 offset:12480\n\t"
                     "ds_read_b64_tr_b16 a[78:79], v238 offset:13248\n\t" "ds_read_b64_tr_b16 a[80:81], v237 offset:24832\n\t" "ds_read_b64_tr_b16 a[82:83], v237 offset:25600\n\t"
                     "ds_read_b64_tr_b16 a[84:85], v237 offset:37248\n\t" "ds_read_b64_tr_b16 a[86:87], v237 offset:38016\n\t" "ds_read_b64_tr_b16 v[40:41], v237 offset:24864\n\t"
                     "ds_read_b64_tr_b16 v[42:43], v237 offset:25632\n\t" "ds_read_b64_tr_b16 v[44:45], v237 offset:37280\n\t" "ds_read_b64_tr_b16 v[46:47], v237 offset:38048\n\t"
                     "ds_read_b64_tr_b16 a[88:89], v238 offset:24896\n\t" "ds_read_b64_tr_b16 a[90:91], v238 offset:25664\n\t" "ds_read_b64_tr_b16 a[92:93], v238 offset:37312\n\t"
                     "ds_read_b64_tr_b16 a[94:95], v238 offset:38080\n\t" "s_waitcnt vmcnt(12) lgkmcnt(0)\n\t" "s_barrier\n\t" "s_add_u32 s42, s46, 0x0\n\t" "s_addc_u32 s43, s47, 0\n\t" "s_add_u32 m0, %20, 0x18400\n\t"
                     "s_nop 0\n\t" "global_load_lds_dwordx4 v232, s[42:43]\n\t" "s_add_u32 m0, %20, 0x18800\n\t" "s_nop 0\n\t" "global_load_lds_dwordx4 v233, s[42:43]\n\t" "s_add_u32 m0, %20, 0x18c00\n\t" "s_nop 0\n\t"
                     "global_load_lds_dwordx4 v234, s[42:43]\n\t" "s_add_u32 m0, %20, 0x19000\n\t" "s_nop 0\n\t" "global_load_lds_dwordx4 v235, s[42:43]\n\t" "s_add_u32 m0, %20, 0x19400\n\t" "s_nop 0\n\t"
                     "global_load_lds_dwordx4 v236, s[42:43]\n\t" "s_add_u32 s42, s46, 0x1400\n\t" "s_addc_u32 s43, s47, 0\n\t" "s_add_u32 m0, %20, 0x19800\n\t" "v_xor_b32 v224, 0x80, v232\n\t"
                     "global_load_lds_dwordx4 v224, s[42:43]\n\t" "s_add_u32 m0, %20, 0x19c00\n\t" "v_xor_b32 v224, 0x80, v233\n\t" "global_load_lds_dwordx4 v224, s[42:43]\n\t" "s_add_u32 m0, %20, 0x1a000\n\t"
                     "v_xor_b32 v224, 0x80, v234\n\t" "global_load_lds_dwordx4 v224, s[42:43]\n\t" "s_add_u32 m0, %20, 0x1a400\n\t" "v_xor_b32 v224, 0x80, v235\n\t" "global_load_lds_dwordx4 v224, s[42:43]\n\t"
                     "s_add_u32 m0, %20, 0x1a800\n\t" "v_xor_b32 v224, 0x80, v236\n\t" "global_load_lds_dwordx4 v224, s[42:43]\n\t" "v_add_u32 v237, 0x0, v227\n\t" "v_add_u32 v238, 0x0, v228\n\t"
                     "ds_read_b64_tr_b16 a[96:97], v237\n\t" "ds_read_b64_tr_b16 a[98:99], v237 offset:768\n\t" "ds_read_b64_tr_b16 a[100:101], v237 offset:12416\n\t" "ds_read_b64_tr_b16 a[102:103], v237 offset:13184\n\t"
                     "ds_read_b64_tr_b16 v[48:49], v237 offset:32\n\t" "ds_read_b64_tr_b16 v[50:51], v237 offset:800\n\t" "ds_read_b64_tr_b16 v[52:53], v237 offset:12448\n\t"
                     "ds_read_b64_tr_b16 v[54:55], v237 offset:13216\n\t" "ds_read_b64_tr_b16 a[104:105], v238 offset:64\n\t" "ds_read_b64_tr_b16 a[106:107], v238 offset:832\n\t"
                     "ds_read_b64_tr_b16 a[108:109], v238 offset:12480\n\t" "ds_read_b64_tr_b16 a[110:111], v238 offset:13248\n\t" "ds_read_b64_tr_b16 a[112:113], v237 offset:24832\n\t"
                     "ds_read_b64_tr_b16 a[114:115], v237 offset:25600\n\t" "ds_read_b64_tr_b16 a[116:117], v237 offset:37248\n\t" "ds_read_b64_tr_b16 a[118:119], v237 offset:38016\n\t"
                     "ds_read_b64_tr_b16 v[56:57], v237 offset:24864\n\t" "ds_read_b64_tr_b16 v[58:59], v237 offset:25632\n\t" "ds_read_b64_tr_b16 v[60:61], v237 offset:37280\n\t"
                     "ds_read_b64_tr_b16 v[62:63], v237 offset:38048\n\t" "ds_read_b64_tr_b16 a[120:121], v238 offset:24896\n\t" "ds_read_b64_tr_b16 a[122:123], v238 offset:25664\n\t"
                     "ds_read_b64_tr_b16 a[124:125], v238 offset:37312\n\t" "ds_read_b64_tr_b16 a[126:127], v238 offset:38080\n\t" "s_waitcnt vmcnt(10) lgkmcnt(0)\n\t" "s_barrier\n\t" "s_add_u32 s42, s46, 0xa000\n\t"
                     "s_addc_u32 s43, s47, 0\n\t" "s_add_u32 m0, %20, 0x0\n\t" "s_nop 0\n\t" "global_load_lds_dwordx4 v232, s[42:43]\n\t" "s_add_u32 m0, %20, 0x400\n\t" "s_nop 0\n\t"
                     "global_load_lds_dwordx4 v233, s[42:43]\n\t" "s_add_u32 m0, %20, 0x800\n\t" "s_nop 0\n\t" "global_load_lds_dwordx4 v234, s[42:43]\n\t" "s_add_u32 m0, %20, 0xc00\n\t" "s_nop 0\n\t"
                     "global_load_lds_dwordx4 v235, s[42:43]\n\t" "s_add_u32 m0, %20, 0x1000\n\t" "s_nop 0\n\t" "global_load_lds_dwordx4 v236, s[42:43]\n\t" "s_add_u32 s42, s46, 0xb400\n\t" "s_addc_u32 s43, s47, 0\n\t"
                     "s_add_u32 m0, %20, 0x1400\n\t" "v_xor_b32 v224, 0x80, v232\n\t" "global_load_lds_dwordx4 v224, s[42:43]\n\t" "s_add_u32 m0, %20, 0x1800\n\t" "v_xor_b32 v224, 0x80, v233\n\t"
                     "global_load_lds_dwordx4 v224, s[42:43]\n\t" "s_add_u32 m0, %20, 0x1c00\n\t" "v_xor_b32 v224, 0x80, v234\n\t" "global_load_lds_dwordx4 v224, s[42:43]\n\t" "s_add_u32 m0, %20, 0x2000\n\t"
                     "v_xor_b32 v224, 0x80, v235\n\t" "global_load_lds_dwordx4 v224, s[42:43]\n\t" "s_add_u32 m0, %20, 0x2400\n\t" "v_xor_b32 v224, 0x80, v236\n\t" "global_load_lds_dwordx4 v224, s[42:43]\n\t"
                     "v_add_u32 v237, 0xc200, v227\n\t" "v_add_u32 v238, 0xc200, v228\n\t" "ds_read_b64_tr_b16 a[128:129], v237\n\t" "ds_read_b64_tr_b16 a[130:131], v237 offset:768\n\t"
                     "ds_read_b64_tr_b16 a[132:133], v237 offset:12416\n\t" "ds_read_b64_tr_b16 a[134:135], v237 offset:13184\n\t" "ds_read_b64_tr_b16 a[136:137], v237 offset:32\n\t"
                     "ds_read_b64_tr_b16 a[138:139], v237 offset:800\n\t" "ds_read_b64_tr_b16 a[140:141], v237 offset:12448\n\t" "ds_read_b64_tr_b16 a[142:143], v237 offset:13216\n\t"
                     "ds_read_b64_tr_b16 a[144:145], v238 offset:64\n\t" "ds_read_b64_tr_b16 a[146:147], v238 offset:832\n\t" "ds_read_b64_tr_b16 a[148:149], v238 offset:12480\n\t"
                     "ds_read_b64_tr_b16 a[150:151], v238 offset:13248\n\t" "ds_read_b64_tr_b16 a[152:153], v237 offset:24832\n\t" "ds_read_b64_tr_b16 a[154:155], v237 offset:25600\n\t"
                     "ds_read_b64_tr_b16 a[156:157], v237 offset:37248\n\t" "ds_read_b64_tr_b16 a[158:159], v237 offset:38016\n\t" "ds_read_b64_tr_b16 a[160:161], v237 offset:24864\n\t"
                     "ds_read_b64_tr_b16 a[162:163], v237 offset:25632\n\t" "ds_read_b64_tr_b16 a[164:165], v237 offset:37280\n\t" "ds_read_b64_tr_b16 a[166:167], v237 offset:38048\n\t"
                     "ds_read_b64_tr_b16 a[168:169], v238 offset:24896\n\t" "ds_read_b64_tr_b16 a[170:171], v238 offset:25664\n\t" "ds_read_b64_tr_b16 a[172:173], v238 offset:37312\n\t"
                     "ds_read_b64_tr_b16 a[174:175], v238 offset:38080\n\t" "s_waitcnt vmcnt(10) lgkmcnt(0)\n\t" "s_barrier\n\t" "s_add_u32 s42, s46, 0x14000\n\t" "s_addc_u32 s43, s47, 0\n\t"
                     "s_add_u32 m0, %20, 0xc200\n\t" "s_nop 0\n\t" "global_load_lds_dwordx4 v232, s[42:43]\n\t" "s_add_u32 m0, %20, 0xc600\n\t" "s_nop 0\n\t" "global_load_lds_dwordx4 v233, s[42:43]\n\t"
                     "s_add_u32 m0, %20, 0xca00\n\t" "s_nop 0\n\t" "global_load_lds_dwordx4 v234, s[42:43]\n\t" "s_add_u32 m0, %20, 0xce00\n\t" "s_nop 0\n\t" "global_load_lds_dwordx4 v235, s[42:43]\n\t"
                     "s_add_u32 m0, %20, 0xd200\n\t" "s_nop 0\n\t" "global_load_lds_dwordx4 v236, s[42:43]\n\t" "s_add_u32 s42, s46, 0x15400\n\t" "s_addc_u32 s43, s47, 0\n\t" "s_add_u32 m0, %20, 0xd600\n\t"
                     "v_xor_b32 v224, 0x80, v232\n\t" "global_load_lds_dwordx4 v224, s[42:43]\n\t" "s_add_u32 m0, %20, 0xda00\n\t" "v_xor_b32 v224, 0x80, v233\n\t" "global_load_lds_dwordx4 v224, s[42:43]\n\t"
                     "s_add_u32 m0, %20, 0xde00\n\t" "v_xor_b32 v224, 0x80, v234\n\t" "global_load_lds_dwordx4 v224, s[42:43]\n\t" "s_add_u32 m0, %20, 0xe200\n\t" "v_xor_b32 v224, 0x80, v235\n\t"
                     "global_load_lds_dwordx4 v224, s[42:43]\n\t" "s_add_u32 m0, %20, 0xe600\n\t" "v_xor_b32 v224, 0x80, v236\n\t" "global_load_lds_dwordx4 v224, s[42:43]\n\t" "s_add_u32 s42, %21, 0x18400\n\t"
                     "v_xor_b32 v237, 0x0, v229\n\t" "v_add_u32 v237, s42, v237\n\t" "v_xor_b32 v238, 0x40, v229\n\t" "v_add_u32 v238, s42, v238\n\t" "v_xor_b32 v239, 0x80, v229\n\t" "v_add_u32 v239, s42, v239\n\t"
                     "v_xor_b32 v226, 0xc0, v229\n\t" "v_add_u32 v226, s42, v226\n\t" "ds_read_b64_tr_b16 a[176:177], v237\n\t" "ds_read_b64_tr_b16 a[178:179], v238 offset:2560\n\t"
                     "ds_read_b64_tr_b16 a[200:201], v239\n\t" "ds_read_b64_tr_b16 a[202:203], v226 offset:2560\n\t" "ds_read_b64_tr_b16 a[224:225], v237 offset:256\n\t"
                     "ds_read_b64_tr_b16 a[226:227], v238 offset:2816\n\t" "ds_read_b64_tr_b16 a[248:249], v239 offset:256\n\t" "ds_read_b64_tr_b16 a[250:251], v226 offset:2816\n\t"
                     "ds_read_b64_tr_b16 v[112:113], v237 offset:512\n\t" "ds_read_b64_tr_b16 v[114:115], v238 offset:3072\n\t" "ds_read_b64_tr_b16 v[136:137], v239 offset:512\n\t"
                     "ds_read_b64_tr_b16 v[138:139], v226 offset:3072\n\t" "ds_read_b64_tr_b16 v[160:161], v237 offset:768\n\t" "ds_read_b64_tr_b16 v[162:163], v238 offset:3328\n\t"
                     "ds_read_b64_tr_b16 v[184:185], v239 offset:768\n\t" "ds_read_b64_tr_b16 v[186:187], v226 offset:3328\n\t" "ds_read_b64_tr_b16 v[208:209], v237 offset:1024\n\t"
                     "ds_read_b64_tr_b16 v[210:211], v238 offset:3584\n\t" "ds_read_b64_tr_b16 v[72:73], v239 offset:1024\n\t" "ds_read_b64_tr_b16 v[74:75], v226 offset:3584\n\t" "s_waitcnt vmcnt(10) lgkmcnt(0)\n\t"
                     "s_barrier\n\t" "s_add_u32 s42, s46, 0x1e000\n\t" "s_addc_u32 s43, s47, 0\n\t" "s_add_u32 m0, %20, 0x18400\n\t" "s_nop 0\n\t" "global_load_lds_dwordx4 v232, s[42:43]\n\t"
                     "s_add_u32 m0, %20, 0x18800\n\t" "s_nop 0\n\t" "global_load_lds_dwordx4 v233, s[42:43]\n\t" "s_add_u32 m0, %20, 0x18c00\n\t" "s_nop 0\n\t" "global_load_lds_dwordx4 v234, s[42:43]\n\t"
                     "s_add_u32 m0, %20, 0x19000\n\t" "s_nop 0\n\t" "global_load_lds_dwordx4 v235, s[42:43]\n\t" "s_add_u32 m0, %20, 0x19400\n\t" "s_nop 0\n\t" "global_load_lds_dwordx4 v236, s[42:43]\n\t"
                     "s_add_u32 s42, s46, 0x1f400\n\t" "s_addc_u32 s43, s47, 0\n\t" "s_add_u32 m0, %20, 0x19800\n\t" "v_xor_b32 v224, 0x80, v232\n\t" "global_load_lds_dwordx4 v224, s[42:43]\n\t"
                     "s_add_u32 m0, %20, 0x19c00\n\t" "v_xor_b32 v224, 0x80, v233\n\t" "global_load_lds_dwordx4 v224, s[42:43]\n\t" "s_add_u32 m0, %20, 0x1a000\n\t" "v_xor_b32 v224, 0x80, v234\n\t"
                     "global_load_lds_dwordx4 v224, s[42:43]\n\t" "s_add_u32 m0, %20, 0x1a400\n\t" "v_xor_b32 v224, 0x80, v235\n\t" "global_load_lds_dwordx4 v224, s[42:43]\n\t" "s_add_u32 m0, %20, 0x1a800\n\t"
                     "v_xor_b32 v224, 0x80, v236\n\t" "global_load_lds_dwordx4 v224, s[42:43]\n\t" "s_add_u32 s42, %21, 0x0\n\t" "v_xor_b32 v237, 0x0, v229\n\t" "v_add_u32 v237, s42, v237\n\t"
                     "v_xor_b32 v238, 0x40, v229\n\t" "v_add_u32 v238, s42, v238\n\t" "v_xor_b32 v239, 0x80, v229\n\t" "v_add_u32 v239, s42, v239\n\t" "v_xor_b32 v226, 0xc0, v229\n\t" "v_add_u32 v226, s42, v226\n\t"
                     "ds_read_b64_tr_b16 a[180:181], v237\n\t" "ds_read_b64_tr_b16 a[182:183], v238 offset:2560\n\t" "ds_read_b64_tr_b16 a[204:205], v239\n\t" "ds_read_b64_tr_b16 a[206:207], v226 offset:2560\n\t"
                     "ds_read_b64_tr_b16 a[228:229], v237 offset:256\n\t" "ds_read_b64_tr_b16 a[230:231], v238 offset:2816\n\t" "ds_read_b64_tr_b16 a[252:253], v239 offset:256\n\t"
                     "ds_read_b64_tr_b16 a[254:255], v226 offset:2816\n\t" "ds_read_b64_tr_b16 v[116:117], v237 offset:512\n\t" "ds_read_b64_tr_b16 v[118:119], v238 offset:3072\n\t"
                     "ds_read_b64_tr_b16 v[140:141], v239 offset:512\n\t" "ds_read_b64_tr_b16 v[142:143], v226 offset:3072\n\t" "ds_read_b64_tr_b16 v[164:165], v237 offset:768\n\t"
                     "ds_read_b64_tr_b16 v[166:167], v238 offset:3328\n\t" "ds_read_b64_tr_b16 v[188:189], v239 offset:768\n\t" "ds_read_b64_tr_b16 v[190:191], v226 offset:3328\n\t"
                     "ds_read_b64_tr_b16 v[212:213], v237 offset:1024\n\t" "ds_read_b64_tr_b16 v[214:215], v238 offset:3584\n\t" "ds_read_b64_tr_b16 v[76:77], v239 offset:1024\n\t"
                     "ds_read_b64_tr_b16 v[78:79], v226 offset:3584\n\t" "s_waitcnt vmcnt(10) lgkmcnt(0)\n\t" "s_barrier\n\t" "v_mov_b32 v225, 0\n\t" "global_load_dwordx2 %12, v231, %19 sc1\n\t"
                     "global_load_dwordx2 %13, v225, %19 offset:512 sc1\n\t" "global_load_dwordx2 %14, v225, %19 offset:1024 sc1\n\t" "s_add_u32 s42, s46, 0x28000\n\t" "s_addc_u32 s43, s47, 0\n\t"
                     "s_add_u32 m0, %20, 0x0\n\t" "s_nop 0\n\t" "global_load_lds_dwordx4 v232, s[42:43]\n\t" "s_add_u32 m0, %20, 0x400\n\t" "s_nop 0\n\t" "global_load_lds_dwordx4 v233, s[42:43]\n\t"
                     "s_add_u32 m0, %20, 0x800\n\t" "s_nop 0\n\t" "global_load_lds_dwordx4 v234, s[42:43]\n\t" "s_add_u32 m0, %20, 0xc00\n\t" "s_nop 0\n\t" "global_load_lds_dwordx4 v235, s[42:43]\n\t"
                     "s_add_u32 m0, %20, 0x1000\n\t" "s_nop 0\n\t" "global_load_lds_dwordx4 v236, s[42:43]\n\t" "s_add_u32 s42, s46, 0x29400\n\t" "s_addc_u32 s43, s47, 0\n\t" "s_add_u32 m0, %20, 0x1400\n\t"
                     "v_xor_b32 v224, 0x80, v232\n\t" "global_load_lds_dwordx4 v224, s[42:43]\n\t" "s_add_u32 m0, %20, 0x1800\n\t" "v_xor_b32 v224, 0x80, v233\n\t" "global_load_lds_dwordx4 v224, s[42:43]\n\t"
                     "s_add_u32 m0, %20, 0x1c00\n\t" "v_xor_b32 v224, 0x80, v234\n\t" "global_load_lds_dwordx4 v224, s[42:43]\n\t" "s_add_u32 m0, %20, 0x2000\n\t" "v_xor_b32 v224, 0x80, v235\n\t"
                     "global_load_lds_dwordx4 v224, s[42:43]\n\t" "s_add_u32 m0, %20, 0x2400\n\t" "v_xor_b32 v224, 0x80, v236\n\t" "global_load_lds_dwordx4 v224, s[42:43]\n\t" "s_add_u32 s42, %21, 0xc200\n\t"
                     "v_xor_b32 v237, 0x0, v229\n\t" "v_add_u32 v237, s42, v237\n\t" "v_xor_b32 v238, 0x40, v229\n\t" "v_add_u32 v238, s42, v238\n\t" "v_xor_b32 v239, 0x80, v229\n\t" "v_add_u32 v239, s42, v239\n\t"
                     "v_xor_b32 v226, 0xc0, v229\n\t" "v_add_u32 v226, s42, v226\n\t" "ds_read_b64_tr_b16 a[184:185], v237\n\t" "ds_read_b64_tr_b16 a[186:187], v238 offset:2560\n\t"
                     "ds_read_b64_tr_b16 a[208:209], v239\n\t" "ds_read_b64_tr_b16 a[210:211], v226 offset:2560\n\t" "ds_read_b64_tr_b16 a[232:233], v237 offset:256\n\t"
                     "ds_read_b64_tr_b16 a[234:235], v238 offset:2816\n\t" "ds_read_b64_tr_b16 v[96:97], v239 offset:256\n\t" "ds_read_b64_tr_b16 v[98:99], v226 offset:2816\n\t"
                     "ds_read_b64_tr_b16 v[120:121], v237 offset:512\n\t" "ds_read_b64_tr_b16 v[122:123], v238 offset:3072\n\t" "ds_read_b64_tr_b16 v[144:145], v239 offset:512\n\t"
                     "ds_read_b64_tr_b16 v[146:147], v226 offset:3072\n\t" "ds_read_b64_tr_b16 v[168:169], v237 offset:768\n\t" "ds_read_b64_tr_b16 v[170:171], v238 offset:3328\n\t"
                     "ds_read_b64_tr_b16 v[192:193], v239 offset:768\n\t" "ds_read_b64_tr_b16 v[194:195], v226 offset:3328\n\t" "ds_read_b64_tr_b16 v[216:217], v237 offset:1024\n\t"
                     "ds_read_b64_tr_b16 v[218:219], v238 offset:3584\n\t" "ds_read_b64_tr_b16 v[80:81], v239 offset:1024\n\t" "ds_read_b64_tr_b16 v[82:83], v226 offset:3584\n\t" "s_waitcnt vmcnt(10) lgkmcnt(0)\n\t"
                     "s_barrier\n\t" "s_add_u32 s42, s46, 0x32000\n\t" "s_addc_u32 s43, s47, 0\n\t" "s_add_u32 m0, %20, 0xc200\n\t" "s_nop 0\n\t" "global_load_lds_dwordx4 v232, s[42:43]\n\t" "s_add_u32 m0, %20, 0xc600\n\t"
                     "s_nop 0\n\t" "global_load_lds_dwordx4 v233, s[42:43]\n\t" "s_add_u32 m0, %20, 0xca00\n\t" "s_nop 0\n\t" "global_load_lds_dwordx4 v234, s[42:43]\n\t" "s_add_u32 m0, %20, 0xce00\n\t" "s_nop 0\n\t"
                     "global_load_lds_dwordx4 v235, s[42:43]\n\t" "s_add_u32 m0, %20, 0xd200\n\t" "s_nop 0\n\t" "global_load_lds_dwordx4 v236, s[42:43]\n\t" "s_add_u32 s42, s46, 0x33400\n\t" "s_addc_u32 s43, s47, 0\n\t"
                     "s_add_u32 m0, %20, 0xd600\n\t" "v_xor_b32 v224, 0x80, v232\n\t" "global_load_lds_dwordx4 v224, s[42:43]\n\t" "s_add_u32 m0, %20, 0xda00\n\t" "v_xor_b32 v224, 0x80, v233\n\t"
                     "global_load_lds_dwordx4 v224, s[42:43]\n\t" "s_add_u32 m0, %20, 0xde00\n\t" "v_xor_b32 v224, 0x80, v234\n\t" "global_load_lds_dwordx4 v224, s[42:43]\n\t" "s_add_u32 m0, %20, 0xe200\n\t"
                     "v_xor_b32 v224, 0x80, v235\n\t" "global_load_lds_dwordx4 v224, s[42:43]\n\t" "s_add_u32 m0, %20, 0xe600\n\t" "v_xor_b32 v224, 0x80, v236\n\t" "global_load_lds_dwordx4 v224, s[42:43]\n\t"
                     "s_add_u32 s42, %21, 0x18400\n\t" "v_xor_b32 v237, 0x0, v229\n\t" "v_add_u32 v237, s42, v237\n\t" "v_xor_b32 v238, 0x40, v229\n\t" "v_add_u32 v238, s42, v238\n\t" "v_xor_b32 v239, 0x80, v229\n\t"
                     "v_add_u32 v239, s42, v239\n\t" "v_xor_b32 v226, 0xc0, v229\n\t" "v_add_u32 v226, s42, v226\n\t" "ds_read_b64_tr_b16 a[188:189], v237\n\t" "ds_read_b64_tr_b16 a[190:191], v238 offset:2560\n\t"
                     "ds_read_b64_tr_b16 a[212:213], v239\n\t" "ds_read_b64_tr_b16 a[214:215], v226 offset:2560\n\t" "ds_read_b64_tr_b16 a[236:237], v237 offset:256\n\t"
                     "ds_read_b64_tr_b16 a[238:239], v238 offset:2816\n\t" "ds_read_b64_tr_b16 v[100:101], v239 offset:256\n\t" "ds_read_b64_tr_b16 v[102:103], v226 offset:2816\n\t"
                     "ds_read_b64_tr_b16 v[124:125], v237 offset:512\n\t" "ds_read_b64_tr_b16 v[126:127], v238 offset:3072\n\t" "ds_read_b64_tr_b16 v[148:149], v239 offset:512\n\t"
                     "ds_read_b64_tr_b16 v[150:151], v226 offset:3072\n\t" "ds_read_b64_tr_b16 v[172:173], v237 offset:768\n\t" "ds_read_b64_tr_b16 v[174:175], v238 offset:3328\n\t"
                     "ds_read_b64_tr_b16 v[196:197], v239 offset:768\n\t" "ds_read_b64_tr_b16 v[198:199], v226 offset:3328\n\t" "ds_read_b64_tr_b16 v[220:221], v237 offset:1024\n\t"
                     "ds_read_b64_tr_b16 v[222:223], v238 offset:3584\n\t" "ds_read_b64_tr_b16 v[84:85], v239 offset:1024\n\t" "ds_read_b64_tr_b16 v[86:87], v226 offset:3584\n\t" "s_waitcnt vmcnt(10) lgkmcnt(0)\n\t"
                     "s_barrier\n\t" "s_add_u32 s42, %21, 0x0\n\t" "v_xor_b32 v237, 0x0, v229\n\t" "v_add_u32 v237, s42, v237\n\t" "v_xor_b32 v238, 0x40, v229\n\t" "v_add_u32 v238, s42, v238\n\t"
                     "v_xor_b32 v239, 0x80, v229\n\t" "v_add_u32 v239, s42, v239\n\t" "v_xor_b32 v226, 0xc0, v229\n\t" "v_add_u32 v226, s42, v226\n\t" "ds_read_b64_tr_b16 a[192:193], v237\n\t"
                     "ds_read_b64_tr_b16 a[194:195], v238 offset:2560\n\t" "ds_read_b64_tr_b16 a[216:217], v239\n\t" "ds_read_b64_tr_b16 a[218:219], v226 offset:2560\n\t"
                     "ds_read_b64_tr_b16 a[240:241], v237 offset:256\n\t" "ds_read_b64_tr_b16 a[242:243], v238 offset:2816\n\t" "ds_read_b64_tr_b16 v[104:105], v239 offset:256\n\t"
                     "ds_read_b64_tr_b16 v[106:107], v226 offset:2816\n\t" "ds_read_b64_tr_b16 v[128:129], v237 offset:512\n\t" "ds_read_b64_tr_b16 v[130:131], v238 offset:3072\n\t"
                     "ds_read_b64_tr_b16 v[152:153], v239 offset:512\n\t" "ds_read_b64_tr_b16 v[154:155], v226 offset:3072\n\t" "ds_read_b64_tr_b16 v[176:177], v237 offset:768\n\t"
                     "ds_read_b64_tr_b16 v[178:179], v238 offset:3328\n\t" "ds_read_b64_tr_b16 v[200:201], v239 offset:768\n\t" "ds_read_b64_tr_b16 v[202:203], v226 offset:3328\n\t"
                     "ds_read_b64_tr_b16 v[64:65], v237 offset:1024\n\t" "ds_read_b64_tr_b16 v[66:67], v238 offset:3584\n\t" "ds_read_b64_tr_b16 v[88:89], v239 offset:1024\n\t"
                     "ds_read_b64_tr_b16 v[90:91], v226 offset:3584\n\t" "s_waitcnt vmcnt(0) lgkmcnt(0)\n\t" "s_barrier\n\t" "s_add_u32 s42, %21, 0xc200\n\t" "v_xor_b32 v237, 0x0, v229\n\t" "v_add_u32 v237, s42, v237\n\t"
                     "v_xor_b32 v238, 0x40, v229\n\t" "v_add_u32 v238, s42, v238\n\t" "v_xor_b32 v239, 0x80, v229\n\t" "v_add_u32 v239, s42, v239\n\t" "v_xor_b32 v226, 0xc0, v229\n\t" "v_add_u32 v226, s42, v226\n\t"
                     "ds_read_b64_tr_b16 a[196:197], v237\n\t" "ds_read_b64_tr_b16 a[198:199], v238 offset:2560\n\t" "ds_read_b64_tr_b16 a[220:221], v239\n\t" "ds_read_b64_tr_b16 a[222:223], v226 offset:2560\n\t"
                     "ds_read_b64_tr_b16 a[244:245], v237 offset:256\n\t" "ds_read_b64_tr_b16 a[246:247], v238 offset:2816\n\t" "ds_read_b64_tr_b16 v[108:109], v239 offset:256\n\t"
                     "ds_read_b64_tr_b16 v[110:111], v226 offset:2816\n\t" "ds_read_b64_tr_b16 v[132:133], v237 offset:512\n\t" "ds_read_b64_tr_b16 v[134:135], v238 offset:3072\n\t"
                     "ds_read_b64_tr_b16 v[156:157], v239 offset:512\n\t" "ds_read_b64_tr_b16 v[158:159], v226 offset:3072\n\t" "ds_read_b64_tr_b16 v[180:181], v237 offset:768\n\t"
                     "ds_read_b64_tr_b16 v[182:183], v238 offset:3328\n\t" "ds_read_b64_tr_b16 v[204:205], v239 offset:768\n\t" "ds_read_b64_tr_b16 v[206:207], v226 offset:3328\n\t"
                     "ds_read_b64_tr_b16 v[68:69], v237 offset:1024\n\t" "ds_read_b64_tr_b16 v[70:71], v238 offset:3584\n\t" "ds_read_b64_tr_b16 v[92:93], v239 offset:1024\n\t"
                     "ds_read_b64_tr_b16 v[94:95], v226 offset:3584\n\t" "s_waitcnt lgkmcnt(0)\n\t" "ds_write_b128 v230, v[0:3]\n\t" "ds_write_b128 v230, v[4:7] offset:1024\n\t"
                     "ds_write_b128 v230, v[8:11] offset:2048\n\t" "ds_write_b128 v230, v[12:15] offset:3072\n\t" "ds_write_b128 v230, v[16:19] offset:4096\n\t" "ds_write_b128 v230, v[20:23] offset:5120\n\t"
                     "ds_write_b128 v230, v[24:27] offset:6144\n\t" "ds_write_b128 v230, v[28:31] offset:7168\n\t" "ds_write_b128 v230, v[32:35] offset:8192\n\t" "ds_write_b128 v230, v[36:39] offset:9216\n\t"
                     "ds_write_b128 v230, v[40:43] offset:10240\n\t" "ds_write_b128 v230, v[44:47] offset:11264\n\t" "ds_write_b128 v230, v[48:51] offset:12288\n\t" "ds_write_b128 v230, v[52:55] offset:13312\n\t"
                     "ds_write_b128 v230, v[56:59] offset:14336\n\t" "ds_write_b128 v230, v[60:63] offset:15360\n\t" "ds_write_b128 v230, v[64:67] offset:16384\n\t" "ds_write_b128 v230, v[68:71] offset:17408\n\t"
                     "ds_write_b128 v230, v[72:75] offset:18432\n\t" "ds_write_b128 v230, v[76:79] offset:19456\n\t" "ds_write_b128 v230, v[80:83] offset:20480\n\t" "ds_write_b128 v230, v[84:87] offset:21504\n\t"
                     "ds_write_b128 v230, v[88:91] offset:22528\n\t" "ds_write_b128 v230, v[92:95] offset:23552\n\t"
                     : "=&{a[0:31]}"(wa[0]), "=&{a[32:63]}"(wa[1]), "=&{a[64:95]}"(wa[2]), "=&{a[96:127]}"(wa[3]), "=&{a[128:159]}"(wa[4]), "=&{a[160:191]}"(wa[5]), "=&{a[192:223]}"(wa[6]), "=&{a[224:255]}"(wa[7]), "=&{v[96:127]}"(wv[0]), "=&{v[128:159]}"(wv[1]), "=&{v[160:191]}"(wv[2]), "=&{v[192:223]}"(wv[3]), "=&v"(y.r), "=&v"(y.g), "=&v"(y.e), "+{v[224:239]}"(pa)
                     : "{s[44:45]}"(p.wv + h * DH * 2 + (size_t)(16 * w) * (HD * 2)), "{s[46:47]}"(p.wo + (size_t)(h * DH + 8 * w) * 1280),
                       "s"(lds + 2 * w * GS), "s"(p.ws + OFF_LAT), "s"(lds + w * 10240), "s"(lds), "s"((h & 4) * 16 * HD * 2)
                     : "v0", "v1", "v2", "v3", "v4", "v5", "v6", "v7", "v8", "v9", "v10", "v11", "v12", "v13", "v14", "v15", "v16", "v17", "v18", "v19", "v20", "v21", "v22", "v23", "v24", "v25", "v26", "v27", "v28", "v29", "v30", "v31", "v32", "v33", "v34", "v35", "v36", "v37", "v38", "v39", "v40", "v41", "v42", "v43", "v44", "v45", "v46", "v47", "v48", "v49", "v50", "v51", "v52", "v53", "v54", "v55", "v56", "v57", "v58", "v59", "v60", "v61", "v62", "v63", "v64", "v65", "v66", "v67", "v68", "v69", "v70", "v71", "v72", "v73", "v74", "v75", "v76", "v77", "v78", "v79", "v80", "v81", "v82", "v83", "v84", "v85", "v86", "v87", "v88", "v89", "v90", "v91", "v92", "v93", "v94", "v95", "s42", "s43", "scc", "memory");
        sd = sync_end(y, p.ws, phi, w == 0);
        if (w == 3) {
            u32 r;
            rdy_add(r, p.ws + OFF_ST);
            asm volatile("s_waitcnt vmcnt(0)" : "+v"(r) : : "memory");
            rdy_end(r, p.ws + OFF_ST, cfg.s1 >> 8 & 255);
        }
        barrier();
    }
    const int lane = lanex(), tid = 64 * w + lane;
    const int n = lane & 15, g = lane >> 4, qq = (lane >> 2) & 3, pp = lane & 3;
    char* const fl = p.ws + OFF_ST;
    const u32 iw = lds + 65536 + ((lane * 8) ^ (lane & 32));
    const u32 qr0 = lds + 65536 + (g >> 1) * 512 + pp * 128 + 64 * (g & 1) + 8 * qq + 32 * (pp >> 1);
    const u32 uo = (u32)((4 * w + (n >> 3)) * 4096 + h * 128 + (n & 7) * 16);
    u32x16 ad = u32x16{(u32)lane * 16 + 1024 + (h & 4) * 256, (lane & 2) ? 0xEEEEu : 0x4444u, lds + (u32)((w & 1) * 24576 + (w >> 1) * 99328 + lane * 16), iw + ((3 * w + (w & 1)) >> 1) * 512,
                       iw + (1 + 3 * (w >> 1)) * 512 + 4 * (w & 1), qr0, qr0 ^ 32, uo + (g & 1) * 8192 + (1 + (g >> 1)) * 65536, uo + (g & 1) * 8192 + (g >> 1) * 8, uo, (u32)lane * 16 + 1024 - (h & 4) * 256, uo, uo, uo, uo, uo};
    asm volatile("" : "+{v[224:239]}"(ad));
    const char* memp = p.ws + mslot(sd, 0);
    int budget = 30000;
    Clock clk; clk.init();
    for (int s = 0; s < NSEG; ++s) {
        int tries = 0;
        u64 bad, tp8, en = ~0ull;
        while (true) {
            const u32 tgt = s ? clk.sched(phi, 0, cfg.G, tries ? 2 : 0) : mt();
            char* up = p.ws + uslot(sd, s);
            asm volatile("ds_read_b128 v[64:67], v226\n\t" "ds_read_b128 v[68:71], v226 offset:1024\n\t" "ds_read_b128 v[72:75], v226 offset:2048\n\t" "ds_read_b128 v[76:79], v226 offset:3072\n\t"
                         "ds_read_b128 v[80:83], v226 offset:4096\n\t" "ds_read_b128 v[84:87], v226 offset:5120\n\t" "ds_read_b128 v[88:91], v226 offset:6144\n\t" "ds_read_b128 v[92:95], v226 offset:7168\n\t"
                         "v_mov_b64 v[40:41], 0\n\t" "v_mov_b64 v[42:43], 0\n\t" "v_mov_b64 v[44:45], 0\n\t" "v_mov_b64 v[46:47], 0\n\t" "v_mov_b64 v[48:49], 0\n\t" "v_mov_b64 v[50:51], 0\n\t" ".Lsp_u:\n\t" "s_memtime vcc\n\t"
                         "s_waitcnt lgkmcnt(0)\n\t" "s_sub_i32 s42, %3, vcc_lo\n\t" "s_cmp_gt_i32 s42, 80\n\t" "s_cbranch_scc1 .Lsp_u\n\t" "s_max_i32 s42, s42, 0\n\t" "s_sub_i32 s42, 92, s42\n\t" "s_and_b32 s42, s42, -4\n\t"
                         "s_getpc_b64 vcc\n\t" "s_add_u32 vcc_lo, vcc_lo, s42\n\t" "s_addc_u32 vcc_hi, vcc_hi, 0\n\t" "s_setpc_b64 vcc\n\t" "s_nop 0\n\t" "s_nop 0\n\t" "s_nop 0\n\t" "s_nop 0\n\t" "s_nop 0\n\t" "s_nop 0\n\t"
                         "s_nop 0\n\t" "s_nop 0\n\t" "s_nop 0\n\t" "s_nop 0\n\t" "s_nop 0\n\t" "s_nop 0\n\t" "s_nop 0\n\t" "s_nop 0\n\t" "s_nop 0\n\t" "s_nop 0\n\t" "s_nop 0\n\t" "s_nop 0\n\t" "s_nop 0\n\t" "s_nop 0\n\t"
                         "global_load_dwordx4 v[0:3], v224, %4 offset:-4096 sc1\n\t" "s_nop 10\n\t" "global_load_dwordx4 v[4:7], v224, %4 offset:-2048 sc1\n\t" "s_nop 15\n\t"
                         "global_load_dwordx4 v[8:11], v234, %4 offset:-3072 sc1\n\t" "s_nop 10\n\t" "global_load_dwordx4 v[12:15], v234, %4 offset:-1024 sc1\n\t" "s_nop 10\n\t" "global_load_dwordx4 v[16:19], v224, %4 sc1\n\t"
                         "s_nop 10\n\t" "global_load_dwordx4 v[20:23], v234, %4 offset:1024 sc1\n\t" "s_nop 10\n\t" "global_load_dwordx4 v[24:27], v224, %4 offset:2048 sc1\n\t" "s_nop 10\n\t"
                         "global_load_dwordx4 v[28:31], v234, %4 offset:3072 sc1\n\t" "s_nop 10\n\t" "global_load_dwordx4 v[32:35], v224, %5 offset:-4096 sc1\n\t" "s_nop 10\n\t"
                         "global_load_dwordx4 v[36:39], v234, %5 offset:-3072 sc1\n\t" "s_waitcnt vmcnt(9)\n\t" "v_smfmac_f32_16x16x64_bf16 v[40:43], v[0:3], a[0:7], v225\n\t"
                         "v_smfmac_f32_16x16x64_bf16 v[44:47], v[0:3], v[64:71], v225\n\t" "ds_read_b128 v[64:67], v226 offset:8192\n\t" "v_smfmac_f32_16x16x64_bf16 v[48:51], v[0:3], a[8:15], v225\n\t"
                         "ds_read_b128 v[68:71], v226 offset:9216\n\t" "v_cmp_eq_u32 vcc, -1, v0\n\t" "s_waitcnt vmcnt(8)\n\t" "v_smfmac_f32_16x16x64_bf16 v[40:43], v[4:7], a[16:23], v225\n\t" "v_mov_b64 v[0:1], 0\n\t"
                         "v_smfmac_f32_16x16x64_bf16 v[44:47], v[4:7], v[72:79], v225\n\t" "s_and_b64 vcc, vcc, %6\n\t" "s_cbranch_scc1 .Lsk_u\n\t" "ds_read_b128 v[72:75], v226 offset:10240\n\t" "v_mov_b64 v[2:3], 0\n\t"
                         "v_smfmac_f32_16x16x64_bf16 v[48:51], v[4:7], a[24:31], v225\n\t" "ds_read_b128 v[76:79], v226 offset:11264\n\t" "s_waitcnt vmcnt(7)\n\t"
                         "v_smfmac_f32_16x16x64_bf16 v[40:43], v[8:11], a[32:39], v225\n\t" "v_mov_b64 v[4:5], 0\n\t" "v_smfmac_f32_16x16x64_bf16 v[44:47], v[8:11], v[80:87], v225\n\t"
                         "ds_read_b128 v[80:83], v226 offset:12288\n\t" "v_mov_b64 v[6:7], 0\n\t" "v_smfmac_f32_16x16x64_bf16 v[48:51], v[8:11], a[40:47], v225\n\t" "ds_read_b128 v[84:87], v226 offset:13312\n\t"
                         "s_waitcnt vmcnt(6)\n\t" "v_smfmac_f32_16x16x64_bf16 v[40:43], v[12:15], a[48:55], v225\n\t" "v_mov_b64 v[8:9], 0\n\t" "v_smfmac_f32_16x16x64_bf16 v[44:47], v[12:15], v[88:95], v225\n\t"
                         "ds_read_b128 v[88:91], v226 offset:14336\n\t" "v_mov_b64 v[10:11], 0\n\t" "v_smfmac_f32_16x16x64_bf16 v[48:51], v[12:15], a[56:63], v225\n\t" "ds_read_b128 v[92:95], v226 offset:15360\n\t"
                         "s_waitcnt vmcnt(5)\n\t" "v_smfmac_f32_16x16x64_bf16 v[40:43], v[16:19], a[64:71], v225\n\t" "v_mov_b64 v[12:13], 0\n\t" "s_waitcnt lgkmcnt(6)\n\t"
                         "v_smfmac_f32_16x16x64_bf16 v[44:47], v[16:19], v[64:71], v225\n\t" "ds_read_b128 v[64:67], v226 offset:16384\n\t" "v_mov_b64 v[14:15], 0\n\t"
                         "v_smfmac_f32_16x16x64_bf16 v[48:51], v[16:19], a[72:79], v225\n\t" "ds_read_b128 v[68:71], v226 offset:17408\n\t" "s_waitcnt vmcnt(4)\n\t"
                         "v_smfmac_f32_16x16x64_bf16 v[40:43], v[20:23], a[80:87], v225\n\t" "v_mov_b64 v[16:17], 0\n\t" "s_waitcnt lgkmcnt(6)\n\t" "v_smfmac_f32_16x16x64_bf16 v[44:47], v[20:23], v[72:79], v225\n\t"
                         "ds_read_b128 v[72:75], v226 offset:18432\n\t" "v_mov_b64 v[18:19], 0\n\t" "v_smfmac_f32_16x16x64_bf16 v[48:51], v[20:23], a[88:95], v225\n\t" "ds_read_b128 v[76:79], v226 offset:19456\n\t"
                         "s_waitcnt vmcnt(3)\n\t" "v_smfmac_f32_16x16x64_bf16 v[40:43], v[24:27], a[96:103], v225\n\t" "v_mov_b64 v[20:21], 0\n\t" "s_waitcnt lgkmcnt(6)\n\t"
                         "v_smfmac_f32_16x16x64_bf16 v[44:47], v[24:27], v[80:87], v225\n\t" "ds_read_b128 v[80:83], v226 offset:20480\n\t" "v_mov_b64 v[22:23], 0\n\t"
                         "v_smfmac_f32_16x16x64_bf16 v[48:51], v[24:27], a[104:111], v225\n\t" "ds_read_b128 v[84:87], v226 offset:21504\n\t" "s_waitcnt vmcnt(2)\n\t"
                         "v_smfmac_f32_16x16x64_bf16 v[40:43], v[28:31], a[112:119], v225\n\t" "v_mov_b64 v[24:25], 0\n\t" "s_waitcnt lgkmcnt(6)\n\t" "v_smfmac_f32_16x16x64_bf16 v[44:47], v[28:31], v[88:95], v225\n\t"
                         "ds_read_b128 v[88:91], v226 offset:22528\n\t" "v_mov_b64 v[26:27], 0\n\t" "v_smfmac_f32_16x16x64_bf16 v[48:51], v[28:31], a[120:127], v225\n\t" "ds_read_b128 v[92:95], v226 offset:23552\n\t"
                         "s_waitcnt vmcnt(1)\n\t" "v_smfmac_f32_16x16x64_bf16 v[40:43], v[32:35], a[128:135], v225\n\t" "v_mov_b64 v[28:29], 0\n\t" "v_smfmac_f32_16x16x64_bf16 v[44:47], v[32:35], a[136:143], v225\n\t"
                         "v_mov_b64 v[30:31], 0\n\t" "v_smfmac_f32_16x16x64_bf16 v[48:51], v[32:35], a[144:151], v225\n\t" "s_waitcnt vmcnt(0)\n\t" "v_smfmac_f32_16x16x64_bf16 v[40:43], v[36:39], a[152:159], v225\n\t"
                         "v_mov_b64 v[32:33], 0\n\t" "v_smfmac_f32_16x16x64_bf16 v[44:47], v[36:39], a[160:167], v225\n\t" "v_mov_b64 v[34:35], 0\n\t" "v_smfmac_f32_16x16x64_bf16 v[48:51], v[36:39], a[168:175], v225\n\t"
                         "v_add_f32 v52, v40, v42\n\t" "v_add_f32 v53, v41, v43\n\t" "v_cmp_u_f32 vcc, v52, v53\n\t" "v_add_f32 v54, v44, v46\n\t" "v_add_f32 v55, v45, v47\n\t" "v_cvt_pk_bf16_f32 v58, v52, v53\n\t"
                         "v_pk_add_f32 v[56:57], v[48:49], v[50:51]\n\t" "v_cvt_pk_bf16_f32 v59, v54, v55\n\t" "v_cvt_pk_bf16_f32 v60, v56, v57\n\t" "ds_write_b64 v227, v[58:59]\n\t" "ds_write_b32 v228, v60\n\t"
                         "v_mov_b64 v[36:37], 0\n\t" "v_mov_b64 v[38:39], 0\n\t" "s_and_b64 vcc, vcc, %6\n\t" "v_mov_b32 v233, 0\n\t" "s_cbranch_scc1 .Lsk_u\n\t" "v_mov_b32 v233, 0\n\t" "v_mov_b32 v233, 0\n\t"
                         "v_mov_b32 v233, 0\n\t" "s_waitcnt lgkmcnt(0)\n\t" "s_barrier\n\t" "ds_read_b64_tr_b16 v[40:41], v229\n\t" "ds_read_b64_tr_b16 v[42:43], v230\n\t" "v_mov_b32 v233, 0\n\t"
                         "ds_read_b64_tr_b16 v[44:45], v229 offset:1024\n\t" "v_mov_b32 v233, 0\n\t" "ds_read_b64_tr_b16 v[46:47], v230 offset:1024\n\t" "v_mov_b32 v233, 0\n\t"
                         "ds_read_b64_tr_b16 v[48:49], v229 offset:2048\n\t" "v_mov_b32 v233, 0\n\t" "ds_read_b64_tr_b16 v[50:51], v230 offset:2048\n\t" "v_mov_b32 v233, 0\n\t" "s_waitcnt lgkmcnt(4)\n\t"
                         "v_smfmac_f32_16x16x64_bf16 v[0:3], v[40:43], a[176:183], v225\n\t" "v_smfmac_f32_16x16x64_bf16 v[4:7], v[40:43], a[200:207], v225\n\t" "s_waitcnt lgkmcnt(2)\n\t"
                         "v_smfmac_f32_16x16x64_bf16 v[0:3], v[44:47], a[184:191], v225\n\t" "v_smfmac_f32_16x16x64_bf16 v[4:7], v[44:47], a[208:215], v225\n\t" "s_waitcnt lgkmcnt(0)\n\t"
                         "v_smfmac_f32_16x16x64_bf16 v[0:3], v[48:51], a[192:199], v225\n\t" "v_smfmac_f32_16x16x64_bf16 v[4:7], v[48:51], a[216:223], v225\n\t"
                         "v_smfmac_f32_16x16x64_bf16 v[8:11], v[40:43], a[224:231], v225\n\t" "v_add_f32 v52, v0, v2\n\t" "v_add_f32 v53, v1, v3\n\t" "v_cvt_pk_bf16_f32 v54, v53, v52\n\t"
                         "v_smfmac_f32_16x16x64_bf16 v[8:11], v[44:47], a[232:239], v225\n\t" "v_add_f32 v52, v4, v6\n\t" "v_add_f32 v53, v5, v7\n\t" "v_cvt_pk_bf16_f32 v55, v53, v52\n\t" "s_nop 1\n\t"
                         "v_permlane16_swap_b32 v54, v55\n\t" "v_smfmac_f32_16x16x64_bf16 v[8:11], v[48:51], a[240:247], v225\n\t" "global_store_dwordx2 v232, v[54:55], %2 sc1\n\t" "s_add_u32 s40, s40, 0x10000\n\t"
                         "s_addc_u32 s41, s41, 0\n\t" "v_smfmac_f32_16x16x64_bf16 v[12:15], v[40:43], a[248:255], v225\n\t" "v_smfmac_f32_16x16x64_bf16 v[12:15], v[44:47], v[96:103], v225\n\t" "v_add_f32 v52, v8, v10\n\t"
                         "v_smfmac_f32_16x16x64_bf16 v[12:15], v[48:51], v[104:111], v225\n\t" "v_add_f32 v53, v9, v11\n\t" "v_smfmac_f32_16x16x64_bf16 v[16:19], v[40:43], v[112:119], v225\n\t"
                         "v_cvt_pk_bf16_f32 v56, v53, v52\n\t" "v_smfmac_f32_16x16x64_bf16 v[16:19], v[44:47], v[120:127], v225\n\t" "v_add_f32 v52, v12, v14\n\t" "v_add_f32 v53, v13, v15\n\t"
                         "v_cvt_pk_bf16_f32 v57, v53, v52\n\t" "s_nop 1\n\t" "v_permlane16_swap_b32 v56, v57\n\t" "v_smfmac_f32_16x16x64_bf16 v[16:19], v[48:51], v[128:135], v225\n\t"
                         "global_store_dwordx2 v232, v[56:57], %2 sc1\n\t" "s_add_u32 s40, s40, 0x10000\n\t" "s_addc_u32 s41, s41, 0\n\t" "v_smfmac_f32_16x16x64_bf16 v[20:23], v[40:43], v[136:143], v225\n\t"
                         "v_smfmac_f32_16x16x64_bf16 v[20:23], v[44:47], v[144:151], v225\n\t" "v_add_f32 v52, v16, v18\n\t" "v_smfmac_f32_16x16x64_bf16 v[20:23], v[48:51], v[152:159], v225\n\t" "v_add_f32 v53, v17, v19\n\t"
                         "v_smfmac_f32_16x16x64_bf16 v[24:27], v[40:43], v[160:167], v225\n\t" "v_cvt_pk_bf16_f32 v58, v53, v52\n\t" "v_smfmac_f32_16x16x64_bf16 v[24:27], v[44:47], v[168:175], v225\n\t"
                         "v_add_f32 v52, v20, v22\n\t" "v_add_f32 v53, v21, v23\n\t" "v_smfmac_f32_16x16x64_bf16 v[24:27], v[48:51], v[176:183], v225\n\t" "v_cvt_pk_bf16_f32 v59, v53, v52\n\t"
                         "v_smfmac_f32_16x16x64_bf16 v[28:31], v[40:43], v[184:191], v225\n\t" "s_nop 0\n\t" "v_permlane16_swap_b32 v58, v59\n\t" "v_smfmac_f32_16x16x64_bf16 v[28:31], v[44:47], v[192:199], v225\n\t"
                         "global_store_dwordx2 v232, v[58:59], %2 sc1\n\t" "v_add_f32 v52, v24, v26\n\t" "v_smfmac_f32_16x16x64_bf16 v[28:31], v[48:51], v[200:207], v225\n\t" "v_add_f32 v53, v25, v27\n\t"
                         "v_smfmac_f32_16x16x64_bf16 v[32:35], v[40:43], v[208:215], v225\n\t" "v_cvt_pk_bf16_f32 v60, v53, v52\n\t" "v_smfmac_f32_16x16x64_bf16 v[32:35], v[44:47], v[216:223], v225\n\t"
                         "v_add_f32 v52, v28, v30\n\t" "v_smfmac_f32_16x16x64_bf16 v[32:35], v[48:51], v[64:71], v225\n\t" "v_add_f32 v53, v29, v31\n\t" "v_smfmac_f32_16x16x64_bf16 v[36:39], v[40:43], v[72:79], v225\n\t"
                         "v_cvt_pk_bf16_f32 v61, v53, v52\n\t" "v_smfmac_f32_16x16x64_bf16 v[36:39], v[44:47], v[80:87], v225\n\t" "v_add_f32 v52, v32, v34\n\t"
                         "v_smfmac_f32_16x16x64_bf16 v[36:39], v[48:51], v[88:95], v225\n\t" "v_add_f32 v53, v33, v35\n\t" "v_cvt_pk_bf16_f32 v62, v53, v52\n\t" "v_mov_b32 v233, 0\n\t" "v_mov_b32 v233, 0\n\t"
                         "v_mov_b32 v233, 0\n\t" "v_mov_b32 v233, 0\n\t" "v_mov_b32 v233, 0\n\t" "v_pk_add_f32 v[52:53], v[36:37], v[38:39]\n\t" "v_cvt_pk_bf16_f32 v63, v53, v52\n\t" "v_permlane32_swap_b32 v60, v62\n\t"
                         "s_nop 0\n\t" "v_permlane32_swap_b32 v61, v63\n\t" "s_nop 1\n\t" "v_permlane16_swap_b32 v60, v61\n\t" "v_permlane16_swap_b32 v62, v63\n\t" "global_store_dwordx4 v231, v[60:63], %2 sc1\n\t"
                         ".Lsk_u:\n\t" "s_memrealtime %1\n\t" "s_mov_b64 %0, vcc\n\t" "s_waitcnt lgkmcnt(0)\n\t"
                         : "=&s"(bad), "=&s"(tp8), "+{s[40:41]}"(up)
                         : "s"(uni((int)tgt)), "s"(memp - 1024), "s"(memp + 15360), "s"(en), "{v[224:239]}"(ad),
                           "{a[0:31]}"(wa[0]), "{a[32:63]}"(wa[1]), "{a[64:95]}"(wa[2]), "{a[96:127]}"(wa[3]), "{a[128:159]}"(wa[4]), "{a[160:191]}"(wa[5]), "{a[192:223]}"(wa[6]), "{a[224:255]}"(wa[7]), "{v[96:127]}"(wv[0]), "{v[128:159]}"(wv[1]), "{v[160:191]}"(wv[2]), "{v[192:223]}"(wv[3])
                         : "v0", "v1", "v2", "v3", "v4", "v5", "v6", "v7", "v8", "v9", "v10", "v11", "v12", "v13", "v14", "v15", "v16", "v17", "v18", "v19", "v20", "v21", "v22", "v23", "v24", "v25", "v26", "v27", "v28", "v29", "v30", "v31", "v32", "v33", "v34", "v35", "v36", "v37", "v38", "v39", "v40", "v41", "v42", "v43", "v44", "v45", "v46", "v47", "v48", "v49", "v50", "v51", "v52", "v53", "v54", "v55", "v56", "v57", "v58", "v59", "v60", "v61", "v62", "v63", "v64", "v65", "v66", "v67", "v68", "v69", "v70", "v71", "v72", "v73", "v74", "v75", "v76", "v77", "v78", "v79", "v80", "v81", "v82", "v83", "v84", "v85", "v86", "v87", "v88", "v89", "v90", "v91", "v92", "v93", "v94", "v95", "vcc", "scc", "s42", "memory");
            if (__builtin_expect(bad == 0, 1)) break;
            waitvm<0>();
            if (--budget < 0 || ((++tries & 15) == 0 && !unwritten(memp, lanex() * 16))) en = 0;
            phi += (u32)cfg.G;
        }
        clk.calib();
        const u32 tt = sched_ld0(s);
        if (s >= NSEG - 2) waitvm<0>();
        barrier();
        if (s >= NSEG - 2 && w == 3) flag_add(fl, F_PROG + 8 * s + (h & 7), 1);
        if (s >= NSEG - 2 && w == 2) flag_add(fl, F_UD + 2 * (s & 1) + (h & 1), 1);
        if (s == 0) {
#pragma unroll
            for (int i = UPF; i < 8; ++i) {
                const int l = 64 * min(i * 4 + w, 29) + lanex();
                pfl(p.wq + h * 384, (u32)((l / 3) * 12288 + (l % 3) * 128), lds);
            }
        }
        if (s == 0) { until(phi + UNAP); phi = go_wg(fl, h, w, lds + SL_PF - 16) - (tt >> 16); edge(clk.r1, clk.m1); clk.calib(); }
        follow(phi, (int)(tt >> 16) + 4 + cfg.T3 - VISS, (u32)tp8, cfg.G);
        phi += tt & 0xffffu;
        memp = p.ws + mslot(sd, s + 1);
    }
    asm volatile(".Lkm_lu:\n\ts_waitcnt vmcnt(0)" : : : "memory");
}

#define VALL "{v[0:3]}"(o), "{v[16:47]}"(rv[0]), "{v[48:79]}"(rv[1]), "{v[80:111]}"(rv[2]), "{v[112:143]}"(rv[3]), "{v[144:175]}"(rv[4]), "{v[176:207]}"(rv[5]), "{v[208:239]}"(rv[6]), "{a[0:31]}"(ra[0]), "{a[32:63]}"(ra[1]), "{a[64:95]}"(ra[2]), "{a[96:127]}"(ra[3])
DI void value_role(const Ptrs& p, const ChainCfg cfg, int vb, int sd, u32 phi, u32 lds, int w) {
    const int lane = lanex(), tid = 64 * w + lane, n = lane & 15, g = lane >> 4;
    const int v = vb;
    const u32 moff = lane * 16;
    char* const fl = p.ws + OFF_ST;
    char* const ub = p.ws + uchunk(v);
    const int xh = (vb >> 2) & 1;
    if (w == 3) {
        const u32x4 ff = u32x4{~0u, ~0u, ~0u, ~0u};
        if (SPL) barrier();
#pragma nounroll
        for (int s = 0; s < NSEG - 2; ++s) {
            dly(s == 1 ? HD1 : cfg.cR3 >> 8 & 255);
            char* const mk = ub + uslot(sd, s + 2);
#pragma unroll
            for (int i = 0; i < 8; ++i) { gst16(mk + i * 1024, moff, ff); gap<HQ>(-1); }
            if (vb < 32 && s) {
                gst16(p.ws + (size_t)(s + 2) * HO_STRIDE + vb * 2048, moff, ff);
                gst16(p.ws + (size_t)(s + 2) * HO_STRIDE + vb * 2048 + 1024, moff, ff);
            }
            if (s == 0) {
                u32 r;
                flag_wait(fl, F_VD + (vb & 3) + (v < 20 ? 0 : 4), 32);
                rdy_add(r, fl);
                asm volatile("s_waitcnt vmcnt(0)" : "+v"(r) : : "memory");
                rdy_end(r, fl, cfg.s1 >> 16);
            }
            waitvm<0>();
            if (SPL && s) barrier();
            barrier();
        }
        return;
    }
    if (w) {
        const u32 dof = (u32)((n >> 3) * 16384 + g * 128 + (n & 7) * 16);
        const char* dp = p.ws + OFF_D + (size_t)v * 32768 - 4096 + w * 8192;
        if (SPL) barrier();
        flag_wait(fl, F_VD + (vb & 3) + (v < 20 ? 0 : 4), 32);
#pragma nounroll
        for (int s = 0; s < NSEG - 1; ++s) {
            u32x4 df[16];
            u32 dfl;
            const int on = uni(1 - s);
            sfor<0, SPL>([&](auto I) { gld16c<I.v * 512 - 4096>(df[I.v], dp, dof); gap<HP0>(on); });
            if (SPL) asm volatile("s_cmp_ge_i32 %0, 0\n\ts_cbranch_scc1 .Lhb\n\ts_barrier\n.Lhb:" : : "s"(on) : "memory", "scc");
            dly(s == 1 ? HD1 : cfg.cR3 & 255);
            sfor<SPL, 16>([&](auto I) { gld16c<I.v * 512 - 4096>(df[I.v], dp, dof); gap<HP>(on); });
            dp += D_STRIDE;
            flag_ldm(dfl, fl, F_VD + pblk(s + 1) * 8 + (vb & RM), pblk(s + 1) != pblk(s));
            const u32 la = lds + moff + (s & 1) * 32768 + (w - 1) * 16384;
            sfor<0, 8>([&](auto I) {
                constexpr int i = I.v;
                waitv<15 - 2 * i>(df[2 * i]); waitv<15 - 2 * i>(df[2 * i + 1]);
                const u32x4 x = df[2 * i], y = df[2 * i + 1];
                const u32 lb = i & 2 ? la - xh * 4096 : la + xh * 4096;
                *LDSP(u32x4, lb + 2048 * i) = u32x4{x[0], y[0], x[2], y[2]};
                *LDSP(u32x4, lb + 2048 * i + 1024) = u32x4{x[1], y[1], x[3], y[3]};
                PIN();
            });
            waitvm<0>();
            asm volatile("" : "+v"(dfl));
            if (uni((int)dfl) < 31) {
                dly(FRD);
                flag_ld(dfl, fl, F_VD + pblk(s + 1) * 8 + (vb & RM));
                asm volatile("s_waitcnt vmcnt(0)" : "+v"(dfl) : : "memory");
            }
            barrier();
            if (uni((int)dfl) < 31) flag_wait(fl, F_VD + pblk(s + 1) * 8 + (vb & RM), 32);
        }
        return;
    }
    u32x32 rv[7], ra[4];
    u32x4 o;
    asm volatile("v_accvgpr_write_b32 a64, 0\n\t" "v_accvgpr_write_b32 a65, 0\n\t" "v_accvgpr_write_b32 a66, 0\n\t" "v_accvgpr_write_b32 a67, 0\n\t" "v_accvgpr_write_b32 a68, 0\n\t" "v_accvgpr_write_b32 a69, 0\n\t"
                 "v_accvgpr_write_b32 a70, 0\n\t" "v_accvgpr_write_b32 a71, 0\n\t" "v_accvgpr_write_b32 a72, 0\n\t" "v_accvgpr_write_b32 a73, 0\n\t" "v_accvgpr_write_b32 a74, 0\n\t" "v_accvgpr_write_b32 a75, 0\n\t"
                 "v_accvgpr_write_b32 a76, 0\n\t" "v_accvgpr_write_b32 a77, 0\n\t" "v_accvgpr_write_b32 a78, 0\n\t" "v_accvgpr_write_b32 a79, 0\n\t" "v_mov_b64 v[112:113], -1\n\t" "v_mov_b64 v[120:121], -1\n\t"
                 "v_mov_b64 v[128:129], -1\n\t" "v_mov_b64 v[136:137], -1\n\t" "v_mov_b64 v[144:145], -1\n\t" "v_mov_b64 v[152:153], -1\n\t" "v_mov_b64 v[160:161], -1\n\t" "v_mov_b64 v[168:169], -1\n\t"
                 "v_mov_b64 v[176:177], -1\n\t" "v_mov_b64 v[184:185], -1\n\t" "v_mov_b64 v[192:193], -1\n\t" "v_mov_b64 v[200:201], -1\n\t" "v_mov_b64 v[208:209], -1\n\t" "v_mov_b64 v[216:217], -1\n\t"
                 "v_mov_b64 v[224:225], -1\n\t" "v_mov_b64 v[232:233], -1\n\t"
                 : "={v[0:3]}"(o), "={v[16:47]}"(rv[0]), "={v[48:79]}"(rv[1]), "={v[80:111]}"(rv[2]), "={v[112:143]}"(rv[3]), "={v[144:175]}"(rv[4]), "={v[176:207]}"(rv[5]), "={v[208:239]}"(rv[6]), "={a[0:31]}"(ra[0]), "={a[32:63]}"(ra[1]), "={a[64:95]}"(ra[2]), "={a[96:127]}"(ra[3]));
    const u32 ho = (u32)(min(lane, 39) * 256 + (lane >> 5) * 8192);
    const u32 pof = (u32)((n & 4) * 128 + (n >> 3) * 256 + g * 64 + (n & 3) * 16 + 2048);
    const u32 uof = (u32)((n >> 3) * 4096 + g * 128 + (n & 7) * 16 + 512);
    u32x8 ad = u32x8{uof + xh * 512, pof + xh * 2048, (u32)(((g >> 1) ^ xh) * 256 + (n >> 2) * 64 + (n & 1) * 16), moff,
                     (lane & 2) ? 0xEEEEu : 0x4444u, (lane & 4) ? 0xEEEEu : 0x4444u, (u32)(v * 256 + (v >> 5) * 8192 + g * 64 + (n >> 3) * 32 + (n & 1) * 16 + (n & 6) * 2),
                     (lane & 1) ? 0x07060302u : 0x01000504u};
    u32 pob = pof - xh * 2048, uob = uof - xh * 512, pmo = moff ^ xh * 512;
    asm volatile("" : "+{v[4:11]}"(ad), "+v"(pob), "+v"(uob), "+v"(pmo));
    auto zero = [&]() {
        asm volatile("v_mov_b64 v[0:1], 0\n\t" "v_mov_b64 v[2:3], 0\n\t" "v_mov_b64 v[48:49], 0\n\t" "v_mov_b64 v[50:51], 0\n\t" "v_mov_b64 v[52:53], 0\n\t" "v_mov_b64 v[54:55], 0\n\t" "v_mov_b64 v[56:57], 0\n\t"
                     "v_mov_b64 v[58:59], 0\n\t" "v_mov_b64 v[60:61], 0\n\t" "v_mov_b64 v[62:63], 0\n\t" "v_mov_b64 v[64:65], 0\n\t" "v_mov_b64 v[66:67], 0\n\t" "v_mov_b64 v[68:69], 0\n\t" "v_mov_b64 v[70:71], 0\n\t"
                     "v_mov_b64 v[72:73], 0\n\t" "v_mov_b64 v[74:75], 0\n\t" "v_mov_b64 v[76:77], 0\n\t" "v_mov_b64 v[78:79], 0\n\t" "v_mov_b64 v[80:81], 0\n\t" "v_mov_b64 v[82:83], 0\n\t" "v_mov_b64 v[84:85], 0\n\t"
                     "v_mov_b64 v[86:87], 0\n\t" "v_mov_b64 v[88:89], 0\n\t" "v_mov_b64 v[90:91], 0\n\t" "v_mov_b64 v[92:93], 0\n\t" "v_mov_b64 v[94:95], 0\n\t" "v_mov_b64 v[96:97], 0\n\t" "v_mov_b64 v[98:99], 0\n\t"
                     "v_mov_b64 v[100:101], 0\n\t" "v_mov_b64 v[102:103], 0\n\t" "v_mov_b64 v[104:105], 0\n\t" "v_mov_b64 v[106:107], 0\n\t" "v_mov_b64 v[108:109], 0\n\t" "v_mov_b64 v[110:111], 0\n\t"
                     : : VALL : "memory");
    };
    const char* memp = p.ws + OFF_XF + 4096;
    char* up = ub + OFF_XF;
    int budget = 30000;
    Clock clk; clk.init();
    u32 tt = 0;
    for (int s = -1; s < NSEG - 1; ++s) {
        const char* const mnx = p.ws + mslot(sd, s + 1);
        bool all = false;
        int tries = 0;
        u64 tp8;
        while (true) {
            const u32 tgt = s >= 0 ? clk.sched(phi, (int)(tt >> 16), cfg.G, tries ? 2 : 1) : mt();
            u64 ok;
            asm volatile("s_cmp_lg_u32 %11, 0\n\t" "s_cbranch_scc0 .Lsp_v0\n\t" ".Lsp_v3:\n\t" "s_memtime vcc\n\t" "s_waitcnt lgkmcnt(0)\n\t" "s_sub_i32 s42, %10, vcc_lo\n\t" "s_cmp_gt_i32 s42, 80\n\t"
                         "s_cbranch_scc1 .Lsp_v3\n\t" "s_max_i32 s42, s42, 0\n\t" "s_sub_i32 s42, 92, s42\n\t" "s_and_b32 s42, s42, -4\n\t" "s_getpc_b64 vcc\n\t" "s_add_u32 vcc_lo, vcc_lo, s42\n\t"
                         "s_addc_u32 vcc_hi, vcc_hi, 0\n\t" "s_setpc_b64 vcc\n\t" "s_nop 0\n\t" "s_nop 0\n\t" "s_nop 0\n\t" "s_nop 0\n\t" "s_nop 0\n\t" "s_nop 0\n\t" "s_nop 0\n\t" "s_nop 0\n\t" "s_nop 0\n\t" "s_nop 0\n\t"
                         "s_nop 0\n\t" "s_nop 0\n\t" "s_nop 0\n\t" "s_nop 0\n\t" "s_nop 0\n\t" "s_nop 0\n\t" "s_nop 0\n\t" "s_nop 0\n\t" "s_nop 0\n\t" "s_nop 0\n\t"
                         "global_load_dwordx4 a[0:3], v5, %7 offset:-4096 sc1\n\t" "global_load_dwordx4 a[4:7], v5, %7 offset:-3072 sc1\n\t" "global_load_dwordx4 a[16:19], v5, %7 sc1\n\t"
                         "global_load_dwordx4 a[20:23], v5, %7 offset:1024 sc1\n\t" "global_load_dwordx4 a[32:35], v5, %8 offset:-4096 sc1\n\t" "global_load_dwordx4 a[36:39], v5, %8 offset:-3072 sc1\n\t"
                         "global_load_dwordx4 a[48:51], v5, %8 sc1\n\t" "global_load_dwordx4 a[52:55], v5, %8 offset:1024 sc1\n\t" "s_nop 14\n\t" "global_load_dwordx4 a[8:11], %12, %7 offset:-2048 sc1\n\t"
                         "global_load_dwordx4 a[12:15], %12, %7 offset:-1024 sc1\n\t" "global_load_dwordx4 a[24:27], %12, %7 offset:2048 sc1\n\t" "global_load_dwordx4 a[28:31], %12, %7 offset:3072 sc1\n\t"
                         "global_load_dwordx4 a[40:43], %12, %8 offset:-2048 sc1\n\t" "global_load_dwordx4 a[44:47], %12, %8 offset:-1024 sc1\n\t" "global_load_dwordx4 a[56:59], %12, %8 offset:2048 sc1\n\t"
                         "global_load_dwordx4 a[60:63], %12, %8 offset:3072 sc1\n\t" "s_nop 15\n\t" "s_nop 15\n\t" "s_nop 14\n\t" "global_load_dwordx4 a[80:83], %14, %6 sc1\n\t"
                         "global_load_dwordx4 a[84:87], %14, %6 offset:1024 sc1\n\t" "global_load_dwordx4 a[88:91], %14, %6 offset:2048 sc1\n\t" "global_load_dwordx4 a[92:95], %14, %6 offset:3072 sc1\n\t"
                         "s_mov_b64 exec, %9\n\t" "global_load_dwordx4 a[64:67], v6, %6 offset:-2048 sc1\n\t" "global_load_dwordx4 a[68:71], v6, %6 offset:-1536 sc1\n\t"
                         "global_load_dwordx4 a[72:75], v6, %6 offset:-1024 sc1\n\t" "global_load_dwordx4 a[76:79], v6, %6 offset:-512 sc1\n\t" "s_mov_b64 exec, -1\n\t" "s_mov_b32 s42, %11\n\t" ".Lsp_v5:\n\t"
                         "s_sub_u32 s42, s42, 1\n\t" "s_cmp_gt_i32 s42, 0\n\t" "s_cbranch_scc1 .Lsp_v5\n\t" "global_load_dwordx4 a[96:99], v4, %3 offset:-512 sc1\n\t" "global_load_dwordx4 a[100:103], %13, %3 sc1\n\t"
                         "global_load_dwordx4 a[104:107], v4, %3 offset:512 sc1\n\t" "global_load_dwordx4 a[108:111], %13, %3 offset:1024 sc1\n\t" "global_load_dwordx4 a[112:115], v4, %3 offset:1536 sc1\n\t"
                         "global_load_dwordx4 a[116:119], %13, %3 offset:2048 sc1\n\t" "global_load_dwordx4 a[120:123], v4, %3 offset:2560 sc1\n\t" "global_load_dwordx4 a[124:127], %13, %3 offset:3072 sc1\n\t"
                         "s_waitcnt vmcnt(31)\n\t" "v_smfmac_f32_16x16x64_bf16 v[48:51], a[0:3], v[112:119], v9\n\t" "s_waitcnt vmcnt(30)\n\t" "v_smfmac_f32_16x16x64_bf16 v[52:55], a[4:7], v[120:127], v9\n\t"
                         "s_waitcnt vmcnt(29)\n\t" "v_smfmac_f32_16x16x64_bf16 v[64:67], a[16:19], v[144:151], v9\n\t" "s_waitcnt vmcnt(28)\n\t" "v_cvt_pk_bf16_f32 v16, v48, v49\n\t" "v_cvt_pk_bf16_f32 v17, v50, v51\n\t"
                         "v_smfmac_f32_16x16x64_bf16 v[68:71], a[20:23], v[152:159], v9\n\t" "s_waitcnt vmcnt(27)\n\t" "v_cvt_pk_bf16_f32 v18, v52, v53\n\t" "v_cvt_pk_bf16_f32 v19, v54, v55\n\t"
                         "v_smfmac_f32_16x16x64_bf16 v[80:83], a[32:35], v[176:183], v9\n\t" "s_waitcnt vmcnt(26)\n\t" "v_cvt_pk_bf16_f32 v24, v64, v65\n\t" "v_cvt_pk_bf16_f32 v25, v66, v67\n\t"
                         "v_smfmac_f32_16x16x64_bf16 v[84:87], a[36:39], v[184:191], v9\n\t" "s_waitcnt vmcnt(25)\n\t" "v_cvt_pk_bf16_f32 v26, v68, v69\n\t" "v_cvt_pk_bf16_f32 v27, v70, v71\n\t"
                         "v_smfmac_f32_16x16x64_bf16 v[96:99], a[48:51], v[208:215], v9\n\t" "s_waitcnt vmcnt(24)\n\t" "v_cvt_pk_bf16_f32 v32, v80, v81\n\t" "v_cvt_pk_bf16_f32 v33, v82, v83\n\t"
                         "v_smfmac_f32_16x16x64_bf16 v[100:103], a[52:55], v[216:223], v9\n\t" "s_waitcnt vmcnt(23)\n\t" "v_cvt_pk_bf16_f32 v34, v84, v85\n\t" "v_cvt_pk_bf16_f32 v35, v86, v87\n\t"
                         "v_smfmac_f32_16x16x64_bf16 v[56:59], a[8:11], v[128:135], v9\n\t" "s_waitcnt vmcnt(22)\n\t" "v_cvt_pk_bf16_f32 v40, v96, v97\n\t" "v_cvt_pk_bf16_f32 v41, v98, v99\n\t"
                         "v_smfmac_f32_16x16x64_bf16 v[60:63], a[12:15], v[136:143], v9\n\t" "s_waitcnt vmcnt(21)\n\t" "v_cvt_pk_bf16_f32 v42, v100, v101\n\t" "v_cvt_pk_bf16_f32 v43, v102, v103\n\t"
                         "v_smfmac_f32_16x16x64_bf16 v[72:75], a[24:27], v[160:167], v9\n\t" "s_waitcnt vmcnt(20)\n\t" "v_cvt_pk_bf16_f32 v20, v56, v57\n\t" "v_cvt_pk_bf16_f32 v21, v58, v59\n\t"
                         "v_smfmac_f32_16x16x64_bf16 v[76:79], a[28:31], v[168:175], v9\n\t" "s_waitcnt vmcnt(19)\n\t" "v_cvt_pk_bf16_f32 v22, v60, v61\n\t" "v_cvt_pk_bf16_f32 v23, v62, v63\n\t"
                         "v_smfmac_f32_16x16x64_bf16 v[88:91], a[40:43], v[192:199], v9\n\t" "s_waitcnt vmcnt(18)\n\t" "v_cvt_pk_bf16_f32 v28, v72, v73\n\t" "v_cvt_pk_bf16_f32 v29, v74, v75\n\t"
                         "v_smfmac_f32_16x16x64_bf16 v[92:95], a[44:47], v[200:207], v9\n\t" "s_waitcnt vmcnt(17)\n\t" "v_cvt_pk_bf16_f32 v30, v76, v77\n\t" "v_cvt_pk_bf16_f32 v31, v78, v79\n\t"
                         "v_smfmac_f32_16x16x64_bf16 v[104:107], a[56:59], v[224:231], v9\n\t" "s_waitcnt vmcnt(16)\n\t" "v_cvt_pk_bf16_f32 v36, v88, v89\n\t" "v_cvt_pk_bf16_f32 v37, v90, v91\n\t"
                         "v_smfmac_f32_16x16x64_bf16 v[108:111], a[60:63], v[232:239], v9\n\t" "s_waitcnt vmcnt(11)\n\t" "v_cvt_pk_bf16_f32 v38, v92, v93\n\t" "v_cvt_pk_bf16_f32 v39, v94, v95\n\t"
                         "v_smfmac_f32_16x16x64_bf16 v[0:3], a[64:67], v[16:23], v8\n\t" "s_waitcnt vmcnt(10)\n\t" "v_cvt_pk_bf16_f32 v44, v104, v105\n\t" "v_cvt_pk_bf16_f32 v45, v106, v107\n\t"
                         "v_smfmac_f32_16x16x64_bf16 v[0:3], a[68:71], v[24:31], v8\n\t" "s_waitcnt vmcnt(9)\n\t" "v_cvt_pk_bf16_f32 v46, v108, v109\n\t" "v_cvt_pk_bf16_f32 v47, v110, v111\n\t"
                         "v_smfmac_f32_16x16x64_bf16 v[0:3], a[72:75], v[32:39], v8\n\t" "s_waitcnt vmcnt(8)\n\t" "v_smfmac_f32_16x16x64_bf16 v[0:3], a[76:79], v[40:47], v8\n\t" "s_waitcnt vmcnt(6)\n\t"
                         "v_smfmac_f32_16x16x64_bf16 v[0:3], a[80:83], a[96:103], v8\n\t" "s_waitcnt vmcnt(4)\n\t" "v_smfmac_f32_16x16x64_bf16 v[0:3], a[84:87], a[104:111], v8\n\t" "s_waitcnt vmcnt(2)\n\t"
                         "v_smfmac_f32_16x16x64_bf16 v[0:3], a[88:91], a[112:119], v8\n\t" "s_waitcnt vmcnt(0)\n\t" "v_smfmac_f32_16x16x64_bf16 v[0:3], a[92:95], a[120:127], v8\n\t" "s_branch .Lv_end\n\t" ".Lsp_v0:\n\t"
                         "s_memtime vcc\n\t" "s_waitcnt lgkmcnt(0)\n\t" "s_sub_i32 s42, %10, vcc_lo\n\t" "s_cmp_gt_i32 s42, 80\n\t" "s_cbranch_scc1 .Lsp_v0\n\t" "s_max_i32 s42, s42, 0\n\t" "s_sub_i32 s42, 92, s42\n\t"
                         "s_and_b32 s42, s42, -4\n\t" "s_getpc_b64 vcc\n\t" "s_add_u32 vcc_lo, vcc_lo, s42\n\t" "s_addc_u32 vcc_hi, vcc_hi, 0\n\t" "s_setpc_b64 vcc\n\t" "s_nop 0\n\t" "s_nop 0\n\t" "s_nop 0\n\t" "s_nop 0\n\t"
                         "s_nop 0\n\t" "s_nop 0\n\t" "s_nop 0\n\t" "s_nop 0\n\t" "s_nop 0\n\t" "s_nop 0\n\t" "s_nop 0\n\t" "s_nop 0\n\t" "s_nop 0\n\t" "s_nop 0\n\t" "s_nop 0\n\t" "s_nop 0\n\t" "s_nop 0\n\t" "s_nop 0\n\t"
                         "s_nop 0\n\t" "s_nop 0\n\t" "global_load_dwordx4 a[96:99], v4, %3 offset:-512 sc1\n\t" "global_load_dwordx4 a[104:107], v4, %3 offset:512 sc1\n\t"
                         "global_load_dwordx4 a[112:115], v4, %3 offset:1536 sc1\n\t" "global_load_dwordx4 a[120:123], v4, %3 offset:2560 sc1\n\t" "global_load_dwordx4 a[100:103], %13, %3 sc1\n\t"
                         "global_load_dwordx4 a[108:111], %13, %3 offset:1024 sc1\n\t" "global_load_dwordx4 a[116:119], %13, %3 offset:2048 sc1\n\t" "global_load_dwordx4 a[124:127], %13, %3 offset:3072 sc1\n\t" "s_nop 9\n\t"
                         "global_load_dwordx4 a[0:3], v5, %7 offset:-4096 sc1\n\t" "global_load_dwordx4 a[4:7], v5, %7 offset:-3072 sc1\n\t" "global_load_dwordx4 a[16:19], v5, %7 sc1\n\t"
                         "global_load_dwordx4 a[20:23], v5, %7 offset:1024 sc1\n\t" "global_load_dwordx4 a[32:35], v5, %8 offset:-4096 sc1\n\t" "global_load_dwordx4 a[36:39], v5, %8 offset:-3072 sc1\n\t"
                         "global_load_dwordx4 a[48:51], v5, %8 sc1\n\t" "global_load_dwordx4 a[52:55], v5, %8 offset:1024 sc1\n\t" "s_nop 14\n\t" "global_load_dwordx4 a[8:11], %12, %7 offset:-2048 sc1\n\t"
                         "global_load_dwordx4 a[12:15], %12, %7 offset:-1024 sc1\n\t" "global_load_dwordx4 a[24:27], %12, %7 offset:2048 sc1\n\t" "global_load_dwordx4 a[28:31], %12, %7 offset:3072 sc1\n\t"
                         "global_load_dwordx4 a[40:43], %12, %8 offset:-2048 sc1\n\t" "global_load_dwordx4 a[44:47], %12, %8 offset:-1024 sc1\n\t" "global_load_dwordx4 a[56:59], %12, %8 offset:2048 sc1\n\t"
                         "global_load_dwordx4 a[60:63], %12, %8 offset:3072 sc1\n\t" "s_nop 15\n\t" "s_nop 15\n\t" "s_nop 14\n\t" "global_load_dwordx4 a[80:83], %14, %6 sc1\n\t"
                         "global_load_dwordx4 a[84:87], %14, %6 offset:1024 sc1\n\t" "global_load_dwordx4 a[88:91], %14, %6 offset:2048 sc1\n\t" "global_load_dwordx4 a[92:95], %14, %6 offset:3072 sc1\n\t"
                         "s_mov_b64 exec, %9\n\t" "global_load_dwordx4 a[64:67], v6, %6 offset:-2048 sc1\n\t" "global_load_dwordx4 a[68:71], v6, %6 offset:-1536 sc1\n\t"
                         "global_load_dwordx4 a[72:75], v6, %6 offset:-1024 sc1\n\t" "global_load_dwordx4 a[76:79], v6, %6 offset:-512 sc1\n\t" "s_mov_b64 exec, -1\n\t" "s_waitcnt vmcnt(23)\n\t"
                         "v_smfmac_f32_16x16x64_bf16 v[48:51], a[0:3], v[112:119], v9\n\t" "s_waitcnt vmcnt(22)\n\t" "v_smfmac_f32_16x16x64_bf16 v[52:55], a[4:7], v[120:127], v9\n\t" "s_waitcnt vmcnt(21)\n\t"
                         "v_smfmac_f32_16x16x64_bf16 v[64:67], a[16:19], v[144:151], v9\n\t" "s_waitcnt vmcnt(20)\n\t" "v_cvt_pk_bf16_f32 v16, v48, v49\n\t" "v_cvt_pk_bf16_f32 v17, v50, v51\n\t"
                         "v_smfmac_f32_16x16x64_bf16 v[68:71], a[20:23], v[152:159], v9\n\t" "s_waitcnt vmcnt(19)\n\t" "v_cvt_pk_bf16_f32 v18, v52, v53\n\t" "v_cvt_pk_bf16_f32 v19, v54, v55\n\t"
                         "v_smfmac_f32_16x16x64_bf16 v[80:83], a[32:35], v[176:183], v9\n\t" "s_waitcnt vmcnt(18)\n\t" "v_cvt_pk_bf16_f32 v24, v64, v65\n\t" "v_cvt_pk_bf16_f32 v25, v66, v67\n\t"
                         "v_smfmac_f32_16x16x64_bf16 v[84:87], a[36:39], v[184:191], v9\n\t" "s_waitcnt vmcnt(17)\n\t" "v_cvt_pk_bf16_f32 v26, v68, v69\n\t" "v_cvt_pk_bf16_f32 v27, v70, v71\n\t"
                         "v_smfmac_f32_16x16x64_bf16 v[96:99], a[48:51], v[208:215], v9\n\t" "s_waitcnt vmcnt(16)\n\t" "v_cvt_pk_bf16_f32 v32, v80, v81\n\t" "v_cvt_pk_bf16_f32 v33, v82, v83\n\t"
                         "v_smfmac_f32_16x16x64_bf16 v[100:103], a[52:55], v[216:223], v9\n\t" "s_waitcnt vmcnt(15)\n\t" "v_cvt_pk_bf16_f32 v34, v84, v85\n\t" "v_cvt_pk_bf16_f32 v35, v86, v87\n\t"
                         "v_smfmac_f32_16x16x64_bf16 v[56:59], a[8:11], v[128:135], v9\n\t" "s_waitcnt vmcnt(14)\n\t" "v_cvt_pk_bf16_f32 v40, v96, v97\n\t" "v_cvt_pk_bf16_f32 v41, v98, v99\n\t"
                         "v_smfmac_f32_16x16x64_bf16 v[60:63], a[12:15], v[136:143], v9\n\t" "s_waitcnt vmcnt(13)\n\t" "v_cvt_pk_bf16_f32 v42, v100, v101\n\t" "v_cvt_pk_bf16_f32 v43, v102, v103\n\t"
                         "v_smfmac_f32_16x16x64_bf16 v[72:75], a[24:27], v[160:167], v9\n\t" "s_waitcnt vmcnt(12)\n\t" "v_cvt_pk_bf16_f32 v20, v56, v57\n\t" "v_cvt_pk_bf16_f32 v21, v58, v59\n\t"
                         "v_smfmac_f32_16x16x64_bf16 v[76:79], a[28:31], v[168:175], v9\n\t" "s_waitcnt vmcnt(11)\n\t" "v_cvt_pk_bf16_f32 v22, v60, v61\n\t" "v_cvt_pk_bf16_f32 v23, v62, v63\n\t"
                         "v_smfmac_f32_16x16x64_bf16 v[88:91], a[40:43], v[192:199], v9\n\t" "s_waitcnt vmcnt(10)\n\t" "v_cvt_pk_bf16_f32 v28, v72, v73\n\t" "v_cvt_pk_bf16_f32 v29, v74, v75\n\t"
                         "v_smfmac_f32_16x16x64_bf16 v[92:95], a[44:47], v[200:207], v9\n\t" "s_waitcnt vmcnt(9)\n\t" "v_cvt_pk_bf16_f32 v30, v76, v77\n\t" "v_cvt_pk_bf16_f32 v31, v78, v79\n\t"
                         "v_smfmac_f32_16x16x64_bf16 v[104:107], a[56:59], v[224:231], v9\n\t" "s_waitcnt vmcnt(8)\n\t" "v_cvt_pk_bf16_f32 v36, v88, v89\n\t"
                         "v_smfmac_f32_16x16x64_bf16 v[108:111], a[60:63], v[232:239], v9\n\t" "s_waitcnt vmcnt(7)\n\t" "v_cvt_pk_bf16_f32 v37, v90, v91\n\t"
                         "v_smfmac_f32_16x16x64_bf16 v[0:3], a[80:83], a[96:103], v8\n\t" "s_waitcnt vmcnt(6)\n\t" "v_cvt_pk_bf16_f32 v38, v92, v93\n\t" "v_smfmac_f32_16x16x64_bf16 v[0:3], a[84:87], a[104:111], v8\n\t"
                         "s_waitcnt vmcnt(5)\n\t" "v_cvt_pk_bf16_f32 v39, v94, v95\n\t" "v_smfmac_f32_16x16x64_bf16 v[0:3], a[88:91], a[112:119], v8\n\t" "s_waitcnt vmcnt(4)\n\t" "v_cvt_pk_bf16_f32 v44, v104, v105\n\t"
                         "v_smfmac_f32_16x16x64_bf16 v[0:3], a[92:95], a[120:127], v8\n\t" "s_waitcnt vmcnt(3)\n\t" "v_cvt_pk_bf16_f32 v45, v106, v107\n\t" "v_smfmac_f32_16x16x64_bf16 v[0:3], a[64:67], v[16:23], v8\n\t"
                         "s_waitcnt vmcnt(2)\n\t" "v_cvt_pk_bf16_f32 v46, v108, v109\n\t" "v_smfmac_f32_16x16x64_bf16 v[0:3], a[68:71], v[24:31], v8\n\t" "s_waitcnt vmcnt(1)\n\t" "v_cvt_pk_bf16_f32 v47, v110, v111\n\t"
                         "v_smfmac_f32_16x16x64_bf16 v[0:3], a[72:75], v[32:39], v8\n\t" "s_waitcnt vmcnt(0)\n\t" "v_smfmac_f32_16x16x64_bf16 v[0:3], a[76:79], v[40:47], v8\n\t" ".Lv_end:\n\t" "s_nop 7\n\t"
                         "v_pk_add_f32 v[0:1], v[0:1], v[2:3]\n\t" "v_cvt_pk_bf16_f32 v2, v0, v1\n\t" "v_cmp_u_f32 vcc, v0, v1\n\t" "s_cmp_eq_u64 vcc, 0\n\t"
                         "v_mov_b32_dpp v3, v2 quad_perm:[1,0,3,2] row_mask:0xf bank_mask:0xf\n\t" "s_cselect_b64 %0, -1, 0\n\t" "v_perm_b32 v0, v2, v3, v11\n\t" "s_mov_b64 exec, %0\n\t"
                         "global_store_dword v10, v0, %5 offset:-4096 sc1\n\t" "global_store_dword v10, v0, %4 offset:-4096 sc1\n\t" "s_memrealtime %1\n\t" "s_mov_b64 exec, -1\n\t" "s_waitcnt lgkmcnt(0)\n\t"
                         : "=&s"(ok), "=&s"(tp8)
                         : "{v[4:11]}"(ad), "s"(up), "s"(mnx), "s"((u64)mnx ^ 8192), "s"(memp + 16384), "s"(memp + 32768 - 2048), "s"(memp + 49152 - 2048), "s"(0xc30000c3c30000c3ull),
                           "s"(uni((int)(tgt - (u32)(v < 16 ? cfg.dU - (v >> 3) * cfg.s2 : cfg.cR2)))), "s"(v < 16 ? 0 : (v < 24 ? 1 : cfg.T3)), "v"(pob), "v"(uob), "v"(pmo), "s"(tries), VALL : "memory", "vcc", "scc", "s42");
            if (__builtin_expect(ok != 0, 1) || s < 0) break;
            if (all || --budget < 0) {
                asm volatile("v_min_u32 v0, -2, v0\n\tglobal_store_dword v10, v0, %0 offset:-4096 sc1\n\tglobal_store_dword v10, v0, %1 offset:-4096 sc1"
                             : : "s"(mnx), "s"((u64)mnx ^ 8192), "{v[4:11]}"(ad), VALL : "memory");
                break;
            }
            if ((++tries & 15) == 0) {
                u32 m = 0;
#pragma nounroll
                for (int i = 0; i < 32; ++i) {
                    u32x4 t;
                    gld16c<0>(t, i < 24 ? memp + 12288 : up, i < 20 ? (4 + i + ((4 + i) & 24)) * 1024 + ad[3] : (i < 24 ? i * 512 - 8192 + ad[2] : (i & 7) * 512 - 512 + (i & 1 ? uob : ad[0]))); waitv<0>(t);
                    m = max3u(max3u(m, t[0], t[1]), t[2], t[3]);
                }
                all = __builtin_amdgcn_ballot_w64(m == 0xffffffffu) == 0;
            }
            phi += (u32)cfg.G;
            zero();
        }
        asm volatile("s_barrier\n\t" "ds_read_b128 v[112:115], %0\n\t" "ds_read_b128 v[116:119], %0 offset:1024\n\t" "ds_read_b128 v[120:123], %0 offset:2048\n\t" "ds_read_b128 v[124:127], %0 offset:3072\n\t"
                     "ds_read_b128 v[128:131], %0 offset:4096\n\t" "ds_read_b128 v[132:135], %0 offset:5120\n\t" "ds_read_b128 v[136:139], %0 offset:6144\n\t" "ds_read_b128 v[140:143], %0 offset:7168\n\t"
                     "ds_read_b128 v[144:147], %0 offset:8192\n\t" "ds_read_b128 v[148:151], %0 offset:9216\n\t" "ds_read_b128 v[152:155], %0 offset:10240\n\t" "ds_read_b128 v[156:159], %0 offset:11264\n\t"
                     "ds_read_b128 v[160:163], %0 offset:12288\n\t" "ds_read_b128 v[164:167], %0 offset:13312\n\t" "ds_read_b128 v[168:171], %0 offset:14336\n\t" "ds_read_b128 v[172:175], %0 offset:15360\n\t"
                     "ds_read_b128 v[176:179], %0 offset:16384\n\t" "ds_read_b128 v[180:183], %0 offset:17408\n\t" "ds_read_b128 v[184:187], %0 offset:18432\n\t" "ds_read_b128 v[188:191], %0 offset:19456\n\t"
                     "ds_read_b128 v[192:195], %0 offset:20480\n\t" "ds_read_b128 v[196:199], %0 offset:21504\n\t" "ds_read_b128 v[200:203], %0 offset:22528\n\t" "ds_read_b128 v[204:207], %0 offset:23552\n\t"
                     "ds_read_b128 v[208:211], %0 offset:24576\n\t" "ds_read_b128 v[212:215], %0 offset:25600\n\t" "ds_read_b128 v[216:219], %0 offset:26624\n\t" "ds_read_b128 v[220:223], %0 offset:27648\n\t"
                     "ds_read_b128 v[224:227], %0 offset:28672\n\t" "ds_read_b128 v[228:231], %0 offset:29696\n\t" "ds_read_b128 v[232:235], %0 offset:30720\n\t" "ds_read_b128 v[236:239], %0 offset:31744\n\t"
                     : : "v"(lds + ad[3] + ((s + 1) & 1) * 32768), VALL : "memory");
        if (s >= 0) {
            follow(phi, (int)(tt & 0xffffu) + 5 * (v >> 2) / 2 - (cfg.dV >> 16 & 255), (u32)tp8, cfg.G);
            if (vb == 0 && lane < 8) gst8(fl, (F_CUR + lane) * 128, u32x2{(u32)s - 1, phi + (tt & 0xffffu)});
        }
        phi += tt & 0xffffu;
        memp = mnx; up = ub + uslot(sd, s + 1);
        zero();
        tt = sched_ld0(s + 1);
        if (s < 0) { phi = go_wait(fl, vb) - (tt >> 16); edge(clk.r1, clk.m1); }
        clk.calib();
        while (s >= 0) {
            u32 h0, h1;
            spin_to(clk.at(phi + (u32)(cfg.dV >> 8 & 255)));
            asm volatile("global_load_dword %0, %1, %2 offset:-4096 sc1" : "=&v"(h1) : "v"(ho & 0xfff), "s"(memp) : "memory");
            spin_to(clk.at(phi + (u32)(cfg.dV & 255)));
            asm volatile("global_load_dword %0, %1, %2 offset:-4096 sc1\n\ts_sleep 5" : "=&v"(h0) : "v"(ho), "s"(memp) : "memory");
            edge(clk.r1, clk.m1);
            asm volatile("s_waitcnt vmcnt(0)" : "+v"(h0), "+v"(h1) : : "memory");
            h0 = max(h0, h1);
            if (__builtin_amdgcn_ballot_w64(h0 == 0xffffffffu) == 0 || --budget < 0) break;
            phi += (u32)cfg.G;
        }
        asm volatile(".Lkm_lv:");
    }
}

DI void xcopy_task(const Ptrs& p, int j, int w) {
    const int tid = 64 * w + lanex();
    Sync y;
    sync_ld(y, p.ws);
    u32x4 xv[10];
    sfor<0, 10>([&](auto I) { gld16<I.v * 128>(xv[I.v], p.x + (size_t)j * 40960, (u32)((tid & 31) * 1280 + (tid >> 5) * 16)); });
    sfor<0, 10>([&](auto I) { waitv<9 - I.v>(xv[I.v]); });
    asm volatile("" : "+v"(y.r), "+v"(y.g), "+v"(y.e));
    sfor<0, 10>([&](auto I) { gst16(p.ws + OFF_XF + (size_t)j * 40960 + I.v * 4096, (u32)tid * 16, xv[I.v]); });
    waitvm<0>();
    barrier();
    const u64 t = sync_tag(y.e);
    if (tid == 0) gst8(p.ws + OFF_LAT, 1536 + (u32)j * 8, u32x2{(u32)t, (u32)(t >> 32)});
}
DI void xcopy_wait(const char* ws, u64 t, int lo = 0, int msk = 63) {
#pragma nounroll
    for (int it = 0; it < (1 << 16); ++it) {
        u32x2 r;
        asm volatile("s_nop 4\n\tglobal_load_dwordx2 %0, %1, %2 offset:1536 sc1\n\ts_waitcnt vmcnt(0)" : "=&v"(r) : "v"((lo + (lanex() & msk)) * 8), "s"(ws + OFF_LAT) : "memory");
        if (__builtin_amdgcn_ballot_w64(r[0] != (u32)t || r[1] != (u32)(t >> 32)) == 0) break;
        __builtin_amdgcn_s_sleep(XSL);
    }
}
DI void init_task(const Ptrs& p, int j, u32 lds, int w) {
    const int lane = lanex(), tid = 64 * w + lane;
    Sync y;
    sync_ld(y, p.ws);
    const u32x4 ff = u32x4{~0u, ~0u, ~0u, ~0u};
    const int i = j * 256 + tid, r = j < 8 ? j & 1 : min(j / 2 - 2, NREG - 1);
    char* const vb = p.ws + (r < 2 ? 0 : OFF_U - (4u << 20)) + ((size_t)r << 21) + 1984 * 1024 + (j < 8 ? j & 6 : (j & 1) * 2 + (j >> 1 == 15) * 4) * 8192;
    const u32 vo = (u32)((w >> 1) * 8192 + (w & 1) * 1024);
    gst16(vb, vo + lane * 16, ff);
    if (i < F_N * 8) gst16(p.ws + OFF_ST, (u32)i * 16, u32x4{i == F_ONE * 8 ? 1000u : ~0u, ~0u, ~0u, ~0u});
    if (i < 512) gst16(p.ws + OFF_GATE, (u32)i * 16, ff);
    gst16(p.ws + 65536, (u32)i * 16, ff);
    if (i < 4096) {
        const int k = (i & 511) + ((i >> 1) & 0x600);
        u32x4 m;
        gld16<0>(m, p.mem0, (u32)((2 * ((k >> 2) & 3) + (k & 1)) * 1280 + min(2 * (k >> 4) + ((k >> 1) & 1), 79) * 16));
        waitv<0>(m);
        gst16(p.ws, (u32)i * 16, k < 640 ? u32x4{min(m[0], 0xfffffffeu), min(m[1], 0xfffffffeu), min(m[2], 0xfffffffeu), min(m[3], 0xfffffffeu)} : ff);
    }
#pragma unroll
    for (int c = 0; c < 6; ++c) gst16(p.ws + OFF_U + c * 131072 - (c == 5) * 122880, (u32)i * 16, ff);
    waitvm<0>();
    asm volatile("" : "+v"(y.r), "+v"(y.g), "+v"(y.e));
    barrier();
    if (w) return;
    u32 dt[2] = {0xffffffu, 0xffffffu};
#pragma unroll
    for (int c = 0; c < 2 * NSAMP; ++c) {
        u32 v;
        const u32 t0 = mt();
        asm volatile("global_load_dword %0, %1, %2 sc1\n\ts_waitcnt vmcnt(0)" : "=&v"(v) : "v"((c & 1) * 8192 + (c >> 1) * 256), "s"(vb) : "memory");
        dt[c & 1] = min(dt[c & 1], mt() - t0);
    }
    const u64 t = sync_tag(y.e);
    const u32 vote = (u32)((dt[0] > dt[1]) ^ xhalf() ^ (int)((vb - p.ws) >> 21)) & 1u;
    if (tid == 0) {
        if (j == 0) gst8(p.ws + OFF_LAT, 512, u32x2{(u32)t, rt()});
        gst8(p.ws + OFF_LAT, (u32)j * 8, u32x2{(u32)t | vote, (u32)(t >> 32)});
    }
}

DI float xmax(float v) {
    const auto a = __builtin_amdgcn_permlane32_swap(__float_as_uint(v), __float_as_uint(v), false, false);
    v = fmaxf(__uint_as_float(a[0]), __uint_as_float(a[1]));
    const auto b = __builtin_amdgcn_permlane16_swap(__float_as_uint(v), __float_as_uint(v), false, false);
    return fmaxf(__uint_as_float(b[0]), __uint_as_float(b[1]));
}
DI float xsum(float v) {
    const auto a = __builtin_amdgcn_permlane32_swap(__float_as_uint(v), __float_as_uint(v), false, false);
    v = __uint_as_float(a[0]) + __uint_as_float(a[1]);
    const auto b = __builtin_amdgcn_permlane16_swap(__float_as_uint(v), __float_as_uint(v), false, false);
    return __uint_as_float(b[0]) + __uint_as_float(b[1]);
}
DI void gst16c(char* sbase, u32 voff, u32x4 v) {
    asm volatile("global_store_dwordx4 %0, %1, %2 sc1\n\ts_nop 1" : : "v"(voff), "v"(v), "s"(sbase) : "memory");
}
#define WAIT6(N, r, o) asm volatile("s_waitcnt vmcnt(" #N ")" : "+v"(r[o]), "+v"(r[o + 1]), "+v"(r[o + 2]), "+v"(r[o + 3]), "+v"(r[o + 4]), "+v"(r[o + 5]) : : "memory")
#define WSLEEP 90
DI void wg_wait(const char* fl, int w, int i0, int t0, int i1, int t1, int i2, int t2, int i3, int t3, int dt, bool acq) {
    if (w == 0) {
        const int lane = lanex(), tg = lane == 1 ? t1 : (lane == 2 ? t2 : (lane == 3 ? t3 : t0));
        const int ix = tg <= 0 ? i0 : (lane == 1 ? i1 : (lane == 2 ? i2 : (lane == 3 ? i3 : i0)));
#pragma nounroll
        for (int it = 0; it < (1 << 14); ++it) {
            u32 v;
            flag_ld(v, fl, ix);
            asm volatile("s_waitcnt vmcnt(0)" : "+v"(v) : : "memory");
            if (__builtin_amdgcn_ballot_w64((int)(v + 1u) < tg) == 0) break;
            const u32 wake = rt() + (u32)(dt + 180 * min(max(t0 - 2 - uni((int)v), 0), 64));
            while ((int)(rt() - wake) < 0) __builtin_amdgcn_s_sleep(4);
        }
        if (acq) acquire();
    }
    barrier();
}
#define TEST 172
DI u32 step_wait(const char* fl, int w, int gr, int t, u32 lds) {
    if (w == 0) {
        u32x2 v;
#pragma nounroll
        for (int it = 0; it < (1 << 14); ++it) {
            asm volatile("s_nop 4\n\tglobal_load_dwordx2 %0, %1, %2 sc1\n\ts_waitcnt vmcnt(0)" : "=&v"(v) : "v"((F_CUR + gr) * 128), "s"(fl) : "memory");
            const int d = t - 1 - uni((int)v[0]);
            if (d <= 0) { v[1] += (u32)(d * TEST); break; }
            const u32 now = rt();
            nap(min(max((int)((u32)uni((int)v[1]) - now) + d * TEST + 10, 40), 2000));
        }
        *LDSP(u32, lds + GRING * GBUF + 4) = v[1];
    }
    barrier();
    return (u32)uni((int)*LDSP(u32, lds + GRING * GBUF + 4));
}
#define PDL 115
#define ODL 240
#define TDT 35
template <bool LATE>
DI void px_wave(const Ptrs& p, const ChainCfg& cfg, int s, int h, int slow, int w, u32 lds, bool keep) {
    const int lane = lanex(), n = lane & 15, g = lane >> 4;
    const char* qb = p.ws + OFF_QX + (size_t)(s * NH + h) * 12288;
    const char* kb = p.ws + OFF_KX + (size_t)(s * NH + h) * 12288;
    const char* mb = p.ws + OFF_KM + (size_t)(s * NH + h) * 3072;
    const u32 mo = (u32)(g * 128 + (n & 7) * 16);
    u32x4 qf[12], kf[12], mf[6];
    sfor<0, 12>([&](auto I) { gld16x<(I.v % 3) * 1024>(qf[I.v], qb + (I.v / 3) * 3072, lane * 16); gap<SMP>(slow); });
    if (!SMP) pace(slow);
    sfor<0, 12>([&](auto I) { if constexpr (LATE || !KNT) gld16c<(I.v % 3) * 1024>(kf[I.v], kb + (I.v / 3) * 3072, lane * 16); else gld16x<(I.v % 3) * 1024>(kf[I.v], kb + (I.v / 3) * 3072, lane * 16); gap<SMP>(slow); });
    if (!SMP) pace(slow);
    if constexpr (LATE) { WAIT6(0, qf, 0); WAIT6(0, qf, 6); WAIT6(0, kf, 0); }
    else {
        sfor<0, 6>([&](auto I) { gld16x<I.v * 512>(mf[I.v], mb, mo); });
        WAIT6(24, qf, 0); WAIT6(18, qf, 6); WAIT6(12, kf, 0);
    }
    f32x4 sc[2][3];
#pragma unroll
    for (int tt = 0; tt < 2; ++tt)
#pragma unroll
        for (int T = 0; T < 3; ++T) sc[tt][T] = f32x4{0.f, 0.f, 0.f, 0.f};
#pragma unroll
    for (int ks = 0; ks < 6; ++ks) { sc[0][0] = mfma16(kf[ks], qf[ks], sc[0][0]); sc[1][0] = mfma16(kf[ks], qf[6 + ks], sc[1][0]); }
    WAIT6(6, kf, 6);
#pragma unroll
    for (int ks = 0; ks < 6; ++ks) sc[1][1] = mfma16(kf[6 + ks], qf[6 + ks], sc[1][1]);
    if constexpr (LATE) {
        until(step_wait(p.ws + OFF_ST, w, h & 7, s - 1, lds) + (u32)(s == NSEG - 1 ? cfg.T : cfg.T + PDX));
        bool ok = false;
#pragma nounroll
        for (int it = 0; it < (1 << 14); ++it) {
            u32 v;
            sfor<0, 6>([&](auto I) { gld16c<I.v * 512>(mf[I.v], mb, mo); });
            flag_ldm(v, p.ws + OFF_ST, (s < NSEG - 2 ? F_CUR : F_PROG + 8 * s) + (h & 7), it > 3);
            WAIT6(0, mf, 0);
            asm volatile("" : "+v"(v));
            u32 m = 0;
#pragma unroll
            for (int i = 0; i < 24; ++i) m = max(m, mf[i >> 2][i & 3]);
            if (ok || __builtin_amdgcn_ballot_w64(m == ~0u) == 0) break;
            ok = it > 3 && uni((int)v) >= (s < NSEG - 2 ? s : 7);
            if (s < NSEG - 1) nap(60);
        }
    }
    WAIT6(0, mf, 0);
#pragma unroll
    for (int ks = 0; ks < 6; ++ks) { sc[0][2] = mfma16(mf[ks], qf[ks], sc[0][2]); sc[1][2] = mfma16(mf[ks], qf[6 + ks], sc[1][2]); }
    const float kscale = 0.07216878364870322f;
    char* pb = p.ws + OFF_PX + (size_t)s * 81920;
#pragma unroll
    for (int tt = 0; tt < 2; ++tt) {
        float v[12], mx = -3.0e38f;
#pragma unroll
        for (int i = 0; i < 12; ++i) {
            const float t = rbf(rbf(sc[tt][i >> 2][i & 3]) * kscale);
            v[i] = (i >= 8 || 4 * g + (i & 3) + 16 * (i >> 2) <= 16 * tt + n) ? t : -3.0e38f;
            mx = fmaxf(mx, v[i]);
        }
        mx = xmax(mx);
        float sx = 0.f, sm = 0.f;
#pragma unroll
        for (int i = 0; i < 12; ++i) {
            v[i] = __builtin_amdgcn_exp2f((v[i] - mx) * 1.4426950408889634f);
            if (i < 8) sx += v[i]; else sm += v[i];
        }
        const float inv = __builtin_amdgcn_rcpf(xsum(sx + 0.5f * sm));
        const u32x4 px = u32x4{cvtpk(v[0] * inv, v[1] * inv), cvtpk(v[2] * inv, v[3] * inv), cvtpk(v[4] * inv, v[5] * inv), cvtpk(v[6] * inv, v[7] * inv)};
        const u32x2 pm = u32x2{cvtpk(v[9] * inv, v[8] * inv), cvtpk(v[11] * inv, v[10] * inv)};
        const int ox = h * 2048 + tt * 1024, om = 65536 + (h >> 2) * 2048 + (h & 3) * 256 + tt * 1024;
        if (!LATE && keep) {
            *LDSP(u32x4, lds + ox + lane * 16) = px;
            *LDSP(u32x2, lds + om + n * 16 + (g & 1) * 8) = pm;
        } else {
            gst16c(pb + ox, lane * 16, px);
            const auto a = __builtin_amdgcn_permlane16_swap(pm[0], pm[0], false, false), b = __builtin_amdgcn_permlane16_swap(pm[1], pm[1], false, false);
            if (g == 0) gst16c(pb + om, (u32)(n * 16), u32x4{a[0], b[0], a[1], b[1]});
        }
    }
}

#define WAIT10(N, r) asm volatile("s_waitcnt vmcnt(" #N ")" : "+v"(r[0]), "+v"(r[1]), "+v"(r[2]), "+v"(r[3]), "+v"(r[4]), "+v"(r[5]), "+v"(r[6]), "+v"(r[7]), "+v"(r[8]), "+v"(r[9]) : : "memory")
DI void out_task(const Ptrs& p, int s, int cg0, int cg1, bool kept, u32 lds, int slow, int w, int sd) {
    u32x4 pf[2][10];
    if (kept) {
        const u32 la = lds + lanex() * 16;
#pragma unroll
        for (int i = 0; i < 20; ++i) pf[i & 1][i >> 1] = ldsr16(la + (i < 16 ? w * 16384 + i * 1024 : 65536 + w * 4096 + (i - 16) * 1024));
    } else {
        const char* pb = p.ws + OFF_PX + (size_t)s * 81920;
        const u32 l16 = lanex() * 16;
        sfor<0, 20>([&](auto I) { gld16c<(I.v & 3) * 1024>(pf[I.v & 1][I.v >> 1], pb + (I.v < 16 ? w * 16384 + (I.v >> 2) * 4096 : 65536 + w * 4096), l16); gap<SMO>(slow); if (!SMO && I.v == 9) pace(slow); });
        WAIT10(0, pf[0]); WAIT10(0, pf[1]);
    }
#pragma nounroll
    for (int cg = cg0; cg < cg1; ++cg) {
    const int lane = lanex(), tid = 64 * w + lane, n = lane & 15, g = lane >> 4;
    const char* db = p.ws + OFF_D + s * D_STRIDE + (size_t)(16 * cg) * 16384 + w * 4096;
    const char* ub = p.ws + uslot(sd, s) + (size_t)(16 * cg) * 4096 + w * 1024;
    const u32 dof = (u32)((n >> 3) * 16384 + g * 128 + (n & 7) * 16), uof = (u32)((n >> 3) * 4096 + g * 128 + (n & 7) * 16);
    u32x4 af[3][10];
    auto aload = [&](u32x4 (&r)[10], int ct) {
        if (!SMO) pace(slow);
        sfor<0, 8>([&](auto I) { gld16x<I.v * 512>(r[I.v], db + ct * 32768, dof); gap<SMO>(slow); });
        gld16x<0>(r[8], ub + uchunk(ct), uof); gap<SMO>(slow); gld16x<512>(r[9], ub + uchunk(ct), uof); gap<SMO>(slow);
    };
    aload(af[0], 0);
    aload(af[1], 1);
    aload(af[2], 2);
    const int tok = 32 * s + (tid >> 3), col = 128 * cg + 8 * (tid & 7);
    u32x4 bb[2]; u32 gv;
    gld16c<0>(bb[0], p.ws + OFF_BOUT, (u32)(s * 40960 + col * 64 + (tid >> 3) * 16)); gld16c<0>(bb[1], p.ws + OFF_BOUT + 4096, (u32)(s * 40960 + col * 64 + (tid >> 3) * 16));
    asm volatile("global_load_dword %0, %1, %2 sc1" : "=v"(gv) : "v"(tok * 4), "s"(p.ws + OFF_GATE) : "memory");
    barrier();
    const u32 wa = lds + (u32)w * 16896 + n * 528 + g * 16;
    sfor<0, 8>([&](auto I) {
        constexpr int ct = I.v;
        u32x4 (&r)[10] = af[ct % 3];
        if constexpr (ct == 0) WAIT10(23, r);
        else if constexpr (ct < 3) WAIT10(23, r);
        else if constexpr (ct < 6) WAIT10(20, r);
        else if constexpr (ct == 6) WAIT10(10, r);
        else WAIT10(0, r);
        f32x4 c0 = f32x4{0.f, 0.f, 0.f, 0.f}, c1 = c0;
#pragma unroll
        for (int i = 0; i < 10; ++i) { c0 = mfma16(r[i], pf[0][i], c0); c1 = mfma16(r[i], pf[1][i], c1); }
        *LDSP(f32x4, wa + ct * 64) = c0;
        *LDSP(f32x4, wa + ct * 64 + 16 * 528) = c1;
        PIN();
        if constexpr (ct + 3 < 8) aload(r, ct + 3);
        PIN();
    });
    asm volatile("" : "+v"(bb[0]), "+v"(bb[1]), "+v"(gv));
    barrier();
    const u32 ra = lds + (u32)(tid >> 3) * 528 + (tid & 7) * 32;
    const float gt = __uint_as_float(gv), g1 = rbf(1.f - gt);
    u32 ov[8];
#pragma unroll
    for (int i = 0; i < 4; ++i) {
        const u32 ri = ra + 16 * (i & 1) + 256 * (i >> 1);
        const f32x4 z = (*LDSP(f32x4, ri) + *LDSP(f32x4, ri + 16896)) + (*LDSP(f32x4, ri + 2 * 16896) + *LDSP(f32x4, ri + 3 * 16896));
#pragma unroll
        for (int j = 0; j < 2; ++j) {
            const u32 b = bb[i >> 1][2 * (i & 1) + j];
            const u32 a = cvtpk(z[2 * j], z[2 * j + 1]);
            const u32 ga = cvtpk(gt * __uint_as_float(a << 16), gt * __uint_as_float(a & 0xffff0000u));
            const u32 gb = cvtpk(g1 * __uint_as_float(b << 16), g1 * __uint_as_float(b & 0xffff0000u));
            ov[2 * i + j] = cvtpk(__uint_as_float(ga << 16) + __uint_as_float(gb << 16), __uint_as_float(ga & 0xffff0000u) + __uint_as_float(gb & 0xffff0000u));
        }
    }
    gst16p(p.out, (u32)(tok * 1280 + col * 2), u32x4{ov[0], ov[1], ov[2], ov[3]});
    gst16p(p.out, (u32)(tok * 1280 + col * 2 + 128), u32x4{ov[4], ov[5], ov[6], ov[7]});
    }
}

DI void out_tail(const Ptrs& p, const ChainCfg& cfg, int s, int cg, u32 lds, int w, int sd, int gr) {
    const int lane = lanex(), tid = 64 * w + lane, n = lane & 15, g = lane >> 4;
    const char* db = p.ws + OFF_D + s * D_STRIDE + (size_t)(4 * cg) * 16384 + w * 4096;
    const char* ub = p.ws + uslot(sd, s) + uchunk(2 * cg) + w * 1024;
    const char* pb = p.ws + OFF_PX + (size_t)s * 81920;
    const u32 dof = (u32)((n >> 3) * 16384 + g * 128 + (n & 7) * 16), uof = (u32)((n >> 3) * 4096 + g * 128 + (n & 7) * 16);
    u32x4 pf[2][10], af[2][10];
    sfor<0, 16>([&](auto I) { gld16c<(I.v & 7) * 512>(af[I.v >> 3][I.v & 7], db + (I.v >> 3) * 32768, dof); });
    const int tok = 32 * s + (tid >> 3), col = 32 * cg + 4 * (tid & 7);
    u32x2 bb; u32 gv;
    asm volatile("global_load_dwordx2 %0, %2, %3 sc1\n\tglobal_load_dword %1, %4, %5 sc1\n\ts_waitcnt vmcnt(0)" : "=&v"(bb), "=&v"(gv)
                 : "v"(s * 40960 + (col >> 3) * 512 + (tid >> 3) * 16 + (col & 7) * 2), "s"(p.ws + OFF_BOUT), "v"(tok * 4), "s"(p.ws + OFF_GATE) : "memory");
    sfor<0, 16>([&](auto I) { waitv<0>(af[I.v >> 3][I.v & 7]); });
    until(step_wait(p.ws + OFF_ST, w, gr, s - 1, lds) + (u32)(s == NSEG - 1 ? cfg.cR : cfg.cR + 2 * PDX));
    const u32 wa = lds + (u32)w * 4608 + n * 144 + g * 16;
#pragma nounroll
    for (int it = 0; it < (1 << 14); ++it) {
        if (it == 4) {
            flag_wait(p.ws + OFF_ST, F_PX + s, 8);
            flag_wait(p.ws + OFF_ST, s < NSEG - 2 ? F_CUR + gr : F_UD + 2 * (s & 1), s < NSEG - 2 ? s + 1 : 16);
            flag_wait(p.ws + OFF_ST, s < NSEG - 2 ? F_ONE : F_UD + 2 * (s & 1) + 1, 16);
        }
        sfor<0, 4>([&](auto I) { gld16c<(I.v & 1) * 512>(af[I.v >> 1][8 + (I.v & 1)], ub + (I.v >> 1) * 16384, uof); });
        sfor<0, 8>([&](auto I) { gld16c<(I.v & 1) * 2048>(pf[0][I.v], pb + w * 16384 + (I.v >> 1) * 4096, lane * 16); });
        gld16c<0>(pf[0][8], pb + 65536 + w * 4096, lane * 16); gld16c<2048>(pf[0][9], pb + 65536 + w * 4096, lane * 16);
        sfor<0, 8>([&](auto I) { gld16c<(I.v & 1) * 2048 + 1024>(pf[1][I.v], pb + w * 16384 + (I.v >> 1) * 4096, lane * 16); });
        gld16c<1024>(pf[1][8], pb + 65536 + w * 4096, lane * 16); gld16c<3072>(pf[1][9], pb + 65536 + w * 4096, lane * 16);
        WAIT10(10, pf[0]); WAIT10(10, af[0]); WAIT10(10, af[1]);
        float chk = 0.f;
#pragma unroll
        for (int tt = 0; tt < 2; ++tt) {
            if (tt) WAIT10(0, pf[1]);
            f32x4 c0 = f32x4{0.f, 0.f, 0.f, 0.f}, c1 = c0;
#pragma unroll
            for (int i = 0; i < 10; ++i) { c0 = mfma16(af[0][i], pf[tt][i], c0); c1 = mfma16(af[1][i], pf[tt][i], c1); }
            *LDSP(f32x4, wa + tt * 2304) = c0;
            *LDSP(f32x4, wa + tt * 2304 + 64) = c1;
            const f32x4 c = c0 + c1;
            chk += (c[0] + c[1]) + (c[2] + c[3]);
            PIN();
        }
        if (it >= 4 || __builtin_amdgcn_ballot_w64((__float_as_uint(chk) & 0x7fffffffu) > 0x7f800000u) == 0) break;
        if (s < NSEG - 1) nap(60);
    }
    barrier();
    const u32 ra = lds + (u32)(tid >> 3) * 144 + (tid & 7) * 16;
    const float gt = __uint_as_float(gv), g1 = rbf(1.f - gt);
    const f32x4 z = (*LDSP(f32x4, ra) + *LDSP(f32x4, ra + 4608)) + (*LDSP(f32x4, ra + 2 * 4608) + *LDSP(f32x4, ra + 3 * 4608));
    u32x2 ov;
#pragma unroll
    for (int j = 0; j < 2; ++j) {
        const u32 b = bb[j], a = cvtpk(z[2 * j], z[2 * j + 1]);
        const u32 ga = cvtpk(gt * __uint_as_float(a << 16), gt * __uint_as_float(a & 0xffff0000u));
        const u32 gb = cvtpk(g1 * __uint_as_float(b << 16), g1 * __uint_as_float(b & 0xffff0000u));
        ov[j] = cvtpk(__uint_as_float(ga << 16) + __uint_as_float(gb << 16), __uint_as_float(ga & 0xffff0000u) + __uint_as_float(gb & 0xffff0000u));
    }
    *(u32x2*)(p.out + tok * 1280 + col * 2) = ov;
}

DI float dot2bf(u32 a, u32 b, float c) { return __builtin_amdgcn_fdot2_f32_bf16(__builtin_bit_cast(bf16x2, a), __builtin_bit_cast(bf16x2, b), c, false); }
DI void battn_task(const Ptrs& p, int t0, u32 lds, int w) {
    const int lane = lanex(), tid = 64 * w + lane, tl = tid & 31, t = t0 + tl;
    const char* const qkv = p.ws + OFF_QKVB + (size_t)(t0 >> 5) * 122880;
    {
        u32 so[3];
#pragma unroll
        for (int i = 0; i < 3; ++i) {
            const int e = 64 * i + lane, sl = e % 48;
            so[i] = (u32)((e / 48) * 512 + (sl < 16 ? (t0 ? 0 : 122880) + 256 + sl * 16 : 122880 - 256 + sl * 16));
        }
#pragma unroll
        for (int i = 0; i < 30; ++i) { glds16c(qkv - 122880 + 40960 + (10 * w + i / 3) * 2048, so[i % 3], lds + (30 * w + i) * 1024); gap<SMB>(-1); }
    }
    u32x4 q[2][5], xv[10], gv[10];
    sfor<0, 10>([&](auto I) { gld16x<(I.v % 5) * 512>(q[I.v / 5][I.v % 5], qkv + (I.v / 5) * 20480, (u32)((tid >> 5) * 2560 + tl * 16)); gap<SMB>(-1); });
    sfor<0, 10>([&](auto I) { gld16<I.v * 128>(xv[I.v], p.x + (size_t)t0 * 1280, (u32)((tid >> 3) * 1280 + (tid & 7) * 16)); gld16<I.v * 128>(gv[I.v], p.gw, (u32)((tid & 7) * 16)); gap<SMB>(-1); });
    sfor<0, 10>([&](auto I) { waitv<0>(q[I.v / 5][I.v % 5]); waitv<0>(xv[I.v]); waitv<0>(gv[I.v]); });
    barrier();
    {
        float a = 0.f;
#pragma unroll
        for (int i = 0; i < 10; ++i)
#pragma unroll
            for (int j = 0; j < 4; ++j) {
                const u32 r = cvtpk(__uint_as_float(xv[i][j] << 16) * __uint_as_float(gv[i][j] << 16), __uint_as_float(xv[i][j] & 0xffff0000u) * __uint_as_float(gv[i][j] & 0xffff0000u));
                a += __uint_as_float(r << 16) + __uint_as_float(r & 0xffff0000u);
            }
        a += __shfl_xor(a, 1); a += __shfl_xor(a, 2); a += __shfl_xor(a, 4);
        const float z = rbf(a);
        if ((tid & 7) == 0) gst4c(p.ws + OFF_GATE, (u32)((t0 + (tid >> 3)) * 4), __float_as_uint(rbf(1.0f / (1.0f + __expf(-z)))));
    }
#pragma unroll
    for (int it = 0; it < 2; ++it) {
        const int m = (tid >> 5) + 8 * it;
        u32 ka = lds + (u32)(m * 3840 + (2 + tl) * 16), va = ka + 61440;
        asm volatile("" : "+v"(ka), "+v"(va));
        u32x4 k[5][8];
#pragma unroll
        for (int i = 0; i < 40; ++i) k[i >> 3][i & 7] = ldsr16(ka + (i >> 3) * 768 + (7 - (i & 7)) * 32);
        PIN();
        float lg[2][8], mx[2] = {-3.0e38f, -3.0e38f};
#pragma unroll
        for (int i = 0; i < 16; ++i) lg[i >> 3][i & 7] = 0.f;
#pragma unroll
        for (int i = 0; i < 5; ++i) {
#pragma unroll
            for (int j = 0; j < 4; ++j)
#pragma unroll
                for (int wv = 0; wv < 8; ++wv) lg[4 * i + j >= 10][wv] = dot2bf(q[it][i][j], k[i][wv][j], lg[4 * i + j >= 10][wv]);
            PIN();
#pragma unroll
            for (int wv = 0; wv < 8; ++wv) k[i][wv] = ldsr16(va + i * 768 + (7 - wv) * 32);
            PIN();
        }
#pragma unroll
        for (int i = 0; i < 16; ++i) {
            float& l = lg[i >> 3][i & 7];
            l = rbf(rbf(l) * 0.22360679774997896f);
            asm volatile("" : "+v"(l));
            l = t - 2 * (i & 7) >= 0 ? l : -3.0e38f;
            mx[i >> 3] = fmaxf(mx[i >> 3], l);
        }
        u32 pk[2][4];
#pragma unroll
        for (int u = 0; u < 2; ++u) {
            float sum = 0.f;
#pragma unroll
            for (int wv = 0; wv < 8; ++wv) { lg[u][wv] = __builtin_amdgcn_exp2f((lg[u][wv] - mx[u]) * 1.4426950408889634f); sum += lg[u][wv]; }
            const float inv = __builtin_amdgcn_rcpf(sum);
#pragma unroll
            for (int wv = 0; wv < 4; ++wv) pk[u][wv] = cvtpk(lg[u][2 * wv] * inv, lg[u][2 * wv + 1] * inv);
        }
#pragma unroll
        for (int i = 0; i < 5; ++i) {
            float o[8];
#pragma unroll
            for (int d = 0; d < 8; ++d) o[d] = 0.f;
#pragma unroll
            for (int wp = 0; wp < 4; ++wp) {
                const u32x4 v0 = k[i][2 * wp], v1 = k[i][2 * wp + 1];
#pragma unroll
                for (int j = 0; j < 4; ++j) {
                    const u32 pw = pk[4 * i + j >= 10][wp];
                    o[2 * j] = dot2bf(__builtin_amdgcn_perm(v1[j], v0[j], 0x05040100u), pw, o[2 * j]);
                    o[2 * j + 1] = dot2bf(__builtin_amdgcn_perm(v1[j], v0[j], 0x07060302u), pw, o[2 * j + 1]);
                }
            }
            gst16c(p.ws + OFF_OB + (size_t)(t0 >> 5) * 40960 + 512 * i, (u32)(m * 2560 + tl * 16), u32x4{cvtpk(o[0], o[1]), cvtpk(o[2], o[3]), cvtpk(o[4], o[5]), cvtpk(o[6], o[7])});
        }
    }
}

#define XPF 1
#define PFQ 0
#define PFN 8
#define OLAG 3
constexpr int QN = 176, QF = 19;
struct QTab { u16 e[QN]; int n, nf; u32 tt[64]; };
typedef const __attribute__((address_space(4))) int* KI;
constexpr int QT_OFF = (88 + 48) / 4;
DI int pick_task(char* fl, KI k, int gr) {
    u32 r;
    asm volatile("s_mov_b64 exec, 1\n\ts_nop 3\n\tglobal_atomic_add %0, %1, %2, %3 sc0 sc1\n\ts_mov_b64 exec, -1\n\ts_waitcnt vmcnt(0)"
                 : "=&v"(r) : "v"((F_QH + gr) * 128), "v"(1), "s"(fl) : "memory");
    const int x = uni((int)r) + 1 + QF;
    if (x >= k[QT_OFF + QN / 2]) return -1;
    const u32 e2 = (u32)k[QT_OFF + (x >> 1)];
    return (int)(x & 1 ? e2 >> 16 : e2 & 0xffffu);
}
__global__ __launch_bounds__(256, 1) void k_main(Ptrs p0, ChainCfg cfg, QTab tab) {
    extern __shared__ __attribute__((aligned(16))) char smem[];
    u32 lds = (u32)(size_t)LDSP(char, smem);
#define HALF 0
    int n = HALF ? (blockIdx.x & 4) * 32 + (int)(blockIdx.x >> 3) * 4 + (blockIdx.x & 3) : blockIdx.x, gr = n & 7;
    int w = uni(threadIdx.x >> 6);
    {
        u64 pc; int len;
        asm volatile("s_getpc_b64 %0\n.Lkm_first:\n\ts_mov_b32 %1, .Lkm_last-.Lkm_first\n\ts_max_i32 %1, %1, .Lkm_lu-.Lkm_first\n\ts_max_i32 %1, %1, .Lkm_lv-.Lkm_first\n\t"
                     "s_max_i32 %1, %1, .Lkm_g2-.Lkm_first\n\ts_max_i32 %1, %1, .Lkm_g4-.Lkm_first" : "=s"(pc), "=s"(len) : : "scc");
        if (w == 3) pfl((const char*)pc, (u32)min((int)(blockIdx.x >> 3) * 8192 + (int)(threadIdx.x & 63) * 128, max(len, 0)), lds);
    }
    karg_warm();
    if (n < NH) { score_role(p0, cfg, n, lds, w); return; }
    if (n < NS) { proj_role(p0, cfg, n - NH, lds, w); return; }
    const bool isv = n < NS + NVB;
#define qslot (lds + GRING * GBUF)
    int sdw = -1;
    int en = isv ? Q_VD + (n & 24) : tab.e[(n - NS - NVB) >> 3] & 511;
    u32 phi0 = 0;
    Sync y;
    if (XPF && n < 160 && w >= 2) pfl(p0.x, (u32)min(((n - NS) >> 3) * 128 + (w & 1) * 64 + lanex(), 1279) * 128, lds);
    if (n >= 160) {
        const int lane = lanex();
        if (n >= 192 && (n & 8)) {
            const int j = (n & 7) + ((n - 192) >> 4) * 8;
            init_task(p0, j, lds, w);
            barrier();
#pragma unroll
            for (int i = 0; i < 3; ++i) pfl(p0.wqkvb, (u32)min(j * 768 + i * 256 + w * 64 + lane, 19199) * 128, lds);
            pfl(j < 25 ? p0.wob : p0.gw, j < 25 ? (u32)(j * 256 + w * 64 + lane) * 128 : (u32)min(lane, 9) * 128, lds);
        } else {
            const int h = n < 192 ? n & 31 : (n & 7) + ((n - 192) >> 4) * 8;
            xcopy_task(p0, h + (n < 192 ? 0 : 32), w);
            if (n < 192) {
#pragma unroll
                for (int i = 0; i < PFN; ++i) {
                    const int l = 64 * min(i * 4 + w, 29) + lane;
                    pfl(p0.wo + (size_t)h * 245760, (u32)(l * 128), lds);
                    if (PFQ) pfl(p0.wq + h * 384, (u32)((l / 3) * 12288 + (l % 3) * 128), lds);
                }
            }
        }
        if (w == 1) {
            sync_ld(y, p0.ws);
            waitvm<0>();
            asm volatile("" : "+v"(y.r), "+v"(y.g), "+v"(y.e));
            if (n < 192) xcopy_wait(p0.ws, sync_tag(y.e));
            else xcopy_wait(p0.ws, sync_tag(y.e), n & 8 ? 4 : 8, n & 8 ? 3 : 7);
        }
        if (w == 0 && n < 192) {
            sync_ld(y, p0.ws);
            waitvm<0>();
            *LDSP(u32, qslot) = (u32)sync_end(y, p0.ws, phi0, false);
        }
        barrier();
        if (w == 0 && n < 192) flag_add(p0.ws + OFF_ST, F_ACK, 1);
        if (n < 192) sdw = uni((int)*LDSP(u32, qslot));
        barrier();
    }
    while (true) {
        typedef char* const __attribute__((address_space(4)))* KP;
        KP kp = (KP)__builtin_amdgcn_kernarg_segment_ptr();
        asm volatile("" : "+s"(kp), "+s"(lds), "+s"(w), "+s"(gr), "+s"(n));
        __builtin_assume(w >= 0 && w < 4 && gr >= 0 && gr < 8);
        const Ptrs p{kp[0], kp[1], kp[2], kp[3], kp[4], kp[5], kp[6], kp[7], kp[8], kp[9], kp[10]};
        const int ty = en & 7, gate = en >> 9;
        const bool fu = ty == Q_PX && (en >> 3 & 63) < 8;
        const int ix = fu ? (en & 504) + gr : en >> 3 & 63, sf = ((KI)kp)[QT_OFF + QN / 2 + 1] & 255;
        char* const fl = p.ws + OFF_ST;
        const bool raw = sdw < 0;
        int t0 = gate - 1, f1 = F_ONE, t1 = 0, f2 = F_ONE, t2 = 0, f3 = F_ONE, t3 = 0, dt = cfg.T2;
        const bool o2 = ty == Q_OUT && ix < 16;
        const int os = o2 ? sf + 8 * (ix >> 2) + gr : (ix >= 40 ? 44 + (8 * ix + gr) / 20 : (8 * ix + gr) / 5);
        const int bb = ty == Q_BO ? (8 * ix + gr) >> 2 : os;
        if (ty == Q_BA) { t0 += gr * BAS >> 3; f1 = F_B + (gr & RM) | ix << 16; t1 = 5; f2 = F_B + (gr & RM) | max(ix - 1, 0) << 16; t2 = 5; }
        if (ty == Q_BO) { f1 = F_B + (gr & RM) | (8 + bb) << 16; t1 = 8; }
        if (ty == Q_PX) { f1 = F_Q + pblk(ix) * 8 + (fu ? gr & RM : gr) | fu << 16; t1 = fu ? 32 : 4; if (fu) t0 += gr * (((KI)kp)[QT_OFF + QN / 2 + 1] >> 8) >> 3; if (ix < PXT) t0 = max(t0, ix + 1); }
        if (ty == Q_OUT) { f3 = F_B + (gr & RM) | (16 + (os >> 3)) << 16; t3 = 4; if (os < 60) { f1 = F_PX + os; t1 = 8; t0 = max(t0, os + (os < PXT ? OLAG : 1)); if (os >= PXT) dt = TDT; } else { f1 = F_VD + 64 + (gr & RM); t1 = 32; } }
        const bool skip = fu ? ix >= sf : ty == Q_OUT && ix < 40 && !o2 && (os < sf + 8 * NO2 || os >= 60);
        if ((gate || ty >= Q_BA) && !skip) wg_wait(fl, w, F_CUR + gr, t0, f1, t1, f2, t2, f3, t3, dt, false);
        int sig = F_END, nrep = 1;
        if (skip) {}
        else if (ty <= Q_QKVB || ty == Q_BO) {
            const bool ab = ty <= Q_KQ;
            sig = ty == Q_VD ? F_VD + (ix >> 2) * 8 : (ty == Q_KQ ? F_Q + (ix >> 2) * 8 : (ix & 4 ? F_Q : F_K + 8 * ix)) + gr;
            if (!ab) sig = F_B | (bb + (ty == Q_BO ? 16 : 0)) << 16;
            nrep = ty == Q_VD || !ab ? RM + 1 : 1;
            if (ty == Q_VD && ix < 4) { sig += n & 32 ? 4 : 0; nrep = 4; }
            run_gemm(p, ty, ab ? ix >> 2 : bb, ab ? gr + 8 * (ix & 3) : 8 * ix + gr - (ty == Q_BO ? 4 : 5) * bb, (n >> 5) & 1, raw && n < 160, y, lds, w);
            if (raw) sdw = sync_end(y, p.ws, phi0, w == 0);
        } else {
            if (ty == Q_BA) {
                battn_task(p, (8 * ix + gr) * 32, lds, w);
                sig = F_B | (8 + ix) << 16; nrep = RM + 1;
            } else if (ty == Q_PX) {
                if (ix < PXT) {
#pragma nounroll
                    for (int i = fu ? 0 : gr; i < (fu ? 8 : gr + 1); ++i) px_wave<false>(p, cfg, ix, i + 8 * w, ix - cfg.fast, w, lds, fu);
                } else px_wave<true>(p, cfg, ix, gr + 8 * w, -1, w, lds, false);
                sig = F_PX + ix;
                if (fu) wg_wait(fl, w, F_ONE, 0, F_B + (gr & RM) | (16 + (ix >> 3)) << 16, 4, F_ONE, 0, F_ONE, 0, dt, false);
            }
            if (ty == Q_OUT || fu) {
                const int cg = 8 * ix + gr - 5 * os;
                if (ix >= 40 && !fu) out_tail(p, cfg, os, 8 * ix + gr - 20 * (os - 44), lds, w, sdw, gr);
                else out_task(p, fu ? ix : os, fu ? 0 : (o2 ? OW * (ix & 3) : cg), fu ? 5 : (o2 ? min(OW * (ix & 3) + OW, 5) : cg + 1), fu, lds, (fu ? ix : os) + 1 - cfg.fast, w, sdw);
                sig = F_END;
            }
            waitvm<0>();
        }
        barrier();
        if (w == 0 && sig != F_END) flag_add(fl, sig, nrep);
        if (w == 1 && sig >= F_Q && sig < F_N) flag_add(fl, sig - gr | 1 << 16, RM + 1);
        if (raw && !isv && w == 0) xcopy_wait(p.ws, sync_tag(y.e));
        if (isv) break;
        if (w == 0) *LDSP(int, qslot) = pick_task(fl, (KI)kp, gr);
        barrier();
        en = uni(*LDSP(int, qslot));
        if (en < 0) return;
        barrier();
    }
    value_role(p0, cfg, n - NS, sdw, phi0, lds, w);
}

#define QCFG 14, 11, 3, 5, 0, 50, 36
#define LVDEF "00?>>=<<;;;<<<<;;::98888999:::::999999:::99888998888877889::;;:6"
constexpr int DX = 340, DUA = 430, DUB = 0, DUC = 4;
static ChainCfg dev_cfg(ChainCfg c) { c.T = PDL; c.cR = ODL; c.s1 = 190 << 8 | 185 << 16; c.T2 = WSLEEP; c.cR2 = DX; c.dU = DUA; c.s2 = DUB; c.T3 = DUC; c.dV |= 4 << 8 | 8 << 16 | 0 << 24; c.cR3 = 4 | 4 << 8 | 0 << 16; return c; }
static QTab set_sched(QTab t, const ChainCfg c, const char* lv) {
    for (int s = 0; s < 64; ++s) {
        const int d = lv[s] - '0';
        t.tt[s] = (u32)((s < c.s1 ? c.T : (s < c.s2 ? c.T2 : c.T3)) + 4 * d) | (u32)((s < c.s1 ? c.cR : (s < c.s2 ? c.cR2 : c.cR3)) + 5 * d / 2) << 16;
    }
    t.tt[0] = (u32)230 | (u32)165 << 16;
    t.tt[1] = (u32)210 | (u32)144 << 16;
    return t;
}
#define QORD "bbV3qbK3s41x40bx51bx60x71s80"
#define GATES "11 13 15 18 25 28 41 43 21 26 36 49 14 18 22 27 33 41"
static QTab make_tab(int lv, int lk, int b0, int d1, int d2, int lg, int nf) {
    QTab t{};
    int n = 0, dl[QN], nq = 0;
    auto add = [&](int ty, int ix) { t.e[n] = (u16)(ty | ix << 3); dl[n++] = ty == Q_VD || ty == Q_KQ ? 8 * (ix / 4 - 1) - (ty == Q_VD ? lv : lk) - (ix >= 32 ? 0 : 0) : 99; };
    for (int i = 1; i < 4; ++i) add(Q_VD, i);
    for (int i = 0; i < 16; ++i) add(i < 4 ? Q_H : ((i & 8) ? Q_VD : Q_KQ), i < 8 ? i : 8 - (i & 1) * 4 + ((i - 8) >> 1));
    for (int i = 0; i < 4; ++i) add(Q_KQ, 8 + i);
    for (const char* c = QORD; *c; ++c) {
        if (*c == 'b') add(Q_QKVB, nq++);
        else if (*c == 'q') for (int i = 4; i < 8; ++i) add(Q_H, i);
        else if (*c == '.') dl[n - 1] |= 1 << 20;
        else if (*c == 'w') { const int b = *++c - '0'; for (int i = 0; i < 16; ++i) add(i & 2 ? Q_KQ : Q_VD, 4 * (b + (i & 1)) + (i >> 2)); }
        else if (*c == 'x') { const int b = c[1] - '0', o = 2 * (c[2] - '0'); c += 2; for (int i = 0; i < 8; ++i) add(i & 2 ? Q_KQ : Q_VD, 4 * (b + (i & 1)) + o + (i >> 2)); }
        else if (*c == 's') { const int b = c[1] - '0', o = 2 * (c[2] - '0'); c += 2; for (int i = 0; i < 4; ++i) add(i & 1 ? Q_KQ : Q_VD, 4 * b + o + (i >> 1)); }
        else if (*c == 'a' || *c == 'c') { add(*c == 'a' ? Q_VD : Q_KQ, 10 * (c[1] - '0') + c[2] - '0'); c += 2; }
        else if (*c == 'n') { const int b = *++c - '0'; for (int i = 0; i < 8; ++i) add(i & 1 ? Q_KQ : Q_VD, 4 * b + (i >> 1) - (i & 1) * 2); }
        else if (*c == 'h' || *c == 'e') { const int b = 4 * (c[1] - '0') + (*c == 'e') * 2; ++c; add(Q_KQ, b); add(Q_KQ, b + 1); }
        else if (*c == 'm') { const int b = c[1] - '0', b2 = c[2] - '0'; c += 2; for (int i = 0; i < 8; ++i) add(i & 1 ? Q_KQ : Q_VD, 4 * (i & 1 ? b2 : b) + ((i >> 1) ^ (i & 2 ? 0 : 0))); }
        else {
            const int ty = (*c | 32) == 'v' ? Q_VD : Q_KQ, two = *c >> 5 & 1, b = *++c - '0';
            for (int i = 0; i < (4 << two); ++i) add(ty, 4 * (b + (i & two)) + (i >> two));
        }
    }
    const int gap = 8;
    for (int i = n - 1, m = 999; i >= 0; --i) { const int pz = dl[i] > 999; m = min(8 * (pz ? dl[i] - (1 << 20) : dl[i]), m - gap - 8 * pz); dl[i] = max(m, 0); }
    auto put = [&](int ty, int ix, int g, int pos) {
        int i = n++;
        for (; dl[i - 1] > pos; --i) { t.e[i] = t.e[i - 1]; dl[i] = dl[i - 1]; }
        t.e[i] = (u16)(ty | ix << 3 | (g + 1) << 9); dl[i] = pos;
    };
    for (int i = 0; i < n; ++i) if (dl[i] >= 8) t.e[i] |= (u16)((dl[i] / 8 + 1) << 9);
    const char* gs = GATES;
    auto gt = [&](int i) { return 10 * (gs[3 * i] - '0') + gs[3 * i + 1] - '0'; };
    for (int i = 0; i < 8; ++i) put(Q_BA, i, gt(i), 8 * gt(i));
    for (int i = 0; i < 4; ++i) put(Q_BO, i, gt(8 + i), 8 * gt(8 + i));
    for (int i = 0; 8 * i < nf; ++i) put(Q_PX, i, gt(12 + i), 8 * (max(gt(12 + i), 8 * i + 1) + b0));
    auto pg = [&](int s) { return max(8 * s + 8, 8 * 46 + (s - nf) * 3); };
    for (int s = nf; s < NSEG; ++s) { const int g = s < PXT ? pg(s) : 8 * max(s - 3, lg); put(Q_PX, s, g / 8, g); }
    for (int k = 0; k < NO2; ++k) for (int c = 0; c * OW < 5; ++c) put(Q_OUT, 4 * k + c, pg(nf + 8 * k + 7) / 8 + OLAG - 1, pg(nf + 8 * k + 7) + 8 * (OLAG - 1));
    for (int x = 5 * (nf + 8 * NO2) / 8; x < 38; ++x) { const int os = (8 * x + 4) / 5, g = pg(min((8 * x + 7) / 5, PXT - 1)); put(Q_OUT, x, g > 8 * ((8 * x + 7) / 5) + 8 ? g / 8 + OLAG - 1 : 0, os < PXT ? pg(os) + 8 * (OLAG - 1) : 8 * (os + 1)); }
    for (int x = 40; x < 50; ++x) { const int g = max(44 + 8 * x / 20 - 3, lg); put(Q_OUT, x, g, 8 * g + 4); }
    t.n = n; t.nf = nf | d1 << 8;
    return t;
}

extern "C" void kernel_launch(void* const* d_in, const int* in_sizes, int n_in, void* d_out, int out_size, void* d_ws,
                              size_t ws_size, hipStream_t stream) {
    Ptrs p{(const char*)d_in[0], (const char*)d_in[1], (const char*)d_in[2], (const char*)d_in[3], (const char*)d_in[4], (const char*)d_in[5],
           (const char*)d_in[8], (const char*)d_in[9], (const char*)d_in[10], (char*)d_out, (char*)(((size_t)d_ws + 0x1fffff) & ~(size_t)0x1fffff)};
    (void)hipFuncSetAttribute((const void*)k_main, hipFuncAttributeMaxDynamicSharedMemorySize, S_LDS);
    k_main<<<256, 256, S_LDS, stream>>>(p, dev_cfg(ChainCfg{CFG}), set_sched(make_tab(QCFG), ChainCfg{CFG}, LVDEF));
    static_assert(sizeof(Ptrs) + sizeof(ChainCfg) + sizeof(QTab) == TT_OFF + 256, "");
}
```

```cpp
#include <hip/hip_runtime.h>
#pragma clang diagnostic ignored "-Wint-to-pointer-cast"

typedef unsigned short u16;
typedef unsigned int u32;
typedef unsigned long long u64;
typedef __bf16 bf16x8 __attribute__((ext_vector_type(8)));
typedef __bf16 bf16x16 __attribute__((ext_vector_type(16)));
typedef short s16x4 __attribute__((ext_vector_type(4)));
typedef float f32x4 __attribute__((ext_vector_type(4)));
typedef unsigned u32x4 __attribute__((ext_vector_type(4)));
typedef unsigned u32x2 __attribute__((ext_vector_type(2)));
typedef unsigned u32x8 __attribute__((ext_vector_type(8)));
typedef unsigned u32x16 __attribute__((ext_vector_type(16)));
typedef unsigned u32x32 __attribute__((ext_vector_type(32)));
#define DI __device__ __forceinline__
#define LDSP(T, a) ((__attribute__((address_space(3))) T*)(a))

constexpr int S_LEN = 2048, DM = 640, NH = 32, DH = 192, NSEG = 64, HD = NH * DH;
#define NSAMP 4
constexpr int NREG = 13;
constexpr int NV = 80, NVB = 40;
constexpr int NS = 64;
#define CFG 147, 106, 104, 20, 57, 16, 147, 106, 52, 147, 106, 6

constexpr size_t U_STRIDE = NV * 4096, D_STRIDE = NV * 16384;
constexpr size_t HO_STRIDE = 65536;
constexpr size_t OFF_ST = NSEG * HO_STRIDE;
enum { F_ACK = 0, F_RDY = 1, F_ONE = 3, F_GO = 8, F_VD = 16, F_QH = 96, F_B = 104, F_CUR = 128, F_PROG = 136 - 8 * 62, F_UD = 152, F_PX = 160, F_K = 224, F_END = 512, F_Q = 520, F_N = 592 };
constexpr size_t OFF_LAT = OFF_ST + F_N * 128;
constexpr size_t OFF_KX = 5u << 20;
constexpr size_t OFF_QX = OFF_KX + (size_t)S_LEN * HD * 2;
constexpr size_t OFF_D = OFF_QX + (size_t)S_LEN * HD * 2;
constexpr size_t OFF_U = (OFF_D + NSEG * D_STRIDE + 0x1fffff) & ~(size_t)0x1fffff;
constexpr size_t OFF_KM = OFF_U + (22u << 20);
constexpr size_t OFF_PX = OFF_KM + (size_t)NSEG * NH * 3072;
constexpr size_t OFF_QKVB = OFF_PX + (size_t)NSEG * 81920;
constexpr size_t OFF_OB = OFF_QKVB + (size_t)S_LEN * 1920 * 2;
constexpr size_t OFF_BOUT = OFF_OB + (size_t)S_LEN * DM * 2;
constexpr size_t OFF_GATE = OFF_BOUT + (size_t)S_LEN * DM * 2;
constexpr size_t OFF_XF = OFF_GATE + S_LEN * 4;
constexpr size_t WS_END = OFF_XF + (size_t)S_LEN * DM * 2;
static_assert(WS_END + (2u << 20) <= (256u << 20), "workspace");

typedef __bf16 bf16x2 __attribute__((ext_vector_type(2)));
typedef float f32x2 __attribute__((ext_vector_type(2)));
DI u32 cvtpk(float lo, float hi) { return __builtin_bit_cast(u32, __builtin_convertvector(f32x2{lo, hi}, bf16x2)); }
DI float rbf(float f) { return __uint_as_float(cvtpk(f, f) << 16); }
DI f32x4 mfma16(u32x4 a, u32x4 b, f32x4 c) {
    return __builtin_amdgcn_mfma_f32_16x16x32_bf16(__builtin_bit_cast(bf16x8, a), __builtin_bit_cast(bf16x8, b), c, 0, 0, 0);
}
DI u32x2 trread(u32 a) {
    return __builtin_bit_cast(u32x2, __builtin_amdgcn_ds_read_tr16_b64_v4i16(LDSP(s16x4, a)));
}
DI void glds16c(const char* sbase, u32 voff, u32 lds) {
    u32 sv;
    asm volatile("s_mov_b32 %0, m0\n\ts_mov_b32 m0, %3\n\ts_nop 0\n\tglobal_load_lds_dwordx4 %1, %2 sc1\n\ts_mov_b32 m0, %0"
                 : "=&s"(sv) : "v"(voff), "s"(sbase), "s"(lds) : "memory");
}
template <int IMM>
DI void gld16(u32x4& r, const char* sbase, u32 voff) {
    asm volatile("s_nop 4\n\tglobal_load_dwordx4 %0, %1, %2 offset:%3" : "=v"(r) : "v"(voff), "s"(sbase), "i"(IMM) : "memory");
}
DI void gst16(char* sbase, u32 voff, u32x4 v) {
    asm volatile("global_store_dwordx4 %0, %1, %2 sc1\n\ts_nop 1" : : "v"(voff), "v"(v), "s"(sbase) : "memory");
}
#define OST " nt"
DI void gst16p(char* sbase, u32 voff, u32x4 v) {
    asm volatile("s_nop 4\n\tglobal_store_dwordx4 %0, %1, %2" OST "\n\ts_nop 1" : : "v"(voff), "v"(v), "s"(sbase) : "memory");
}
DI void gst8(char* sbase, u32 voff, u32x2 v) {
    asm volatile("global_store_dwordx2 %0, %1, %2 sc1\n\ts_nop 1" : : "v"(voff), "v"(v), "s"(sbase) : "memory");
}
template <int N>
DI void waitvm() { asm volatile("s_waitcnt vmcnt(%0)" : : "i"(N) : "memory"); }
DI void barrier() { asm volatile("s_waitcnt lgkmcnt(0)\n\ts_barrier" : : : "memory"); }
#define PIN() __builtin_amdgcn_sched_barrier(0)
DI int uni(int v) { return __builtin_amdgcn_readfirstlane(v); }
DI int lanex() {
    int t;
    asm volatile("v_mbcnt_lo_u32_b32 %0, -1, 0\n\tv_mbcnt_hi_u32_b32 %0, -1, %0" : "=v"(t));
    __builtin_assume(t >= 0 && t < 64);
    return t;
}
DI void flag_ld(u32& r, const char* fl, int idx) {
    asm volatile("s_nop 4\n\tglobal_load_dword %0, %1, %2 sc1" : "=v"(r) : "v"((idx & 0xffff) * 128 + (idx >> 14)), "s"(fl) : "memory");
}
DI void flag_ldm(u32& r, const char* fl, int idx, bool on) {
    asm volatile("v_mov_b32 %0, 1000\n\ts_mov_b32 exec_lo, %3\n\ts_mov_b32 exec_hi, 0\n\tglobal_load_dword %0, %1, %2 sc1\n\ts_mov_b64 exec, -1" : "=&v"(r) : "v"(idx * 128), "s"(fl), "s"(uni(on ? 1 : 0)) : "memory");
}
DI void flag_wait(const char* fl, int idx, int target) {
#pragma nounroll
    for (int it = 0; it < (1 << 16); ++it) {
        u32 v;
        flag_ld(v, fl, idx);
        asm volatile("s_waitcnt vmcnt(0)" : "+v"(v) : : "memory");
        if (uni((int)(v + 1u)) >= target) break;
        __builtin_amdgcn_s_sleep(6);
    }
}
DI void flag_add(char* fl, int idx, int nrep) {
    const int lane = lanex();
    if (lane < nrep) asm volatile("s_nop 4\n\tglobal_atomic_add %0, %1, %2 sc1" : : "v"(((idx & 0xffff) + lane) * 128 + (idx >> 14)), "v"(1), "s"(fl) : "memory");
}
DI void acquire() { asm volatile("buffer_inv sc1\n\ts_waitcnt vmcnt(0)" : : : "memory"); }

template <int V> struct IC { static constexpr int v = V; };
template <int B, int E, class Fn>
DI void sfor(Fn&& fn) { if constexpr (B < E) { fn(IC<B>{}); sfor<B + 1, E>(fn); } }
enum { T_QK = 0, T_VD = 1, T_BL = 3 };
constexpr int GBUF = 24576, GRING = 6;
struct GemmArgs {
    const char *w1, *w1b;
    const char* w2;
    const char* x;
    char *out, *outb;
    int ldw;
    char* sig;
    int type, np1, tn;
    int sV, sP, sQ;
    int xb;
};
DI const char* unip(const char* p) {
    const u64 v = (u64)p;
    return (const char*)(((u64)(u32)uni((int)(v >> 32)) << 32) | (u32)uni((int)v));
}
template <int Z>
DI void mf1(f32x4& c, const u32x4& a, const u32x4& b) {
    if constexpr (Z) asm volatile("v_mfma_f32_16x16x32_bf16 %0, %1, %2, 0" : "=&v"(c) : "v"(a), "a"(b));
    else asm volatile("v_mfma_f32_16x16x32_bf16 %0, %1, %2, %0" : "+v"(c) : "v"(a), "a"(b));
}
template <int Z>
DI void mf2(f32x4& c, const u32x4& a, const u32x4& b) {
    if constexpr (Z) asm volatile("v_mfma_f32_16x16x32_bf16 %0, %1, %2, 0" : "=&v"(c) : "v"(a), "v"(b));
    else asm volatile("v_mfma_f32_16x16x32_bf16 %0, %1, %2, %0" : "+v"(c) : "v"(a), "v"(b));
}
template <int IMM>
DI void gld16a(u32x4& r, const char* sbase, u32 voff) {
    asm volatile("global_load_dwordx4 %0, %1, %2 offset:%3 sc1" : "+a"(r) : "v"(voff), "s"(sbase), "i"(IMM) : "memory");
}
DI void setm0(u32 v) { asm volatile("s_mov_b32 m0, %0" : : "s"(v) : "memory"); }
template <int IMM>
DI void gldsm(const char* sbase, u32 voff) {
    asm volatile("global_load_lds_dwordx4 %0, %1 offset:%2" : : "v"(voff), "s"(sbase), "i"(IMM) : "memory");
}
DI void gst16n(char* sbase, u32 voff, u32x4 v) {
    asm volatile("global_store_dwordx4 %0, %1, %2 sc1" : : "v"(voff), "v"(v), "s"(sbase) : "memory");
}
template <int N>
DI void waitbar() { asm volatile("s_waitcnt vmcnt(%0)\n\ts_barrier" : : "i"(N) : "memory"); }
#define LGKM(n) __builtin_amdgcn_s_waitcnt(0xC07F | ((n) << 8))

#define XSL 10
DI int xhalf() { u32 x; asm volatile("s_getreg_b32 %0, hwreg(HW_REG_XCC_ID)" : "=s"(x)); return (int)((x >> 2) & 1); }
struct Sync { u32x2 r, g, e; };
DI void sync_ld(Sync& y, const char* ws) {
    asm volatile("s_nop 4\n\tglobal_load_dwordx2 %0, %3, %5 sc1\n\tglobal_load_dwordx2 %1, %4, %5 offset:512 sc1\n\tglobal_load_dwordx2 %2, %4, %5 offset:1024 sc1"
                 : "=&v"(y.r), "=&v"(y.g), "=&v"(y.e) : "v"((lanex() & 31) * 8), "v"(0), "s"(ws + OFF_LAT) : "memory");
}
DI u64 sync_tag(u32x2 e) { return (((u64)e[1] << 32 | e[0]) + 1) << 1; }
DI int sync_end(Sync& y, const char* ws, u32& phi, bool ack) {
    asm volatile("" : "+v"(y.r), "+v"(y.g), "+v"(y.e));
#pragma nounroll
    for (int it = 0; it < (1 << 16); ++it) {
        const u64 t = sync_tag(y.e);
        if (__builtin_amdgcn_ballot_w64((y.r[0] & ~1u) != (u32)t || y.r[1] != (u32)(t >> 32) || y.g[0] != (u32)t) == 0) break;
        __builtin_amdgcn_s_sleep(XSL);
        sync_ld(y, ws);
        asm volatile("s_waitcnt vmcnt(0)" : "+v"(y.r), "+v"(y.g), "+v"(y.e) : : "memory");
    }
    if (ack) flag_add((char*)ws + OFF_ST, F_ACK, 1);
    phi = (u32)uni((int)y.g[1]);
    const u32 b = (u32)__builtin_amdgcn_ballot_w64(y.r[0] & 1), all = __builtin_popcount(b) > 16;
    u32 q = 0;
#pragma unroll
    for (int r = 0; r < NREG; ++r) {
        const u32 c = r < 2 ? __builtin_popcount(b & (0x55u << r)) + all : 2 * __builtin_popcount((b >> (4 + 2 * r)) & 3) + all;
        q |= ((c > 2) ^ (u32)((r < 2 ? r : r + (OFF_U >> 21)) & 1)) << r;
    }
    return uni((int)(q ^ 3u * xhalf()));
}
DI void pace(int on) {
    asm volatile("s_cmp_ge_i32 %0, 0\n\ts_cbranch_scc1 .Lp%=\n\ts_waitcnt vmcnt(0)\n.Lp%=:" : : "s"(on) : "memory", "scc");
}
#define SMO 48
#define RM 7
#define BLS 1
#define EPG 1
#define PXT 58
#define PDX 40
#define SPL 0
#define HP0 48
#define UPF 5
#define FRD 25
#define KNT 1
#define BAS 3
#define NO2 2
#define OW 2
#define GSL 6
#define UNAP 700
#define HD1 40
#define PRG 0
#define SMB 48
#define SMP SMO
template <int C>
DI void gap(int on) {
    if constexpr (C > 0) asm volatile("s_cmp_ge_i32 %0, 0\n\ts_cbranch_scc1 .Lg%=\n\ts_nop %1\n\ts_nop %2\n\ts_nop %3\n.Lg%=:" : : "s"(on), "n"(C / 12 - 1), "n"(C / 12 - 1), "n"(C / 12 - 1) : "memory", "scc");
}
DI void early_add(char* ctr, int c) {
    asm volatile("s_cmp_lg_u32 %1, 1\n\ts_cbranch_scc1 .Le%=\n\ts_mov_b64 exec, 1\n\ts_nop 1\n\tglobal_atomic_add %2, %3, %0 sc1\n\ts_mov_b64 exec, -1\n.Le%=:"
                 : : "s"(ctr), "s"(c), "v"(0), "v"(1) : "memory", "scc");
}
#define XL 2
template <int NT>
DI void gemm_task(const GemmArgs& g, Sync& y, const char* ws, u32 lds, int w) {
    constexpr int FD = 5, NX = 2 * NT, OPS = NX + 6, BI = 24 - FD;
    const int lane = lanex();
    const int n = lane & 15, q = lane >> 4, qq = (lane >> 2) & 3, pp = lane & 3;
    const bool p2 = g.type == T_VD;
    const int rw = lane >> 3, sp = (lane >> 1) & 3;
    const u32 v2b = (u32)(rw * 1280 + 16 * (lane & 1) + 5120);
    auto v2o = [&](int i) { return v2b + (u32)(((sp ^ (rw >> 1)) << 5) + i * 9216); };
    u32 wv[6];
#pragma unroll
    for (int i = 0; i < 6; ++i)
        wv[i] = (u32)((rw + 8 * (i / 3)) * g.ldw + ((sp ^ (((rw >> 1) & 1) + 2 * (i / 3))) << 5) + 16 * (lane & 1) + (i % 3) * 128 + 5120 - 1024 * i);
    const char* const wp2 = g.w2 + (size_t)(48 * w) * 1280 - 2048;
    const char* wp = unip(g.w1 + (size_t)(16 * w) * g.ldw - 2048);
    int wstep = uni(64 * g.ldw);
    u32 wdst = lds + (u32)w * 6144 + 3072;
    const u32 wdlast = wdst + (GRING - 1) * GBUF;
    auto wring = [&]() { wdst = wdst == wdlast ? wdst - (GRING - 1) * GBUF : wdst + GBUF; };
    auto wload = [&](int i) {
        setm0(wdst);
        wring();
        asm volatile("s_nop 0");
        sfor<0, 6>([&](auto I) { gldsm<1024 * (I.v - 3)>(wp, wv[I.v]); });
        wp += wstep;
    };
    u32x4 vf[NT][6];
    u32x4 fr[FD + 1];
    u32x4 xr[5][NX];
    auto xtie = [&](u32x4 (&x)[NX]) {
#pragma unroll
        for (int i = 0; i < NX; i += 4) asm volatile("" : "+a"(x[i]), "+a"(x[i + 1]), "+a"(x[i + 2]), "+a"(x[i + 3]));
    };
#pragma unroll
    for (int i = 0; i < 5 * NX; ++i) asm volatile("" : "=a"(xr[i / NX][i % NX]));
    u32x2 flo;
    u32 c[4], slot = 0;
    int sd = GBUF;
    auto sring = [&]() { sd = slot == (GRING - 1) * GBUF ? -(GRING - 1) * GBUF : GBUF; slot += (u32)sd; };
    {
        u32 xo[NT];
#pragma unroll
        for (int tt = 0; tt < NT; ++tt) {
            const int tok = 16 * (tt & 1) + n;
            xo[tt] = (u32)((NT / 2 * w + (tt >> 1)) * 40960 + (g.xb ? q * 512 + tok * 16 : q * 16 + tok * 1280));
        }
        f32x4 acc[12][NT];
        const bool rm = NT == 4 && g.type == T_BL;
        const int sw = (qq >> 1) + 2 * (q & 1);
#pragma unroll
        for (int t = 0; t < 4; ++t)
            c[t] = lds + q * 3072 + qq * 128 + (rm ? ((((t & 2) + (pp >> 1)) ^ sw) << 5) + 16 * (pp & 1) + 8 * ((pp >> 1) ^ (t & 1)) : ((t ^ sw) << 5) + 8 * pp);
        auto rlo = [&](int f) { flo = trread(c[f & 3] + (f / 12) * 12288 + ((f % 12) >> 2) * 1024); };
        auto rhi = [&](int f) {
            const u32x2 hi = trread(c[f & 3] + (f / 12) * 12288 + ((f % 12) >> 2) * 1024 + 512);
            fr[f % (FD + 1)] = u32x4{flo[0], flo[1], hi[0], hi[1]};
        };
        const int xk = uni(g.xb ? 2048 : 64);
        const char *xp = g.x, *xq = xp + xk;
        sfor<0, 4>([&](auto J) {
            if constexpr (J.v < XL) { sfor<0, NX>([&](auto I) { gld16a<0>(xr[J.v][I.v], I.v & 1 ? xq : xp, xo[I.v >> 1]); }); xp += 2 * xk; xq += 2 * xk; }
            wload(J.v);
            if constexpr (NT == 4 && PRG && J.v < 3) asm volatile("s_sleep %0" : : "n"(PRG));
        });
        waitbar<18 + (XL - 1) * NX>();
        xtie(xr[0]);
#pragma unroll
        for (int i = 0; i < FD; ++i) { rlo(i); rhi(i); }
        char* po = g.out;
#pragma nounroll
        for (int ph = g.np1; ph > 0; --ph) {
            sfor<0, 10>([&](auto J) {
                constexpr int j = J.v;
                u32x4 (&xc)[NX] = xr[j % 5];
                u32x4 (&xn)[NX] = xr[(j + XL) % 5];
                sfor<0, 24>([&](auto I) {
                    constexpr int idx = I.v, kk = idx / 12, T = idx % 12, z = j == 0 && kk == 0, f = (idx + FD) % 24;
                    constexpr int mo = NT == 4 ? (idx < BI && idx % 4 != 2 ? idx - (idx + 1) / 4 : -1) : (idx < BI && idx % 2 == 0 ? idx / 2 : -1);
                    auto mem = [&]() {
                        if constexpr (mo >= NX) gldsm<1024 * (mo - NX - 3)>(wp, wv[mo - NX]);
                        else if constexpr (mo >= 0) gld16a<0>(xn[mo], mo & 1 ? xq : xp, xo[mo >> 1]);
                    };
                    auto misc = [&]() {
                        if constexpr (idx == 1) wring();
                        if constexpr (idx == 13) sring();
                        if constexpr (j == 1 && idx == 22) early_add(g.sig, ph + w);
                        if constexpr (idx >= 15 && idx < BI) { c[idx - 15] += (u32)sd; asm volatile("" : "+v"(c[idx - 15])); }
                        const bool more = ph > 1;
                        if constexpr (j != 5) {
                            if constexpr (idx == 20) wp += wstep;
                        } else {
                            if constexpr (idx == 19) { wp = more ? g.w1b + (size_t)(16 * w) * g.ldw - 2048 : wp2; wstep = more ? wstep : (p2 ? 128 : 0); }
                            if constexpr (idx >= 20 && idx < 23) {
#pragma unroll
                                for (int i = 2 * (idx - 20); i < 2 * (idx - 19); ++i) { wv[i] = more ? wv[i] : (p2 ? v2o(i) : 5120u); asm volatile("" : "+v"(wv[i])); }
                            }
                        }
                        if constexpr (j != 9 - XL) {
                            if constexpr (idx == 21) xp += 2 * xk;
                            if constexpr (idx == 22) xq += 2 * xk;
                        } else if constexpr (idx == 23) {
                            xp = g.x; xq = xp + xk;
#pragma unroll
                            for (int i = 0; i < NT; ++i) { xo[i] = more ? xo[i] : 0u; asm volatile("" : "+v"(xo[i])); }
                        }
                    };
                    if constexpr (idx == BI) { waitbar<6 + (XL - 1) * OPS>(); xtie(xr[(j + 1) % 5]); }
                    if constexpr (idx % 2 == 0) LGKM(6);
                    PIN();
                    const u32x4 a = fr[idx % (FD + 1)];
                    mf1<z>(acc[T][0], a, xc[kk]); PIN();
                    rlo(f);
                    if constexpr (idx == 0) setm0(wdst);
                    if constexpr (NT == 2) rhi(f);
                    PIN();
                    mf1<z>(acc[T][1], a, xc[2 + kk]); PIN();
                    if constexpr (NT == 4) {
                        rhi(f); PIN();
                        mf1<z>(acc[T][2], a, xc[4 + kk]); PIN();
                        mem(); PIN();
                        mf1<z>(acc[T][3], a, xc[6 + kk]); PIN();
                    } else { mem(); PIN(); }
                    misc();
                    PIN();
                });
            });
            asm volatile("s_nop 7\n\ts_nop 1");
            PIN();
#pragma unroll
            for (int tt = 0; tt < NT; ++tt)
#pragma unroll
                for (int ks = 0; ks < 6; ++ks)
                    vf[tt][ks] = u32x4{cvtpk(acc[2 * ks][tt][0], acc[2 * ks][tt][1]), cvtpk(acc[2 * ks][tt][2], acc[2 * ks][tt][3]),
                                       cvtpk(acc[2 * ks + 1][tt][0], acc[2 * ks + 1][tt][1]), cvtpk(acc[2 * ks + 1][tt][2], acc[2 * ks + 1][tt][3])};
            if (g.type == T_QK) {
#pragma unroll
                for (int tt = 0; tt < NT; ++tt)
#pragma unroll
                    for (int ks = 0; ks < 6; ++ks) {
                        gst16(po + (size_t)(NT / 2 * w + (tt >> 1)) * (NH * 12288) + (tt & 1) * 6144 + ks * 1024, lane * 16,
                              u32x4{vf[tt][ks][0], vf[tt][ks][2], vf[tt][ks][1], vf[tt][ks][3]});
                        if (NT == 4 && EPG) asm volatile("s_sleep %0" : : "n"(EPG));
                    }
            } else if (rm) {
                const bool sx = q >= 2;
#pragma unroll
                for (int i = 0; i < 3 * NT; ++i) {
                    const int tp = i / 6, ks = i % 6;
                    u32x4 o[2];
#pragma unroll
                    for (int j = 0; j < 4; ++j) {
                        const u32x4 &v = vf[2 * tp][ks], &u = vf[2 * tp + 1][ks];
                        if (BLS) {
                            const auto r = __builtin_amdgcn_permlane32_swap(v[j], u[j], false, false);
                            o[0][j] = r[0]; o[1][j ^ 2] = r[1];
                        } else { o[0][j] = sx ? v[j ^ 2] : v[j]; o[1][j] = sx ? u[j ^ 2] : u[j]; }
                    }
#pragma unroll
                    for (int j = 0; j < 2; ++j) {
                        gst16(po + (size_t)(2 * w + tp) * g.sV + j * (BLS ? 1024 : 256) + ks * 2048, BLS ? (u32)((q & 1) * 512 + (q >> 1) * 256 + n * 16) : (u32)(q * 512 + n * 16), o[j]);
                        if (EPG) asm volatile("s_sleep %0" : : "n"(EPG));
                    }
                }
            }
            po = g.outb;
            PIN();
        }
    }
    if (p2) {
        constexpr int HI = 2048;
        const int r = 4 * (q & 1) + qq, sw = r >> 1;
#pragma unroll
        for (int t = 0; t < 4; ++t) c[t] = lds + slot + (q >> 1) * 1024 + r * 128 + ((t ^ sw) << 5) + 8 * pp;
        sring();
        u32 so = (u32)((n >> 3) * g.sV + q * g.sQ + (n & 7) * 16);
        char* const o0 = g.out + (size_t)(NT / 2 * w) * g.sP;
        char* const o1 = o0 + g.sP;
        const int sv2 = 2 * g.sV;
        auto rlo = [&](int f) { flo = trread(c[f / 6] + (f % 6) * 4096); };
        auto rhi = [&](int f) {
            const u32x2 hi = trread(c[f / 6] + (f % 6) * 4096 + HI);
            fr[f % (FD + 1)] = u32x4{flo[0], flo[1], hi[0], hi[1]};
        };
        f32x4 a2[2][NT];
        u32 e[NT / 2][4];
        auto cv = [&](f32x4 (&a)[NT], int i) { e[i >> 2][i & 3] = cvtpk(a[i >> 1][2 * (i & 1)], a[i >> 1][2 * (i & 1) + 1]); asm volatile("" : "+v"(e[i >> 2][i & 3])); };
        auto st = [&](int pl) { gst16n(pl ? o1 : o0, so, u32x4{e[pl][0], e[pl][1], e[pl][2], e[pl][3]}); };
        PIN();
#pragma unroll
        for (int i = 0; i < FD; ++i) { rlo(i); rhi(i); }
#pragma unroll
        for (int tt = 0; tt < NT; ++tt) a2[1][tt] = f32x4{0.f, 0.f, 0.f, 0.f};
#pragma nounroll
        for (int t = 0; t < g.tn; ++t) {
            sfor<0, 24>([&](auto I) {
                constexpr int idx = I.v, cb = idx / 6, ks = idx % 6, f = (idx + FD) % 24;
                f32x4 (&ac)[NT] = a2[cb & 1];
                f32x4 (&ap)[NT] = a2[(cb & 1) ^ 1];
                constexpr int mo = cb < 2 ? (ks == 0 ? 3 * cb : (ks >= 4 ? 3 * cb + ks - 3 : -1)) : -1;
                auto mem = [&]() {
                    if constexpr (mo >= 0) gldsm<1024 * (mo - 3)>(wp, wv[mo]);
                    if constexpr (ks == 3 || (NT == 4 && ks == 2)) { if (cb > 0 || t > 0) st(NT == 4 ? ks - 2 : 0); }
                };
                auto misc = [&]() {
                    if constexpr (idx == 12) wring();
                    if constexpr (idx == 23) sring();
                    if constexpr (idx == 16) { wp += t < g.tn - 5 ? 128 : 0; }
                    if constexpr (ks == 0) { c[cb] += (u32)sd; asm volatile("" : "+v"(c[cb])); }
                    if constexpr (ks == 4) { so += (cb > 0 || t > 0) ? (u32)sv2 : 0u; asm volatile("" : "+v"(so)); }
                };
                if constexpr (idx == BI) waitbar<NT == 4 ? 38 : 28>();
                if constexpr (idx % 2 == 0) LGKM(6);
                PIN();
                const u32x4 b = fr[idx % (FD + 1)];
                mf2<ks == 0>(ac[0], vf[0][ks], b); PIN();
                rlo(f);
                if constexpr (idx == 0) setm0(wdst);
                if constexpr (NT == 2) rhi(f);
                if constexpr (NT == 4 && ks >= 1 && ks <= 3) cv(ap, ks == 1 ? 0 : (ks == 2 ? 4 : 7));
                PIN();
                mf2<ks == 0>(ac[1], vf[1][ks], b); PIN();
                if constexpr (NT == 4) {
                    rhi(f);
                    if constexpr (ks == 1 || ks == 2) cv(ap, ks == 1 ? 1 : 5);
                    PIN();
                    mf2<ks == 0>(ac[2], vf[2][ks], b); PIN();
                    mem();
                    if constexpr (ks == 1) { cv(ap, 2); cv(ap, 3); }
                    PIN();
                    mf2<ks == 0>(ac[3], vf[3][ks], b); PIN();
                    if constexpr (ks == 2) cv(ap, 6);
                } else {
                    if constexpr (ks == 1 || ks == 2) { cv(ap, 2 * ks - 2); cv(ap, 2 * ks - 1); }
                    mem(); PIN();
                }
                misc();
                PIN();
            });
            asm volatile("s_nop 7\n\ts_nop 1");
        }
        PIN();
#pragma unroll
        for (int i = 0; i < 2 * NT; ++i) cv(a2[1], i);
#pragma unroll
        for (int pl = 0; pl < NT / 2; ++pl) st(pl);
        asm volatile("s_nop 1");
    }
    if constexpr (NT == 2) asm volatile(".Lkm_g2:"); else asm volatile(".Lkm_g4:");
    sync_ld(y, ws);
    waitvm<0>();
    asm volatile("" : "+v"(y.r), "+v"(y.g), "+v"(y.e));
    sfor<0, 5>([&](auto I) { xtie(xr[I.v]); });
}

struct Ptrs {
    const char *x, *mem0, *wq, *wk, *wv, *wo, *wqkvb, *wob, *gw;
    char* out;
    char* ws;
};

enum { Q_H = 0, Q_VD = 1, Q_KQ = 2, Q_QKVB = 3, Q_BA = 4, Q_BO = 5, Q_PX = 6, Q_OUT = 7 };
DI void run_gemm(const Ptrs& p, int ty, int b, int i, int hf, bool raw, Sync& y, u32 lds, int w) {
    GemmArgs g;
    g.sV = g.sP = g.sQ = 0; g.w2 = p.wo; g.ldw = HD * 2; g.np1 = 1; g.tn = 10;
    g.sig = p.ws + OFF_ST + F_END * 128;
    const char* xf = raw ? p.x : p.ws + OFF_XF;
    g.xb = !raw;
    int s0 = 8 * b;
    if (ty == Q_VD) {
        s0 = b < 2 ? 4 * b : s0 - 8;
        g.type = T_VD; g.w1 = g.w1b = p.wv + i * DH * 2;
        g.w2 = p.wo + (size_t)i * DH * 1280;
        g.out = g.outb = p.ws + OFF_D + (size_t)s0 * D_STRIDE + i * 512;
        g.sV = 16384; g.sP = (int)D_STRIDE; g.sQ = 128;
        if (b == 0) { g.tn = 5; g.w2 += hf * 640; g.out += hf * 40 * 16384; }
    } else if (ty == Q_H) {
        s0 = 0;
        g.type = T_QK; g.w1 = g.w1b = (b ? p.wq : p.wk) + i * DH * 2;
        g.out = g.outb = p.ws + (b ? OFF_QX : OFF_KX) + (size_t)i * 12288;
        b = 0;
    } else if (ty == Q_KQ) {
        s0 = b < 2 ? 4 * b : s0 - 8;
        g.type = T_QK; g.w1 = p.wk + i * DH * 2; g.w1b = p.wq + i * DH * 2;
        g.out = p.ws + OFF_KX + (size_t)(s0 * NH + i) * 12288;
        g.outb = g.out + (OFF_QX - OFF_KX);
        g.np1 = 2;
        g.sig = p.ws + OFF_ST + (F_K + b * 32 + i) * 128;
    } else if (ty == Q_QKVB) {
        g.type = T_BL; g.w1 = p.wqkvb + i * 768; g.w1b = g.w1 + 384; g.ldw = 3840;
        g.sV = 1920 * 64; g.np1 = 2;
        g.out = p.ws + OFF_QKVB + (size_t)s0 * g.sV + i * 48 * 512; g.outb = g.out + 24 * 512;
    } else {
        const int c0 = min(i * DH, DM - DH);
        g.type = T_BL; g.w1 = g.w1b = p.wob + c0 * 2; g.ldw = 1280;
        xf = p.ws + OFF_OB;
        g.sV = 40960;
        g.out = g.outb = p.ws + OFF_BOUT + (size_t)s0 * 40960 + c0 * 64;
    }
    g.x = xf + (size_t)s0 * 40960;
    if (ty <= Q_KQ && b < 2) gemm_task<2>(g, y, p.ws, lds, w);
    else gemm_task<4>(g, y, p.ws, lds, w);
}
DI int pblk(int s) { return s < 8 ? s >> 2 : min(s >> 3, 7) + 1; }

template <int IMM>
DI void gld16c(u32x4& r, const char* sbase, u32 voff) {
    asm volatile("global_load_dwordx4 %0, %1, %2 offset:%3 sc1" : "=v"(r) : "v"(voff), "s"(sbase), "i"(IMM) : "memory");
}
#define XP1 " nt"
template <int IMM>
DI void gld16x(u32x4& r, const char* sbase, u32 voff) {
    asm volatile("global_load_dwordx4 %0, %1, %2 offset:%3 sc1" XP1 : "=v"(r) : "v"(voff), "s"(sbase), "i"(IMM) : "memory");
}
DI void gst4c(char* sbase, u32 voff, u32 v) {
    asm volatile("global_store_dword %0, %1, %2 sc1" : : "v"(voff), "v"(v), "s"(sbase) : "memory");
}
template <int N>
DI void waitv(u32x4& r) { asm volatile("s_waitcnt vmcnt(%1)" : "+v"(r) : "i"(N) : "memory"); }
DI u32 mt() { return (u32)__builtin_amdgcn_s_memtime(); }
DI u32 rt() { return (u32)__builtin_amdgcn_s_memrealtime(); }
DI void spin_to(u32 tgt) { while ((int)(mt() - tgt) < 0) {} }
DI u32 max3u(u32 a, u32 b, u32 c) { return max(max(a, b), c); }
DI u32x4 ldsr16(u32 a) { return *LDSP(u32x4, a); }
DI void clocks(u32& r, u32& m) {
    u64 a, b;
    asm volatile("s_memrealtime %0\n\ts_memtime %1\n\ts_waitcnt lgkmcnt(0)" : "=s"(a), "=s"(b) : : "memory");
    r = (u32)a; m = (u32)b;
}
#define SLATE 20
DI void edge(u32& r, u32& m) {
    u64 a, t[9];
    u32 n = 0;
#define RT(i) "s_memrealtime %" #i "\n\ts_nop 1\n\t"
#define RC(i) "\n\ts_cmp_eq_u64 %" #i ", %1\n\ts_addc_u32 %10, %10, 0"
    asm volatile("s_memtime %0\n\t" RT(1) RT(2) RT(3) RT(4) RT(5) RT(6) RT(7) RT(8) "s_memrealtime %9\n\ts_waitcnt lgkmcnt(0)" RC(2) RC(3) RC(4) RC(5) RC(6) RC(7) RC(8) RC(9)
                 : "=&s"(a), "=&s"(t[0]), "=&s"(t[1]), "=&s"(t[2]), "=&s"(t[3]), "=&s"(t[4]), "=&s"(t[5]), "=&s"(t[6]), "=&s"(t[7]), "=&s"(t[8]), "+s"(n) : : "scc", "memory");
    m = (u32)a + 12 * n;
    r = (u32)t[0] + 4;
}
#define SPB 90
struct Clock {
    u32 r0, m0, r1, m1; int cpt;
    DI void init() { edge(r0, m0); r1 = r0; m1 = m0; cpt = 24 * 64; }
    DI u32 sched(u32& phi, int ofs, int G, int mode) {
        if (mode == 0) edge(r1, m1);
        u32 r = r1, m = m1;
        if (mode == 2) clocks(r, m);
        int rem = (int)(phi + (u32)ofs - r);
        if (__builtin_expect(rem < -SLATE, 0)) { const int k = (-rem / G + 1) * G; phi += (u32)k; rem += k; }
        return m + (u32)((min(rem, 3000) * cpt) >> 6) - SPB;
    }
    DI u32 at(u32 t) { u32 r, m; clocks(r, m); return m + (u32)((min(max((int)(t - r), 0), 3000) * cpt) >> 6); }
    DI void calib() {
        const int dr = (int)(r1 - r0);
        if (dr >= 60) { int c = (int)fminf(fmaxf((float)(int)(m1 - m0) * 64.f * __builtin_amdgcn_rcpf((float)dr), 256.f), 2560.f); asm volatile("" : "+v"(c)); cpt = uni(c); r0 = r1; m0 = m1; }
    }
};
#define VISS 0
DI void follow(u32& phi, int ofs, u32 t, int G) { const int late = (int)(t - phi) - ofs; if (__builtin_expect(late > 0, 0)) phi += (u32)((late / G + 1) * G); }
DI bool unwritten(const char* base, u32 off) {
#pragma nounroll
    for (int i = 0; i < 10; ++i) {
        u32x4 t;
        gld16c<-4096>(t, base + (i + (i & 8)) * 1024, off); waitv<0>(t);
        if (__builtin_amdgcn_ballot_w64(max3u(max(t[0], t[1]), t[2], t[3]) == 0xffffffffu) != 0) return true;
    }
    return false;
}

DI size_t uslot(int sd, int s) { const int r = (s * 43) >> 8; return OFF_U + (u32)uni(s * (int)U_STRIDE + r * (int)((2u << 20) - 6 * U_STRIDE) + ((sd >> (2 + r)) & 1) * 8192); }
DI int uchunk(int v) { return (2 * (v & 3) + ((v >> 2) & 1) + (v & ~7)) * 8192; }
DI size_t mslot(int sd, int s) { return (u32)uni(s * (int)HO_STRIDE + ((sd >> (s >> 5)) & 1) * 8192 + 4096); }
struct ChainCfg { int T, cR, G, dV, fast, s1, T2, cR2, s2, T3, cR3, dU; };
constexpr int TT_OFF = 88 + 48 + 360;
DI void karg_warm() {
    u32 a[13];
    asm volatile("s_load_dword %0, %13, 0\n\t" "s_load_dword %1, %13, 64\n\t" "s_load_dword %2, %13, 128\n\t" "s_load_dword %3, %13, 192\n\t" "s_load_dword %4, %13, 256\n\t" "s_load_dword %5, %13, 320\n\t" "s_load_dword %6, %13, 384\n\t" "s_load_dword %7, %13, 448\n\t" "s_load_dword %8, %13, 512\n\t" "s_load_dword %9, %13, 576\n\t" "s_load_dword %10, %13, 640\n\t" "s_load_dword %11, %13, 704\n\t" "s_load_dword %12, %13, 748\n\t" "s_waitcnt lgkmcnt(0)"
                 : "=&s"(a[0]), "=&s"(a[1]), "=&s"(a[2]), "=&s"(a[3]), "=&s"(a[4]), "=&s"(a[5]), "=&s"(a[6]), "=&s"(a[7]), "=&s"(a[8]), "=&s"(a[9]), "=&s"(a[10]), "=&s"(a[11]), "=&s"(a[12])
                 : "s"(__builtin_amdgcn_kernarg_segment_ptr()) : "memory");
}
DI u32 sched_ld0(int s) {
    u32 r;
    asm volatile("s_load_dword %0, %1, %2 offset:%3\n\ts_waitcnt lgkmcnt(0)" : "=s"(r) : "s"(__builtin_amdgcn_kernarg_segment_ptr()), "s"(4 * s), "n"(TT_OFF) : "memory");
    return r;
}
#define HP 60
#define HQ 96

constexpr int SL_W = 0;
constexpr int SL_IMG = 131072;
constexpr int SL_KX = SL_IMG + 6656;
constexpr int SL_PF = SL_KX + 12288 + 1024;
constexpr int S_LDS = SL_PF + 256;
DI void nap(int t) { const u32 wake = rt() + (u32)t; while ((int)(rt() - wake) < 0) __builtin_amdgcn_s_sleep(4); }
DI void until(u32 t) { while ((int)(rt() - t) < 0) __builtin_amdgcn_s_sleep(1); }
DI void dly(int n) {
    asm volatile(".Ld%=:\n\ts_sub_u32 %0, %0, 1\n\ts_nop 0\n\ts_cbranch_scc0 .Ld%=" : "+s"(n) : : "scc", "memory");
}
DI void rdy_add(u32& r, char* fl) {
    asm volatile("s_mov_b64 exec, 1\n\ts_nop 3\n\tglobal_atomic_add %0, %1, %2, %3 sc0 sc1\n\ts_mov_b64 exec, -1" : "=&v"(r) : "v"(F_RDY * 128), "v"(1), "s"(fl) : "memory");
}
DI void rdy_end(u32 r, char* fl, int dt) {
    if (uni((int)r) == NS + NVB - 2 && lanex() < 8) gst4c(fl, (F_GO + lanex()) * 128, (rt() + (u32)dt) & ~1u);
}
DI u32 go_wait(const char* fl, int i) {
    u32 v;
#pragma nounroll
    for (int it = 0; it < (1 << 16); ++it) {
        flag_ld(v, fl, F_GO + (i & 7));
        asm volatile("s_waitcnt vmcnt(0)" : "+v"(v) : : "memory");
        if (uni((int)v) != -1) break;
        __builtin_amdgcn_s_sleep(GSL);
    }
    return (u32)uni((int)v);
}
DI u32 go_wg(const char* fl, int i, int w, u32 la) {
    if (w == 0) *LDSP(u32, la) = go_wait(fl, i);
    barrier();
    return (u32)uni((int)*LDSP(u32, la));
}
DI void pfl(const char* sbase, u32 voff, u32 lds) {
    asm volatile("s_mov_b32 m0, %2\n\ts_nop 3\n\tglobal_load_lds_dword %0, %1" : : "v"(voff), "s"(sbase), "s"(lds + SL_PF) : "memory");
}
DI void score_role(const Ptrs& p, const ChainCfg cfg, int h, u32 lds, int w) {
    const int lane = lanex(), tid = 64 * w + lane;
    const int n = lane & 15, g = lane >> 4, qq = (lane >> 2) & 3, pp = lane & 3;
    u32x32 wa[8], wv[3];
    const u32 wreg = lds + SL_W + (u32)w * 32768;
    const u32 lfa = wreg + lane * 16;
    const char* kxp = p.ws + OFF_KX + (size_t)h * 12288 + w * 3072;
    const u32 kxl = lds + SL_KX;
    char* const fl = p.ws + OFF_ST;
    const u32 kxw = kxl + w * 3072 - (w < 2 ? 16 : 0);
    auto kxdma = [&]() {
        asm volatile("s_mov_b32 m0, %2\n\ts_nop 3\n\tglobal_load_lds_dwordx4 %0, %1 sc1\n\tglobal_load_lds_dwordx4 %0, %1 offset:1024 sc1\n\tglobal_load_lds_dwordx4 %0, %1 offset:2048 sc1"
                     : : "v"(lanex() * 16), "s"(kxp), "s"(kxw) : "memory");
    };
    u32 kfl, phi;
    int sd;
    {
        constexpr int GS = 3104;
        Sync y;
        if (w < 3) {
#pragma unroll
            for (int i = PXT; i < NSEG; ++i) gst16(p.ws + OFF_KM + (i * NH + h) * 3072 + w * 1024, lane * 16, u32x4{~0u, ~0u, ~0u, ~0u});
#pragma unroll
            for (int i = NSEG - 4; i < NSEG; ++i) gst16(p.ws + OFF_PX + i * 81920 + (w < 2 ? h * 2048 + w * 1024 : 65536 + h * 512), w < 2 ? lane * 16 : (lane * 16) & 511, u32x4{~0u, ~0u, ~0u, ~0u});
        }
        auto dvo = [&](int i) { const int ix = i * 64 + lane; return (u32)((ix / 24) * (HD * 2) + (ix % 24) * 16); };
        const u32 tra = lds + (2 * (g >> 1) + (qq >> 1)) * GS + (4 * (g & 1) + (qq & 1)) * 384 + 8 * pp;
        u32x8 pa = u32x8{dvo(0), dvo(1), dvo(2), tra + 96 * w + 32 * (w & 1), tra + 96 * w - 64 * (w & 1), tra + 32 * w, lfa, (u32)(lane & 31) * 8};
        asm volatile("s_nop 4\n\t" "s_add_u32 s42, %21, 0x0\n\t" "s_add_u32 s42, s46, s42\n\t" "s_addc_u32 s43, s47, 0\n\t" "s_add_u32 m0, %18, 0x0\n\t" "s_nop 0\n\t" "global_load_lds_dwordx4 v224, s[42:43]\n\t"
                     "s_add_u32 m0, %18, 0x400\n\t" "s_nop 0\n\t" "global_load_lds_dwordx4 v225, s[42:43]\n\t" "s_add_u32 m0, %18, 0x800\n\t" "s_nop 0\n\t" "global_load_lds_dwordx4 v226, s[42:43]\n\t"
                     "s_add_u32 s42, %21, 0x18000\n\t" "s_add_u32 s42, s46, s42\n\t" "s_addc_u32 s43, s47, 0\n\t" "s_add_u32 m0, %18, 0xc20\n\t" "s_nop 0\n\t" "global_load_lds_dwordx4 v224, s[42:43]\n\t"
                     "s_add_u32 m0, %18, 0x1020\n\t" "s_nop 0\n\t" "global_load_lds_dwordx4 v225, s[42:43]\n\t" "s_add_u32 m0, %18, 0x1420\n\t" "s_nop 0\n\t" "global_load_lds_dwordx4 v226, s[42:43]\n\t"
                     "s_sub_u32 s42, 0xc0000, %21\n\t" "s_add_u32 s42, s46, s42\n\t" "s_addc_u32 s43, s47, 0\n\t" "s_add_u32 m0, %18, 0x6100\n\t" "s_nop 0\n\t" "global_load_lds_dwordx4 v224, s[42:43]\n\t"
                     "s_add_u32 m0, %18, 0x6500\n\t" "s_nop 0\n\t" "global_load_lds_dwordx4 v225, s[42:43]\n\t" "s_add_u32 m0, %18, 0x6900\n\t" "s_nop 0\n\t" "global_load_lds_dwordx4 v226, s[42:43]\n\t"
                     "s_sub_u32 s42, 0xd8000, %21\n\t" "s_add_u32 s42, s46, s42\n\t" "s_addc_u32 s43, s47, 0\n\t" "s_add_u32 m0, %18, 0x6d20\n\t" "s_nop 0\n\t" "global_load_lds_dwordx4 v224, s[42:43]\n\t"
                     "s_add_u32 m0, %18, 0x7120\n\t" "s_nop 0\n\t" "global_load_lds_dwordx4 v225, s[42:43]\n\t" "s_add_u32 m0, %18, 0x7520\n\t" "s_nop 0\n\t" "global_load_lds_dwordx4 v226, s[42:43]\n\t"
                     "s_add_u32 s42, %21, 0x180000\n\t" "s_add_u32 s42, s46, s42\n\t" "s_addc_u32 s43, s47, 0\n\t" "s_add_u32 m0, %18, 0xc200\n\t" "s_nop 0\n\t" "global_load_lds_dwordx4 v224, s[42:43]\n\t"
                     "s_add_u32 m0, %18, 0xc600\n\t" "s_nop 0\n\t" "global_load_lds_dwordx4 v225, s[42:43]\n\t" "s_add_u32 m0, %18, 0xca00\n\t" "s_nop 0\n\t" "global_load_lds_dwordx4 v226, s[42:43]\n\t"
                     "s_add_u32 s42, %21, 0x198000\n\t" "s_add_u32 s42, s46, s42\n\t" "s_addc_u32 s43, s47, 0\n\t" "s_add_u32 m0, %18, 0xce20\n\t" "s_nop 0\n\t" "global_load_lds_dwordx4 v224, s[42:43]\n\t"
                     "s_add_u32 m0, %18, 0xd220\n\t" "s_nop 0\n\t" "global_load_lds_dwordx4 v225, s[42:43]\n\t" "s_add_u32 m0, %18, 0xd620\n\t" "s_nop 0\n\t" "global_load_lds_dwordx4 v226, s[42:43]\n\t"
                     "s_sub_u32 s42, 0x240000, %21\n\t" "s_add_u32 s42, s46, s42\n\t" "s_addc_u32 s43, s47, 0\n\t" "s_add_u32 m0, %18, 0x12300\n\t" "s_nop 0\n\t" "global_load_lds_dwordx4 v224, s[42:43]\n\t"
                     "s_add_u32 m0, %18, 0x12700\n\t" "s_nop 0\n\t" "global_load_lds_dwordx4 v225, s[42:43]\n\t" "s_add_u32 m0, %18, 0x12b00\n\t" "s_nop 0\n\t" "global_load_lds_dwordx4 v226, s[42:43]\n\t"
                     "s_sub_u32 s42, 0x258000, %21\n\t" "s_add_u32 s42, s46, s42\n\t" "s_addc_u32 s43, s47, 0\n\t" "s_add_u32 m0, %18, 0x12f20\n\t" "s_nop 0\n\t" "global_load_lds_dwordx4 v224, s[42:43]\n\t"
                     "s_add_u32 m0, %18, 0x13320\n\t" "s_nop 0\n\t" "global_load_lds_dwordx4 v225, s[42:43]\n\t" "s_add_u32 m0, %18, 0x13720\n\t" "s_nop 0\n\t" "global_load_lds_dwordx4 v226, s[42:43]\n\t"
                     "s_waitcnt vmcnt(12) lgkmcnt(0)\n\t" "s_barrier\n\t" "s_add_u32 s42, %21, 0x300000\n\t" "s_add_u32 s42, s46, s42\n\t" "s_addc_u32 s43, s47, 0\n\t" "s_add_u32 m0, %18, 0x18400\n\t" "s_nop 0\n\t"
                     "global_load_lds_dwordx4 v224, s[42:43]\n\t" "s_add_u32 m0, %18, 0x18800\n\t" "s_nop 0\n\t" "global_load_lds_dwordx4 v225, s[42:43]\n\t" "s_add_u32 m0, %18, 0x18c00\n\t" "s_nop 0\n\t"
                     "global_load_lds_dwordx4 v226, s[42:43]\n\t" "s_add_u32 s42, %21, 0x318000\n\t" "s_add_u32 s42, s46, s42\n\t" "s_addc_u32 s43, s47, 0\n\t" "s_add_u32 m0, %18, 0x19020\n\t" "s_nop 0\n\t"
                     "global_load_lds_dwordx4 v224, s[42:43]\n\t" "s_add_u32 m0, %18, 0x19420\n\t" "s_nop 0\n\t" "global_load_lds_dwordx4 v225, s[42:43]\n\t" "s_add_u32 m0, %18, 0x19820\n\t" "s_nop 0\n\t"
                     "global_load_lds_dwordx4 v226, s[42:43]\n\t" "s_sub_u32 s42, 0x3c0000, %21\n\t" "s_add_u32 s42, s46, s42\n\t" "s_addc_u32 s43, s47, 0\n\t" "s_add_u32 m0, %18, 0x1e500\n\t" "s_nop 0\n\t"
                     "global_load_lds_dwordx4 v224, s[42:43]\n\t" "s_add_u32 m0, %18, 0x1e900\n\t" "s_nop 0\n\t" "global_load_lds_dwordx4 v225, s[42:43]\n\t" "s_add_u32 m0, %18, 0x1ed00\n\t" "s_nop 0\n\t"
                     "global_load_lds_dwordx4 v226, s[42:43]\n\t" "s_sub_u32 s42, 0x3d8000, %21\n\t" "s_add_u32 s42, s46, s42\n\t" "s_addc_u32 s43, s47, 0\n\t" "s_add_u32 m0, %18, 0x1f120\n\t" "s_nop 0\n\t"
                     "global_load_lds_dwordx4 v224, s[42:43]\n\t" "s_add_u32 m0, %18, 0x1f520\n\t" "s_nop 0\n\t" "global_load_lds_dwordx4 v225, s[42:43]\n\t" "s_add_u32 m0, %18, 0x1f920\n\t" "s_nop 0\n\t"
                     "global_load_lds_dwordx4 v226, s[42:43]\n\t" "v_add_u32 v232, 0x0, v229\n\t" "ds_read_b64_tr_b16 v[8:9], v232\n\t" "ds_read_b64_tr_b16 v[10:11], v232 offset:768\n\t"
                     "ds_read_b64_tr_b16 v[12:13], v232 offset:12416\n\t" "ds_read_b64_tr_b16 v[14:15], v232 offset:13184\n\t" "ds_read_b64_tr_b16 a[96:97], v232 offset:128\n\t"
                     "ds_read_b64_tr_b16 a[98:99], v232 offset:896\n\t" "ds_read_b64_tr_b16 a[100:101], v232 offset:12544\n\t" "ds_read_b64_tr_b16 a[102:103], v232 offset:13312\n\t"
                     "ds_read_b64_tr_b16 a[176:177], v232 offset:256\n\t" "ds_read_b64_tr_b16 a[178:179], v232 offset:1024\n\t" "ds_read_b64_tr_b16 a[180:181], v232 offset:12672\n\t"
                     "ds_read_b64_tr_b16 a[182:183], v232 offset:13440\n\t" "ds_read_b64_tr_b16 v[40:41], v232 offset:24832\n\t" "ds_read_b64_tr_b16 v[42:43], v232 offset:25600\n\t"
                     "ds_read_b64_tr_b16 v[44:45], v232 offset:37248\n\t" "ds_read_b64_tr_b16 v[46:47], v232 offset:38016\n\t" "ds_read_b64_tr_b16 a[112:113], v232 offset:24960\n\t"
                     "ds_read_b64_tr_b16 a[114:115], v232 offset:25728\n\t" "ds_read_b64_tr_b16 a[116:117], v232 offset:37376\n\t" "ds_read_b64_tr_b16 a[118:119], v232 offset:38144\n\t"
                     "ds_read_b64_tr_b16 a[192:193], v232 offset:25088\n\t" "ds_read_b64_tr_b16 a[194:195], v232 offset:25856\n\t" "ds_read_b64_tr_b16 a[196:197], v232 offset:37504\n\t"
                     "ds_read_b64_tr_b16 a[198:199], v232 offset:38272\n\t" "s_waitcnt vmcnt(12) lgkmcnt(0)\n\t" "s_barrier\n\t" "s_add_u32 s42, %21, 0x480000\n\t" "s_add_u32 s42, s46, s42\n\t" "s_addc_u32 s43, s47, 0\n\t"
                     "s_add_u32 m0, %18, 0x0\n\t" "s_nop 0\n\t" "global_load_lds_dwordx4 v224, s[42:43]\n\t" "s_add_u32 m0, %18, 0x400\n\t" "s_nop 0\n\t" "global_load_lds_dwordx4 v225, s[42:43]\n\t"
                     "s_add_u32 m0, %18, 0x800\n\t" "s_nop 0\n\t" "global_load_lds_dwordx4 v226, s[42:43]\n\t" "s_add_u32 s42, %21, 0x498000\n\t" "s_add_u32 s42, s46, s42\n\t" "s_addc_u32 s43, s47, 0\n\t"
                     "s_add_u32 m0, %18, 0xc20\n\t" "s_nop 0\n\t" "global_load_lds_dwordx4 v224, s[42:43]\n\t" "s_add_u32 m0, %18, 0x1020\n\t" "s_nop 0\n\t" "global_load_lds_dwordx4 v225, s[42:43]\n\t"
                     "s_add_u32 m0, %18, 0x1420\n\t" "s_nop 0\n\t" "global_load_lds_dwordx4 v226, s[42:43]\n\t" "s_sub_u32 s42, 0x540000, %21\n\t" "s_add_u32 s42, s46, s42\n\t" "s_addc_u32 s43, s47, 0\n\t"
                     "s_add_u32 m0, %18, 0x6100\n\t" "s_nop 0\n\t" "global_load_lds_dwordx4 v224, s[42:43]\n\t" "s_add_u32 m0, %18, 0x6500\n\t" "s_nop 0\n\t" "global_load_lds_dwordx4 v225, s[42:43]\n\t"
                     "s_add_u32 m0, %18, 0x6900\n\t" "s_nop 0\n\t" "global_load_lds_dwordx4 v226, s[42:43]\n\t" "s_sub_u32 s42, 0x558000, %21\n\t" "s_add_u32 s42, s46, s42\n\t" "s_addc_u32 s43, s47, 0\n\t"
                     "s_add_u32 m0, %18, 0x6d20\n\t" "s_nop 0\n\t" "global_load_lds_dwordx4 v224, s[42:43]\n\t" "s_add_u32 m0, %18, 0x7120\n\t" "s_nop 0\n\t" "global_load_lds_dwordx4 v225, s[42:43]\n\t"
                     "s_add_u32 m0, %18, 0x7520\n\t" "s_nop 0\n\t" "global_load_lds_dwordx4 v226, s[42:43]\n\t" "v_add_u32 v232, 0xc200, v229\n\t" "ds_read_b64_tr_b16 v[24:25], v232\n\t"
                     "ds_read_b64_tr_b16 v[26:27], v232 offset:768\n\t" "ds_read_b64_tr_b16 v[28:29], v232 offset:12416\n\t" "ds_read_b64_tr_b16 v[30:31], v232 offset:13184\n\t"
                     "ds_read_b64_tr_b16 a[104:105], v232 offset:128\n\t" "ds_read_b64_tr_b16 a[106:107], v232 offset:896\n\t" "ds_read_b64_tr_b16 a[108:109], v232 offset:12544\n\t"
                     "ds_read_b64_tr_b16 a[110:111], v232 offset:13312\n\t" "ds_read_b64_tr_b16 a[184:185], v232 offset:256\n\t" "ds_read_b64_tr_b16 a[186:187], v232 offset:1024\n\t"
                     "ds_read_b64_tr_b16 a[188:189], v232 offset:12672\n\t" "ds_read_b64_tr_b16 a[190:191], v232 offset:13440\n\t" "ds_read_b64_tr_b16 v[56:57], v232 offset:24832\n\t"
                     "ds_read_b64_tr_b16 v[58:59], v232 offset:25600\n\t" "ds_read_b64_tr_b16 v[60:61], v232 offset:37248\n\t" "ds_read_b64_tr_b16 v[62:63], v232 offset:38016\n\t"
                     "ds_read_b64_tr_b16 a[120:121], v232 offset:24960\n\t" "ds_read_b64_tr_b16 a[122:123], v232 offset:25728\n\t" "ds_read_b64_tr_b16 a[124:125], v232 offset:37376\n\t"
                     "ds_read_b64_tr_b16 a[126:127], v232 offset:38144\n\t" "ds_read_b64_tr_b16 a[200:201], v232 offset:25088\n\t" "ds_read_b64_tr_b16 a[202:203], v232 offset:25856\n\t"
                     "ds_read_b64_tr_b16 a[204:205], v232 offset:37504\n\t" "ds_read_b64_tr_b16 a[206:207], v232 offset:38272\n\t" "s_waitcnt vmcnt(12) lgkmcnt(0)\n\t" "s_barrier\n\t" "s_add_u32 s42, %21, 0x600000\n\t"
                     "s_add_u32 s42, s46, s42\n\t" "s_addc_u32 s43, s47, 0\n\t" "s_add_u32 m0, %18, 0xc200\n\t" "s_nop 0\n\t" "global_load_lds_dwordx4 v224, s[42:43]\n\t" "s_add_u32 m0, %18, 0xc600\n\t" "s_nop 0\n\t"
                     "global_load_lds_dwordx4 v225, s[42:43]\n\t" "s_add_u32 m0, %18, 0xca00\n\t" "s_nop 0\n\t" "global_load_lds_dwordx4 v226, s[42:43]\n\t" "s_add_u32 s42, %21, 0x618000\n\t" "s_add_u32 s42, s46, s42\n\t"
                     "s_addc_u32 s43, s47, 0\n\t" "s_add_u32 m0, %18, 0xce20\n\t" "s_nop 0\n\t" "global_load_lds_dwordx4 v224, s[42:43]\n\t" "s_add_u32 m0, %18, 0xd220\n\t" "s_nop 0\n\t"
                     "global_load_lds_dwordx4 v225, s[42:43]\n\t" "s_add_u32 m0, %18, 0xd620\n\t" "s_nop 0\n\t" "global_load_lds_dwordx4 v226, s[42:43]\n\t" "s_sub_u32 s42, 0x6c0000, %21\n\t" "s_add_u32 s42, s46, s42\n\t"
                     "s_addc_u32 s43, s47, 0\n\t" "s_add_u32 m0, %18, 0x12300\n\t" "s_nop 0\n\t" "global_load_lds_dwordx4 v224, s[42:43]\n\t" "s_add_u32 m0, %18, 0x12700\n\t" "s_nop 0\n\t"
                     "global_load_lds_dwordx4 v225, s[42:43]\n\t" "s_add_u32 m0, %18, 0x12b00\n\t" "s_nop 0\n\t" "global_load_lds_dwordx4 v226, s[42:43]\n\t" "s_sub_u32 s42, 0x6d8000, %21\n\t" "s_add_u32 s42, s46, s42\n\t"
                     "s_addc_u32 s43, s47, 0\n\t" "s_add_u32 m0, %18, 0x12f20\n\t" "s_nop 0\n\t" "global_load_lds_dwordx4 v224, s[42:43]\n\t" "s_add_u32 m0, %18, 0x13320\n\t" "s_nop 0\n\t"
                     "global_load_lds_dwordx4 v225, s[42:43]\n\t" "s_add_u32 m0, %18, 0x13720\n\t" "s_nop 0\n\t" "global_load_lds_dwordx4 v226, s[42:43]\n\t" "v_add_u32 v232, 0x18400, v229\n\t"
                     "ds_read_b64_tr_b16 v[72:73], v232\n\t" "ds_read_b64_tr_b16 v[74:75], v232 offset:768\n\t" "ds_read_b64_tr_b16 v[76:77], v232 offset:12416\n\t" "ds_read_b64_tr_b16 v[78:79], v232 offset:13184\n\t"
                     "ds_read_b64_tr_b16 a[128:129], v232 offset:128\n\t" "ds_read_b64_tr_b16 a[130:131], v232 offset:896\n\t" "ds_read_b64_tr_b16 a[132:133], v232 offset:12544\n\t"
                     "ds_read_b64_tr_b16 a[134:135], v232 offset:13312\n\t" "ds_read_b64_tr_b16 a[208:209], v232 offset:256\n\t" "ds_read_b64_tr_b16 a[210:211], v232 offset:1024\n\t"
                     "ds_read_b64_tr_b16 a[212:213], v232 offset:12672\n\t" "ds_read_b64_tr_b16 a[214:215], v232 offset:13440\n\t" "ds_read_b64_tr_b16 v[88:89], v232 offset:24832\n\t"
                     "ds_read_b64_tr_b16 v[90:91], v232 offset:25600\n\t" "ds_read_b64_tr_b16 v[92:93], v232 offset:37248\n\t" "ds_read_b64_tr_b16 v[94:95], v232 offset:38016\n\t"
                     "ds_read_b64_tr_b16 a[136:137], v232 offset:24960\n\t" "ds_read_b64_tr_b16 a[138:139], v232 offset:25728\n\t" "ds_read_b64_tr_b16 a[140:141], v232 offset:37376\n\t"
                     "ds_read_b64_tr_b16 a[142:143], v232 offset:38144\n\t" "ds_read_b64_tr_b16 a[216:217], v232 offset:25088\n\t" "ds_read_b64_tr_b16 a[218:219], v232 offset:25856\n\t"
                     "ds_read_b64_tr_b16 a[220:221], v232 offset:37504\n\t" "ds_read_b64_tr_b16 a[222:223], v232 offset:38272\n\t" "s_waitcnt vmcnt(12) lgkmcnt(0)\n\t" "s_barrier\n\t" "s_add_u32 s42, %21, 0x0\n\t"
                     "s_add_u32 s42, s44, s42\n\t" "s_addc_u32 s43, s45, 0\n\t" "s_add_u32 m0, %18, 0x18400\n\t" "s_nop 0\n\t" "global_load_lds_dwordx4 v224, s[42:43]\n\t" "s_add_u32 m0, %18, 0x18800\n\t" "s_nop 0\n\t"
                     "global_load_lds_dwordx4 v225, s[42:43]\n\t" "s_add_u32 m0, %18, 0x18c00\n\t" "s_nop 0\n\t" "global_load_lds_dwordx4 v226, s[42:43]\n\t" "s_add_u32 s42, %21, 0x18000\n\t" "s_add_u32 s42, s44, s42\n\t"
                     "s_addc_u32 s43, s45, 0\n\t" "s_add_u32 m0, %18, 0x19020\n\t" "s_nop 0\n\t" "global_load_lds_dwordx4 v224, s[42:43]\n\t" "s_add_u32 m0, %18, 0x19420\n\t" "s_nop 0\n\t"
                     "global_load_lds_dwordx4 v225, s[42:43]\n\t" "s_add_u32 m0, %18, 0x19820\n\t" "s_nop 0\n\t" "global_load_lds_dwordx4 v226, s[42:43]\n\t" "s_sub_u32 s42, 0xc0000, %21\n\t" "s_add_u32 s42, s44, s42\n\t"
                     "s_addc_u32 s43, s45, 0\n\t" "s_add_u32 m0, %18, 0x1e500\n\t" "s_nop 0\n\t" "global_load_lds_dwordx4 v224, s[42:43]\n\t" "s_add_u32 m0, %18, 0x1e900\n\t" "s_nop 0\n\t"
                     "global_load_lds_dwordx4 v225, s[42:43]\n\t" "s_add_u32 m0, %18, 0x1ed00\n\t" "s_nop 0\n\t" "global_load_lds_dwordx4 v226, s[42:43]\n\t" "s_sub_u32 s42, 0xd8000, %21\n\t" "s_add_u32 s42, s44, s42\n\t"
                     "s_addc_u32 s43, s45, 0\n\t" "s_add_u32 m0, %18, 0x1f120\n\t" "s_nop 0\n\t" "global_load_lds_dwordx4 v224, s[42:43]\n\t" "s_add_u32 m0, %18, 0x1f520\n\t" "s_nop 0\n\t"
                     "global_load_lds_dwordx4 v225, s[42:43]\n\t" "s_add_u32 m0, %18, 0x1f920\n\t" "s_nop 0\n\t" "global_load_lds_dwordx4 v226, s[42:43]\n\t" "v_add_u32 v232, 0x0, v229\n\t"
                     "ds_read_b64_tr_b16 v[104:105], v232\n\t" "ds_read_b64_tr_b16 v[106:107], v232 offset:768\n\t" "ds_read_b64_tr_b16 v[108:109], v232 offset:12416\n\t"
                     "ds_read_b64_tr_b16 v[110:111], v232 offset:13184\n\t" "ds_read_b64_tr_b16 a[144:145], v232 offset:128\n\t" "ds_read_b64_tr_b16 a[146:147], v232 offset:896\n\t"
                     "ds_read_b64_tr_b16 a[148:149], v232 offset:12544\n\t" "ds_read_b64_tr_b16 a[150:151], v232 offset:13312\n\t" "ds_read_b64_tr_b16 a[224:225], v232 offset:256\n\t"
                     "ds_read_b64_tr_b16 a[226:227], v232 offset:1024\n\t" "ds_read_b64_tr_b16 a[228:229], v232 offset:12672\n\t" "ds_read_b64_tr_b16 a[230:231], v232 offset:13440\n\t"
                     "ds_read_b64_tr_b16 v[120:121], v232 offset:24832\n\t" "ds_read_b64_tr_b16 v[122:123], v232 offset:25600\n\t" "ds_read_b64_tr_b16 v[124:125], v232 offset:37248\n\t"
                     "ds_read_b64_tr_b16 v[126:127], v232 offset:38016\n\t" "ds_read_b64_tr_b16 a[152:153], v232 offset:24960\n\t" "ds_read_b64_tr_b16 a[154:155], v232 offset:25728\n\t"
                     "ds_read_b64_tr_b16 a[156:157], v232 offset:37376\n\t" "ds_read_b64_tr_b16 a[158:159], v232 offset:38144\n\t" "ds_read_b64_tr_b16 a[232:233], v232 offset:25088\n\t"
                     "ds_read_b64_tr_b16 a[234:235], v232 offset:25856\n\t" "ds_read_b64_tr_b16 a[236:237], v232 offset:37504\n\t" "ds_read_b64_tr_b16 a[238:239], v232 offset:38272\n\t" "s_waitcnt vmcnt(12) lgkmcnt(0)\n\t"
                     "s_barrier\n\t" "s_add_u32 s42, %21, 0x180000\n\t" "s_add_u32 s42, s44, s42\n\t" "s_addc_u32 s43, s45, 0\n\t" "s_add_u32 m0, %18, 0x0\n\t" "s_nop 0\n\t" "global_load_lds_dwordx4 v224, s[42:43]\n\t"
                     "s_add_u32 m0, %18, 0x400\n\t" "s_nop 0\n\t" "global_load_lds_dwordx4 v225, s[42:43]\n\t" "s_add_u32 m0, %18, 0x800\n\t" "s_nop 0\n\t" "global_load_lds_dwordx4 v226, s[42:43]\n\t"
                     "s_add_u32 s42, %21, 0x198000\n\t" "s_add_u32 s42, s44, s42\n\t" "s_addc_u32 s43, s45, 0\n\t" "s_add_u32 m0, %18, 0xc20\n\t" "s_nop 0\n\t" "global_load_lds_dwordx4 v224, s[42:43]\n\t"
                     "s_add_u32 m0, %18, 0x1020\n\t" "s_nop 0\n\t" "global_load_lds_dwordx4 v225, s[42:43]\n\t" "s_add_u32 m0, %18, 0x1420\n\t" "s_nop 0\n\t" "global_load_lds_dwordx4 v226, s[42:43]\n\t"
                     "s_sub_u32 s42, 0x240000, %21\n\t" "s_add_u32 s42, s44, s42\n\t" "s_addc_u32 s43, s45, 0\n\t" "s_add_u32 m0, %18, 0x6100\n\t" "s_nop 0\n\t" "global_load_lds_dwordx4 v224, s[42:43]\n\t"
                     "s_add_u32 m0, %18, 0x6500\n\t" "s_nop 0\n\t" "global_load_lds_dwordx4 v225, s[42:43]\n\t" "s_add_u32 m0, %18, 0x6900\n\t" "s_nop 0\n\t" "global_load_lds_dwordx4 v226, s[42:43]\n\t"
                     "s_sub_u32 s42, 0x258000, %21\n\t" "s_add_u32 s42, s44, s42\n\t" "s_addc_u32 s43, s45, 0\n\t" "s_add_u32 m0, %18, 0x6d20\n\t" "s_nop 0\n\t" "global_load_lds_dwordx4 v224, s[42:43]\n\t"
                     "s_add_u32 m0, %18, 0x7120\n\t" "s_nop 0\n\t" "global_load_lds_dwordx4 v225, s[42:43]\n\t" "s_add_u32 m0, %18, 0x7520\n\t" "s_nop 0\n\t" "global_load_lds_dwordx4 v226, s[42:43]\n\t"
                     "v_add_u32 v232, 0xc200, v229\n\t" "ds_read_b64_tr_b16 a[56:57], v232\n\t" "ds_read_b64_tr_b16 a[58:59], v232 offset:768\n\t" "ds_read_b64_tr_b16 a[60:61], v232 offset:12416\n\t"
                     "ds_read_b64_tr_b16 a[62:63], v232 offset:13184\n\t" "ds_read_b64_tr_b16 a[160:161], v232 offset:128\n\t" "ds_read_b64_tr_b16 a[162:163], v232 offset:896\n\t"
                     "ds_read_b64_tr_b16 a[164:165], v232 offset:12544\n\t" "ds_read_b64_tr_b16 a[166:167], v232 offset:13312\n\t" "ds_read_b64_tr_b16 a[240:241], v232 offset:256\n\t"
                     "ds_read_b64_tr_b16 a[242:243], v232 offset:1024\n\t" "ds_read_b64_tr_b16 a[244:245], v232 offset:12672\n\t" "ds_read_b64_tr_b16 a[246:247], v232 offset:13440\n\t"
                     "ds_read_b64_tr_b16 a[88:89], v232 offset:24832\n\t" "ds_read_b64_tr_b16 a[90:91], v232 offset:25600\n\t" "ds_read_b64_tr_b16 a[92:93], v232 offset:37248\n\t"
                     "ds_read_b64_tr_b16 a[94:95], v232 offset:38016\n\t" "ds_read_b64_tr_b16 a[168:169], v232 offset:24960\n\t" "ds_read_b64_tr_b16 a[170:171], v232 offset:25728\n\t"
                     "ds_read_b64_tr_b16 a[172:173], v232 offset:37376\n\t" "ds_read_b64_tr_b16 a[174:175], v232 offset:38144\n\t" "ds_read_b64_tr_b16 a[248:249], v232 offset:25088\n\t"
                     "ds_read_b64_tr_b16 a[250:251], v232 offset:25856\n\t" "ds_read_b64_tr_b16 a[252:253], v232 offset:37504\n\t" "ds_read_b64_tr_b16 a[254:255], v232 offset:38272\n\t" "s_waitcnt vmcnt(12) lgkmcnt(0)\n\t"
                     "s_barrier\n\t" "s_add_u32 s42, %21, 0x300000\n\t" "s_add_u32 s42, s44, s42\n\t" "s_addc_u32 s43, s45, 0\n\t" "s_add_u32 m0, %18, 0xc200\n\t" "s_nop 0\n\t" "global_load_lds_dwordx4 v224, s[42:43]\n\t"
                     "s_add_u32 m0, %18, 0xc600\n\t" "s_nop 0\n\t" "global_load_lds_dwordx4 v225, s[42:43]\n\t" "s_add_u32 m0, %18, 0xca00\n\t" "s_nop 0\n\t" "global_load_lds_dwordx4 v226, s[42:43]\n\t"
                     "s_add_u32 s42, %21, 0x318000\n\t" "s_add_u32 s42, s44, s42\n\t" "s_addc_u32 s43, s45, 0\n\t" "s_add_u32 m0, %18, 0xce20\n\t" "s_nop 0\n\t" "global_load_lds_dwordx4 v224, s[42:43]\n\t"
                     "s_add_u32 m0, %18, 0xd220\n\t" "s_nop 0\n\t" "global_load_lds_dwordx4 v225, s[42:43]\n\t" "s_add_u32 m0, %18, 0xd620\n\t" "s_nop 0\n\t" "global_load_lds_dwordx4 v226, s[42:43]\n\t"
                     "s_sub_u32 s42, 0x3c0000, %21\n\t" "s_add_u32 s42, s44, s42\n\t" "s_addc_u32 s43, s45, 0\n\t" "s_add_u32 m0, %18, 0x12300\n\t" "s_nop 0\n\t" "global_load_lds_dwordx4 v224, s[42:43]\n\t"
                     "s_add_u32 m0, %18, 0x12700\n\t" "s_nop 0\n\t" "global_load_lds_dwordx4 v225, s[42:43]\n\t" "s_add_u32 m0, %18, 0x12b00\n\t" "s_nop 0\n\t" "global_load_lds_dwordx4 v226, s[42:43]\n\t"
                     "s_sub_u32 s42, 0x3d8000, %21\n\t" "s_add_u32 s42, s44, s42\n\t" "s_addc_u32 s43, s45, 0\n\t" "s_add_u32 m0, %18, 0x12f20\n\t" "s_nop 0\n\t" "global_load_lds_dwordx4 v224, s[42:43]\n\t"
                     "s_add_u32 m0, %18, 0x13320\n\t" "s_nop 0\n\t" "global_load_lds_dwordx4 v225, s[42:43]\n\t" "s_add_u32 m0, %18, 0x13720\n\t" "s_nop 0\n\t" "global_load_lds_dwordx4 v226, s[42:43]\n\t"
                     "v_add_u32 v232, 0x18400, v227\n\t" "v_add_u32 v233, 0x18400, v228\n\t" "ds_read_b64_tr_b16 v[128:129], v232\n\t" "ds_read_b64_tr_b16 v[130:131], v232 offset:768\n\t"
                     "ds_read_b64_tr_b16 v[132:133], v232 offset:12416\n\t" "ds_read_b64_tr_b16 v[134:135], v232 offset:13184\n\t" "ds_read_b64_tr_b16 v[0:1], v232 offset:32\n\t"
                     "ds_read_b64_tr_b16 v[2:3], v232 offset:800\n\t" "ds_read_b64_tr_b16 v[4:5], v232 offset:12448\n\t" "ds_read_b64_tr_b16 v[6:7], v232 offset:13216\n\t"
                     "ds_read_b64_tr_b16 v[136:137], v233 offset:64\n\t" "ds_read_b64_tr_b16 v[138:139], v233 offset:832\n\t" "ds_read_b64_tr_b16 v[140:141], v233 offset:12480\n\t"
                     "ds_read_b64_tr_b16 v[142:143], v233 offset:13248\n\t" "ds_read_b64_tr_b16 v[160:161], v232 offset:24832\n\t" "ds_read_b64_tr_b16 v[162:163], v232 offset:25600\n\t"
                     "ds_read_b64_tr_b16 v[164:165], v232 offset:37248\n\t" "ds_read_b64_tr_b16 v[166:167], v232 offset:38016\n\t" "ds_read_b64_tr_b16 v[32:33], v232 offset:24864\n\t"
                     "ds_read_b64_tr_b16 v[34:35], v232 offset:25632\n\t" "ds_read_b64_tr_b16 v[36:37], v232 offset:37280\n\t" "ds_read_b64_tr_b16 v[38:39], v232 offset:38048\n\t"
                     "ds_read_b64_tr_b16 v[168:169], v233 offset:24896\n\t" "ds_read_b64_tr_b16 v[170:171], v233 offset:25664\n\t" "ds_read_b64_tr_b16 v[172:173], v233 offset:37312\n\t"
                     "ds_read_b64_tr_b16 v[174:175], v233 offset:38080\n\t" "s_waitcnt vmcnt(12) lgkmcnt(0)\n\t" "s_barrier\n\t" "v_mov_b32 %14, 0\n\t" "global_load_dwordx2 %11, v231, %19 sc1\n\t"
                     "global_load_dwordx2 %12, %14, %19 offset:512 sc1\n\t" "global_load_dwordx2 %13, %14, %19 offset:1024 sc1\n\t" "s_add_u32 s42, %21, 0x480000\n\t" "s_add_u32 s42, s44, s42\n\t"
                     "s_addc_u32 s43, s45, 0\n\t" "s_add_u32 m0, %18, 0x18400\n\t" "s_nop 0\n\t" "global_load_lds_dwordx4 v224, s[42:43]\n\t" "s_add_u32 m0, %18, 0x18800\n\t" "s_nop 0\n\t"
                     "global_load_lds_dwordx4 v225, s[42:43]\n\t" "s_add_u32 m0, %18, 0x18c00\n\t" "s_nop 0\n\t" "global_load_lds_dwordx4 v226, s[42:43]\n\t" "s_add_u32 s42, %21, 0x498000\n\t" "s_add_u32 s42, s44, s42\n\t"
                     "s_addc_u32 s43, s45, 0\n\t" "s_add_u32 m0, %18, 0x19020\n\t" "s_nop 0\n\t" "global_load_lds_dwordx4 v224, s[42:43]\n\t" "s_add_u32 m0, %18, 0x19420\n\t" "s_nop 0\n\t"
                     "global_load_lds_dwordx4 v225, s[42:43]\n\t" "s_add_u32 m0, %18, 0x19820\n\t" "s_nop 0\n\t" "global_load_lds_dwordx4 v226, s[42:43]\n\t" "s_sub_u32 s42, 0x540000, %21\n\t" "s_add_u32 s42, s44, s42\n\t"
                     "s_addc_u32 s43, s45, 0\n\t" "s_add_u32 m0, %18, 0x1e500\n\t" "s_nop 0\n\t" "global_load_lds_dwordx4 v224, s[42:43]\n\t" "s_add_u32 m0, %18, 0x1e900\n\t" "s_nop 0\n\t"
                     "global_load_lds_dwordx4 v225, s[42:43]\n\t" "s_add_u32 m0, %18, 0x1ed00\n\t" "s_nop 0\n\t" "global_load_lds_dwordx4 v226, s[42:43]\n\t" "s_sub_u32 s42, 0x558000, %21\n\t" "s_add_u32 s42, s44, s42\n\t"
                     "s_addc_u32 s43, s45, 0\n\t" "s_add_u32 m0, %18, 0x1f120\n\t" "s_nop 0\n\t" "global_load_lds_dwordx4 v224, s[42:43]\n\t" "s_add_u32 m0, %18, 0x1f520\n\t" "s_nop 0\n\t"
                     "global_load_lds_dwordx4 v225, s[42:43]\n\t" "s_add_u32 m0, %18, 0x1f920\n\t" "s_nop 0\n\t" "global_load_lds_dwordx4 v226, s[42:43]\n\t" "v_add_u32 v232, 0x0, v227\n\t" "v_add_u32 v233, 0x0, v228\n\t"
                     "ds_read_b64_tr_b16 v[144:145], v232\n\t" "ds_read_b64_tr_b16 v[146:147], v232 offset:768\n\t" "ds_read_b64_tr_b16 v[148:149], v232 offset:12416\n\t"
                     "ds_read_b64_tr_b16 v[150:151], v232 offset:13184\n\t" "ds_read_b64_tr_b16 v[16:17], v232 offset:32\n\t" "ds_read_b64_tr_b16 v[18:19], v232 offset:800\n\t"
                     "ds_read_b64_tr_b16 v[20:21], v232 offset:12448\n\t" "ds_read_b64_tr_b16 v[22:23], v232 offset:13216\n\t" "ds_read_b64_tr_b16 v[152:153], v233 offset:64\n\t"
                     "ds_read_b64_tr_b16 v[154:155], v233 offset:832\n\t" "ds_read_b64_tr_b16 v[156:157], v233 offset:12480\n\t" "ds_read_b64_tr_b16 v[158:159], v233 offset:13248\n\t"
                     "ds_read_b64_tr_b16 v[176:177], v232 offset:24832\n\t" "ds_read_b64_tr_b16 v[178:179], v232 offset:25600\n\t" "ds_read_b64_tr_b16 v[180:181], v232 offset:37248\n\t"
                     "ds_read_b64_tr_b16 v[182:183], v232 offset:38016\n\t" "ds_read_b64_tr_b16 v[48:49], v232 offset:24864\n\t" "ds_read_b64_tr_b16 v[50:51], v232 offset:25632\n\t"
                     "ds_read_b64_tr_b16 v[52:53], v232 offset:37280\n\t" "ds_read_b64_tr_b16 v[54:55], v232 offset:38048\n\t" "ds_read_b64_tr_b16 v[184:185], v233 offset:24896\n\t"
                     "ds_read_b64_tr_b16 v[186:187], v233 offset:25664\n\t" "ds_read_b64_tr_b16 v[188:189], v233 offset:37312\n\t" "ds_read_b64_tr_b16 v[190:191], v233 offset:38080\n\t" "s_waitcnt vmcnt(12) lgkmcnt(0)\n\t"
                     "s_barrier\n\t" "s_add_u32 s42, %21, 0x600000\n\t" "s_add_u32 s42, s44, s42\n\t" "s_addc_u32 s43, s45, 0\n\t" "s_add_u32 m0, %18, 0x0\n\t" "s_nop 0\n\t" "global_load_lds_dwordx4 v224, s[42:43]\n\t"
                     "s_add_u32 m0, %18, 0x400\n\t" "s_nop 0\n\t" "global_load_lds_dwordx4 v225, s[42:43]\n\t" "s_add_u32 m0, %18, 0x800\n\t" "s_nop 0\n\t" "global_load_lds_dwordx4 v226, s[42:43]\n\t"
                     "s_add_u32 s42, %21, 0x618000\n\t" "s_add_u32 s42, s44, s42\n\t" "s_addc_u32 s43, s45, 0\n\t" "s_add_u32 m0, %18, 0xc20\n\t" "s_nop 0\n\t" "global_load_lds_dwordx4 v224, s[42:43]\n\t"
                     "s_add_u32 m0, %18, 0x1020\n\t" "s_nop 0\n\t" "global_load_lds_dwordx4 v225, s[42:43]\n\t" "s_add_u32 m0, %18, 0x1420\n\t" "s_nop 0\n\t" "global_load_lds_dwordx4 v226, s[42:43]\n\t"
                     "s_sub_u32 s42, 0x6c0000, %21\n\t" "s_add_u32 s42, s44, s42\n\t" "s_addc_u32 s43, s45, 0\n\t" "s_add_u32 m0, %18, 0x6100\n\t" "s_nop 0\n\t" "global_load_lds_dwordx4 v224, s[42:43]\n\t"
                     "s_add_u32 m0, %18, 0x6500\n\t" "s_nop 0\n\t" "global_load_lds_dwordx4 v225, s[42:43]\n\t" "s_add_u32 m0, %18, 0x6900\n\t" "s_nop 0\n\t" "global_load_lds_dwordx4 v226, s[42:43]\n\t"
                     "s_sub_u32 s42, 0x6d8000, %21\n\t" "s_add_u32 s42, s44, s42\n\t" "s_addc_u32 s43, s45, 0\n\t" "s_add_u32 m0, %18, 0x6d20\n\t" "s_nop 0\n\t" "global_load_lds_dwordx4 v224, s[42:43]\n\t"
                     "s_add_u32 m0, %18, 0x7120\n\t" "s_nop 0\n\t" "global_load_lds_dwordx4 v225, s[42:43]\n\t" "s_add_u32 m0, %18, 0x7520\n\t" "s_nop 0\n\t" "global_load_lds_dwordx4 v226, s[42:43]\n\t"
                     "global_load_dword %14, %14, %20 sc1\n\t" "v_add_u32 v232, 0xc200, v227\n\t" "v_add_u32 v233, 0xc200, v228\n\t" "ds_read_b64_tr_b16 v[192:193], v232\n\t"
                     "ds_read_b64_tr_b16 v[194:195], v232 offset:768\n\t" "ds_read_b64_tr_b16 v[196:197], v232 offset:12416\n\t" "ds_read_b64_tr_b16 v[198:199], v232 offset:13184\n\t"
                     "ds_read_b64_tr_b16 v[64:65], v232 offset:32\n\t" "ds_read_b64_tr_b16 v[66:67], v232 offset:800\n\t" "ds_read_b64_tr_b16 v[68:69], v232 offset:12448\n\t"
                     "ds_read_b64_tr_b16 v[70:71], v232 offset:13216\n\t" "ds_read_b64_tr_b16 v[200:201], v233 offset:64\n\t" "ds_read_b64_tr_b16 v[202:203], v233 offset:832\n\t"
                     "ds_read_b64_tr_b16 v[204:205], v233 offset:12480\n\t" "ds_read_b64_tr_b16 v[206:207], v233 offset:13248\n\t" "ds_read_b64_tr_b16 v[208:209], v232 offset:24832\n\t"
                     "ds_read_b64_tr_b16 v[210:211], v232 offset:25600\n\t" "ds_read_b64_tr_b16 v[212:213], v232 offset:37248\n\t" "ds_read_b64_tr_b16 v[214:215], v232 offset:38016\n\t"
                     "ds_read_b64_tr_b16 v[80:81], v232 offset:24864\n\t" "ds_read_b64_tr_b16 v[82:83], v232 offset:25632\n\t" "ds_read_b64_tr_b16 v[84:85], v232 offset:37280\n\t"
                     "ds_read_b64_tr_b16 v[86:87], v232 offset:38048\n\t" "ds_read_b64_tr_b16 v[216:217], v233 offset:24896\n\t" "ds_read_b64_tr_b16 v[218:219], v233 offset:25664\n\t"
                     "ds_read_b64_tr_b16 v[220:221], v233 offset:37312\n\t" "ds_read_b64_tr_b16 v[222:223], v233 offset:38080\n\t" "s_waitcnt vmcnt(13) lgkmcnt(0)\n\t" "s_barrier\n\t" "v_add_u32 v232, 0x18400, v227\n\t"
                     "v_add_u32 v233, 0x18400, v228\n\t" "ds_read_b64_tr_b16 a[0:1], v232\n\t" "ds_read_b64_tr_b16 a[2:3], v232 offset:768\n\t" "ds_read_b64_tr_b16 a[4:5], v232 offset:12416\n\t"
                     "ds_read_b64_tr_b16 a[6:7], v232 offset:13184\n\t" "ds_read_b64_tr_b16 v[96:97], v232 offset:32\n\t" "ds_read_b64_tr_b16 v[98:99], v232 offset:800\n\t"
                     "ds_read_b64_tr_b16 v[100:101], v232 offset:12448\n\t" "ds_read_b64_tr_b16 v[102:103], v232 offset:13216\n\t" "ds_read_b64_tr_b16 a[8:9], v233 offset:64\n\t"
                     "ds_read_b64_tr_b16 a[10:11], v233 offset:832\n\t" "ds_read_b64_tr_b16 a[12:13], v233 offset:12480\n\t" "ds_read_b64_tr_b16 a[14:15], v233 offset:13248\n\t"
                     "ds_read_b64_tr_b16 a[16:17], v232 offset:24832\n\t" "ds_read_b64_tr_b16 a[18:19], v232 offset:25600\n\t" "ds_read_b64_tr_b16 a[20:21], v232 offset:37248\n\t"
                     "ds_read_b64_tr_b16 a[22:23], v232 offset:38016\n\t" "ds_read_b64_tr_b16 v[112:113], v232 offset:24864\n\t" "ds_read_b64_tr_b16 v[114:115], v232 offset:25632\n\t"
                     "ds_read_b64_tr_b16 v[116:117], v232 offset:37280\n\t" "ds_read_b64_tr_b16 v[118:119], v232 offset:38048\n\t" "ds_read_b64_tr_b16 a[24:25], v233 offset:24896\n\t"
                     "ds_read_b64_tr_b16 a[26:27], v233 offset:25664\n\t" "ds_read_b64_tr_b16 a[28:29], v233 offset:37312\n\t" "ds_read_b64_tr_b16 a[30:31], v233 offset:38080\n\t" "s_waitcnt vmcnt(0) lgkmcnt(0)\n\t"
                     "s_barrier\n\t" "v_add_u32 v232, 0x0, v227\n\t" "v_add_u32 v233, 0x0, v228\n\t" "ds_read_b64_tr_b16 a[32:33], v232\n\t" "ds_read_b64_tr_b16 a[34:35], v232 offset:768\n\t"
                     "ds_read_b64_tr_b16 a[36:37], v232 offset:12416\n\t" "ds_read_b64_tr_b16 a[38:39], v232 offset:13184\n\t" "ds_read_b64_tr_b16 a[40:41], v232 offset:32\n\t"
                     "ds_read_b64_tr_b16 a[42:43], v232 offset:800\n\t" "ds_read_b64_tr_b16 a[44:45], v232 offset:12448\n\t" "ds_read_b64_tr_b16 a[46:47], v232 offset:13216\n\t"
                     "ds_read_b64_tr_b16 a[48:49], v233 offset:64\n\t" "ds_read_b64_tr_b16 a[50:51], v233 offset:832\n\t" "ds_read_b64_tr_b16 a[52:53], v233 offset:12480\n\t"
                     "ds_read_b64_tr_b16 a[54:55], v233 offset:13248\n\t" "ds_read_b64_tr_b16 a[64:65], v232 offset:24832\n\t" "ds_read_b64_tr_b16 a[66:67], v232 offset:25600\n\t"
                     "ds_read_b64_tr_b16 a[68:69], v232 offset:37248\n\t" "ds_read_b64_tr_b16 a[70:71], v232 offset:38016\n\t" "ds_read_b64_tr_b16 a[72:73], v232 offset:24864\n\t"
                     "ds_read_b64_tr_b16 a[74:75], v232 offset:25632\n\t" "ds_read_b64_tr_b16 a[76:77], v232 offset:37280\n\t" "ds_read_b64_tr_b16 a[78:79], v232 offset:38048\n\t"
                     "ds_read_b64_tr_b16 a[80:81], v233 offset:24896\n\t" "ds_read_b64_tr_b16 a[82:83], v233 offset:25664\n\t" "ds_read_b64_tr_b16 a[84:85], v233 offset:37312\n\t"
                     "ds_read_b64_tr_b16 a[86:87], v233 offset:38080\n\t" "s_waitcnt lgkmcnt(0)\n\t" "s_barrier\n\t" "ds_write_b128 v230, v[0:3]\n\t" "ds_write_b128 v230, v[4:7] offset:1024\n\t"
                     "ds_write_b128 v230, v[8:11] offset:2048\n\t" "ds_write_b128 v230, v[12:15] offset:3072\n\t" "ds_write_b128 v230, v[16:19] offset:4096\n\t" "ds_write_b128 v230, v[20:23] offset:5120\n\t"
                     "ds_write_b128 v230, v[24:27] offset:6144\n\t" "ds_write_b128 v230, v[28:31] offset:7168\n\t" "ds_write_b128 v230, v[32:35] offset:8192\n\t" "ds_write_b128 v230, v[36:39] offset:9216\n\t"
                     "ds_write_b128 v230, v[40:43] offset:10240\n\t" "ds_write_b128 v230, v[44:47] offset:11264\n\t" "ds_write_b128 v230, v[48:51] offset:12288\n\t" "ds_write_b128 v230, v[52:55] offset:13312\n\t"
                     "ds_write_b128 v230, v[56:59] offset:14336\n\t" "ds_write_b128 v230, v[60:63] offset:15360\n\t" "ds_write_b128 v230, v[64:67] offset:16384\n\t" "ds_write_b128 v230, v[68:71] offset:17408\n\t"
                     "ds_write_b128 v230, v[72:75] offset:18432\n\t" "ds_write_b128 v230, v[76:79] offset:19456\n\t" "ds_write_b128 v230, v[80:83] offset:20480\n\t" "ds_write_b128 v230, v[84:87] offset:21504\n\t"
                     "ds_write_b128 v230, v[88:91] offset:22528\n\t" "ds_write_b128 v230, v[92:95] offset:23552\n\t" "ds_write_b128 v230, v[96:99] offset:24576\n\t" "ds_write_b128 v230, v[100:103] offset:25600\n\t"
                     "ds_write_b128 v230, v[104:107] offset:26624\n\t" "ds_write_b128 v230, v[108:111] offset:27648\n\t" "ds_write_b128 v230, v[112:115] offset:28672\n\t" "ds_write_b128 v230, v[116:119] offset:29696\n\t"
                     "ds_write_b128 v230, v[120:123] offset:30720\n\t" "ds_write_b128 v230, v[124:127] offset:31744\n\t"
                     : "=&{a[0:31]}"(wa[0]), "=&{a[32:63]}"(wa[1]), "=&{a[64:95]}"(wa[2]), "=&{a[96:127]}"(wa[3]), "=&{a[128:159]}"(wa[4]), "=&{a[160:191]}"(wa[5]), "=&{a[192:223]}"(wa[6]), "=&{a[224:255]}"(wa[7]), "=&{v[128:159]}"(wv[0]), "=&{v[160:191]}"(wv[1]), "=&{v[192:223]}"(wv[2]), "=&v"(y.r), "=&v"(y.g), "=&v"(y.e), "=&v"(kfl)
                     : "{v[224:231]}"(pa), "{s[44:45]}"(p.wq + h * DH * 2 + (size_t)(16 * w) * (HD * 2)), "{s[46:47]}"(p.wk + h * DH * 2 + (size_t)(16 * w) * (HD * 2)),
                       "s"(lds + 2 * w * GS), "s"(p.ws + OFF_LAT), "s"(fl + (F_K + h) * 128), "s"((h & 4) * 16 * HD * 2)
                     : "v0", "v1", "v2", "v3", "v4", "v5", "v6", "v7", "v8", "v9", "v10", "v11", "v12", "v13", "v14", "v15", "v16", "v17", "v18", "v19", "v20", "v21", "v22", "v23", "v24", "v25", "v26", "v27", "v28", "v29", "v30", "v31", "v32", "v33", "v34", "v35", "v36", "v37", "v38", "v39", "v40", "v41", "v42", "v43", "v44", "v45", "v46", "v47", "v48", "v49", "v50", "v51", "v52", "v53", "v54", "v55", "v56", "v57", "v58", "v59", "v60", "v61", "v62", "v63", "v64", "v65", "v66", "v67", "v68", "v69", "v70", "v71", "v72", "v73", "v74", "v75", "v76", "v77", "v78", "v79", "v80", "v81", "v82", "v83", "v84", "v85", "v86", "v87", "v88", "v89", "v90", "v91", "v92", "v93", "v94", "v95", "v96", "v97", "v98", "v99", "v100", "v101", "v102", "v103", "v104", "v105", "v106", "v107", "v108", "v109", "v110", "v111", "v112", "v113", "v114", "v115", "v116", "v117", "v118", "v119", "v120", "v121", "v122", "v123", "v124", "v125", "v126", "v127", "v232", "v233", "s42", "s43", "scc", "memory");
        const u64 t = sync_tag(y.e);
        const bool rdy = __builtin_amdgcn_ballot_w64((y.r[0] & ~1u) != (u32)t || y.r[1] != (u32)(t >> 32) || y.g[0] != (u32)t) == 0 && uni((int)kfl) != -1;
        if (rdy) kxdma();
        sd = sync_end(y, p.ws, phi, w == 0);
        if (!rdy) { flag_wait(fl, F_K + h, 1); kxdma(); }
        if (w == 3) rdy_add(kfl, fl);
        kxp += NH * 12288;
    }
    const u32 sidx = (lane & 2) ? 0xEEEEu : 0x4444u;
    const u32 moff = lane * 16;
    u32 kxa = kxl - 16 + (n & 1) * 6160 + (16 * g + (n & 8) + 2 * (n & 2) + ((n >> 1) & 2)) * 16;
    const u64 mb0 = 0xffff0000ffff0000ull;
    const int tp = (3 * w + (w & 1)) >> 1, ts = 1 + 3 * (w >> 1);
    u32 qw0 = lds + SL_IMG + tp * 512 + lane * 8, qw1 = lds + SL_IMG + ts * 512 + lane * 8 + 4 * (w & 1);
    const u32 kw0 = lds + SL_IMG + 3072 + w * 288 + n * 16 + 4 * g;
    const u32 qr0 = lds + SL_IMG + (g >> 1) * 512 + w * 128 + 64 * (g & 1) + 8 * qq;
    const u32 kr0 = lds + SL_IMG + 3072 + (qq >> 1) * 288 + g * 64 + (qq & 1) * 16 + 8 * (pp & 1);
    const u32 xso = 32768 + (h >> 4) * 16384 + (h & 15) * 512 + (w >> 1) * 256 + (n >> 3) * 64 + (n & 4) * 32 + (w & 1) * 32 + (g & 1) * 16 + (n & 3) * 4 + (g >> 1) * 8192;
    const u32 pso = (n < 8 ? 20480 + (h >> 1) * 256 + (n & 2) * 16 + (h & 1) * 8 + (n & 4) : 18432 + (h >> 2) * 256 + (h & 1) * 8 + (h & 2) * 2) + w * 64 + (g & 1) * 16 + (g >> 1) * 8192;
    const u64 psm = 0x01aa01aa01aa01aaull;
    const u32 kmo = (u32)(((n >> 3) * 4 + g) * 128 + (n & 7) * 16);
    u32x16 ad = u32x16{moff, sidx, lfa, kxa, qw0, qw1, kw0, kr0 + 1152 * min(w, 2) + (pp >> 1) * 576, qr0, kr0, xso, pso, 0x3dd53b94u, kmo, kmo, kmo};
    asm volatile("" : "+{v[224:239]}"(ad));
    const char* memp = p.ws + mslot(sd, 0);
    char* kmp = p.ws + OFF_KM + h * 3072 + min(w, 2) * 1024;
    const float kscale = 0.07216878364870322f;
    int budget = 30000;
    Clock clk; clk.init();
    if (w == 3) { asm volatile("s_waitcnt vmcnt(0)" : "+v"(kfl) : : "memory"); rdy_end(kfl, fl, cfg.s1 >> 8 & 255); }
    u32x32 wk[3];
    asm volatile("" : "={v[64:95]}"(wk[0]), "={v[96:127]}"(wk[1]), "={v[32:63]}"(wk[2]));
    const int kfo = (F_K + 32 + h) * 128;
    for (int s = 0; s < NSEG; ++s) {
        int tries = 0;
        u64 bad, tp8, en = ~0ull;
        u32 tt;
        int kf;
        while (true) {
            const u32 tgt = s ? clk.sched(phi, 0, cfg.G, tries ? 2 : 0) : mt();
            asm volatile("s_cmp_lg_u32 %12, 0\n\t" "s_cbranch_scc1 .Lrt_s\n\t" "s_cmp_lg_u32 %13, 0\n\t" "s_cbranch_scc1 .Lsp_s\n\t" ".Lrt_s:\n\t" "ds_read_b128 v[64:67], v226\n\t" "v_mov_b64 v[40:41], 0\n\t"
                         "ds_read_b128 v[68:71], v226 offset:1024\n\t" "v_mov_b64 v[42:43], 0\n\t" "ds_read_b128 v[72:75], v226 offset:2048\n\t" "v_mov_b64 v[44:45], 0\n\t" "ds_read_b128 v[76:79], v226 offset:3072\n\t"
                         "v_mov_b64 v[46:47], 0\n\t" "ds_read_b128 v[80:83], v226 offset:4096\n\t" "v_mov_b64 v[48:49], 0\n\t" "ds_read_b128 v[84:87], v226 offset:5120\n\t" "v_mov_b64 v[50:51], 0\n\t"
                         "ds_read_b128 v[88:91], v226 offset:6144\n\t" "v_mov_b64 v[52:53], 0\n\t" "ds_read_b128 v[92:95], v226 offset:7168\n\t" "v_mov_b64 v[54:55], 0\n\t" "ds_read_b128 v[96:99], v226 offset:8192\n\t"
                         "v_mov_b64 v[56:57], 0\n\t" "ds_read_b128 v[100:103], v226 offset:9216\n\t" "v_mov_b64 v[58:59], 0\n\t" "ds_read_b128 v[104:107], v226 offset:10240\n\t" "v_mov_b64 v[60:61], 0\n\t"
                         "ds_read_b128 v[108:111], v226 offset:11264\n\t" "v_mov_b64 v[62:63], 0\n\t" "ds_read_b128 v[112:115], v226 offset:12288\n\t" "ds_read_b128 v[116:119], v226 offset:13312\n\t"
                         "ds_read_b128 v[120:123], v226 offset:14336\n\t" "ds_read_b128 v[124:127], v226 offset:15360\n\t" ".p2align 5\n\t" "s_nop 0\n\t" "s_nop 0\n\t" "s_nop 0\n\t" "s_nop 0\n\t" "s_nop 0\n\t" ".Lsp_s:\n\t"
                         "s_memtime vcc\n\t" "s_waitcnt lgkmcnt(0)\n\t" "s_sub_i32 s42, %4, vcc_lo\n\t" "s_cmp_gt_i32 s42, 80\n\t" "s_cbranch_scc1 .Lsp_s\n\t" "s_max_i32 s42, s42, 0\n\t" "s_sub_i32 s42, 92, s42\n\t"
                         "s_and_b32 s42, s42, -4\n\t" "s_getpc_b64 vcc\n\t" "s_add_u32 vcc_lo, vcc_lo, s42\n\t" "s_addc_u32 vcc_hi, vcc_hi, 0\n\t" "s_setpc_b64 vcc\n\t" "s_nop 0\n\t" "s_nop 0\n\t" "s_nop 0\n\t" "s_nop 0\n\t"
                         "s_nop 0\n\t" "s_nop 0\n\t" "s_nop 0\n\t" "s_nop 0\n\t" "s_nop 0\n\t" "s_nop 0\n\t" "s_nop 0\n\t" "s_nop 0\n\t" "s_nop 0\n\t" "s_nop 0\n\t" "s_nop 0\n\t" "s_nop 0\n\t" "s_nop 0\n\t" "s_nop 0\n\t"
                         "s_nop 0\n\t" "s_nop 0\n\t" "global_load_dwordx4 v[0:3], v224, %5 offset:-4096 sc1\n\t" "v_add_u32 v238, 0x4000, v224\n\t" "s_nop 9\n\t" "global_load_dwordx4 v[4:7], v224, %5 offset:-2048 sc1\n\t"
                         "s_nop 15\n\t" "global_load_dwordx4 v[8:11], v224, %6 offset:-3072 sc1\n\t" "s_nop 10\n\t" "global_load_dwordx4 v[12:15], v224, %6 offset:-1024 sc1\n\t" "s_nop 10\n\t"
                         "global_load_dwordx4 v[16:19], v224, %5 sc1\n\t" "s_nop 10\n\t" "global_load_dwordx4 v[20:23], v224, %6 offset:1024 sc1\n\t" "s_nop 10\n\t" "global_load_dwordx4 v[24:27], v224, %5 offset:2048 sc1\n\t"
                         "s_nop 10\n\t" "global_load_dwordx4 v[28:31], v224, %6 offset:3072 sc1\n\t" "s_nop 10\n\t" "global_load_dwordx4 v[32:35], v238, %5 offset:-4096 sc1\n\t" "s_nop 10\n\t"
                         "global_load_dwordx4 v[36:39], v238, %6 offset:-3072 sc1\n\t" "s_cmp_lg_u32 %12, 0\n\t" "s_cbranch_scc1 .Lnb_s\n\t" "s_waitcnt vmcnt(10)\n\t" "s_barrier\n\t" ".Lnb_s:\n\t" "s_waitcnt vmcnt(9)\n\t"
                         "v_smfmac_f32_16x16x64_bf16 v[40:43], v[0:3], v[128:135], v225\n\t" "v_smfmac_f32_16x16x64_bf16 v[44:47], v[0:3], v[64:71], v225\n\t" "ds_read_b128 v[64:67], v226 offset:16384\n\t"
                         "v_smfmac_f32_16x16x64_bf16 v[48:51], v[0:3], v[136:143], v225\n\t" "ds_read_b128 v[68:71], v226 offset:17408\n\t" "v_smfmac_f32_16x16x64_bf16 v[52:55], v[0:3], v[72:79], v225\n\t"
                         "ds_read_b128 v[72:75], v226 offset:18432\n\t" "s_waitcnt vmcnt(8)\n\t" "v_smfmac_f32_16x16x64_bf16 v[40:43], v[4:7], v[144:151], v225\n\t" "ds_read_b128 v[76:79], v226 offset:19456\n\t"
                         "v_smfmac_f32_16x16x64_bf16 v[44:47], v[4:7], v[80:87], v225\n\t" "ds_read_b128 v[80:83], v226 offset:20480\n\t" "v_smfmac_f32_16x16x64_bf16 v[48:51], v[4:7], v[152:159], v225\n\t"
                         "ds_read_b128 v[84:87], v226 offset:21504\n\t" "v_smfmac_f32_16x16x64_bf16 v[52:55], v[4:7], v[88:95], v225\n\t" "ds_read_b128 v[88:91], v226 offset:22528\n\t" "s_waitcnt vmcnt(7)\n\t"
                         "v_smfmac_f32_16x16x64_bf16 v[40:43], v[8:11], v[160:167], v225\n\t" "ds_read_b128 v[92:95], v226 offset:23552\n\t" "v_smfmac_f32_16x16x64_bf16 v[44:47], v[8:11], v[96:103], v225\n\t"
                         "ds_read_b128 v[96:99], v226 offset:24576\n\t" "v_smfmac_f32_16x16x64_bf16 v[48:51], v[8:11], v[168:175], v225\n\t" "ds_read_b128 v[100:103], v226 offset:25600\n\t"
                         "v_smfmac_f32_16x16x64_bf16 v[52:55], v[8:11], v[104:111], v225\n\t" "ds_read_b128 v[104:107], v226 offset:26624\n\t" "s_waitcnt vmcnt(6)\n\t"
                         "v_smfmac_f32_16x16x64_bf16 v[40:43], v[12:15], v[176:183], v225\n\t" "ds_read_b128 v[108:111], v226 offset:27648\n\t" "v_smfmac_f32_16x16x64_bf16 v[44:47], v[12:15], v[112:119], v225\n\t"
                         "ds_read_b128 v[112:115], v226 offset:28672\n\t" "v_smfmac_f32_16x16x64_bf16 v[48:51], v[12:15], v[184:191], v225\n\t" "ds_read_b128 v[116:119], v226 offset:29696\n\t"
                         "v_smfmac_f32_16x16x64_bf16 v[52:55], v[12:15], v[120:127], v225\n\t" "ds_read_b128 v[120:123], v226 offset:30720\n\t" "s_waitcnt vmcnt(5)\n\t"
                         "v_smfmac_f32_16x16x64_bf16 v[40:43], v[16:19], v[192:199], v225\n\t" "ds_read_b128 v[124:127], v226 offset:31744\n\t" "s_waitcnt lgkmcnt(14)\n\t"
                         "v_smfmac_f32_16x16x64_bf16 v[44:47], v[16:19], v[64:71], v225\n\t" "ds_read_b128 v[64:67], v227\n\t" "v_smfmac_f32_16x16x64_bf16 v[48:51], v[16:19], v[200:207], v225\n\t"
                         "ds_read_b128 v[68:71], v227 offset:1024\n\t" "s_waitcnt lgkmcnt(14)\n\t" "v_smfmac_f32_16x16x64_bf16 v[52:55], v[16:19], v[72:79], v225\n\t" "ds_read_b128 v[72:75], v227 offset:16\n\t"
                         "s_waitcnt vmcnt(4)\n\t" "v_smfmac_f32_16x16x64_bf16 v[40:43], v[20:23], v[208:215], v225\n\t" "ds_read_b128 v[76:79], v227 offset:1040\n\t" "s_waitcnt lgkmcnt(14)\n\t"
                         "v_smfmac_f32_16x16x64_bf16 v[44:47], v[20:23], v[80:87], v225\n\t" "ds_read_b128 v[80:83], v227 offset:2048\n\t" "v_smfmac_f32_16x16x64_bf16 v[48:51], v[20:23], v[216:223], v225\n\t"
                         "ds_read_b128 v[84:87], v227 offset:3072\n\t" "s_waitcnt lgkmcnt(14)\n\t" "v_smfmac_f32_16x16x64_bf16 v[52:55], v[20:23], v[88:95], v225\n\t" "ds_read_b128 v[88:91], v227 offset:2064\n\t"
                         "s_waitcnt vmcnt(3)\n\t" "v_smfmac_f32_16x16x64_bf16 v[40:43], v[24:27], a[0:7], v225\n\t" "ds_read_b128 v[92:95], v227 offset:3088\n\t" "s_waitcnt lgkmcnt(14)\n\t"
                         "v_smfmac_f32_16x16x64_bf16 v[44:47], v[24:27], v[96:103], v225\n\t" "ds_read_b128 v[96:99], v227 offset:4096\n\t" "v_smfmac_f32_16x16x64_bf16 v[48:51], v[24:27], a[8:15], v225\n\t"
                         "ds_read_b128 v[100:103], v227 offset:5120\n\t" "s_waitcnt lgkmcnt(14)\n\t" "v_smfmac_f32_16x16x64_bf16 v[52:55], v[24:27], v[104:111], v225\n\t" "ds_read_b128 v[104:107], v227 offset:4112\n\t"
                         "s_waitcnt vmcnt(2)\n\t" "v_smfmac_f32_16x16x64_bf16 v[40:43], v[28:31], a[16:23], v225\n\t" "ds_read_b128 v[108:111], v227 offset:5136\n\t" "s_waitcnt lgkmcnt(14)\n\t"
                         "v_smfmac_f32_16x16x64_bf16 v[44:47], v[28:31], v[112:119], v225\n\t" "v_smfmac_f32_16x16x64_bf16 v[48:51], v[28:31], a[24:31], v225\n\t" "s_waitcnt lgkmcnt(12)\n\t"
                         "v_smfmac_f32_16x16x64_bf16 v[52:55], v[28:31], v[120:127], v225\n\t" "s_waitcnt vmcnt(1)\n\t" "v_smfmac_f32_16x16x64_bf16 v[40:43], v[32:35], a[32:39], v225\n\t" "v_mov_b64 v[112:113], 0\n\t"
                         "v_smfmac_f32_16x16x64_bf16 v[44:47], v[32:35], a[40:47], v225\n\t" "v_mov_b64 v[114:115], 0\n\t" "v_smfmac_f32_16x16x64_bf16 v[48:51], v[32:35], a[48:55], v225\n\t" "v_mov_b64 v[116:117], 0\n\t"
                         "v_smfmac_f32_16x16x64_bf16 v[52:55], v[32:35], a[56:63], v225\n\t" "v_mov_b64 v[118:119], 0\n\t" "s_waitcnt vmcnt(0)\n\t" "v_smfmac_f32_16x16x64_bf16 v[40:43], v[36:39], a[64:71], v225\n\t"
                         "v_mov_b64 v[120:121], 0\n\t" "v_smfmac_f32_16x16x64_bf16 v[44:47], v[36:39], a[72:79], v225\n\t" "v_mov_b64 v[122:123], 0\n\t" "v_smfmac_f32_16x16x64_bf16 v[48:51], v[36:39], a[80:87], v225\n\t"
                         "v_mov_b64 v[124:125], 0\n\t" "v_smfmac_f32_16x16x64_bf16 v[52:55], v[36:39], a[88:95], v225\n\t" "v_add_f32 v40, v40, v42\n\t" "v_add_f32 v41, v41, v43\n\t"
                         "v_smfmac_f32_16x16x64_bf16 v[56:59], v[0:3], a[96:103], v225\n\t" "v_cmp_u_f32 vcc, v40, v41\n\t" "v_smfmac_f32_16x16x64_bf16 v[56:59], v[4:7], a[104:111], v225\n\t"
                         "v_cvt_pk_bf16_f32 v42, v40, v41\n\t" "v_add_f32 v44, v44, v46\n\t" "v_smfmac_f32_16x16x64_bf16 v[56:59], v[8:11], a[112:119], v225\n\t" "s_and_b64 vcc, vcc, %11\n\t" "s_cbranch_scc1 .Lsk_s\n\t"
                         "v_smfmac_f32_16x16x64_bf16 v[56:59], v[12:15], a[120:127], v225\n\t" "v_add_f32 v45, v45, v47\n\t" "v_cvt_pk_bf16_f32 v43, v44, v45\n\t"
                         "v_smfmac_f32_16x16x64_bf16 v[56:59], v[16:19], a[128:135], v225\n\t" "ds_write_b64 v228, v[42:43]\n\t" "v_add_f32 v48, v48, v50\n\t"
                         "v_smfmac_f32_16x16x64_bf16 v[56:59], v[20:23], a[136:143], v225\n\t" "v_add_f32 v49, v49, v51\n\t" "v_cvt_pk_bf16_f32 v46, v48, v49\n\t"
                         "v_smfmac_f32_16x16x64_bf16 v[56:59], v[24:27], a[144:151], v225\n\t" "ds_write_b32 v229, v46\n\t" "v_add_f32 v52, v52, v54\n\t" "v_smfmac_f32_16x16x64_bf16 v[56:59], v[28:31], a[152:159], v225\n\t"
                         "v_add_f32 v53, v53, v55\n\t" "v_cvt_pk_bf16_f32 v54, v52, v53\n\t" "v_smfmac_f32_16x16x64_bf16 v[56:59], v[32:35], a[160:167], v225\n\t" "ds_write_b32 v230, v54\n\t"
                         "v_smfmac_f32_16x16x64_bf16 v[56:59], v[36:39], a[168:175], v225\n\t" "v_smfmac_f32_16x16x64_bf16 v[60:63], v[0:3], a[176:183], v225\n\t"
                         "v_smfmac_f32_16x16x64_bf16 v[60:63], v[4:7], a[184:191], v225\n\t" "v_add_f32 v56, v56, v58\n\t" "v_smfmac_f32_16x16x64_bf16 v[60:63], v[8:11], a[192:199], v225\n\t" "s_waitcnt lgkmcnt(0)\n\t"
                         "s_barrier\n\t" "v_smfmac_f32_16x16x64_bf16 v[60:63], v[12:15], a[200:207], v225\n\t" "ds_read_b64_tr_b16 v[40:41], v232\n\t" "v_add_f32 v57, v57, v59\n\t"
                         "v_smfmac_f32_16x16x64_bf16 v[60:63], v[16:19], a[208:215], v225\n\t" "ds_read_b64_tr_b16 v[42:43], v232 offset:32\n\t" "v_cvt_pk_bf16_f32 v58, v56, v57\n\t"
                         "v_smfmac_f32_16x16x64_bf16 v[60:63], v[20:23], a[216:223], v225\n\t" "ds_write_b32 v230, v58 offset:1152\n\t" "v_smfmac_f32_16x16x64_bf16 v[60:63], v[24:27], a[224:231], v225\n\t"
                         "ds_read_b64_tr_b16 v[44:45], v232 offset:1024\n\t" "ds_read_b64_tr_b16 v[46:47], v232 offset:1056\n\t" "v_smfmac_f32_16x16x64_bf16 v[60:63], v[28:31], a[232:239], v225\n\t"
                         "ds_read_b64_tr_b16 v[48:49], v232 offset:2048\n\t" "ds_read_b64_tr_b16 v[50:51], v232 offset:2080\n\t" "v_smfmac_f32_16x16x64_bf16 v[60:63], v[32:35], a[240:247], v225\n\t"
                         "ds_read_b64_tr_b16 v[0:1], v233\n\t" "ds_read_b64_tr_b16 v[2:3], v233 offset:32\n\t" "v_smfmac_f32_16x16x64_bf16 v[60:63], v[36:39], a[248:255], v225\n\t"
                         "ds_read_b64_tr_b16 v[4:5], v233 offset:576\n\t" "ds_read_b64_tr_b16 v[6:7], v233 offset:608\n\t" "s_waitcnt lgkmcnt(9)\n\t" "v_smfmac_f32_16x16x64_bf16 v[112:115], v[40:43], v[64:71], v225\n\t"
                         "s_waitcnt lgkmcnt(8)\n\t" "s_barrier\n\t" "v_smfmac_f32_16x16x64_bf16 v[116:119], v[40:43], v[72:79], v225\n\t" "ds_read_b64_tr_b16 v[8:9], v233 offset:1152\n\t" "s_waitcnt lgkmcnt(7)\n\t"
                         "v_add_f32 v60, v60, v62\n\t" "v_add_f32 v61, v61, v63\n\t" "v_smfmac_f32_16x16x64_bf16 v[112:115], v[44:47], v[80:87], v225\n\t" "ds_read_b64_tr_b16 v[10:11], v233 offset:1184\n\t"
                         "v_cvt_pk_bf16_f32 v62, v60, v61\n\t" "v_smfmac_f32_16x16x64_bf16 v[116:119], v[44:47], v[88:95], v225\n\t" "ds_write_b32 v230, v62 offset:2304\n\t" "s_waitcnt lgkmcnt(7)\n\t"
                         "v_smfmac_f32_16x16x64_bf16 v[112:115], v[48:51], v[96:103], v225\n\t" "ds_read_b64_tr_b16 v[12:13], v233 offset:1728\n\t" "v_smfmac_f32_16x16x64_bf16 v[116:119], v[48:51], v[104:111], v225\n\t"
                         "ds_read_b64_tr_b16 v[14:15], v233 offset:1760\n\t" "s_waitcnt lgkmcnt(5)\n\t" "v_smfmac_f32_16x16x64_bf16 v[120:123], v[40:43], v[0:7], v225\n\t" "v_cndmask_b32 v24, v112, v113, %8\n\t"
                         "v_cndmask_b32 v25, v114, v115, %8\n\t" "v_add_f32 v24, v24, v25\n\t" "v_pk_add_f32 v[26:27], v[116:117], v[118:119]\n\t" "v_cndmask_b32 v25, v26, v27, %8\n\t" "v_max_f32 v26, v24, v25\n\t"
                         "s_waitcnt lgkmcnt(2)\n\t" "s_barrier\n\t" "ds_read_b64_tr_b16 v[16:17], v233 offset:2304\n\t" "v_max_f32_dpp v27, v26, v26 row_ror:4 row_mask:0xf bank_mask:0xf\n\t"
                         "ds_read_b64_tr_b16 v[18:19], v233 offset:2336\n\t" "v_max_f32_dpp v27, v26, v27 row_ror:8 row_mask:0xf bank_mask:0xf\n\t" "v_max_f32_dpp v27, v26, v27 row_ror:12 row_mask:0xf bank_mask:0xf\n\t"
                         "ds_read_b64_tr_b16 v[20:21], v233 offset:2880\n\t" "ds_read_b64_tr_b16 v[22:23], v233 offset:2912\n\t" "v_max_f32_dpp v30, v27, v27 row_ror:1 row_mask:0xf bank_mask:0xf\n\t"
                         "v_max_f32_dpp v30, v27, v30 row_ror:2 row_mask:0xf bank_mask:0xf\n\t" "v_max_f32_dpp v30, v27, v30 row_ror:3 row_mask:0xf bank_mask:0xf\n\t" "s_waitcnt lgkmcnt(4)\n\t"
                         "v_pk_add_f32 v[24:25], v[24:25], v[30:31] op_sel_hi:[1,0] neg_lo:[0,1] neg_hi:[0,1]\n\t" "v_pk_mul_f32 v[24:25], v[24:25], v[236:237] op_sel_hi:[1,0]\n\t" "v_exp_f32 v24, v24\n\t"
                         "v_exp_f32 v25, v25\n\t" "v_mul_f32 v31, %10, v30\n\t" "v_cvt_pk_bf16_f32 v28, |v24|, |v25|\n\t" "v_smfmac_f32_16x16x64_bf16 v[120:123], v[44:47], v[8:15], v225\n\t"
                         "global_store_dword v234, v28, %7 offset:-4096 sc1\n\t" "s_waitcnt lgkmcnt(0)\n\t" "v_add_f32 v26, v24, v25\n\t" "v_smfmac_f32_16x16x64_bf16 v[120:123], v[48:51], v[16:23], v225\n\t"
                         "s_nop 0\n\t" "v_add_f32_dpp v27, v26, v26 row_ror:4 row_mask:0xf bank_mask:0xf\n\t" "v_add_f32_dpp v27, v26, v27 row_ror:8 row_mask:0xf bank_mask:0xf\n\t"
                         "v_add_f32_dpp v27, v26, v27 row_ror:12 row_mask:0xf bank_mask:0xf\n\t" "s_nop 1\n\t" "v_add_f32_dpp v29, v27, v27 row_ror:1 row_mask:0xf bank_mask:0xf\n\t"
                         "v_pk_add_f32 v[24:25], v[120:121], v[122:123]\n\t" "v_cndmask_b32 v24, v24, v25, %8\n\t" "v_max_f32 v26, v24, v30\n\t" "v_add_f32_dpp v29, v27, v29 row_ror:2 row_mask:0xf bank_mask:0xf\n\t"
                         "v_add_f32_dpp v29, v27, v29 row_ror:3 row_mask:0xf bank_mask:0xf\n\t" "v_max_f32_dpp v27, v26, v26 row_ror:1 row_mask:0xf bank_mask:0xf\n\t"
                         "v_max_f32_dpp v27, v26, v27 row_ror:2 row_mask:0xf bank_mask:0xf\n\t" "v_max_f32_dpp v27, v26, v27 row_ror:3 row_mask:0xf bank_mask:0xf\n\t" "v_mul_f32 v24, %10, v24\n\t"
                         "v_mul_f32 v29, 0x3e000000, v29\n\t" "v_max_f32_dpp v26, v27, v27 row_ror:4 row_mask:0xf bank_mask:0xf\n\t" "v_fma_f32 v24, -v26, %10, v24\n\t" "v_fma_f32 v25, -v26, %10, v31\n\t"
                         "v_exp_f32 v24, v24\n\t" "v_exp_f32 v25, v25\n\t" "s_nop 0\n\t" "v_fma_f32 v26, v25, v29, v24\n\t" "v_mov_b32_dpp v124, v24 quad_perm:[0,1,2,3] row_mask:0xf bank_mask:0x3\n\t"
                         "v_mov_b32_dpp v125, v24 quad_perm:[1,0,3,2] row_mask:0xf bank_mask:0x3\n\t" "v_add_f32_dpp v27, v26, v26 row_ror:1 row_mask:0xf bank_mask:0xf\n\t"
                         "v_add_f32_dpp v27, v26, v27 row_ror:2 row_mask:0xf bank_mask:0xf\n\t" "v_add_f32_dpp v27, v26, v27 row_ror:3 row_mask:0xf bank_mask:0xf\n\t"
                         "v_mov_b32_dpp v124, v25 quad_perm:[0,1,2,3] row_mask:0x5 bank_mask:0x4\n\t" "v_mov_b32_dpp v125, v25 quad_perm:[0,1,2,3] row_mask:0xa bank_mask:0x4\n\t"
                         "v_add_f32_dpp v26, v27, v27 row_ror:4 row_mask:0xf bank_mask:0xf\n\t" "v_rcp_f32 v26, v26\n\t" "s_mov_b64 exec, %9\n\t" "v_pk_mul_f32 v[124:125], v[124:125], v[26:27] op_sel_hi:[1,0]\n\t"
                         "v_cvt_pk_bf16_f32 v24, |v124|, |v125|\n\t" "global_store_dword v235, v24, %7 offset:-4096 sc1\n\t" "s_mov_b64 exec, -1\n\t" "s_memrealtime %1\n\t" "s_load_dword %2, %14, %13 offset:%15\n\t"
                         "ds_read_b64_tr_b16 v[36:37], v231\n\t" "ds_read_b64_tr_b16 v[38:39], v231 offset:32\n\t" "v_readfirstlane_b32 %3, v237\n\t" "s_mov_b32 m0, %17\n\t" "v_mov_b64 v[40:41], 0\n\t"
                         "v_mov_b64 v[42:43], 0\n\t" "v_mov_b64 v[44:45], 0\n\t" "v_mov_b64 v[46:47], 0\n\t" "v_mov_b64 v[48:49], 0\n\t" "v_mov_b64 v[50:51], 0\n\t" "v_mov_b64 v[52:53], 0\n\t" "v_mov_b64 v[54:55], 0\n\t"
                         "v_mov_b64 v[56:57], 0\n\t" "v_mov_b64 v[58:59], 0\n\t" "v_mov_b64 v[60:61], 0\n\t" "v_mov_b64 v[62:63], 0\n\t" "v_mov_b32 v237, %18\n\t" "s_waitcnt lgkmcnt(0)\n\t" "ds_read_b128 v[64:67], v226\n\t"
                         "ds_read_b128 v[68:71], v226 offset:1024\n\t" "ds_read_b128 v[72:75], v226 offset:2048\n\t" "ds_read_b128 v[76:79], v226 offset:3072\n\t" "ds_read_b128 v[80:83], v226 offset:4096\n\t"
                         "ds_read_b128 v[84:87], v226 offset:5120\n\t" "s_cmp_eq_u32 %3, -1\n\t" "s_cbranch_scc1 .Lnk_s\n\t" "global_load_lds_dwordx4 v224, %16 sc1\n\t" "global_load_lds_dwordx4 v224, %16 offset:1024 sc1\n\t"
                         "global_load_lds_dwordx4 v224, %16 offset:2048 sc1\n\t" ".Lnk_s:\n\t" "global_store_dwordx4 v239, v[36:39], %21 sc1\n\t" "ds_read_b128 v[88:91], v226 offset:6144\n\t"
                         "ds_read_b128 v[92:95], v226 offset:7168\n\t" "ds_read_b128 v[96:99], v226 offset:8192\n\t" "ds_read_b128 v[100:103], v226 offset:9216\n\t" "ds_read_b128 v[104:107], v226 offset:10240\n\t"
                         "ds_read_b128 v[108:111], v226 offset:11264\n\t" "ds_read_b128 v[112:115], v226 offset:12288\n\t" "ds_read_b128 v[116:119], v226 offset:13312\n\t" "ds_read_b128 v[120:123], v226 offset:14336\n\t"
                         "ds_read_b128 v[124:127], v226 offset:15360\n\t" "s_and_b64 exec, %19, 1\n\t" "s_cbranch_execz .Lnf_s\n\t" "s_sleep 4\n\t" "global_load_dword v237, v237, %20 sc1\n\t" ".Lnf_s:\n\t"
                         "s_mov_b64 exec, -1\n\t" ".Lsk_s:\n\t" "s_mov_b64 %0, vcc\n\t"
                         : "=&s"(bad), "=&s"(tp8), "=&s"(tt), "=&s"(kf)
                         : "s"(uni((int)tgt)), "s"(memp + (h & 4) * 256), "s"(memp - (h & 4) * 256), "s"((u64)memp & ~8192ull), "s"(mb0), "s"(psm), "s"(kscale * 1.4426950408889634f), "s"(en), "s"(tries), "s"(4 * s),
                           "s"(__builtin_amdgcn_kernarg_segment_ptr()), "n"(TT_OFF), "s"(kxp), "s"(kxw), "s"(kfo + (s < 6 ? 0 : (s + 2) >> 3) * 4096), "s"(0x0040404040404044ull >> s), "s"(fl), "s"(kmp),
                           "{v[224:239]}"(ad), "{v[64:95]}"(wk[0]), "{v[96:127]}"(wk[1]), "{v[32:63]}"(wk[2]),
                           "{a[0:31]}"(wa[0]), "{a[32:63]}"(wa[1]), "{a[64:95]}"(wa[2]), "{a[96:127]}"(wa[3]), "{a[128:159]}"(wa[4]), "{a[160:191]}"(wa[5]), "{a[192:223]}"(wa[6]), "{a[224:255]}"(wa[7]), "{v[128:159]}"(wv[0]), "{v[160:191]}"(wv[1]), "{v[192:223]}"(wv[2])
                         : "v0", "v1", "v2", "v3", "v4", "v5", "v6", "v7", "v8", "v9", "v10", "v11", "v12", "v13", "v14", "v15", "v16", "v17", "v18", "v19", "v20", "v21", "v22", "v23", "v24", "v25", "v26", "v27", "v28", "v29", "v30", "v31", "vcc", "scc", "s42", "memory");
            if (__builtin_expect(bad == 0, 1)) break;
            waitvm<0>();
            if (((++tries & 15) == 0 && !unwritten(memp, ad[0])) || --budget < 0) en = 0;
            phi += (u32)cfg.G;
        }
        if (__builtin_expect(kf == -1, 0)) { flag_wait(fl, F_K + pblk(s + 1) * 32 + h, 1); kxdma(); }
        kxp += s < NSEG - 2 ? NH * 12288 : 0;
        clk.calib();
        if (__builtin_expect(s >= NSEG - 2, 0)) {
            waitvm<0>();
            barrier();
            if (w == 3) flag_add(fl, F_PROG + 8 * s + (h & 7), 1);
        }
        if (s == 0) { phi = go_wg(fl, h, w, lds + SL_PF - 16) - (tt >> 16); edge(clk.r1, clk.m1); clk.calib(); }
        follow(phi, (int)(tt >> 16) - (cfg.dV >> 24), (u32)tp8, cfg.G);
        phi += tt & 0xffffu;
        memp = p.ws + mslot(sd, s + 1); kmp += NH * 3072;
    }
    if (h == 0 && w == 0) {
        flag_wait(fl, F_ACK, 256);
        Sync y;
        sync_ld(y, p.ws);
        asm volatile("s_waitcnt vmcnt(0)" : "+v"(y.r), "+v"(y.g), "+v"(y.e) : : "memory");
        const u64 t = ((u64)y.e[1] << 32 | y.e[0]) + 1;
        if (lanex() == 0) gst8(p.ws + OFF_LAT, 1024, u32x2{(u32)t, (u32)(t >> 32)});
    }
    asm volatile(".Lkm_last:\n\ts_waitcnt vmcnt(0)" : : : "memory");
}

DI void proj_role(const Ptrs& p, const ChainCfg cfg, int h, u32 lds, int w) {
    u32x32 wa[8], wv[4];
    u32 phi;
    int sd;
    {
        const int lane = lanex(), tid = 64 * w + lane;
        const int g = lane >> 4, qq = (lane >> 2) & 3, pp = lane & 3;
        constexpr int GS = 3104;
        auto dvo = [&](int i) { const int ix = i * 64 + lane; return (u32)((ix / 24) * (HD * 2) + (ix % 24) * 16); };
        auto ovo = [&](int i) { const int u = i * 64 + lane, r = u / 80; return (u32)(r * 1280 + (((u % 80) ^ (2 * (r ^ (w & 2)))) << 4)); };
        const u32 tra = lds + (2 * (g >> 1) + (qq >> 1)) * GS + (4 * (g & 1) + (qq & 1)) * 384 + 96 * w + 32 * (w & 1) + 8 * pp;
        const u32 tro = (4 * g + (qq & 1) + 16 * (qq >> 1)) * 1280 + 8 * pp + ((u32)(2 * w ^ 2 * ((4 * g + qq) & 7)) << 4);
        u32x16 pa = u32x16{dvo(0), dvo(1), dvo(2), tra, tra - 96 * (w & 1), tro, lds + (u32)((w & 1) * 24576 + (w >> 1) * 99328 + lane * 16), (u32)(lane & 31) * 8,
                           ovo(0), ovo(1), ovo(2), ovo(3), ovo(4), tro, tro, tro};
        Sync y;
        asm volatile("s_nop 4\n\t" "s_add_u32 s42, %22, 0x0\n\t" "s_add_u32 s42, s44, s42\n\t" "s_addc_u32 s43, s45, 0\n\t" "s_add_u32 m0, %18, 0x0\n\t" "s_nop 0\n\t" "global_load_lds_dwordx4 v224, s[42:43]\n\t"
                     "s_add_u32 m0, %18, 0x400\n\t" "s_nop 0\n\t" "global_load_lds_dwordx4 v225, s[42:43]\n\t" "s_add_u32 m0, %18, 0x800\n\t" "s_nop 0\n\t" "global_load_lds_dwordx4 v226, s[42:43]\n\t"
                     "s_add_u32 s42, %22, 0x18000\n\t" "s_add_u32 s42, s44, s42\n\t" "s_addc_u32 s43, s45, 0\n\t" "s_add_u32 m0, %18, 0xc20\n\t" "s_nop 0\n\t" "global_load_lds_dwordx4 v224, s[42:43]\n\t"
                     "s_add_u32 m0, %18, 0x1020\n\t" "s_nop 0\n\t" "global_load_lds_dwordx4 v225, s[42:43]\n\t" "s_add_u32 m0, %18, 0x1420\n\t" "s_nop 0\n\t" "global_load_lds_dwordx4 v226, s[42:43]\n\t"
                     "s_sub_u32 s42, 0xc0000, %22\n\t" "s_add_u32 s42, s44, s42\n\t" "s_addc_u32 s43, s45, 0\n\t" "s_add_u32 m0, %18, 0x6100\n\t" "s_nop 0\n\t" "global_load_lds_dwordx4 v224, s[42:43]\n\t"
                     "s_add_u32 m0, %18, 0x6500\n\t" "s_nop 0\n\t" "global_load_lds_dwordx4 v225, s[42:43]\n\t" "s_add_u32 m0, %18, 0x6900\n\t" "s_nop 0\n\t" "global_load_lds_dwordx4 v226, s[42:43]\n\t"
                     "s_sub_u32 s42, 0xd8000, %22\n\t" "s_add_u32 s42, s44, s42\n\t" "s_addc_u32 s43, s45, 0\n\t" "s_add_u32 m0, %18, 0x6d20\n\t" "s_nop 0\n\t" "global_load_lds_dwordx4 v224, s[42:43]\n\t"
                     "s_add_u32 m0, %18, 0x7120\n\t" "s_nop 0\n\t" "global_load_lds_dwordx4 v225, s[42:43]\n\t" "s_add_u32 m0, %18, 0x7520\n\t" "s_nop 0\n\t" "global_load_lds_dwordx4 v226, s[42:43]\n\t"
                     "s_add_u32 s42, %22, 0x180000\n\t" "s_add_u32 s42, s44, s42\n\t" "s_addc_u32 s43, s45, 0\n\t" "s_add_u32 m0, %18, 0xc200\n\t" "s_nop 0\n\t" "global_load_lds_dwordx4 v224, s[42:43]\n\t"
                     "s_add_u32 m0, %18, 0xc600\n\t" "s_nop 0\n\t" "global_load_lds_dwordx4 v225, s[42:43]\n\t" "s_add_u32 m0, %18, 0xca00\n\t" "s_nop 0\n\t" "global_load_lds_dwordx4 v226, s[42:43]\n\t"
                     "s_add_u32 s42, %22, 0x198000\n\t" "s_add_u32 s42, s44, s42\n\t" "s_addc_u32 s43, s45, 0\n\t" "s_add_u32 m0, %18, 0xce20\n\t" "s_nop 0\n\t" "global_load_lds_dwordx4 v224, s[42:43]\n\t"
                     "s_add_u32 m0, %18, 0xd220\n\t" "s_nop 0\n\t" "global_load_lds_dwordx4 v225, s[42:43]\n\t" "s_add_u32 m0, %18, 0xd620\n\t" "s_nop 0\n\t" "global_load_lds_dwordx4 v226, s[42:43]\n\t"
                     "s_sub_u32 s42, 0x240000, %22\n\t" "s_add_u32 s42, s44, s42\n\t" "s_addc_u32 s43, s45, 0\n\t" "s_add_u32 m0, %18, 0x12300\n\t" "s_nop 0\n\t" "global_load_lds_dwordx4 v224, s[42:43]\n\t"
                     "s_add_u32 m0, %18, 0x12700\n\t" "s_nop 0\n\t" "global_load_lds_dwordx4 v225, s[42:43]\n\t" "s_add_u32 m0, %18, 0x12b00\n\t" "s_nop 0\n\t" "global_load_lds_dwordx4 v226, s[42:43]\n\t"
                     "s_sub_u32 s42, 0x258000, %22\n\t" "s_add_u32 s42, s44, s42\n\t" "s_addc_u32 s43, s45, 0\n\t" "s_add_u32 m0, %18, 0x12f20\n\t" "s_nop 0\n\t" "global_load_lds_dwordx4 v224, s[42:43]\n\t"
                     "s_add_u32 m0, %18, 0x13320\n\t" "s_nop 0\n\t" "global_load_lds_dwordx4 v225, s[42:43]\n\t" "s_add_u32 m0, %18, 0x13720\n\t" "s_nop 0\n\t" "global_load_lds_dwordx4 v226, s[42:43]\n\t"
                     "s_waitcnt vmcnt(12) lgkmcnt(0)\n\t" "s_barrier\n\t" "s_add_u32 s42, %22, 0x300000\n\t" "s_add_u32 s42, s44, s42\n\t" "s_addc_u32 s43, s45, 0\n\t" "s_add_u32 m0, %18, 0x18400\n\t" "s_nop 0\n\t"
                     "global_load_lds_dwordx4 v224, s[42:43]\n\t" "s_add_u32 m0, %18, 0x18800\n\t" "s_nop 0\n\t" "global_load_lds_dwordx4 v225, s[42:43]\n\t" "s_add_u32 m0, %18, 0x18c00\n\t" "s_nop 0\n\t"
                     "global_load_lds_dwordx4 v226, s[42:43]\n\t" "s_add_u32 s42, %22, 0x318000\n\t" "s_add_u32 s42, s44, s42\n\t" "s_addc_u32 s43, s45, 0\n\t" "s_add_u32 m0, %18, 0x19020\n\t" "s_nop 0\n\t"
                     "global_load_lds_dwordx4 v224, s[42:43]\n\t" "s_add_u32 m0, %18, 0x19420\n\t" "s_nop 0\n\t" "global_load_lds_dwordx4 v225, s[42:43]\n\t" "s_add_u32 m0, %18, 0x19820\n\t" "s_nop 0\n\t"
                     "global_load_lds_dwordx4 v226, s[42:43]\n\t" "s_sub_u32 s42, 0x3c0000, %22\n\t" "s_add_u32 s42, s44, s42\n\t" "s_addc_u32 s43, s45, 0\n\t" "s_add_u32 m0, %18, 0x1e500\n\t" "s_nop 0\n\t"
                     "global_load_lds_dwordx4 v224, s[42:43]\n\t" "s_add_u32 m0, %18, 0x1e900\n\t" "s_nop 0\n\t" "global_load_lds_dwordx4 v225, s[42:43]\n\t" "s_add_u32 m0, %18, 0x1ed00\n\t" "s_nop 0\n\t"
                     "global_load_lds_dwordx4 v226, s[42:43]\n\t" "s_sub_u32 s42, 0x3d8000, %22\n\t" "s_add_u32 s42, s44, s42\n\t" "s_addc_u32 s43, s45, 0\n\t" "s_add_u32 m0, %18, 0x1f120\n\t" "s_nop 0\n\t"
                     "global_load_lds_dwordx4 v224, s[42:43]\n\t" "s_add_u32 m0, %18, 0x1f520\n\t" "s_nop 0\n\t" "global_load_lds_dwordx4 v225, s[42:43]\n\t" "s_add_u32 m0, %18, 0x1f920\n\t" "s_nop 0\n\t"
                     "global_load_lds_dwordx4 v226, s[42:43]\n\t" "v_add_u32 v237, 0x0, v227\n\t" "v_add_u32 v238, 0x0, v228\n\t" "ds_read_b64_tr_b16 a[0:1], v237\n\t" "ds_read_b64_tr_b16 a[2:3], v237 offset:768\n\t"
                     "ds_read_b64_tr_b16 a[4:5], v237 offset:12416\n\t" "ds_read_b64_tr_b16 a[6:7], v237 offset:13184\n\t" "ds_read_b64_tr_b16 v[0:1], v237 offset:32\n\t" "ds_read_b64_tr_b16 v[2:3], v237 offset:800\n\t"
                     "ds_read_b64_tr_b16 v[4:5], v237 offset:12448\n\t" "ds_read_b64_tr_b16 v[6:7], v237 offset:13216\n\t" "ds_read_b64_tr_b16 a[8:9], v238 offset:64\n\t" "ds_read_b64_tr_b16 a[10:11], v238 offset:832\n\t"
                     "ds_read_b64_tr_b16 a[12:13], v238 offset:12480\n\t" "ds_read_b64_tr_b16 a[14:15], v238 offset:13248\n\t" "ds_read_b64_tr_b16 a[32:33], v237 offset:24832\n\t"
                     "ds_read_b64_tr_b16 a[34:35], v237 offset:25600\n\t" "ds_read_b64_tr_b16 a[36:37], v237 offset:37248\n\t" "ds_read_b64_tr_b16 a[38:39], v237 offset:38016\n\t"
                     "ds_read_b64_tr_b16 v[16:17], v237 offset:24864\n\t" "ds_read_b64_tr_b16 v[18:19], v237 offset:25632\n\t" "ds_read_b64_tr_b16 v[20:21], v237 offset:37280\n\t"
                     "ds_read_b64_tr_b16 v[22:23], v237 offset:38048\n\t" "ds_read_b64_tr_b16 a[40:41], v238 offset:24896\n\t" "ds_read_b64_tr_b16 a[42:43], v238 offset:25664\n\t"
                     "ds_read_b64_tr_b16 a[44:45], v238 offset:37312\n\t" "ds_read_b64_tr_b16 a[46:47], v238 offset:38080\n\t" "s_waitcnt vmcnt(12) lgkmcnt(0)\n\t" "s_barrier\n\t" "s_add_u32 s42, %22, 0x480000\n\t"
                     "s_add_u32 s42, s44, s42\n\t" "s_addc_u32 s43, s45, 0\n\t" "s_add_u32 m0, %18, 0x0\n\t" "s_nop 0\n\t" "global_load_lds_dwordx4 v224, s[42:43]\n\t" "s_add_u32 m0, %18, 0x400\n\t" "s_nop 0\n\t"
                     "global_load_lds_dwordx4 v225, s[42:43]\n\t" "s_add_u32 m0, %18, 0x800\n\t" "s_nop 0\n\t" "global_load_lds_dwordx4 v226, s[42:43]\n\t" "s_add_u32 s42, %22, 0x498000\n\t" "s_add_u32 s42, s44, s42\n\t"
                     "s_addc_u32 s43, s45, 0\n\t" "s_add_u32 m0, %18, 0xc20\n\t" "s_nop 0\n\t" "global_load_lds_dwordx4 v224, s[42:43]\n\t" "s_add_u32 m0, %18, 0x1020\n\t" "s_nop 0\n\t"
                     "global_load_lds_dwordx4 v225, s[42:43]\n\t" "s_add_u32 m0, %18, 0x1420\n\t" "s_nop 0\n\t" "global_load_lds_dwordx4 v226, s[42:43]\n\t" "s_sub_u32 s42, 0x540000, %22\n\t" "s_add_u32 s42, s44, s42\n\t"
                     "s_addc_u32 s43, s45, 0\n\t" "s_add_u32 m0, %18, 0x6100\n\t" "s_nop 0\n\t" "global_load_lds_dwordx4 v224, s[42:43]\n\t" "s_add_u32 m0, %18, 0x6500\n\t" "s_nop 0\n\t"
                     "global_load_lds_dwordx4 v225, s[42:43]\n\t" "s_add_u32 m0, %18, 0x6900\n\t" "s_nop 0\n\t" "global_load_lds_dwordx4 v226, s[42:43]\n\t" "s_sub_u32 s42, 0x558000, %22\n\t" "s_add_u32 s42, s44, s42\n\t"
                     "s_addc_u32 s43, s45, 0\n\t" "s_add_u32 m0, %18, 0x6d20\n\t" "s_nop 0\n\t" "global_load_lds_dwordx4 v224, s[42:43]\n\t" "s_add_u32 m0, %18, 0x7120\n\t" "s_nop 0\n\t"
                     "global_load_lds_dwordx4 v225, s[42:43]\n\t" "s_add_u32 m0, %18, 0x7520\n\t" "s_nop 0\n\t" "global_load_lds_dwordx4 v226, s[42:43]\n\t" "v_add_u32 v237, 0xc200, v227\n\t"
                     "v_add_u32 v238, 0xc200, v228\n\t" "ds_read_b64_tr_b16 a[16:17], v237\n\t" "ds_read_b64_tr_b16 a[18:19], v237 offset:768\n\t" "ds_read_b64_tr_b16 a[20:21], v237 offset:12416\n\t"
                     "ds_read_b64_tr_b16 a[22:23], v237 offset:13184\n\t" "ds_read_b64_tr_b16 v[8:9], v237 offset:32\n\t" "ds_read_b64_tr_b16 v[10:11], v237 offset:800\n\t"
                     "ds_read_b64_tr_b16 v[12:13], v237 offset:12448\n\t" "ds_read_b64_tr_b16 v[14:15], v237 offset:13216\n\t" "ds_read_b64_tr_b16 a[24:25], v238 offset:64\n\t"
                     "ds_read_b64_tr_b16 a[26:27], v238 offset:832\n\t" "ds_read_b64_tr_b16 a[28:29], v238 offset:12480\n\t" "ds_read_b64_tr_b16 a[30:31], v238 offset:13248\n\t"
                     "ds_read_b64_tr_b16 a[48:49], v237 offset:24832\n\t" "ds_read_b64_tr_b16 a[50:51], v237 offset:25600\n\t" "ds_read_b64_tr_b16 a[52:53], v237 offset:37248\n\t"
                     "ds_read_b64_tr_b16 a[54:55], v237 offset:38016\n\t" "ds_read_b64_tr_b16 v[24:25], v237 offset:24864\n\t" "ds_read_b64_tr_b16 v[26:27], v237 offset:25632\n\t"
                     "ds_read_b64_tr_b16 v[28:29], v237 offset:37280\n\t" "ds_read_b64_tr_b16 v[30:31], v237 offset:38048\n\t" "ds_read_b64_tr_b16 a[56:57], v238 offset:24896\n\t"
                     "ds_read_b64_tr_b16 a[58:59], v238 offset:25664\n\t" "ds_read_b64_tr_b16 a[60:61], v238 offset:37312\n\t" "ds_read_b64_tr_b16 a[62:63], v238 offset:38080\n\t" "s_waitcnt vmcnt(12) lgkmcnt(0)\n\t"
                     "s_barrier\n\t" "s_add_u32 s42, %22, 0x600000\n\t" "s_add_u32 s42, s44, s42\n\t" "s_addc_u32 s43, s45, 0\n\t" "s_add_u32 m0, %18, 0xc200\n\t" "s_nop 0\n\t" "global_load_lds_dwordx4 v224, s[42:43]\n\t"
                     "s_add_u32 m0, %18, 0xc600\n\t" "s_nop 0\n\t" "global_load_lds_dwordx4 v225, s[42:43]\n\t" "s_add_u32 m0, %18, 0xca00\n\t" "s_nop 0\n\t" "global_load_lds_dwordx4 v226, s[42:43]\n\t"
                     "s_add_u32 s42, %22, 0x618000\n\t" "s_add_u32 s42, s44, s42\n\t" "s_addc_u32 s43, s45, 0\n\t" "s_add_u32 m0, %18, 0xce20\n\t" "s_nop 0\n\t" "global_load_lds_dwordx4 v224, s[42:43]\n\t"
                     "s_add_u32 m0, %18, 0xd220\n\t" "s_nop 0\n\t" "global_load_lds_dwordx4 v225, s[42:43]\n\t" "s_add_u32 m0, %18, 0xd620\n\t" "s_nop 0\n\t" "global_load_lds_dwordx4 v226, s[42:43]\n\t"
                     "s_sub_u32 s42, 0x6c0000, %22\n\t" "s_add_u32 s42, s44, s42\n\t" "s_addc_u32 s43, s45, 0\n\t" "s_add_u32 m0, %18, 0x12300\n\t" "s_nop 0\n\t" "global_load_lds_dwordx4 v224, s[42:43]\n\t"
                     "s_add_u32 m0, %18, 0x12700\n\t" "s_nop 0\n\t" "global_load_lds_dwordx4 v225, s[42:43]\n\t" "s_add_u32 m0, %18, 0x12b00\n\t" "s_nop 0\n\t" "global_load_lds_dwordx4 v226, s[42:43]\n\t"
                     "s_sub_u32 s42, 0x6d8000, %22\n\t" "s_add_u32 s42, s44, s42\n\t" "s_addc_u32 s43, s45, 0\n\t" "s_add_u32 m0, %18, 0x12f20\n\t" "s_nop 0\n\t" "global_load_lds_dwordx4 v224, s[42:43]\n\t"
                     "s_add_u32 m0, %18, 0x13320\n\t" "s_nop 0\n\t" "global_load_lds_dwordx4 v225, s[42:43]\n\t" "s_add_u32 m0, %18, 0x13720\n\t" "s_nop 0\n\t" "global_load_lds_dwordx4 v226, s[42:43]\n\t"
                     "v_add_u32 v237, 0x18400, v227\n\t" "v_add_u32 v238, 0x18400, v228\n\t" "ds_read_b64_tr_b16 a[64:65], v237\n\t" "ds_read_b64_tr_b16 a[66:67], v237 offset:768\n\t"
                     "ds_read_b64_tr_b16 a[68:69], v237 offset:12416\n\t" "ds_read_b64_tr_b16 a[70:71], v237 offset:13184\n\t" "ds_read_b64_tr_b16 v[32:33], v237 offset:32\n\t"
                     "ds_read_b64_tr_b16 v[34:35], v237 offset:800\n\t" "ds_read_b64_tr_b16 v[36:37], v237 offset:12448\n\t" "ds_read_b64_tr_b16 v[38:39], v237 offset:13216\n\t"
                     "ds_read_b64_tr_b16 a[72:73], v238 offset:64\n\t" "ds_read_b64_tr_b16 a[74:75], v238 offset:832\n\t" "ds_read_b64_tr_b16 a[76:77], v238 offset:12480\n\t"
                     "ds_read_b64_tr_b16 a[78:79], v238 offset:13248\n\t" "ds_read_b64_tr_b16 a[80:81], v237 offset:24832\n\t" "ds_read_b64_tr_b16 a[82:83], v237 offset:25600\n\t"
                     "ds_read_b64_tr_b16 a[84:85], v237 offset:37248\n\t" "ds_read_b64_tr_b16 a[86:87], v237 offset:38016\n\t" "ds_read_b64_tr_b16 v[40:41], v237 offset:24864\n\t"
                     "ds_read_b64_tr_b16 v[42:43], v237 offset:25632\n\t" "ds_read_b64_tr_b16 v[44:45], v237 offset:37280\n\t" "ds_read_b64_tr_b16 v[46:47], v237 offset:38048\n\t"
                     "ds_read_b64_tr_b16 a[88:89], v238 offset:24896\n\t" "ds_read_b64_tr_b16 a[90:91], v238 offset:25664\n\t" "ds_read_b64_tr_b16 a[92:93], v238 offset:37312\n\t"
                     "ds_read_b64_tr_b16 a[94:95], v238 offset:38080\n\t" "s_waitcnt vmcnt(12) lgkmcnt(0)\n\t" "s_barrier\n\t" "s_add_u32 s42, s46, 0x0\n\t" "s_addc_u32 s43, s47, 0\n\t" "s_add_u32 m0, %20, 0x18400\n\t"
                     "s_nop 0\n\t" "global_load_lds_dwordx4 v232, s[42:43]\n\t" "s_add_u32 m0, %20, 0x18800\n\t" "s_nop 0\n\t" "global_load_lds_dwordx4 v233, s[42:43]\n\t" "s_add_u32 m0, %20, 0x18c00\n\t" "s_nop 0\n\t"
                     "global_load_lds_dwordx4 v234, s[42:43]\n\t" "s_add_u32 m0, %20, 0x19000\n\t" "s_nop 0\n\t" "global_load_lds_dwordx4 v235, s[42:43]\n\t" "s_add_u32 m0, %20, 0x19400\n\t" "s_nop 0\n\t"
                     "global_load_lds_dwordx4 v236, s[42:43]\n\t" "s_add_u32 s42, s46, 0x1400\n\t" "s_addc_u32 s43, s47, 0\n\t" "s_add_u32 m0, %20, 0x19800\n\t" "v_xor_b32 v224, 0x80, v232\n\t"
                     "global_load_lds_dwordx4 v224, s[42:43]\n\t" "s_add_u32 m0, %20, 0x19c00\n\t" "v_xor_b32 v224, 0x80, v233\n\t" "global_load_lds_dwordx4 v224, s[42:43]\n\t" "s_add_u32 m0, %20, 0x1a000\n\t"
                     "v_xor_b32 v224, 0x80, v234\n\t" "global_load_lds_dwordx4 v224, s[42:43]\n\t" "s_add_u32 m0, %20, 0x1a400\n\t" "v_xor_b32 v224, 0x80, v235\n\t" "global_load_lds_dwordx4 v224, s[42:43]\n\t"
                     "s_add_u32 m0, %20, 0x1a800\n\t" "v_xor_b32 v224, 0x80, v236\n\t" "global_load_lds_dwordx4 v224, s[42:43]\n\t" "v_add_u32 v237, 0x0, v227\n\t" "v_add_u32 v238, 0x0, v228\n\t"
                     "ds_read_b64_tr_b16 a[96:97], v237\n\t" "ds_read_b64_tr_b16 a[98:99], v237 offset:768\n\t" "ds_read_b64_tr_b16 a[100:101], v237 offset:12416\n\t" "ds_read_b64_tr_b16 a[102:103], v237 offset:13184\n\t"
                     "ds_read_b64_tr_b16 v[48:49], v237 offset:32\n\t" "ds_read_b64_tr_b16 v[50:51], v237 offset:800\n\t" "ds_read_b64_tr_b16 v[52:53], v237 offset:12448\n\t"
                     "ds_read_b64_tr_b16 v[54:55], v237 offset:13216\n\t" "ds_read_b64_tr_b16 a[104:105], v238 offset:64\n\t" "ds_read_b64_tr_b16 a[106:107], v238 offset:832\n\t"
                     "ds_read_b64_tr_b16 a[108:109], v238 offset:12480\n\t" "ds_read_b64_tr_b16 a[110:111], v238 offset:13248\n\t" "ds_read_b64_tr_b16 a[112:113], v237 offset:24832\n\t"
                     "ds_read_b64_tr_b16 a[114:115], v237 offset:25600\n\t" "ds_read_b64_tr_b16 a[116:117], v237 offset:37248\n\t" "ds_read_b64_tr_b16 a[118:119], v237 offset:38016\n\t"
                     "ds_read_b64_tr_b16 v[56:57], v237 offset:24864\n\t" "ds_read_b64_tr_b16 v[58:59], v237 offset:25632\n\t" "ds_read_b64_tr_b16 v[60:61], v237 offset:37280\n\t"
                     "ds_read_b64_tr_b16 v[62:63], v237 offset:38048\n\t" "ds_read_b64_tr_b16 a[120:121], v238 offset:24896\n\t" "ds_read_b64_tr_b16 a[122:123], v238 offset:25664\n\t"
                     "ds_read_b64_tr_b16 a[124:125], v238 offset:37312\n\t" "ds_read_b64_tr_b16 a[126:127], v238 offset:38080\n\t" "s_waitcnt vmcnt(10) lgkmcnt(0)\n\t" "s_barrier\n\t" "s_add_u32 s42, s46, 0xa000\n\t"
                     "s_addc_u32 s43, s47, 0\n\t" "s_add_u32 m0, %20, 0x0\n\t" "s_nop 0\n\t" "global_load_lds_dwordx4 v232, s[42:43]\n\t" "s_add_u32 m0, %20, 0x400\n\t" "s_nop 0\n\t"
                     "global_load_lds_dwordx4 v233, s[42:43]\n\t" "s_add_u32 m0, %20, 0x800\n\t" "s_nop 0\n\t" "global_load_lds_dwordx4 v234, s[42:43]\n\t" "s_add_u32 m0, %20, 0xc00\n\t" "s_nop 0\n\t"
                     "global_load_lds_dwordx4 v235, s[42:43]\n\t" "s_add_u32 m0, %20, 0x1000\n\t" "s_nop 0\n\t" "global_load_lds_dwordx4 v236, s[42:43]\n\t" "s_add_u32 s42, s46, 0xb400\n\t" "s_addc_u32 s43, s47, 0\n\t"
                     "s_add_u32 m0, %20, 0x1400\n\t" "v_xor_b32 v224, 0x80, v232\n\t" "global_load_lds_dwordx4 v224, s[42:43]\n\t" "s_add_u32 m0, %20, 0x1800\n\t" "v_xor_b32 v224, 0x80, v233\n\t"
                     "global_load_lds_dwordx4 v224, s[42:43]\n\t" "s_add_u32 m0, %20, 0x1c00\n\t" "v_xor_b32 v224, 0x80, v234\n\t" "global_load_lds_dwordx4 v224, s[42:43]\n\t" "s_add_u32 m0, %20, 0x2000\n\t"
                     "v_xor_b32 v224, 0x80, v235\n\t" "global_load_lds_dwordx4 v224, s[42:43]\n\t" "s_add_u32 m0, %20, 0x2400\n\t" "v_xor_b32 v224, 0x80, v236\n\t" "global_load_lds_dwordx4 v224, s[42:43]\n\t"
                     "v_add_u32 v237, 0xc200, v227\n\t" "v_add_u32 v238, 0xc200, v228\n\t" "ds_read_b64_tr_b16 a[128:129], v237\n\t" "ds_read_b64_tr_b16 a[130:131], v237 offset:768\n\t"
                     "ds_read_b64_tr_b16 a[132:133], v237 offset:12416\n\t" "ds_read_b64_tr_b16 a[134:135], v237 offset:13184\n\t" "ds_read_b64_tr_b16 a[136:137], v237 offset:32\n\t"
                     "ds_read_b64_tr_b16 a[138:139], v237 offset:800\n\t" "ds_read_b64_tr_b16 a[140:141], v237 offset:12448\n\t" "ds_read_b64_tr_b16 a[142:143], v237 offset:13216\n\t"
                     "ds_read_b64_tr_b16 a[144:145], v238 offset:64\n\t" "ds_read_b64_tr_b16 a[146:147], v238 offset:832\n\t" "ds_read_b64_tr_b16 a[148:149], v238 offset:12480\n\t"
                     "ds_read_b64_tr_b16 a[150:151], v238 offset:13248\n\t" "ds_read_b64_tr_b16 a[152:153], v237 offset:24832\n\t" "ds_read_b64_tr_b16 a[154:155], v237 offset:25600\n\t"
                     "ds_read_b64_tr_b16 a[156:157], v237 offset:37248\n\t" "ds_read_b64_tr_b16 a[158:159], v237 offset:38016\n\t" "ds_read_b64_tr_b16 a[160:161], v237 offset:24864\n\t"
                     "ds_read_b64_tr_b16 a[162:163], v237 offset:25632\n\t" "ds_read_b64_tr_b16 a[164:165], v237 offset:37280\n\t" "ds_read_b64_tr_b16 a[166:167], v237 offset:38048\n\t"
                     "ds_read_b64_tr_b16 a[168:169], v238 offset:24896\n\t" "ds_read_b64_tr_b16 a[170:171], v238 offset:25664\n\t" "ds_read_b64_tr_b16 a[172:173], v238 offset:37312\n\t"
                     "ds_read_b64_tr_b16 a[174:175], v238 offset:38080\n\t" "s_waitcnt vmcnt(10) lgkmcnt(0)\n\t" "s_barrier\n\t" "s_add_u32 s42, s46, 0x14000\n\t" "s_addc_u32 s43, s47, 0\n\t"
                     "s_add_u32 m0, %20, 0xc200\n\t" "s_nop 0\n\t" "global_load_lds_dwordx4 v232, s[42:43]\n\t" "s_add_u32 m0, %20, 0xc600\n\t" "s_nop 0\n\t" "global_load_lds_dwordx4 v233, s[42:43]\n\t"
                     "s_add_u32 m0, %20, 0xca00\n\t" "s_nop 0\n\t" "global_load_lds_dwordx4 v234, s[42:43]\n\t" "s_add_u32 m0, %20, 0xce00\n\t" "s_nop 0\n\t" "global_load_lds_dwordx4 v235, s[42:43]\n\t"
                     "s_add_u32 m0, %20, 0xd200\n\t" "s_nop 0\n\t" "global_load_lds_dwordx4 v236, s[42:43]\n\t" "s_add_u32 s42, s46, 0x15400\n\t" "s_addc_u32 s43, s47, 0\n\t" "s_add_u32 m0, %20, 0xd600\n\t"
                     "v_xor_b32 v224, 0x80, v232\n\t" "global_load_lds_dwordx4 v224, s[42:43]\n\t" "s_add_u32 m0, %20, 0xda00\n\t" "v_xor_b32 v224, 0x80, v233\n\t" "global_load_lds_dwordx4 v224, s[42:43]\n\t"
                     "s_add_u32 m0, %20, 0xde00\n\t" "v_xor_b32 v224, 0x80, v234\n\t" "global_load_lds_dwordx4 v224, s[42:43]\n\t" "s_add_u32 m0, %20, 0xe200\n\t" "v_xor_b32 v224, 0x80, v235\n\t"
                     "global_load_lds_dwordx4 v224, s[42:43]\n\t" "s_add_u32 m0, %20, 0xe600\n\t" "v_xor_b32 v224, 0x80, v236\n\t" "global_load_lds_dwordx4 v224, s[42:43]\n\t" "s_add_u32 s42, %21, 0x18400\n\t"
                     "v_xor_b32 v237, 0x0, v229\n\t" "v_add_u32 v237, s42, v237\n\t" "v_xor_b32 v238, 0x40, v229\n\t" "v_add_u32 v238, s42, v238\n\t" "v_xor_b32 v239, 0x80, v229\n\t" "v_add_u32 v239, s42, v239\n\t"
                     "v_xor_b32 v226, 0xc0, v229\n\t" "v_add_u32 v226, s42, v226\n\t" "ds_read_b64_tr_b16 a[176:177], v237\n\t" "ds_read_b64_tr_b16 a[178:179], v238 offset:2560\n\t"
                     "ds_read_b64_tr_b16 a[200:201], v239\n\t" "ds_read_b64_tr_b16 a[202:203], v226 offset:2560\n\t" "ds_read_b64_tr_b16 a[224:225], v237 offset:256\n\t"
                     "ds_read_b64_tr_b16 a[226:227], v238 offset:2816\n\t" "ds_read_b64_tr_b16 a[248:249], v239 offset:256\n\t" "ds_read_b64_tr_b16 a[250:251], v226 offset:2816\n\t"
                     "ds_read_b64_tr_b16 v[112:113], v237 offset:512\n\t" "ds_read_b64_tr_b16 v[114:115], v238 offset:3072\n\t" "ds_read_b64_tr_b16 v[136:137], v239 offset:512\n\t"
                     "ds_read_b64_tr_b16 v[138:139], v226 offset:3072\n\t" "ds_read_b64_tr_b16 v[160:161], v237 offset:768\n\t" "ds_read_b64_tr_b16 v[162:163], v238 offset:3328\n\t"
                     "ds_read_b64_tr_b16 v[184:185], v239 offset:768\n\t" "ds_read_b64_tr_b16 v[186:187], v226 offset:3328\n\t" "ds_read_b64_tr_b16 v[208:209], v237 offset:1024\n\t"
                     "ds_read_b64_tr_b16 v[210:211], v238 offset:3584\n\t" "ds_read_b64_tr_b16 v[72:73], v239 offset:1024\n\t" "ds_read_b64_tr_b16 v[74:75], v226 offset:3584\n\t" "s_waitcnt vmcnt(10) lgkmcnt(0)\n\t"
                     "s_barrier\n\t" "s_add_u32 s42, s46, 0x1e000\n\t" "s_addc_u32 s43, s47, 0\n\t" "s_add_u32 m0, %20, 0x18400\n\t" "s_nop 0\n\t" "global_load_lds_dwordx4 v232, s[42:43]\n\t"
                     "s_add_u32 m0, %20, 0x18800\n\t" "s_nop 0\n\t" "global_load_lds_dwordx4 v233, s[42:43]\n\t" "s_add_u32 m0, %20, 0x18c00\n\t" "s_nop 0\n\t" "global_load_lds_dwordx4 v234, s[42:43]\n\t"
                     "s_add_u32 m0, %20, 0x19000\n\t" "s_nop 0\n\t" "global_load_lds_dwordx4 v235, s[42:43]\n\t" "s_add_u32 m0, %20, 0x19400\n\t" "s_nop 0\n\t" "global_load_lds_dwordx4 v236, s[42:43]\n\t"
                     "s_add_u32 s42, s46, 0x1f400\n\t" "s_addc_u32 s43, s47, 0\n\t" "s_add_u32 m0, %20, 0x19800\n\t" "v_xor_b32 v224, 0x80, v232\n\t" "global_load_lds_dwordx4 v224, s[42:43]\n\t"
                     "s_add_u32 m0, %20, 0x19c00\n\t" "v_xor_b32 v224, 0x80, v233\n\t" "global_load_lds_dwordx4 v224, s[42:43]\n\t" "s_add_u32 m0, %20, 0x1a000\n\t" "v_xor_b32 v224, 0x80, v234\n\t"
                     "global_load_lds_dwordx4 v224, s[42:43]\n\t" "s_add_u32 m0, %20, 0x1a400\n\t" "v_xor_b32 v224, 0x80, v235\n\t" "global_load_lds_dwordx4 v224, s[42:43]\n\t" "s_add_u32 m0, %20, 0x1a800\n\t"
                     "v_xor_b32 v224, 0x80, v236\n\t" "global_load_lds_dwordx4 v224, s[42:43]\n\t" "s_add_u32 s42, %21, 0x0\n\t" "v_xor_b32 v237, 0x0, v229\n\t" "v_add_u32 v237, s42, v237\n\t"
                     "v_xor_b32 v238, 0x40, v229\n\t" "v_add_u32 v238, s42, v238\n\t" "v_xor_b32 v239, 0x80, v229\n\t" "v_add_u32 v239, s42, v239\n\t" "v_xor_b32 v226, 0xc0, v229\n\t" "v_add_u32 v226, s42, v226\n\t"
                     "ds_read_b64_tr_b16 a[180:181], v237\n\t" "ds_read_b64_tr_b16 a[182:183], v238 offset:2560\n\t" "ds_read_b64_tr_b16 a[204:205], v239\n\t" "ds_read_b64_tr_b16 a[206:207], v226 offset:2560\n\t"
                     "ds_read_b64_tr_b16 a[228:229], v237 offset:256\n\t" "ds_read_b64_tr_b16 a[230:231], v238 offset:2816\n\t" "ds_read_b64_tr_b16 a[252:253], v239 offset:256\n\t"
                     "ds_read_b64_tr_b16 a[254:255], v226 offset:2816\n\t" "ds_read_b64_tr_b16 v[116:117], v237 offset:512\n\t" "ds_read_b64_tr_b16 v[118:119], v238 offset:3072\n\t"
                     "ds_read_b64_tr_b16 v[140:141], v239 offset:512\n\t" "ds_read_b64_tr_b16 v[142:143], v226 offset:3072\n\t" "ds_read_b64_tr_b16 v[164:165], v237 offset:768\n\t"
                     "ds_read_b64_tr_b16 v[166:167], v238 offset:3328\n\t" "ds_read_b64_tr_b16 v[188:189], v239 offset:768\n\t" "ds_read_b64_tr_b16 v[190:191], v226 offset:3328\n\t"
                     "ds_read_b64_tr_b16 v[212:213], v237 offset:1024\n\t" "ds_read_b64_tr_b16 v[214:215], v238 offset:3584\n\t" "ds_read_b64_tr_b16 v[76:77], v239 offset:1024\n\t"
                     "ds_read_b64_tr_b16 v[78:79], v226 offset:3584\n\t" "s_waitcnt vmcnt(10) lgkmcnt(0)\n\t" "s_barrier\n\t" "v_mov_b32 v225, 0\n\t" "global_load_dwordx2 %12, v231, %19 sc1\n\t"
                     "global_load_dwordx2 %13, v225, %19 offset:512 sc1\n\t" "global_load_dwordx2 %14, v225, %19 offset:1024 sc1\n\t" "s_add_u32 s42, s46, 0x28000\n\t" "s_addc_u32 s43, s47, 0\n\t"
                     "s_add_u32 m0, %20, 0x0\n\t" "s_nop 0\n\t" "global_load_lds_dwordx4 v232, s[42:43]\n\t" "s_add_u32 m0, %20, 0x400\n\t" "s_nop 0\n\t" "global_load_lds_dwordx4 v233, s[42:43]\n\t"
                     "s_add_u32 m0, %20, 0x800\n\t" "s_nop 0\n\t" "global_load_lds_dwordx4 v234, s[42:43]\n\t" "s_add_u32 m0, %20, 0xc00\n\t" "s_nop 0\n\t" "global_load_lds_dwordx4 v235, s[42:43]\n\t"
                     "s_add_u32 m0, %20, 0x1000\n\t" "s_nop 0\n\t" "global_load_lds_dwordx4 v236, s[42:43]\n\t" "s_add_u32 s42, s46, 0x29400\n\t" "s_addc_u32 s43, s47, 0\n\t" "s_add_u32 m0, %20, 0x1400\n\t"
                     "v_xor_b32 v224, 0x80, v232\n\t" "global_load_lds_dwordx4 v224, s[42:43]\n\t" "s_add_u32 m0, %20, 0x1800\n\t" "v_xor_b32 v224, 0x80, v233\n\t" "global_load_lds_dwordx4 v224, s[42:43]\n\t"
                     "s_add_u32 m0, %20, 0x1c00\n\t" "v_xor_b32 v224, 0x80, v234\n\t" "global_load_lds_dwordx4 v224, s[42:43]\n\t" "s_add_u32 m0, %20, 0x2000\n\t" "v_xor_b32 v224, 0x80, v235\n\t"
                     "global_load_lds_dwordx4 v224, s[42:43]\n\t" "s_add_u32 m0, %20, 0x2400\n\t" "v_xor_b32 v224, 0x80, v236\n\t" "global_load_lds_dwordx4 v224, s[42:43]\n\t" "s_add_u32 s42, %21, 0xc200\n\t"
                     "v_xor_b32 v237, 0x0, v229\n\t" "v_add_u32 v237, s42, v237\n\t" "v_xor_b32 v238, 0x40, v229\n\t" "v_add_u32 v238, s42, v238\n\t" "v_xor_b32 v239, 0x80, v229\n\t" "v_add_u32 v239, s42, v239\n\t"
                     "v_xor_b32 v226, 0xc0, v229\n\t" "v_add_u32 v226, s42, v226\n\t" "ds_read_b64_tr_b16 a[184:185], v237\n\t" "ds_read_b64_tr_b16 a[186:187], v238 offset:2560\n\t"
                     "ds_read_b64_tr_b16 a[208:209], v239\n\t" "ds_read_b64_tr_b16 a[210:211], v226 offset:2560\n\t" "ds_read_b64_tr_b16 a[232:233], v237 offset:256\n\t"
                     "ds_read_b64_tr_b16 a[234:235], v238 offset:2816\n\t" "ds_read_b64_tr_b16 v[96:97], v239 offset:256\n\t" "ds_read_b64_tr_b16 v[98:99], v226 offset:2816\n\t"
                     "ds_read_b64_tr_b16 v[120:121], v237 offset:512\n\t" "ds_read_b64_tr_b16 v[122:123], v238 offset:3072\n\t" "ds_read_b64_tr_b16 v[144:145], v239 offset:512\n\t"
                     "ds_read_b64_tr_b16 v[146:147], v226 offset:3072\n\t" "ds_read_b64_tr_b16 v[168:169], v237 offset:768\n\t" "ds_read_b64_tr_b16 v[170:171], v238 offset:3328\n\t"
                     "ds_read_b64_tr_b16 v[192:193], v239 offset:768\n\t" "ds_read_b64_tr_b16 v[194:195], v226 offset:3328\n\t" "ds_read_b64_tr_b16 v[216:217], v237 offset:1024\n\t"
                     "ds_read_b64_tr_b16 v[218:219], v238 offset:3584\n\t" "ds_read_b64_tr_b16 v[80:81], v239 offset:1024\n\t" "ds_read_b64_tr_b16 v[82:83], v226 offset:3584\n\t" "s_waitcnt vmcnt(10) lgkmcnt(0)\n\t"
                     "s_barrier\n\t" "s_add_u32 s42, s46, 0x32000\n\t" "s_addc_u32 s43, s47, 0\n\t" "s_add_u32 m0, %20, 0xc200\n\t" "s_nop 0\n\t" "global_load_lds_dwordx4 v232, s[42:43]\n\t" "s_add_u32 m0, %20, 0xc600\n\t"
                     "s_nop 0\n\t" "global_load_lds_dwordx4 v233, s[42:43]\n\t" "s_add_u32 m0, %20, 0xca00\n\t" "s_nop 0\n\t" "global_load_lds_dwordx4 v234, s[42:43]\n\t" "s_add_u32 m0, %20, 0xce00\n\t" "s_nop 0\n\t"
                     "global_load_lds_dwordx4 v235, s[42:43]\n\t" "s_add_u32 m0, %20, 0xd200\n\t" "s_nop 0\n\t" "global_load_lds_dwordx4 v236, s[42:43]\n\t" "s_add_u32 s42, s46, 0x33400\n\t" "s_addc_u32 s43, s47, 0\n\t"
                     "s_add_u32 m0, %20, 0xd600\n\t" "v_xor_b32 v224, 0x80, v232\n\t" "global_load_lds_dwordx4 v224, s[42:43]\n\t" "s_add_u32 m0, %20, 0xda00\n\t" "v_xor_b32 v224, 0x80, v233\n\t"
                     "global_load_lds_dwordx4 v224, s[42:43]\n\t" "s_add_u32 m0, %20, 0xde00\n\t" "v_xor_b32 v224, 0x80, v234\n\t" "global_load_lds_dwordx4 v224, s[42:43]\n\t" "s_add_u32 m0, %20, 0xe200\n\t"
                     "v_xor_b32 v224, 0x80, v235\n\t" "global_load_lds_dwordx4 v224, s[42:43]\n\t" "s_add_u32 m0, %20, 0xe600\n\t" "v_xor_b32 v224, 0x80, v236\n\t" "global_load_lds_dwordx4 v224, s[42:43]\n\t"
                     "s_add_u32 s42, %21, 0x18400\n\t" "v_xor_b32 v237, 0x0, v229\n\t" "v_add_u32 v237, s42, v237\n\t" "v_xor_b32 v238, 0x40, v229\n\t" "v_add_u32 v238, s42, v238\n\t" "v_xor_b32 v239, 0x80, v229\n\t"
                     "v_add_u32 v239, s42, v239\n\t" "v_xor_b32 v226, 0xc0, v229\n\t" "v_add_u32 v226, s42, v226\n\t" "ds_read_b64_tr_b16 a[188:189], v237\n\t" "ds_read_b64_tr_b16 a[190:191], v238 offset:2560\n\t"
                     "ds_read_b64_tr_b16 a[212:213], v239\n\t" "ds_read_b64_tr_b16 a[214:215], v226 offset:2560\n\t" "ds_read_b64_tr_b16 a[236:237], v237 offset:256\n\t"
                     "ds_read_b64_tr_b16 a[238:239], v238 offset:2816\n\t" "ds_read_b64_tr_b16 v[100:101], v239 offset:256\n\t" "ds_read_b64_tr_b16 v[102:103], v226 offset:2816\n\t"
                     "ds_read_b64_tr_b16 v[124:125], v237 offset:512\n\t" "ds_read_b64_tr_b16 v[126:127], v238 offset:3072\n\t" "ds_read_b64_tr_b16 v[148:149], v239 offset:512\n\t"
                     "ds_read_b64_tr_b16 v[150:151], v226 offset:3072\n\t" "ds_read_b64_tr_b16 v[172:173], v237 offset:768\n\t" "ds_read_b64_tr_b16 v[174:175], v238 offset:3328\n\t"
                     "ds_read_b64_tr_b16 v[196:197], v239 offset:768\n\t" "ds_read_b64_tr_b16 v[198:199], v226 offset:3328\n\t" "ds_read_b64_tr_b16 v[220:221], v237 offset:1024\n\t"
                     "ds_read_b64_tr_b16 v[222:223], v238 offset:3584\n\t" "ds_read_b64_tr_b16 v[84:85], v239 offset:1024\n\t" "ds_read_b64_tr_b16 v[86:87], v226 offset:3584\n\t" "s_waitcnt vmcnt(10) lgkmcnt(0)\n\t"
                     "s_barrier\n\t" "s_add_u32 s42, %21, 0x0\n\t" "v_xor_b32 v237, 0x0, v229\n\t" "v_add_u32 v237, s42, v237\n\t" "v_xor_b32 v238, 0x40, v229\n\t" "v_add_u32 v238, s42, v238\n\t"
                     "v_xor_b32 v239, 0x80, v229\n\t" "v_add_u32 v239, s42, v239\n\t" "v_xor_b32 v226, 0xc0, v229\n\t" "v_add_u32 v226, s42, v226\n\t" "ds_read_b64_tr_b16 a[192:193], v237\n\t"
                     "ds_read_b64_tr_b16 a[194:195], v238 offset:2560\n\t" "ds_read_b64_tr_b16 a[216:217], v239\n\t" "ds_read_b64_tr_b16 a[218:219], v226 offset:2560\n\t"
                     "ds_read_b64_tr_b16 a[240:241], v237 offset:256\n\t" "ds_read_b64_tr_b16 a[242:243], v238 offset:2816\n\t" "ds_read_b64_tr_b16 v[104:105], v239 offset:256\n\t"
                     "ds_read_b64_tr_b16 v[106:107], v226 offset:2816\n\t" "ds_read_b64_tr_b16 v[128:129], v237 offset:512\n\t" "ds_read_b64_tr_b16 v[130:131], v238 offset:3072\n\t"
                     "ds_read_b64_tr_b16 v[152:153], v239 offset:512\n\t" "ds_read_b64_tr_b16 v[154:155], v226 offset:3072\n\t" "ds_read_b64_tr_b16 v[176:177], v237 offset:768\n\t"
                     "ds_read_b64_tr_b16 v[178:179], v238 offset:3328\n\t" "ds_read_b64_tr_b16 v[200:201], v239 offset:768\n\t" "ds_read_b64_tr_b16 v[202:203], v226 offset:3328\n\t"
                     "ds_read_b64_tr_b16 v[64:65], v237 offset:1024\n\t" "ds_read_b64_tr_b16 v[66:67], v238 offset:3584\n\t" "ds_read_b64_tr_b16 v[88:89], v239 offset:1024\n\t"
                     "ds_read_b64_tr_b16 v[90:91], v226 offset:3584\n\t" "s_waitcnt vmcnt(0) lgkmcnt(0)\n\t" "s_barrier\n\t" "s_add_u32 s42, %21, 0xc200\n\t" "v_xor_b32 v237, 0x0, v229\n\t" "v_add_u32 v237, s42, v237\n\t"
                     "v_xor_b32 v238, 0x40, v229\n\t" "v_add_u32 v238, s42, v238\n\t" "v_xor_b32 v239, 0x80, v229\n\t" "v_add_u32 v239, s42, v239\n\t" "v_xor_b32 v226, 0xc0, v229\n\t" "v_add_u32 v226, s42, v226\n\t"
                     "ds_read_b64_tr_b16 a[196:197], v237\n\t" "ds_read_b64_tr_b16 a[198:199], v238 offset:2560\n\t" "ds_read_b64_tr_b16 a[220:221], v239\n\t" "ds_read_b64_tr_b16 a[222:223], v226 offset:2560\n\t"
                     "ds_read_b64_tr_b16 a[244:245], v237 offset:256\n\t" "ds_read_b64_tr_b16 a[246:247], v238 offset:2816\n\t" "ds_read_b64_tr_b16 v[108:109], v239 offset:256\n\t"
                     "ds_read_b64_tr_b16 v[110:111], v226 offset:2816\n\t" "ds_read_b64_tr_b16 v[132:133], v237 offset:512\n\t" "ds_read_b64_tr_b16 v[134:135], v238 offset:3072\n\t"
                     "ds_read_b64_tr_b16 v[156:157], v239 offset:512\n\t" "ds_read_b64_tr_b16 v[158:159], v226 offset:3072\n\t" "ds_read_b64_tr_b16 v[180:181], v237 offset:768\n\t"
                     "ds_read_b64_tr_b16 v[182:183], v238 offset:3328\n\t" "ds_read_b64_tr_b16 v[204:205], v239 offset:768\n\t" "ds_read_b64_tr_b16 v[206:207], v226 offset:3328\n\t"
                     "ds_read_b64_tr_b16 v[68:69], v237 offset:1024\n\t" "ds_read_b64_tr_b16 v[70:71], v238 offset:3584\n\t" "ds_read_b64_tr_b16 v[92:93], v239 offset:1024\n\t"
                     "ds_read_b64_tr_b16 v[94:95], v226 offset:3584\n\t" "s_waitcnt lgkmcnt(0)\n\t" "ds_write_b128 v230, v[0:3]\n\t" "ds_write_b128 v230, v[4:7] offset:1024\n\t"
                     "ds_write_b128 v230, v[8:11] offset:2048\n\t" "ds_write_b128 v230, v[12:15] offset:3072\n\t" "ds_write_b128 v230, v[16:19] offset:4096\n\t" "ds_write_b128 v230, v[20:23] offset:5120\n\t"
                     "ds_write_b128 v230, v[24:27] offset:6144\n\t" "ds_write_b128 v230, v[28:31] offset:7168\n\t" "ds_write_b128 v230, v[32:35] offset:8192\n\t" "ds_write_b128 v230, v[36:39] offset:9216\n\t"
                     "ds_write_b128 v230, v[40:43] offset:10240\n\t" "ds_write_b128 v230, v[44:47] offset:11264\n\t" "ds_write_b128 v230, v[48:51] offset:12288\n\t" "ds_write_b128 v230, v[52:55] offset:13312\n\t"
                     "ds_write_b128 v230, v[56:59] offset:14336\n\t" "ds_write_b128 v230, v[60:63] offset:15360\n\t" "ds_write_b128 v230, v[64:67] offset:16384\n\t" "ds_write_b128 v230, v[68:71] offset:17408\n\t"
                     "ds_write_b128 v230, v[72:75] offset:18432\n\t" "ds_write_b128 v230, v[76:79] offset:19456\n\t" "ds_write_b128 v230, v[80:83] offset:20480\n\t" "ds_write_b128 v230, v[84:87] offset:21504\n\t"
                     "ds_write_b128 v230, v[88:91] offset:22528\n\t" "ds_write_b128 v230, v[92:95] offset:23552\n\t"
                     : "=&{a[0:31]}"(wa[0]), "=&{a[32:63]}"(wa[1]), "=&{a[64:95]}"(wa[2]), "=&{a[96:127]}"(wa[3]), "=&{a[128:159]}"(wa[4]), "=&{a[160:191]}"(wa[5]), "=&{a[192:223]}"(wa[6]), "=&{a[224:255]}"(wa[7]), "=&{v[96:127]}"(wv[0]), "=&{v[128:159]}"(wv[1]), "=&{v[160:191]}"(wv[2]), "=&{v[192:223]}"(wv[3]), "=&v"(y.r), "=&v"(y.g), "=&v"(y.e), "+{v[224:239]}"(pa)
                     : "{s[44:45]}"(p.wv + h * DH * 2 + (size_t)(16 * w) * (HD * 2)), "{s[46:47]}"(p.wo + (size_t)(h * DH + 8 * w) * 1280),
                       "s"(lds + 2 * w * GS), "s"(p.ws + OFF_LAT), "s"(lds + w * 10240), "s"(lds), "s"((h & 4) * 16 * HD * 2)
                     : "v0", "v1", "v2", "v3", "v4", "v5", "v6", "v7", "v8", "v9", "v10", "v11", "v12", "v13", "v14", "v15", "v16", "v17", "v18", "v19", "v20", "v21", "v22", "v23", "v24", "v25", "v26", "v27", "v28", "v29", "v30", "v31", "v32", "v33", "v34", "v35", "v36", "v37", "v38", "v39", "v40", "v41", "v42", "v43", "v44", "v45", "v46", "v47", "v48", "v49", "v50", "v51", "v52", "v53", "v54", "v55", "v56", "v57", "v58", "v59", "v60", "v61", "v62", "v63", "v64", "v65", "v66", "v67", "v68", "v69", "v70", "v71", "v72", "v73", "v74", "v75", "v76", "v77", "v78", "v79", "v80", "v81", "v82", "v83", "v84", "v85", "v86", "v87", "v88", "v89", "v90", "v91", "v92", "v93", "v94", "v95", "s42", "s43", "scc", "memory");
        sd = sync_end(y, p.ws, phi, w == 0);
        if (w == 3) {
            u32 r;
            rdy_add(r, p.ws + OFF_ST);
            asm volatile("s_waitcnt vmcnt(0)" : "+v"(r) : : "memory");
            rdy_end(r, p.ws + OFF_ST, cfg.s1 >> 8 & 255);
        }
        barrier();
    }
    const int lane = lanex(), tid = 64 * w + lane;
    const int n = lane & 15, g = lane >> 4, qq = (lane >> 2) & 3, pp = lane & 3;
    char* const fl = p.ws + OFF_ST;
    const u32 iw = lds + 65536 + ((lane * 8) ^ (lane & 32));
    const u32 qr0 = lds + 65536 + (g >> 1) * 512 + pp * 128 + 64 * (g & 1) + 8 * qq + 32 * (pp >> 1);
    const u32 uo = (u32)((4 * w + (n >> 3)) * 4096 + h * 128 + (n & 7) * 16);
    u32x16 ad = u32x16{(u32)lane * 16 + 1024 + (h & 4) * 256, (lane & 2) ? 0xEEEEu : 0x4444u, lds + (u32)((w & 1) * 24576 + (w >> 1) * 99328 + lane * 16), iw + ((3 * w + (w & 1)) >> 1) * 512,
                       iw + (1 + 3 * (w >> 1)) * 512 + 4 * (w & 1), qr0, qr0 ^ 32, uo + (g & 1) * 8192 + (1 + (g >> 1)) * 65536, uo + (g & 1) * 8192 + (g >> 1) * 8, uo, (u32)lane * 16 + 1024 - (h & 4) * 256, uo, uo, uo, uo, uo};
    asm volatile("" : "+{v[224:239]}"(ad));
    const char* memp = p.ws + mslot(sd, 0);
    int budget = 30000;
    Clock clk; clk.init();
    for (int s = 0; s < NSEG; ++s) {
        int tries = 0;
        u64 bad, tp8, en = ~0ull;
        while (true) {
            const u32 tgt = s ? clk.sched(phi, 0, cfg.G, tries ? 2 : 0) : mt();
            char* up = p.ws + uslot(sd, s);
            asm volatile("ds_read_b128 v[64:67], v226\n\t" "ds_read_b128 v[68:71], v226 offset:1024\n\t" "ds_read_b128 v[72:75], v226 offset:2048\n\t" "ds_read_b128 v[76:79], v226 offset:3072\n\t"
                         "ds_read_b128 v[80:83], v226 offset:4096\n\t" "ds_read_b128 v[84:87], v226 offset:5120\n\t" "ds_read_b128 v[88:91], v226 offset:6144\n\t" "ds_read_b128 v[92:95], v226 offset:7168\n\t"
                         "v_mov_b64 v[40:41], 0\n\t" "v_mov_b64 v[42:43], 0\n\t" "v_mov_b64 v[44:45], 0\n\t" "v_mov_b64 v[46:47], 0\n\t" "v_mov_b64 v[48:49], 0\n\t" "v_mov_b64 v[50:51], 0\n\t" ".p2align 5\n\t" ".Lsp_u:\n\t"
                         "s_memtime vcc\n\t" "s_waitcnt lgkmcnt(0)\n\t" "s_sub_i32 s42, %3, vcc_lo\n\t" "s_cmp_gt_i32 s42, 80\n\t" "s_cbranch_scc1 .Lsp_u\n\t" "s_max_i32 s42, s42, 0\n\t" "s_sub_i32 s42, 92, s42\n\t"
                         "s_and_b32 s42, s42, -4\n\t" "s_getpc_b64 vcc\n\t" "s_add_u32 vcc_lo, vcc_lo, s42\n\t" "s_addc_u32 vcc_hi, vcc_hi, 0\n\t" "s_setpc_b64 vcc\n\t" "s_nop 0\n\t" "s_nop 0\n\t" "s_nop 0\n\t" "s_nop 0\n\t"
                         "s_nop 0\n\t" "s_nop 0\n\t" "s_nop 0\n\t" "s_nop 0\n\t" "s_nop 0\n\t" "s_nop 0\n\t" "s_nop 0\n\t" "s_nop 0\n\t" "s_nop 0\n\t" "s_nop 0\n\t" "s_nop 0\n\t" "s_nop 0\n\t" "s_nop 0\n\t" "s_nop 0\n\t"
                         "s_nop 0\n\t" "s_nop 0\n\t" "global_load_dwordx4 v[0:3], v224, %4 offset:-4096 sc1\n\t" "s_nop 10\n\t" "global_load_dwordx4 v[4:7], v224, %4 offset:-2048 sc1\n\t" "s_nop 15\n\t"
                         "global_load_dwordx4 v[8:11], v234, %4 offset:-3072 sc1\n\t" "s_nop 10\n\t" "global_load_dwordx4 v[12:15], v234, %4 offset:-1024 sc1\n\t" "s_nop 10\n\t" "global_load_dwordx4 v[16:19], v224, %4 sc1\n\t"
                         "s_nop 10\n\t" "global_load_dwordx4 v[20:23], v234, %4 offset:1024 sc1\n\t" "s_nop 10\n\t" "global_load_dwordx4 v[24:27], v224, %4 offset:2048 sc1\n\t" "s_nop 10\n\t"
                         "global_load_dwordx4 v[28:31], v234, %4 offset:3072 sc1\n\t" "s_nop 10\n\t" "global_load_dwordx4 v[32:35], v224, %5 offset:-4096 sc1\n\t" "s_nop 10\n\t"
                         "global_load_dwordx4 v[36:39], v234, %5 offset:-3072 sc1\n\t" "s_waitcnt vmcnt(9)\n\t" "v_smfmac_f32_16x16x64_bf16 v[40:43], v[0:3], a[0:7], v225\n\t"
                         "v_smfmac_f32_16x16x64_bf16 v[44:47], v[0:3], v[64:71], v225\n\t" "ds_read_b128 v[64:67], v226 offset:8192\n\t" "v_smfmac_f32_16x16x64_bf16 v[48:51], v[0:3], a[8:15], v225\n\t"
                         "ds_read_b128 v[68:71], v226 offset:9216\n\t" "v_cmp_eq_u32 vcc, -1, v0\n\t" "s_waitcnt vmcnt(8)\n\t" "v_smfmac_f32_16x16x64_bf16 v[40:43], v[4:7], a[16:23], v225\n\t" "v_mov_b64 v[0:1], 0\n\t"
                         "v_smfmac_f32_16x16x64_bf16 v[44:47], v[4:7], v[72:79], v225\n\t" "s_and_b64 vcc, vcc, %6\n\t" "s_cbranch_scc1 .Lsk_u\n\t" "ds_read_b128 v[72:75], v226 offset:10240\n\t" "v_mov_b64 v[2:3], 0\n\t"
                         "v_smfmac_f32_16x16x64_bf16 v[48:51], v[4:7], a[24:31], v225\n\t" "ds_read_b128 v[76:79], v226 offset:11264\n\t" "s_waitcnt vmcnt(7)\n\t"
                         "v_smfmac_f32_16x16x64_bf16 v[40:43], v[8:11], a[32:39], v225\n\t" "v_mov_b64 v[4:5], 0\n\t" "v_smfmac_f32_16x16x64_bf16 v[44:47], v[8:11], v[80:87], v225\n\t"
                         "ds_read_b128 v[80:83], v226 offset:12288\n\t" "v_mov_b64 v[6:7], 0\n\t" "v_smfmac_f32_16x16x64_bf16 v[48:51], v[8:11], a[40:47], v225\n\t" "ds_read_b128 v[84:87], v226 offset:13312\n\t"
                         "s_waitcnt vmcnt(6)\n\t" "v_smfmac_f32_16x16x64_bf16 v[40:43], v[12:15], a[48:55], v225\n\t" "v_mov_b64 v[8:9], 0\n\t" "v_smfmac_f32_16x16x64_bf16 v[44:47], v[12:15], v[88:95], v225\n\t"
                         "ds_read_b128 v[88:91], v226 offset:14336\n\t" "v_mov_b64 v[10:11], 0\n\t" "v_smfmac_f32_16x16x64_bf16 v[48:51], v[12:15], a[56:63], v225\n\t" "ds_read_b128 v[92:95], v226 offset:15360\n\t"
                         "s_waitcnt vmcnt(5)\n\t" "v_smfmac_f32_16x16x64_bf16 v[40:43], v[16:19], a[64:71], v225\n\t" "v_mov_b64 v[12:13], 0\n\t" "s_waitcnt lgkmcnt(6)\n\t"
                         "v_smfmac_f32_16x16x64_bf16 v[44:47], v[16:19], v[64:71], v225\n\t" "ds_read_b128 v[64:67], v226 offset:16384\n\t" "v_mov_b64 v[14:15], 0\n\t"
                         "v_smfmac_f32_16x16x64_bf16 v[48:51], v[16:19], a[72:79], v225\n\t" "ds_read_b128 v[68:71], v226 offset:17408\n\t" "s_waitcnt vmcnt(4)\n\t"
                         "v_smfmac_f32_16x16x64_bf16 v[40:43], v[20:23], a[80:87], v225\n\t" "v_mov_b64 v[16:17], 0\n\t" "s_waitcnt lgkmcnt(6)\n\t" "v_smfmac_f32_16x16x64_bf16 v[44:47], v[20:23], v[72:79], v225\n\t"
                         "ds_read_b128 v[72:75], v226 offset:18432\n\t" "v_mov_b64 v[18:19], 0\n\t" "v_smfmac_f32_16x16x64_bf16 v[48:51], v[20:23], a[88:95], v225\n\t" "ds_read_b128 v[76:79], v226 offset:19456\n\t"
                         "s_waitcnt vmcnt(3)\n\t" "v_smfmac_f32_16x16x64_bf16 v[40:43], v[24:27], a[96:103], v225\n\t" "v_mov_b64 v[20:21], 0\n\t" "s_waitcnt lgkmcnt(6)\n\t"
                         "v_smfmac_f32_16x16x64_bf16 v[44:47], v[24:27], v[80:87], v225\n\t" "ds_read_b128 v[80:83], v226 offset:20480\n\t" "v_mov_b64 v[22:23], 0\n\t"
                         "v_smfmac_f32_16x16x64_bf16 v[48:51], v[24:27], a[104:111], v225\n\t" "ds_read_b128 v[84:87], v226 offset:21504\n\t" "s_waitcnt vmcnt(2)\n\t"
                         "v_smfmac_f32_16x16x64_bf16 v[40:43], v[28:31], a[112:119], v225\n\t" "v_mov_b64 v[24:25], 0\n\t" "s_waitcnt lgkmcnt(6)\n\t" "v_smfmac_f32_16x16x64_bf16 v[44:47], v[28:31], v[88:95], v225\n\t"
                         "ds_read_b128 v[88:91], v226 offset:22528\n\t" "v_mov_b64 v[26:27], 0\n\t" "v_smfmac_f32_16x16x64_bf16 v[48:51], v[28:31], a[120:127], v225\n\t" "ds_read_b128 v[92:95], v226 offset:23552\n\t"
                         "s_waitcnt vmcnt(1)\n\t" "v_smfmac_f32_16x16x64_bf16 v[40:43], v[32:35], a[128:135], v225\n\t" "v_mov_b64 v[28:29], 0\n\t" "v_smfmac_f32_16x16x64_bf16 v[44:47], v[32:35], a[136:143], v225\n\t"
                         "v_mov_b64 v[30:31], 0\n\t" "v_smfmac_f32_16x16x64_bf16 v[48:51], v[32:35], a[144:151], v225\n\t" "s_waitcnt vmcnt(0)\n\t" "v_smfmac_f32_16x16x64_bf16 v[40:43], v[36:39], a[152:159], v225\n\t"
                         "v_mov_b64 v[32:33], 0\n\t" "v_smfmac_f32_16x16x64_bf16 v[44:47], v[36:39], a[160:167], v225\n\t" "v_mov_b64 v[34:35], 0\n\t" "v_smfmac_f32_16x16x64_bf16 v[48:51], v[36:39], a[168:175], v225\n\t"
                         "v_add_f32 v52, v40, v42\n\t" "v_add_f32 v53, v41, v43\n\t" "v_cmp_u_f32 vcc, v52, v53\n\t" "v_add_f32 v54, v44, v46\n\t" "v_add_f32 v55, v45, v47\n\t" "v_cvt_pk_bf16_f32 v58, v52, v53\n\t"
                         "v_pk_add_f32 v[56:57], v[48:49], v[50:51]\n\t" "v_cvt_pk_bf16_f32 v59, v54, v55\n\t" "v_cvt_pk_bf16_f32 v60, v56, v57\n\t" "ds_write_b64 v227, v[58:59]\n\t" "ds_write_b32 v228, v60\n\t"
                         "v_mov_b64 v[36:37], 0\n\t" "v_mov_b64 v[38:39], 0\n\t" "s_and_b64 vcc, vcc, %6\n\t" "v_mov_b32 v233, 0\n\t" "s_cbranch_scc1 .Lsk_u\n\t" "v_mov_b32 v233, 0\n\t" "v_mov_b32 v233, 0\n\t"
                         "v_mov_b32 v233, 0\n\t" "s_waitcnt lgkmcnt(0)\n\t" "s_barrier\n\t" "ds_read_b64_tr_b16 v[40:41], v229\n\t" "ds_read_b64_tr_b16 v[42:43], v230\n\t" "v_mov_b32 v233, 0\n\t"
                         "ds_read_b64_tr_b16 v[44:45], v229 offset:1024\n\t" "v_mov_b32 v233, 0\n\t" "ds_read_b64_tr_b16 v[46:47], v230 offset:1024\n\t" "v_mov_b32 v233, 0\n\t"
                         "ds_read_b64_tr_b16 v[48:49], v229 offset:2048\n\t" "v_mov_b32 v233, 0\n\t" "ds_read_b64_tr_b16 v[50:51], v230 offset:2048\n\t" "v_mov_b32 v233, 0\n\t" "s_waitcnt lgkmcnt(4)\n\t"
                         "v_smfmac_f32_16x16x64_bf16 v[0:3], v[40:43], a[176:183], v225\n\t" "v_smfmac_f32_16x16x64_bf16 v[4:7], v[40:43], a[200:207], v225\n\t" "s_waitcnt lgkmcnt(2)\n\t"
                         "v_smfmac_f32_16x16x64_bf16 v[0:3], v[44:47], a[184:191], v225\n\t" "v_smfmac_f32_16x16x64_bf16 v[4:7], v[44:47], a[208:215], v225\n\t" "s_waitcnt lgkmcnt(0)\n\t"
                         "v_smfmac_f32_16x16x64_bf16 v[0:3], v[48:51], a[192:199], v225\n\t" "v_smfmac_f32_16x16x64_bf16 v[4:7], v[48:51], a[216:223], v225\n\t"
                         "v_smfmac_f32_16x16x64_bf16 v[8:11], v[40:43], a[224:231], v225\n\t" "v_add_f32 v52, v0, v2\n\t" "v_add_f32 v53, v1, v3\n\t" "v_cvt_pk_bf16_f32 v54, v53, v52\n\t"
                         "v_smfmac_f32_16x16x64_bf16 v[8:11], v[44:47], a[232:239], v225\n\t" "v_add_f32 v52, v4, v6\n\t" "v_add_f32 v53, v5, v7\n\t" "v_cvt_pk_bf16_f32 v55, v53, v52\n\t" "s_nop 1\n\t"
                         "v_permlane16_swap_b32 v54, v55\n\t" "v_smfmac_f32_16x16x64_bf16 v[8:11], v[48:51], a[240:247], v225\n\t" "global_store_dwordx2 v232, v[54:55], %2 sc1\n\t" "s_add_u32 s40, s40, 0x10000\n\t"
                         "s_addc_u32 s41, s41, 0\n\t" "v_smfmac_f32_16x16x64_bf16 v[12:15], v[40:43], a[248:255], v225\n\t" "v_smfmac_f32_16x16x64_bf16 v[12:15], v[44:47], v[96:103], v225\n\t" "v_add_f32 v52, v8, v10\n\t"
                         "v_smfmac_f32_16x16x64_bf16 v[12:15], v[48:51], v[104:111], v225\n\t" "v_add_f32 v53, v9, v11\n\t" "v_smfmac_f32_16x16x64_bf16 v[16:19], v[40:43], v[112:119], v225\n\t"
                         "v_cvt_pk_bf16_f32 v56, v53, v52\n\t" "v_smfmac_f32_16x16x64_bf16 v[16:19], v[44:47], v[120:127], v225\n\t" "v_add_f32 v52, v12, v14\n\t" "v_add_f32 v53, v13, v15\n\t"
                         "v_cvt_pk_bf16_f32 v57, v53, v52\n\t" "s_nop 1\n\t" "v_permlane16_swap_b32 v56, v57\n\t" "v_smfmac_f32_16x16x64_bf16 v[16:19], v[48:51], v[128:135], v225\n\t"
                         "global_store_dwordx2 v232, v[56:57], %2 sc1\n\t" "s_add_u32 s40, s40, 0x10000\n\t" "s_addc_u32 s41, s41, 0\n\t" "v_smfmac_f32_16x16x64_bf16 v[20:23], v[40:43], v[136:143], v225\n\t"
                         "v_smfmac_f32_16x16x64_bf16 v[20:23], v[44:47], v[144:151], v225\n\t" "v_add_f32 v52, v16, v18\n\t" "v_smfmac_f32_16x16x64_bf16 v[20:23], v[48:51], v[152:159], v225\n\t" "v_add_f32 v53, v17, v19\n\t"
                         "v_smfmac_f32_16x16x64_bf16 v[24:27], v[40:43], v[160:167], v225\n\t" "v_cvt_pk_bf16_f32 v58, v53, v52\n\t" "v_smfmac_f32_16x16x64_bf16 v[24:27], v[44:47], v[168:175], v225\n\t"
                         "v_add_f32 v52, v20, v22\n\t" "v_add_f32 v53, v21, v23\n\t" "v_smfmac_f32_16x16x64_bf16 v[24:27], v[48:51], v[176:183], v225\n\t" "v_cvt_pk_bf16_f32 v59, v53, v52\n\t"
                         "v_smfmac_f32_16x16x64_bf16 v[28:31], v[40:43], v[184:191], v225\n\t" "s_nop 0\n\t" "v_permlane16_swap_b32 v58, v59\n\t" "v_smfmac_f32_16x16x64_bf16 v[28:31], v[44:47], v[192:199], v225\n\t"
                         "global_store_dwordx2 v232, v[58:59], %2 sc1\n\t" "v_add_f32 v52, v24, v26\n\t" "v_smfmac_f32_16x16x64_bf16 v[28:31], v[48:51], v[200:207], v225\n\t" "v_add_f32 v53, v25, v27\n\t"
                         "v_smfmac_f32_16x16x64_bf16 v[32:35], v[40:43], v[208:215], v225\n\t" "v_cvt_pk_bf16_f32 v60, v53, v52\n\t" "v_smfmac_f32_16x16x64_bf16 v[32:35], v[44:47], v[216:223], v225\n\t"
                         "v_add_f32 v52, v28, v30\n\t" "v_smfmac_f32_16x16x64_bf16 v[32:35], v[48:51], v[64:71], v225\n\t" "v_add_f32 v53, v29, v31\n\t" "v_smfmac_f32_16x16x64_bf16 v[36:39], v[40:43], v[72:79], v225\n\t"
                         "v_cvt_pk_bf16_f32 v61, v53, v52\n\t" "v_smfmac_f32_16x16x64_bf16 v[36:39], v[44:47], v[80:87], v225\n\t" "v_add_f32 v52, v32, v34\n\t"
                         "v_smfmac_f32_16x16x64_bf16 v[36:39], v[48:51], v[88:95], v225\n\t" "v_add_f32 v53, v33, v35\n\t" "v_cvt_pk_bf16_f32 v62, v53, v52\n\t" "v_mov_b32 v233, 0\n\t" "v_mov_b32 v233, 0\n\t"
                         "v_mov_b32 v233, 0\n\t" "v_mov_b32 v233, 0\n\t" "v_mov_b32 v233, 0\n\t" "v_pk_add_f32 v[52:53], v[36:37], v[38:39]\n\t" "v_cvt_pk_bf16_f32 v63, v53, v52\n\t" "v_permlane32_swap_b32 v60, v62\n\t"
                         "s_nop 0\n\t" "v_permlane32_swap_b32 v61, v63\n\t" "s_nop 1\n\t" "v_permlane16_swap_b32 v60, v61\n\t" "v_permlane16_swap_b32 v62, v63\n\t" "global_store_dwordx4 v231, v[60:63], %2 sc1\n\t"
                         ".Lsk_u:\n\t" "s_memrealtime %1\n\t" "s_mov_b64 %0, vcc\n\t" "s_waitcnt lgkmcnt(0)\n\t"
                         : "=&s"(bad), "=&s"(tp8), "+{s[40:41]}"(up)
                         : "s"(uni((int)tgt)), "s"(memp - 1024), "s"(memp + 15360), "s"(en), "{v[224:239]}"(ad),
                           "{a[0:31]}"(wa[0]), "{a[32:63]}"(wa[1]), "{a[64:95]}"(wa[2]), "{a[96:127]}"(wa[3]), "{a[128:159]}"(wa[4]), "{a[160:191]}"(wa[5]), "{a[192:223]}"(wa[6]), "{a[224:255]}"(wa[7]), "{v[96:127]}"(wv[0]), "{v[128:159]}"(wv[1]), "{v[160:191]}"(wv[2]), "{v[192:223]}"(wv[3])
                         : "v0", "v1", "v2", "v3", "v4", "v5", "v6", "v7", "v8", "v9", "v10", "v11", "v12", "v13", "v14", "v15", "v16", "v17", "v18", "v19", "v20", "v21", "v22", "v23", "v24", "v25", "v26", "v27", "v28", "v29", "v30", "v31", "v32", "v33", "v34", "v35", "v36", "v37", "v38", "v39", "v40", "v41", "v42", "v43", "v44", "v45", "v46", "v47", "v48", "v49", "v50", "v51", "v52", "v53", "v54", "v55", "v56", "v57", "v58", "v59", "v60", "v61", "v62", "v63", "v64", "v65", "v66", "v67", "v68", "v69", "v70", "v71", "v72", "v73", "v74", "v75", "v76", "v77", "v78", "v79", "v80", "v81", "v82", "v83", "v84", "v85", "v86", "v87", "v88", "v89", "v90", "v91", "v92", "v93", "v94", "v95", "vcc", "scc", "s42", "memory");
            if (__builtin_expect(bad == 0, 1)) break;
            waitvm<0>();
            if (--budget < 0 || ((++tries & 15) == 0 && !unwritten(memp, lanex() * 16))) en = 0;
            phi += (u32)cfg.G;
        }
        clk.calib();
        const u32 tt = sched_ld0(s);
        if (s >= NSEG - 2) waitvm<0>();
        barrier();
        if (s >= NSEG - 2 && w == 3) flag_add(fl, F_PROG + 8 * s + (h & 7), 1);
        if (s >= NSEG - 2 && w == 2) flag_add(fl, F_UD + 2 * (s & 1) + (h & 1), 1);
        if (s == 0) {
#pragma unroll
            for (int i = UPF; i < 8; ++i) {
                const int l = 64 * min(i * 4 + w, 29) + lanex();
                pfl(p.wq + h * 384, (u32)((l / 3) * 12288 + (l % 3) * 128), lds);
            }
        }
        if (s == 0) { until(phi + UNAP); phi = go_wg(fl, h, w, lds + SL_PF - 16) - (tt >> 16); edge(clk.r1, clk.m1); clk.calib(); }
        follow(phi, (int)(tt >> 16) + 4 + cfg.T3 - VISS, (u32)tp8, cfg.G);
        phi += tt & 0xffffu;
        memp = p.ws + mslot(sd, s + 1);
    }
    asm volatile(".Lkm_lu:\n\ts_waitcnt vmcnt(0)" : : : "memory");
}

#define VALL "{v[0:3]}"(o), "{v[16:47]}"(rv[0]), "{v[48:79]}"(rv[1]), "{v[80:111]}"(rv[2]), "{v[112:143]}"(rv[3]), "{v[144:175]}"(rv[4]), "{v[176:207]}"(rv[5]), "{v[208:239]}"(rv[6]), "{a[0:31]}"(ra[0]), "{a[32:63]}"(ra[1]), "{a[64:95]}"(ra[2]), "{a[96:127]}"(ra[3])
DI void value_role(const Ptrs& p, const ChainCfg cfg, int vb, int sd, u32 phi, u32 lds, int w) {
    const int lane = lanex(), tid = 64 * w + lane, n = lane & 15, g = lane >> 4;
    const int v = vb;
    const u32 moff = lane * 16;
    char* const fl = p.ws + OFF_ST;
    char* const ub = p.ws + uchunk(v);
    const int xh = (vb >> 2) & 1;
    if (w == 3) {
        const u32x4 ff = u32x4{~0u, ~0u, ~0u, ~0u};
        if (SPL) barrier();
#pragma nounroll
        for (int s = 0; s < NSEG - 2; ++s) {
            dly(s == 1 ? HD1 : cfg.cR3 >> 8 & 255);
            char* const mk = ub + uslot(sd, s + 2);
#pragma unroll
            for (int i = 0; i < 8; ++i) { gst16(mk + i * 1024, moff, ff); gap<HQ>(-1); }
            if (vb < 32 && s) {
                gst16(p.ws + (size_t)(s + 2) * HO_STRIDE + vb * 2048, moff, ff);
                gst16(p.ws + (size_t)(s + 2) * HO_STRIDE + vb * 2048 + 1024, moff, ff);
            }
            if (s == 0) {
                u32 r;
                flag_wait(fl, F_VD + (vb & 3) + (v < 20 ? 0 : 4), 32);
                rdy_add(r, fl);
                asm volatile("s_waitcnt vmcnt(0)" : "+v"(r) : : "memory");
                rdy_end(r, fl, cfg.s1 >> 16);
            }
            waitvm<0>();
            if (SPL && s) barrier();
            barrier();
        }
        return;
    }
    if (w) {
        const u32 dof = (u32)((n >> 3) * 16384 + g * 128 + (n & 7) * 16);
        const char* dp = p.ws + OFF_D + (size_t)v * 32768 - 4096 + w * 8192;
        if (SPL) barrier();
        flag_wait(fl, F_VD + (vb & 3) + (v < 20 ? 0 : 4), 32);
#pragma nounroll
        for (int s = 0; s < NSEG - 1; ++s) {
            u32x4 df[16];
            u32 dfl;
            const int on = uni(1 - s);
            sfor<0, SPL>([&](auto I) { gld16c<I.v * 512 - 4096>(df[I.v], dp, dof); gap<HP0>(on); });
            if (SPL) asm volatile("s_cmp_ge_i32 %0, 0\n\ts_cbranch_scc1 .Lhb\n\ts_barrier\n.Lhb:" : : "s"(on) : "memory", "scc");
            dly(s == 1 ? HD1 : cfg.cR3 & 255);
            sfor<SPL, 16>([&](auto I) { gld16c<I.v * 512 - 4096>(df[I.v], dp, dof); gap<HP>(on); });
            dp += D_STRIDE;
            flag_ldm(dfl, fl, F_VD + pblk(s + 1) * 8 + (vb & RM), pblk(s + 1) != pblk(s));
            const u32 la = lds + moff + (s & 1) * 32768 + (w - 1) * 16384;
            sfor<0, 8>([&](auto I) {
                constexpr int i = I.v;
                waitv<15 - 2 * i>(df[2 * i]); waitv<15 - 2 * i>(df[2 * i + 1]);
                const u32x4 x = df[2 * i], y = df[2 * i + 1];
                const u32 lb = i & 2 ? la - xh * 4096 : la + xh * 4096;
                *LDSP(u32x4, lb + 2048 * i) = u32x4{x[0], y[0], x[2], y[2]};
                *LDSP(u32x4, lb + 2048 * i + 1024) = u32x4{x[1], y[1], x[3], y[3]};
                PIN();
            });
            waitvm<0>();
            asm volatile("" : "+v"(dfl));
            if (uni((int)dfl) < 31) {
                dly(FRD);
                flag_ld(dfl, fl, F_VD + pblk(s + 1) * 8 + (vb & RM));
                asm volatile("s_waitcnt vmcnt(0)" : "+v"(dfl) : : "memory");
            }
            barrier();
            if (uni((int)dfl) < 31) flag_wait(fl, F_VD + pblk(s + 1) * 8 + (vb & RM), 32);
        }
        return;
    }
    u32x32 rv[7], ra[4];
    u32x4 o;
    asm volatile("v_accvgpr_write_b32 a64, 0\n\t" "v_accvgpr_write_b32 a65, 0\n\t" "v_accvgpr_write_b32 a66, 0\n\t" "v_accvgpr_write_b32 a67, 0\n\t" "v_accvgpr_write_b32 a68, 0\n\t" "v_accvgpr_write_b32 a69, 0\n\t"
                 "v_accvgpr_write_b32 a70, 0\n\t" "v_accvgpr_write_b32 a71, 0\n\t" "v_accvgpr_write_b32 a72, 0\n\t" "v_accvgpr_write_b32 a73, 0\n\t" "v_accvgpr_write_b32 a74, 0\n\t" "v_accvgpr_write_b32 a75, 0\n\t"
                 "v_accvgpr_write_b32 a76, 0\n\t" "v_accvgpr_write_b32 a77, 0\n\t" "v_accvgpr_write_b32 a78, 0\n\t" "v_accvgpr_write_b32 a79, 0\n\t" "v_mov_b64 v[112:113], -1\n\t" "v_mov_b64 v[120:121], -1\n\t"
                 "v_mov_b64 v[128:129], -1\n\t" "v_mov_b64 v[136:137], -1\n\t" "v_mov_b64 v[144:145], -1\n\t" "v_mov_b64 v[152:153], -1\n\t" "v_mov_b64 v[160:161], -1\n\t" "v_mov_b64 v[168:169], -1\n\t"
                 "v_mov_b64 v[176:177], -1\n\t" "v_mov_b64 v[184:185], -1\n\t" "v_mov_b64 v[192:193], -1\n\t" "v_mov_b64 v[200:201], -1\n\t" "v_mov_b64 v[208:209], -1\n\t" "v_mov_b64 v[216:217], -1\n\t"
                 "v_mov_b64 v[224:225], -1\n\t" "v_mov_b64 v[232:233], -1\n\t"
                 : "={v[0:3]}"(o), "={v[16:47]}"(rv[0]), "={v[48:79]}"(rv[1]), "={v[80:111]}"(rv[2]), "={v[112:143]}"(rv[3]), "={v[144:175]}"(rv[4]), "={v[176:207]}"(rv[5]), "={v[208:239]}"(rv[6]), "={a[0:31]}"(ra[0]), "={a[32:63]}"(ra[1]), "={a[64:95]}"(ra[2]), "={a[96:127]}"(ra[3]));
    const u32 ho = (u32)(min(lane, 39) * 256 + (lane >> 5) * 8192);
    const u32 pof = (u32)((n & 4) * 128 + (n >> 3) * 256 + g * 64 + (n & 3) * 16 + 2048);
    const u32 uof = (u32)((n >> 3) * 4096 + g * 128 + (n & 7) * 16 + 512);
    u32x8 ad = u32x8{uof + xh * 512, pof + xh * 2048, (u32)(((g >> 1) ^ xh) * 256 + (n >> 2) * 64 + (n & 1) * 16), moff,
                     (lane & 2) ? 0xEEEEu : 0x4444u, (lane & 4) ? 0xEEEEu : 0x4444u, (u32)(v * 256 + (v >> 5) * 8192 + g * 64 + (n >> 3) * 32 + (n & 1) * 16 + (n & 6) * 2),
                     (lane & 1) ? 0x07060302u : 0x01000504u};
    u32 pob = pof - xh * 2048, uob = uof - xh * 512, pmo = moff ^ xh * 512;
    asm volatile("" : "+{v[4:11]}"(ad), "+v"(pob), "+v"(uob), "+v"(pmo));
    auto zero = [&]() {
        asm volatile("v_mov_b64 v[0:1], 0\n\t" "v_mov_b64 v[2:3], 0\n\t" "v_mov_b64 v[48:49], 0\n\t" "v_mov_b64 v[50:51], 0\n\t" "v_mov_b64 v[52:53], 0\n\t" "v_mov_b64 v[54:55], 0\n\t" "v_mov_b64 v[56:57], 0\n\t"
                     "v_mov_b64 v[58:59], 0\n\t" "v_mov_b64 v[60:61], 0\n\t" "v_mov_b64 v[62:63], 0\n\t" "v_mov_b64 v[64:65], 0\n\t" "v_mov_b64 v[66:67], 0\n\t" "v_mov_b64 v[68:69], 0\n\t" "v_mov_b64 v[70:71], 0\n\t"
                     "v_mov_b64 v[72:73], 0\n\t" "v_mov_b64 v[74:75], 0\n\t" "v_mov_b64 v[76:77], 0\n\t" "v_mov_b64 v[78:79], 0\n\t" "v_mov_b64 v[80:81], 0\n\t" "v_mov_b64 v[82:83], 0\n\t" "v_mov_b64 v[84:85], 0\n\t"
                     "v_mov_b64 v[86:87], 0\n\t" "v_mov_b64 v[88:89], 0\n\t" "v_mov_b64 v[90:91], 0\n\t" "v_mov_b64 v[92:93], 0\n\t" "v_mov_b64 v[94:95], 0\n\t" "v_mov_b64 v[96:97], 0\n\t" "v_mov_b64 v[98:99], 0\n\t"
                     "v_mov_b64 v[100:101], 0\n\t" "v_mov_b64 v[102:103], 0\n\t" "v_mov_b64 v[104:105], 0\n\t" "v_mov_b64 v[106:107], 0\n\t" "v_mov_b64 v[108:109], 0\n\t" "v_mov_b64 v[110:111], 0\n\t"
                     : : VALL : "memory");
    };
    const char* memp = p.ws + OFF_XF + 4096;
    char* up = ub + OFF_XF;
    int budget = 30000;
    Clock clk; clk.init();
    u32 tt = 0;
    for (int s = -1; s < NSEG - 1; ++s) {
        const char* const mnx = p.ws + mslot(sd, s + 1);
        bool all = false;
        int tries = 0;
        u64 tp8;
        while (true) {
            const u32 tgt = s >= 0 ? clk.sched(phi, (int)(tt >> 16), cfg.G, tries ? 2 : 1) : mt();
            u64 ok;
            asm volatile("s_cmp_lg_u32 %11, 0\n\t" "s_cbranch_scc0 .Lsp_v0\n\t" ".p2align 5\n\t" ".Lsp_v3:\n\t" "s_memtime vcc\n\t" "s_waitcnt lgkmcnt(0)\n\t" "s_sub_i32 s42, %10, vcc_lo\n\t" "s_cmp_gt_i32 s42, 80\n\t"
                         "s_cbranch_scc1 .Lsp_v3\n\t" "s_max_i32 s42, s42, 0\n\t" "s_sub_i32 s42, 92, s42\n\t" "s_and_b32 s42, s42, -4\n\t" "s_getpc_b64 vcc\n\t" "s_add_u32 vcc_lo, vcc_lo, s42\n\t"
                         "s_addc_u32 vcc_hi, vcc_hi, 0\n\t" "s_setpc_b64 vcc\n\t" "s_nop 0\n\t" "s_nop 0\n\t" "s_nop 0\n\t" "s_nop 0\n\t" "s_nop 0\n\t" "s_nop 0\n\t" "s_nop 0\n\t" "s_nop 0\n\t" "s_nop 0\n\t" "s_nop 0\n\t"
                         "s_nop 0\n\t" "s_nop 0\n\t" "s_nop 0\n\t" "s_nop 0\n\t" "s_nop 0\n\t" "s_nop 0\n\t" "s_nop 0\n\t" "s_nop 0\n\t" "s_nop 0\n\t" "s_nop 0\n\t"
                         "global_load_dwordx4 a[0:3], v5, %7 offset:-4096 sc1\n\t" "global_load_dwordx4 a[4:7], v5, %7 offset:-3072 sc1\n\t" "global_load_dwordx4 a[16:19], v5, %7 sc1\n\t"
                         "global_load_dwordx4 a[20:23], v5, %7 offset:1024 sc1\n\t" "global_load_dwordx4 a[32:35], v5, %8 offset:-4096 sc1\n\t" "global_load_dwordx4 a[36:39], v5, %8 offset:-3072 sc1\n\t"
                         "global_load_dwordx4 a[48:51], v5, %8 sc1\n\t" "global_load_dwordx4 a[52:55], v5, %8 offset:1024 sc1\n\t" "s_nop 14\n\t" "global_load_dwordx4 a[8:11], %12, %7 offset:-2048 sc1\n\t"
                         "global_load_dwordx4 a[12:15], %12, %7 offset:-1024 sc1\n\t" "global_load_dwordx4 a[24:27], %12, %7 offset:2048 sc1\n\t" "global_load_dwordx4 a[28:31], %12, %7 offset:3072 sc1\n\t"
                         "global_load_dwordx4 a[40:43], %12, %8 offset:-2048 sc1\n\t" "global_load_dwordx4 a[44:47], %12, %8 offset:-1024 sc1\n\t" "global_load_dwordx4 a[56:59], %12, %8 offset:2048 sc1\n\t"
                         "global_load_dwordx4 a[60:63], %12, %8 offset:3072 sc1\n\t" "s_nop 15\n\t" "s_nop 15\n\t" "s_nop 15\n\t" "s_nop 8\n\t" "global_load_dwordx4 a[80:83], %14, %6 sc1\n\t"
                         "global_load_dwordx4 a[84:87], %14, %6 offset:1024 sc1\n\t" "global_load_dwordx4 a[88:91], %14, %6 offset:2048 sc1\n\t" "global_load_dwordx4 a[92:95], %14, %6 offset:3072 sc1\n\t"
                         "s_mov_b64 exec, %9\n\t" "global_load_dwordx4 a[64:67], v6, %6 offset:-2048 sc1\n\t" "global_load_dwordx4 a[68:71], v6, %6 offset:-1536 sc1\n\t"
                         "global_load_dwordx4 a[72:75], v6, %6 offset:-1024 sc1\n\t" "global_load_dwordx4 a[76:79], v6, %6 offset:-512 sc1\n\t" "s_mov_b64 exec, -1\n\t" "s_mov_b32 s42, %11\n\t" ".Lsp_v5:\n\t"
                         "s_sub_u32 s42, s42, 1\n\t" "s_cmp_gt_i32 s42, 0\n\t" "s_cbranch_scc1 .Lsp_v5\n\t" "global_load_dwordx4 a[96:99], v4, %3 offset:-512 sc1\n\t" "global_load_dwordx4 a[100:103], %13, %3 sc1\n\t"
                         "global_load_dwordx4 a[104:107], v4, %3 offset:512 sc1\n\t" "global_load_dwordx4 a[108:111], %13, %3 offset:1024 sc1\n\t" "global_load_dwordx4 a[112:115], v4, %3 offset:1536 sc1\n\t"
                         "global_load_dwordx4 a[116:119], %13, %3 offset:2048 sc1\n\t" "global_load_dwordx4 a[120:123], v4, %3 offset:2560 sc1\n\t" "global_load_dwordx4 a[124:127], %13, %3 offset:3072 sc1\n\t"
                         "s_waitcnt vmcnt(31)\n\t" "v_smfmac_f32_16x16x64_bf16 v[48:51], a[0:3], v[112:119], v9\n\t" "s_waitcnt vmcnt(30)\n\t" "v_smfmac_f32_16x16x64_bf16 v[52:55], a[4:7], v[120:127], v9\n\t"
                         "s_waitcnt vmcnt(29)\n\t" "v_smfmac_f32_16x16x64_bf16 v[64:67], a[16:19], v[144:151], v9\n\t" "s_waitcnt vmcnt(28)\n\t" "v_cvt_pk_bf16_f32 v16, v48, v49\n\t" "v_cvt_pk_bf16_f32 v17, v50, v51\n\t"
                         "v_smfmac_f32_16x16x64_bf16 v[68:71], a[20:23], v[152:159], v9\n\t" "s_waitcnt vmcnt(27)\n\t" "v_cvt_pk_bf16_f32 v18, v52, v53\n\t" "v_cvt_pk_bf16_f32 v19, v54, v55\n\t"
                         "v_smfmac_f32_16x16x64_bf16 v[80:83], a[32:35], v[176:183], v9\n\t" "s_waitcnt vmcnt(26)\n\t" "v_cvt_pk_bf16_f32 v24, v64, v65\n\t" "v_cvt_pk_bf16_f32 v25, v66, v67\n\t"
                         "v_smfmac_f32_16x16x64_bf16 v[84:87], a[36:39], v[184:191], v9\n\t" "s_waitcnt vmcnt(25)\n\t" "v_cvt_pk_bf16_f32 v26, v68, v69\n\t" "v_cvt_pk_bf16_f32 v27, v70, v71\n\t"
                         "v_smfmac_f32_16x16x64_bf16 v[96:99], a[48:51], v[208:215], v9\n\t" "s_waitcnt vmcnt(24)\n\t" "v_cvt_pk_bf16_f32 v32, v80, v81\n\t" "v_cvt_pk_bf16_f32 v33, v82, v83\n\t"
                         "v_smfmac_f32_16x16x64_bf16 v[100:103], a[52:55], v[216:223], v9\n\t" "s_waitcnt vmcnt(23)\n\t" "v_cvt_pk_bf16_f32 v34, v84, v85\n\t" "v_cvt_pk_bf16_f32 v35, v86, v87\n\t"
                         "v_smfmac_f32_16x16x64_bf16 v[56:59], a[8:11], v[128:135], v9\n\t" "s_waitcnt vmcnt(22)\n\t" "v_cvt_pk_bf16_f32 v40, v96, v97\n\t" "v_cvt_pk_bf16_f32 v41, v98, v99\n\t"
                         "v_smfmac_f32_16x16x64_bf16 v[60:63], a[12:15], v[136:143], v9\n\t" "s_waitcnt vmcnt(21)\n\t" "v_cvt_pk_bf16_f32 v42, v100, v101\n\t" "v_cvt_pk_bf16_f32 v43, v102, v103\n\t"
                         "v_smfmac_f32_16x16x64_bf16 v[72:75], a[24:27], v[160:167], v9\n\t" "s_waitcnt vmcnt(20)\n\t" "v_cvt_pk_bf16_f32 v20, v56, v57\n\t" "v_cvt_pk_bf16_f32 v21, v58, v59\n\t"
                         "v_smfmac_f32_16x16x64_bf16 v[76:79], a[28:31], v[168:175], v9\n\t" "s_waitcnt vmcnt(19)\n\t" "v_cvt_pk_bf16_f32 v22, v60, v61\n\t" "v_cvt_pk_bf16_f32 v23, v62, v63\n\t"
                         "v_smfmac_f32_16x16x64_bf16 v[88:91], a[40:43], v[192:199], v9\n\t" "s_waitcnt vmcnt(18)\n\t" "v_cvt_pk_bf16_f32 v28, v72, v73\n\t" "v_cvt_pk_bf16_f32 v29, v74, v75\n\t"
                         "v_smfmac_f32_16x16x64_bf16 v[92:95], a[44:47], v[200:207], v9\n\t" "s_waitcnt vmcnt(17)\n\t" "v_cvt_pk_bf16_f32 v30, v76, v77\n\t" "v_cvt_pk_bf16_f32 v31, v78, v79\n\t"
                         "v_smfmac_f32_16x16x64_bf16 v[104:107], a[56:59], v[224:231], v9\n\t" "s_waitcnt vmcnt(16)\n\t" "v_cvt_pk_bf16_f32 v36, v88, v89\n\t" "v_cvt_pk_bf16_f32 v37, v90, v91\n\t"
                         "v_smfmac_f32_16x16x64_bf16 v[108:111], a[60:63], v[232:239], v9\n\t" "s_waitcnt vmcnt(11)\n\t" "v_cvt_pk_bf16_f32 v38, v92, v93\n\t" "v_cvt_pk_bf16_f32 v39, v94, v95\n\t"
                         "v_smfmac_f32_16x16x64_bf16 v[0:3], a[64:67], v[16:23], v8\n\t" "s_waitcnt vmcnt(10)\n\t" "v_cvt_pk_bf16_f32 v44, v104, v105\n\t" "v_cvt_pk_bf16_f32 v45, v106, v107\n\t"
                         "v_smfmac_f32_16x16x64_bf16 v[0:3], a[68:71], v[24:31], v8\n\t" "s_waitcnt vmcnt(9)\n\t" "v_cvt_pk_bf16_f32 v46, v108, v109\n\t" "v_cvt_pk_bf16_f32 v47, v110, v111\n\t"
                         "v_smfmac_f32_16x16x64_bf16 v[0:3], a[72:75], v[32:39], v8\n\t" "s_waitcnt vmcnt(8)\n\t" "v_smfmac_f32_16x16x64_bf16 v[0:3], a[76:79], v[40:47], v8\n\t" "s_waitcnt vmcnt(6)\n\t"
                         "v_smfmac_f32_16x16x64_bf16 v[0:3], a[80:83], a[96:103], v8\n\t" "s_waitcnt vmcnt(4)\n\t" "v_smfmac_f32_16x16x64_bf16 v[0:3], a[84:87], a[104:111], v8\n\t" "s_waitcnt vmcnt(2)\n\t"
                         "v_smfmac_f32_16x16x64_bf16 v[0:3], a[88:91], a[112:119], v8\n\t" "s_waitcnt vmcnt(0)\n\t" "v_smfmac_f32_16x16x64_bf16 v[0:3], a[92:95], a[120:127], v8\n\t" "s_branch .Lv_end\n\t"
                         ".p2align 5\n\t" "s_nop 0\n\t" "s_nop 0\n\t" "s_nop 0\n\t" ".Lsp_v0:\n\t" "s_memtime vcc\n\t" "s_waitcnt lgkmcnt(0)\n\t" "s_sub_i32 s42, %10, vcc_lo\n\t" "s_cmp_gt_i32 s42, 80\n\t"
                         "s_cbranch_scc1 .Lsp_v0\n\t" "s_max_i32 s42, s42, 0\n\t" "s_sub_i32 s42, 92, s42\n\t" "s_and_b32 s42, s42, -4\n\t" "s_getpc_b64 vcc\n\t" "s_add_u32 vcc_lo, vcc_lo, s42\n\t"
                         "s_addc_u32 vcc_hi, vcc_hi, 0\n\t" "s_setpc_b64 vcc\n\t" "s_nop 0\n\t" "s_nop 0\n\t" "s_nop 0\n\t" "s_nop 0\n\t" "s_nop 0\n\t" "s_nop 0\n\t" "s_nop 0\n\t" "s_nop 0\n\t" "s_nop 0\n\t" "s_nop 0\n\t"
                         "s_nop 0\n\t" "s_nop 0\n\t" "s_nop 0\n\t" "s_nop 0\n\t" "s_nop 0\n\t" "s_nop 0\n\t" "s_nop 0\n\t" "s_nop 0\n\t" "s_nop 0\n\t" "s_nop 0\n\t"
                         "global_load_dwordx4 a[96:99], v4, %3 offset:-512 sc1\n\t" "global_load_dwordx4 a[104:107], v4, %3 offset:512 sc1\n\t" "global_load_dwordx4 a[112:115], v4, %3 offset:1536 sc1\n\t"
                         "global_load_dwordx4 a[120:123], v4, %3 offset:2560 sc1\n\t" "global_load_dwordx4 a[100:103], %13, %3 sc1\n\t" "global_load_dwordx4 a[108:111], %13, %3 offset:1024 sc1\n\t"
                         "global_load_dwordx4 a[116:119], %13, %3 offset:2048 sc1\n\t" "global_load_dwordx4 a[124:127], %13, %3 offset:3072 sc1\n\t" "s_nop 9\n\t"
                         "global_load_dwordx4 a[0:3], v5, %7 offset:-4096 sc1\n\t" "global_load_dwordx4 a[4:7], v5, %7 offset:-3072 sc1\n\t" "global_load_dwordx4 a[16:19], v5, %7 sc1\n\t"
                         "global_load_dwordx4 a[20:23], v5, %7 offset:1024 sc1\n\t" "global_load_dwordx4 a[32:35], v5, %8 offset:-4096 sc1\n\t" "global_load_dwordx4 a[36:39], v5, %8 offset:-3072 sc1\n\t"
                         "global_load_dwordx4 a[48:51], v5, %8 sc1\n\t" "global_load_dwordx4 a[52:55], v5, %8 offset:1024 sc1\n\t" "s_nop 14\n\t" "global_load_dwordx4 a[8:11], %12, %7 offset:-2048 sc1\n\t"
                         "global_load_dwordx4 a[12:15], %12, %7 offset:-1024 sc1\n\t" "global_load_dwordx4 a[24:27], %12, %7 offset:2048 sc1\n\t" "global_load_dwordx4 a[28:31], %12, %7 offset:3072 sc1\n\t"
                         "global_load_dwordx4 a[40:43], %12, %8 offset:-2048 sc1\n\t" "global_load_dwordx4 a[44:47], %12, %8 offset:-1024 sc1\n\t" "global_load_dwordx4 a[56:59], %12, %8 offset:2048 sc1\n\t"
                         "global_load_dwordx4 a[60:63], %12, %8 offset:3072 sc1\n\t" "s_nop 15\n\t" "s_nop 15\n\t" "s_nop 15\n\t" "s_nop 8\n\t" "global_load_dwordx4 a[80:83], %14, %6 sc1\n\t"
                         "global_load_dwordx4 a[84:87], %14, %6 offset:1024 sc1\n\t" "global_load_dwordx4 a[88:91], %14, %6 offset:2048 sc1\n\t" "global_load_dwordx4 a[92:95], %14, %6 offset:3072 sc1\n\t"
                         "s_mov_b64 exec, %9\n\t" "global_load_dwordx4 a[64:67], v6, %6 offset:-2048 sc1\n\t" "global_load_dwordx4 a[68:71], v6, %6 offset:-1536 sc1\n\t"
                         "global_load_dwordx4 a[72:75], v6, %6 offset:-1024 sc1\n\t" "global_load_dwordx4 a[76:79], v6, %6 offset:-512 sc1\n\t" "s_mov_b64 exec, -1\n\t" "s_waitcnt vmcnt(23)\n\t"
                         "v_smfmac_f32_16x16x64_bf16 v[48:51], a[0:3], v[112:119], v9\n\t" "s_waitcnt vmcnt(22)\n\t" "v_smfmac_f32_16x16x64_bf16 v[52:55], a[4:7], v[120:127], v9\n\t" "s_waitcnt vmcnt(21)\n\t"
                         "v_smfmac_f32_16x16x64_bf16 v[64:67], a[16:19], v[144:151], v9\n\t" "s_waitcnt vmcnt(20)\n\t" "v_cvt_pk_bf16_f32 v16, v48, v49\n\t" "v_cvt_pk_bf16_f32 v17, v50, v51\n\t"
                         "v_smfmac_f32_16x16x64_bf16 v[68:71], a[20:23], v[152:159], v9\n\t" "s_waitcnt vmcnt(19)\n\t" "v_cvt_pk_bf16_f32 v18, v52, v53\n\t" "v_cvt_pk_bf16_f32 v19, v54, v55\n\t"
                         "v_smfmac_f32_16x16x64_bf16 v[80:83], a[32:35], v[176:183], v9\n\t" "s_waitcnt vmcnt(18)\n\t" "v_cvt_pk_bf16_f32 v24, v64, v65\n\t" "v_cvt_pk_bf16_f32 v25, v66, v67\n\t"
                         "v_smfmac_f32_16x16x64_bf16 v[84:87], a[36:39], v[184:191], v9\n\t" "s_waitcnt vmcnt(17)\n\t" "v_cvt_pk_bf16_f32 v26, v68, v69\n\t" "v_cvt_pk_bf16_f32 v27, v70, v71\n\t"
                         "v_smfmac_f32_16x16x64_bf16 v[96:99], a[48:51], v[208:215], v9\n\t" "s_waitcnt vmcnt(16)\n\t" "v_cvt_pk_bf16_f32 v32, v80, v81\n\t" "v_cvt_pk_bf16_f32 v33, v82, v83\n\t"
                         "v_smfmac_f32_16x16x64_bf16 v[100:103], a[52:55], v[216:223], v9\n\t" "s_waitcnt vmcnt(15)\n\t" "v_cvt_pk_bf16_f32 v34, v84, v85\n\t" "v_cvt_pk_bf16_f32 v35, v86, v87\n\t"
                         "v_smfmac_f32_16x16x64_bf16 v[56:59], a[8:11], v[128:135], v9\n\t" "s_waitcnt vmcnt(14)\n\t" "v_cvt_pk_bf16_f32 v40, v96, v97\n\t" "v_cvt_pk_bf16_f32 v41, v98, v99\n\t"
                         "v_smfmac_f32_16x16x64_bf16 v[60:63], a[12:15], v[136:143], v9\n\t" "s_waitcnt vmcnt(13)\n\t" "v_cvt_pk_bf16_f32 v42, v100, v101\n\t" "v_cvt_pk_bf16_f32 v43, v102, v103\n\t"
                         "v_smfmac_f32_16x16x64_bf16 v[72:75], a[24:27], v[160:167], v9\n\t" "s_waitcnt vmcnt(12)\n\t" "v_cvt_pk_bf16_f32 v20, v56, v57\n\t" "v_cvt_pk_bf16_f32 v21, v58, v59\n\t"
                         "v_smfmac_f32_16x16x64_bf16 v[76:79], a[28:31], v[168:175], v9\n\t" "s_waitcnt vmcnt(11)\n\t" "v_cvt_pk_bf16_f32 v22, v60, v61\n\t" "v_cvt_pk_bf16_f32 v23, v62, v63\n\t"
                         "v_smfmac_f32_16x16x64_bf16 v[88:91], a[40:43], v[192:199], v9\n\t" "s_waitcnt vmcnt(10)\n\t" "v_cvt_pk_bf16_f32 v28, v72, v73\n\t" "v_cvt_pk_bf16_f32 v29, v74, v75\n\t"
                         "v_smfmac_f32_16x16x64_bf16 v[92:95], a[44:47], v[200:207], v9\n\t" "s_waitcnt vmcnt(9)\n\t" "v_cvt_pk_bf16_f32 v30, v76, v77\n\t" "v_cvt_pk_bf16_f32 v31, v78, v79\n\t"
                         "v_smfmac_f32_16x16x64_bf16 v[104:107], a[56:59], v[224:231], v9\n\t" "s_waitcnt vmcnt(8)\n\t" "v_cvt_pk_bf16_f32 v36, v88, v89\n\t"
                         "v_smfmac_f32_16x16x64_bf16 v[108:111], a[60:63], v[232:239], v9\n\t" "s_waitcnt vmcnt(7)\n\t" "v_cvt_pk_bf16_f32 v37, v90, v91\n\t"
                         "v_smfmac_f32_16x16x64_bf16 v[0:3], a[80:83], a[96:103], v8\n\t" "s_waitcnt vmcnt(6)\n\t" "v_cvt_pk_bf16_f32 v38, v92, v93\n\t" "v_smfmac_f32_16x16x64_bf16 v[0:3], a[84:87], a[104:111], v8\n\t"
                         "s_waitcnt vmcnt(5)\n\t" "v_cvt_pk_bf16_f32 v39, v94, v95\n\t" "v_smfmac_f32_16x16x64_bf16 v[0:3], a[88:91], a[112:119], v8\n\t" "s_waitcnt vmcnt(4)\n\t" "v_cvt_pk_bf16_f32 v44, v104, v105\n\t"
                         "v_smfmac_f32_16x16x64_bf16 v[0:3], a[92:95], a[120:127], v8\n\t" "s_waitcnt vmcnt(3)\n\t" "v_cvt_pk_bf16_f32 v45, v106, v107\n\t" "v_smfmac_f32_16x16x64_bf16 v[0:3], a[64:67], v[16:23], v8\n\t"
                         "s_waitcnt vmcnt(2)\n\t" "v_cvt_pk_bf16_f32 v46, v108, v109\n\t" "v_smfmac_f32_16x16x64_bf16 v[0:3], a[68:71], v[24:31], v8\n\t" "s_waitcnt vmcnt(1)\n\t" "v_cvt_pk_bf16_f32 v47, v110, v111\n\t"
                         "v_smfmac_f32_16x16x64_bf16 v[0:3], a[72:75], v[32:39], v8\n\t" "s_waitcnt vmcnt(0)\n\t" "v_smfmac_f32_16x16x64_bf16 v[0:3], a[76:79], v[40:47], v8\n\t" ".Lv_end:\n\t" "s_nop 7\n\t"
                         "v_pk_add_f32 v[0:1], v[0:1], v[2:3]\n\t" "v_cvt_pk_bf16_f32 v2, v0, v1\n\t" "v_cmp_u_f32 vcc, v0, v1\n\t" "s_cmp_eq_u64 vcc, 0\n\t"
                         "v_mov_b32_dpp v3, v2 quad_perm:[1,0,3,2] row_mask:0xf bank_mask:0xf\n\t" "s_cselect_b64 %0, -1, 0\n\t" "v_perm_b32 v0, v2, v3, v11\n\t" "s_mov_b64 exec, %0\n\t"
                         "global_store_dword v10, v0, %5 offset:-4096 sc1\n\t" "global_store_dword v10, v0, %4 offset:-4096 sc1\n\t" "s_memrealtime %1\n\t" "s_mov_b64 exec, -1\n\t" "s_waitcnt lgkmcnt(0)\n\t"
                         : "=&s"(ok), "=&s"(tp8)
                         : "{v[4:11]}"(ad), "s"(up), "s"(mnx), "s"((u64)mnx ^ 8192), "s"(memp + 16384), "s"(memp + 32768 - 2048), "s"(memp + 49152 - 2048), "s"(0xc30000c3c30000c3ull),
                           "s"(uni((int)(tgt - (u32)(v < 16 ? cfg.dU - (v >> 3) * cfg.s2 : cfg.cR2)))), "s"(v < 16 ? 0 : (v < 24 ? 1 : cfg.T3)), "v"(pob), "v"(uob), "v"(pmo), "s"(tries), VALL : "memory", "vcc", "scc", "s42");
            if (__builtin_expect(ok != 0, 1) || s < 0) break;
            if (all || --budget < 0) {
                asm volatile("v_min_u32 v0, -2, v0\n\tglobal_store_dword v10, v0, %0 offset:-4096 sc1\n\tglobal_store_dword v10, v0, %1 offset:-4096 sc1"
                             : : "s"(mnx), "s"((u64)mnx ^ 8192), "{v[4:11]}"(ad), VALL : "memory");
                break;
            }
            if ((++tries & 15) == 0) {
                u32 m = 0;
#pragma nounroll
                for (int i = 0; i < 32; ++i) {
                    u32x4 t;
                    gld16c<0>(t, i < 24 ? memp + 12288 : up, i < 20 ? (4 + i + ((4 + i) & 24)) * 1024 + ad[3] : (i < 24 ? i * 512 - 8192 + ad[2] : (i & 7) * 512 - 512 + (i & 1 ? uob : ad[0]))); waitv<0>(t);
                    m = max3u(max3u(m, t[0], t[1]), t[2], t[3]);
                }
                all = __builtin_amdgcn_ballot_w64(m == 0xffffffffu) == 0;
            }
            phi += (u32)cfg.G;
            zero();
        }
        asm volatile("s_barrier\n\t" "ds_read_b128 v[112:115], %0\n\t" "ds_read_b128 v[116:119], %0 offset:1024\n\t" "ds_read_b128 v[120:123], %0 offset:2048\n\t" "ds_read_b128 v[124:127], %0 offset:3072\n\t"
                     "ds_read_b128 v[128:131], %0 offset:4096\n\t" "ds_read_b128 v[132:135], %0 offset:5120\n\t" "ds_read_b128 v[136:139], %0 offset:6144\n\t" "ds_read_b128 v[140:143], %0 offset:7168\n\t"
                     "ds_read_b128 v[144:147], %0 offset:8192\n\t" "ds_read_b128 v[148:151], %0 offset:9216\n\t" "ds_read_b128 v[152:155], %0 offset:10240\n\t" "ds_read_b128 v[156:159], %0 offset:11264\n\t"
                     "ds_read_b128 v[160:163], %0 offset:12288\n\t" "ds_read_b128 v[164:167], %0 offset:13312\n\t" "ds_read_b128 v[168:171], %0 offset:14336\n\t" "ds_read_b128 v[172:175], %0 offset:15360\n\t"
                     "ds_read_b128 v[176:179], %0 offset:16384\n\t" "ds_read_b128 v[180:183], %0 offset:17408\n\t" "ds_read_b128 v[184:187], %0 offset:18432\n\t" "ds_read_b128 v[188:191], %0 offset:19456\n\t"
                     "ds_read_b128 v[192:195], %0 offset:20480\n\t" "ds_read_b128 v[196:199], %0 offset:21504\n\t" "ds_read_b128 v[200:203], %0 offset:22528\n\t" "ds_read_b128 v[204:207], %0 offset:23552\n\t"
                     "ds_read_b128 v[208:211], %0 offset:24576\n\t" "ds_read_b128 v[212:215], %0 offset:25600\n\t" "ds_read_b128 v[216:219], %0 offset:26624\n\t" "ds_read_b128 v[220:223], %0 offset:27648\n\t"
                     "ds_read_b128 v[224:227], %0 offset:28672\n\t" "ds_read_b128 v[228:231], %0 offset:29696\n\t" "ds_read_b128 v[232:235], %0 offset:30720\n\t" "ds_read_b128 v[236:239], %0 offset:31744\n\t"
                     : : "v"(lds + ad[3] + ((s + 1) & 1) * 32768), VALL : "memory");
        if (s >= 0) {
            follow(phi, (int)(tt & 0xffffu) + 5 * (v >> 2) / 2 - (cfg.dV >> 16 & 255), (u32)tp8, cfg.G);
            if (vb == NVB - 1 && lane < 8) gst8(fl, (F_CUR + lane) * 128, u32x2{(u32)s - 1, phi + (tt & 0xffffu)});
        }
        phi += tt & 0xffffu;
        memp = mnx; up = ub + uslot(sd, s + 1);
        zero();
        tt = sched_ld0(s + 1);
        if (s < 0) { phi = go_wait(fl, vb) - (tt >> 16); edge(clk.r1, clk.m1); }
        clk.calib();
        while (s >= 0) {
            u32 h0, h1;
            spin_to(clk.at(phi + (u32)(cfg.dV >> 8 & 255)));
            asm volatile("global_load_dword %0, %1, %2 offset:-4096 sc1" : "=&v"(h1) : "v"(ho & 0xfff), "s"(memp) : "memory");
            spin_to(clk.at(phi + (u32)(cfg.dV & 255)));
            asm volatile("global_load_dword %0, %1, %2 offset:-4096 sc1\n\ts_sleep 5" : "=&v"(h0) : "v"(ho), "s"(memp) : "memory");
            edge(clk.r1, clk.m1);
            asm volatile("s_waitcnt vmcnt(0)" : "+v"(h0), "+v"(h1) : : "memory");
            h0 = max(h0, h1);
            if (__builtin_amdgcn_ballot_w64(h0 == 0xffffffffu) == 0 || --budget < 0) break;
            phi += (u32)cfg.G;
        }
        asm volatile(".Lkm_lv:");
    }
}

DI void xcopy_task(const Ptrs& p, int j, int w) {
    const int tid = 64 * w + lanex();
    Sync y;
    sync_ld(y, p.ws);
    u32x4 xv[10];
    sfor<0, 10>([&](auto I) { gld16<I.v * 128>(xv[I.v], p.x + (size_t)j * 40960, (u32)((tid & 31) * 1280 + (tid >> 5) * 16)); });
    sfor<0, 10>([&](auto I) { waitv<9 - I.v>(xv[I.v]); });
    asm volatile("" : "+v"(y.r), "+v"(y.g), "+v"(y.e));
    sfor<0, 10>([&](auto I) { gst16(p.ws + OFF_XF + (size_t)j * 40960 + I.v * 4096, (u32)tid * 16, xv[I.v]); });
    waitvm<0>();
    barrier();
    const u64 t = sync_tag(y.e);
    if (tid == 0) gst8(p.ws + OFF_LAT, 1536 + (u32)j * 8, u32x2{(u32)t, (u32)(t >> 32)});
}
DI void xcopy_wait(const char* ws, u64 t, int lo = 0, int msk = 63) {
#pragma nounroll
    for (int it = 0; it < (1 << 16); ++it) {
        u32x2 r;
        asm volatile("s_nop 4\n\tglobal_load_dwordx2 %0, %1, %2 offset:1536 sc1\n\ts_waitcnt vmcnt(0)" : "=&v"(r) : "v"((lo + (lanex() & msk)) * 8), "s"(ws + OFF_LAT) : "memory");
        if (__builtin_amdgcn_ballot_w64(r[0] != (u32)t || r[1] != (u32)(t >> 32)) == 0) break;
        __builtin_amdgcn_s_sleep(XSL);
    }
}
DI void init_task(const Ptrs& p, int j, u32 lds, int w) {
    const int lane = lanex(), tid = 64 * w + lane;
    Sync y;
    sync_ld(y, p.ws);
    const u32x4 ff = u32x4{~0u, ~0u, ~0u, ~0u};
    const int i = j * 256 + tid, r = j < 8 ? j & 1 : min(j / 2 - 2, NREG - 1);
    char* const vb = p.ws + (r < 2 ? 0 : OFF_U - (4u << 20)) + ((size_t)r << 21) + 1984 * 1024 + (j < 8 ? j & 6 : (j & 1) * 2 + (j >> 1 == 15) * 4) * 8192;
    const u32 vo = (u32)((w >> 1) * 8192 + (w & 1) * 1024);
    gst16(vb, vo + lane * 16, ff);
    if (i < F_N * 8) gst16(p.ws + OFF_ST, (u32)i * 16, u32x4{i == F_ONE * 8 ? 1000u : ~0u, ~0u, ~0u, ~0u});
    if (i < 512) gst16(p.ws + OFF_GATE, (u32)i * 16, ff);
    gst16(p.ws + 65536, (u32)i * 16, ff);
    if (i < 4096) {
        const int k = (i & 511) + ((i >> 1) & 0x600);
        u32x4 m;
        gld16<0>(m, p.mem0, (u32)((2 * ((k >> 2) & 3) + (k & 1)) * 1280 + min(2 * (k >> 4) + ((k >> 1) & 1), 79) * 16));
        waitv<0>(m);
        gst16(p.ws, (u32)i * 16, k < 640 ? u32x4{min(m[0], 0xfffffffeu), min(m[1], 0xfffffffeu), min(m[2], 0xfffffffeu), min(m[3], 0xfffffffeu)} : ff);
    }
#pragma unroll
    for (int c = 0; c < 6; ++c) gst16(p.ws + OFF_U + c * 131072 - (c == 5) * 122880, (u32)i * 16, ff);
    waitvm<0>();
    asm volatile("" : "+v"(y.r), "+v"(y.g), "+v"(y.e));
    barrier();
    if (w) return;
    u32 dt[2] = {0xffffffu, 0xffffffu};
#pragma unroll
    for (int c = 0; c < 2 * NSAMP; ++c) {
        u32 v;
        const u32 t0 = mt();
        asm volatile("global_load_dword %0, %1, %2 sc1\n\ts_waitcnt vmcnt(0)" : "=&v"(v) : "v"((c & 1) * 8192 + (c >> 1) * 256), "s"(vb) : "memory");
        dt[c & 1] = min(dt[c & 1], mt() - t0);
    }
    const u64 t = sync_tag(y.e);
    const u32 vote = (u32)((dt[0] > dt[1]) ^ xhalf() ^ (int)((vb - p.ws) >> 21)) & 1u;
    if (tid == 0) {
        if (j == 0) gst8(p.ws + OFF_LAT, 512, u32x2{(u32)t, rt()});
        gst8(p.ws + OFF_LAT, (u32)j * 8, u32x2{(u32)t | vote, (u32)(t >> 32)});
    }
}

DI float xmax(float v) {
    const auto a = __builtin_amdgcn_permlane32_swap(__float_as_uint(v), __float_as_uint(v), false, false);
    v = fmaxf(__uint_as_float(a[0]), __uint_as_float(a[1]));
    const auto b = __builtin_amdgcn_permlane16_swap(__float_as_uint(v), __float_as_uint(v), false, false);
    return fmaxf(__uint_as_float(b[0]), __uint_as_float(b[1]));
}
DI float xsum(float v) {
    const auto a = __builtin_amdgcn_permlane32_swap(__float_as_uint(v), __float_as_uint(v), false, false);
    v = __uint_as_float(a[0]) + __uint_as_float(a[1]);
    const auto b = __builtin_amdgcn_permlane16_swap(__float_as_uint(v), __float_as_uint(v), false, false);
    return __uint_as_float(b[0]) + __uint_as_float(b[1]);
}
DI void gst16c(char* sbase, u32 voff, u32x4 v) {
    asm volatile("global_store_dwordx4 %0, %1, %2 sc1\n\ts_nop 1" : : "v"(voff), "v"(v), "s"(sbase) : "memory");
}
#define WAIT6(N, r, o) asm volatile("s_waitcnt vmcnt(" #N ")" : "+v"(r[o]), "+v"(r[o + 1]), "+v"(r[o + 2]), "+v"(r[o + 3]), "+v"(r[o + 4]), "+v"(r[o + 5]) : : "memory")
#define WSLEEP 90
DI void wg_wait(const char* fl, int w, int i0, int t0, int i1, int t1, int i2, int t2, int i3, int t3, int dt, bool acq) {
    if (w == 0) {
        const int lane = lanex(), tg = lane == 1 ? t1 : (lane == 2 ? t2 : (lane == 3 ? t3 : t0));
        const int ix = tg <= 0 ? i0 : (lane == 1 ? i1 : (lane == 2 ? i2 : (lane == 3 ? i3 : i0)));
#pragma nounroll
        for (int it = 0; it < (1 << 14); ++it) {
            u32 v;
            flag_ld(v, fl, ix);
            asm volatile("s_waitcnt vmcnt(0)" : "+v"(v) : : "memory");
            if (__builtin_amdgcn_ballot_w64((int)(v + 1u) < tg) == 0) break;
            const u32 wake = rt() + (u32)(dt + 180 * min(max(t0 - 2 - uni((int)v), 0), 64));
            while ((int)(rt() - wake) < 0) __builtin_amdgcn_s_sleep(4);
        }
        if (acq) acquire();
    }
    barrier();
}
#define TEST 172
DI u32 step_wait(const char* fl, int w, int gr, int t, u32 lds) {
    if (w == 0) {
        u32x2 v;
#pragma nounroll
        for (int it = 0; it < (1 << 14); ++it) {
            asm volatile("s_nop 4\n\tglobal_load_dwordx2 %0, %1, %2 sc1\n\ts_waitcnt vmcnt(0)" : "=&v"(v) : "v"((F_CUR + gr) * 128), "s"(fl) : "memory");
            const int d = t - 1 - uni((int)v[0]);
            if (d <= 0) { v[1] += (u32)(d * TEST); break; }
            const u32 now = rt();
            nap(min(max((int)((u32)uni((int)v[1]) - now) + d * TEST + 10, 40), 2000));
        }
        *LDSP(u32, lds + GRING * GBUF + 4) = v[1];
    }
    barrier();
    return (u32)uni((int)*LDSP(u32, lds + GRING * GBUF + 4));
}
#define PDL 115
#define ODL 240
#define TDT 35
template <bool LATE>
DI void px_wave(const Ptrs& p, const ChainCfg& cfg, int s, int h, int slow, int w, u32 lds, bool keep) {
    const int lane = lanex(), n = lane & 15, g = lane >> 4;
    const char* qb = p.ws + OFF_QX + (size_t)(s * NH + h) * 12288;
    const char* kb = p.ws + OFF_KX + (size_t)(s * NH + h) * 12288;
    const char* mb = p.ws + OFF_KM + (size_t)(s * NH + h) * 3072;
    const u32 mo = (u32)(g * 128 + (n & 7) * 16);
    u32x4 qf[12], kf[12], mf[6];
    sfor<0, 12>([&](auto I) { gld16x<(I.v % 3) * 1024>(qf[I.v], qb + (I.v / 3) * 3072, lane * 16); gap<SMP>(slow); });
    if (!SMP) pace(slow);
    sfor<0, 12>([&](auto I) { if constexpr (LATE || !KNT) gld16c<(I.v % 3) * 1024>(kf[I.v], kb + (I.v / 3) * 3072, lane * 16); else gld16x<(I.v % 3) * 1024>(kf[I.v], kb + (I.v / 3) * 3072, lane * 16); gap<SMP>(slow); });
    if (!SMP) pace(slow);
    if constexpr (LATE) { WAIT6(0, qf, 0); WAIT6(0, qf, 6); WAIT6(0, kf, 0); }
    else {
        sfor<0, 6>([&](auto I) { gld16x<I.v * 512>(mf[I.v], mb, mo); });
        WAIT6(24, qf, 0); WAIT6(18, qf, 6); WAIT6(12, kf, 0);
    }
    f32x4 sc[2][3];
#pragma unroll
    for (int tt = 0; tt < 2; ++tt)
#pragma unroll
        for (int T = 0; T < 3; ++T) sc[tt][T] = f32x4{0.f, 0.f, 0.f, 0.f};
#pragma unroll
    for (int ks = 0; ks < 6; ++ks) { sc[0][0] = mfma16(kf[ks], qf[ks], sc[0][0]); sc[1][0] = mfma16(kf[ks], qf[6 + ks], sc[1][0]); }
    WAIT6(6, kf, 6);
#pragma unroll
    for (int ks = 0; ks < 6; ++ks) sc[1][1] = mfma16(kf[6 + ks], qf[6 + ks], sc[1][1]);
    if constexpr (LATE) {
        until(step_wait(p.ws + OFF_ST, w, h & 7, s - 1, lds) + (u32)(s == NSEG - 1 ? cfg.T : cfg.T + PDX));
        bool ok = false;
#pragma nounroll
        for (int it = 0; it < (1 << 14); ++it) {
            u32 v;
            sfor<0, 6>([&](auto I) { gld16c<I.v * 512>(mf[I.v], mb, mo); });
            flag_ldm(v, p.ws + OFF_ST, (s < NSEG - 2 ? F_CUR : F_PROG + 8 * s) + (h & 7), it > 3);
            WAIT6(0, mf, 0);
            asm volatile("" : "+v"(v));
            u32 m = 0;
#pragma unroll
            for (int i = 0; i < 24; ++i) m = max(m, mf[i >> 2][i & 3]);
            if (ok || __builtin_amdgcn_ballot_w64(m == ~0u) == 0) break;
            ok = it > 3 && uni((int)v) >= (s < NSEG - 2 ? s : 7);
            if (s < NSEG - 1) nap(60);
        }
    }
    WAIT6(0, mf, 0);
#pragma unroll
    for (int ks = 0; ks < 6; ++ks) { sc[0][2] = mfma16(mf[ks], qf[ks], sc[0][2]); sc[1][2] = mfma16(mf[ks], qf[6 + ks], sc[1][2]); }
    const float kscale = 0.07216878364870322f;
    char* pb = p.ws + OFF_PX + (size_t)s * 81920;
#pragma unroll
    for (int tt = 0; tt < 2; ++tt) {
        float v[12], mx = -3.0e38f;
#pragma unroll
        for (int i = 0; i < 12; ++i) {
            const float t = rbf(rbf(sc[tt][i >> 2][i & 3]) * kscale);
            v[i] = (i >= 8 || 4 * g + (i & 3) + 16 * (i >> 2) <= 16 * tt + n) ? t : -3.0e38f;
            mx = fmaxf(mx, v[i]);
        }
        mx = xmax(mx);
        float sx = 0.f, sm = 0.f;
#pragma unroll
        for (int i = 0; i < 12; ++i) {
            v[i] = __builtin_amdgcn_exp2f((v[i] - mx) * 1.4426950408889634f);
            if (i < 8) sx += v[i]; else sm += v[i];
        }
        const float inv = __builtin_amdgcn_rcpf(xsum(sx + 0.5f * sm));
        const u32x4 px = u32x4{cvtpk(v[0] * inv, v[1] * inv), cvtpk(v[2] * inv, v[3] * inv), cvtpk(v[4] * inv, v[5] * inv), cvtpk(v[6] * inv, v[7] * inv)};
        const u32x2 pm = u32x2{cvtpk(v[9] * inv, v[8] * inv), cvtpk(v[11] * inv, v[10] * inv)};
        const int ox = h * 2048 + tt * 1024, om = 65536 + (h >> 2) * 2048 + (h & 3) * 256 + tt * 1024;
        if (!LATE && keep) {
            *LDSP(u32x4, lds + ox + lane * 16) = px;
            *LDSP(u32x2, lds + om + n * 16 + (g & 1) * 8) = pm;
        } else {
            gst16c(pb + ox, lane * 16, px);
            const auto a = __builtin_amdgcn_permlane16_swap(pm[0], pm[0], false, false), b = __builtin_amdgcn_permlane16_swap(pm[1], pm[1], false, false);
            if (g == 0) gst16c(pb + om, (u32)(n * 16), u32x4{a[0], b[0], a[1], b[1]});
        }
    }
}

#define WAIT10(N, r) asm volatile("s_waitcnt vmcnt(" #N ")" : "+v"(r[0]), "+v"(r[1]), "+v"(r[2]), "+v"(r[3]), "+v"(r[4]), "+v"(r[5]), "+v"(r[6]), "+v"(r[7]), "+v"(r[8]), "+v"(r[9]) : : "memory")
DI void out_task(const Ptrs& p, int s, int cg0, int cg1, bool kept, u32 lds, int slow, int w, int sd) {
    u32x4 pf[2][10];
    if (kept) {
        const u32 la = lds + lanex() * 16;
#pragma unroll
        for (int i = 0; i < 20; ++i) pf[i & 1][i >> 1] = ldsr16(la + (i < 16 ? w * 16384 + i * 1024 : 65536 + w * 4096 + (i - 16) * 1024));
    } else {
        const char* pb = p.ws + OFF_PX + (size_t)s * 81920;
        const u32 l16 = lanex() * 16;
        sfor<0, 20>([&](auto I) { gld16c<(I.v & 3) * 1024>(pf[I.v & 1][I.v >> 1], pb + (I.v < 16 ? w * 16384 + (I.v >> 2) * 4096 : 65536 + w * 4096), l16); gap<SMO>(slow); if (!SMO && I.v == 9) pace(slow); });
        WAIT10(0, pf[0]); WAIT10(0, pf[1]);
    }
#pragma nounroll
    for (int cg = cg0; cg < cg1; ++cg) {
    const int lane = lanex(), tid = 64 * w + lane, n = lane & 15, g = lane >> 4;
    const char* db = p.ws + OFF_D + s * D_STRIDE + (size_t)(16 * cg) * 16384 + w * 4096;
    const char* ub = p.ws + uslot(sd, s) + (size_t)(16 * cg) * 4096 + w * 1024;
    const u32 dof = (u32)((n >> 3) * 16384 + g * 128 + (n & 7) * 16), uof = (u32)((n >> 3) * 4096 + g * 128 + (n & 7) * 16);
    u32x4 af[3][10];
    auto aload = [&](u32x4 (&r)[10], int ct) {
        if (!SMO) pace(slow);
        sfor<0, 8>([&](auto I) { gld16x<I.v * 512>(r[I.v], db + ct * 32768, dof); gap<SMO>(slow); });
        gld16x<0>(r[8], ub + uchunk(ct), uof); gap<SMO>(slow); gld16x<512>(r[9], ub + uchunk(ct), uof); gap<SMO>(slow);
    };
    aload(af[0], 0);
    aload(af[1], 1);
    aload(af[2], 2);
    const int tok = 32 * s + (tid >> 3), col = 128 * cg + 8 * (tid & 7);
    u32x4 bb[2]; u32 gv;
    gld16c<0>(bb[0], p.ws + OFF_BOUT, (u32)(s * 40960 + col * 64 + (tid >> 3) * 16)); gld16c<0>(bb[1], p.ws + OFF_BOUT + 4096, (u32)(s * 40960 + col * 64 + (tid >> 3) * 16));
    asm volatile("global_load_dword %0, %1, %2 sc1" : "=v"(gv) : "v"(tok * 4), "s"(p.ws + OFF_GATE) : "memory");
    barrier();
    const u32 wa = lds + (u32)w * 16896 + n * 528 + g * 16;
    sfor<0, 8>([&](auto I) {
        constexpr int ct = I.v;
        u32x4 (&r)[10] = af[ct % 3];
        if constexpr (ct == 0) WAIT10(23, r);
        else if constexpr (ct < 3) WAIT10(23, r);
        else if constexpr (ct < 6) WAIT10(20, r);
        else if constexpr (ct == 6) WAIT10(10, r);
        else WAIT10(0, r);
        f32x4 c0 = f32x4{0.f, 0.f, 0.f, 0.f}, c1 = c0;
#pragma unroll
        for (int i = 0; i < 10; ++i) { c0 = mfma16(r[i], pf[0][i], c0); c1 = mfma16(r[i], pf[1][i], c1); }
        *LDSP(f32x4, wa + ct * 64) = c0;
        *LDSP(f32x4, wa + ct * 64 + 16 * 528) = c1;
        PIN();
        if constexpr (ct + 3 < 8) aload(r, ct + 3);
        PIN();
    });
    asm volatile("" : "+v"(bb[0]), "+v"(bb[1]), "+v"(gv));
    barrier();
    const u32 ra = lds + (u32)(tid >> 3) * 528 + (tid & 7) * 32;
    const float gt = __uint_as_float(gv), g1 = rbf(1.f - gt);
    u32 ov[8];
#pragma unroll
    for (int i = 0; i < 4; ++i) {
        const u32 ri = ra + 16 * (i & 1) + 256 * (i >> 1);
        const f32x4 z = (*LDSP(f32x4, ri) + *LDSP(f32x4, ri + 16896)) + (*LDSP(f32x4, ri + 2 * 16896) + *LDSP(f32x4, ri + 3 * 16896));
#pragma unroll
        for (int j = 0; j < 2; ++j) {
            const u32 b = bb[i >> 1][2 * (i & 1) + j];
            const u32 a = cvtpk(z[2 * j], z[2 * j + 1]);
            const u32 ga = cvtpk(gt * __uint_as_float(a << 16), gt * __uint_as_float(a & 0xffff0000u));
            const u32 gb = cvtpk(g1 * __uint_as_float(b << 16), g1 * __uint_as_float(b & 0xffff0000u));
            ov[2 * i + j] = cvtpk(__uint_as_float(ga << 16) + __uint_as_float(gb << 16), __uint_as_float(ga & 0xffff0000u) + __uint_as_float(gb & 0xffff0000u));
        }
    }
    gst16p(p.out, (u32)(tok * 1280 + col * 2), u32x4{ov[0], ov[1], ov[2], ov[3]});
    gst16p(p.out, (u32)(tok * 1280 + col * 2 + 128), u32x4{ov[4], ov[5], ov[6], ov[7]});
    }
}

DI void out_tail(const Ptrs& p, const ChainCfg& cfg, int s, int cg, u32 lds, int w, int sd, int gr) {
    const int lane = lanex(), tid = 64 * w + lane, n = lane & 15, g = lane >> 4;
    const char* db = p.ws + OFF_D + s * D_STRIDE + (size_t)(4 * cg) * 16384 + w * 4096;
    const char* ub = p.ws + uslot(sd, s) + uchunk(2 * cg) + w * 1024;
    const char* pb = p.ws + OFF_PX + (size_t)s * 81920;
    const u32 dof = (u32)((n >> 3) * 16384 + g * 128 + (n & 7) * 16), uof = (u32)((n >> 3) * 4096 + g * 128 + (n & 7) * 16);
    u32x4 pf[2][10], af[2][10];
    sfor<0, 16>([&](auto I) { gld16c<(I.v & 7) * 512>(af[I.v >> 3][I.v & 7], db + (I.v >> 3) * 32768, dof); });
    const int tok = 32 * s + (tid >> 3), col = 32 * cg + 4 * (tid & 7);
    u32x2 bb; u32 gv;
    asm volatile("global_load_dwordx2 %0, %2, %3 sc1\n\tglobal_load_dword %1, %4, %5 sc1\n\ts_waitcnt vmcnt(0)" : "=&v"(bb), "=&v"(gv)
                 : "v"(s * 40960 + (col >> 3) * 512 + (tid >> 3) * 16 + (col & 7) * 2), "s"(p.ws + OFF_BOUT), "v"(tok * 4), "s"(p.ws + OFF_GATE) : "memory");
    sfor<0, 16>([&](auto I) { waitv<0>(af[I.v >> 3][I.v & 7]); });
    until(step_wait(p.ws + OFF_ST, w, gr, s - 1, lds) + (u32)(s == NSEG - 1 ? cfg.cR : cfg.cR + 2 * PDX));
    const u32 wa = lds + (u32)w * 4608 + n * 144 + g * 16;
#pragma nounroll
    for (int it = 0; it < (1 << 14); ++it) {
        if (it == 4) {
            flag_wait(p.ws + OFF_ST, F_PX + s, 8);
            flag_wait(p.ws + OFF_ST, s < NSEG - 2 ? F_CUR + gr : F_UD + 2 * (s & 1), s < NSEG - 2 ? s + 1 : 16);
            flag_wait(p.ws + OFF_ST, s < NSEG - 2 ? F_ONE : F_UD + 2 * (s & 1) + 1, 16);
        }
        sfor<0, 4>([&](auto I) { gld16c<(I.v & 1) * 512>(af[I.v >> 1][8 + (I.v & 1)], ub + (I.v >> 1) * 16384, uof); });
        sfor<0, 8>([&](auto I) { gld16c<(I.v & 1) * 2048>(pf[0][I.v], pb + w * 16384 + (I.v >> 1) * 4096, lane * 16); });
        gld16c<0>(pf[0][8], pb + 65536 + w * 4096, lane * 16); gld16c<2048>(pf[0][9], pb + 65536 + w * 4096, lane * 16);
        sfor<0, 8>([&](auto I) { gld16c<(I.v & 1) * 2048 + 1024>(pf[1][I.v], pb + w * 16384 + (I.v >> 1) * 4096, lane * 16); });
        gld16c<1024>(pf[1][8], pb + 65536 + w * 4096, lane * 16); gld16c<3072>(pf[1][9], pb + 65536 + w * 4096, lane * 16);
        WAIT10(10, pf[0]); WAIT10(10, af[0]); WAIT10(10, af[1]);
        float chk = 0.f;
#pragma unroll
        for (int tt = 0; tt < 2; ++tt) {
            if (tt) WAIT10(0, pf[1]);
            f32x4 c0 = f32x4{0.f, 0.f, 0.f, 0.f}, c1 = c0;
#pragma unroll
            for (int i = 0; i < 10; ++i) { c0 = mfma16(af[0][i], pf[tt][i], c0); c1 = mfma16(af[1][i], pf[tt][i], c1); }
            *LDSP(f32x4, wa + tt * 2304) = c0;
            *LDSP(f32x4, wa + tt * 2304 + 64) = c1;
            const f32x4 c = c0 + c1;
            chk += (c[0] + c[1]) + (c[2] + c[3]);
            PIN();
        }
        if (it >= 4 || __builtin_amdgcn_ballot_w64((__float_as_uint(chk) & 0x7fffffffu) > 0x7f800000u) == 0) break;
        if (s < NSEG - 1) nap(60);
    }
    barrier();
    const u32 ra = lds + (u32)(tid >> 3) * 144 + (tid & 7) * 16;
    const float gt = __uint_as_float(gv), g1 = rbf(1.f - gt);
    const f32x4 z = (*LDSP(f32x4, ra) + *LDSP(f32x4, ra + 4608)) + (*LDSP(f32x4, ra + 2 * 4608) + *LDSP(f32x4, ra + 3 * 4608));
    u32x2 ov;
#pragma unroll
    for (int j = 0; j < 2; ++j) {
        const u32 b = bb[j], a = cvtpk(z[2 * j], z[2 * j + 1]);
        const u32 ga = cvtpk(gt * __uint_as_float(a << 16), gt * __uint_as_float(a & 0xffff0000u));
        const u32 gb = cvtpk(g1 * __uint_as_float(b << 16), g1 * __uint_as_float(b & 0xffff0000u));
        ov[j] = cvtpk(__uint_as_float(ga << 16) + __uint_as_float(gb << 16), __uint_as_float(ga & 0xffff0000u) + __uint_as_float(gb & 0xffff0000u));
    }
    *(u32x2*)(p.out + tok * 1280 + col * 2) = ov;
}

DI float dot2bf(u32 a, u32 b, float c) { return __builtin_amdgcn_fdot2_f32_bf16(__builtin_bit_cast(bf16x2, a), __builtin_bit_cast(bf16x2, b), c, false); }
DI void battn_task(const Ptrs& p, int t0, u32 lds, int w) {
    const int lane = lanex(), tid = 64 * w + lane, tl = tid & 31, t = t0 + tl;
    const char* const qkv = p.ws + OFF_QKVB + (size_t)(t0 >> 5) * 122880;
    {
        u32 so[3];
#pragma unroll
        for (int i = 0; i < 3; ++i) {
            const int e = 64 * i + lane, sl = e % 48;
            so[i] = (u32)((e / 48) * 512 + (sl < 16 ? (t0 ? 0 : 122880) + 256 + sl * 16 : 122880 - 256 + sl * 16));
        }
#pragma unroll
        for (int i = 0; i < 30; ++i) { glds16c(qkv - 122880 + 40960 + (10 * w + i / 3) * 2048, so[i % 3], lds + (30 * w + i) * 1024); gap<SMB>(-1); }
    }
    u32x4 q[2][5], xv[10], gv[10];
    sfor<0, 10>([&](auto I) { gld16x<(I.v % 5) * 512>(q[I.v / 5][I.v % 5], qkv + (I.v / 5) * 20480, (u32)((tid >> 5) * 2560 + tl * 16)); gap<SMB>(-1); });
    sfor<0, 10>([&](auto I) { gld16<I.v * 128>(xv[I.v], p.x + (size_t)t0 * 1280, (u32)((tid >> 3) * 1280 + (tid & 7) * 16)); gld16<I.v * 128>(gv[I.v], p.gw, (u32)((tid & 7) * 16)); gap<SMB>(-1); });
    sfor<0, 10>([&](auto I) { waitv<0>(q[I.v / 5][I.v % 5]); waitv<0>(xv[I.v]); waitv<0>(gv[I.v]); });
    barrier();
    {
        float a = 0.f;
#pragma unroll
        for (int i = 0; i < 10; ++i)
#pragma unroll
            for (int j = 0; j < 4; ++j) {
                const u32 r = cvtpk(__uint_as_float(xv[i][j] << 16) * __uint_as_float(gv[i][j] << 16), __uint_as_float(xv[i][j] & 0xffff0000u) * __uint_as_float(gv[i][j] & 0xffff0000u));
                a += __uint_as_float(r << 16) + __uint_as_float(r & 0xffff0000u);
            }
        a += __shfl_xor(a, 1); a += __shfl_xor(a, 2); a += __shfl_xor(a, 4);
        const float z = rbf(a);
        if ((tid & 7) == 0) gst4c(p.ws + OFF_GATE, (u32)((t0 + (tid >> 3)) * 4), __float_as_uint(rbf(1.0f / (1.0f + __expf(-z)))));
    }
#pragma unroll
    for (int it = 0; it < 2; ++it) {
        const int m = (tid >> 5) + 8 * it;
        u32 ka = lds + (u32)(m * 3840 + (2 + tl) * 16), va = ka + 61440;
        asm volatile("" : "+v"(ka), "+v"(va));
        u32x4 k[5][8];
#pragma unroll
        for (int i = 0; i < 40; ++i) k[i >> 3][i & 7] = ldsr16(ka + (i >> 3) * 768 + (7 - (i & 7)) * 32);
        PIN();
        float lg[2][8], mx[2] = {-3.0e38f, -3.0e38f};
#pragma unroll
        for (int i = 0; i < 16; ++i) lg[i >> 3][i & 7] = 0.f;
#pragma unroll
        for (int i = 0; i < 5; ++i) {
#pragma unroll
            for (int j = 0; j < 4; ++j)
#pragma unroll
                for (int wv = 0; wv < 8; ++wv) lg[4 * i + j >= 10][wv] = dot2bf(q[it][i][j], k[i][wv][j], lg[4 * i + j >= 10][wv]);
            PIN();
#pragma unroll
            for (int wv = 0; wv < 8; ++wv) k[i][wv] = ldsr16(va + i * 768 + (7 - wv) * 32);
            PIN();
        }
#pragma unroll
        for (int i = 0; i < 16; ++i) {
            float& l = lg[i >> 3][i & 7];
            l = rbf(rbf(l) * 0.22360679774997896f);
            asm volatile("" : "+v"(l));
            l = t - 2 * (i & 7) >= 0 ? l : -3.0e38f;
            mx[i >> 3] = fmaxf(mx[i >> 3], l);
        }
        u32 pk[2][4];
#pragma unroll
        for (int u = 0; u < 2; ++u) {
            float sum = 0.f;
#pragma unroll
            for (int wv = 0; wv < 8; ++wv) { lg[u][wv] = __builtin_amdgcn_exp2f((lg[u][wv] - mx[u]) * 1.4426950408889634f); sum += lg[u][wv]; }
            const float inv = __builtin_amdgcn_rcpf(sum);
#pragma unroll
            for (int wv = 0; wv < 4; ++wv) pk[u][wv] = cvtpk(lg[u][2 * wv] * inv, lg[u][2 * wv + 1] * inv);
        }
#pragma unroll
        for (int i = 0; i < 5; ++i) {
            float o[8];
#pragma unroll
            for (int d = 0; d < 8; ++d) o[d] = 0.f;
#pragma unroll
            for (int wp = 0; wp < 4; ++wp) {
                const u32x4 v0 = k[i][2 * wp], v1 = k[i][2 * wp + 1];
#pragma unroll
                for (int j = 0; j < 4; ++j) {
                    const u32 pw = pk[4 * i + j >= 10][wp];
                    o[2 * j] = dot2bf(__builtin_amdgcn_perm(v1[j], v0[j], 0x05040100u), pw, o[2 * j]);
                    o[2 * j + 1] = dot2bf(__builtin_amdgcn_perm(v1[j], v0[j], 0x07060302u), pw, o[2 * j + 1]);
                }
            }
            gst16c(p.ws + OFF_OB + (size_t)(t0 >> 5) * 40960 + 512 * i, (u32)(m * 2560 + tl * 16), u32x4{cvtpk(o[0], o[1]), cvtpk(o[2], o[3]), cvtpk(o[4], o[5]), cvtpk(o[6], o[7])});
        }
    }
}

#define XPF 1
#define PFQ 0
#define PFN 8
#define OLAG 3
constexpr int QN = 176, QF = 19;
struct QTab { u16 e[QN]; int n, nf; u32 tt[64]; };
typedef const __attribute__((address_space(4))) int* KI;
constexpr int QT_OFF = (88 + 48) / 4;
DI int pick_task(char* fl, KI k, int gr) {
    u32 r;
    asm volatile("s_mov_b64 exec, 1\n\ts_nop 3\n\tglobal_atomic_add %0, %1, %2, %3 sc0 sc1\n\ts_mov_b64 exec, -1\n\ts_waitcnt vmcnt(0)"
                 : "=&v"(r) : "v"((F_QH + gr) * 128), "v"(1), "s"(fl) : "memory");
    const int x = uni((int)r) + 1 + QF;
    if (x >= k[QT_OFF + QN / 2]) return -1;
    const u32 e2 = (u32)k[QT_OFF + (x >> 1)];
    return (int)(x & 1 ? e2 >> 16 : e2 & 0xffffu);
}
__global__ __launch_bounds__(256, 1) void k_main(Ptrs p0, ChainCfg cfg, QTab tab) {
    extern __shared__ __attribute__((aligned(16))) char smem[];
    u32 lds = (u32)(size_t)LDSP(char, smem);
#define HALF 0
    int n = HALF ? (blockIdx.x & 4) * 32 + (int)(blockIdx.x >> 3) * 4 + (blockIdx.x & 3) : blockIdx.x, gr = n & 7;
    int w = uni(threadIdx.x >> 6);
    {
        u64 pc; int len;
        asm volatile("s_getpc_b64 %0\n.Lkm_first:\n\ts_mov_b32 %1, .Lkm_last-.Lkm_first\n\ts_max_i32 %1, %1, .Lkm_lu-.Lkm_first\n\ts_max_i32 %1, %1, .Lkm_lv-.Lkm_first\n\t"
                     "s_max_i32 %1, %1, .Lkm_g2-.Lkm_first\n\ts_max_i32 %1, %1, .Lkm_g4-.Lkm_first" : "=s"(pc), "=s"(len) : : "scc");
        if (w == 3) pfl((const char*)pc, (u32)min((int)(blockIdx.x >> 3) * 8192 + (int)(threadIdx.x & 63) * 128, max(len, 0)), lds);
    }
    karg_warm();
    if (n < NH) { score_role(p0, cfg, n, lds, w); return; }
    if (n < NS) { proj_role(p0, cfg, n - NH, lds, w); return; }
    const bool isv = n < NS + NVB;
#define qslot (lds + GRING * GBUF)
    int sdw = -1;
    int en = isv ? Q_VD + (n & 24) : tab.e[(n - NS - NVB) >> 3] & 511;
    u32 phi0 = 0;
    Sync y;
    if (XPF && n < 160 && w >= 2) pfl(p0.x, (u32)min(((n - NS) >> 3) * 128 + (w & 1) * 64 + lanex(), 1279) * 128, lds);
    if (n >= 160) {
        const int lane = lanex();
        if (n >= 192 && (n & 8)) {
            const int j = (n & 7) + ((n - 192) >> 4) * 8;
            init_task(p0, j, lds, w);
            barrier();
#pragma unroll
            for (int i = 0; i < 3; ++i) pfl(p0.wqkvb, (u32)min(j * 768 + i * 256 + w * 64 + lane, 19199) * 128, lds);
            pfl(j < 25 ? p0.wob : p0.gw, j < 25 ? (u32)(j * 256 + w * 64 + lane) * 128 : (u32)min(lane, 9) * 128, lds);
        } else {
            const int h = n < 192 ? n & 31 : (n & 7) + ((n - 192) >> 4) * 8;
            xcopy_task(p0, h + (n < 192 ? 0 : 32), w);
            if (n < 192) {
#pragma unroll
                for (int i = 0; i < PFN; ++i) {
                    const int l = 64 * min(i * 4 + w, 29) + lane;
                    pfl(p0.wo + (size_t)h * 245760, (u32)(l * 128), lds);
                    if (PFQ) pfl(p0.wq + h * 384, (u32)((l / 3) * 12288 + (l % 3) * 128), lds);
                }
            }
        }
        if (w == 1) {
            sync_ld(y, p0.ws);
            waitvm<0>();
            asm volatile("" : "+v"(y.r), "+v"(y.g), "+v"(y.e));
            if (n < 192) xcopy_wait(p0.ws, sync_tag(y.e));
            else xcopy_wait(p0.ws, sync_tag(y.e), n & 8 ? 4 : 8, n & 8 ? 3 : 7);
        }
        if (w == 0 && n < 192) {
            sync_ld(y, p0.ws);
            waitvm<0>();
            *LDSP(u32, qslot) = (u32)sync_end(y, p0.ws, phi0, false);
        }
        barrier();
        if (w == 0 && n < 192) flag_add(p0.ws + OFF_ST, F_ACK, 1);
        if (n < 192) sdw = uni((int)*LDSP(u32, qslot));
        barrier();
    }
    while (true) {
        typedef char* const __attribute__((address_space(4)))* KP;
        KP kp = (KP)__builtin_amdgcn_kernarg_segment_ptr();
        asm volatile("" : "+s"(kp), "+s"(lds), "+s"(w), "+s"(gr), "+s"(n));
        __builtin_assume(w >= 0 && w < 4 && gr >= 0 && gr < 8);
        const Ptrs p{kp[0], kp[1], kp[2], kp[3], kp[4], kp[5], kp[6], kp[7], kp[8], kp[9], kp[10]};
        const int ty = en & 7, gate = en >> 9;
        const bool fu = ty == Q_PX && (en >> 3 & 63) < 8;
        const int ix = fu ? (en & 504) + gr : en >> 3 & 63, sf = ((KI)kp)[QT_OFF + QN / 2 + 1] & 255;
        char* const fl = p.ws + OFF_ST;
        const bool raw = sdw < 0;
        int t0 = gate - 1, f1 = F_ONE, t1 = 0, f2 = F_ONE, t2 = 0, f3 = F_ONE, t3 = 0, dt = cfg.T2;
        const bool o2 = ty == Q_OUT && ix < 16;
        const int os = o2 ? sf + 8 * (ix >> 2) + gr : (ix >= 40 ? 44 + (8 * ix + gr) / 20 : (8 * ix + gr) / 5);
        const int bb = ty == Q_BO ? (8 * ix + gr) >> 2 : os;
        if (ty == Q_BA) { t0 += gr * BAS >> 3; f1 = F_B + (gr & RM) | ix << 16; t1 = 5; f2 = F_B + (gr & RM) | max(ix - 1, 0) << 16; t2 = 5; }
        if (ty == Q_BO) { f1 = F_B + (gr & RM) | (8 + bb) << 16; t1 = 8; }
        if (ty == Q_PX) { f1 = F_Q + pblk(ix) * 8 + (fu ? gr & RM : gr) | fu << 16; t1 = fu ? 32 : 4; if (fu) t0 += gr * (((KI)kp)[QT_OFF + QN / 2 + 1] >> 8) >> 3; if (ix < PXT) t0 = max(t0, ix + 1); }
        if (ty == Q_OUT) { f3 = F_B + (gr & RM) | (16 + (os >> 3)) << 16; t3 = 4; if (os < 60) { f1 = F_PX + os; t1 = 8; t0 = max(t0, os + (os < PXT ? OLAG : 1)); if (os >= PXT) dt = TDT; } else { f1 = F_VD + 64 + (gr & RM); t1 = 32; } }
        const bool skip = fu ? ix >= sf : ty == Q_OUT && ix < 40 && !o2 && (os < sf + 8 * NO2 || os >= 60);
        if ((gate || ty >= Q_BA) && !skip) wg_wait(fl, w, F_CUR + gr, t0, f1, t1, f2, t2, f3, t3, dt, false);
        int sig = F_END, nrep = 1;
        if (skip) {}
        else if (ty <= Q_QKVB || ty == Q_BO) {
            const bool ab = ty <= Q_KQ;
            sig = ty == Q_VD ? F_VD + (ix >> 2) * 8 : (ty == Q_KQ ? F_Q + (ix >> 2) * 8 : (ix & 4 ? F_Q : F_K + 8 * ix)) + gr;
            if (!ab) sig = F_B | (bb + (ty == Q_BO ? 16 : 0)) << 16;
            nrep = ty == Q_VD || !ab ? RM + 1 : 1;
            if (ty == Q_VD && ix < 4) { sig += n & 32 ? 4 : 0; nrep = 4; }
            run_gemm(p, ty, ab ? ix >> 2 : bb, ab ? gr + 8 * (ix & 3) : 8 * ix + gr - (ty == Q_BO ? 4 : 5) * bb, (n >> 5) & 1, raw && n < 160, y, lds, w);
            if (raw) sdw = sync_end(y, p.ws, phi0, w == 0);
        } else {
            if (ty == Q_BA) {
                battn_task(p, (8 * ix + gr) * 32, lds, w);
                sig = F_B | (8 + ix) << 16; nrep = RM + 1;
            } else if (ty == Q_PX) {
                if (ix < PXT) {
#pragma nounroll
                    for (int i = fu ? 0 : gr; i < (fu ? 8 : gr + 1); ++i) px_wave<false>(p, cfg, ix, i + 8 * w, ix - cfg.fast, w, lds, fu);
                } else px_wave<true>(p, cfg, ix, gr + 8 * w, -1, w, lds, false);
                sig = F_PX + ix;
                if (fu) wg_wait(fl, w, F_ONE, 0, F_B + (gr & RM) | (16 + (ix >> 3)) << 16, 4, F_ONE, 0, F_ONE, 0, dt, false);
            }
            if (ty == Q_OUT || fu) {
                const int cg = 8 * ix + gr - 5 * os;
                if (ix >= 40 && !fu) out_tail(p, cfg, os, 8 * ix + gr - 20 * (os - 44), lds, w, sdw, gr);
                else out_task(p, fu ? ix : os, fu ? 0 : (o2 ? OW * (ix & 3) : cg), fu ? 5 : (o2 ? min(OW * (ix & 3) + OW, 5) : cg + 1), fu, lds, (fu ? ix : os) + 1 - cfg.fast, w, sdw);
                sig = F_END;
            }
            waitvm<0>();
        }
        barrier();
        if (w == 0 && sig != F_END) flag_add(fl, sig, nrep);
        if (w == 1 && sig >= F_Q && sig < F_N) flag_add(fl, sig - gr | 1 << 16, RM + 1);
        if (raw && !isv && w == 0) xcopy_wait(p.ws, sync_tag(y.e));
        if (isv) break;
        if (w == 0) *LDSP(int, qslot) = pick_task(fl, (KI)kp, gr);
        barrier();
        en = uni(*LDSP(int, qslot));
        if (en < 0) return;
        barrier();
    }
    value_role(p0, cfg, n - NS, sdw, phi0, lds, w);
}

#define QCFG 14, 11, 3, 5, 0, 50, 36
#define LVDEF "00?>>=<<;;;<<<<;;::98888999:::::999999:::99888998888877889::;;:6"
constexpr int DX = 380, DUA = 470, DUB = 0, DUC = 4;
static ChainCfg dev_cfg(ChainCfg c) { c.T = PDL; c.cR = ODL; c.s1 = 190 << 8 | 185 << 16; c.T2 = WSLEEP; c.cR2 = DX; c.dU = DUA; c.s2 = DUB; c.T3 = DUC; c.dV |= 4 << 8 | 8 << 16 | 0 << 24; c.cR3 = 4 | 4 << 8 | 0 << 16; return c; }
static QTab set_sched(QTab t, const ChainCfg c, const char* lv) {
    for (int s = 0; s < 64; ++s) {
        const int d = lv[s] - '0';
        t.tt[s] = (u32)((s < c.s1 ? c.T : (s < c.s2 ? c.T2 : c.T3)) + 4 * d) | (u32)((s < c.s1 ? c.cR : (s < c.s2 ? c.cR2 : c.cR3)) + 5 * d / 2) << 16;
    }
    t.tt[0] = (u32)230 | (u32)165 << 16;
    t.tt[1] = (u32)210 | (u32)144 << 16;
    return t;
}
#define QORD "bbV3qbK3s41x40bx51bx60x71s80"
#define GATES "11 13 15 18 25 28 41 43 21 26 36 49 14 18 22 27 33 41"
static QTab make_tab(int lv, int lk, int b0, int d1, int d2, int lg, int nf) {
    QTab t{};
    int n = 0, dl[QN], nq = 0;
    auto add = [&](int ty, int ix) { t.e[n] = (u16)(ty | ix << 3); dl[n++] = ty == Q_VD || ty == Q_KQ ? 8 * (ix / 4 - 1) - (ty == Q_VD ? lv : lk) - (ix >= 32 ? 0 : 0) : 99; };
    for (int i = 1; i < 4; ++i) add(Q_VD, i);
    for (int i = 0; i < 16; ++i) add(i < 4 ? Q_H : ((i & 8) ? Q_VD : Q_KQ), i < 8 ? i : 8 - (i & 1) * 4 + ((i - 8) >> 1));
    for (int i = 0; i < 4; ++i) add(Q_KQ, 8 + i);
    for (const char* c = QORD; *c; ++c) {
        if (*c == 'b') add(Q_QKVB, nq++);
        else if (*c == 'q') for (int i = 4; i < 8; ++i) add(Q_H, i);
        else if (*c == '.') dl[n - 1] |= 1 << 20;
        else if (*c == 'w') { const int b = *++c - '0'; for (int i = 0; i < 16; ++i) add(i & 2 ? Q_KQ : Q_VD, 4 * (b + (i & 1)) + (i >> 2)); }
        else if (*c == 'x') { const int b = c[1] - '0', o = 2 * (c[2] - '0'); c += 2; for (int i = 0; i < 8; ++i) add(i & 2 ? Q_KQ : Q_VD, 4 * (b + (i & 1)) + o + (i >> 2)); }
        else if (*c == 's') { const int b = c[1] - '0', o = 2 * (c[2] - '0'); c += 2; for (int i = 0; i < 4; ++i) add(i & 1 ? Q_KQ : Q_VD, 4 * b + o + (i >> 1)); }
        else if (*c == 'a' || *c == 'c') { add(*c == 'a' ? Q_VD : Q_KQ, 10 * (c[1] - '0') + c[2] - '0'); c += 2; }
        else if (*c == 'n') { const int b = *++c - '0'; for (int i = 0; i < 8; ++i) add(i & 1 ? Q_KQ : Q_VD, 4 * b + (i >> 1) - (i & 1) * 2); }
        else if (*c == 'h' || *c == 'e') { const int b = 4 * (c[1] - '0') + (*c == 'e') * 2; ++c; add(Q_KQ, b); add(Q_KQ, b + 1); }
        else if (*c == 'm') { const int b = c[1] - '0', b2 = c[2] - '0'; c += 2; for (int i = 0; i < 8; ++i) add(i & 1 ? Q_KQ : Q_VD, 4 * (i & 1 ? b2 : b) + ((i >> 1) ^ (i & 2 ? 0 : 0))); }
        else {
            const int ty = (*c | 32) == 'v' ? Q_VD : Q_KQ, two = *c >> 5 & 1, b = *++c - '0';
            for (int i = 0; i < (4 << two); ++i) add(ty, 4 * (b + (i & two)) + (i >> two));
        }
    }
    const int gap = 8;
    for (int i = n - 1, m = 999; i >= 0; --i) { const int pz = dl[i] > 999; m = min(8 * (pz ? dl[i] - (1 << 20) : dl[i]), m - gap - 8 * pz); dl[i] = max(m, 0); }
    auto put = [&](int ty, int ix, int g, int pos) {
        int i = n++;
        for (; dl[i - 1] > pos; --i) { t.e[i] = t.e[i - 1]; dl[i] = dl[i - 1]; }
        t.e[i] = (u16)(ty | ix << 3 | (g + 1) << 9); dl[i] = pos;
    };
    for (int i = 0; i < n; ++i) if (dl[i] >= 8) t.e[i] |= (u16)((dl[i] / 8 + 1) << 9);
    const char* gs = GATES;
    auto gt = [&](int i) { return 10 * (gs[3 * i] - '0') + gs[3 * i + 1] - '0'; };
    for (int i = 0; i < 8; ++i) put(Q_BA, i, gt(i), 8 * gt(i));
    for (int i = 0; i < 4; ++i) put(Q_BO, i, gt(8 + i), 8 * gt(8 + i));
    for (int i = 0; 8 * i < nf; ++i) put(Q_PX, i, gt(12 + i), 8 * (max(gt(12 + i), 8 * i + 1) + b0));
    auto pg = [&](int s) { return max(8 * s + 8, 8 * 46 + (s - nf) * 3); };
    for (int s = nf; s < NSEG; ++s) { const int g = s < PXT ? pg(s) : 8 * max(s - 3, lg); put(Q_PX, s, g / 8, g); }
    for (int k = 0; k < NO2; ++k) for (int c = 0; c * OW < 5; ++c) put(Q_OUT, 4 * k + c, pg(nf + 8 * k + 7) / 8 + OLAG - 1, pg(nf + 8 * k + 7) + 8 * (OLAG - 1));
    for (int x = 5 * (nf + 8 * NO2) / 8; x < 38; ++x) { const int os = (8 * x + 4) / 5, g = pg(min((8 * x + 7) / 5, PXT - 1)); put(Q_OUT, x, g > 8 * ((8 * x + 7) / 5) + 8 ? g / 8 + OLAG - 1 : 0, os < PXT ? pg(os) + 8 * (OLAG - 1) : 8 * (os + 1)); }
    for (int x = 40; x < 50; ++x) { const int g = max(44 + 8 * x / 20 - 3, lg); put(Q_OUT, x, g, 8 * g + 4); }
    t.n = n; t.nf = nf | d1 << 8;
    return t;
}

extern "C" void kernel_launch(void* const* d_in, const int* in_sizes, int n_in, void* d_out, int out_size, void* d_ws,
                              size_t ws_size, hipStream_t stream) {
    Ptrs p{(const char*)d_in[0], (const char*)d_in[1], (const char*)d_in[2], (const char*)d_in[3], (const char*)d_in[4], (const char*)d_in[5],
           (const char*)d_in[8], (const char*)d_in[9], (const char*)d_in[10], (char*)d_out, (char*)(((size_t)d_ws + 0x1fffff) & ~(size_t)0x1fffff)};
    (void)hipFuncSetAttribute((const void*)k_main, hipFuncAttributeMaxDynamicSharedMemorySize, S_LDS);
    k_main<<<256, 256, S_LDS, stream>>>(p, dev_cfg(ChainCfg{CFG}), set_sched(make_tab(QCFG), ChainCfg{CFG}, LVDEF));
    static_assert(sizeof(Ptrs) + sizeof(ChainCfg) + sizeof(QTab) == TT_OFF + 256, "");
}
```

```cpp
#include <hip/hip_runtime.h>
#pragma clang diagnostic ignored "-Wint-to-pointer-cast"

typedef unsigned short u16;
typedef unsigned int u32;
typedef unsigned long long u64;
typedef __bf16 bf16x8 __attribute__((ext_vector_type(8)));
typedef __bf16 bf16x16 __attribute__((ext_vector_type(16)));
typedef short s16x4 __attribute__((ext_vector_type(4)));
typedef float f32x4 __attribute__((ext_vector_type(4)));
typedef unsigned u32x4 __attribute__((ext_vector_type(4)));
typedef unsigned u32x2 __attribute__((ext_vector_type(2)));
typedef unsigned u32x8 __attribute__((ext_vector_type(8)));
typedef unsigned u32x16 __attribute__((ext_vector_type(16)));
typedef unsigned u32x32 __attribute__((ext_vector_type(32)));
#define DI __device__ __forceinline__
#define LDSP(T, a) ((__attribute__((address_space(3))) T*)(a))

constexpr int S_LEN = 2048, DM = 640, NH = 32, DH = 192, NSEG = 64, HD = NH * DH;
#define NSAMP 4
constexpr int NREG = 13;
constexpr int NV = 80, NVB = 40;
constexpr int NS = 64;
#define CFG 149, 107, 108, 20, 62, 16, 149, 107, 52, 149, 107, 6

constexpr size_t U_STRIDE = NV * 4096, D_STRIDE = NV * 16384;
constexpr size_t HO_STRIDE = 65536;
constexpr size_t OFF_ST = NSEG * HO_STRIDE;
enum { F_ACK = 0, F_RDY = 1, F_ONE = 3, F_GO = 8, F_VD = 16, F_QH = 96, F_B = 104, F_CUR = 128, F_PROG = 136 - 8 * 62, F_UD = 152, F_PX = 160, F_K = 224, F_END = 512, F_Q = 520, F_N = 592 };
constexpr size_t OFF_LAT = OFF_ST + F_N * 128;
constexpr size_t OFF_KX = 5u << 20;
constexpr size_t OFF_QX = OFF_KX + (size_t)S_LEN * HD * 2;
constexpr size_t OFF_D = OFF_QX + (size_t)S_LEN * HD * 2;
constexpr size_t OFF_U = (OFF_D + NSEG * D_STRIDE + 0x1fffff) & ~(size_t)0x1fffff;
constexpr size_t OFF_KM = OFF_U + (22u << 20);
constexpr size_t OFF_PX = OFF_KM + (size_t)NSEG * NH * 3072;
constexpr size_t OFF_QKVB = OFF_PX + (size_t)NSEG * 81920;
constexpr size_t OFF_OB = OFF_QKVB + (size_t)S_LEN * 1920 * 2;
constexpr size_t OFF_BOUT = OFF_OB + (size_t)S_LEN * DM * 2;
constexpr size_t OFF_GATE = OFF_BOUT + (size_t)S_LEN * DM * 2;
constexpr size_t OFF_XF = OFF_GATE + S_LEN * 4;
constexpr size_t WS_END = OFF_XF + (size_t)S_LEN * DM * 2;
static_assert(WS_END + (2u << 20) <= (256u << 20), "workspace");

typedef __bf16 bf16x2 __attribute__((ext_vector_type(2)));
typedef float f32x2 __attribute__((ext_vector_type(2)));
DI u32 cvtpk(float lo, float hi) { return __builtin_bit_cast(u32, __builtin_convertvector(f32x2{lo, hi}, bf16x2)); }
DI float rbf(float f) { return __uint_as_float(cvtpk(f, f) << 16); }
DI f32x4 mfma16(u32x4 a, u32x4 b, f32x4 c) {
    return __builtin_amdgcn_mfma_f32_16x16x32_bf16(__builtin_bit_cast(bf16x8, a), __builtin_bit_cast(bf16x8, b), c, 0, 0, 0);
}
DI u32x2 trread(u32 a) {
    return __builtin_bit_cast(u32x2, __builtin_amdgcn_ds_read_tr16_b64_v4i16(LDSP(s16x4, a)));
}
DI void glds16c(const char* sbase, u32 voff, u32 lds) {
    u32 sv;
    asm volatile("s_mov_b32 %0, m0\n\ts_mov_b32 m0, %3\n\ts_nop 0\n\tglobal_load_lds_dwordx4 %1, %2 sc1\n\ts_mov_b32 m0, %0"
                 : "=&s"(sv) : "v"(voff), "s"(sbase), "s"(lds) : "memory");
}
template <int IMM>
DI void gld16(u32x4& r, const char* sbase, u32 voff) {
    asm volatile("s_nop 4\n\tglobal_load_dwordx4 %0, %1, %2 offset:%3" : "=v"(r) : "v"(voff), "s"(sbase), "i"(IMM) : "memory");
}
DI void gst16(char* sbase, u32 voff, u32x4 v) {
    asm volatile("global_store_dwordx4 %0, %1, %2 sc1\n\ts_nop 1" : : "v"(voff), "v"(v), "s"(sbase) : "memory");
}
#define OST " nt"
DI void gst16p(char* sbase, u32 voff, u32x4 v) {
    asm volatile("s_nop 4\n\tglobal_store_dwordx4 %0, %1, %2" OST "\n\ts_nop 1" : : "v"(voff), "v"(v), "s"(sbase) : "memory");
}
DI void gst8(char* sbase, u32 voff, u32x2 v) {
    asm volatile("global_store_dwordx2 %0, %1, %2 sc1\n\ts_nop 1" : : "v"(voff), "v"(v), "s"(sbase) : "memory");
}
template <int N>
DI void waitvm() { asm volatile("s_waitcnt vmcnt(%0)" : : "i"(N) : "memory"); }
DI void barrier() { asm volatile("s_waitcnt lgkmcnt(0)\n\ts_barrier" : : : "memory"); }
#define PIN() __builtin_amdgcn_sched_barrier(0)
DI int uni(int v) { return __builtin_amdgcn_readfirstlane(v); }
DI int lanex() {
    int t;
    asm volatile("v_mbcnt_lo_u32_b32 %0, -1, 0\n\tv_mbcnt_hi_u32_b32 %0, -1, %0" : "=v"(t));
    __builtin_assume(t >= 0 && t < 64);
    return t;
}
DI void flag_ld(u32& r, const char* fl, int idx) {
    asm volatile("s_nop 4\n\tglobal_load_dword %0, %1, %2 sc1" : "=v"(r) : "v"((idx & 0xffff) * 128 + (idx >> 14)), "s"(fl) : "memory");
}
DI void flag_ldm(u32& r, const char* fl, int idx, bool on) {
    asm volatile("v_mov_b32 %0, 1000\n\ts_mov_b32 exec_lo, %3\n\ts_mov_b32 exec_hi, 0\n\tglobal_load_dword %0, %1, %2 sc1\n\ts_mov_b64 exec, -1" : "=&v"(r) : "v"(idx * 128), "s"(fl), "s"(uni(on ? 1 : 0)) : "memory");
}
DI void flag_wait(const char* fl, int idx, int target) {
#pragma nounroll
    for (int it = 0; it < (1 << 16); ++it) {
        u32 v;
        flag_ld(v, fl, idx);
        asm volatile("s_waitcnt vmcnt(0)" : "+v"(v) : : "memory");
        if (uni((int)(v + 1u)) >= target) break;
        __builtin_amdgcn_s_sleep(6);
    }
}
DI void flag_add(char* fl, int idx, int nrep) {
    const int lane = lanex();
    if (lane < nrep) asm volatile("s_nop 4\n\tglobal_atomic_add %0, %1, %2 sc1" : : "v"(((idx & 0xffff) + lane) * 128 + (idx >> 14)), "v"(1), "s"(fl) : "memory");
}
DI void acquire() { asm volatile("buffer_inv sc1\n\ts_waitcnt vmcnt(0)" : : : "memory"); }

template <int V> struct IC { static constexpr int v = V; };
template <int B, int E, class Fn>
DI void sfor(Fn&& fn) { if constexpr (B < E) { fn(IC<B>{}); sfor<B + 1, E>(fn); } }
enum { T_QK = 0, T_VD = 1, T_BL = 3 };
constexpr int GBUF = 24576, GRING = 6;
struct GemmArgs {
    const char *w1, *w1b;
    const char* w2;
    const char* x;
    char *out, *outb;
    int ldw;
    char* sig;
    int type, np1, tn;
    int sV, sP, sQ;
    int xb;
};
DI const char* unip(const char* p) {
    const u64 v = (u64)p;
    return (const char*)(((u64)(u32)uni((int)(v >> 32)) << 32) | (u32)uni((int)v));
}
template <int Z>
DI void mf1(f32x4& c, const u32x4& a, const u32x4& b) {
    if constexpr (Z) asm volatile("v_mfma_f32_16x16x32_bf16 %0, %1, %2, 0" : "=&v"(c) : "v"(a), "a"(b));
    else asm volatile("v_mfma_f32_16x16x32_bf16 %0, %1, %2, %0" : "+v"(c) : "v"(a), "a"(b));
}
template <int Z>
DI void mf2(f32x4& c, const u32x4& a, const u32x4& b) {
    if constexpr (Z) asm volatile("v_mfma_f32_16x16x32_bf16 %0, %1, %2, 0" : "=&v"(c) : "v"(a), "v"(b));
    else asm volatile("v_mfma_f32_16x16x32_bf16 %0, %1, %2, %0" : "+v"(c) : "v"(a), "v"(b));
}
template <int IMM>
DI void gld16a(u32x4& r, const char* sbase, u32 voff) {
    asm volatile("global_load_dwordx4 %0, %1, %2 offset:%3 sc1" : "+a"(r) : "v"(voff), "s"(sbase), "i"(IMM) : "memory");
}
DI void setm0(u32 v) { asm volatile("s_mov_b32 m0, %0" : : "s"(v) : "memory"); }
template <int IMM>
DI void gldsm(const char* sbase, u32 voff) {
    asm volatile("global_load_lds_dwordx4 %0, %1 offset:%2" : : "v"(voff), "s"(sbase), "i"(IMM) : "memory");
}
DI void gst16n(char* sbase, u32 voff, u32x4 v) {
    asm volatile("global_store_dwordx4 %0, %1, %2 sc1" : : "v"(voff), "v"(v), "s"(sbase) : "memory");
}
template <int N>
DI void waitbar() { asm volatile("s_waitcnt vmcnt(%0)\n\ts_barrier" : : "i"(N) : "memory"); }
#define LGKM(n) __builtin_amdgcn_s_waitcnt(0xC07F | ((n) << 8))

#define XSL 10
DI int xhalf() { u32 x; asm volatile("s_getreg_b32 %0, hwreg(HW_REG_XCC_ID)" : "=s"(x)); return (int)((x >> 2) & 1); }
struct Sync { u32x2 r, g, e; };
DI void sync_ld(Sync& y, const char* ws) {
    asm volatile("s_nop 4\n\tglobal_load_dwordx2 %0, %3, %5 sc1\n\tglobal_load_dwordx2 %1, %4, %5 offset:512 sc1\n\tglobal_load_dwordx2 %2, %4, %5 offset:1024 sc1"
                 : "=&v"(y.r), "=&v"(y.g), "=&v"(y.e) : "v"((lanex() & 31) * 8), "v"(0), "s"(ws + OFF_LAT) : "memory");
}
DI u64 sync_tag(u32x2 e) { return (((u64)e[1] << 32 | e[0]) + 1) << 1; }
DI int sync_end(Sync& y, const char* ws, u32& phi, bool ack) {
    asm volatile("" : "+v"(y.r), "+v"(y.g), "+v"(y.e));
#pragma nounroll
    for (int it = 0; it < (1 << 16); ++it) {
        const u64 t = sync_tag(y.e);
        if (__builtin_amdgcn_ballot_w64((y.r[0] & ~1u) != (u32)t || y.r[1] != (u32)(t >> 32) || y.g[0] != (u32)t) == 0) break;
        __builtin_amdgcn_s_sleep(XSL);
        sync_ld(y, ws);
        asm volatile("s_waitcnt vmcnt(0)" : "+v"(y.r), "+v"(y.g), "+v"(y.e) : : "memory");
    }
    if (ack) flag_add((char*)ws + OFF_ST, F_ACK, 1);
    phi = (u32)uni((int)y.g[1]);
    const u32 b = (u32)__builtin_amdgcn_ballot_w64(y.r[0] & 1), all = __builtin_popcount(b) > 16;
    u32 q = 0;
#pragma unroll
    for (int r = 0; r < NREG; ++r) {
        const u32 c = r < 2 ? __builtin_popcount(b & (0x55u << r)) + all : 2 * __builtin_popcount((b >> (4 + 2 * r)) & 3) + all;
        q |= ((c > 2) ^ (u32)((r < 2 ? r : r + (OFF_U >> 21)) & 1)) << r;
    }
    return uni((int)(q ^ 3u * xhalf()));
}
DI void pace(int on) {
    asm volatile("s_cmp_ge_i32 %0, 0\n\ts_cbranch_scc1 .Lp%=\n\ts_waitcnt vmcnt(0)\n.Lp%=:" : : "s"(on) : "memory", "scc");
}
#define SMO 48
#define RM 7
#define BLS 1
#define EPG 1
#define PXT 58
#define PDX 40
#define SPL 0
#define HP0 48
#define UPF 5
#define FRD 25
#define KNT 1
#define BAS 3
#define NO2 2
#define OW 2
#define GSL 6
#define UNAP 700
#define HD1 40
#define PRG 0
#define TG1 0
#define TG2 0
#define TGS 62
#define XFW 0
#define SMB 48
#define SMP SMO
template <int C>
DI void gap(int on) {
    if constexpr (C > 0) asm volatile("s_cmp_ge_i32 %0, 0\n\ts_cbranch_scc1 .Lg%=\n\ts_nop %1\n\ts_nop %2\n\ts_nop %3\n.Lg%=:" : : "s"(on), "n"(C / 12 - 1), "n"(C / 12 - 1), "n"(C / 12 - 1) : "memory", "scc");
}
DI void early_add(char* ctr, int c) {
    asm volatile("s_cmp_lg_u32 %1, 1\n\ts_cbranch_scc1 .Le%=\n\ts_mov_b64 exec, 1\n\ts_nop 1\n\tglobal_atomic_add %2, %3, %0 sc1\n\ts_mov_b64 exec, -1\n.Le%=:"
                 : : "s"(ctr), "s"(c), "v"(0), "v"(1) : "memory", "scc");
}
#define XL 2
template <int NT>
DI void gemm_task(const GemmArgs& g, Sync& y, const char* ws, u32 lds, int w) {
    constexpr int FD = 5, NX = 2 * NT, OPS = NX + 6, BI = 24 - FD;
    const int lane = lanex();
    const int n = lane & 15, q = lane >> 4, qq = (lane >> 2) & 3, pp = lane & 3;
    const bool p2 = g.type == T_VD;
    const int rw = lane >> 3, sp = (lane >> 1) & 3;
    const u32 v2b = (u32)(rw * 1280 + 16 * (lane & 1) + 5120);
    auto v2o = [&](int i) { return v2b + (u32)(((sp ^ (rw >> 1)) << 5) + i * 9216); };
    u32 wv[6];
#pragma unroll
    for (int i = 0; i < 6; ++i)
        wv[i] = (u32)((rw + 8 * (i / 3)) * g.ldw + ((sp ^ (((rw >> 1) & 1) + 2 * (i / 3))) << 5) + 16 * (lane & 1) + (i % 3) * 128 + 5120 - 1024 * i);
    const char* const wp2 = g.w2 + (size_t)(48 * w) * 1280 - 2048;
    const char* wp = unip(g.w1 + (size_t)(16 * w) * g.ldw - 2048);
    int wstep = uni(64 * g.ldw);
    u32 wdst = lds + (u32)w * 6144 + 3072;
    const u32 wdlast = wdst + (GRING - 1) * GBUF;
    auto wring = [&]() { wdst = wdst == wdlast ? wdst - (GRING - 1) * GBUF : wdst + GBUF; };
    auto wload = [&](int i) {
        setm0(wdst);
        wring();
        asm volatile("s_nop 0");
        sfor<0, 6>([&](auto I) { gldsm<1024 * (I.v - 3)>(wp, wv[I.v]); });
        wp += wstep;
    };
    u32x4 vf[NT][6];
    u32x4 fr[FD + 1];
    u32x4 xr[5][NX];
    auto xtie = [&](u32x4 (&x)[NX]) {
#pragma unroll
        for (int i = 0; i < NX; i += 4) asm volatile("" : "+a"(x[i]), "+a"(x[i + 1]), "+a"(x[i + 2]), "+a"(x[i + 3]));
    };
#pragma unroll
    for (int i = 0; i < 5 * NX; ++i) asm volatile("" : "=a"(xr[i / NX][i % NX]));
    u32x2 flo;
    u32 c[4], slot = 0;
    int sd = GBUF;
    auto sring = [&]() { sd = slot == (GRING - 1) * GBUF ? -(GRING - 1) * GBUF : GBUF; slot += (u32)sd; };
    {
        u32 xo[NT];
#pragma unroll
        for (int tt = 0; tt < NT; ++tt) {
            const int tok = 16 * (tt & 1) + n;
            xo[tt] = (u32)((NT / 2 * w + (tt >> 1)) * 40960 + (g.xb ? q * 512 + tok * 16 : q * 16 + tok * 1280));
        }
        f32x4 acc[12][NT];
        const bool rm = NT == 4 && g.type == T_BL;
        const int sw = (qq >> 1) + 2 * (q & 1);
#pragma unroll
        for (int t = 0; t < 4; ++t)
            c[t] = lds + q * 3072 + qq * 128 + (rm ? ((((t & 2) + (pp >> 1)) ^ sw) << 5) + 16 * (pp & 1) + 8 * ((pp >> 1) ^ (t & 1)) : ((t ^ sw) << 5) + 8 * pp);
        auto rlo = [&](int f) { flo = trread(c[f & 3] + (f / 12) * 12288 + ((f % 12) >> 2) * 1024); };
        auto rhi = [&](int f) {
            const u32x2 hi = trread(c[f & 3] + (f / 12) * 12288 + ((f % 12) >> 2) * 1024 + 512);
            fr[f % (FD + 1)] = u32x4{flo[0], flo[1], hi[0], hi[1]};
        };
        const int xk = uni(g.xb ? 2048 : 64);
        const char *xp = g.x, *xq = xp + xk;
        sfor<0, 4>([&](auto J) {
            if constexpr (J.v < XL) { sfor<0, NX>([&](auto I) { gld16a<0>(xr[J.v][I.v], I.v & 1 ? xq : xp, xo[I.v >> 1]); }); xp += 2 * xk; xq += 2 * xk; }
            wload(J.v);
            if constexpr (NT == 4 && PRG && J.v < 3) asm volatile("s_sleep %0" : : "n"(PRG));
        });
        waitbar<18 + (XL - 1) * NX>();
        xtie(xr[0]);
#pragma unroll
        for (int i = 0; i < FD; ++i) { rlo(i); rhi(i); }
        char* po = g.out;
#pragma nounroll
        for (int ph = g.np1; ph > 0; --ph) {
            sfor<0, 10>([&](auto J) {
                constexpr int j = J.v;
                u32x4 (&xc)[NX] = xr[j % 5];
                u32x4 (&xn)[NX] = xr[(j + XL) % 5];
                sfor<0, 24>([&](auto I) {
                    constexpr int idx = I.v, kk = idx / 12, T = idx % 12, z = j == 0 && kk == 0, f = (idx + FD) % 24;
                    constexpr int mo = NT == 4 ? (idx < BI && idx % 4 != 2 ? idx - (idx + 1) / 4 : -1) : (idx < BI && idx % 2 == 0 ? idx / 2 : -1);
                    auto mem = [&]() {
                        if constexpr (mo >= NX) gldsm<1024 * (mo - NX - 3)>(wp, wv[mo - NX]);
                        else if constexpr (mo >= 0) gld16a<0>(xn[mo], mo & 1 ? xq : xp, xo[mo >> 1]);
                    };
                    auto misc = [&]() {
                        if constexpr (idx == 1) wring();
                        if constexpr (idx == 13) sring();
                        if constexpr (j == 1 && idx == 22) early_add(g.sig, ph + w);
                        if constexpr (idx >= 15 && idx < BI) { c[idx - 15] += (u32)sd; asm volatile("" : "+v"(c[idx - 15])); }
                        const bool more = ph > 1;
                        if constexpr (j != 5) {
                            if constexpr (idx == 20) wp += wstep;
                        } else {
                            if constexpr (idx == 19) { wp = more ? g.w1b + (size_t)(16 * w) * g.ldw - 2048 : wp2; wstep = more ? wstep : (p2 ? 128 : 0); }
                            if constexpr (idx >= 20 && idx < 23) {
#pragma unroll
                                for (int i = 2 * (idx - 20); i < 2 * (idx - 19); ++i) { wv[i] = more ? wv[i] : (p2 ? v2o(i) : 5120u); asm volatile("" : "+v"(wv[i])); }
                            }
                        }
                        if constexpr (j != 9 - XL) {
                            if constexpr (idx == 21) xp += 2 * xk;
                            if constexpr (idx == 22) xq += 2 * xk;
                        } else if constexpr (idx == 23) {
                            xp = g.x; xq = xp + xk;
#pragma unroll
                            for (int i = 0; i < NT; ++i) { xo[i] = more ? xo[i] : 0u; asm volatile("" : "+v"(xo[i])); }
                        }
                    };
                    if constexpr (idx == BI) { waitbar<6 + (XL - 1) * OPS>(); xtie(xr[(j + 1) % 5]); }
                    if constexpr (idx % 2 == 0) LGKM(6);
                    PIN();
                    const u32x4 a = fr[idx % (FD + 1)];
                    mf1<z>(acc[T][0], a, xc[kk]); PIN();
                    rlo(f);
                    if constexpr (idx == 0) setm0(wdst);
                    if constexpr (NT == 2) rhi(f);
                    PIN();
                    mf1<z>(acc[T][1], a, xc[2 + kk]); PIN();
                    if constexpr (NT == 4) {
                        rhi(f); PIN();
                        mf1<z>(acc[T][2], a, xc[4 + kk]); PIN();
                        mem(); PIN();
                        mf1<z>(acc[T][3], a, xc[6 + kk]); PIN();
                    } else { mem(); PIN(); }
                    misc();
                    PIN();
                });
            });
            asm volatile("s_nop 7\n\ts_nop 1");
            PIN();
#pragma unroll
            for (int tt = 0; tt < NT; ++tt)
#pragma unroll
                for (int ks = 0; ks < 6; ++ks)
                    vf[tt][ks] = u32x4{cvtpk(acc[2 * ks][tt][0], acc[2 * ks][tt][1]), cvtpk(acc[2 * ks][tt][2], acc[2 * ks][tt][3]),
                                       cvtpk(acc[2 * ks + 1][tt][0], acc[2 * ks + 1][tt][1]), cvtpk(acc[2 * ks + 1][tt][2], acc[2 * ks + 1][tt][3])};
            if (g.type == T_QK) {
#pragma unroll
                for (int tt = 0; tt < NT; ++tt)
#pragma unroll
                    for (int ks = 0; ks < 6; ++ks) {
                        gst16(po + (size_t)(NT / 2 * w + (tt >> 1)) * (NH * 12288) + (tt & 1) * 6144 + ks * 1024, lane * 16,
                              u32x4{vf[tt][ks][0], vf[tt][ks][2], vf[tt][ks][1], vf[tt][ks][3]});
                        if (NT == 4 && EPG) asm volatile("s_sleep %0" : : "n"(EPG));
                    }
            } else if (rm) {
                const bool sx = q >= 2;
#pragma unroll
                for (int i = 0; i < 3 * NT; ++i) {
                    const int tp = i / 6, ks = i % 6;
                    u32x4 o[2];
#pragma unroll
                    for (int j = 0; j < 4; ++j) {
                        const u32x4 &v = vf[2 * tp][ks], &u = vf[2 * tp + 1][ks];
                        if (BLS) {
                            const auto r = __builtin_amdgcn_permlane32_swap(v[j], u[j], false, false);
                            o[0][j] = r[0]; o[1][j ^ 2] = r[1];
                        } else { o[0][j] = sx ? v[j ^ 2] : v[j]; o[1][j] = sx ? u[j ^ 2] : u[j]; }
                    }
#pragma unroll
                    for (int j = 0; j < 2; ++j) {
                        gst16(po + (size_t)(2 * w + tp) * g.sV + j * (BLS ? 1024 : 256) + ks * 2048, BLS ? (u32)((q & 1) * 512 + (q >> 1) * 256 + n * 16) : (u32)(q * 512 + n * 16), o[j]);
                        if (EPG) asm volatile("s_sleep %0" : : "n"(EPG));
                    }
                }
            }
            po = g.outb;
            PIN();
        }
    }
    if (p2) {
        constexpr int HI = 2048;
        const int r = 4 * (q & 1) + qq, sw = r >> 1;
#pragma unroll
        for (int t = 0; t < 4; ++t) c[t] = lds + slot + (q >> 1) * 1024 + r * 128 + ((t ^ sw) << 5) + 8 * pp;
        sring();
        u32 so = (u32)((n >> 3) * g.sV + q * g.sQ + (n & 7) * 16);
        char* const o0 = g.out + (size_t)(NT / 2 * w) * g.sP;
        char* const o1 = o0 + g.sP;
        const int sv2 = 2 * g.sV;
        auto rlo = [&](int f) { flo = trread(c[f / 6] + (f % 6) * 4096); };
        auto rhi = [&](int f) {
            const u32x2 hi = trread(c[f / 6] + (f % 6) * 4096 + HI);
            fr[f % (FD + 1)] = u32x4{flo[0], flo[1], hi[0], hi[1]};
        };
        f32x4 a2[2][NT];
        u32 e[NT / 2][4];
        auto cv = [&](f32x4 (&a)[NT], int i) { e[i >> 2][i & 3] = cvtpk(a[i >> 1][2 * (i & 1)], a[i >> 1][2 * (i & 1) + 1]); asm volatile("" : "+v"(e[i >> 2][i & 3])); };
        auto st = [&](int pl) { gst16n(pl ? o1 : o0, so, u32x4{e[pl][0], e[pl][1], e[pl][2], e[pl][3]}); };
        PIN();
#pragma unroll
        for (int i = 0; i < FD; ++i) { rlo(i); rhi(i); }
#pragma unroll
        for (int tt = 0; tt < NT; ++tt) a2[1][tt] = f32x4{0.f, 0.f, 0.f, 0.f};
#pragma nounroll
        for (int t = 0; t < g.tn; ++t) {
            sfor<0, 24>([&](auto I) {
                constexpr int idx = I.v, cb = idx / 6, ks = idx % 6, f = (idx + FD) % 24;
                f32x4 (&ac)[NT] = a2[cb & 1];
                f32x4 (&ap)[NT] = a2[(cb & 1) ^ 1];
                constexpr int mo = cb < 2 ? (ks == 0 ? 3 * cb : (ks >= 4 ? 3 * cb + ks - 3 : -1)) : -1;
                auto mem = [&]() {
                    if constexpr (mo >= 0) gldsm<1024 * (mo - 3)>(wp, wv[mo]);
                    if constexpr (ks == 3 || (NT == 4 && ks == 2)) { if (cb > 0 || t > 0) st(NT == 4 ? ks - 2 : 0); }
                };
                auto misc = [&]() {
                    if constexpr (idx == 12) wring();
                    if constexpr (idx == 23) sring();
                    if constexpr (idx == 16) { wp += t < g.tn - 5 ? 128 : 0; }
                    if constexpr (ks == 0) { c[cb] += (u32)sd; asm volatile("" : "+v"(c[cb])); }
                    if constexpr (ks == 4) { so += (cb > 0 || t > 0) ? (u32)sv2 : 0u; asm volatile("" : "+v"(so)); }
                };
                if constexpr (idx == BI) waitbar<NT == 4 ? 38 : 28>();
                if constexpr (idx % 2 == 0) LGKM(6);
                PIN();
                const u32x4 b = fr[idx % (FD + 1)];
                mf2<ks == 0>(ac[0], vf[0][ks], b); PIN();
                rlo(f);
                if constexpr (idx == 0) setm0(wdst);
                if constexpr (NT == 2) rhi(f);
                if constexpr (NT == 4 && ks >= 1 && ks <= 3) cv(ap, ks == 1 ? 0 : (ks == 2 ? 4 : 7));
                PIN();
                mf2<ks == 0>(ac[1], vf[1][ks], b); PIN();
                if constexpr (NT == 4) {
                    rhi(f);
                    if constexpr (ks == 1 || ks == 2) cv(ap, ks == 1 ? 1 : 5);
                    PIN();
                    mf2<ks == 0>(ac[2], vf[2][ks], b); PIN();
                    mem();
                    if constexpr (ks == 1) { cv(ap, 2); cv(ap, 3); }
                    PIN();
                    mf2<ks == 0>(ac[3], vf[3][ks], b); PIN();
                    if constexpr (ks == 2) cv(ap, 6);
                } else {
                    if constexpr (ks == 1 || ks == 2) { cv(ap, 2 * ks - 2); cv(ap, 2 * ks - 1); }
                    mem(); PIN();
                }
                misc();
                PIN();
            });
            asm volatile("s_nop 7\n\ts_nop 1");
        }
        PIN();
#pragma unroll
        for (int i = 0; i < 2 * NT; ++i) cv(a2[1], i);
#pragma unroll
        for (int pl = 0; pl < NT / 2; ++pl) st(pl);
        asm volatile("s_nop 1");
    }
    if constexpr (NT == 2) asm volatile(".Lkm_g2:"); else asm volatile(".Lkm_g4:");
    sync_ld(y, ws);
    waitvm<0>();
    asm volatile("" : "+v"(y.r), "+v"(y.g), "+v"(y.e));
    sfor<0, 5>([&](auto I) { xtie(xr[I.v]); });
}

struct Ptrs {
    const char *x, *mem0, *wq, *wk, *wv, *wo, *wqkvb, *wob, *gw;
    char* out;
    char* ws;
};

enum { Q_H = 0, Q_VD = 1, Q_KQ = 2, Q_QKVB = 3, Q_BA = 4, Q_BO = 5, Q_PX = 6, Q_OUT = 7 };
DI void run_gemm(const Ptrs& p, int ty, int b, int i, int hf, bool raw, Sync& y, u32 lds, int w) {
    GemmArgs g;
    g.sV = g.sP = g.sQ = 0; g.w2 = p.wo; g.ldw = HD * 2; g.np1 = 1; g.tn = 10;
    g.sig = p.ws + OFF_ST + F_END * 128;
    const char* xf = raw ? p.x : p.ws + OFF_XF;
    g.xb = !raw;
    int s0 = 8 * b;
    if (ty == Q_VD) {
        s0 = b < 2 ? 4 * b : s0 - 8;
        g.type = T_VD; g.w1 = g.w1b = p.wv + i * DH * 2;
        g.w2 = p.wo + (size_t)i * DH * 1280;
        g.out = g.outb = p.ws + OFF_D + (size_t)s0 * D_STRIDE + i * 512;
        g.sV = 16384; g.sP = (int)D_STRIDE; g.sQ = 128;
        if (b == 0) { g.tn = 5; g.w2 += hf * 640; g.out += hf * 40 * 16384; }
    } else if (ty == Q_H) {
        s0 = 0;
        g.type = T_QK; g.w1 = g.w1b = (b ? p.wq : p.wk) + i * DH * 2;
        g.out = g.outb = p.ws + (b ? OFF_QX : OFF_KX) + (size_t)i * 12288;
        b = 0;
    } else if (ty == Q_KQ) {
        s0 = b < 2 ? 4 * b : s0 - 8;
        g.type = T_QK; g.w1 = p.wk + i * DH * 2; g.w1b = p.wq + i * DH * 2;
        g.out = p.ws + OFF_KX + (size_t)(s0 * NH + i) * 12288;
        g.outb = g.out + (OFF_QX - OFF_KX);
        g.np1 = 2;
        g.sig = p.ws + OFF_ST + (F_K + b * 32 + i) * 128;
    } else if (ty == Q_QKVB) {
        g.type = T_BL; g.w1 = p.wqkvb + i * 768; g.w1b = g.w1 + 384; g.ldw = 3840;
        g.sV = 1920 * 64; g.np1 = 2;
        g.out = p.ws + OFF_QKVB + (size_t)s0 * g.sV + i * 48 * 512; g.outb = g.out + 24 * 512;
    } else {
        const int c0 = min(i * DH, DM - DH);
        g.type = T_BL; g.w1 = g.w1b = p.wob + c0 * 2; g.ldw = 1280;
        xf = p.ws + OFF_OB;
        g.sV = 40960;
        g.out = g.outb = p.ws + OFF_BOUT + (size_t)s0 * 40960 + c0 * 64;
    }
    g.x = xf + (size_t)s0 * 40960;
    if (ty <= Q_KQ && b < 2) gemm_task<2>(g, y, p.ws, lds, w);
    else gemm_task<4>(g, y, p.ws, lds, w);
}
DI int pblk(int s) { return s < 8 ? s >> 2 : min(s >> 3, 7) + 1; }

template <int IMM>
DI void gld16c(u32x4& r, const char* sbase, u32 voff) {
    asm volatile("global_load_dwordx4 %0, %1, %2 offset:%3 sc1" : "=v"(r) : "v"(voff), "s"(sbase), "i"(IMM) : "memory");
}
#define XP1 " nt"
template <int IMM>
DI void gld16x(u32x4& r, const char* sbase, u32 voff) {
    asm volatile("global_load_dwordx4 %0, %1, %2 offset:%3 sc1" XP1 : "=v"(r) : "v"(voff), "s"(sbase), "i"(IMM) : "memory");
}
DI void gst4c(char* sbase, u32 voff, u32 v) {
    asm volatile("global_store_dword %0, %1, %2 sc1" : : "v"(voff), "v"(v), "s"(sbase) : "memory");
}
template <int N>
DI void waitv(u32x4& r) { asm volatile("s_waitcnt vmcnt(%1)" : "+v"(r) : "i"(N) : "memory"); }
DI u32 mt() { return (u32)__builtin_amdgcn_s_memtime(); }
DI u32 rt() { return (u32)__builtin_amdgcn_s_memrealtime(); }
DI void spin_to(u32 tgt) { while ((int)(mt() - tgt) < 0) {} }
DI u32 max3u(u32 a, u32 b, u32 c) { return max(max(a, b), c); }
DI u32x4 ldsr16(u32 a) { return *LDSP(u32x4, a); }
DI void clocks(u32& r, u32& m) {
    u64 a, b;
    asm volatile("s_memrealtime %0\n\ts_memtime %1\n\ts_waitcnt lgkmcnt(0)" : "=s"(a), "=s"(b) : : "memory");
    r = (u32)a; m = (u32)b;
}
#define SLATE 20
DI void edge(u32& r, u32& m) {
    u64 a, t[9];
    u32 n = 0;
#define RT(i) "s_memrealtime %" #i "\n\ts_nop 1\n\t"
#define RC(i) "\n\ts_cmp_eq_u64 %" #i ", %1\n\ts_addc_u32 %10, %10, 0"
    asm volatile("s_memtime %0\n\t" RT(1) RT(2) RT(3) RT(4) RT(5) RT(6) RT(7) RT(8) "s_memrealtime %9\n\ts_waitcnt lgkmcnt(0)" RC(2) RC(3) RC(4) RC(5) RC(6) RC(7) RC(8) RC(9)
                 : "=&s"(a), "=&s"(t[0]), "=&s"(t[1]), "=&s"(t[2]), "=&s"(t[3]), "=&s"(t[4]), "=&s"(t[5]), "=&s"(t[6]), "=&s"(t[7]), "=&s"(t[8]), "+s"(n) : : "scc", "memory");
    m = (u32)a + 12 * n;
    r = (u32)t[0] + 4;
}
#define SPB 90
struct Clock {
    u32 r0, m0, r1, m1; int cpt;
    DI void init() { edge(r0, m0); r1 = r0; m1 = m0; cpt = 24 * 64; }
    DI u32 sched(u32& phi, int ofs, int G, int mode) {
        if (mode == 0) edge(r1, m1);
        u32 r = r1, m = m1;
        if (mode == 2) clocks(r, m);
        int rem = (int)(phi + (u32)ofs - r);
        if (__builtin_expect(rem < -SLATE, 0)) { const int k = (-rem / G + 1) * G; phi += (u32)k; rem += k; }
        return m + (u32)((min(rem, 3000) * cpt) >> 6) - SPB;
    }
    DI u32 at(u32 t) { u32 r, m; clocks(r, m); return m + (u32)((min(max((int)(t - r), 0), 3000) * cpt) >> 6); }
    DI void calib() {
        const int dr = (int)(r1 - r0);
        if (dr >= 60) { int c = (int)fminf(fmaxf((float)(int)(m1 - m0) * 64.f * __builtin_amdgcn_rcpf((float)dr), 256.f), 2560.f); asm volatile("" : "+v"(c)); cpt = uni(c); r0 = r1; m0 = m1; }
    }
};
#define VISS 0
DI void follow(u32& phi, int ofs, u32 t, int G) { const int late = (int)(t - phi) - ofs; if (__builtin_expect(late > 0, 0)) phi += (u32)((late / G + 1) * G); }
DI bool unwritten(const char* base, u32 off) {
#pragma nounroll
    for (int i = 0; i < 10; ++i) {
        u32x4 t;
        gld16c<-4096>(t, base + (i + (i & 8)) * 1024, off); waitv<0>(t);
        if (__builtin_amdgcn_ballot_w64(max3u(max(t[0], t[1]), t[2], t[3]) == 0xffffffffu) != 0) return true;
    }
    return false;
}

DI size_t uslot(int sd, int s) { const int r = (s * 43) >> 8; return OFF_U + (u32)uni(s * (int)U_STRIDE + r * (int)((2u << 20) - 6 * U_STRIDE) + ((sd >> (2 + r)) & 1) * 8192); }
DI int uchunk(int v) { return (2 * (v & 3) + ((v >> 2) & 1) + (v & ~7)) * 8192; }
DI size_t mslot(int sd, int s) { return (u32)uni(s * (int)HO_STRIDE + ((sd >> (s >> 5)) & 1) * 8192 + 4096); }
struct ChainCfg { int T, cR, G, dV, fast, s1, T2, cR2, s2, T3, cR3, dU; };
constexpr int TT_OFF = 88 + 48 + 360;
DI void karg_warm() {
    u32 a[13];
    asm volatile("s_load_dword %0, %13, 0\n\t" "s_load_dword %1, %13, 64\n\t" "s_load_dword %2, %13, 128\n\t" "s_load_dword %3, %13, 192\n\t" "s_load_dword %4, %13, 256\n\t" "s_load_dword %5, %13, 320\n\t" "s_load_dword %6, %13, 384\n\t" "s_load_dword %7, %13, 448\n\t" "s_load_dword %8, %13, 512\n\t" "s_load_dword %9, %13, 576\n\t" "s_load_dword %10, %13, 640\n\t" "s_load_dword %11, %13, 704\n\t" "s_load_dword %12, %13, 748\n\t" "s_waitcnt lgkmcnt(0)"
                 : "=&s"(a[0]), "=&s"(a[1]), "=&s"(a[2]), "=&s"(a[3]), "=&s"(a[4]), "=&s"(a[5]), "=&s"(a[6]), "=&s"(a[7]), "=&s"(a[8]), "=&s"(a[9]), "=&s"(a[10]), "=&s"(a[11]), "=&s"(a[12])
                 : "s"(__builtin_amdgcn_kernarg_segment_ptr()) : "memory");
}
DI u32 sched_ld0(int s) {
    u32 r;
    asm volatile("s_load_dword %0, %1, %2 offset:%3\n\ts_waitcnt lgkmcnt(0)" : "=s"(r) : "s"(__builtin_amdgcn_kernarg_segment_ptr()), "s"(4 * s), "n"(TT_OFF) : "memory");
    return r;
}
#define HP 60
#define HQ 96

constexpr int SL_W = 0;
constexpr int SL_IMG = 131072;
constexpr int SL_KX = SL_IMG + 6656;
constexpr int SL_PF = SL_KX + 12288 + 1024;
constexpr int S_LDS = SL_PF + 256;
DI void nap(int t) { const u32 wake = rt() + (u32)t; while ((int)(rt() - wake) < 0) __builtin_amdgcn_s_sleep(4); }
DI void until(u32 t) { while ((int)(rt() - t) < 0) __builtin_amdgcn_s_sleep(1); }
DI void dly(int n) {
    asm volatile(".Ld%=:\n\ts_sub_u32 %0, %0, 1\n\ts_nop 0\n\ts_cbranch_scc0 .Ld%=" : "+s"(n) : : "scc", "memory");
}
DI void rdy_add(u32& r, char* fl) {
    asm volatile("s_mov_b64 exec, 1\n\ts_nop 3\n\tglobal_atomic_add %0, %1, %2, %3 sc0 sc1\n\ts_mov_b64 exec, -1" : "=&v"(r) : "v"(F_RDY * 128), "v"(1), "s"(fl) : "memory");
}
DI void rdy_end(u32 r, char* fl, int dt) {
    if (uni((int)r) == NS + NVB - 2 && lanex() < 8) gst4c(fl, (F_GO + lanex()) * 128, (rt() + (u32)dt) & ~1u);
}
DI u32 go_wait(const char* fl, int i) {
    u32 v;
#pragma nounroll
    for (int it = 0; it < (1 << 16); ++it) {
        flag_ld(v, fl, F_GO + (i & 7));
        asm volatile("s_waitcnt vmcnt(0)" : "+v"(v) : : "memory");
        if (uni((int)v) != -1) break;
        __builtin_amdgcn_s_sleep(GSL);
    }
    return (u32)uni((int)v);
}
DI u32 go_wg(const char* fl, int i, int w, u32 la) {
    if (w == 0) *LDSP(u32, la) = go_wait(fl, i);
    barrier();
    return (u32)uni((int)*LDSP(u32, la));
}
DI void pfl(const char* sbase, u32 voff, u32 lds) {
    asm volatile("s_mov_b32 m0, %2\n\ts_nop 3\n\tglobal_load_lds_dword %0, %1" : : "v"(voff), "s"(sbase), "s"(lds + SL_PF) : "memory");
}
DI void score_role(const Ptrs& p, const ChainCfg cfg, int h, u32 lds, int w) {
    const int lane = lanex(), tid = 64 * w + lane;
    const int n = lane & 15, g = lane >> 4, qq = (lane >> 2) & 3, pp = lane & 3;
    u32x32 wa[8], wv[3];
    const u32 wreg = lds + SL_W + (u32)w * 32768;
    const u32 lfa = wreg + lane * 16;
    const char* kxp = p.ws + OFF_KX + (size_t)h * 12288 + w * 3072;
    const u32 kxl = lds + SL_KX;
    char* const fl = p.ws + OFF_ST;
    const u32 kxw = kxl + w * 3072 - (w < 2 ? 16 : 0);
    auto kxdma = [&]() {
        asm volatile("s_mov_b32 m0, %2\n\ts_nop 3\n\tglobal_load_lds_dwordx4 %0, %1 sc1\n\tglobal_load_lds_dwordx4 %0, %1 offset:1024 sc1\n\tglobal_load_lds_dwordx4 %0, %1 offset:2048 sc1"
                     : : "v"(lanex() * 16), "s"(kxp), "s"(kxw) : "memory");
    };
    u32 kfl, phi;
    int sd;
    {
        constexpr int GS = 3104;
        Sync y;
        if (w < 3) {
#pragma unroll
            for (int i = PXT; i < NSEG; ++i) gst16(p.ws + OFF_KM + (i * NH + h) * 3072 + w * 1024, lane * 16, u32x4{~0u, ~0u, ~0u, ~0u});
#pragma unroll
            for (int i = NSEG - 4; i < NSEG; ++i) gst16(p.ws + OFF_PX + i * 81920 + (w < 2 ? h * 2048 + w * 1024 : 65536 + h * 512), w < 2 ? lane * 16 : (lane * 16) & 511, u32x4{~0u, ~0u, ~0u, ~0u});
        }
        auto dvo = [&](int i) { const int ix = i * 64 + lane; return (u32)((ix / 24) * (HD * 2) + (ix % 24) * 16); };
        const u32 tra = lds + (2 * (g >> 1) + (qq >> 1)) * GS + (4 * (g & 1) + (qq & 1)) * 384 + 8 * pp;
        u32x8 pa = u32x8{dvo(0), dvo(1), dvo(2), tra + 96 * w + 32 * (w & 1), tra + 96 * w - 64 * (w & 1), tra + 32 * w, lfa, (u32)(lane & 31) * 8};
        asm volatile("s_nop 4\n\t" "s_add_u32 s42, %21, 0x0\n\t" "s_add_u32 s42, s46, s42\n\t" "s_addc_u32 s43, s47, 0\n\t" "s_add_u32 m0, %18, 0x0\n\t" "s_nop 0\n\t" "global_load_lds_dwordx4 v224, s[42:43]\n\t"
                     "s_add_u32 m0, %18, 0x400\n\t" "s_nop 0\n\t" "global_load_lds_dwordx4 v225, s[42:43]\n\t" "s_add_u32 m0, %18, 0x800\n\t" "s_nop 0\n\t" "global_load_lds_dwordx4 v226, s[42:43]\n\t"
                     "s_add_u32 s42, %21, 0x18000\n\t" "s_add_u32 s42, s46, s42\n\t" "s_addc_u32 s43, s47, 0\n\t" "s_add_u32 m0, %18, 0xc20\n\t" "s_nop 0\n\t" "global_load_lds_dwordx4 v224, s[42:43]\n\t"
                     "s_add_u32 m0, %18, 0x1020\n\t" "s_nop 0\n\t" "global_load_lds_dwordx4 v225, s[42:43]\n\t" "s_add_u32 m0, %18, 0x1420\n\t" "s_nop 0\n\t" "global_load_lds_dwordx4 v226, s[42:43]\n\t"
                     "s_sub_u32 s42, 0xc0000, %21\n\t" "s_add_u32 s42, s46, s42\n\t" "s_addc_u32 s43, s47, 0\n\t" "s_add_u32 m0, %18, 0x6100\n\t" "s_nop 0\n\t" "global_load_lds_dwordx4 v224, s[42:43]\n\t"
                     "s_add_u32 m0, %18, 0x6500\n\t" "s_nop 0\n\t" "global_load_lds_dwordx4 v225, s[42:43]\n\t" "s_add_u32 m0, %18, 0x6900\n\t" "s_nop 0\n\t" "global_load_lds_dwordx4 v226, s[42:43]\n\t"
                     "s_sub_u32 s42, 0xd8000, %21\n\t" "s_add_u32 s42, s46, s42\n\t" "s_addc_u32 s43, s47, 0\n\t" "s_add_u32 m0, %18, 0x6d20\n\t" "s_nop 0\n\t" "global_load_lds_dwordx4 v224, s[42:43]\n\t"
                     "s_add_u32 m0, %18, 0x7120\n\t" "s_nop 0\n\t" "global_load_lds_dwordx4 v225, s[42:43]\n\t" "s_add_u32 m0, %18, 0x7520\n\t" "s_nop 0\n\t" "global_load_lds_dwordx4 v226, s[42:43]\n\t"
                     "s_add_u32 s42, %21, 0x180000\n\t" "s_add_u32 s42, s46, s42\n\t" "s_addc_u32 s43, s47, 0\n\t" "s_add_u32 m0, %18, 0xc200\n\t" "s_nop 0\n\t" "global_load_lds_dwordx4 v224, s[42:43]\n\t"
                     "s_add_u32 m0, %18, 0xc600\n\t" "s_nop 0\n\t" "global_load_lds_dwordx4 v225, s[42:43]\n\t" "s_add_u32 m0, %18, 0xca00\n\t" "s_nop 0\n\t" "global_load_lds_dwordx4 v226, s[42:43]\n\t"
                     "s_add_u32 s42, %21, 0x198000\n\t" "s_add_u32 s42, s46, s42\n\t" "s_addc_u32 s43, s47, 0\n\t" "s_add_u32 m0, %18, 0xce20\n\t" "s_nop 0\n\t" "global_load_lds_dwordx4 v224, s[42:43]\n\t"
                     "s_add_u32 m0, %18, 0xd220\n\t" "s_nop 0\n\t" "global_load_lds_dwordx4 v225, s[42:43]\n\t" "s_add_u32 m0, %18, 0xd620\n\t" "s_nop 0\n\t" "global_load_lds_dwordx4 v226, s[42:43]\n\t"
                     "s_sub_u32 s42, 0x240000, %21\n\t" "s_add_u32 s42, s46, s42\n\t" "s_addc_u32 s43, s47, 0\n\t" "s_add_u32 m0, %18, 0x12300\n\t" "s_nop 0\n\t" "global_load_lds_dwordx4 v224, s[42:43]\n\t"
                     "s_add_u32 m0, %18, 0x12700\n\t" "s_nop 0\n\t" "global_load_lds_dwordx4 v225, s[42:43]\n\t" "s_add_u32 m0, %18, 0x12b00\n\t" "s_nop 0\n\t" "global_load_lds_dwordx4 v226, s[42:43]\n\t"
                     "s_sub_u32 s42, 0x258000, %21\n\t" "s_add_u32 s42, s46, s42\n\t" "s_addc_u32 s43, s47, 0\n\t" "s_add_u32 m0, %18, 0x12f20\n\t" "s_nop 0\n\t" "global_load_lds_dwordx4 v224, s[42:43]\n\t"
                     "s_add_u32 m0, %18, 0x13320\n\t" "s_nop 0\n\t" "global_load_lds_dwordx4 v225, s[42:43]\n\t" "s_add_u32 m0, %18, 0x13720\n\t" "s_nop 0\n\t" "global_load_lds_dwordx4 v226, s[42:43]\n\t"
                     "s_waitcnt vmcnt(12) lgkmcnt(0)\n\t" "s_barrier\n\t" "s_add_u32 s42, %21, 0x300000\n\t" "s_add_u32 s42, s46, s42\n\t" "s_addc_u32 s43, s47, 0\n\t" "s_add_u32 m0, %18, 0x18400\n\t" "s_nop 0\n\t"
                     "global_load_lds_dwordx4 v224, s[42:43]\n\t" "s_add_u32 m0, %18, 0x18800\n\t" "s_nop 0\n\t" "global_load_lds_dwordx4 v225, s[42:43]\n\t" "s_add_u32 m0, %18, 0x18c00\n\t" "s_nop 0\n\t"
                     "global_load_lds_dwordx4 v226, s[42:43]\n\t" "s_add_u32 s42, %21, 0x318000\n\t" "s_add_u32 s42, s46, s42\n\t" "s_addc_u32 s43, s47, 0\n\t" "s_add_u32 m0, %18, 0x19020\n\t" "s_nop 0\n\t"
                     "global_load_lds_dwordx4 v224, s[42:43]\n\t" "s_add_u32 m0, %18, 0x19420\n\t" "s_nop 0\n\t" "global_load_lds_dwordx4 v225, s[42:43]\n\t" "s_add_u32 m0, %18, 0x19820\n\t" "s_nop 0\n\t"
                     "global_load_lds_dwordx4 v226, s[42:43]\n\t" "s_sub_u32 s42, 0x3c0000, %21\n\t" "s_add_u32 s42, s46, s42\n\t" "s_addc_u32 s43, s47, 0\n\t" "s_add_u32 m0, %18, 0x1e500\n\t" "s_nop 0\n\t"
                     "global_load_lds_dwordx4 v224, s[42:43]\n\t" "s_add_u32 m0, %18, 0x1e900\n\t" "s_nop 0\n\t" "global_load_lds_dwordx4 v225, s[42:43]\n\t" "s_add_u32 m0, %18, 0x1ed00\n\t" "s_nop 0\n\t"
                     "global_load_lds_dwordx4 v226, s[42:43]\n\t" "s_sub_u32 s42, 0x3d8000, %21\n\t" "s_add_u32 s42, s46, s42\n\t" "s_addc_u32 s43, s47, 0\n\t" "s_add_u32 m0, %18, 0x1f120\n\t" "s_nop 0\n\t"
                     "global_load_lds_dwordx4 v224, s[42:43]\n\t" "s_add_u32 m0, %18, 0x1f520\n\t" "s_nop 0\n\t" "global_load_lds_dwordx4 v225, s[42:43]\n\t" "s_add_u32 m0, %18, 0x1f920\n\t" "s_nop 0\n\t"
                     "global_load_lds_dwordx4 v226, s[42:43]\n\t" "v_add_u32 v232, 0x0, v229\n\t" "ds_read_b64_tr_b16 v[8:9], v232\n\t" "ds_read_b64_tr_b16 v[10:11], v232 offset:768\n\t"
                     "ds_read_b64_tr_b16 v[12:13], v232 offset:12416\n\t" "ds_read_b64_tr_b16 v[14:15], v232 offset:13184\n\t" "ds_read_b64_tr_b16 a[96:97], v232 offset:128\n\t"
                     "ds_read_b64_tr_b16 a[98:99], v232 offset:896\n\t" "ds_read_b64_tr_b16 a[100:101], v232 offset:12544\n\t" "ds_read_b64_tr_b16 a[102:103], v232 offset:13312\n\t"
                     "ds_read_b64_tr_b16 a[176:177], v232 offset:256\n\t" "ds_read_b64_tr_b16 a[178:179], v232 offset:1024\n\t" "ds_read_b64_tr_b16 a[180:181], v232 offset:12672\n\t"
                     "ds_read_b64_tr_b16 a[182:183], v232 offset:13440\n\t" "ds_read_b64_tr_b16 v[40:41], v232 offset:24832\n\t" "ds_read_b64_tr_b16 v[42:43], v232 offset:25600\n\t"
                     "ds_read_b64_tr_b16 v[44:45], v232 offset:37248\n\t" "ds_read_b64_tr_b16 v[46:47], v232 offset:38016\n\t" "ds_read_b64_tr_b16 a[112:113], v232 offset:24960\n\t"
                     "ds_read_b64_tr_b16 a[114:115], v232 offset:25728\n\t" "ds_read_b64_tr_b16 a[116:117], v232 offset:37376\n\t" "ds_read_b64_tr_b16 a[118:119], v232 offset:38144\n\t"
                     "ds_read_b64_tr_b16 a[192:193], v232 offset:25088\n\t" "ds_read_b64_tr_b16 a[194:195], v232 offset:25856\n\t" "ds_read_b64_tr_b16 a[196:197], v232 offset:37504\n\t"
                     "ds_read_b64_tr_b16 a[198:199], v232 offset:38272\n\t" "s_waitcnt vmcnt(12) lgkmcnt(0)\n\t" "s_barrier\n\t" "s_add_u32 s42, %21, 0x480000\n\t" "s_add_u32 s42, s46, s42\n\t" "s_addc_u32 s43, s47, 0\n\t"
                     "s_add_u32 m0, %18, 0x0\n\t" "s_nop 0\n\t" "global_load_lds_dwordx4 v224, s[42:43]\n\t" "s_add_u32 m0, %18, 0x400\n\t" "s_nop 0\n\t" "global_load_lds_dwordx4 v225, s[42:43]\n\t"
                     "s_add_u32 m0, %18, 0x800\n\t" "s_nop 0\n\t" "global_load_lds_dwordx4 v226, s[42:43]\n\t" "s_add_u32 s42, %21, 0x498000\n\t" "s_add_u32 s42, s46, s42\n\t" "s_addc_u32 s43, s47, 0\n\t"
                     "s_add_u32 m0, %18, 0xc20\n\t" "s_nop 0\n\t" "global_load_lds_dwordx4 v224, s[42:43]\n\t" "s_add_u32 m0, %18, 0x1020\n\t" "s_nop 0\n\t" "global_load_lds_dwordx4 v225, s[42:43]\n\t"
                     "s_add_u32 m0, %18, 0x1420\n\t" "s_nop 0\n\t" "global_load_lds_dwordx4 v226, s[42:43]\n\t" "s_sub_u32 s42, 0x540000, %21\n\t" "s_add_u32 s42, s46, s42\n\t" "s_addc_u32 s43, s47, 0\n\t"
                     "s_add_u32 m0, %18, 0x6100\n\t" "s_nop 0\n\t" "global_load_lds_dwordx4 v224, s[42:43]\n\t" "s_add_u32 m0, %18, 0x6500\n\t" "s_nop 0\n\t" "global_load_lds_dwordx4 v225, s[42:43]\n\t"
                     "s_add_u32 m0, %18, 0x6900\n\t" "s_nop 0\n\t" "global_load_lds_dwordx4 v226, s[42:43]\n\t" "s_sub_u32 s42, 0x558000, %21\n\t" "s_add_u32 s42, s46, s42\n\t" "s_addc_u32 s43, s47, 0\n\t"
                     "s_add_u32 m0, %18, 0x6d20\n\t" "s_nop 0\n\t" "global_load_lds_dwordx4 v224, s[42:43]\n\t" "s_add_u32 m0, %18, 0x7120\n\t" "s_nop 0\n\t" "global_load_lds_dwordx4 v225, s[42:43]\n\t"
                     "s_add_u32 m0, %18, 0x7520\n\t" "s_nop 0\n\t" "global_load_lds_dwordx4 v226, s[42:43]\n\t" "v_add_u32 v232, 0xc200, v229\n\t" "ds_read_b64_tr_b16 v[24:25], v232\n\t"
                     "ds_read_b64_tr_b16 v[26:27], v232 offset:768\n\t" "ds_read_b64_tr_b16 v[28:29], v232 offset:12416\n\t" "ds_read_b64_tr_b16 v[30:31], v232 offset:13184\n\t"
                     "ds_read_b64_tr_b16 a[104:105], v232 offset:128\n\t" "ds_read_b64_tr_b16 a[106:107], v232 offset:896\n\t" "ds_read_b64_tr_b16 a[108:109], v232 offset:12544\n\t"
                     "ds_read_b64_tr_b16 a[110:111], v232 offset:13312\n\t" "ds_read_b64_tr_b16 a[184:185], v232 offset:256\n\t" "ds_read_b64_tr_b16 a[186:187], v232 offset:1024\n\t"
                     "ds_read_b64_tr_b16 a[188:189], v232 offset:12672\n\t" "ds_read_b64_tr_b16 a[190:191], v232 offset:13440\n\t" "ds_read_b64_tr_b16 v[56:57], v232 offset:24832\n\t"
                     "ds_read_b64_tr_b16 v[58:59], v232 offset:25600\n\t" "ds_read_b64_tr_b16 v[60:61], v232 offset:37248\n\t" "ds_read_b64_tr_b16 v[62:63], v232 offset:38016\n\t"
                     "ds_read_b64_tr_b16 a[120:121], v232 offset:24960\n\t" "ds_read_b64_tr_b16 a[122:123], v232 offset:25728\n\t" "ds_read_b64_tr_b16 a[124:125], v232 offset:37376\n\t"
                     "ds_read_b64_tr_b16 a[126:127], v232 offset:38144\n\t" "ds_read_b64_tr_b16 a[200:201], v232 offset:25088\n\t" "ds_read_b64_tr_b16 a[202:203], v232 offset:25856\n\t"
                     "ds_read_b64_tr_b16 a[204:205], v232 offset:37504\n\t" "ds_read_b64_tr_b16 a[206:207], v232 offset:38272\n\t" "s_waitcnt vmcnt(12) lgkmcnt(0)\n\t" "s_barrier\n\t" "s_add_u32 s42, %21, 0x600000\n\t"
                     "s_add_u32 s42, s46, s42\n\t" "s_addc_u32 s43, s47, 0\n\t" "s_add_u32 m0, %18, 0xc200\n\t" "s_nop 0\n\t" "global_load_lds_dwordx4 v224, s[42:43]\n\t" "s_add_u32 m0, %18, 0xc600\n\t" "s_nop 0\n\t"
                     "global_load_lds_dwordx4 v225, s[42:43]\n\t" "s_add_u32 m0, %18, 0xca00\n\t" "s_nop 0\n\t" "global_load_lds_dwordx4 v226, s[42:43]\n\t" "s_add_u32 s42, %21, 0x618000\n\t" "s_add_u32 s42, s46, s42\n\t"
                     "s_addc_u32 s43, s47, 0\n\t" "s_add_u32 m0, %18, 0xce20\n\t" "s_nop 0\n\t" "global_load_lds_dwordx4 v224, s[42:43]\n\t" "s_add_u32 m0, %18, 0xd220\n\t" "s_nop 0\n\t"
                     "global_load_lds_dwordx4 v225, s[42:43]\n\t" "s_add_u32 m0, %18, 0xd620\n\t" "s_nop 0\n\t" "global_load_lds_dwordx4 v226, s[42:43]\n\t" "s_sub_u32 s42, 0x6c0000, %21\n\t" "s_add_u32 s42, s46, s42\n\t"
                     "s_addc_u32 s43, s47, 0\n\t" "s_add_u32 m0, %18, 0x12300\n\t" "s_nop 0\n\t" "global_load_lds_dwordx4 v224, s[42:43]\n\t" "s_add_u32 m0, %18, 0x12700\n\t" "s_nop 0\n\t"
                     "global_load_lds_dwordx4 v225, s[42:43]\n\t" "s_add_u32 m0, %18, 0x12b00\n\t" "s_nop 0\n\t" "global_load_lds_dwordx4 v226, s[42:43]\n\t" "s_sub_u32 s42, 0x6d8000, %21\n\t" "s_add_u32 s42, s46, s42\n\t"
                     "s_addc_u32 s43, s47, 0\n\t" "s_add_u32 m0, %18, 0x12f20\n\t" "s_nop 0\n\t" "global_load_lds_dwordx4 v224, s[42:43]\n\t" "s_add_u32 m0, %18, 0x13320\n\t" "s_nop 0\n\t"
                     "global_load_lds_dwordx4 v225, s[42:43]\n\t" "s_add_u32 m0, %18, 0x13720\n\t" "s_nop 0\n\t" "global_load_lds_dwordx4 v226, s[42:43]\n\t" "v_add_u32 v232, 0x18400, v229\n\t"
                     "ds_read_b64_tr_b16 v[72:73], v232\n\t" "ds_read_b64_tr_b16 v[74:75], v232 offset:768\n\t" "ds_read_b64_tr_b16 v[76:77], v232 offset:12416\n\t" "ds_read_b64_tr_b16 v[78:79], v232 offset:13184\n\t"
                     "ds_read_b64_tr_b16 a[128:129], v232 offset:128\n\t" "ds_read_b64_tr_b16 a[130:131], v232 offset:896\n\t" "ds_read_b64_tr_b16 a[132:133], v232 offset:12544\n\t"
                     "ds_read_b64_tr_b16 a[134:135], v232 offset:13312\n\t" "ds_read_b64_tr_b16 a[208:209], v232 offset:256\n\t" "ds_read_b64_tr_b16 a[210:211], v232 offset:1024\n\t"
                     "ds_read_b64_tr_b16 a[212:213], v232 offset:12672\n\t" "ds_read_b64_tr_b16 a[214:215], v232 offset:13440\n\t" "ds_read_b64_tr_b16 v[88:89], v232 offset:24832\n\t"
                     "ds_read_b64_tr_b16 v[90:91], v232 offset:25600\n\t" "ds_read_b64_tr_b16 v[92:93], v232 offset:37248\n\t" "ds_read_b64_tr_b16 v[94:95], v232 offset:38016\n\t"
                     "ds_read_b64_tr_b16 a[136:137], v232 offset:24960\n\t" "ds_read_b64_tr_b16 a[138:139], v232 offset:25728\n\t" "ds_read_b64_tr_b16 a[140:141], v232 offset:37376\n\t"
                     "ds_read_b64_tr_b16 a[142:143], v232 offset:38144\n\t" "ds_read_b64_tr_b16 a[216:217], v232 offset:25088\n\t" "ds_read_b64_tr_b16 a[218:219], v232 offset:25856\n\t"
                     "ds_read_b64_tr_b16 a[220:221], v232 offset:37504\n\t" "ds_read_b64_tr_b16 a[222:223], v232 offset:38272\n\t" "s_waitcnt vmcnt(12) lgkmcnt(0)\n\t" "s_barrier\n\t" "s_add_u32 s42, %21, 0x0\n\t"
                     "s_add_u32 s42, s44, s42\n\t" "s_addc_u32 s43, s45, 0\n\t" "s_add_u32 m0, %18, 0x18400\n\t" "s_nop 0\n\t" "global_load_lds_dwordx4 v224, s[42:43]\n\t" "s_add_u32 m0, %18, 0x18800\n\t" "s_nop 0\n\t"
                     "global_load_lds_dwordx4 v225, s[42:43]\n\t" "s_add_u32 m0, %18, 0x18c00\n\t" "s_nop 0\n\t" "global_load_lds_dwordx4 v226, s[42:43]\n\t" "s_add_u32 s42, %21, 0x18000\n\t" "s_add_u32 s42, s44, s42\n\t"
                     "s_addc_u32 s43, s45, 0\n\t" "s_add_u32 m0, %18, 0x19020\n\t" "s_nop 0\n\t" "global_load_lds_dwordx4 v224, s[42:43]\n\t" "s_add_u32 m0, %18, 0x19420\n\t" "s_nop 0\n\t"
                     "global_load_lds_dwordx4 v225, s[42:43]\n\t" "s_add_u32 m0, %18, 0x19820\n\t" "s_nop 0\n\t" "global_load_lds_dwordx4 v226, s[42:43]\n\t" "s_sub_u32 s42, 0xc0000, %21\n\t" "s_add_u32 s42, s44, s42\n\t"
                     "s_addc_u32 s43, s45, 0\n\t" "s_add_u32 m0, %18, 0x1e500\n\t" "s_nop 0\n\t" "global_load_lds_dwordx4 v224, s[42:43]\n\t" "s_add_u32 m0, %18, 0x1e900\n\t" "s_nop 0\n\t"
                     "global_load_lds_dwordx4 v225, s[42:43]\n\t" "s_add_u32 m0, %18, 0x1ed00\n\t" "s_nop 0\n\t" "global_load_lds_dwordx4 v226, s[42:43]\n\t" "s_sub_u32 s42, 0xd8000, %21\n\t" "s_add_u32 s42, s44, s42\n\t"
                     "s_addc_u32 s43, s45, 0\n\t" "s_add_u32 m0, %18, 0x1f120\n\t" "s_nop 0\n\t" "global_load_lds_dwordx4 v224, s[42:43]\n\t" "s_add_u32 m0, %18, 0x1f520\n\t" "s_nop 0\n\t"
                     "global_load_lds_dwordx4 v225, s[42:43]\n\t" "s_add_u32 m0, %18, 0x1f920\n\t" "s_nop 0\n\t" "global_load_lds_dwordx4 v226, s[42:43]\n\t" "v_add_u32 v232, 0x0, v229\n\t"
                     "ds_read_b64_tr_b16 v[104:105], v232\n\t" "ds_read_b64_tr_b16 v[106:107], v232 offset:768\n\t" "ds_read_b64_tr_b16 v[108:109], v232 offset:12416\n\t"
                     "ds_read_b64_tr_b16 v[110:111], v232 offset:13184\n\t" "ds_read_b64_tr_b16 a[144:145], v232 offset:128\n\t" "ds_read_b64_tr_b16 a[146:147], v232 offset:896\n\t"
                     "ds_read_b64_tr_b16 a[148:149], v232 offset:12544\n\t" "ds_read_b64_tr_b16 a[150:151], v232 offset:13312\n\t" "ds_read_b64_tr_b16 a[224:225], v232 offset:256\n\t"
                     "ds_read_b64_tr_b16 a[226:227], v232 offset:1024\n\t" "ds_read_b64_tr_b16 a[228:229], v232 offset:12672\n\t" "ds_read_b64_tr_b16 a[230:231], v232 offset:13440\n\t"
                     "ds_read_b64_tr_b16 v[120:121], v232 offset:24832\n\t" "ds_read_b64_tr_b16 v[122:123], v232 offset:25600\n\t" "ds_read_b64_tr_b16 v[124:125], v232 offset:37248\n\t"
                     "ds_read_b64_tr_b16 v[126:127], v232 offset:38016\n\t" "ds_read_b64_tr_b16 a[152:153], v232 offset:24960\n\t" "ds_read_b64_tr_b16 a[154:155], v232 offset:25728\n\t"
                     "ds_read_b64_tr_b16 a[156:157], v232 offset:37376\n\t" "ds_read_b64_tr_b16 a[158:159], v232 offset:38144\n\t" "ds_read_b64_tr_b16 a[232:233], v232 offset:25088\n\t"
                     "ds_read_b64_tr_b16 a[234:235], v232 offset:25856\n\t" "ds_read_b64_tr_b16 a[236:237], v232 offset:37504\n\t" "ds_read_b64_tr_b16 a[238:239], v232 offset:38272\n\t" "s_waitcnt vmcnt(12) lgkmcnt(0)\n\t"
                     "s_barrier\n\t" "s_add_u32 s42, %21, 0x180000\n\t" "s_add_u32 s42, s44, s42\n\t" "s_addc_u32 s43, s45, 0\n\t" "s_add_u32 m0, %18, 0x0\n\t" "s_nop 0\n\t" "global_load_lds_dwordx4 v224, s[42:43]\n\t"
                     "s_add_u32 m0, %18, 0x400\n\t" "s_nop 0\n\t" "global_load_lds_dwordx4 v225, s[42:43]\n\t" "s_add_u32 m0, %18, 0x800\n\t" "s_nop 0\n\t" "global_load_lds_dwordx4 v226, s[42:43]\n\t"
                     "s_add_u32 s42, %21, 0x198000\n\t" "s_add_u32 s42, s44, s42\n\t" "s_addc_u32 s43, s45, 0\n\t" "s_add_u32 m0, %18, 0xc20\n\t" "s_nop 0\n\t" "global_load_lds_dwordx4 v224, s[42:43]\n\t"
                     "s_add_u32 m0, %18, 0x1020\n\t" "s_nop 0\n\t" "global_load_lds_dwordx4 v225, s[42:43]\n\t" "s_add_u32 m0, %18, 0x1420\n\t" "s_nop 0\n\t" "global_load_lds_dwordx4 v226, s[42:43]\n\t"
                     "s_sub_u32 s42, 0x240000, %21\n\t" "s_add_u32 s42, s44, s42\n\t" "s_addc_u32 s43, s45, 0\n\t" "s_add_u32 m0, %18, 0x6100\n\t" "s_nop 0\n\t" "global_load_lds_dwordx4 v224, s[42:43]\n\t"
                     "s_add_u32 m0, %18, 0x6500\n\t" "s_nop 0\n\t" "global_load_lds_dwordx4 v225, s[42:43]\n\t" "s_add_u32 m0, %18, 0x6900\n\t" "s_nop 0\n\t" "global_load_lds_dwordx4 v226, s[42:43]\n\t"
                     "s_sub_u32 s42, 0x258000, %21\n\t" "s_add_u32 s42, s44, s42\n\t" "s_addc_u32 s43, s45, 0\n\t" "s_add_u32 m0, %18, 0x6d20\n\t" "s_nop 0\n\t" "global_load_lds_dwordx4 v224, s[42:43]\n\t"
                     "s_add_u32 m0, %18, 0x7120\n\t" "s_nop 0\n\t" "global_load_lds_dwordx4 v225, s[42:43]\n\t" "s_add_u32 m0, %18, 0x7520\n\t" "s_nop 0\n\t" "global_load_lds_dwordx4 v226, s[42:43]\n\t"
                     "v_add_u32 v232, 0xc200, v229\n\t" "ds_read_b64_tr_b16 a[56:57], v232\n\t" "ds_read_b64_tr_b16 a[58:59], v232 offset:768\n\t" "ds_read_b64_tr_b16 a[60:61], v232 offset:12416\n\t"
                     "ds_read_b64_tr_b16 a[62:63], v232 offset:13184\n\t" "ds_read_b64_tr_b16 a[160:161], v232 offset:128\n\t" "ds_read_b64_tr_b16 a[162:163], v232 offset:896\n\t"
                     "ds_read_b64_tr_b16 a[164:165], v232 offset:12544\n\t" "ds_read_b64_tr_b16 a[166:167], v232 offset:13312\n\t" "ds_read_b64_tr_b16 a[240:241], v232 offset:256\n\t"
                     "ds_read_b64_tr_b16 a[242:243], v232 offset:1024\n\t" "ds_read_b64_tr_b16 a[244:245], v232 offset:12672\n\t" "ds_read_b64_tr_b16 a[246:247], v232 offset:13440\n\t"
                     "ds_read_b64_tr_b16 a[88:89], v232 offset:24832\n\t" "ds_read_b64_tr_b16 a[90:91], v232 offset:25600\n\t" "ds_read_b64_tr_b16 a[92:93], v232 offset:37248\n\t"
                     "ds_read_b64_tr_b16 a[94:95], v232 offset:38016\n\t" "ds_read_b64_tr_b16 a[168:169], v232 offset:24960\n\t" "ds_read_b64_tr_b16 a[170:171], v232 offset:25728\n\t"
                     "ds_read_b64_tr_b16 a[172:173], v232 offset:37376\n\t" "ds_read_b64_tr_b16 a[174:175], v232 offset:38144\n\t" "ds_read_b64_tr_b16 a[248:249], v232 offset:25088\n\t"
                     "ds_read_b64_tr_b16 a[250:251], v232 offset:25856\n\t" "ds_read_b64_tr_b16 a[252:253], v232 offset:37504\n\t" "ds_read_b64_tr_b16 a[254:255], v232 offset:38272\n\t" "s_waitcnt vmcnt(12) lgkmcnt(0)\n\t"
                     "s_barrier\n\t" "s_add_u32 s42, %21, 0x300000\n\t" "s_add_u32 s42, s44, s42\n\t" "s_addc_u32 s43, s45, 0\n\t" "s_add_u32 m0, %18, 0xc200\n\t" "s_nop 0\n\t" "global_load_lds_dwordx4 v224, s[42:43]\n\t"
                     "s_add_u32 m0, %18, 0xc600\n\t" "s_nop 0\n\t" "global_load_lds_dwordx4 v225, s[42:43]\n\t" "s_add_u32 m0, %18, 0xca00\n\t" "s_nop 0\n\t" "global_load_lds_dwordx4 v226, s[42:43]\n\t"
                     "s_add_u32 s42, %21, 0x318000\n\t" "s_add_u32 s42, s44, s42\n\t" "s_addc_u32 s43, s45, 0\n\t" "s_add_u32 m0, %18, 0xce20\n\t" "s_nop 0\n\t" "global_load_lds_dwordx4 v224, s[42:43]\n\t"
                     "s_add_u32 m0, %18, 0xd220\n\t" "s_nop 0\n\t" "global_load_lds_dwordx4 v225, s[42:43]\n\t" "s_add_u32 m0, %18, 0xd620\n\t" "s_nop 0\n\t" "global_load_lds_dwordx4 v226, s[42:43]\n\t"
                     "s_sub_u32 s42, 0x3c0000, %21\n\t" "s_add_u32 s42, s44, s42\n\t" "s_addc_u32 s43, s45, 0\n\t" "s_add_u32 m0, %18, 0x12300\n\t" "s_nop 0\n\t" "global_load_lds_dwordx4 v224, s[42:43]\n\t"
                     "s_add_u32 m0, %18, 0x12700\n\t" "s_nop 0\n\t" "global_load_lds_dwordx4 v225, s[42:43]\n\t" "s_add_u32 m0, %18, 0x12b00\n\t" "s_nop 0\n\t" "global_load_lds_dwordx4 v226, s[42:43]\n\t"
                     "s_sub_u32 s42, 0x3d8000, %21\n\t" "s_add_u32 s42, s44, s42\n\t" "s_addc_u32 s43, s45, 0\n\t" "s_add_u32 m0, %18, 0x12f20\n\t" "s_nop 0\n\t" "global_load_lds_dwordx4 v224, s[42:43]\n\t"
                     "s_add_u32 m0, %18, 0x13320\n\t" "s_nop 0\n\t" "global_load_lds_dwordx4 v225, s[42:43]\n\t" "s_add_u32 m0, %18, 0x13720\n\t" "s_nop 0\n\t" "global_load_lds_dwordx4 v226, s[42:43]\n\t"
                     "v_add_u32 v232, 0x18400, v227\n\t" "v_add_u32 v233, 0x18400, v228\n\t" "ds_read_b64_tr_b16 v[128:129], v232\n\t" "ds_read_b64_tr_b16 v[130:131], v232 offset:768\n\t"
                     "ds_read_b64_tr_b16 v[132:133], v232 offset:12416\n\t" "ds_read_b64_tr_b16 v[134:135], v232 offset:13184\n\t" "ds_read_b64_tr_b16 v[0:1], v232 offset:32\n\t"
                     "ds_read_b64_tr_b16 v[2:3], v232 offset:800\n\t" "ds_read_b64_tr_b16 v[4:5], v232 offset:12448\n\t" "ds_read_b64_tr_b16 v[6:7], v232 offset:13216\n\t"
                     "ds_read_b64_tr_b16 v[136:137], v233 offset:64\n\t" "ds_read_b64_tr_b16 v[138:139], v233 offset:832\n\t" "ds_read_b64_tr_b16 v[140:141], v233 offset:12480\n\t"
                     "ds_read_b64_tr_b16 v[142:143], v233 offset:13248\n\t" "ds_read_b64_tr_b16 v[160:161], v232 offset:24832\n\t" "ds_read_b64_tr_b16 v[162:163], v232 offset:25600\n\t"
                     "ds_read_b64_tr_b16 v[164:165], v232 offset:37248\n\t" "ds_read_b64_tr_b16 v[166:167], v232 offset:38016\n\t" "ds_read_b64_tr_b16 v[32:33], v232 offset:24864\n\t"
                     "ds_read_b64_tr_b16 v[34:35], v232 offset:25632\n\t" "ds_read_b64_tr_b16 v[36:37], v232 offset:37280\n\t" "ds_read_b64_tr_b16 v[38:39], v232 offset:38048\n\t"
                     "ds_read_b64_tr_b16 v[168:169], v233 offset:24896\n\t" "ds_read_b64_tr_b16 v[170:171], v233 offset:25664\n\t" "ds_read_b64_tr_b16 v[172:173], v233 offset:37312\n\t"
                     "ds_read_b64_tr_b16 v[174:175], v233 offset:38080\n\t" "s_waitcnt vmcnt(12) lgkmcnt(0)\n\t" "s_barrier\n\t" "v_mov_b32 %14, 0\n\t" "global_load_dwordx2 %11, v231, %19 sc1\n\t"
                     "global_load_dwordx2 %12, %14, %19 offset:512 sc1\n\t" "global_load_dwordx2 %13, %14, %19 offset:1024 sc1\n\t" "s_add_u32 s42, %21, 0x480000\n\t" "s_add_u32 s42, s44, s42\n\t"
                     "s_addc_u32 s43, s45, 0\n\t" "s_add_u32 m0, %18, 0x18400\n\t" "s_nop 0\n\t" "global_load_lds_dwordx4 v224, s[42:43]\n\t" "s_add_u32 m0, %18, 0x18800\n\t" "s_nop 0\n\t"
                     "global_load_lds_dwordx4 v225, s[42:43]\n\t" "s_add_u32 m0, %18, 0x18c00\n\t" "s_nop 0\n\t" "global_load_lds_dwordx4 v226, s[42:43]\n\t" "s_add_u32 s42, %21, 0x498000\n\t" "s_add_u32 s42, s44, s42\n\t"
                     "s_addc_u32 s43, s45, 0\n\t" "s_add_u32 m0, %18, 0x19020\n\t" "s_nop 0\n\t" "global_load_lds_dwordx4 v224, s[42:43]\n\t" "s_add_u32 m0, %18, 0x19420\n\t" "s_nop 0\n\t"
                     "global_load_lds_dwordx4 v225, s[42:43]\n\t" "s_add_u32 m0, %18, 0x19820\n\t" "s_nop 0\n\t" "global_load_lds_dwordx4 v226, s[42:43]\n\t" "s_sub_u32 s42, 0x540000, %21\n\t" "s_add_u32 s42, s44, s42\n\t"
                     "s_addc_u32 s43, s45, 0\n\t" "s_add_u32 m0, %18, 0x1e500\n\t" "s_nop 0\n\t" "global_load_lds_dwordx4 v224, s[42:43]\n\t" "s_add_u32 m0, %18, 0x1e900\n\t" "s_nop 0\n\t"
                     "global_load_lds_dwordx4 v225, s[42:43]\n\t" "s_add_u32 m0, %18, 0x1ed00\n\t" "s_nop 0\n\t" "global_load_lds_dwordx4 v226, s[42:43]\n\t" "s_sub_u32 s42, 0x558000, %21\n\t" "s_add_u32 s42, s44, s42\n\t"
                     "s_addc_u32 s43, s45, 0\n\t" "s_add_u32 m0, %18, 0x1f120\n\t" "s_nop 0\n\t" "global_load_lds_dwordx4 v224, s[42:43]\n\t" "s_add_u32 m0, %18, 0x1f520\n\t" "s_nop 0\n\t"
                     "global_load_lds_dwordx4 v225, s[42:43]\n\t" "s_add_u32 m0, %18, 0x1f920\n\t" "s_nop 0\n\t" "global_load_lds_dwordx4 v226, s[42:43]\n\t" "v_add_u32 v232, 0x0, v227\n\t" "v_add_u32 v233, 0x0, v228\n\t"
                     "ds_read_b64_tr_b16 v[144:145], v232\n\t" "ds_read_b64_tr_b16 v[146:147], v232 offset:768\n\t" "ds_read_b64_tr_b16 v[148:149], v232 offset:12416\n\t"
                     "ds_read_b64_tr_b16 v[150:151], v232 offset:13184\n\t" "ds_read_b64_tr_b16 v[16:17], v232 offset:32\n\t" "ds_read_b64_tr_b16 v[18:19], v232 offset:800\n\t"
                     "ds_read_b64_tr_b16 v[20:21], v232 offset:12448\n\t" "ds_read_b64_tr_b16 v[22:23], v232 offset:13216\n\t" "ds_read_b64_tr_b16 v[152:153], v233 offset:64\n\t"
                     "ds_read_b64_tr_b16 v[154:155], v233 offset:832\n\t" "ds_read_b64_tr_b16 v[156:157], v233 offset:12480\n\t" "ds_read_b64_tr_b16 v[158:159], v233 offset:13248\n\t"
                     "ds_read_b64_tr_b16 v[176:177], v232 offset:24832\n\t" "ds_read_b64_tr_b16 v[178:179], v232 offset:25600\n\t" "ds_read_b64_tr_b16 v[180:181], v232 offset:37248\n\t"
                     "ds_read_b64_tr_b16 v[182:183], v232 offset:38016\n\t" "ds_read_b64_tr_b16 v[48:49], v232 offset:24864\n\t" "ds_read_b64_tr_b16 v[50:51], v232 offset:25632\n\t"
                     "ds_read_b64_tr_b16 v[52:53], v232 offset:37280\n\t" "ds_read_b64_tr_b16 v[54:55], v232 offset:38048\n\t" "ds_read_b64_tr_b16 v[184:185], v233 offset:24896\n\t"
                     "ds_read_b64_tr_b16 v[186:187], v233 offset:25664\n\t" "ds_read_b64_tr_b16 v[188:189], v233 offset:37312\n\t" "ds_read_b64_tr_b16 v[190:191], v233 offset:38080\n\t" "s_waitcnt vmcnt(12) lgkmcnt(0)\n\t"
                     "s_barrier\n\t" "s_add_u32 s42, %21, 0x600000\n\t" "s_add_u32 s42, s44, s42\n\t" "s_addc_u32 s43, s45, 0\n\t" "s_add_u32 m0, %18, 0x0\n\t" "s_nop 0\n\t" "global_load_lds_dwordx4 v224, s[42:43]\n\t"
                     "s_add_u32 m0, %18, 0x400\n\t" "s_nop 0\n\t" "global_load_lds_dwordx4 v225, s[42:43]\n\t" "s_add_u32 m0, %18, 0x800\n\t" "s_nop 0\n\t" "global_load_lds_dwordx4 v226, s[42:43]\n\t"
                     "s_add_u32 s42, %21, 0x618000\n\t" "s_add_u32 s42, s44, s42\n\t" "s_addc_u32 s43, s45, 0\n\t" "s_add_u32 m0, %18, 0xc20\n\t" "s_nop 0\n\t" "global_load_lds_dwordx4 v224, s[42:43]\n\t"
                     "s_add_u32 m0, %18, 0x1020\n\t" "s_nop 0\n\t" "global_load_lds_dwordx4 v225, s[42:43]\n\t" "s_add_u32 m0, %18, 0x1420\n\t" "s_nop 0\n\t" "global_load_lds_dwordx4 v226, s[42:43]\n\t"
                     "s_sub_u32 s42, 0x6c0000, %21\n\t" "s_add_u32 s42, s44, s42\n\t" "s_addc_u32 s43, s45, 0\n\t" "s_add_u32 m0, %18, 0x6100\n\t" "s_nop 0\n\t" "global_load_lds_dwordx4 v224, s[42:43]\n\t"
                     "s_add_u32 m0, %18, 0x6500\n\t" "s_nop 0\n\t" "global_load_lds_dwordx4 v225, s[42:43]\n\t" "s_add_u32 m0, %18, 0x6900\n\t" "s_nop 0\n\t" "global_load_lds_dwordx4 v226, s[42:43]\n\t"
                     "s_sub_u32 s42, 0x6d8000, %21\n\t" "s_add_u32 s42, s44, s42\n\t" "s_addc_u32 s43, s45, 0\n\t" "s_add_u32 m0, %18, 0x6d20\n\t" "s_nop 0\n\t" "global_load_lds_dwordx4 v224, s[42:43]\n\t"
                     "s_add_u32 m0, %18, 0x7120\n\t" "s_nop 0\n\t" "global_load_lds_dwordx4 v225, s[42:43]\n\t" "s_add_u32 m0, %18, 0x7520\n\t" "s_nop 0\n\t" "global_load_lds_dwordx4 v226, s[42:43]\n\t"
                     "global_load_dword %14, %14, %20 sc1\n\t" "v_add_u32 v232, 0xc200, v227\n\t" "v_add_u32 v233, 0xc200, v228\n\t" "ds_read_b64_tr_b16 v[192:193], v232\n\t"
                     "ds_read_b64_tr_b16 v[194:195], v232 offset:768\n\t" "ds_read_b64_tr_b16 v[196:197], v232 offset:12416\n\t" "ds_read_b64_tr_b16 v[198:199], v232 offset:13184\n\t"
                     "ds_read_b64_tr_b16 v[64:65], v232 offset:32\n\t" "ds_read_b64_tr_b16 v[66:67], v232 offset:800\n\t" "ds_read_b64_tr_b16 v[68:69], v232 offset:12448\n\t"
                     "ds_read_b64_tr_b16 v[70:71], v232 offset:13216\n\t" "ds_read_b64_tr_b16 v[200:201], v233 offset:64\n\t" "ds_read_b64_tr_b16 v[202:203], v233 offset:832\n\t"
                     "ds_read_b64_tr_b16 v[204:205], v233 offset:12480\n\t" "ds_read_b64_tr_b16 v[206:207], v233 offset:13248\n\t" "ds_read_b64_tr_b16 v[208:209], v232 offset:24832\n\t"
                     "ds_read_b64_tr_b16 v[210:211], v232 offset:25600\n\t" "ds_read_b64_tr_b16 v[212:213], v232 offset:37248\n\t" "ds_read_b64_tr_b16 v[214:215], v232 offset:38016\n\t"
                     "ds_read_b64_tr_b16 v[80:81], v232 offset:24864\n\t" "ds_read_b64_tr_b16 v[82:83], v232 offset:25632\n\t" "ds_read_b64_tr_b16 v[84:85], v232 offset:37280\n\t"
                     "ds_read_b64_tr_b16 v[86:87], v232 offset:38048\n\t" "ds_read_b64_tr_b16 v[216:217], v233 offset:24896\n\t" "ds_read_b64_tr_b16 v[218:219], v233 offset:25664\n\t"
                     "ds_read_b64_tr_b16 v[220:221], v233 offset:37312\n\t" "ds_read_b64_tr_b16 v[222:223], v233 offset:38080\n\t" "s_waitcnt vmcnt(13) lgkmcnt(0)\n\t" "s_barrier\n\t" "v_add_u32 v232, 0x18400, v227\n\t"
                     "v_add_u32 v233, 0x18400, v228\n\t" "ds_read_b64_tr_b16 a[0:1], v232\n\t" "ds_read_b64_tr_b16 a[2:3], v232 offset:768\n\t" "ds_read_b64_tr_b16 a[4:5], v232 offset:12416\n\t"
                     "ds_read_b64_tr_b16 a[6:7], v232 offset:13184\n\t" "ds_read_b64_tr_b16 v[96:97], v232 offset:32\n\t" "ds_read_b64_tr_b16 v[98:99], v232 offset:800\n\t"
                     "ds_read_b64_tr_b16 v[100:101], v232 offset:12448\n\t" "ds_read_b64_tr_b16 v[102:103], v232 offset:13216\n\t" "ds_read_b64_tr_b16 a[8:9], v233 offset:64\n\t"
                     "ds_read_b64_tr_b16 a[10:11], v233 offset:832\n\t" "ds_read_b64_tr_b16 a[12:13], v233 offset:12480\n\t" "ds_read_b64_tr_b16 a[14:15], v233 offset:13248\n\t"
                     "ds_read_b64_tr_b16 a[16:17], v232 offset:24832\n\t" "ds_read_b64_tr_b16 a[18:19], v232 offset:25600\n\t" "ds_read_b64_tr_b16 a[20:21], v232 offset:37248\n\t"
                     "ds_read_b64_tr_b16 a[22:23], v232 offset:38016\n\t" "ds_read_b64_tr_b16 v[112:113], v232 offset:24864\n\t" "ds_read_b64_tr_b16 v[114:115], v232 offset:25632\n\t"
                     "ds_read_b64_tr_b16 v[116:117], v232 offset:37280\n\t" "ds_read_b64_tr_b16 v[118:119], v232 offset:38048\n\t" "ds_read_b64_tr_b16 a[24:25], v233 offset:24896\n\t"
                     "ds_read_b64_tr_b16 a[26:27], v233 offset:25664\n\t" "ds_read_b64_tr_b16 a[28:29], v233 offset:37312\n\t" "ds_read_b64_tr_b16 a[30:31], v233 offset:38080\n\t" "s_waitcnt vmcnt(0) lgkmcnt(0)\n\t"
                     "s_barrier\n\t" "v_add_u32 v232, 0x0, v227\n\t" "v_add_u32 v233, 0x0, v228\n\t" "ds_read_b64_tr_b16 a[32:33], v232\n\t" "ds_read_b64_tr_b16 a[34:35], v232 offset:768\n\t"
                     "ds_read_b64_tr_b16 a[36:37], v232 offset:12416\n\t" "ds_read_b64_tr_b16 a[38:39], v232 offset:13184\n\t" "ds_read_b64_tr_b16 a[40:41], v232 offset:32\n\t"
                     "ds_read_b64_tr_b16 a[42:43], v232 offset:800\n\t" "ds_read_b64_tr_b16 a[44:45], v232 offset:12448\n\t" "ds_read_b64_tr_b16 a[46:47], v232 offset:13216\n\t"
                     "ds_read_b64_tr_b16 a[48:49], v233 offset:64\n\t" "ds_read_b64_tr_b16 a[50:51], v233 offset:832\n\t" "ds_read_b64_tr_b16 a[52:53], v233 offset:12480\n\t"
                     "ds_read_b64_tr_b16 a[54:55], v233 offset:13248\n\t" "ds_read_b64_tr_b16 a[64:65], v232 offset:24832\n\t" "ds_read_b64_tr_b16 a[66:67], v232 offset:25600\n\t"
                     "ds_read_b64_tr_b16 a[68:69], v232 offset:37248\n\t" "ds_read_b64_tr_b16 a[70:71], v232 offset:38016\n\t" "ds_read_b64_tr_b16 a[72:73], v232 offset:24864\n\t"
                     "ds_read_b64_tr_b16 a[74:75], v232 offset:25632\n\t" "ds_read_b64_tr_b16 a[76:77], v232 offset:37280\n\t" "ds_read_b64_tr_b16 a[78:79], v232 offset:38048\n\t"
                     "ds_read_b64_tr_b16 a[80:81], v233 offset:24896\n\t" "ds_read_b64_tr_b16 a[82:83], v233 offset:25664\n\t" "ds_read_b64_tr_b16 a[84:85], v233 offset:37312\n\t"
                     "ds_read_b64_tr_b16 a[86:87], v233 offset:38080\n\t" "s_waitcnt lgkmcnt(0)\n\t" "s_barrier\n\t" "ds_write_b128 v230, v[0:3]\n\t" "ds_write_b128 v230, v[4:7] offset:1024\n\t"
                     "ds_write_b128 v230, v[8:11] offset:2048\n\t" "ds_write_b128 v230, v[12:15] offset:3072\n\t" "ds_write_b128 v230, v[16:19] offset:4096\n\t" "ds_write_b128 v230, v[20:23] offset:5120\n\t"
                     "ds_write_b128 v230, v[24:27] offset:6144\n\t" "ds_write_b128 v230, v[28:31] offset:7168\n\t" "ds_write_b128 v230, v[32:35] offset:8192\n\t" "ds_write_b128 v230, v[36:39] offset:9216\n\t"
                     "ds_write_b128 v230, v[40:43] offset:10240\n\t" "ds_write_b128 v230, v[44:47] offset:11264\n\t" "ds_write_b128 v230, v[48:51] offset:12288\n\t" "ds_write_b128 v230, v[52:55] offset:13312\n\t"
                     "ds_write_b128 v230, v[56:59] offset:14336\n\t" "ds_write_b128 v230, v[60:63] offset:15360\n\t" "ds_write_b128 v230, v[64:67] offset:16384\n\t" "ds_write_b128 v230, v[68:71] offset:17408\n\t"
                     "ds_write_b128 v230, v[72:75] offset:18432\n\t" "ds_write_b128 v230, v[76:79] offset:19456\n\t" "ds_write_b128 v230, v[80:83] offset:20480\n\t" "ds_write_b128 v230, v[84:87] offset:21504\n\t"
                     "ds_write_b128 v230, v[88:91] offset:22528\n\t" "ds_write_b128 v230, v[92:95] offset:23552\n\t" "ds_write_b128 v230, v[96:99] offset:24576\n\t" "ds_write_b128 v230, v[100:103] offset:25600\n\t"
                     "ds_write_b128 v230, v[104:107] offset:26624\n\t" "ds_write_b128 v230, v[108:111] offset:27648\n\t" "ds_write_b128 v230, v[112:115] offset:28672\n\t" "ds_write_b128 v230, v[116:119] offset:29696\n\t"
                     "ds_write_b128 v230, v[120:123] offset:30720\n\t" "ds_write_b128 v230, v[124:127] offset:31744\n\t"
                     : "=&{a[0:31]}"(wa[0]), "=&{a[32:63]}"(wa[1]), "=&{a[64:95]}"(wa[2]), "=&{a[96:127]}"(wa[3]), "=&{a[128:159]}"(wa[4]), "=&{a[160:191]}"(wa[5]), "=&{a[192:223]}"(wa[6]), "=&{a[224:255]}"(wa[7]), "=&{v[128:159]}"(wv[0]), "=&{v[160:191]}"(wv[1]), "=&{v[192:223]}"(wv[2]), "=&v"(y.r), "=&v"(y.g), "=&v"(y.e), "=&v"(kfl)
                     : "{v[224:231]}"(pa), "{s[44:45]}"(p.wq + h * DH * 2 + (size_t)(16 * w) * (HD * 2)), "{s[46:47]}"(p.wk + h * DH * 2 + (size_t)(16 * w) * (HD * 2)),
                       "s"(lds + 2 * w * GS), "s"(p.ws + OFF_LAT), "s"(fl + (F_K + h) * 128), "s"((h & 4) * 16 * HD * 2)
                     : "v0", "v1", "v2", "v3", "v4", "v5", "v6", "v7", "v8", "v9", "v10", "v11", "v12", "v13", "v14", "v15", "v16", "v17", "v18", "v19", "v20", "v21", "v22", "v23", "v24", "v25", "v26", "v27", "v28", "v29", "v30", "v31", "v32", "v33", "v34", "v35", "v36", "v37", "v38", "v39", "v40", "v41", "v42", "v43", "v44", "v45", "v46", "v47", "v48", "v49", "v50", "v51", "v52", "v53", "v54", "v55", "v56", "v57", "v58", "v59", "v60", "v61", "v62", "v63", "v64", "v65", "v66", "v67", "v68", "v69", "v70", "v71", "v72", "v73", "v74", "v75", "v76", "v77", "v78", "v79", "v80", "v81", "v82", "v83", "v84", "v85", "v86", "v87", "v88", "v89", "v90", "v91", "v92", "v93", "v94", "v95", "v96", "v97", "v98", "v99", "v100", "v101", "v102", "v103", "v104", "v105", "v106", "v107", "v108", "v109", "v110", "v111", "v112", "v113", "v114", "v115", "v116", "v117", "v118", "v119", "v120", "v121", "v122", "v123", "v124", "v125", "v126", "v127", "v232", "v233", "s42", "s43", "scc", "memory");
        const u64 t = sync_tag(y.e);
        const bool rdy = __builtin_amdgcn_ballot_w64((y.r[0] & ~1u) != (u32)t || y.r[1] != (u32)(t >> 32) || y.g[0] != (u32)t) == 0 && uni((int)kfl) != -1;
        if (rdy) kxdma();
        sd = sync_end(y, p.ws, phi, w == 0);
        if (!rdy) { flag_wait(fl, F_K + h, 1); kxdma(); }
        if (w == 3) rdy_add(kfl, fl);
        kxp += NH * 12288;
    }
    const u32 sidx = (lane & 2) ? 0xEEEEu : 0x4444u;
    const u32 moff = lane * 16;
    u32 kxa = kxl - 16 + (n & 1) * 6160 + (16 * g + (n & 8) + 2 * (n & 2) + ((n >> 1) & 2)) * 16;
    const u64 mb0 = 0xffff0000ffff0000ull;
    const int tp = (3 * w + (w & 1)) >> 1, ts = 1 + 3 * (w >> 1);
    u32 qw0 = lds + SL_IMG + tp * 512 + lane * 8, qw1 = lds + SL_IMG + ts * 512 + lane * 8 + 4 * (w & 1);
    const u32 kw0 = lds + SL_IMG + 3072 + w * 288 + n * 16 + 4 * g;
    const u32 qr0 = lds + SL_IMG + (g >> 1) * 512 + w * 128 + 64 * (g & 1) + 8 * qq;
    const u32 kr0 = lds + SL_IMG + 3072 + (qq >> 1) * 288 + g * 64 + (qq & 1) * 16 + 8 * (pp & 1);
    const u32 xso = 32768 + (h >> 4) * 16384 + (h & 15) * 512 + (w >> 1) * 256 + (n >> 3) * 64 + (n & 4) * 32 + (w & 1) * 32 + (g & 1) * 16 + (n & 3) * 4 + (g >> 1) * 8192;
    const u32 pso = (n < 8 ? 20480 + (h >> 1) * 256 + (n & 2) * 16 + (h & 1) * 8 + (n & 4) : 18432 + (h >> 2) * 256 + (h & 1) * 8 + (h & 2) * 2) + w * 64 + (g & 1) * 16 + (g >> 1) * 8192;
    const u64 psm = 0x01aa01aa01aa01aaull;
    const u32 kmo = (u32)(((n >> 3) * 4 + g) * 128 + (n & 7) * 16);
    u32x16 ad = u32x16{moff, sidx, lfa, kxa, qw0, qw1, kw0, kr0 + 1152 * min(w, 2) + (pp >> 1) * 576, qr0, kr0, xso, pso, 0x3dd53b94u, kmo, kmo, kmo};
    asm volatile("" : "+{v[224:239]}"(ad));
    const char* memp = p.ws + mslot(sd, 0);
    char* kmp = p.ws + OFF_KM + h * 3072 + min(w, 2) * 1024;
    const float kscale = 0.07216878364870322f;
    int budget = 30000;
    Clock clk; clk.init();
    if (w == 3) { asm volatile("s_waitcnt vmcnt(0)" : "+v"(kfl) : : "memory"); rdy_end(kfl, fl, cfg.s1 >> 8 & 255); }
    u32x32 wk[3];
    asm volatile("" : "={v[64:95]}"(wk[0]), "={v[96:127]}"(wk[1]), "={v[32:63]}"(wk[2]));
    const int kfo = (F_K + 32 + h) * 128;
    for (int s = 0; s < NSEG; ++s) {
        int tries = 0;
        u64 bad, tp8, en = ~0ull;
        u32 tt;
        int kf;
        while (true) {
            const u32 tgt = s ? clk.sched(phi, 0, cfg.G, tries ? 2 : 0) : mt();
            asm volatile("s_or_b32 s42, %12, %13\n\t" "s_cbranch_scc1 .Lsp_s\n\t" "s_mov_b64 vcc, 0\n\t" ".Lrt_s:\n\t" "s_nop 7\n\t" "ds_read_b128 v[64:67], v226\n\t" "v_mov_b64 v[40:41], 0\n\t"
                         "ds_read_b128 v[68:71], v226 offset:1024\n\t" "v_mov_b64 v[42:43], 0\n\t" "ds_read_b128 v[72:75], v226 offset:2048\n\t" "v_mov_b64 v[44:45], 0\n\t" "ds_read_b128 v[76:79], v226 offset:3072\n\t"
                         "v_mov_b64 v[46:47], 0\n\t" "ds_read_b128 v[80:83], v226 offset:4096\n\t" "v_mov_b64 v[48:49], 0\n\t" "ds_read_b128 v[84:87], v226 offset:5120\n\t" "v_mov_b64 v[50:51], 0\n\t"
                         "ds_read_b128 v[88:91], v226 offset:6144\n\t" "v_mov_b64 v[52:53], 0\n\t" "ds_read_b128 v[92:95], v226 offset:7168\n\t" "v_mov_b64 v[54:55], 0\n\t" "ds_read_b128 v[96:99], v226 offset:8192\n\t"
                         "v_mov_b64 v[56:57], 0\n\t" "ds_read_b128 v[100:103], v226 offset:9216\n\t" "v_mov_b64 v[58:59], 0\n\t" "ds_read_b128 v[104:107], v226 offset:10240\n\t" "v_mov_b64 v[60:61], 0\n\t"
                         "ds_read_b128 v[108:111], v226 offset:11264\n\t" "v_mov_b64 v[62:63], 0\n\t" "ds_read_b128 v[112:115], v226 offset:12288\n\t" "ds_read_b128 v[116:119], v226 offset:13312\n\t"
                         "ds_read_b128 v[120:123], v226 offset:14336\n\t" "ds_read_b128 v[124:127], v226 offset:15360\n\t" "s_cbranch_vccnz .Lsk_s\n\t" ".p2align 5\n\t" "s_nop 0\n\t" "s_nop 0\n\t" "s_nop 0\n\t" "s_nop 0\n\t"
                         "s_nop 0\n\t" ".Lsp_s:\n\t" "s_memtime vcc\n\t" "s_waitcnt lgkmcnt(0)\n\t" "s_sub_i32 s42, %4, vcc_lo\n\t" "s_cmp_gt_i32 s42, 80\n\t" "s_cbranch_scc1 .Lsp_s\n\t" "s_max_i32 s42, s42, 0\n\t"
                         "s_sub_i32 s42, 92, s42\n\t" "s_and_b32 s42, s42, -4\n\t" "s_getpc_b64 vcc\n\t" "s_add_u32 vcc_lo, vcc_lo, s42\n\t" "s_addc_u32 vcc_hi, vcc_hi, 0\n\t" "s_setpc_b64 vcc\n\t" "s_nop 0\n\t" "s_nop 0\n\t"
                         "s_nop 0\n\t" "s_nop 0\n\t" "s_nop 0\n\t" "s_nop 0\n\t" "s_nop 0\n\t" "s_nop 0\n\t" "s_nop 0\n\t" "s_nop 0\n\t" "s_nop 0\n\t" "s_nop 0\n\t" "s_nop 0\n\t" "s_nop 0\n\t" "s_nop 0\n\t" "s_nop 0\n\t"
                         "s_nop 0\n\t" "s_nop 0\n\t" "s_nop 0\n\t" "s_nop 0\n\t" "global_load_dwordx4 v[0:3], v224, %5 offset:-4096 sc1\n\t" "v_add_u32 v238, 0x4000, v224\n\t" "s_nop 9\n\t"
                         "global_load_dwordx4 v[4:7], v224, %5 offset:-2048 sc1\n\t" "s_nop 15\n\t" "global_load_dwordx4 v[8:11], v224, %6 offset:-3072 sc1\n\t" "s_nop 10\n\t"
                         "global_load_dwordx4 v[12:15], v224, %6 offset:-1024 sc1\n\t" "s_nop 10\n\t" "global_load_dwordx4 v[16:19], v224, %5 sc1\n\t" "s_nop 10\n\t" "global_load_dwordx4 v[20:23], v224, %6 offset:1024 sc1\n\t"
                         "s_nop 10\n\t" "global_load_dwordx4 v[24:27], v224, %5 offset:2048 sc1\n\t" "s_nop 10\n\t" "global_load_dwordx4 v[28:31], v224, %6 offset:3072 sc1\n\t" "s_nop 10\n\t"
                         "global_load_dwordx4 v[32:35], v238, %5 offset:-4096 sc1\n\t" "s_nop 10\n\t" "global_load_dwordx4 v[36:39], v238, %6 offset:-3072 sc1\n\t" "s_cmp_lg_u32 %12, 0\n\t" "s_cbranch_scc1 .Lnb_s\n\t"
                         "s_waitcnt vmcnt(10)\n\t" "s_barrier\n\t" ".Lnb_s:\n\t" "s_waitcnt vmcnt(9)\n\t" "v_smfmac_f32_16x16x64_bf16 v[40:43], v[0:3], v[128:135], v225\n\t"
                         "v_smfmac_f32_16x16x64_bf16 v[44:47], v[0:3], v[64:71], v225\n\t" "ds_read_b128 v[64:67], v226 offset:16384\n\t" "v_smfmac_f32_16x16x64_bf16 v[48:51], v[0:3], v[136:143], v225\n\t"
                         "ds_read_b128 v[68:71], v226 offset:17408\n\t" "v_smfmac_f32_16x16x64_bf16 v[52:55], v[0:3], v[72:79], v225\n\t" "ds_read_b128 v[72:75], v226 offset:18432\n\t" "s_waitcnt vmcnt(8)\n\t"
                         "v_smfmac_f32_16x16x64_bf16 v[40:43], v[4:7], v[144:151], v225\n\t" "ds_read_b128 v[76:79], v226 offset:19456\n\t" "v_smfmac_f32_16x16x64_bf16 v[44:47], v[4:7], v[80:87], v225\n\t"
                         "ds_read_b128 v[80:83], v226 offset:20480\n\t" "v_smfmac_f32_16x16x64_bf16 v[48:51], v[4:7], v[152:159], v225\n\t" "ds_read_b128 v[84:87], v226 offset:21504\n\t"
                         "v_smfmac_f32_16x16x64_bf16 v[52:55], v[4:7], v[88:95], v225\n\t" "ds_read_b128 v[88:91], v226 offset:22528\n\t" "s_waitcnt vmcnt(7)\n\t"
                         "v_smfmac_f32_16x16x64_bf16 v[40:43], v[8:11], v[160:167], v225\n\t" "ds_read_b128 v[92:95], v226 offset:23552\n\t" "v_smfmac_f32_16x16x64_bf16 v[44:47], v[8:11], v[96:103], v225\n\t"
                         "ds_read_b128 v[96:99], v226 offset:24576\n\t" "v_smfmac_f32_16x16x64_bf16 v[48:51], v[8:11], v[168:175], v225\n\t" "ds_read_b128 v[100:103], v226 offset:25600\n\t"
                         "v_smfmac_f32_16x16x64_bf16 v[52:55], v[8:11], v[104:111], v225\n\t" "ds_read_b128 v[104:107], v226 offset:26624\n\t" "s_waitcnt vmcnt(6)\n\t"
                         "v_smfmac_f32_16x16x64_bf16 v[40:43], v[12:15], v[176:183], v225\n\t" "ds_read_b128 v[108:111], v226 offset:27648\n\t" "v_smfmac_f32_16x16x64_bf16 v[44:47], v[12:15], v[112:119], v225\n\t"
                         "ds_read_b128 v[112:115], v226 offset:28672\n\t" "v_smfmac_f32_16x16x64_bf16 v[48:51], v[12:15], v[184:191], v225\n\t" "ds_read_b128 v[116:119], v226 offset:29696\n\t"
                         "v_smfmac_f32_16x16x64_bf16 v[52:55], v[12:15], v[120:127], v225\n\t" "ds_read_b128 v[120:123], v226 offset:30720\n\t" "s_waitcnt vmcnt(5)\n\t"
                         "v_smfmac_f32_16x16x64_bf16 v[40:43], v[16:19], v[192:199], v225\n\t" "ds_read_b128 v[124:127], v226 offset:31744\n\t" "s_waitcnt lgkmcnt(14)\n\t"
                         "v_smfmac_f32_16x16x64_bf16 v[44:47], v[16:19], v[64:71], v225\n\t" "ds_read_b128 v[64:67], v227\n\t" "v_smfmac_f32_16x16x64_bf16 v[48:51], v[16:19], v[200:207], v225\n\t"
                         "ds_read_b128 v[68:71], v227 offset:1024\n\t" "s_waitcnt lgkmcnt(14)\n\t" "v_smfmac_f32_16x16x64_bf16 v[52:55], v[16:19], v[72:79], v225\n\t" "ds_read_b128 v[72:75], v227 offset:16\n\t"
                         "s_waitcnt vmcnt(4)\n\t" "v_smfmac_f32_16x16x64_bf16 v[40:43], v[20:23], v[208:215], v225\n\t" "ds_read_b128 v[76:79], v227 offset:1040\n\t" "s_waitcnt lgkmcnt(14)\n\t"
                         "v_smfmac_f32_16x16x64_bf16 v[44:47], v[20:23], v[80:87], v225\n\t" "ds_read_b128 v[80:83], v227 offset:2048\n\t" "v_smfmac_f32_16x16x64_bf16 v[48:51], v[20:23], v[216:223], v225\n\t"
                         "ds_read_b128 v[84:87], v227 offset:3072\n\t" "s_waitcnt lgkmcnt(14)\n\t" "v_smfmac_f32_16x16x64_bf16 v[52:55], v[20:23], v[88:95], v225\n\t" "ds_read_b128 v[88:91], v227 offset:2064\n\t"
                         "s_waitcnt vmcnt(3)\n\t" "v_smfmac_f32_16x16x64_bf16 v[40:43], v[24:27], a[0:7], v225\n\t" "ds_read_b128 v[92:95], v227 offset:3088\n\t" "s_waitcnt lgkmcnt(14)\n\t"
                         "v_smfmac_f32_16x16x64_bf16 v[44:47], v[24:27], v[96:103], v225\n\t" "ds_read_b128 v[96:99], v227 offset:4096\n\t" "v_smfmac_f32_16x16x64_bf16 v[48:51], v[24:27], a[8:15], v225\n\t"
                         "ds_read_b128 v[100:103], v227 offset:5120\n\t" "s_waitcnt lgkmcnt(14)\n\t" "v_smfmac_f32_16x16x64_bf16 v[52:55], v[24:27], v[104:111], v225\n\t" "ds_read_b128 v[104:107], v227 offset:4112\n\t"
                         "s_waitcnt vmcnt(2)\n\t" "v_smfmac_f32_16x16x64_bf16 v[40:43], v[28:31], a[16:23], v225\n\t" "ds_read_b128 v[108:111], v227 offset:5136\n\t" "s_waitcnt lgkmcnt(14)\n\t"
                         "v_smfmac_f32_16x16x64_bf16 v[44:47], v[28:31], v[112:119], v225\n\t" "v_smfmac_f32_16x16x64_bf16 v[48:51], v[28:31], a[24:31], v225\n\t" "s_waitcnt lgkmcnt(12)\n\t"
                         "v_smfmac_f32_16x16x64_bf16 v[52:55], v[28:31], v[120:127], v225\n\t" "s_waitcnt vmcnt(1)\n\t" "v_smfmac_f32_16x16x64_bf16 v[40:43], v[32:35], a[32:39], v225\n\t" "v_mov_b64 v[112:113], 0\n\t"
                         "v_smfmac_f32_16x16x64_bf16 v[44:47], v[32:35], a[40:47], v225\n\t" "v_mov_b64 v[114:115], 0\n\t" "v_smfmac_f32_16x16x64_bf16 v[48:51], v[32:35], a[48:55], v225\n\t" "v_mov_b64 v[116:117], 0\n\t"
                         "v_smfmac_f32_16x16x64_bf16 v[52:55], v[32:35], a[56:63], v225\n\t" "v_mov_b64 v[118:119], 0\n\t" "s_waitcnt vmcnt(0)\n\t" "v_smfmac_f32_16x16x64_bf16 v[40:43], v[36:39], a[64:71], v225\n\t"
                         "v_mov_b64 v[120:121], 0\n\t" "v_smfmac_f32_16x16x64_bf16 v[44:47], v[36:39], a[72:79], v225\n\t" "v_mov_b64 v[122:123], 0\n\t" "v_smfmac_f32_16x16x64_bf16 v[48:51], v[36:39], a[80:87], v225\n\t"
                         "v_mov_b64 v[124:125], 0\n\t" "v_smfmac_f32_16x16x64_bf16 v[52:55], v[36:39], a[88:95], v225\n\t" "v_add_f32 v40, v40, v42\n\t" "v_add_f32 v41, v41, v43\n\t"
                         "v_smfmac_f32_16x16x64_bf16 v[56:59], v[0:3], a[96:103], v225\n\t" "v_cmp_u_f32 vcc, v40, v41\n\t" "v_smfmac_f32_16x16x64_bf16 v[56:59], v[4:7], a[104:111], v225\n\t"
                         "v_cvt_pk_bf16_f32 v42, v40, v41\n\t" "v_add_f32 v44, v44, v46\n\t" "v_smfmac_f32_16x16x64_bf16 v[56:59], v[8:11], a[112:119], v225\n\t" "s_and_b64 vcc, vcc, %11\n\t" "s_cbranch_scc1 .Lrt_s\n\t"
                         "v_smfmac_f32_16x16x64_bf16 v[56:59], v[12:15], a[120:127], v225\n\t" "v_add_f32 v45, v45, v47\n\t" "v_cvt_pk_bf16_f32 v43, v44, v45\n\t"
                         "v_smfmac_f32_16x16x64_bf16 v[56:59], v[16:19], a[128:135], v225\n\t" "ds_write_b64 v228, v[42:43]\n\t" "v_add_f32 v48, v48, v50\n\t"
                         "v_smfmac_f32_16x16x64_bf16 v[56:59], v[20:23], a[136:143], v225\n\t" "v_add_f32 v49, v49, v51\n\t" "v_cvt_pk_bf16_f32 v46, v48, v49\n\t"
                         "v_smfmac_f32_16x16x64_bf16 v[56:59], v[24:27], a[144:151], v225\n\t" "ds_write_b32 v229, v46\n\t" "v_add_f32 v52, v52, v54\n\t" "v_smfmac_f32_16x16x64_bf16 v[56:59], v[28:31], a[152:159], v225\n\t"
                         "v_add_f32 v53, v53, v55\n\t" "v_cvt_pk_bf16_f32 v54, v52, v53\n\t" "v_smfmac_f32_16x16x64_bf16 v[56:59], v[32:35], a[160:167], v225\n\t" "ds_write_b32 v230, v54\n\t"
                         "v_smfmac_f32_16x16x64_bf16 v[56:59], v[36:39], a[168:175], v225\n\t" "v_smfmac_f32_16x16x64_bf16 v[60:63], v[0:3], a[176:183], v225\n\t"
                         "v_smfmac_f32_16x16x64_bf16 v[60:63], v[4:7], a[184:191], v225\n\t" "v_add_f32 v56, v56, v58\n\t" "v_smfmac_f32_16x16x64_bf16 v[60:63], v[8:11], a[192:199], v225\n\t" "s_waitcnt lgkmcnt(0)\n\t"
                         "s_barrier\n\t" "v_smfmac_f32_16x16x64_bf16 v[60:63], v[12:15], a[200:207], v225\n\t" "ds_read_b64_tr_b16 v[40:41], v232\n\t" "v_add_f32 v57, v57, v59\n\t"
                         "v_smfmac_f32_16x16x64_bf16 v[60:63], v[16:19], a[208:215], v225\n\t" "ds_read_b64_tr_b16 v[42:43], v232 offset:32\n\t" "v_cvt_pk_bf16_f32 v58, v56, v57\n\t"
                         "v_smfmac_f32_16x16x64_bf16 v[60:63], v[20:23], a[216:223], v225\n\t" "ds_write_b32 v230, v58 offset:1152\n\t" "v_smfmac_f32_16x16x64_bf16 v[60:63], v[24:27], a[224:231], v225\n\t"
                         "ds_read_b64_tr_b16 v[44:45], v232 offset:1024\n\t" "ds_read_b64_tr_b16 v[46:47], v232 offset:1056\n\t" "v_smfmac_f32_16x16x64_bf16 v[60:63], v[28:31], a[232:239], v225\n\t"
                         "ds_read_b64_tr_b16 v[48:49], v232 offset:2048\n\t" "ds_read_b64_tr_b16 v[50:51], v232 offset:2080\n\t" "v_smfmac_f32_16x16x64_bf16 v[60:63], v[32:35], a[240:247], v225\n\t"
                         "ds_read_b64_tr_b16 v[0:1], v233\n\t" "ds_read_b64_tr_b16 v[2:3], v233 offset:32\n\t" "v_smfmac_f32_16x16x64_bf16 v[60:63], v[36:39], a[248:255], v225\n\t"
                         "ds_read_b64_tr_b16 v[4:5], v233 offset:576\n\t" "ds_read_b64_tr_b16 v[6:7], v233 offset:608\n\t" "s_waitcnt lgkmcnt(9)\n\t" "v_smfmac_f32_16x16x64_bf16 v[112:115], v[40:43], v[64:71], v225\n\t"
                         "s_waitcnt lgkmcnt(8)\n\t" "s_barrier\n\t" "v_smfmac_f32_16x16x64_bf16 v[116:119], v[40:43], v[72:79], v225\n\t" "ds_read_b64_tr_b16 v[8:9], v233 offset:1152\n\t" "s_waitcnt lgkmcnt(7)\n\t"
                         "v_add_f32 v60, v60, v62\n\t" "v_add_f32 v61, v61, v63\n\t" "v_smfmac_f32_16x16x64_bf16 v[112:115], v[44:47], v[80:87], v225\n\t" "ds_read_b64_tr_b16 v[10:11], v233 offset:1184\n\t"
                         "v_cvt_pk_bf16_f32 v62, v60, v61\n\t" "v_smfmac_f32_16x16x64_bf16 v[116:119], v[44:47], v[88:95], v225\n\t" "ds_write_b32 v230, v62 offset:2304\n\t" "s_waitcnt lgkmcnt(7)\n\t"
                         "v_smfmac_f32_16x16x64_bf16 v[112:115], v[48:51], v[96:103], v225\n\t" "ds_read_b64_tr_b16 v[12:13], v233 offset:1728\n\t" "v_smfmac_f32_16x16x64_bf16 v[116:119], v[48:51], v[104:111], v225\n\t"
                         "ds_read_b64_tr_b16 v[14:15], v233 offset:1760\n\t" "s_waitcnt lgkmcnt(5)\n\t" "v_smfmac_f32_16x16x64_bf16 v[120:123], v[40:43], v[0:7], v225\n\t" "v_cndmask_b32 v24, v112, v113, %8\n\t"
                         "v_cndmask_b32 v25, v114, v115, %8\n\t" "v_add_f32 v24, v24, v25\n\t" "v_pk_add_f32 v[26:27], v[116:117], v[118:119]\n\t" "v_cndmask_b32 v25, v26, v27, %8\n\t" "v_max_f32 v26, v24, v25\n\t"
                         "s_waitcnt lgkmcnt(2)\n\t" "s_barrier\n\t" "ds_read_b64_tr_b16 v[16:17], v233 offset:2304\n\t" "v_max_f32_dpp v27, v26, v26 row_ror:4 row_mask:0xf bank_mask:0xf\n\t"
                         "ds_read_b64_tr_b16 v[18:19], v233 offset:2336\n\t" "v_max_f32_dpp v27, v26, v27 row_ror:8 row_mask:0xf bank_mask:0xf\n\t" "v_max_f32_dpp v27, v26, v27 row_ror:12 row_mask:0xf bank_mask:0xf\n\t"
                         "ds_read_b64_tr_b16 v[20:21], v233 offset:2880\n\t" "ds_read_b64_tr_b16 v[22:23], v233 offset:2912\n\t" "v_max_f32_dpp v30, v27, v27 row_ror:1 row_mask:0xf bank_mask:0xf\n\t"
                         "v_max_f32_dpp v30, v27, v30 row_ror:2 row_mask:0xf bank_mask:0xf\n\t" "v_max_f32_dpp v30, v27, v30 row_ror:3 row_mask:0xf bank_mask:0xf\n\t" "s_waitcnt lgkmcnt(4)\n\t"
                         "v_pk_add_f32 v[24:25], v[24:25], v[30:31] op_sel_hi:[1,0] neg_lo:[0,1] neg_hi:[0,1]\n\t" "v_pk_mul_f32 v[24:25], v[24:25], v[236:237] op_sel_hi:[1,0]\n\t" "v_exp_f32 v24, v24\n\t"
                         "v_exp_f32 v25, v25\n\t" "v_mul_f32 v31, %10, v30\n\t" "v_cvt_pk_bf16_f32 v28, |v24|, |v25|\n\t" "v_smfmac_f32_16x16x64_bf16 v[120:123], v[44:47], v[8:15], v225\n\t"
                         "global_store_dword v234, v28, %7 offset:-4096 sc1\n\t" "s_waitcnt lgkmcnt(0)\n\t" "v_add_f32 v26, v24, v25\n\t" "v_smfmac_f32_16x16x64_bf16 v[120:123], v[48:51], v[16:23], v225\n\t"
                         "s_nop 0\n\t" "v_add_f32_dpp v27, v26, v26 row_ror:4 row_mask:0xf bank_mask:0xf\n\t" "v_add_f32_dpp v27, v26, v27 row_ror:8 row_mask:0xf bank_mask:0xf\n\t"
                         "v_add_f32_dpp v27, v26, v27 row_ror:12 row_mask:0xf bank_mask:0xf\n\t" "s_nop 1\n\t" "v_add_f32_dpp v29, v27, v27 row_ror:1 row_mask:0xf bank_mask:0xf\n\t"
                         "v_pk_add_f32 v[24:25], v[120:121], v[122:123]\n\t" "v_cndmask_b32 v24, v24, v25, %8\n\t" "v_max_f32 v26, v24, v30\n\t" "v_add_f32_dpp v29, v27, v29 row_ror:2 row_mask:0xf bank_mask:0xf\n\t"
                         "v_add_f32_dpp v29, v27, v29 row_ror:3 row_mask:0xf bank_mask:0xf\n\t" "v_max_f32_dpp v27, v26, v26 row_ror:1 row_mask:0xf bank_mask:0xf\n\t"
                         "v_max_f32_dpp v27, v26, v27 row_ror:2 row_mask:0xf bank_mask:0xf\n\t" "v_max_f32_dpp v27, v26, v27 row_ror:3 row_mask:0xf bank_mask:0xf\n\t" "v_mul_f32 v24, %10, v24\n\t"
                         "v_mul_f32 v29, 0x3e000000, v29\n\t" "v_max_f32_dpp v26, v27, v27 row_ror:4 row_mask:0xf bank_mask:0xf\n\t" "v_fma_f32 v24, -v26, %10, v24\n\t" "v_fma_f32 v25, -v26, %10, v31\n\t"
                         "v_exp_f32 v24, v24\n\t" "v_exp_f32 v25, v25\n\t" "s_nop 0\n\t" "v_fma_f32 v26, v25, v29, v24\n\t" "v_mov_b32_dpp v124, v24 quad_perm:[0,1,2,3] row_mask:0xf bank_mask:0x3\n\t"
                         "v_mov_b32_dpp v125, v24 quad_perm:[1,0,3,2] row_mask:0xf bank_mask:0x3\n\t" "v_add_f32_dpp v27, v26, v26 row_ror:1 row_mask:0xf bank_mask:0xf\n\t"
                         "v_add_f32_dpp v27, v26, v27 row_ror:2 row_mask:0xf bank_mask:0xf\n\t" "v_add_f32_dpp v27, v26, v27 row_ror:3 row_mask:0xf bank_mask:0xf\n\t"
                         "v_mov_b32_dpp v124, v25 quad_perm:[0,1,2,3] row_mask:0x5 bank_mask:0x4\n\t" "v_mov_b32_dpp v125, v25 quad_perm:[0,1,2,3] row_mask:0xa bank_mask:0x4\n\t"
                         "v_add_f32_dpp v26, v27, v27 row_ror:4 row_mask:0xf bank_mask:0xf\n\t" "v_rcp_f32 v26, v26\n\t" "s_mov_b64 exec, %9\n\t" "v_pk_mul_f32 v[124:125], v[124:125], v[26:27] op_sel_hi:[1,0]\n\t"
                         "v_cvt_pk_bf16_f32 v24, |v124|, |v125|\n\t" "global_store_dword v235, v24, %7 offset:-4096 sc1\n\t" "s_mov_b64 exec, -1\n\t" "s_memrealtime %1\n\t" "s_load_dword %2, %14, %13 offset:%15\n\t"
                         "ds_read_b64_tr_b16 v[36:37], v231\n\t" "ds_read_b64_tr_b16 v[38:39], v231 offset:32\n\t" "v_readfirstlane_b32 %3, v237\n\t" "s_mov_b32 m0, %17\n\t" "v_mov_b64 v[40:41], 0\n\t"
                         "v_mov_b64 v[42:43], 0\n\t" "v_mov_b64 v[44:45], 0\n\t" "v_mov_b64 v[46:47], 0\n\t" "v_mov_b64 v[48:49], 0\n\t" "v_mov_b64 v[50:51], 0\n\t" "v_mov_b64 v[52:53], 0\n\t" "v_mov_b64 v[54:55], 0\n\t"
                         "v_mov_b64 v[56:57], 0\n\t" "v_mov_b64 v[58:59], 0\n\t" "v_mov_b64 v[60:61], 0\n\t" "v_mov_b64 v[62:63], 0\n\t" "v_mov_b32 v237, %18\n\t" "s_waitcnt lgkmcnt(0)\n\t" "ds_read_b128 v[64:67], v226\n\t"
                         "ds_read_b128 v[68:71], v226 offset:1024\n\t" "ds_read_b128 v[72:75], v226 offset:2048\n\t" "ds_read_b128 v[76:79], v226 offset:3072\n\t" "ds_read_b128 v[80:83], v226 offset:4096\n\t"
                         "ds_read_b128 v[84:87], v226 offset:5120\n\t" "s_cmp_eq_u32 %3, -1\n\t" "s_cbranch_scc1 .Lnk_s\n\t" "global_load_lds_dwordx4 v224, %16 sc1\n\t" "global_load_lds_dwordx4 v224, %16 offset:1024 sc1\n\t"
                         "global_load_lds_dwordx4 v224, %16 offset:2048 sc1\n\t" ".Lnk_s:\n\t" "global_store_dwordx4 v239, v[36:39], %21 sc1\n\t" "ds_read_b128 v[88:91], v226 offset:6144\n\t"
                         "ds_read_b128 v[92:95], v226 offset:7168\n\t" "ds_read_b128 v[96:99], v226 offset:8192\n\t" "ds_read_b128 v[100:103], v226 offset:9216\n\t" "ds_read_b128 v[104:107], v226 offset:10240\n\t"
                         "ds_read_b128 v[108:111], v226 offset:11264\n\t" "ds_read_b128 v[112:115], v226 offset:12288\n\t" "ds_read_b128 v[116:119], v226 offset:13312\n\t" "ds_read_b128 v[120:123], v226 offset:14336\n\t"
                         "ds_read_b128 v[124:127], v226 offset:15360\n\t" "s_and_b64 exec, %19, 1\n\t" "s_cbranch_execz .Lnf_s\n\t" "s_sleep 4\n\t" "global_load_dword v237, v237, %20 sc1\n\t" ".Lnf_s:\n\t"
                         "s_mov_b64 exec, -1\n\t" ".Lsk_s:\n\t" "s_mov_b64 %0, vcc\n\t"
                         : "=&s"(bad), "=&s"(tp8), "=&s"(tt), "=&s"(kf)
                         : "s"(uni((int)tgt)), "s"(memp + (h & 4) * 256), "s"(memp - (h & 4) * 256), "s"((u64)memp & ~8192ull), "s"(mb0), "s"(psm), "s"(kscale * 1.4426950408889634f), "s"(en), "s"(tries), "s"(4 * s),
                           "s"(__builtin_amdgcn_kernarg_segment_ptr()), "n"(TT_OFF), "s"(kxp), "s"(kxw), "s"(kfo + (s < 6 ? 0 : (s + 2) >> 3) * 4096), "s"(0x0040404040404044ull >> s), "s"(fl), "s"(kmp),
                           "{v[224:239]}"(ad), "{v[64:95]}"(wk[0]), "{v[96:127]}"(wk[1]), "{v[32:63]}"(wk[2]),
                           "{a[0:31]}"(wa[0]), "{a[32:63]}"(wa[1]), "{a[64:95]}"(wa[2]), "{a[96:127]}"(wa[3]), "{a[128:159]}"(wa[4]), "{a[160:191]}"(wa[5]), "{a[192:223]}"(wa[6]), "{a[224:255]}"(wa[7]), "{v[128:159]}"(wv[0]), "{v[160:191]}"(wv[1]), "{v[192:223]}"(wv[2])
                         : "v0", "v1", "v2", "v3", "v4", "v5", "v6", "v7", "v8", "v9", "v10", "v11", "v12", "v13", "v14", "v15", "v16", "v17", "v18", "v19", "v20", "v21", "v22", "v23", "v24", "v25", "v26", "v27", "v28", "v29", "v30", "v31", "vcc", "scc", "s42", "memory");
            if (__builtin_expect(bad == 0, 1)) break;
            waitvm<0>();
            if (((++tries & 15) == 0 && !unwritten(memp, ad[0])) || --budget < 0) en = 0;
            phi += (u32)cfg.G;
        }
        if (__builtin_expect(kf == -1, 0)) { flag_wait(fl, F_K + pblk(s + 1) * 32 + h, 1); kxdma(); }
        kxp += s < NSEG - 2 ? NH * 12288 : 0;
        clk.calib();
        if (__builtin_expect(s >= NSEG - 2, 0)) {
            waitvm<0>();
            barrier();
            if (w == 3) flag_add(fl, F_PROG + 8 * s + (h & 7), 1);
        }
        if (s == 0) { phi = go_wg(fl, h, w, lds + SL_PF - 16) - (tt >> 16); edge(clk.r1, clk.m1); clk.calib(); }
        follow(phi, (int)(tt >> 16) - (cfg.dV >> 24), (u32)tp8, cfg.G);
        phi += tt & 0xffffu;
        memp = p.ws + mslot(sd, s + 1); kmp += NH * 3072;
    }
    if (h == 0 && w == 0) {
        flag_wait(fl, F_ACK, 256);
        Sync y;
        sync_ld(y, p.ws);
        asm volatile("s_waitcnt vmcnt(0)" : "+v"(y.r), "+v"(y.g), "+v"(y.e) : : "memory");
        const u64 t = ((u64)y.e[1] << 32 | y.e[0]) + 1;
        if (lanex() == 0) gst8(p.ws + OFF_LAT, 1024, u32x2{(u32)t, (u32)(t >> 32)});
    }
    asm volatile(".Lkm_last:\n\ts_waitcnt vmcnt(0)" : : : "memory");
}

DI void proj_role(const Ptrs& p, const ChainCfg cfg, int h, u32 lds, int w) {
    u32x32 wa[8], wv[4];
    u32 phi;
    int sd;
    {
        const int lane = lanex(), tid = 64 * w + lane;
        const int g = lane >> 4, qq = (lane >> 2) & 3, pp = lane & 3;
        constexpr int GS = 3104;
        auto dvo = [&](int i) { const int ix = i * 64 + lane; return (u32)((ix / 24) * (HD * 2) + (ix % 24) * 16); };
        auto ovo = [&](int i) { const int u = i * 64 + lane, r = u / 80; return (u32)(r * 1280 + (((u % 80) ^ (2 * (r ^ (w & 2)))) << 4)); };
        const u32 tra = lds + (2 * (g >> 1) + (qq >> 1)) * GS + (4 * (g & 1) + (qq & 1)) * 384 + 96 * w + 32 * (w & 1) + 8 * pp;
        const u32 tro = (4 * g + (qq & 1) + 16 * (qq >> 1)) * 1280 + 8 * pp + ((u32)(2 * w ^ 2 * ((4 * g + qq) & 7)) << 4);
        u32x16 pa = u32x16{dvo(0), dvo(1), dvo(2), tra, tra - 96 * (w & 1), tro, lds + (u32)((w & 1) * 24576 + (w >> 1) * 99328 + lane * 16), (u32)(lane & 31) * 8,
                           ovo(0), ovo(1), ovo(2), ovo(3), ovo(4), tro, tro, tro};
        Sync y;
        asm volatile("s_nop 4\n\t" "s_add_u32 s42, %22, 0x0\n\t" "s_add_u32 s42, s44, s42\n\t" "s_addc_u32 s43, s45, 0\n\t" "s_add_u32 m0, %18, 0x0\n\t" "s_nop 0\n\t" "global_load_lds_dwordx4 v224, s[42:43]\n\t"
                     "s_add_u32 m0, %18, 0x400\n\t" "s_nop 0\n\t" "global_load_lds_dwordx4 v225, s[42:43]\n\t" "s_add_u32 m0, %18, 0x800\n\t" "s_nop 0\n\t" "global_load_lds_dwordx4 v226, s[42:43]\n\t"
                     "s_add_u32 s42, %22, 0x18000\n\t" "s_add_u32 s42, s44, s42\n\t" "s_addc_u32 s43, s45, 0\n\t" "s_add_u32 m0, %18, 0xc20\n\t" "s_nop 0\n\t" "global_load_lds_dwordx4 v224, s[42:43]\n\t"
                     "s_add_u32 m0, %18, 0x1020\n\t" "s_nop 0\n\t" "global_load_lds_dwordx4 v225, s[42:43]\n\t" "s_add_u32 m0, %18, 0x1420\n\t" "s_nop 0\n\t" "global_load_lds_dwordx4 v226, s[42:43]\n\t"
                     "s_sub_u32 s42, 0xc0000, %22\n\t" "s_add_u32 s42, s44, s42\n\t" "s_addc_u32 s43, s45, 0\n\t" "s_add_u32 m0, %18, 0x6100\n\t" "s_nop 0\n\t" "global_load_lds_dwordx4 v224, s[42:43]\n\t"
                     "s_add_u32 m0, %18, 0x6500\n\t" "s_nop 0\n\t" "global_load_lds_dwordx4 v225, s[42:43]\n\t" "s_add_u32 m0, %18, 0x6900\n\t" "s_nop 0\n\t" "global_load_lds_dwordx4 v226, s[42:43]\n\t"
                     "s_sub_u32 s42, 0xd8000, %22\n\t" "s_add_u32 s42, s44, s42\n\t" "s_addc_u32 s43, s45, 0\n\t" "s_add_u32 m0, %18, 0x6d20\n\t" "s_nop 0\n\t" "global_load_lds_dwordx4 v224, s[42:43]\n\t"
                     "s_add_u32 m0, %18, 0x7120\n\t" "s_nop 0\n\t" "global_load_lds_dwordx4 v225, s[42:43]\n\t" "s_add_u32 m0, %18, 0x7520\n\t" "s_nop 0\n\t" "global_load_lds_dwordx4 v226, s[42:43]\n\t"
                     "s_add_u32 s42, %22, 0x180000\n\t" "s_add_u32 s42, s44, s42\n\t" "s_addc_u32 s43, s45, 0\n\t" "s_add_u32 m0, %18, 0xc200\n\t" "s_nop 0\n\t" "global_load_lds_dwordx4 v224, s[42:43]\n\t"
                     "s_add_u32 m0, %18, 0xc600\n\t" "s_nop 0\n\t" "global_load_lds_dwordx4 v225, s[42:43]\n\t" "s_add_u32 m0, %18, 0xca00\n\t" "s_nop 0\n\t" "global_load_lds_dwordx4 v226, s[42:43]\n\t"
                     "s_add_u32 s42, %22, 0x198000\n\t" "s_add_u32 s42, s44, s42\n\t" "s_addc_u32 s43, s45, 0\n\t" "s_add_u32 m0, %18, 0xce20\n\t" "s_nop 0\n\t" "global_load_lds_dwordx4 v224, s[42:43]\n\t"
                     "s_add_u32 m0, %18, 0xd220\n\t" "s_nop 0\n\t" "global_load_lds_dwordx4 v225, s[42:43]\n\t" "s_add_u32 m0, %18, 0xd620\n\t" "s_nop 0\n\t" "global_load_lds_dwordx4 v226, s[42:43]\n\t"
                     "s_sub_u32 s42, 0x240000, %22\n\t" "s_add_u32 s42, s44, s42\n\t" "s_addc_u32 s43, s45, 0\n\t" "s_add_u32 m0, %18, 0x12300\n\t" "s_nop 0\n\t" "global_load_lds_dwordx4 v224, s[42:43]\n\t"
                     "s_add_u32 m0, %18, 0x12700\n\t" "s_nop 0\n\t" "global_load_lds_dwordx4 v225, s[42:43]\n\t" "s_add_u32 m0, %18, 0x12b00\n\t" "s_nop 0\n\t" "global_load_lds_dwordx4 v226, s[42:43]\n\t"
                     "s_sub_u32 s42, 0x258000, %22\n\t" "s_add_u32 s42, s44, s42\n\t" "s_addc_u32 s43, s45, 0\n\t" "s_add_u32 m0, %18, 0x12f20\n\t" "s_nop 0\n\t" "global_load_lds_dwordx4 v224, s[42:43]\n\t"
                     "s_add_u32 m0, %18, 0x13320\n\t" "s_nop 0\n\t" "global_load_lds_dwordx4 v225, s[42:43]\n\t" "s_add_u32 m0, %18, 0x13720\n\t" "s_nop 0\n\t" "global_load_lds_dwordx4 v226, s[42:43]\n\t"
                     "s_waitcnt vmcnt(12) lgkmcnt(0)\n\t" "s_barrier\n\t" "s_add_u32 s42, %22, 0x300000\n\t" "s_add_u32 s42, s44, s42\n\t" "s_addc_u32 s43, s45, 0\n\t" "s_add_u32 m0, %18, 0x18400\n\t" "s_nop 0\n\t"
                     "global_load_lds_dwordx4 v224, s[42:43]\n\t" "s_add_u32 m0, %18, 0x18800\n\t" "s_nop 0\n\t" "global_load_lds_dwordx4 v225, s[42:43]\n\t" "s_add_u32 m0, %18, 0x18c00\n\t" "s_nop 0\n\t"
                     "global_load_lds_dwordx4 v226, s[42:43]\n\t" "s_add_u32 s42, %22, 0x318000\n\t" "s_add_u32 s42, s44, s42\n\t" "s_addc_u32 s43, s45, 0\n\t" "s_add_u32 m0, %18, 0x19020\n\t" "s_nop 0\n\t"
                     "global_load_lds_dwordx4 v224, s[42:43]\n\t" "s_add_u32 m0, %18, 0x19420\n\t" "s_nop 0\n\t" "global_load_lds_dwordx4 v225, s[42:43]\n\t" "s_add_u32 m0, %18, 0x19820\n\t" "s_nop 0\n\t"
                     "global_load_lds_dwordx4 v226, s[42:43]\n\t" "s_sub_u32 s42, 0x3c0000, %22\n\t" "s_add_u32 s42, s44, s42\n\t" "s_addc_u32 s43, s45, 0\n\t" "s_add_u32 m0, %18, 0x1e500\n\t" "s_nop 0\n\t"
                     "global_load_lds_dwordx4 v224, s[42:43]\n\t" "s_add_u32 m0, %18, 0x1e900\n\t" "s_nop 0\n\t" "global_load_lds_dwordx4 v225, s[42:43]\n\t" "s_add_u32 m0, %18, 0x1ed00\n\t" "s_nop 0\n\t"
                     "global_load_lds_dwordx4 v226, s[42:43]\n\t" "s_sub_u32 s42, 0x3d8000, %22\n\t" "s_add_u32 s42, s44, s42\n\t" "s_addc_u32 s43, s45, 0\n\t" "s_add_u32 m0, %18, 0x1f120\n\t" "s_nop 0\n\t"
                     "global_load_lds_dwordx4 v224, s[42:43]\n\t" "s_add_u32 m0, %18, 0x1f520\n\t" "s_nop 0\n\t" "global_load_lds_dwordx4 v225, s[42:43]\n\t" "s_add_u32 m0, %18, 0x1f920\n\t" "s_nop 0\n\t"
                     "global_load_lds_dwordx4 v226, s[42:43]\n\t" "v_add_u32 v237, 0x0, v227\n\t" "v_add_u32 v238, 0x0, v228\n\t" "ds_read_b64_tr_b16 a[0:1], v237\n\t" "ds_read_b64_tr_b16 a[2:3], v237 offset:768\n\t"
                     "ds_read_b64_tr_b16 a[4:5], v237 offset:12416\n\t" "ds_read_b64_tr_b16 a[6:7], v237 offset:13184\n\t" "ds_read_b64_tr_b16 v[0:1], v237 offset:32\n\t" "ds_read_b64_tr_b16 v[2:3], v237 offset:800\n\t"
                     "ds_read_b64_tr_b16 v[4:5], v237 offset:12448\n\t" "ds_read_b64_tr_b16 v[6:7], v237 offset:13216\n\t" "ds_read_b64_tr_b16 a[8:9], v238 offset:64\n\t" "ds_read_b64_tr_b16 a[10:11], v238 offset:832\n\t"
                     "ds_read_b64_tr_b16 a[12:13], v238 offset:12480\n\t" "ds_read_b64_tr_b16 a[14:15], v238 offset:13248\n\t" "ds_read_b64_tr_b16 a[32:33], v237 offset:24832\n\t"
                     "ds_read_b64_tr_b16 a[34:35], v237 offset:25600\n\t" "ds_read_b64_tr_b16 a[36:37], v237 offset:37248\n\t" "ds_read_b64_tr_b16 a[38:39], v237 offset:38016\n\t"
                     "ds_read_b64_tr_b16 v[16:17], v237 offset:24864\n\t" "ds_read_b64_tr_b16 v[18:19], v237 offset:25632\n\t" "ds_read_b64_tr_b16 v[20:21], v237 offset:37280\n\t"
                     "ds_read_b64_tr_b16 v[22:23], v237 offset:38048\n\t" "ds_read_b64_tr_b16 a[40:41], v238 offset:24896\n\t" "ds_read_b64_tr_b16 a[42:43], v238 offset:25664\n\t"
                     "ds_read_b64_tr_b16 a[44:45], v238 offset:37312\n\t" "ds_read_b64_tr_b16 a[46:47], v238 offset:38080\n\t" "s_waitcnt vmcnt(12) lgkmcnt(0)\n\t" "s_barrier\n\t" "s_add_u32 s42, %22, 0x480000\n\t"
                     "s_add_u32 s42, s44, s42\n\t" "s_addc_u32 s43, s45, 0\n\t" "s_add_u32 m0, %18, 0x0\n\t" "s_nop 0\n\t" "global_load_lds_dwordx4 v224, s[42:43]\n\t" "s_add_u32 m0, %18, 0x400\n\t" "s_nop 0\n\t"
                     "global_load_lds_dwordx4 v225, s[42:43]\n\t" "s_add_u32 m0, %18, 0x800\n\t" "s_nop 0\n\t" "global_load_lds_dwordx4 v226, s[42:43]\n\t" "s_add_u32 s42, %22, 0x498000\n\t" "s_add_u32 s42, s44, s42\n\t"
                     "s_addc_u32 s43, s45, 0\n\t" "s_add_u32 m0, %18, 0xc20\n\t" "s_nop 0\n\t" "global_load_lds_dwordx4 v224, s[42:43]\n\t" "s_add_u32 m0, %18, 0x1020\n\t" "s_nop 0\n\t"
                     "global_load_lds_dwordx4 v225, s[42:43]\n\t" "s_add_u32 m0, %18, 0x1420\n\t" "s_nop 0\n\t" "global_load_lds_dwordx4 v226, s[42:43]\n\t" "s_sub_u32 s42, 0x540000, %22\n\t" "s_add_u32 s42, s44, s42\n\t"
                     "s_addc_u32 s43, s45, 0\n\t" "s_add_u32 m0, %18, 0x6100\n\t" "s_nop 0\n\t" "global_load_lds_dwordx4 v224, s[42:43]\n\t" "s_add_u32 m0, %18, 0x6500\n\t" "s_nop 0\n\t"
                     "global_load_lds_dwordx4 v225, s[42:43]\n\t" "s_add_u32 m0, %18, 0x6900\n\t" "s_nop 0\n\t" "global_load_lds_dwordx4 v226, s[42:43]\n\t" "s_sub_u32 s42, 0x558000, %22\n\t" "s_add_u32 s42, s44, s42\n\t"
                     "s_addc_u32 s43, s45, 0\n\t" "s_add_u32 m0, %18, 0x6d20\n\t" "s_nop 0\n\t" "global_load_lds_dwordx4 v224, s[42:43]\n\t" "s_add_u32 m0, %18, 0x7120\n\t" "s_nop 0\n\t"
                     "global_load_lds_dwordx4 v225, s[42:43]\n\t" "s_add_u32 m0, %18, 0x7520\n\t" "s_nop 0\n\t" "global_load_lds_dwordx4 v226, s[42:43]\n\t" "v_add_u32 v237, 0xc200, v227\n\t"
                     "v_add_u32 v238, 0xc200, v228\n\t" "ds_read_b64_tr_b16 a[16:17], v237\n\t" "ds_read_b64_tr_b16 a[18:19], v237 offset:768\n\t" "ds_read_b64_tr_b16 a[20:21], v237 offset:12416\n\t"
                     "ds_read_b64_tr_b16 a[22:23], v237 offset:13184\n\t" "ds_read_b64_tr_b16 v[8:9], v237 offset:32\n\t" "ds_read_b64_tr_b16 v[10:11], v237 offset:800\n\t"
                     "ds_read_b64_tr_b16 v[12:13], v237 offset:12448\n\t" "ds_read_b64_tr_b16 v[14:15], v237 offset:13216\n\t" "ds_read_b64_tr_b16 a[24:25], v238 offset:64\n\t"
                     "ds_read_b64_tr_b16 a[26:27], v238 offset:832\n\t" "ds_read_b64_tr_b16 a[28:29], v238 offset:12480\n\t" "ds_read_b64_tr_b16 a[30:31], v238 offset:13248\n\t"
                     "ds_read_b64_tr_b16 a[48:49], v237 offset:24832\n\t" "ds_read_b64_tr_b16 a[50:51], v237 offset:25600\n\t" "ds_read_b64_tr_b16 a[52:53], v237 offset:37248\n\t"
                     "ds_read_b64_tr_b16 a[54:55], v237 offset:38016\n\t" "ds_read_b64_tr_b16 v[24:25], v237 offset:24864\n\t" "ds_read_b64_tr_b16 v[26:27], v237 offset:25632\n\t"
                     "ds_read_b64_tr_b16 v[28:29], v237 offset:37280\n\t" "ds_read_b64_tr_b16 v[30:31], v237 offset:38048\n\t" "ds_read_b64_tr_b16 a[56:57], v238 offset:24896\n\t"
                     "ds_read_b64_tr_b16 a[58:59], v238 offset:25664\n\t" "ds_read_b64_tr_b16 a[60:61], v238 offset:37312\n\t" "ds_read_b64_tr_b16 a[62:63], v238 offset:38080\n\t" "s_waitcnt vmcnt(12) lgkmcnt(0)\n\t"
                     "s_barrier\n\t" "s_add_u32 s42, %22, 0x600000\n\t" "s_add_u32 s42, s44, s42\n\t" "s_addc_u32 s43, s45, 0\n\t" "s_add_u32 m0, %18, 0xc200\n\t" "s_nop 0\n\t" "global_load_lds_dwordx4 v224, s[42:43]\n\t"
                     "s_add_u32 m0, %18, 0xc600\n\t" "s_nop 0\n\t" "global_load_lds_dwordx4 v225, s[42:43]\n\t" "s_add_u32 m0, %18, 0xca00\n\t" "s_nop 0\n\t" "global_load_lds_dwordx4 v226, s[42:43]\n\t"
                     "s_add_u32 s42, %22, 0x618000\n\t" "s_add_u32 s42, s44, s42\n\t" "s_addc_u32 s43, s45, 0\n\t" "s_add_u32 m0, %18, 0xce20\n\t" "s_nop 0\n\t" "global_load_lds_dwordx4 v224, s[42:43]\n\t"
                     "s_add_u32 m0, %18, 0xd220\n\t" "s_nop 0\n\t" "global_load_lds_dwordx4 v225, s[42:43]\n\t" "s_add_u32 m0, %18, 0xd620\n\t" "s_nop 0\n\t" "global_load_lds_dwordx4 v226, s[42:43]\n\t"
                     "s_sub_u32 s42, 0x6c0000, %22\n\t" "s_add_u32 s42, s44, s42\n\t" "s_addc_u32 s43, s45, 0\n\t" "s_add_u32 m0, %18, 0x12300\n\t" "s_nop 0\n\t" "global_load_lds_dwordx4 v224, s[42:43]\n\t"
                     "s_add_u32 m0, %18, 0x12700\n\t" "s_nop 0\n\t" "global_load_lds_dwordx4 v225, s[42:43]\n\t" "s_add_u32 m0, %18, 0x12b00\n\t" "s_nop 0\n\t" "global_load_lds_dwordx4 v226, s[42:43]\n\t"
                     "s_sub_u32 s42, 0x6d8000, %22\n\t" "s_add_u32 s42, s44, s42\n\t" "s_addc_u32 s43, s45, 0\n\t" "s_add_u32 m0, %18, 0x12f20\n\t" "s_nop 0\n\t" "global_load_lds_dwordx4 v224, s[42:43]\n\t"
                     "s_add_u32 m0, %18, 0x13320\n\t" "s_nop 0\n\t" "global_load_lds_dwordx4 v225, s[42:43]\n\t" "s_add_u32 m0, %18, 0x13720\n\t" "s_nop 0\n\t" "global_load_lds_dwordx4 v226, s[42:43]\n\t"
                     "v_add_u32 v237, 0x18400, v227\n\t" "v_add_u32 v238, 0x18400, v228\n\t" "ds_read_b64_tr_b16 a[64:65], v237\n\t" "ds_read_b64_tr_b16 a[66:67], v237 offset:768\n\t"
                     "ds_read_b64_tr_b16 a[68:69], v237 offset:12416\n\t" "ds_read_b64_tr_b16 a[70:71], v237 offset:13184\n\t" "ds_read_b64_tr_b16 v[32:33], v237 offset:32\n\t"
                     "ds_read_b64_tr_b16 v[34:35], v237 offset:800\n\t" "ds_read_b64_tr_b16 v[36:37], v237 offset:12448\n\t" "ds_read_b64_tr_b16 v[38:39], v237 offset:13216\n\t"
                     "ds_read_b64_tr_b16 a[72:73], v238 offset:64\n\t" "ds_read_b64_tr_b16 a[74:75], v238 offset:832\n\t" "ds_read_b64_tr_b16 a[76:77], v238 offset:12480\n\t"
                     "ds_read_b64_tr_b16 a[78:79], v238 offset:13248\n\t" "ds_read_b64_tr_b16 a[80:81], v237 offset:24832\n\t" "ds_read_b64_tr_b16 a[82:83], v237 offset:25600\n\t"
                     "ds_read_b64_tr_b16 a[84:85], v237 offset:37248\n\t" "ds_read_b64_tr_b16 a[86:87], v237 offset:38016\n\t" "ds_read_b64_tr_b16 v[40:41], v237 offset:24864\n\t"
                     "ds_read_b64_tr_b16 v[42:43], v237 offset:25632\n\t" "ds_read_b64_tr_b16 v[44:45], v237 offset:37280\n\t" "ds_read_b64_tr_b16 v[46:47], v237 offset:38048\n\t"
                     "ds_read_b64_tr_b16 a[88:89], v238 offset:24896\n\t" "ds_read_b64_tr_b16 a[90:91], v238 offset:25664\n\t" "ds_read_b64_tr_b16 a[92:93], v238 offset:37312\n\t"
                     "ds_read_b64_tr_b16 a[94:95], v238 offset:38080\n\t" "s_waitcnt vmcnt(12) lgkmcnt(0)\n\t" "s_barrier\n\t" "s_add_u32 s42, s46, 0x0\n\t" "s_addc_u32 s43, s47, 0\n\t" "s_add_u32 m0, %20, 0x18400\n\t"
                     "s_nop 0\n\t" "global_load_lds_dwordx4 v232, s[42:43]\n\t" "s_add_u32 m0, %20, 0x18800\n\t" "s_nop 0\n\t" "global_load_lds_dwordx4 v233, s[42:43]\n\t" "s_add_u32 m0, %20, 0x18c00\n\t" "s_nop 0\n\t"
                     "global_load_lds_dwordx4 v234, s[42:43]\n\t" "s_add_u32 m0, %20, 0x19000\n\t" "s_nop 0\n\t" "global_load_lds_dwordx4 v235, s[42:43]\n\t" "s_add_u32 m0, %20, 0x19400\n\t" "s_nop 0\n\t"
                     "global_load_lds_dwordx4 v236, s[42:43]\n\t" "s_add_u32 s42, s46, 0x1400\n\t" "s_addc_u32 s43, s47, 0\n\t" "s_add_u32 m0, %20, 0x19800\n\t" "v_xor_b32 v224, 0x80, v232\n\t"
                     "global_load_lds_dwordx4 v224, s[42:43]\n\t" "s_add_u32 m0, %20, 0x19c00\n\t" "v_xor_b32 v224, 0x80, v233\n\t" "global_load_lds_dwordx4 v224, s[42:43]\n\t" "s_add_u32 m0, %20, 0x1a000\n\t"
                     "v_xor_b32 v224, 0x80, v234\n\t" "global_load_lds_dwordx4 v224, s[42:43]\n\t" "s_add_u32 m0, %20, 0x1a400\n\t" "v_xor_b32 v224, 0x80, v235\n\t" "global_load_lds_dwordx4 v224, s[42:43]\n\t"
                     "s_add_u32 m0, %20, 0x1a800\n\t" "v_xor_b32 v224, 0x80, v236\n\t" "global_load_lds_dwordx4 v224, s[42:43]\n\t" "v_add_u32 v237, 0x0, v227\n\t" "v_add_u32 v238, 0x0, v228\n\t"
                     "ds_read_b64_tr_b16 a[96:97], v237\n\t" "ds_read_b64_tr_b16 a[98:99], v237 offset:768\n\t" "ds_read_b64_tr_b16 a[100:101], v237 offset:12416\n\t" "ds_read_b64_tr_b16 a[102:103], v237 offset:13184\n\t"
                     "ds_read_b64_tr_b16 v[48:49], v237 offset:32\n\t" "ds_read_b64_tr_b16 v[50:51], v237 offset:800\n\t" "ds_read_b64_tr_b16 v[52:53], v237 offset:12448\n\t"
                     "ds_read_b64_tr_b16 v[54:55], v237 offset:13216\n\t" "ds_read_b64_tr_b16 a[104:105], v238 offset:64\n\t" "ds_read_b64_tr_b16 a[106:107], v238 offset:832\n\t"
                     "ds_read_b64_tr_b16 a[108:109], v238 offset:12480\n\t" "ds_read_b64_tr_b16 a[110:111], v238 offset:13248\n\t" "ds_read_b64_tr_b16 a[112:113], v237 offset:24832\n\t"
                     "ds_read_b64_tr_b16 a[114:115], v237 offset:25600\n\t" "ds_read_b64_tr_b16 a[116:117], v237 offset:37248\n\t" "ds_read_b64_tr_b16 a[118:119], v237 offset:38016\n\t"
                     "ds_read_b64_tr_b16 v[56:57], v237 offset:24864\n\t" "ds_read_b64_tr_b16 v[58:59], v237 offset:25632\n\t" "ds_read_b64_tr_b16 v[60:61], v237 offset:37280\n\t"
                     "ds_read_b64_tr_b16 v[62:63], v237 offset:38048\n\t" "ds_read_b64_tr_b16 a[120:121], v238 offset:24896\n\t" "ds_read_b64_tr_b16 a[122:123], v238 offset:25664\n\t"
                     "ds_read_b64_tr_b16 a[124:125], v238 offset:37312\n\t" "ds_read_b64_tr_b16 a[126:127], v238 offset:38080\n\t" "s_waitcnt vmcnt(10) lgkmcnt(0)\n\t" "s_barrier\n\t" "s_add_u32 s42, s46, 0xa000\n\t"
                     "s_addc_u32 s43, s47, 0\n\t" "s_add_u32 m0, %20, 0x0\n\t" "s_nop 0\n\t" "global_load_lds_dwordx4 v232, s[42:43]\n\t" "s_add_u32 m0, %20, 0x400\n\t" "s_nop 0\n\t"
                     "global_load_lds_dwordx4 v233, s[42:43]\n\t" "s_add_u32 m0, %20, 0x800\n\t" "s_nop 0\n\t" "global_load_lds_dwordx4 v234, s[42:43]\n\t" "s_add_u32 m0, %20, 0xc00\n\t" "s_nop 0\n\t"
                     "global_load_lds_dwordx4 v235, s[42:43]\n\t" "s_add_u32 m0, %20, 0x1000\n\t" "s_nop 0\n\t" "global_load_lds_dwordx4 v236, s[42:43]\n\t" "s_add_u32 s42, s46, 0xb400\n\t" "s_addc_u32 s43, s47, 0\n\t"
                     "s_add_u32 m0, %20, 0x1400\n\t" "v_xor_b32 v224, 0x80, v232\n\t" "global_load_lds_dwordx4 v224, s[42:43]\n\t" "s_add_u32 m0, %20, 0x1800\n\t" "v_xor_b32 v224, 0x80, v233\n\t"
                     "global_load_lds_dwordx4 v224, s[42:43]\n\t" "s_add_u32 m0, %20, 0x1c00\n\t" "v_xor_b32 v224, 0x80, v234\n\t" "global_load_lds_dwordx4 v224, s[42:43]\n\t" "s_add_u32 m0, %20, 0x2000\n\t"
                     "v_xor_b32 v224, 0x80, v235\n\t" "global_load_lds_dwordx4 v224, s[42:43]\n\t" "s_add_u32 m0, %20, 0x2400\n\t" "v_xor_b32 v224, 0x80, v236\n\t" "global_load_lds_dwordx4 v224, s[42:43]\n\t"
                     "v_add_u32 v237, 0xc200, v227\n\t" "v_add_u32 v238, 0xc200, v228\n\t" "ds_read_b64_tr_b16 a[128:129], v237\n\t" "ds_read_b64_tr_b16 a[130:131], v237 offset:768\n\t"
                     "ds_read_b64_tr_b16 a[132:133], v237 offset:12416\n\t" "ds_read_b64_tr_b16 a[134:135], v237 offset:13184\n\t" "ds_read_b64_tr_b16 a[136:137], v237 offset:32\n\t"
                     "ds_read_b64_tr_b16 a[138:139], v237 offset:800\n\t" "ds_read_b64_tr_b16 a[140:141], v237 offset:12448\n\t" "ds_read_b64_tr_b16 a[142:143], v237 offset:13216\n\t"
                     "ds_read_b64_tr_b16 a[144:145], v238 offset:64\n\t" "ds_read_b64_tr_b16 a[146:147], v238 offset:832\n\t" "ds_read_b64_tr_b16 a[148:149], v238 offset:12480\n\t"
                     "ds_read_b64_tr_b16 a[150:151], v238 offset:13248\n\t" "ds_read_b64_tr_b16 a[152:153], v237 offset:24832\n\t" "ds_read_b64_tr_b16 a[154:155], v237 offset:25600\n\t"
                     "ds_read_b64_tr_b16 a[156:157], v237 offset:37248\n\t" "ds_read_b64_tr_b16 a[158:159], v237 offset:38016\n\t" "ds_read_b64_tr_b16 a[160:161], v237 offset:24864\n\t"
                     "ds_read_b64_tr_b16 a[162:163], v237 offset:25632\n\t" "ds_read_b64_tr_b16 a[164:165], v237 offset:37280\n\t" "ds_read_b64_tr_b16 a[166:167], v237 offset:38048\n\t"
                     "ds_read_b64_tr_b16 a[168:169], v238 offset:24896\n\t" "ds_read_b64_tr_b16 a[170:171], v238 offset:25664\n\t" "ds_read_b64_tr_b16 a[172:173], v238 offset:37312\n\t"
                     "ds_read_b64_tr_b16 a[174:175], v238 offset:38080\n\t" "s_waitcnt vmcnt(10) lgkmcnt(0)\n\t" "s_barrier\n\t" "s_add_u32 s42, s46, 0x14000\n\t" "s_addc_u32 s43, s47, 0\n\t"
                     "s_add_u32 m0, %20, 0xc200\n\t" "s_nop 0\n\t" "global_load_lds_dwordx4 v232, s[42:43]\n\t" "s_add_u32 m0, %20, 0xc600\n\t" "s_nop 0\n\t" "global_load_lds_dwordx4 v233, s[42:43]\n\t"
                     "s_add_u32 m0, %20, 0xca00\n\t" "s_nop 0\n\t" "global_load_lds_dwordx4 v234, s[42:43]\n\t" "s_add_u32 m0, %20, 0xce00\n\t" "s_nop 0\n\t" "global_load_lds_dwordx4 v235, s[42:43]\n\t"
                     "s_add_u32 m0, %20, 0xd200\n\t" "s_nop 0\n\t" "global_load_lds_dwordx4 v236, s[42:43]\n\t" "s_add_u32 s42, s46, 0x15400\n\t" "s_addc_u32 s43, s47, 0\n\t" "s_add_u32 m0, %20, 0xd600\n\t"
                     "v_xor_b32 v224, 0x80, v232\n\t" "global_load_lds_dwordx4 v224, s[42:43]\n\t" "s_add_u32 m0, %20, 0xda00\n\t" "v_xor_b32 v224, 0x80, v233\n\t" "global_load_lds_dwordx4 v224, s[42:43]\n\t"
                     "s_add_u32 m0, %20, 0xde00\n\t" "v_xor_b32 v224, 0x80, v234\n\t" "global_load_lds_dwordx4 v224, s[42:43]\n\t" "s_add_u32 m0, %20, 0xe200\n\t" "v_xor_b32 v224, 0x80, v235\n\t"
                     "global_load_lds_dwordx4 v224, s[42:43]\n\t" "s_add_u32 m0, %20, 0xe600\n\t" "v_xor_b32 v224, 0x80, v236\n\t" "global_load_lds_dwordx4 v224, s[42:43]\n\t" "s_add_u32 s42, %21, 0x18400\n\t"
                     "v_xor_b32 v237, 0x0, v229\n\t" "v_add_u32 v237, s42, v237\n\t" "v_xor_b32 v238, 0x40, v229\n\t" "v_add_u32 v238, s42, v238\n\t" "v_xor_b32 v239, 0x80, v229\n\t" "v_add_u32 v239, s42, v239\n\t"
                     "v_xor_b32 v226, 0xc0, v229\n\t" "v_add_u32 v226, s42, v226\n\t" "ds_read_b64_tr_b16 a[176:177], v237\n\t" "ds_read_b64_tr_b16 a[178:179], v238 offset:2560\n\t"
                     "ds_read_b64_tr_b16 a[200:201], v239\n\t" "ds_read_b64_tr_b16 a[202:203], v226 offset:2560\n\t" "ds_read_b64_tr_b16 a[224:225], v237 offset:256\n\t"
                     "ds_read_b64_tr_b16 a[226:227], v238 offset:2816\n\t" "ds_read_b64_tr_b16 a[248:249], v239 offset:256\n\t" "ds_read_b64_tr_b16 a[250:251], v226 offset:2816\n\t"
                     "ds_read_b64_tr_b16 v[112:113], v237 offset:512\n\t" "ds_read_b64_tr_b16 v[114:115], v238 offset:3072\n\t" "ds_read_b64_tr_b16 v[136:137], v239 offset:512\n\t"
                     "ds_read_b64_tr_b16 v[138:139], v226 offset:3072\n\t" "ds_read_b64_tr_b16 v[160:161], v237 offset:768\n\t" "ds_read_b64_tr_b16 v[162:163], v238 offset:3328\n\t"
                     "ds_read_b64_tr_b16 v[184:185], v239 offset:768\n\t" "ds_read_b64_tr_b16 v[186:187], v226 offset:3328\n\t" "ds_read_b64_tr_b16 v[208:209], v237 offset:1024\n\t"
                     "ds_read_b64_tr_b16 v[210:211], v238 offset:3584\n\t" "ds_read_b64_tr_b16 v[72:73], v239 offset:1024\n\t" "ds_read_b64_tr_b16 v[74:75], v226 offset:3584\n\t" "s_waitcnt vmcnt(10) lgkmcnt(0)\n\t"
                     "s_barrier\n\t" "s_add_u32 s42, s46, 0x1e000\n\t" "s_addc_u32 s43, s47, 0\n\t" "s_add_u32 m0, %20, 0x18400\n\t" "s_nop 0\n\t" "global_load_lds_dwordx4 v232, s[42:43]\n\t"
                     "s_add_u32 m0, %20, 0x18800\n\t" "s_nop 0\n\t" "global_load_lds_dwordx4 v233, s[42:43]\n\t" "s_add_u32 m0, %20, 0x18c00\n\t" "s_nop 0\n\t" "global_load_lds_dwordx4 v234, s[42:43]\n\t"
                     "s_add_u32 m0, %20, 0x19000\n\t" "s_nop 0\n\t" "global_load_lds_dwordx4 v235, s[42:43]\n\t" "s_add_u32 m0, %20, 0x19400\n\t" "s_nop 0\n\t" "global_load_lds_dwordx4 v236, s[42:43]\n\t"
                     "s_add_u32 s42, s46, 0x1f400\n\t" "s_addc_u32 s43, s47, 0\n\t" "s_add_u32 m0, %20, 0x19800\n\t" "v_xor_b32 v224, 0x80, v232\n\t" "global_load_lds_dwordx4 v224, s[42:43]\n\t"
                     "s_add_u32 m0, %20, 0x19c00\n\t" "v_xor_b32 v224, 0x80, v233\n\t" "global_load_lds_dwordx4 v224, s[42:43]\n\t" "s_add_u32 m0, %20, 0x1a000\n\t" "v_xor_b32 v224, 0x80, v234\n\t"
                     "global_load_lds_dwordx4 v224, s[42:43]\n\t" "s_add_u32 m0, %20, 0x1a400\n\t" "v_xor_b32 v224, 0x80, v235\n\t" "global_load_lds_dwordx4 v224, s[42:43]\n\t" "s_add_u32 m0, %20, 0x1a800\n\t"
                     "v_xor_b32 v224, 0x80, v236\n\t" "global_load_lds_dwordx4 v224, s[42:43]\n\t" "s_add_u32 s42, %21, 0x0\n\t" "v_xor_b32 v237, 0x0, v229\n\t" "v_add_u32 v237, s42, v237\n\t"
                     "v_xor_b32 v238, 0x40, v229\n\t" "v_add_u32 v238, s42, v238\n\t" "v_xor_b32 v239, 0x80, v229\n\t" "v_add_u32 v239, s42, v239\n\t" "v_xor_b32 v226, 0xc0, v229\n\t" "v_add_u32 v226, s42, v226\n\t"
                     "ds_read_b64_tr_b16 a[180:181], v237\n\t" "ds_read_b64_tr_b16 a[182:183], v238 offset:2560\n\t" "ds_read_b64_tr_b16 a[204:205], v239\n\t" "ds_read_b64_tr_b16 a[206:207], v226 offset:2560\n\t"
                     "ds_read_b64_tr_b16 a[228:229], v237 offset:256\n\t" "ds_read_b64_tr_b16 a[230:231], v238 offset:2816\n\t" "ds_read_b64_tr_b16 a[252:253], v239 offset:256\n\t"
                     "ds_read_b64_tr_b16 a[254:255], v226 offset:2816\n\t" "ds_read_b64_tr_b16 v[116:117], v237 offset:512\n\t" "ds_read_b64_tr_b16 v[118:119], v238 offset:3072\n\t"
                     "ds_read_b64_tr_b16 v[140:141], v239 offset:512\n\t" "ds_read_b64_tr_b16 v[142:143], v226 offset:3072\n\t" "ds_read_b64_tr_b16 v[164:165], v237 offset:768\n\t"
                     "ds_read_b64_tr_b16 v[166:167], v238 offset:3328\n\t" "ds_read_b64_tr_b16 v[188:189], v239 offset:768\n\t" "ds_read_b64_tr_b16 v[190:191], v226 offset:3328\n\t"
                     "ds_read_b64_tr_b16 v[212:213], v237 offset:1024\n\t" "ds_read_b64_tr_b16 v[214:215], v238 offset:3584\n\t" "ds_read_b64_tr_b16 v[76:77], v239 offset:1024\n\t"
                     "ds_read_b64_tr_b16 v[78:79], v226 offset:3584\n\t" "s_waitcnt vmcnt(10) lgkmcnt(0)\n\t" "s_barrier\n\t" "v_mov_b32 v225, 0\n\t" "global_load_dwordx2 %12, v231, %19 sc1\n\t"
                     "global_load_dwordx2 %13, v225, %19 offset:512 sc1\n\t" "global_load_dwordx2 %14, v225, %19 offset:1024 sc1\n\t" "s_add_u32 s42, s46, 0x28000\n\t" "s_addc_u32 s43, s47, 0\n\t"
                     "s_add_u32 m0, %20, 0x0\n\t" "s_nop 0\n\t" "global_load_lds_dwordx4 v232, s[42:43]\n\t" "s_add_u32 m0, %20, 0x400\n\t" "s_nop 0\n\t" "global_load_lds_dwordx4 v233, s[42:43]\n\t"
                     "s_add_u32 m0, %20, 0x800\n\t" "s_nop 0\n\t" "global_load_lds_dwordx4 v234, s[42:43]\n\t" "s_add_u32 m0, %20, 0xc00\n\t" "s_nop 0\n\t" "global_load_lds_dwordx4 v235, s[42:43]\n\t"
                     "s_add_u32 m0, %20, 0x1000\n\t" "s_nop 0\n\t" "global_load_lds_dwordx4 v236, s[42:43]\n\t" "s_add_u32 s42, s46, 0x29400\n\t" "s_addc_u32 s43, s47, 0\n\t" "s_add_u32 m0, %20, 0x1400\n\t"
                     "v_xor_b32 v224, 0x80, v232\n\t" "global_load_lds_dwordx4 v224, s[42:43]\n\t" "s_add_u32 m0, %20, 0x1800\n\t" "v_xor_b32 v224, 0x80, v233\n\t" "global_load_lds_dwordx4 v224, s[42:43]\n\t"
                     "s_add_u32 m0, %20, 0x1c00\n\t" "v_xor_b32 v224, 0x80, v234\n\t" "global_load_lds_dwordx4 v224, s[42:43]\n\t" "s_add_u32 m0, %20, 0x2000\n\t" "v_xor_b32 v224, 0x80, v235\n\t"
                     "global_load_lds_dwordx4 v224, s[42:43]\n\t" "s_add_u32 m0, %20, 0x2400\n\t" "v_xor_b32 v224, 0x80, v236\n\t" "global_load_lds_dwordx4 v224, s[42:43]\n\t" "s_add_u32 s42, %21, 0xc200\n\t"
                     "v_xor_b32 v237, 0x0, v229\n\t" "v_add_u32 v237, s42, v237\n\t" "v_xor_b32 v238, 0x40, v229\n\t" "v_add_u32 v238, s42, v238\n\t" "v_xor_b32 v239, 0x80, v229\n\t" "v_add_u32 v239, s42, v239\n\t"
                     "v_xor_b32 v226, 0xc0, v229\n\t" "v_add_u32 v226, s42, v226\n\t" "ds_read_b64_tr_b16 a[184:185], v237\n\t" "ds_read_b64_tr_b16 a[186:187], v238 offset:2560\n\t"
                     "ds_read_b64_tr_b16 a[208:209], v239\n\t" "ds_read_b64_tr_b16 a[210:211], v226 offset:2560\n\t" "ds_read_b64_tr_b16 a[232:233], v237 offset:256\n\t"
                     "ds_read_b64_tr_b16 a[234:235], v238 offset:2816\n\t" "ds_read_b64_tr_b16 v[96:97], v239 offset:256\n\t" "ds_read_b64_tr_b16 v[98:99], v226 offset:2816\n\t"
                     "ds_read_b64_tr_b16 v[120:121], v237 offset:512\n\t" "ds_read_b64_tr_b16 v[122:123], v238 offset:3072\n\t" "ds_read_b64_tr_b16 v[144:145], v239 offset:512\n\t"
                     "ds_read_b64_tr_b16 v[146:147], v226 offset:3072\n\t" "ds_read_b64_tr_b16 v[168:169], v237 offset:768\n\t" "ds_read_b64_tr_b16 v[170:171], v238 offset:3328\n\t"
                     "ds_read_b64_tr_b16 v[192:193], v239 offset:768\n\t" "ds_read_b64_tr_b16 v[194:195], v226 offset:3328\n\t" "ds_read_b64_tr_b16 v[216:217], v237 offset:1024\n\t"
                     "ds_read_b64_tr_b16 v[218:219], v238 offset:3584\n\t" "ds_read_b64_tr_b16 v[80:81], v239 offset:1024\n\t" "ds_read_b64_tr_b16 v[82:83], v226 offset:3584\n\t" "s_waitcnt vmcnt(10) lgkmcnt(0)\n\t"
                     "s_barrier\n\t" "s_add_u32 s42, s46, 0x32000\n\t" "s_addc_u32 s43, s47, 0\n\t" "s_add_u32 m0, %20, 0xc200\n\t" "s_nop 0\n\t" "global_load_lds_dwordx4 v232, s[42:43]\n\t" "s_add_u32 m0, %20, 0xc600\n\t"
                     "s_nop 0\n\t" "global_load_lds_dwordx4 v233, s[42:43]\n\t" "s_add_u32 m0, %20, 0xca00\n\t" "s_nop 0\n\t" "global_load_lds_dwordx4 v234, s[42:43]\n\t" "s_add_u32 m0, %20, 0xce00\n\t" "s_nop 0\n\t"
                     "global_load_lds_dwordx4 v235, s[42:43]\n\t" "s_add_u32 m0, %20, 0xd200\n\t" "s_nop 0\n\t" "global_load_lds_dwordx4 v236, s[42:43]\n\t" "s_add_u32 s42, s46, 0x33400\n\t" "s_addc_u32 s43, s47, 0\n\t"
                     "s_add_u32 m0, %20, 0xd600\n\t" "v_xor_b32 v224, 0x80, v232\n\t" "global_load_lds_dwordx4 v224, s[42:43]\n\t" "s_add_u32 m0, %20, 0xda00\n\t" "v_xor_b32 v224, 0x80, v233\n\t"
                     "global_load_lds_dwordx4 v224, s[42:43]\n\t" "s_add_u32 m0, %20, 0xde00\n\t" "v_xor_b32 v224, 0x80, v234\n\t" "global_load_lds_dwordx4 v224, s[42:43]\n\t" "s_add_u32 m0, %20, 0xe200\n\t"
                     "v_xor_b32 v224, 0x80, v235\n\t" "global_load_lds_dwordx4 v224, s[42:43]\n\t" "s_add_u32 m0, %20, 0xe600\n\t" "v_xor_b32 v224, 0x80, v236\n\t" "global_load_lds_dwordx4 v224, s[42:43]\n\t"
                     "s_add_u32 s42, %21, 0x18400\n\t" "v_xor_b32 v237, 0x0, v229\n\t" "v_add_u32 v237, s42, v237\n\t" "v_xor_b32 v238, 0x40, v229\n\t" "v_add_u32 v238, s42, v238\n\t" "v_xor_b32 v239, 0x80, v229\n\t"
                     "v_add_u32 v239, s42, v239\n\t" "v_xor_b32 v226, 0xc0, v229\n\t" "v_add_u32 v226, s42, v226\n\t" "ds_read_b64_tr_b16 a[188:189], v237\n\t" "ds_read_b64_tr_b16 a[190:191], v238 offset:2560\n\t"
                     "ds_read_b64_tr_b16 a[212:213], v239\n\t" "ds_read_b64_tr_b16 a[214:215], v226 offset:2560\n\t" "ds_read_b64_tr_b16 a[236:237], v237 offset:256\n\t"
                     "ds_read_b64_tr_b16 a[238:239], v238 offset:2816\n\t" "ds_read_b64_tr_b16 v[100:101], v239 offset:256\n\t" "ds_read_b64_tr_b16 v[102:103], v226 offset:2816\n\t"
                     "ds_read_b64_tr_b16 v[124:125], v237 offset:512\n\t" "ds_read_b64_tr_b16 v[126:127], v238 offset:3072\n\t" "ds_read_b64_tr_b16 v[148:149], v239 offset:512\n\t"
                     "ds_read_b64_tr_b16 v[150:151], v226 offset:3072\n\t" "ds_read_b64_tr_b16 v[172:173], v237 offset:768\n\t" "ds_read_b64_tr_b16 v[174:175], v238 offset:3328\n\t"
                     "ds_read_b64_tr_b16 v[196:197], v239 offset:768\n\t" "ds_read_b64_tr_b16 v[198:199], v226 offset:3328\n\t" "ds_read_b64_tr_b16 v[220:221], v237 offset:1024\n\t"
                     "ds_read_b64_tr_b16 v[222:223], v238 offset:3584\n\t" "ds_read_b64_tr_b16 v[84:85], v239 offset:1024\n\t" "ds_read_b64_tr_b16 v[86:87], v226 offset:3584\n\t" "s_waitcnt vmcnt(10) lgkmcnt(0)\n\t"
                     "s_barrier\n\t" "s_add_u32 s42, %21, 0x0\n\t" "v_xor_b32 v237, 0x0, v229\n\t" "v_add_u32 v237, s42, v237\n\t" "v_xor_b32 v238, 0x40, v229\n\t" "v_add_u32 v238, s42, v238\n\t"
                     "v_xor_b32 v239, 0x80, v229\n\t" "v_add_u32 v239, s42, v239\n\t" "v_xor_b32 v226, 0xc0, v229\n\t" "v_add_u32 v226, s42, v226\n\t" "ds_read_b64_tr_b16 a[192:193], v237\n\t"
                     "ds_read_b64_tr_b16 a[194:195], v238 offset:2560\n\t" "ds_read_b64_tr_b16 a[216:217], v239\n\t" "ds_read_b64_tr_b16 a[218:219], v226 offset:2560\n\t"
                     "ds_read_b64_tr_b16 a[240:241], v237 offset:256\n\t" "ds_read_b64_tr_b16 a[242:243], v238 offset:2816\n\t" "ds_read_b64_tr_b16 v[104:105], v239 offset:256\n\t"
                     "ds_read_b64_tr_b16 v[106:107], v226 offset:2816\n\t" "ds_read_b64_tr_b16 v[128:129], v237 offset:512\n\t" "ds_read_b64_tr_b16 v[130:131], v238 offset:3072\n\t"
                     "ds_read_b64_tr_b16 v[152:153], v239 offset:512\n\t" "ds_read_b64_tr_b16 v[154:155], v226 offset:3072\n\t" "ds_read_b64_tr_b16 v[176:177], v237 offset:768\n\t"
                     "ds_read_b64_tr_b16 v[178:179], v238 offset:3328\n\t" "ds_read_b64_tr_b16 v[200:201], v239 offset:768\n\t" "ds_read_b64_tr_b16 v[202:203], v226 offset:3328\n\t"
                     "ds_read_b64_tr_b16 v[64:65], v237 offset:1024\n\t" "ds_read_b64_tr_b16 v[66:67], v238 offset:3584\n\t" "ds_read_b64_tr_b16 v[88:89], v239 offset:1024\n\t"
                     "ds_read_b64_tr_b16 v[90:91], v226 offset:3584\n\t" "s_waitcnt vmcnt(0) lgkmcnt(0)\n\t" "s_barrier\n\t" "s_add_u32 s42, %21, 0xc200\n\t" "v_xor_b32 v237, 0x0, v229\n\t" "v_add_u32 v237, s42, v237\n\t"
                     "v_xor_b32 v238, 0x40, v229\n\t" "v_add_u32 v238, s42, v238\n\t" "v_xor_b32 v239, 0x80, v229\n\t" "v_add_u32 v239, s42, v239\n\t" "v_xor_b32 v226, 0xc0, v229\n\t" "v_add_u32 v226, s42, v226\n\t"
                     "ds_read_b64_tr_b16 a[196:197], v237\n\t" "ds_read_b64_tr_b16 a[198:199], v238 offset:2560\n\t" "ds_read_b64_tr_b16 a[220:221], v239\n\t" "ds_read_b64_tr_b16 a[222:223], v226 offset:2560\n\t"
                     "ds_read_b64_tr_b16 a[244:245], v237 offset:256\n\t" "ds_read_b64_tr_b16 a[246:247], v238 offset:2816\n\t" "ds_read_b64_tr_b16 v[108:109], v239 offset:256\n\t"
                     "ds_read_b64_tr_b16 v[110:111], v226 offset:2816\n\t" "ds_read_b64_tr_b16 v[132:133], v237 offset:512\n\t" "ds_read_b64_tr_b16 v[134:135], v238 offset:3072\n\t"
                     "ds_read_b64_tr_b16 v[156:157], v239 offset:512\n\t" "ds_read_b64_tr_b16 v[158:159], v226 offset:3072\n\t" "ds_read_b64_tr_b16 v[180:181], v237 offset:768\n\t"
                     "ds_read_b64_tr_b16 v[182:183], v238 offset:3328\n\t" "ds_read_b64_tr_b16 v[204:205], v239 offset:768\n\t" "ds_read_b64_tr_b16 v[206:207], v226 offset:3328\n\t"
                     "ds_read_b64_tr_b16 v[68:69], v237 offset:1024\n\t" "ds_read_b64_tr_b16 v[70:71], v238 offset:3584\n\t" "ds_read_b64_tr_b16 v[92:93], v239 offset:1024\n\t"
                     "ds_read_b64_tr_b16 v[94:95], v226 offset:3584\n\t" "s_waitcnt lgkmcnt(0)\n\t" "ds_write_b128 v230, v[0:3]\n\t" "ds_write_b128 v230, v[4:7] offset:1024\n\t"
                     "ds_write_b128 v230, v[8:11] offset:2048\n\t" "ds_write_b128 v230, v[12:15] offset:3072\n\t" "ds_write_b128 v230, v[16:19] offset:4096\n\t" "ds_write_b128 v230, v[20:23] offset:5120\n\t"
                     "ds_write_b128 v230, v[24:27] offset:6144\n\t" "ds_write_b128 v230, v[28:31] offset:7168\n\t" "ds_write_b128 v230, v[32:35] offset:8192\n\t" "ds_write_b128 v230, v[36:39] offset:9216\n\t"
                     "ds_write_b128 v230, v[40:43] offset:10240\n\t" "ds_write_b128 v230, v[44:47] offset:11264\n\t" "ds_write_b128 v230, v[48:51] offset:12288\n\t" "ds_write_b128 v230, v[52:55] offset:13312\n\t"
                     "ds_write_b128 v230, v[56:59] offset:14336\n\t" "ds_write_b128 v230, v[60:63] offset:15360\n\t" "ds_write_b128 v230, v[64:67] offset:16384\n\t" "ds_write_b128 v230, v[68:71] offset:17408\n\t"
                     "ds_write_b128 v230, v[72:75] offset:18432\n\t" "ds_write_b128 v230, v[76:79] offset:19456\n\t" "ds_write_b128 v230, v[80:83] offset:20480\n\t" "ds_write_b128 v230, v[84:87] offset:21504\n\t"
                     "ds_write_b128 v230, v[88:91] offset:22528\n\t" "ds_write_b128 v230, v[92:95] offset:23552\n\t"
                     : "=&{a[0:31]}"(wa[0]), "=&{a[32:63]}"(wa[1]), "=&{a[64:95]}"(wa[2]), "=&{a[96:127]}"(wa[3]), "=&{a[128:159]}"(wa[4]), "=&{a[160:191]}"(wa[5]), "=&{a[192:223]}"(wa[6]), "=&{a[224:255]}"(wa[7]), "=&{v[96:127]}"(wv[0]), "=&{v[128:159]}"(wv[1]), "=&{v[160:191]}"(wv[2]), "=&{v[192:223]}"(wv[3]), "=&v"(y.r), "=&v"(y.g), "=&v"(y.e), "+{v[224:239]}"(pa)
                     : "{s[44:45]}"(p.wv + h * DH * 2 + (size_t)(16 * w) * (HD * 2)), "{s[46:47]}"(p.wo + (size_t)(h * DH + 8 * w) * 1280),
                       "s"(lds + 2 * w * GS), "s"(p.ws + OFF_LAT), "s"(lds + w * 10240), "s"(lds), "s"((h & 4) * 16 * HD * 2)
                     : "v0", "v1", "v2", "v3", "v4", "v5", "v6", "v7", "v8", "v9", "v10", "v11", "v12", "v13", "v14", "v15", "v16", "v17", "v18", "v19", "v20", "v21", "v22", "v23", "v24", "v25", "v26", "v27", "v28", "v29", "v30", "v31", "v32", "v33", "v34", "v35", "v36", "v37", "v38", "v39", "v40", "v41", "v42", "v43", "v44", "v45", "v46", "v47", "v48", "v49", "v50", "v51", "v52", "v53", "v54", "v55", "v56", "v57", "v58", "v59", "v60", "v61", "v62", "v63", "v64", "v65", "v66", "v67", "v68", "v69", "v70", "v71", "v72", "v73", "v74", "v75", "v76", "v77", "v78", "v79", "v80", "v81", "v82", "v83", "v84", "v85", "v86", "v87", "v88", "v89", "v90", "v91", "v92", "v93", "v94", "v95", "s42", "s43", "scc", "memory");
        sd = sync_end(y, p.ws, phi, w == 0);
        if (w == 3) {
            u32 r;
            rdy_add(r, p.ws + OFF_ST);
            asm volatile("s_waitcnt vmcnt(0)" : "+v"(r) : : "memory");
            rdy_end(r, p.ws + OFF_ST, cfg.s1 >> 8 & 255);
        }
        barrier();
    }
    const int lane = lanex(), tid = 64 * w + lane;
    const int n = lane & 15, g = lane >> 4, qq = (lane >> 2) & 3, pp = lane & 3;
    char* const fl = p.ws + OFF_ST;
    const u32 iw = lds + 65536 + ((lane * 8) ^ (lane & 32));
    const u32 qr0 = lds + 65536 + (g >> 1) * 512 + pp * 128 + 64 * (g & 1) + 8 * qq + 32 * (pp >> 1);
    const u32 uo = (u32)((4 * w + (n >> 3)) * 4096 + h * 128 + (n & 7) * 16);
    u32x16 ad = u32x16{(u32)lane * 16 + 1024 + (h & 4) * 256, (lane & 2) ? 0xEEEEu : 0x4444u, lds + (u32)((w & 1) * 24576 + (w >> 1) * 99328 + lane * 16), iw + ((3 * w + (w & 1)) >> 1) * 512,
                       iw + (1 + 3 * (w >> 1)) * 512 + 4 * (w & 1), qr0, qr0 ^ 32, uo + (g & 1) * 8192 + (1 + (g >> 1)) * 65536, uo + (g & 1) * 8192 + (g >> 1) * 8, uo, (u32)lane * 16 + 1024 - (h & 4) * 256, uo, uo, uo, uo, uo};
    asm volatile("" : "+{v[224:239]}"(ad));
    const char* memp = p.ws + mslot(sd, 0);
    int budget = 30000;
    Clock clk; clk.init();
    for (int s = 0; s < NSEG; ++s) {
        int tries = 0;
        u64 bad, tp8, en = ~0ull;
        while (true) {
            const u32 tgt = s ? clk.sched(phi, 0, cfg.G, tries ? 2 : 0) : mt();
            char* up = p.ws + uslot(sd, s);
            asm volatile("ds_read_b128 v[64:67], v226\n\t" "ds_read_b128 v[68:71], v226 offset:1024\n\t" "ds_read_b128 v[72:75], v226 offset:2048\n\t" "ds_read_b128 v[76:79], v226 offset:3072\n\t"
                         "ds_read_b128 v[80:83], v226 offset:4096\n\t" "ds_read_b128 v[84:87], v226 offset:5120\n\t" "ds_read_b128 v[88:91], v226 offset:6144\n\t" "ds_read_b128 v[92:95], v226 offset:7168\n\t"
                         "v_mov_b64 v[40:41], 0\n\t" "v_mov_b64 v[42:43], 0\n\t" "v_mov_b64 v[44:45], 0\n\t" "v_mov_b64 v[46:47], 0\n\t" "v_mov_b64 v[48:49], 0\n\t" "v_mov_b64 v[50:51], 0\n\t" ".p2align 5\n\t" ".Lsp_u:\n\t"
                         "s_memtime vcc\n\t" "s_waitcnt lgkmcnt(0)\n\t" "s_sub_i32 s42, %3, vcc_lo\n\t" "s_cmp_gt_i32 s42, 80\n\t" "s_cbranch_scc1 .Lsp_u\n\t" "s_max_i32 s42, s42, 0\n\t" "s_sub_i32 s42, 92, s42\n\t"
                         "s_and_b32 s42, s42, -4\n\t" "s_getpc_b64 vcc\n\t" "s_add_u32 vcc_lo, vcc_lo, s42\n\t" "s_addc_u32 vcc_hi, vcc_hi, 0\n\t" "s_setpc_b64 vcc\n\t" "s_nop 0\n\t" "s_nop 0\n\t" "s_nop 0\n\t" "s_nop 0\n\t"
                         "s_nop 0\n\t" "s_nop 0\n\t" "s_nop 0\n\t" "s_nop 0\n\t" "s_nop 0\n\t" "s_nop 0\n\t" "s_nop 0\n\t" "s_nop 0\n\t" "s_nop 0\n\t" "s_nop 0\n\t" "s_nop 0\n\t" "s_nop 0\n\t" "s_nop 0\n\t" "s_nop 0\n\t"
                         "s_nop 0\n\t" "s_nop 0\n\t" "global_load_dwordx4 v[0:3], v224, %4 offset:-4096 sc1\n\t" "s_nop 10\n\t" "global_load_dwordx4 v[4:7], v224, %4 offset:-2048 sc1\n\t" "s_nop 15\n\t"
                         "global_load_dwordx4 v[8:11], v234, %4 offset:-3072 sc1\n\t" "s_nop 10\n\t" "global_load_dwordx4 v[12:15], v234, %4 offset:-1024 sc1\n\t" "s_nop 10\n\t" "global_load_dwordx4 v[16:19], v224, %4 sc1\n\t"
                         "s_nop 10\n\t" "global_load_dwordx4 v[20:23], v234, %4 offset:1024 sc1\n\t" "s_nop 10\n\t" "global_load_dwordx4 v[24:27], v224, %4 offset:2048 sc1\n\t" "s_nop 10\n\t"
                         "global_load_dwordx4 v[28:31], v234, %4 offset:3072 sc1\n\t" "s_nop 10\n\t" "global_load_dwordx4 v[32:35], v224, %5 offset:-4096 sc1\n\t" "s_nop 10\n\t"
                         "global_load_dwordx4 v[36:39], v234, %5 offset:-3072 sc1\n\t" "s_waitcnt vmcnt(9)\n\t" "v_smfmac_f32_16x16x64_bf16 v[40:43], v[0:3], a[0:7], v225\n\t"
                         "v_smfmac_f32_16x16x64_bf16 v[44:47], v[0:3], v[64:71], v225\n\t" "ds_read_b128 v[64:67], v226 offset:8192\n\t" "v_smfmac_f32_16x16x64_bf16 v[48:51], v[0:3], a[8:15], v225\n\t"
                         "ds_read_b128 v[68:71], v226 offset:9216\n\t" "v_cmp_eq_u32 vcc, -1, v0\n\t" "s_waitcnt vmcnt(8)\n\t" "v_smfmac_f32_16x16x64_bf16 v[40:43], v[4:7], a[16:23], v225\n\t" "v_mov_b64 v[0:1], 0\n\t"
                         "v_smfmac_f32_16x16x64_bf16 v[44:47], v[4:7], v[72:79], v225\n\t" "s_and_b64 vcc, vcc, %6\n\t" "s_cbranch_scc1 .Lsk_u\n\t" "ds_read_b128 v[72:75], v226 offset:10240\n\t" "v_mov_b64 v[2:3], 0\n\t"
                         "v_smfmac_f32_16x16x64_bf16 v[48:51], v[4:7], a[24:31], v225\n\t" "ds_read_b128 v[76:79], v226 offset:11264\n\t" "s_waitcnt vmcnt(7)\n\t"
                         "v_smfmac_f32_16x16x64_bf16 v[40:43], v[8:11], a[32:39], v225\n\t" "v_mov_b64 v[4:5], 0\n\t" "v_smfmac_f32_16x16x64_bf16 v[44:47], v[8:11], v[80:87], v225\n\t"
                         "ds_read_b128 v[80:83], v226 offset:12288\n\t" "v_mov_b64 v[6:7], 0\n\t" "v_smfmac_f32_16x16x64_bf16 v[48:51], v[8:11], a[40:47], v225\n\t" "ds_read_b128 v[84:87], v226 offset:13312\n\t"
                         "s_waitcnt vmcnt(6)\n\t" "v_smfmac_f32_16x16x64_bf16 v[40:43], v[12:15], a[48:55], v225\n\t" "v_mov_b64 v[8:9], 0\n\t" "v_smfmac_f32_16x16x64_bf16 v[44:47], v[12:15], v[88:95], v225\n\t"
                         "ds_read_b128 v[88:91], v226 offset:14336\n\t" "v_mov_b64 v[10:11], 0\n\t" "v_smfmac_f32_16x16x64_bf16 v[48:51], v[12:15], a[56:63], v225\n\t" "ds_read_b128 v[92:95], v226 offset:15360\n\t"
                         "s_waitcnt vmcnt(5)\n\t" "v_smfmac_f32_16x16x64_bf16 v[40:43], v[16:19], a[64:71], v225\n\t" "v_mov_b64 v[12:13], 0\n\t" "s_waitcnt lgkmcnt(6)\n\t"
                         "v_smfmac_f32_16x16x64_bf16 v[44:47], v[16:19], v[64:71], v225\n\t" "ds_read_b128 v[64:67], v226 offset:16384\n\t" "v_mov_b64 v[14:15], 0\n\t"
                         "v_smfmac_f32_16x16x64_bf16 v[48:51], v[16:19], a[72:79], v225\n\t" "ds_read_b128 v[68:71], v226 offset:17408\n\t" "s_waitcnt vmcnt(4)\n\t"
                         "v_smfmac_f32_16x16x64_bf16 v[40:43], v[20:23], a[80:87], v225\n\t" "v_mov_b64 v[16:17], 0\n\t" "s_waitcnt lgkmcnt(6)\n\t" "v_smfmac_f32_16x16x64_bf16 v[44:47], v[20:23], v[72:79], v225\n\t"
                         "ds_read_b128 v[72:75], v226 offset:18432\n\t" "v_mov_b64 v[18:19], 0\n\t" "v_smfmac_f32_16x16x64_bf16 v[48:51], v[20:23], a[88:95], v225\n\t" "ds_read_b128 v[76:79], v226 offset:19456\n\t"
                         "s_waitcnt vmcnt(3)\n\t" "v_smfmac_f32_16x16x64_bf16 v[40:43], v[24:27], a[96:103], v225\n\t" "v_mov_b64 v[20:21], 0\n\t" "s_waitcnt lgkmcnt(6)\n\t"
                         "v_smfmac_f32_16x16x64_bf16 v[44:47], v[24:27], v[80:87], v225\n\t" "ds_read_b128 v[80:83], v226 offset:20480\n\t" "v_mov_b64 v[22:23], 0\n\t"
                         "v_smfmac_f32_16x16x64_bf16 v[48:51], v[24:27], a[104:111], v225\n\t" "ds_read_b128 v[84:87], v226 offset:21504\n\t" "s_waitcnt vmcnt(2)\n\t"
                         "v_smfmac_f32_16x16x64_bf16 v[40:43], v[28:31], a[112:119], v225\n\t" "v_mov_b64 v[24:25], 0\n\t" "s_waitcnt lgkmcnt(6)\n\t" "v_smfmac_f32_16x16x64_bf16 v[44:47], v[28:31], v[88:95], v225\n\t"
                         "ds_read_b128 v[88:91], v226 offset:22528\n\t" "v_mov_b64 v[26:27], 0\n\t" "v_smfmac_f32_16x16x64_bf16 v[48:51], v[28:31], a[120:127], v225\n\t" "ds_read_b128 v[92:95], v226 offset:23552\n\t"
                         "s_waitcnt vmcnt(1)\n\t" "v_smfmac_f32_16x16x64_bf16 v[40:43], v[32:35], a[128:135], v225\n\t" "v_mov_b64 v[28:29], 0\n\t" "v_smfmac_f32_16x16x64_bf16 v[44:47], v[32:35], a[136:143], v225\n\t"
                         "v_mov_b64 v[30:31], 0\n\t" "v_smfmac_f32_16x16x64_bf16 v[48:51], v[32:35], a[144:151], v225\n\t" "s_waitcnt vmcnt(0)\n\t" "v_smfmac_f32_16x16x64_bf16 v[40:43], v[36:39], a[152:159], v225\n\t"
                         "v_mov_b64 v[32:33], 0\n\t" "v_smfmac_f32_16x16x64_bf16 v[44:47], v[36:39], a[160:167], v225\n\t" "v_mov_b64 v[34:35], 0\n\t" "v_smfmac_f32_16x16x64_bf16 v[48:51], v[36:39], a[168:175], v225\n\t"
                         "v_add_f32 v52, v40, v42\n\t" "v_add_f32 v53, v41, v43\n\t" "v_cmp_u_f32 vcc, v52, v53\n\t" "v_add_f32 v54, v44, v46\n\t" "v_add_f32 v55, v45, v47\n\t" "v_cvt_pk_bf16_f32 v58, v52, v53\n\t"
                         "v_pk_add_f32 v[56:57], v[48:49], v[50:51]\n\t" "v_cvt_pk_bf16_f32 v59, v54, v55\n\t" "v_cvt_pk_bf16_f32 v60, v56, v57\n\t" "ds_write_b64 v227, v[58:59]\n\t" "ds_write_b32 v228, v60\n\t"
                         "v_mov_b64 v[36:37], 0\n\t" "v_mov_b64 v[38:39], 0\n\t" "s_and_b64 vcc, vcc, %6\n\t" "v_mov_b32 v233, 0\n\t" "s_cbranch_scc1 .Lsk_u\n\t" "v_mov_b32 v233, 0\n\t" "v_mov_b32 v233, 0\n\t"
                         "v_mov_b32 v233, 0\n\t" "s_waitcnt lgkmcnt(0)\n\t" "s_barrier\n\t" "ds_read_b64_tr_b16 v[40:41], v229\n\t" "ds_read_b64_tr_b16 v[42:43], v230\n\t" "v_mov_b32 v233, 0\n\t"
                         "ds_read_b64_tr_b16 v[44:45], v229 offset:1024\n\t" "v_mov_b32 v233, 0\n\t" "ds_read_b64_tr_b16 v[46:47], v230 offset:1024\n\t" "v_mov_b32 v233, 0\n\t"
                         "ds_read_b64_tr_b16 v[48:49], v229 offset:2048\n\t" "v_mov_b32 v233, 0\n\t" "ds_read_b64_tr_b16 v[50:51], v230 offset:2048\n\t" "v_mov_b32 v233, 0\n\t" "s_waitcnt lgkmcnt(4)\n\t"
                         "v_smfmac_f32_16x16x64_bf16 v[0:3], v[40:43], a[176:183], v225\n\t" "v_smfmac_f32_16x16x64_bf16 v[4:7], v[40:43], a[200:207], v225\n\t" "s_waitcnt lgkmcnt(2)\n\t"
                         "v_smfmac_f32_16x16x64_bf16 v[0:3], v[44:47], a[184:191], v225\n\t" "v_smfmac_f32_16x16x64_bf16 v[4:7], v[44:47], a[208:215], v225\n\t" "s_waitcnt lgkmcnt(0)\n\t"
                         "v_smfmac_f32_16x16x64_bf16 v[0:3], v[48:51], a[192:199], v225\n\t" "v_smfmac_f32_16x16x64_bf16 v[4:7], v[48:51], a[216:223], v225\n\t"
                         "v_smfmac_f32_16x16x64_bf16 v[8:11], v[40:43], a[224:231], v225\n\t" "v_add_f32 v52, v0, v2\n\t" "v_add_f32 v53, v1, v3\n\t" "v_cvt_pk_bf16_f32 v54, v53, v52\n\t"
                         "v_smfmac_f32_16x16x64_bf16 v[8:11], v[44:47], a[232:239], v225\n\t" "v_add_f32 v52, v4, v6\n\t" "v_add_f32 v53, v5, v7\n\t" "v_cvt_pk_bf16_f32 v55, v53, v52\n\t" "s_nop 1\n\t"
                         "v_permlane16_swap_b32 v54, v55\n\t" "v_smfmac_f32_16x16x64_bf16 v[8:11], v[48:51], a[240:247], v225\n\t" "global_store_dwordx2 v232, v[54:55], %2 sc1\n\t" "s_add_u32 s40, s40, 0x10000\n\t"
                         "s_addc_u32 s41, s41, 0\n\t" "v_smfmac_f32_16x16x64_bf16 v[12:15], v[40:43], a[248:255], v225\n\t" "v_smfmac_f32_16x16x64_bf16 v[12:15], v[44:47], v[96:103], v225\n\t" "v_add_f32 v52, v8, v10\n\t"
                         "v_smfmac_f32_16x16x64_bf16 v[12:15], v[48:51], v[104:111], v225\n\t" "v_add_f32 v53, v9, v11\n\t" "v_smfmac_f32_16x16x64_bf16 v[16:19], v[40:43], v[112:119], v225\n\t"
                         "v_cvt_pk_bf16_f32 v56, v53, v52\n\t" "v_smfmac_f32_16x16x64_bf16 v[16:19], v[44:47], v[120:127], v225\n\t" "v_add_f32 v52, v12, v14\n\t" "v_add_f32 v53, v13, v15\n\t"
                         "v_cvt_pk_bf16_f32 v57, v53, v52\n\t" "s_nop 1\n\t" "v_permlane16_swap_b32 v56, v57\n\t" "v_smfmac_f32_16x16x64_bf16 v[16:19], v[48:51], v[128:135], v225\n\t"
                         "global_store_dwordx2 v232, v[56:57], %2 sc1\n\t" "s_add_u32 s40, s40, 0x10000\n\t" "s_addc_u32 s41, s41, 0\n\t" "v_smfmac_f32_16x16x64_bf16 v[20:23], v[40:43], v[136:143], v225\n\t"
                         "v_smfmac_f32_16x16x64_bf16 v[20:23], v[44:47], v[144:151], v225\n\t" "v_add_f32 v52, v16, v18\n\t" "v_smfmac_f32_16x16x64_bf16 v[20:23], v[48:51], v[152:159], v225\n\t" "v_add_f32 v53, v17, v19\n\t"
                         "v_smfmac_f32_16x16x64_bf16 v[24:27], v[40:43], v[160:167], v225\n\t" "v_cvt_pk_bf16_f32 v58, v53, v52\n\t" "v_smfmac_f32_16x16x64_bf16 v[24:27], v[44:47], v[168:175], v225\n\t"
                         "v_add_f32 v52, v20, v22\n\t" "v_add_f32 v53, v21, v23\n\t" "v_smfmac_f32_16x16x64_bf16 v[24:27], v[48:51], v[176:183], v225\n\t" "v_cvt_pk_bf16_f32 v59, v53, v52\n\t"
                         "v_smfmac_f32_16x16x64_bf16 v[28:31], v[40:43], v[184:191], v225\n\t" "s_nop 0\n\t" "v_permlane16_swap_b32 v58, v59\n\t" "v_smfmac_f32_16x16x64_bf16 v[28:31], v[44:47], v[192:199], v225\n\t"
                         "global_store_dwordx2 v232, v[58:59], %2 sc1\n\t" "v_add_f32 v52, v24, v26\n\t" "v_smfmac_f32_16x16x64_bf16 v[28:31], v[48:51], v[200:207], v225\n\t" "v_add_f32 v53, v25, v27\n\t"
                         "v_smfmac_f32_16x16x64_bf16 v[32:35], v[40:43], v[208:215], v225\n\t" "v_cvt_pk_bf16_f32 v60, v53, v52\n\t" "v_smfmac_f32_16x16x64_bf16 v[32:35], v[44:47], v[216:223], v225\n\t"
                         "v_add_f32 v52, v28, v30\n\t" "v_smfmac_f32_16x16x64_bf16 v[32:35], v[48:51], v[64:71], v225\n\t" "v_add_f32 v53, v29, v31\n\t" "v_smfmac_f32_16x16x64_bf16 v[36:39], v[40:43], v[72:79], v225\n\t"
                         "v_cvt_pk_bf16_f32 v61, v53, v52\n\t" "v_smfmac_f32_16x16x64_bf16 v[36:39], v[44:47], v[80:87], v225\n\t" "v_add_f32 v52, v32, v34\n\t"
                         "v_smfmac_f32_16x16x64_bf16 v[36:39], v[48:51], v[88:95], v225\n\t" "v_add_f32 v53, v33, v35\n\t" "v_cvt_pk_bf16_f32 v62, v53, v52\n\t" "v_mov_b32 v233, 0\n\t" "v_mov_b32 v233, 0\n\t"
                         "v_mov_b32 v233, 0\n\t" "v_mov_b32 v233, 0\n\t" "v_mov_b32 v233, 0\n\t" "v_pk_add_f32 v[52:53], v[36:37], v[38:39]\n\t" "v_cvt_pk_bf16_f32 v63, v53, v52\n\t" "v_permlane32_swap_b32 v60, v62\n\t"
                         "s_nop 0\n\t" "v_permlane32_swap_b32 v61, v63\n\t" "s_nop 1\n\t" "v_permlane16_swap_b32 v60, v61\n\t" "v_permlane16_swap_b32 v62, v63\n\t" "global_store_dwordx4 v231, v[60:63], %2 sc1\n\t"
                         ".Lsk_u:\n\t" "s_memrealtime %1\n\t" "s_mov_b64 %0, vcc\n\t" "s_waitcnt lgkmcnt(0)\n\t"
                         : "=&s"(bad), "=&s"(tp8), "+{s[40:41]}"(up)
                         : "s"(uni((int)tgt)), "s"(memp - 1024), "s"(memp + 15360), "s"(en), "{v[224:239]}"(ad),
                           "{a[0:31]}"(wa[0]), "{a[32:63]}"(wa[1]), "{a[64:95]}"(wa[2]), "{a[96:127]}"(wa[3]), "{a[128:159]}"(wa[4]), "{a[160:191]}"(wa[5]), "{a[192:223]}"(wa[6]), "{a[224:255]}"(wa[7]), "{v[96:127]}"(wv[0]), "{v[128:159]}"(wv[1]), "{v[160:191]}"(wv[2]), "{v[192:223]}"(wv[3])
                         : "v0", "v1", "v2", "v3", "v4", "v5", "v6", "v7", "v8", "v9", "v10", "v11", "v12", "v13", "v14", "v15", "v16", "v17", "v18", "v19", "v20", "v21", "v22", "v23", "v24", "v25", "v26", "v27", "v28", "v29", "v30", "v31", "v32", "v33", "v34", "v35", "v36", "v37", "v38", "v39", "v40", "v41", "v42", "v43", "v44", "v45", "v46", "v47", "v48", "v49", "v50", "v51", "v52", "v53", "v54", "v55", "v56", "v57", "v58", "v59", "v60", "v61", "v62", "v63", "v64", "v65", "v66", "v67", "v68", "v69", "v70", "v71", "v72", "v73", "v74", "v75", "v76", "v77", "v78", "v79", "v80", "v81", "v82", "v83", "v84", "v85", "v86", "v87", "v88", "v89", "v90", "v91", "v92", "v93", "v94", "v95", "vcc", "scc", "s42", "memory");
            if (__builtin_expect(bad == 0, 1)) break;
            waitvm<0>();
            if (--budget < 0 || ((++tries & 15) == 0 && !unwritten(memp, lanex() * 16))) en = 0;
            phi += (u32)cfg.G;
        }
        clk.calib();
        const u32 tt = sched_ld0(s);
        if (s >= NSEG - 2) waitvm<0>();
        barrier();
        if (s >= NSEG - 2 && w == 3) flag_add(fl, F_PROG + 8 * s + (h & 7), 1);
        if (s >= NSEG - 2 && w == 2) flag_add(fl, F_UD + 2 * (s & 1) + (h & 1), 1);
        if (s == 0) {
#pragma unroll
            for (int i = UPF; i < 8; ++i) {
                const int l = 64 * min(i * 4 + w, 29) + lanex();
                pfl(p.wq + h * 384, (u32)((l / 3) * 12288 + (l % 3) * 128), lds);
            }
        }
        if (s == 0) { until(phi + UNAP); phi = go_wg(fl, h, w, lds + SL_PF - 16) - (tt >> 16); edge(clk.r1, clk.m1); clk.calib(); }
        follow(phi, (int)(tt >> 16) + 4 + cfg.T3 - VISS, (u32)tp8, cfg.G);
        phi += tt & 0xffffu;
        memp = p.ws + mslot(sd, s + 1);
    }
    asm volatile(".Lkm_lu:\n\ts_waitcnt vmcnt(0)" : : : "memory");
}

#define VALL "{v[0:3]}"(o), "{v[16:47]}"(rv[0]), "{v[48:79]}"(rv[1]), "{v[80:111]}"(rv[2]), "{v[112:143]}"(rv[3]), "{v[144:175]}"(rv[4]), "{v[176:207]}"(rv[5]), "{v[208:239]}"(rv[6]), "{a[0:31]}"(ra[0]), "{a[32:63]}"(ra[1]), "{a[64:95]}"(ra[2]), "{a[96:127]}"(ra[3])
DI void value_role(const Ptrs& p, const ChainCfg cfg, int vb, int sd, u32 phi, u32 lds, int w) {
    const int lane = lanex(), tid = 64 * w + lane, n = lane & 15, g = lane >> 4;
    const int v = vb;
    const u32 moff = lane * 16;
    char* const fl = p.ws + OFF_ST;
    char* const ub = p.ws + uchunk(v);
    const int xh = (vb >> 2) & 1;
    if (w == 3) {
        const u32x4 ff = u32x4{~0u, ~0u, ~0u, ~0u};
        if (SPL) barrier();
#pragma nounroll
        for (int s = 0; s < NSEG - 2; ++s) {
            dly(s == 1 ? HD1 : cfg.cR3 >> 8 & 255);
            char* const mk = ub + uslot(sd, s + 2);
#pragma unroll
            for (int i = 0; i < 8; ++i) { gst16(mk + i * 1024, moff, ff); gap<HQ>(-1); }
            if (vb < 32 && s) {
                gst16(p.ws + (size_t)(s + 2) * HO_STRIDE + vb * 2048, moff, ff);
                gst16(p.ws + (size_t)(s + 2) * HO_STRIDE + vb * 2048 + 1024, moff, ff);
            }
            if (s == 0) {
                u32 r;
                flag_wait(fl, F_VD + (vb & 3) + (v < 20 ? 0 : 4), 32);
                rdy_add(r, fl);
                asm volatile("s_waitcnt vmcnt(0)" : "+v"(r) : : "memory");
                rdy_end(r, fl, cfg.s1 >> 16);
            }
            waitvm<0>();
            if (SPL && s) barrier();
            barrier();
        }
        return;
    }
    if (w) {
        const u32 dof = (u32)((n >> 3) * 16384 + g * 128 + (n & 7) * 16);
        const char* dp = p.ws + OFF_D + (size_t)v * 32768 - 4096 + w * 8192;
        if (SPL) barrier();
        flag_wait(fl, F_VD + (vb & 3) + (v < 20 ? 0 : 4), 32);
#pragma nounroll
        for (int s = 0; s < NSEG - 1; ++s) {
            u32x4 df[16];
            u32 dfl;
            const int on = uni(1 - s);
            sfor<0, SPL>([&](auto I) { gld16c<I.v * 512 - 4096>(df[I.v], dp, dof); gap<HP0>(on); });
            if (SPL) asm volatile("s_cmp_ge_i32 %0, 0\n\ts_cbranch_scc1 .Lhb\n\ts_barrier\n.Lhb:" : : "s"(on) : "memory", "scc");
            dly(s == 1 ? HD1 : cfg.cR3 & 255);
            sfor<SPL, 16>([&](auto I) { gld16c<I.v * 512 - 4096>(df[I.v], dp, dof); gap<HP>(on); });
            dp += D_STRIDE;
            flag_ldm(dfl, fl, F_VD + pblk(s + 1) * 8 + (vb & RM), pblk(s + 1) != pblk(s));
            const u32 la = lds + moff + (s & 1) * 32768 + (w - 1) * 16384;
            sfor<0, 8>([&](auto I) {
                constexpr int i = I.v;
                waitv<15 - 2 * i>(df[2 * i]); waitv<15 - 2 * i>(df[2 * i + 1]);
                const u32x4 x = df[2 * i], y = df[2 * i + 1];
                const u32 lb = i & 2 ? la - xh * 4096 : la + xh * 4096;
                *LDSP(u32x4, lb + 2048 * i) = u32x4{x[0], y[0], x[2], y[2]};
                *LDSP(u32x4, lb + 2048 * i + 1024) = u32x4{x[1], y[1], x[3], y[3]};
                PIN();
            });
            waitvm<0>();
            asm volatile("" : "+v"(dfl));
            if (uni((int)dfl) < 31) {
                dly(FRD);
                flag_ld(dfl, fl, F_VD + pblk(s + 1) * 8 + (vb & RM));
                asm volatile("s_waitcnt vmcnt(0)" : "+v"(dfl) : : "memory");
            }
            barrier();
            if (uni((int)dfl) < 31) flag_wait(fl, F_VD + pblk(s + 1) * 8 + (vb & RM), 32);
        }
        return;
    }
    u32x32 rv[7], ra[4];
    u32x4 o;
    asm volatile("v_accvgpr_write_b32 a64, 0\n\t" "v_accvgpr_write_b32 a65, 0\n\t" "v_accvgpr_write_b32 a66, 0\n\t" "v_accvgpr_write_b32 a67, 0\n\t" "v_accvgpr_write_b32 a68, 0\n\t" "v_accvgpr_write_b32 a69, 0\n\t"
                 "v_accvgpr_write_b32 a70, 0\n\t" "v_accvgpr_write_b32 a71, 0\n\t" "v_accvgpr_write_b32 a72, 0\n\t" "v_accvgpr_write_b32 a73, 0\n\t" "v_accvgpr_write_b32 a74, 0\n\t" "v_accvgpr_write_b32 a75, 0\n\t"
                 "v_accvgpr_write_b32 a76, 0\n\t" "v_accvgpr_write_b32 a77, 0\n\t" "v_accvgpr_write_b32 a78, 0\n\t" "v_accvgpr_write_b32 a79, 0\n\t" "v_mov_b64 v[112:113], -1\n\t" "v_mov_b64 v[120:121], -1\n\t"
                 "v_mov_b64 v[128:129], -1\n\t" "v_mov_b64 v[136:137], -1\n\t" "v_mov_b64 v[144:145], -1\n\t" "v_mov_b64 v[152:153], -1\n\t" "v_mov_b64 v[160:161], -1\n\t" "v_mov_b64 v[168:169], -1\n\t"
                 "v_mov_b64 v[176:177], -1\n\t" "v_mov_b64 v[184:185], -1\n\t" "v_mov_b64 v[192:193], -1\n\t" "v_mov_b64 v[200:201], -1\n\t" "v_mov_b64 v[208:209], -1\n\t" "v_mov_b64 v[216:217], -1\n\t"
                 "v_mov_b64 v[224:225], -1\n\t" "v_mov_b64 v[232:233], -1\n\t"
                 : "={v[0:3]}"(o), "={v[16:47]}"(rv[0]), "={v[48:79]}"(rv[1]), "={v[80:111]}"(rv[2]), "={v[112:143]}"(rv[3]), "={v[144:175]}"(rv[4]), "={v[176:207]}"(rv[5]), "={v[208:239]}"(rv[6]), "={a[0:31]}"(ra[0]), "={a[32:63]}"(ra[1]), "={a[64:95]}"(ra[2]), "={a[96:127]}"(ra[3]));
    const u32 ho = (u32)(min(lane, 39) * 256 + (lane >> 5) * 8192);
    const u32 pof = (u32)((n & 4) * 128 + (n >> 3) * 256 + g * 64 + (n & 3) * 16 + 2048);
    const u32 uof = (u32)((n >> 3) * 4096 + g * 128 + (n & 7) * 16 + 512);
    u32x8 ad = u32x8{uof + xh * 512, pof + xh * 2048, (u32)(((g >> 1) ^ xh) * 256 + (n >> 2) * 64 + (n & 1) * 16), moff,
                     (lane & 2) ? 0xEEEEu : 0x4444u, (lane & 4) ? 0xEEEEu : 0x4444u, (u32)(v * 256 + (v >> 5) * 8192 + g * 64 + (n >> 3) * 32 + (n & 1) * 16 + (n & 6) * 2),
                     (lane & 1) ? 0x07060302u : 0x01000504u};
    u32 pob = pof - xh * 2048, uob = uof - xh * 512, pmo = moff ^ xh * 512;
    asm volatile("" : "+{v[4:11]}"(ad), "+v"(pob), "+v"(uob), "+v"(pmo));
    auto zero = [&]() {
        asm volatile("v_mov_b64 v[0:1], 0\n\t" "v_mov_b64 v[2:3], 0\n\t" "v_mov_b64 v[48:49], 0\n\t" "v_mov_b64 v[50:51], 0\n\t" "v_mov_b64 v[52:53], 0\n\t" "v_mov_b64 v[54:55], 0\n\t" "v_mov_b64 v[56:57], 0\n\t"
                     "v_mov_b64 v[58:59], 0\n\t" "v_mov_b64 v[60:61], 0\n\t" "v_mov_b64 v[62:63], 0\n\t" "v_mov_b64 v[64:65], 0\n\t" "v_mov_b64 v[66:67], 0\n\t" "v_mov_b64 v[68:69], 0\n\t" "v_mov_b64 v[70:71], 0\n\t"
                     "v_mov_b64 v[72:73], 0\n\t" "v_mov_b64 v[74:75], 0\n\t" "v_mov_b64 v[76:77], 0\n\t" "v_mov_b64 v[78:79], 0\n\t" "v_mov_b64 v[80:81], 0\n\t" "v_mov_b64 v[82:83], 0\n\t" "v_mov_b64 v[84:85], 0\n\t"
                     "v_mov_b64 v[86:87], 0\n\t" "v_mov_b64 v[88:89], 0\n\t" "v_mov_b64 v[90:91], 0\n\t" "v_mov_b64 v[92:93], 0\n\t" "v_mov_b64 v[94:95], 0\n\t" "v_mov_b64 v[96:97], 0\n\t" "v_mov_b64 v[98:99], 0\n\t"
                     "v_mov_b64 v[100:101], 0\n\t" "v_mov_b64 v[102:103], 0\n\t" "v_mov_b64 v[104:105], 0\n\t" "v_mov_b64 v[106:107], 0\n\t" "v_mov_b64 v[108:109], 0\n\t" "v_mov_b64 v[110:111], 0\n\t"
                     : : VALL : "memory");
    };
    const char* memp = p.ws + OFF_XF + 4096;
    char* up = ub + OFF_XF;
    int budget = 30000;
    Clock clk; clk.init();
    u32 tt = 0;
    for (int s = -1; s < NSEG - 1; ++s) {
        const char* const mnx = p.ws + mslot(sd, s + 1);
        bool all = false;
        int tries = 0;
        u64 tp8;
        while (true) {
            const u32 tgt = s >= 0 ? clk.sched(phi, (int)(tt >> 16), cfg.G, tries ? 2 : 1) : mt();
            u64 ok;
            asm volatile("s_cmp_lg_u32 %11, 0\n\t" "s_cbranch_scc0 .Lsp_v0\n\t" ".p2align 5\n\t" ".Lsp_v3:\n\t" "s_memtime vcc\n\t" "s_waitcnt lgkmcnt(0)\n\t" "s_sub_i32 s42, %10, vcc_lo\n\t" "s_cmp_gt_i32 s42, 80\n\t"
                         "s_cbranch_scc1 .Lsp_v3\n\t" "s_max_i32 s42, s42, 0\n\t" "s_sub_i32 s42, 92, s42\n\t" "s_and_b32 s42, s42, -4\n\t" "s_getpc_b64 vcc\n\t" "s_add_u32 vcc_lo, vcc_lo, s42\n\t"
                         "s_addc_u32 vcc_hi, vcc_hi, 0\n\t" "s_setpc_b64 vcc\n\t" "s_nop 0\n\t" "s_nop 0\n\t" "s_nop 0\n\t" "s_nop 0\n\t" "s_nop 0\n\t" "s_nop 0\n\t" "s_nop 0\n\t" "s_nop 0\n\t" "s_nop 0\n\t" "s_nop 0\n\t"
                         "s_nop 0\n\t" "s_nop 0\n\t" "s_nop 0\n\t" "s_nop 0\n\t" "s_nop 0\n\t" "s_nop 0\n\t" "s_nop 0\n\t" "s_nop 0\n\t" "s_nop 0\n\t" "s_nop 0\n\t"
                         "global_load_dwordx4 a[0:3], v5, %7 offset:-4096 sc1\n\t" "global_load_dwordx4 a[4:7], v5, %7 offset:-3072 sc1\n\t" "global_load_dwordx4 a[16:19], v5, %7 sc1\n\t"
                         "global_load_dwordx4 a[20:23], v5, %7 offset:1024 sc1\n\t" "global_load_dwordx4 a[32:35], v5, %8 offset:-4096 sc1\n\t" "global_load_dwordx4 a[36:39], v5, %8 offset:-3072 sc1\n\t"
                         "global_load_dwordx4 a[48:51], v5, %8 sc1\n\t" "global_load_dwordx4 a[52:55], v5, %8 offset:1024 sc1\n\t" "s_nop 14\n\t" "global_load_dwordx4 a[8:11], %12, %7 offset:-2048 sc1\n\t"
                         "global_load_dwordx4 a[12:15], %12, %7 offset:-1024 sc1\n\t" "global_load_dwordx4 a[24:27], %12, %7 offset:2048 sc1\n\t" "global_load_dwordx4 a[28:31], %12, %7 offset:3072 sc1\n\t"
                         "global_load_dwordx4 a[40:43], %12, %8 offset:-2048 sc1\n\t" "global_load_dwordx4 a[44:47], %12, %8 offset:-1024 sc1\n\t" "global_load_dwordx4 a[56:59], %12, %8 offset:2048 sc1\n\t"
                         "global_load_dwordx4 a[60:63], %12, %8 offset:3072 sc1\n\t" "s_nop 15\n\t" "s_nop 15\n\t" "s_nop 15\n\t" "s_nop 8\n\t" "global_load_dwordx4 a[80:83], %14, %6 sc1\n\t"
                         "global_load_dwordx4 a[84:87], %14, %6 offset:1024 sc1\n\t" "global_load_dwordx4 a[88:91], %14, %6 offset:2048 sc1\n\t" "global_load_dwordx4 a[92:95], %14, %6 offset:3072 sc1\n\t"
                         "s_mov_b64 exec, %9\n\t" "global_load_dwordx4 a[64:67], v6, %6 offset:-2048 sc1\n\t" "global_load_dwordx4 a[68:71], v6, %6 offset:-1536 sc1\n\t"
                         "global_load_dwordx4 a[72:75], v6, %6 offset:-1024 sc1\n\t" "global_load_dwordx4 a[76:79], v6, %6 offset:-512 sc1\n\t" "s_mov_b64 exec, -1\n\t" "s_mov_b32 s42, %11\n\t" ".Lsp_v5:\n\t"
                         "s_sub_u32 s42, s42, 1\n\t" "s_cmp_gt_i32 s42, 0\n\t" "s_cbranch_scc1 .Lsp_v5\n\t" "global_load_dwordx4 a[96:99], v4, %3 offset:-512 sc1\n\t" "global_load_dwordx4 a[100:103], %13, %3 sc1\n\t"
                         "global_load_dwordx4 a[104:107], v4, %3 offset:512 sc1\n\t" "global_load_dwordx4 a[108:111], %13, %3 offset:1024 sc1\n\t" "global_load_dwordx4 a[112:115], v4, %3 offset:1536 sc1\n\t"
                         "global_load_dwordx4 a[116:119], %13, %3 offset:2048 sc1\n\t" "global_load_dwordx4 a[120:123], v4, %3 offset:2560 sc1\n\t" "global_load_dwordx4 a[124:127], %13, %3 offset:3072 sc1\n\t"
                         "s_waitcnt vmcnt(31)\n\t" "v_smfmac_f32_16x16x64_bf16 v[48:51], a[0:3], v[112:119], v9\n\t" "s_waitcnt vmcnt(30)\n\t" "v_smfmac_f32_16x16x64_bf16 v[52:55], a[4:7], v[120:127], v9\n\t"
                         "s_waitcnt vmcnt(29)\n\t" "v_smfmac_f32_16x16x64_bf16 v[64:67], a[16:19], v[144:151], v9\n\t" "s_waitcnt vmcnt(28)\n\t" "v_cvt_pk_bf16_f32 v16, v48, v49\n\t" "v_cvt_pk_bf16_f32 v17, v50, v51\n\t"
                         "v_smfmac_f32_16x16x64_bf16 v[68:71], a[20:23], v[152:159], v9\n\t" "s_waitcnt vmcnt(27)\n\t" "v_cvt_pk_bf16_f32 v18, v52, v53\n\t" "v_cvt_pk_bf16_f32 v19, v54, v55\n\t"
                         "v_smfmac_f32_16x16x64_bf16 v[80:83], a[32:35], v[176:183], v9\n\t" "s_waitcnt vmcnt(26)\n\t" "v_cvt_pk_bf16_f32 v24, v64, v65\n\t" "v_cvt_pk_bf16_f32 v25, v66, v67\n\t"
                         "v_smfmac_f32_16x16x64_bf16 v[84:87], a[36:39], v[184:191], v9\n\t" "s_waitcnt vmcnt(25)\n\t" "v_cvt_pk_bf16_f32 v26, v68, v69\n\t" "v_cvt_pk_bf16_f32 v27, v70, v71\n\t"
                         "v_smfmac_f32_16x16x64_bf16 v[96:99], a[48:51], v[208:215], v9\n\t" "s_waitcnt vmcnt(24)\n\t" "v_cvt_pk_bf16_f32 v32, v80, v81\n\t" "v_cvt_pk_bf16_f32 v33, v82, v83\n\t"
                         "v_smfmac_f32_16x16x64_bf16 v[100:103], a[52:55], v[216:223], v9\n\t" "s_waitcnt vmcnt(23)\n\t" "v_cvt_pk_bf16_f32 v34, v84, v85\n\t" "v_cvt_pk_bf16_f32 v35, v86, v87\n\t"
                         "v_smfmac_f32_16x16x64_bf16 v[56:59], a[8:11], v[128:135], v9\n\t" "s_waitcnt vmcnt(22)\n\t" "v_cvt_pk_bf16_f32 v40, v96, v97\n\t" "v_cvt_pk_bf16_f32 v41, v98, v99\n\t"
                         "v_smfmac_f32_16x16x64_bf16 v[60:63], a[12:15], v[136:143], v9\n\t" "s_waitcnt vmcnt(21)\n\t" "v_cvt_pk_bf16_f32 v42, v100, v101\n\t" "v_cvt_pk_bf16_f32 v43, v102, v103\n\t"
                         "v_smfmac_f32_16x16x64_bf16 v[72:75], a[24:27], v[160:167], v9\n\t" "s_waitcnt vmcnt(20)\n\t" "v_cvt_pk_bf16_f32 v20, v56, v57\n\t" "v_cvt_pk_bf16_f32 v21, v58, v59\n\t"
                         "v_smfmac_f32_16x16x64_bf16 v[76:79], a[28:31], v[168:175], v9\n\t" "s_waitcnt vmcnt(19)\n\t" "v_cvt_pk_bf16_f32 v22, v60, v61\n\t" "v_cvt_pk_bf16_f32 v23, v62, v63\n\t"
                         "v_smfmac_f32_16x16x64_bf16 v[88:91], a[40:43], v[192:199], v9\n\t" "s_waitcnt vmcnt(18)\n\t" "v_cvt_pk_bf16_f32 v28, v72, v73\n\t" "v_cvt_pk_bf16_f32 v29, v74, v75\n\t"
                         "v_smfmac_f32_16x16x64_bf16 v[92:95], a[44:47], v[200:207], v9\n\t" "s_waitcnt vmcnt(17)\n\t" "v_cvt_pk_bf16_f32 v30, v76, v77\n\t" "v_cvt_pk_bf16_f32 v31, v78, v79\n\t"
                         "v_smfmac_f32_16x16x64_bf16 v[104:107], a[56:59], v[224:231], v9\n\t" "s_waitcnt vmcnt(16)\n\t" "v_cvt_pk_bf16_f32 v36, v88, v89\n\t" "v_cvt_pk_bf16_f32 v37, v90, v91\n\t"
                         "v_smfmac_f32_16x16x64_bf16 v[108:111], a[60:63], v[232:239], v9\n\t" "s_waitcnt vmcnt(11)\n\t" "v_cvt_pk_bf16_f32 v38, v92, v93\n\t" "v_cvt_pk_bf16_f32 v39, v94, v95\n\t"
                         "v_smfmac_f32_16x16x64_bf16 v[0:3], a[64:67], v[16:23], v8\n\t" "s_waitcnt vmcnt(10)\n\t" "v_cvt_pk_bf16_f32 v44, v104, v105\n\t" "v_cvt_pk_bf16_f32 v45, v106, v107\n\t"
                         "v_smfmac_f32_16x16x64_bf16 v[0:3], a[68:71], v[24:31], v8\n\t" "s_waitcnt vmcnt(9)\n\t" "v_cvt_pk_bf16_f32 v46, v108, v109\n\t" "v_cvt_pk_bf16_f32 v47, v110, v111\n\t"
                         "v_smfmac_f32_16x16x64_bf16 v[0:3], a[72:75], v[32:39], v8\n\t" "s_waitcnt vmcnt(8)\n\t" "v_smfmac_f32_16x16x64_bf16 v[0:3], a[76:79], v[40:47], v8\n\t" "s_waitcnt vmcnt(6)\n\t"
                         "v_smfmac_f32_16x16x64_bf16 v[0:3], a[80:83], a[96:103], v8\n\t" "s_waitcnt vmcnt(4)\n\t" "v_smfmac_f32_16x16x64_bf16 v[0:3], a[84:87], a[104:111], v8\n\t" "s_waitcnt vmcnt(2)\n\t"
                         "v_smfmac_f32_16x16x64_bf16 v[0:3], a[88:91], a[112:119], v8\n\t" "s_waitcnt vmcnt(0)\n\t" "v_smfmac_f32_16x16x64_bf16 v[0:3], a[92:95], a[120:127], v8\n\t" "s_branch .Lv_end\n\t"
                         ".p2align 5\n\t" "s_nop 0\n\t" "s_nop 0\n\t" "s_nop 0\n\t" ".Lsp_v0:\n\t" "s_memtime vcc\n\t" "s_waitcnt lgkmcnt(0)\n\t" "s_sub_i32 s42, %10, vcc_lo\n\t" "s_cmp_gt_i32 s42, 80\n\t"
                         "s_cbranch_scc1 .Lsp_v0\n\t" "s_max_i32 s42, s42, 0\n\t" "s_sub_i32 s42, 92, s42\n\t" "s_and_b32 s42, s42, -4\n\t" "s_getpc_b64 vcc\n\t" "s_add_u32 vcc_lo, vcc_lo, s42\n\t"
                         "s_addc_u32 vcc_hi, vcc_hi, 0\n\t" "s_setpc_b64 vcc\n\t" "s_nop 0\n\t" "s_nop 0\n\t" "s_nop 0\n\t" "s_nop 0\n\t" "s_nop 0\n\t" "s_nop 0\n\t" "s_nop 0\n\t" "s_nop 0\n\t" "s_nop 0\n\t" "s_nop 0\n\t"
                         "s_nop 0\n\t" "s_nop 0\n\t" "s_nop 0\n\t" "s_nop 0\n\t" "s_nop 0\n\t" "s_nop 0\n\t" "s_nop 0\n\t" "s_nop 0\n\t" "s_nop 0\n\t" "s_nop 0\n\t"
                         "global_load_dwordx4 a[96:99], v4, %3 offset:-512 sc1\n\t" "global_load_dwordx4 a[104:107], v4, %3 offset:512 sc1\n\t" "global_load_dwordx4 a[112:115], v4, %3 offset:1536 sc1\n\t"
                         "global_load_dwordx4 a[120:123], v4, %3 offset:2560 sc1\n\t" "global_load_dwordx4 a[100:103], %13, %3 sc1\n\t" "global_load_dwordx4 a[108:111], %13, %3 offset:1024 sc1\n\t"
                         "global_load_dwordx4 a[116:119], %13, %3 offset:2048 sc1\n\t" "global_load_dwordx4 a[124:127], %13, %3 offset:3072 sc1\n\t" "s_nop 9\n\t"
                         "global_load_dwordx4 a[0:3], v5, %7 offset:-4096 sc1\n\t" "global_load_dwordx4 a[4:7], v5, %7 offset:-3072 sc1\n\t" "global_load_dwordx4 a[16:19], v5, %7 sc1\n\t"
                         "global_load_dwordx4 a[20:23], v5, %7 offset:1024 sc1\n\t" "global_load_dwordx4 a[32:35], v5, %8 offset:-4096 sc1\n\t" "global_load_dwordx4 a[36:39], v5, %8 offset:-3072 sc1\n\t"
                         "global_load_dwordx4 a[48:51], v5, %8 sc1\n\t" "global_load_dwordx4 a[52:55], v5, %8 offset:1024 sc1\n\t" "s_nop 14\n\t" "global_load_dwordx4 a[8:11], %12, %7 offset:-2048 sc1\n\t"
                         "global_load_dwordx4 a[12:15], %12, %7 offset:-1024 sc1\n\t" "global_load_dwordx4 a[24:27], %12, %7 offset:2048 sc1\n\t" "global_load_dwordx4 a[28:31], %12, %7 offset:3072 sc1\n\t"
                         "global_load_dwordx4 a[40:43], %12, %8 offset:-2048 sc1\n\t" "global_load_dwordx4 a[44:47], %12, %8 offset:-1024 sc1\n\t" "global_load_dwordx4 a[56:59], %12, %8 offset:2048 sc1\n\t"
                         "global_load_dwordx4 a[60:63], %12, %8 offset:3072 sc1\n\t" "s_nop 15\n\t" "s_nop 15\n\t" "s_nop 15\n\t" "s_nop 8\n\t" "global_load_dwordx4 a[80:83], %14, %6 sc1\n\t"
                         "global_load_dwordx4 a[84:87], %14, %6 offset:1024 sc1\n\t" "global_load_dwordx4 a[88:91], %14, %6 offset:2048 sc1\n\t" "global_load_dwordx4 a[92:95], %14, %6 offset:3072 sc1\n\t"
                         "s_mov_b64 exec, %9\n\t" "global_load_dwordx4 a[64:67], v6, %6 offset:-2048 sc1\n\t" "global_load_dwordx4 a[68:71], v6, %6 offset:-1536 sc1\n\t"
                         "global_load_dwordx4 a[72:75], v6, %6 offset:-1024 sc1\n\t" "global_load_dwordx4 a[76:79], v6, %6 offset:-512 sc1\n\t" "s_mov_b64 exec, -1\n\t" "s_waitcnt vmcnt(23)\n\t"
                         "v_smfmac_f32_16x16x64_bf16 v[48:51], a[0:3], v[112:119], v9\n\t" "s_waitcnt vmcnt(22)\n\t" "v_smfmac_f32_16x16x64_bf16 v[52:55], a[4:7], v[120:127], v9\n\t" "s_waitcnt vmcnt(21)\n\t"
                         "v_smfmac_f32_16x16x64_bf16 v[64:67], a[16:19], v[144:151], v9\n\t" "s_waitcnt vmcnt(20)\n\t" "v_cvt_pk_bf16_f32 v16, v48, v49\n\t" "v_cvt_pk_bf16_f32 v17, v50, v51\n\t"
                         "v_smfmac_f32_16x16x64_bf16 v[68:71], a[20:23], v[152:159], v9\n\t" "s_waitcnt vmcnt(19)\n\t" "v_cvt_pk_bf16_f32 v18, v52, v53\n\t" "v_cvt_pk_bf16_f32 v19, v54, v55\n\t"
                         "v_smfmac_f32_16x16x64_bf16 v[80:83], a[32:35], v[176:183], v9\n\t" "s_waitcnt vmcnt(18)\n\t" "v_cvt_pk_bf16_f32 v24, v64, v65\n\t" "v_cvt_pk_bf16_f32 v25, v66, v67\n\t"
                         "v_smfmac_f32_16x16x64_bf16 v[84:87], a[36:39], v[184:191], v9\n\t" "s_waitcnt vmcnt(17)\n\t" "v_cvt_pk_bf16_f32 v26, v68, v69\n\t" "v_cvt_pk_bf16_f32 v27, v70, v71\n\t"
                         "v_smfmac_f32_16x16x64_bf16 v[96:99], a[48:51], v[208:215], v9\n\t" "s_waitcnt vmcnt(16)\n\t" "v_cvt_pk_bf16_f32 v32, v80, v81\n\t" "v_cvt_pk_bf16_f32 v33, v82, v83\n\t"
                         "v_smfmac_f32_16x16x64_bf16 v[100:103], a[52:55], v[216:223], v9\n\t" "s_waitcnt vmcnt(15)\n\t" "v_cvt_pk_bf16_f32 v34, v84, v85\n\t" "v_cvt_pk_bf16_f32 v35, v86, v87\n\t"
                         "v_smfmac_f32_16x16x64_bf16 v[56:59], a[8:11], v[128:135], v9\n\t" "s_waitcnt vmcnt(14)\n\t" "v_cvt_pk_bf16_f32 v40, v96, v97\n\t" "v_cvt_pk_bf16_f32 v41, v98, v99\n\t"
                         "v_smfmac_f32_16x16x64_bf16 v[60:63], a[12:15], v[136:143], v9\n\t" "s_waitcnt vmcnt(13)\n\t" "v_cvt_pk_bf16_f32 v42, v100, v101\n\t" "v_cvt_pk_bf16_f32 v43, v102, v103\n\t"
                         "v_smfmac_f32_16x16x64_bf16 v[72:75], a[24:27], v[160:167], v9\n\t" "s_waitcnt vmcnt(12)\n\t" "v_cvt_pk_bf16_f32 v20, v56, v57\n\t" "v_cvt_pk_bf16_f32 v21, v58, v59\n\t"
                         "v_smfmac_f32_16x16x64_bf16 v[76:79], a[28:31], v[168:175], v9\n\t" "s_waitcnt vmcnt(11)\n\t" "v_cvt_pk_bf16_f32 v22, v60, v61\n\t" "v_cvt_pk_bf16_f32 v23, v62, v63\n\t"
                         "v_smfmac_f32_16x16x64_bf16 v[88:91], a[40:43], v[192:199], v9\n\t" "s_waitcnt vmcnt(10)\n\t" "v_cvt_pk_bf16_f32 v28, v72, v73\n\t" "v_cvt_pk_bf16_f32 v29, v74, v75\n\t"
                         "v_smfmac_f32_16x16x64_bf16 v[92:95], a[44:47], v[200:207], v9\n\t" "s_waitcnt vmcnt(9)\n\t" "v_cvt_pk_bf16_f32 v30, v76, v77\n\t" "v_cvt_pk_bf16_f32 v31, v78, v79\n\t"
                         "v_smfmac_f32_16x16x64_bf16 v[104:107], a[56:59], v[224:231], v9\n\t" "s_waitcnt vmcnt(8)\n\t" "v_cvt_pk_bf16_f32 v36, v88, v89\n\t"
                         "v_smfmac_f32_16x16x64_bf16 v[108:111], a[60:63], v[232:239], v9\n\t" "s_waitcnt vmcnt(7)\n\t" "v_cvt_pk_bf16_f32 v37, v90, v91\n\t"
                         "v_smfmac_f32_16x16x64_bf16 v[0:3], a[80:83], a[96:103], v8\n\t" "s_waitcnt vmcnt(6)\n\t" "v_cvt_pk_bf16_f32 v38, v92, v93\n\t" "v_smfmac_f32_16x16x64_bf16 v[0:3], a[84:87], a[104:111], v8\n\t"
                         "s_waitcnt vmcnt(5)\n\t" "v_cvt_pk_bf16_f32 v39, v94, v95\n\t" "v_smfmac_f32_16x16x64_bf16 v[0:3], a[88:91], a[112:119], v8\n\t" "s_waitcnt vmcnt(4)\n\t" "v_cvt_pk_bf16_f32 v44, v104, v105\n\t"
                         "v_smfmac_f32_16x16x64_bf16 v[0:3], a[92:95], a[120:127], v8\n\t" "s_waitcnt vmcnt(3)\n\t" "v_cvt_pk_bf16_f32 v45, v106, v107\n\t" "v_smfmac_f32_16x16x64_bf16 v[0:3], a[64:67], v[16:23], v8\n\t"
                         "s_waitcnt vmcnt(2)\n\t" "v_cvt_pk_bf16_f32 v46, v108, v109\n\t" "v_smfmac_f32_16x16x64_bf16 v[0:3], a[68:71], v[24:31], v8\n\t" "s_waitcnt vmcnt(1)\n\t" "v_cvt_pk_bf16_f32 v47, v110, v111\n\t"
                         "v_smfmac_f32_16x16x64_bf16 v[0:3], a[72:75], v[32:39], v8\n\t" "s_waitcnt vmcnt(0)\n\t" "v_smfmac_f32_16x16x64_bf16 v[0:3], a[76:79], v[40:47], v8\n\t" ".Lv_end:\n\t" "s_nop 7\n\t"
                         "v_pk_add_f32 v[0:1], v[0:1], v[2:3]\n\t" "v_cvt_pk_bf16_f32 v2, v0, v1\n\t" "v_cmp_u_f32 vcc, v0, v1\n\t" "s_cmp_eq_u64 vcc, 0\n\t"
                         "v_mov_b32_dpp v3, v2 quad_perm:[1,0,3,2] row_mask:0xf bank_mask:0xf\n\t" "s_cselect_b64 %0, -1, 0\n\t" "v_perm_b32 v0, v2, v3, v11\n\t" "s_mov_b64 exec, %0\n\t"
                         "global_store_dword v10, v0, %5 offset:-4096 sc1\n\t" "global_store_dword v10, v0, %4 offset:-4096 sc1\n\t" "s_memrealtime %1\n\t" "s_mov_b64 exec, -1\n\t" "s_waitcnt lgkmcnt(0)\n\t"
                         : "=&s"(ok), "=&s"(tp8)
                         : "{v[4:11]}"(ad), "s"(up), "s"(mnx), "s"((u64)mnx ^ 8192), "s"(memp + 16384), "s"(memp + 32768 - 2048), "s"(memp + 49152 - 2048), "s"(0xc30000c3c30000c3ull),
                           "s"(uni((int)(tgt - (u32)(v < 16 ? cfg.dU - (v >> 3) * cfg.s2 : cfg.cR2)))), "s"(v < 16 ? 0 : (v < 24 ? 1 : cfg.T3)), "v"(pob), "v"(uob), "v"(pmo), "s"(tries), VALL : "memory", "vcc", "scc", "s42");
            if (__builtin_expect(ok != 0, 1) || s < 0) break;
            if (all || --budget < 0) {
                asm volatile("v_min_u32 v0, -2, v0\n\tglobal_store_dword v10, v0, %0 offset:-4096 sc1\n\tglobal_store_dword v10, v0, %1 offset:-4096 sc1"
                             : : "s"(mnx), "s"((u64)mnx ^ 8192), "{v[4:11]}"(ad), VALL : "memory");
                break;
            }
            if ((++tries & 15) == 0) {
                u32 m = 0;
#pragma nounroll
                for (int i = 0; i < 32; ++i) {
                    u32x4 t;
                    gld16c<0>(t, i < 24 ? memp + 12288 : up, i < 20 ? (4 + i + ((4 + i) & 24)) * 1024 + ad[3] : (i < 24 ? i * 512 - 8192 + ad[2] : (i & 7) * 512 - 512 + (i & 1 ? uob : ad[0]))); waitv<0>(t);
                    m = max3u(max3u(m, t[0], t[1]), t[2], t[3]);
                }
                all = __builtin_amdgcn_ballot_w64(m == 0xffffffffu) == 0;
            }
            phi += (u32)cfg.G;
            zero();
        }
        asm volatile("s_barrier\n\t" "ds_read_b128 v[112:115], %0\n\t" "ds_read_b128 v[116:119], %0 offset:1024\n\t" "ds_read_b128 v[120:123], %0 offset:2048\n\t" "ds_read_b128 v[124:127], %0 offset:3072\n\t"
                     "ds_read_b128 v[128:131], %0 offset:4096\n\t" "ds_read_b128 v[132:135], %0 offset:5120\n\t" "ds_read_b128 v[136:139], %0 offset:6144\n\t" "ds_read_b128 v[140:143], %0 offset:7168\n\t"
                     "ds_read_b128 v[144:147], %0 offset:8192\n\t" "ds_read_b128 v[148:151], %0 offset:9216\n\t" "ds_read_b128 v[152:155], %0 offset:10240\n\t" "ds_read_b128 v[156:159], %0 offset:11264\n\t"
                     "ds_read_b128 v[160:163], %0 offset:12288\n\t" "ds_read_b128 v[164:167], %0 offset:13312\n\t" "ds_read_b128 v[168:171], %0 offset:14336\n\t" "ds_read_b128 v[172:175], %0 offset:15360\n\t"
                     "ds_read_b128 v[176:179], %0 offset:16384\n\t" "ds_read_b128 v[180:183], %0 offset:17408\n\t" "ds_read_b128 v[184:187], %0 offset:18432\n\t" "ds_read_b128 v[188:191], %0 offset:19456\n\t"
                     "ds_read_b128 v[192:195], %0 offset:20480\n\t" "ds_read_b128 v[196:199], %0 offset:21504\n\t" "ds_read_b128 v[200:203], %0 offset:22528\n\t" "ds_read_b128 v[204:207], %0 offset:23552\n\t"
                     "ds_read_b128 v[208:211], %0 offset:24576\n\t" "ds_read_b128 v[212:215], %0 offset:25600\n\t" "ds_read_b128 v[216:219], %0 offset:26624\n\t" "ds_read_b128 v[220:223], %0 offset:27648\n\t"
                     "ds_read_b128 v[224:227], %0 offset:28672\n\t" "ds_read_b128 v[228:231], %0 offset:29696\n\t" "ds_read_b128 v[232:235], %0 offset:30720\n\t" "ds_read_b128 v[236:239], %0 offset:31744\n\t"
                     : : "v"(lds + ad[3] + ((s + 1) & 1) * 32768), VALL : "memory");
        if (s >= 0) {
            follow(phi, (int)(tt & 0xffffu) + 5 * (v >> 2) / 2 - (cfg.dV >> 16 & 255), (u32)tp8, cfg.G);
            if (vb == NVB - 1 && lane < 8) gst8(fl, (F_CUR + lane) * 128, u32x2{(u32)s - 1, phi + (tt & 0xffffu)});
        }
        phi += tt & 0xffffu;
        memp = mnx; up = ub + uslot(sd, s + 1);
        zero();
        tt = sched_ld0(s + 1);
        if (s < 0) { phi = go_wait(fl, vb) - (tt >> 16); edge(clk.r1, clk.m1); }
        clk.calib();
        while (s >= 0) {
            u32 h0, h1;
            spin_to(clk.at(phi + (u32)(cfg.dV >> 8 & 255)));
            asm volatile("global_load_dword %0, %1, %2 offset:-4096 sc1" : "=&v"(h1) : "v"(ho & 0xfff), "s"(memp) : "memory");
            spin_to(clk.at(phi + (u32)(cfg.dV & 255)));
            asm volatile("global_load_dword %0, %1, %2 offset:-4096 sc1\n\ts_sleep 5" : "=&v"(h0) : "v"(ho), "s"(memp) : "memory");
            edge(clk.r1, clk.m1);
            asm volatile("s_waitcnt vmcnt(0)" : "+v"(h0), "+v"(h1) : : "memory");
            h0 = max(h0, h1);
            if (__builtin_amdgcn_ballot_w64(h0 == 0xffffffffu) == 0 || --budget < 0) break;
            phi += (u32)cfg.G;
        }
        asm volatile(".Lkm_lv:");
    }
}

DI void xcopy_task(const Ptrs& p, int j, int w) {
    const int tid = 64 * w + lanex();
    Sync y;
    sync_ld(y, p.ws);
    u32x4 xv[10];
    sfor<0, 10>([&](auto I) { gld16<I.v * 128>(xv[I.v], p.x + (size_t)j * 40960, (u32)((tid & 31) * 1280 + (tid >> 5) * 16)); });
    sfor<0, 10>([&](auto I) { waitv<9 - I.v>(xv[I.v]); });
    asm volatile("" : "+v"(y.r), "+v"(y.g), "+v"(y.e));
    sfor<0, 10>([&](auto I) { gst16(p.ws + OFF_XF + (size_t)j * 40960 + I.v * 4096, (u32)tid * 16, xv[I.v]); });
    waitvm<0>();
    barrier();
    const u64 t = sync_tag(y.e);
    if (tid == 0) gst8(p.ws + OFF_LAT, 1536 + (u32)j * 8, u32x2{(u32)t, (u32)(t >> 32)});
}
DI void xcopy_wait(const char* ws, u64 t, int lo = 0, int msk = 63) {
#pragma nounroll
    for (int it = 0; it < (1 << 16); ++it) {
        u32x2 r;
        asm volatile("s_nop 4\n\tglobal_load_dwordx2 %0, %1, %2 offset:1536 sc1\n\ts_waitcnt vmcnt(0)" : "=&v"(r) : "v"((lo + (lanex() & msk)) * 8), "s"(ws + OFF_LAT) : "memory");
        if (__builtin_amdgcn_ballot_w64(r[0] != (u32)t || r[1] != (u32)(t >> 32)) == 0) break;
        __builtin_amdgcn_s_sleep(XSL);
    }
}
DI void init_task(const Ptrs& p, int j, u32 lds, int w) {
    const int lane = lanex(), tid = 64 * w + lane;
    Sync y;
    sync_ld(y, p.ws);
    const u32x4 ff = u32x4{~0u, ~0u, ~0u, ~0u};
    const int i = j * 256 + tid, r = j < 8 ? j & 1 : min(j / 2 - 2, NREG - 1);
    char* const vb = p.ws + (r < 2 ? 0 : OFF_U - (4u << 20)) + ((size_t)r << 21) + 1984 * 1024 + (j < 8 ? j & 6 : (j & 1) * 2 + (j >> 1 == 15) * 4) * 8192;
    const u32 vo = (u32)((w >> 1) * 8192 + (w & 1) * 1024);
    gst16(vb, vo + lane * 16, ff);
    if (i < F_N * 8) gst16(p.ws + OFF_ST, (u32)i * 16, u32x4{i == F_ONE * 8 ? 1000u : ~0u, ~0u, ~0u, ~0u});
    if (i < 512) gst16(p.ws + OFF_GATE, (u32)i * 16, ff);
    gst16(p.ws + 65536, (u32)i * 16, ff);
    if (i < 4096) {
        const int k = (i & 511) + ((i >> 1) & 0x600);
        u32x4 m;
        gld16<0>(m, p.mem0, (u32)((2 * ((k >> 2) & 3) + (k & 1)) * 1280 + min(2 * (k >> 4) + ((k >> 1) & 1), 79) * 16));
        waitv<0>(m);
        gst16(p.ws, (u32)i * 16, k < 640 ? u32x4{min(m[0], 0xfffffffeu), min(m[1], 0xfffffffeu), min(m[2], 0xfffffffeu), min(m[3], 0xfffffffeu)} : ff);
    }
#pragma unroll
    for (int c = 0; c < 6; ++c) gst16(p.ws + OFF_U + c * 131072 - (c == 5) * 122880, (u32)i * 16, ff);
    waitvm<0>();
    asm volatile("" : "+v"(y.r), "+v"(y.g), "+v"(y.e));
    barrier();
    if (w) return;
    u32 dt[2] = {0xffffffu, 0xffffffu};
#pragma unroll
    for (int c = 0; c < 2 * NSAMP; ++c) {
        u32 v;
        const u32 t0 = mt();
        asm volatile("global_load_dword %0, %1, %2 sc1\n\ts_waitcnt vmcnt(0)" : "=&v"(v) : "v"((c & 1) * 8192 + (c >> 1) * 256), "s"(vb) : "memory");
        dt[c & 1] = min(dt[c & 1], mt() - t0);
    }
    const u64 t = sync_tag(y.e);
    const u32 vote = (u32)((dt[0] > dt[1]) ^ xhalf() ^ (int)((vb - p.ws) >> 21)) & 1u;
    if (tid == 0) {
        if (j == 0) gst8(p.ws + OFF_LAT, 512, u32x2{(u32)t, rt()});
        gst8(p.ws + OFF_LAT, (u32)j * 8, u32x2{(u32)t | vote, (u32)(t >> 32)});
    }
}

DI float xmax(float v) {
    const auto a = __builtin_amdgcn_permlane32_swap(__float_as_uint(v), __float_as_uint(v), false, false);
    v = fmaxf(__uint_as_float(a[0]), __uint_as_float(a[1]));
    const auto b = __builtin_amdgcn_permlane16_swap(__float_as_uint(v), __float_as_uint(v), false, false);
    return fmaxf(__uint_as_float(b[0]), __uint_as_float(b[1]));
}
DI float xsum(float v) {
    const auto a = __builtin_amdgcn_permlane32_swap(__float_as_uint(v), __float_as_uint(v), false, false);
    v = __uint_as_float(a[0]) + __uint_as_float(a[1]);
    const auto b = __builtin_amdgcn_permlane16_swap(__float_as_uint(v), __float_as_uint(v), false, false);
    return __uint_as_float(b[0]) + __uint_as_float(b[1]);
}
DI void gst16c(char* sbase, u32 voff, u32x4 v) {
    asm volatile("global_store_dwordx4 %0, %1, %2 sc1\n\ts_nop 1" : : "v"(voff), "v"(v), "s"(sbase) : "memory");
}
#define WAIT6(N, r, o) asm volatile("s_waitcnt vmcnt(" #N ")" : "+v"(r[o]), "+v"(r[o + 1]), "+v"(r[o + 2]), "+v"(r[o + 3]), "+v"(r[o + 4]), "+v"(r[o + 5]) : : "memory")
#define WSLEEP 90
DI void wg_wait(const char* fl, int w, int i0, int t0, int i1, int t1, int i2, int t2, int i3, int t3, int dt, bool acq) {
    if (w == 0) {
        const int lane = lanex(), tg = lane == 1 ? t1 : (lane == 2 ? t2 : (lane == 3 ? t3 : t0));
        const int ix = tg <= 0 ? i0 : (lane == 1 ? i1 : (lane == 2 ? i2 : (lane == 3 ? i3 : i0)));
#pragma nounroll
        for (int it = 0; it < (1 << 14); ++it) {
            u32 v;
            flag_ld(v, fl, ix);
            asm volatile("s_waitcnt vmcnt(0)" : "+v"(v) : : "memory");
            if (__builtin_amdgcn_ballot_w64((int)(v + 1u) < tg) == 0) break;
            const u32 wake = rt() + (u32)(dt + 180 * min(max(t0 - 2 - uni((int)v), 0), 64));
            while ((int)(rt() - wake) < 0) __builtin_amdgcn_s_sleep(4);
        }
        if (acq) acquire();
    }
    barrier();
}
#define TEST 172
DI u32 step_wait(const char* fl, int w, int gr, int t, u32 lds) {
    if (w == 0) {
        u32x2 v;
#pragma nounroll
        for (int it = 0; it < (1 << 14); ++it) {
            asm volatile("s_nop 4\n\tglobal_load_dwordx2 %0, %1, %2 sc1\n\ts_waitcnt vmcnt(0)" : "=&v"(v) : "v"((F_CUR + gr) * 128), "s"(fl) : "memory");
            const int d = t - 1 - uni((int)v[0]);
            if (d <= 0) { v[1] += (u32)(d * TEST); break; }
            const u32 now = rt();
            nap(min(max((int)((u32)uni((int)v[1]) - now) + d * TEST + 10, 40), 2000));
        }
        *LDSP(u32, lds + GRING * GBUF + 4) = v[1];
    }
    barrier();
    return (u32)uni((int)*LDSP(u32, lds + GRING * GBUF + 4));
}
#define PDL 115
#define ODL 240
#define TDT 35
template <bool LATE>
DI void px_wave(const Ptrs& p, const ChainCfg& cfg, int s, int h, int slow, int w, u32 lds, bool keep) {
    const int lane = lanex(), n = lane & 15, g = lane >> 4;
    const char* qb = p.ws + OFF_QX + (size_t)(s * NH + h) * 12288;
    const char* kb = p.ws + OFF_KX + (size_t)(s * NH + h) * 12288;
    const char* mb = p.ws + OFF_KM + (size_t)(s * NH + h) * 3072;
    const u32 mo = (u32)(g * 128 + (n & 7) * 16);
    u32x4 qf[12], kf[12], mf[6];
    sfor<0, 12>([&](auto I) { gld16x<(I.v % 3) * 1024>(qf[I.v], qb + (I.v / 3) * 3072, lane * 16); gap<SMP>(slow); });
    if (!SMP) pace(slow);
    sfor<0, 12>([&](auto I) { if constexpr (LATE || !KNT) gld16c<(I.v % 3) * 1024>(kf[I.v], kb + (I.v / 3) * 3072, lane * 16); else gld16x<(I.v % 3) * 1024>(kf[I.v], kb + (I.v / 3) * 3072, lane * 16); gap<SMP>(slow); });
    if (!SMP) pace(slow);
    if constexpr (LATE) { WAIT6(0, qf, 0); WAIT6(0, qf, 6); WAIT6(0, kf, 0); }
    else {
        sfor<0, 6>([&](auto I) { gld16x<I.v * 512>(mf[I.v], mb, mo); });
        WAIT6(24, qf, 0); WAIT6(18, qf, 6); WAIT6(12, kf, 0);
    }
    f32x4 sc[2][3];
#pragma unroll
    for (int tt = 0; tt < 2; ++tt)
#pragma unroll
        for (int T = 0; T < 3; ++T) sc[tt][T] = f32x4{0.f, 0.f, 0.f, 0.f};
#pragma unroll
    for (int ks = 0; ks < 6; ++ks) { sc[0][0] = mfma16(kf[ks], qf[ks], sc[0][0]); sc[1][0] = mfma16(kf[ks], qf[6 + ks], sc[1][0]); }
    WAIT6(6, kf, 6);
#pragma unroll
    for (int ks = 0; ks < 6; ++ks) sc[1][1] = mfma16(kf[6 + ks], qf[6 + ks], sc[1][1]);
    if constexpr (LATE) {
        until(step_wait(p.ws + OFF_ST, w, h & 7, s - 1, lds) + (u32)(s == NSEG - 1 ? cfg.T : cfg.T + PDX));
        bool ok = false;
#pragma nounroll
        for (int it = 0; it < (1 << 14); ++it) {
            u32 v;
            sfor<0, 6>([&](auto I) { gld16c<I.v * 512>(mf[I.v], mb, mo); });
            flag_ldm(v, p.ws + OFF_ST, (s < NSEG - 2 ? F_CUR : F_PROG + 8 * s) + (h & 7), it > 3);
            WAIT6(0, mf, 0);
            asm volatile("" : "+v"(v));
            u32 m = 0;
#pragma unroll
            for (int i = 0; i < 24; ++i) m = max(m, mf[i >> 2][i & 3]);
            if (ok || __builtin_amdgcn_ballot_w64(m == ~0u) == 0) break;
            ok = it > 3 && uni((int)v) >= (s < NSEG - 2 ? s : 7);
            if (s < NSEG - 1) nap(60);
        }
    }
    WAIT6(0, mf, 0);
#pragma unroll
    for (int ks = 0; ks < 6; ++ks) { sc[0][2] = mfma16(mf[ks], qf[ks], sc[0][2]); sc[1][2] = mfma16(mf[ks], qf[6 + ks], sc[1][2]); }
    const float kscale = 0.07216878364870322f;
    char* pb = p.ws + OFF_PX + (size_t)s * 81920;
#pragma unroll
    for (int tt = 0; tt < 2; ++tt) {
        float v[12], mx = -3.0e38f;
#pragma unroll
        for (int i = 0; i < 12; ++i) {
            const float t = rbf(rbf(sc[tt][i >> 2][i & 3]) * kscale);
            v[i] = (i >= 8 || 4 * g + (i & 3) + 16 * (i >> 2) <= 16 * tt + n) ? t : -3.0e38f;
            mx = fmaxf(mx, v[i]);
        }
        mx = xmax(mx);
        float sx = 0.f, sm = 0.f;
#pragma unroll
        for (int i = 0; i < 12; ++i) {
            v[i] = __builtin_amdgcn_exp2f((v[i] - mx) * 1.4426950408889634f);
            if (i < 8) sx += v[i]; else sm += v[i];
        }
        const float inv = __builtin_amdgcn_rcpf(xsum(sx + 0.5f * sm));
        const u32x4 px = u32x4{cvtpk(v[0] * inv, v[1] * inv), cvtpk(v[2] * inv, v[3] * inv), cvtpk(v[4] * inv, v[5] * inv), cvtpk(v[6] * inv, v[7] * inv)};
        const u32x2 pm = u32x2{cvtpk(v[9] * inv, v[8] * inv), cvtpk(v[11] * inv, v[10] * inv)};
        const int ox = h * 2048 + tt * 1024, om = 65536 + (h >> 2) * 2048 + (h & 3) * 256 + tt * 1024;
        if (!LATE && keep) {
            *LDSP(u32x4, lds + ox + lane * 16) = px;
            *LDSP(u32x2, lds + om + n * 16 + (g & 1) * 8) = pm;
        } else {
            gst16c(pb + ox, lane * 16, px);
            const auto a = __builtin_amdgcn_permlane16_swap(pm[0], pm[0], false, false), b = __builtin_amdgcn_permlane16_swap(pm[1], pm[1], false, false);
            if (g == 0) gst16c(pb + om, (u32)(n * 16), u32x4{a[0], b[0], a[1], b[1]});
        }
    }
}

#define WAIT10(N, r) asm volatile("s_waitcnt vmcnt(" #N ")" : "+v"(r[0]), "+v"(r[1]), "+v"(r[2]), "+v"(r[3]), "+v"(r[4]), "+v"(r[5]), "+v"(r[6]), "+v"(r[7]), "+v"(r[8]), "+v"(r[9]) : : "memory")
DI void out_task(const Ptrs& p, int s, int cg0, int cg1, bool kept, u32 lds, int slow, int w, int sd) {
    u32x4 pf[2][10];
    if (kept) {
        const u32 la = lds + lanex() * 16;
#pragma unroll
        for (int i = 0; i < 20; ++i) pf[i & 1][i >> 1] = ldsr16(la + (i < 16 ? w * 16384 + i * 1024 : 65536 + w * 4096 + (i - 16) * 1024));
    } else {
        const char* pb = p.ws + OFF_PX + (size_t)s * 81920;
        const u32 l16 = lanex() * 16;
        sfor<0, 20>([&](auto I) { gld16c<(I.v & 3) * 1024>(pf[I.v & 1][I.v >> 1], pb + (I.v < 16 ? w * 16384 + (I.v >> 2) * 4096 : 65536 + w * 4096), l16); gap<SMO>(slow); if (!SMO && I.v == 9) pace(slow); });
        WAIT10(0, pf[0]); WAIT10(0, pf[1]);
    }
#pragma nounroll
    for (int cg = cg0; cg < cg1; ++cg) {
    const int lane = lanex(), tid = 64 * w + lane, n = lane & 15, g = lane >> 4;
    const char* db = p.ws + OFF_D + s * D_STRIDE + (size_t)(16 * cg) * 16384 + w * 4096;
    const char* ub = p.ws + uslot(sd, s) + (size_t)(16 * cg) * 4096 + w * 1024;
    const u32 dof = (u32)((n >> 3) * 16384 + g * 128 + (n & 7) * 16), uof = (u32)((n >> 3) * 4096 + g * 128 + (n & 7) * 16);
    u32x4 af[3][10];
    auto aload = [&](u32x4 (&r)[10], int ct) {
        if (!SMO) pace(slow);
        sfor<0, 8>([&](auto I) { gld16x<I.v * 512>(r[I.v], db + ct * 32768, dof); gap<SMO>(slow); });
        gld16x<0>(r[8], ub + uchunk(ct), uof); gap<SMO>(slow); gld16x<512>(r[9], ub + uchunk(ct), uof); gap<SMO>(slow);
    };
    aload(af[0], 0);
    aload(af[1], 1);
    aload(af[2], 2);
    const int tok = 32 * s + (tid >> 3), col = 128 * cg + 8 * (tid & 7);
    u32x4 bb[2]; u32 gv;
    gld16c<0>(bb[0], p.ws + OFF_BOUT, (u32)(s * 40960 + col * 64 + (tid >> 3) * 16)); gld16c<0>(bb[1], p.ws + OFF_BOUT + 4096, (u32)(s * 40960 + col * 64 + (tid >> 3) * 16));
    asm volatile("global_load_dword %0, %1, %2 sc1" : "=v"(gv) : "v"(tok * 4), "s"(p.ws + OFF_GATE) : "memory");
    barrier();
    const u32 wa = lds + (u32)w * 16896 + n * 528 + g * 16;
    sfor<0, 8>([&](auto I) {
        constexpr int ct = I.v;
        u32x4 (&r)[10] = af[ct % 3];
        if constexpr (ct == 0) WAIT10(23, r);
        else if constexpr (ct < 3) WAIT10(23, r);
        else if constexpr (ct < 6) WAIT10(20, r);
        else if constexpr (ct == 6) WAIT10(10, r);
        else WAIT10(0, r);
        f32x4 c0 = f32x4{0.f, 0.f, 0.f, 0.f}, c1 = c0;
#pragma unroll
        for (int i = 0; i < 10; ++i) { c0 = mfma16(r[i], pf[0][i], c0); c1 = mfma16(r[i], pf[1][i], c1); }
        *LDSP(f32x4, wa + ct * 64) = c0;
        *LDSP(f32x4, wa + ct * 64 + 16 * 528) = c1;
        PIN();
        if constexpr (ct + 3 < 8) aload(r, ct + 3);
        PIN();
    });
    asm volatile("" : "+v"(bb[0]), "+v"(bb[1]), "+v"(gv));
    barrier();
    const u32 ra = lds + (u32)(tid >> 3) * 528 + (tid & 7) * 32;
    const float gt = __uint_as_float(gv), g1 = rbf(1.f - gt);
    u32 ov[8];
#pragma unroll
    for (int i = 0; i < 4; ++i) {
        const u32 ri = ra + 16 * (i & 1) + 256 * (i >> 1);
        const f32x4 z = (*LDSP(f32x4, ri) + *LDSP(f32x4, ri + 16896)) + (*LDSP(f32x4, ri + 2 * 16896) + *LDSP(f32x4, ri + 3 * 16896));
#pragma unroll
        for (int j = 0; j < 2; ++j) {
            const u32 b = bb[i >> 1][2 * (i & 1) + j];
            const u32 a = cvtpk(z[2 * j], z[2 * j + 1]);
            const u32 ga = cvtpk(gt * __uint_as_float(a << 16), gt * __uint_as_float(a & 0xffff0000u));
            const u32 gb = cvtpk(g1 * __uint_as_float(b << 16), g1 * __uint_as_float(b & 0xffff0000u));
            ov[2 * i + j] = cvtpk(__uint_as_float(ga << 16) + __uint_as_float(gb << 16), __uint_as_float(ga & 0xffff0000u) + __uint_as_float(gb & 0xffff0000u));
        }
    }
    gst16p(p.out, (u32)(tok * 1280 + col * 2), u32x4{ov[0], ov[1], ov[2], ov[3]});
    gst16p(p.out, (u32)(tok * 1280 + col * 2 + 128), u32x4{ov[4], ov[5], ov[6], ov[7]});
    }
}

DI void out_tail(const Ptrs& p, const ChainCfg& cfg, int s, int cg, u32 lds, int w, int sd, int gr) {
    const int lane = lanex(), tid = 64 * w + lane, n = lane & 15, g = lane >> 4;
    const char* db = p.ws + OFF_D + s * D_STRIDE + (size_t)(4 * cg) * 16384 + w * 4096;
    const char* ub = p.ws + uslot(sd, s) + uchunk(2 * cg) + w * 1024;
    const char* pb = p.ws + OFF_PX + (size_t)s * 81920;
    const u32 dof = (u32)((n >> 3) * 16384 + g * 128 + (n & 7) * 16), uof = (u32)((n >> 3) * 4096 + g * 128 + (n & 7) * 16);
    const int slow = uni(s - TGS);
    u32x4 pf[2][10], af[2][10];
    sfor<0, 16>([&](auto I) { gld16c<(I.v & 7) * 512>(af[I.v >> 3][I.v & 7], db + (I.v >> 3) * 32768, dof); gap<TG1>(-1); });
    const int tok = 32 * s + (tid >> 3), col = 32 * cg + 4 * (tid & 7);
    u32x2 bb; u32 gv;
    asm volatile("global_load_dwordx2 %0, %2, %3 sc1\n\tglobal_load_dword %1, %4, %5 sc1\n\ts_waitcnt vmcnt(0)" : "=&v"(bb), "=&v"(gv)
                 : "v"(s * 40960 + (col >> 3) * 512 + (tid >> 3) * 16 + (col & 7) * 2), "s"(p.ws + OFF_BOUT), "v"(tok * 4), "s"(p.ws + OFF_GATE) : "memory");
    sfor<0, 16>([&](auto I) { waitv<0>(af[I.v >> 3][I.v & 7]); });
    until(step_wait(p.ws + OFF_ST, w, gr, s - 1, lds) + (u32)(s == NSEG - 1 ? cfg.cR : cfg.cR + 2 * PDX));
    const u32 wa = lds + (u32)w * 4608 + n * 144 + g * 16;
#pragma nounroll
    for (int it = 0; it < (1 << 14); ++it) {
        if (it == 4) {
            flag_wait(p.ws + OFF_ST, F_PX + s, 8);
            flag_wait(p.ws + OFF_ST, s < NSEG - 2 ? F_CUR + gr : F_UD + 2 * (s & 1), s < NSEG - 2 ? s + 1 : 16);
            flag_wait(p.ws + OFF_ST, s < NSEG - 2 ? F_ONE : F_UD + 2 * (s & 1) + 1, 16);
        }
        sfor<0, 4>([&](auto I) { gld16c<(I.v & 1) * 512>(af[I.v >> 1][8 + (I.v & 1)], ub + (I.v >> 1) * 16384, uof); });
        sfor<0, 8>([&](auto I) { gld16c<(I.v & 1) * 2048>(pf[0][I.v], pb + w * 16384 + (I.v >> 1) * 4096, lane * 16); gap<TG2>(slow); });
        gld16c<0>(pf[0][8], pb + 65536 + w * 4096, lane * 16); gld16c<2048>(pf[0][9], pb + 65536 + w * 4096, lane * 16);
        sfor<0, 8>([&](auto I) { gld16c<(I.v & 1) * 2048 + 1024>(pf[1][I.v], pb + w * 16384 + (I.v >> 1) * 4096, lane * 16); gap<TG2>(slow); });
        gld16c<1024>(pf[1][8], pb + 65536 + w * 4096, lane * 16); gld16c<3072>(pf[1][9], pb + 65536 + w * 4096, lane * 16);
        WAIT10(10, pf[0]); WAIT10(10, af[0]); WAIT10(10, af[1]);
        float chk = 0.f;
#pragma unroll
        for (int tt = 0; tt < 2; ++tt) {
            if (tt) WAIT10(0, pf[1]);
            f32x4 c0 = f32x4{0.f, 0.f, 0.f, 0.f}, c1 = c0;
#pragma unroll
            for (int i = 0; i < 10; ++i) { c0 = mfma16(af[0][i], pf[tt][i], c0); c1 = mfma16(af[1][i], pf[tt][i], c1); }
            *LDSP(f32x4, wa + tt * 2304) = c0;
            *LDSP(f32x4, wa + tt * 2304 + 64) = c1;
            const f32x4 c = c0 + c1;
            chk += (c[0] + c[1]) + (c[2] + c[3]);
            PIN();
        }
        if (it >= 4 || __builtin_amdgcn_ballot_w64((__float_as_uint(chk) & 0x7fffffffu) > 0x7f800000u) == 0) break;
        if (s < NSEG - 1) nap(60);
    }
    barrier();
    const u32 ra = lds + (u32)(tid >> 3) * 144 + (tid & 7) * 16;
    const float gt = __uint_as_float(gv), g1 = rbf(1.f - gt);
    const f32x4 z = (*LDSP(f32x4, ra) + *LDSP(f32x4, ra + 4608)) + (*LDSP(f32x4, ra + 2 * 4608) + *LDSP(f32x4, ra + 3 * 4608));
    u32x2 ov;
#pragma unroll
    for (int j = 0; j < 2; ++j) {
        const u32 b = bb[j], a = cvtpk(z[2 * j], z[2 * j + 1]);
        const u32 ga = cvtpk(gt * __uint_as_float(a << 16), gt * __uint_as_float(a & 0xffff0000u));
        const u32 gb = cvtpk(g1 * __uint_as_float(b << 16), g1 * __uint_as_float(b & 0xffff0000u));
        ov[j] = cvtpk(__uint_as_float(ga << 16) + __uint_as_float(gb << 16), __uint_as_float(ga & 0xffff0000u) + __uint_as_float(gb & 0xffff0000u));
    }
    *(u32x2*)(p.out + tok * 1280 + col * 2) = ov;
}

DI float dot2bf(u32 a, u32 b, float c) { return __builtin_amdgcn_fdot2_f32_bf16(__builtin_bit_cast(bf16x2, a), __builtin_bit_cast(bf16x2, b), c, false); }
DI void battn_task(const Ptrs& p, int t0, u32 lds, int w) {
    const int lane = lanex(), tid = 64 * w + lane, tl = tid & 31, t = t0 + tl;
    const char* const qkv = p.ws + OFF_QKVB + (size_t)(t0 >> 5) * 122880;
    {
        u32 so[3];
#pragma unroll
        for (int i = 0; i < 3; ++i) {
            const int e = 64 * i + lane, sl = e % 48;
            so[i] = (u32)((e / 48) * 512 + (sl < 16 ? (t0 ? 0 : 122880) + 256 + sl * 16 : 122880 - 256 + sl * 16));
        }
#pragma unroll
        for (int i = 0; i < 30; ++i) { glds16c(qkv - 122880 + 40960 + (10 * w + i / 3) * 2048, so[i % 3], lds + (30 * w + i) * 1024); gap<SMB>(-1); }
    }
    u32x4 q[2][5], xv[10], gv[10];
    sfor<0, 10>([&](auto I) { gld16x<(I.v % 5) * 512>(q[I.v / 5][I.v % 5], qkv + (I.v / 5) * 20480, (u32)((tid >> 5) * 2560 + tl * 16)); gap<SMB>(-1); });
    sfor<0, 10>([&](auto I) { gld16<I.v * 128>(xv[I.v], p.x + (size_t)t0 * 1280, (u32)((tid >> 3) * 1280 + (tid & 7) * 16)); gld16<I.v * 128>(gv[I.v], p.gw, (u32)((tid & 7) * 16)); gap<SMB>(-1); });
    sfor<0, 10>([&](auto I) { waitv<0>(q[I.v / 5][I.v % 5]); waitv<0>(xv[I.v]); waitv<0>(gv[I.v]); });
    barrier();
    {
        float a = 0.f;
#pragma unroll
        for (int i = 0; i < 10; ++i)
#pragma unroll
            for (int j = 0; j < 4; ++j) {
                const u32 r = cvtpk(__uint_as_float(xv[i][j] << 16) * __uint_as_float(gv[i][j] << 16), __uint_as_float(xv[i][j] & 0xffff0000u) * __uint_as_float(gv[i][j] & 0xffff0000u));
                a += __uint_as_float(r << 16) + __uint_as_float(r & 0xffff0000u);
            }
        a += __shfl_xor(a, 1); a += __shfl_xor(a, 2); a += __shfl_xor(a, 4);
        const float z = rbf(a);
        if ((tid & 7) == 0) gst4c(p.ws + OFF_GATE, (u32)((t0 + (tid >> 3)) * 4), __float_as_uint(rbf(1.0f / (1.0f + __expf(-z)))));
    }
#pragma unroll
    for (int it = 0; it < 2; ++it) {
        const int m = (tid >> 5) + 8 * it;
        u32 ka = lds + (u32)(m * 3840 + (2 + tl) * 16), va = ka + 61440;
        asm volatile("" : "+v"(ka), "+v"(va));
        u32x4 k[5][8];
#pragma unroll
        for (int i = 0; i < 40; ++i) k[i >> 3][i & 7] = ldsr16(ka + (i >> 3) * 768 + (7 - (i & 7)) * 32);
        PIN();
        float lg[2][8], mx[2] = {-3.0e38f, -3.0e38f};
#pragma unroll
        for (int i = 0; i < 16; ++i) lg[i >> 3][i & 7] = 0.f;
#pragma unroll
        for (int i = 0; i < 5; ++i) {
#pragma unroll
            for (int j = 0; j < 4; ++j)
#pragma unroll
                for (int wv = 0; wv < 8; ++wv) lg[4 * i + j >= 10][wv] = dot2bf(q[it][i][j], k[i][wv][j], lg[4 * i + j >= 10][wv]);
            PIN();
#pragma unroll
            for (int wv = 0; wv < 8; ++wv) k[i][wv] = ldsr16(va + i * 768 + (7 - wv) * 32);
            PIN();
        }
#pragma unroll
        for (int i = 0; i < 16; ++i) {
            float& l = lg[i >> 3][i & 7];
            l = rbf(rbf(l) * 0.22360679774997896f);
            asm volatile("" : "+v"(l));
            l = t - 2 * (i & 7) >= 0 ? l : -3.0e38f;
            mx[i >> 3] = fmaxf(mx[i >> 3], l);
        }
        u32 pk[2][4];
#pragma unroll
        for (int u = 0; u < 2; ++u) {
            float sum = 0.f;
#pragma unroll
            for (int wv = 0; wv < 8; ++wv) { lg[u][wv] = __builtin_amdgcn_exp2f((lg[u][wv] - mx[u]) * 1.4426950408889634f); sum += lg[u][wv]; }
            const float inv = __builtin_amdgcn_rcpf(sum);
#pragma unroll
            for (int wv = 0; wv < 4; ++wv) pk[u][wv] = cvtpk(lg[u][2 * wv] * inv, lg[u][2 * wv + 1] * inv);
        }
#pragma unroll
        for (int i = 0; i < 5; ++i) {
            float o[8];
#pragma unroll
            for (int d = 0; d < 8; ++d) o[d] = 0.f;
#pragma unroll
            for (int wp = 0; wp < 4; ++wp) {
                const u32x4 v0 = k[i][2 * wp], v1 = k[i][2 * wp + 1];
#pragma unroll
                for (int j = 0; j < 4; ++j) {
                    const u32 pw = pk[4 * i + j >= 10][wp];
                    o[2 * j] = dot2bf(__builtin_amdgcn_perm(v1[j], v0[j], 0x05040100u), pw, o[2 * j]);
                    o[2 * j + 1] = dot2bf(__builtin_amdgcn_perm(v1[j], v0[j], 0x07060302u), pw, o[2 * j + 1]);
                }
            }
            gst16c(p.ws + OFF_OB + (size_t)(t0 >> 5) * 40960 + 512 * i, (u32)(m * 2560 + tl * 16), u32x4{cvtpk(o[0], o[1]), cvtpk(o[2], o[3]), cvtpk(o[4], o[5]), cvtpk(o[6], o[7])});
        }
    }
}

#define XPF 1
#define PFQ 0
#define PFN 8
#define OLAG 3
constexpr int QN = 176, QF = 19;
struct QTab { u16 e[QN]; int n, nf; u32 tt[64]; };
typedef const __attribute__((address_space(4))) int* KI;
constexpr int QT_OFF = (88 + 48) / 4;
DI int pick_task(char* fl, KI k, int gr) {
    u32 r;
    asm volatile("s_mov_b64 exec, 1\n\ts_nop 3\n\tglobal_atomic_add %0, %1, %2, %3 sc0 sc1\n\ts_mov_b64 exec, -1\n\ts_waitcnt vmcnt(0)"
                 : "=&v"(r) : "v"((F_QH + gr) * 128), "v"(1), "s"(fl) : "memory");
    const int x = uni((int)r) + 1 + QF;
    if (x >= k[QT_OFF + QN / 2]) return -1;
    const u32 e2 = (u32)k[QT_OFF + (x >> 1)];
    return (int)(x & 1 ? e2 >> 16 : e2 & 0xffffu);
}
__global__ __launch_bounds__(256, 1) void k_main(Ptrs p0, ChainCfg cfg, QTab tab) {
    extern __shared__ __attribute__((aligned(16))) char smem[];
    u32 lds = (u32)(size_t)LDSP(char, smem);
#define HALF 0
    int n = HALF ? (blockIdx.x & 4) * 32 + (int)(blockIdx.x >> 3) * 4 + (blockIdx.x & 3) : blockIdx.x, gr = n & 7;
    int w = uni(threadIdx.x >> 6);
    {
        u64 pc; int len;
        asm volatile("s_getpc_b64 %0\n.Lkm_first:\n\ts_mov_b32 %1, .Lkm_last-.Lkm_first\n\ts_max_i32 %1, %1, .Lkm_lu-.Lkm_first\n\ts_max_i32 %1, %1, .Lkm_lv-.Lkm_first\n\t"
                     "s_max_i32 %1, %1, .Lkm_g2-.Lkm_first\n\ts_max_i32 %1, %1, .Lkm_g4-.Lkm_first" : "=s"(pc), "=s"(len) : : "scc");
        if (w == 3) pfl((const char*)pc, (u32)min((int)(blockIdx.x >> 3) * 8192 + (int)(threadIdx.x & 63) * 128, max(len, 0)), lds);
    }
    karg_warm();
    if (n < NH) { score_role(p0, cfg, n, lds, w); return; }
    if (n < NS) { proj_role(p0, cfg, n - NH, lds, w); return; }
    const bool isv = n < NS + NVB;
#define qslot (lds + GRING * GBUF)
    int sdw = -1;
    int en = isv ? Q_VD + (n & 24) : tab.e[(n - NS - NVB) >> 3] & 511;
    u32 phi0 = 0;
    Sync y;
    if (XFW && n < 160) {
        const int h = gr + (en & 24);
#pragma unroll
        for (int i = 0; i < XFW; ++i) { const int l = (i * 4 + w) * 64 + lanex(); pfl(((en & 7) == Q_VD ? p0.wv : p0.wk) + h * 384, (u32)((l / 3) * 12288 + (l % 3) * 128), lds); }
        if (w == 1) {
            sync_ld(y, p0.ws);
            waitvm<0>();
            asm volatile("" : "+v"(y.r), "+v"(y.g), "+v"(y.e));
            xcopy_wait(p0.ws, sync_tag(y.e), 0, 3);
        }
        barrier();
    }
    if (!XFW && XPF && n < 160 && w >= 2) pfl(p0.x, (u32)min(((n - NS) >> 3) * 128 + (w & 1) * 64 + lanex(), 1279) * 128, lds);
    if (n >= 160) {
        const int lane = lanex();
        if (n >= 192 && (n & 8)) {
            const int j = (n & 7) + ((n - 192) >> 4) * 8;
            init_task(p0, j, lds, w);
            barrier();
#pragma unroll
            for (int i = 0; i < 3; ++i) pfl(p0.wqkvb, (u32)min(j * 768 + i * 256 + w * 64 + lane, 19199) * 128, lds);
            pfl(j < 25 ? p0.wob : p0.gw, j < 25 ? (u32)(j * 256 + w * 64 + lane) * 128 : (u32)min(lane, 9) * 128, lds);
        } else {
            const int h = n < 192 ? n & 31 : (n & 7) + ((n - 192) >> 4) * 8;
            xcopy_task(p0, h + (n < 192 ? 0 : 32), w);
            if (n < 192) {
#pragma unroll
                for (int i = 0; i < PFN; ++i) {
                    const int l = 64 * min(i * 4 + w, 29) + lane;
                    pfl(p0.wo + (size_t)h * 245760, (u32)(l * 128), lds);
                    if (i >= 8 - PFQ) pfl(p0.wq + h * 384, (u32)((l / 3) * 12288 + (l % 3) * 128), lds);
                }
            }
        }
        if (w == 1) {
            sync_ld(y, p0.ws);
            waitvm<0>();
            asm volatile("" : "+v"(y.r), "+v"(y.g), "+v"(y.e));
            if (n < 192) xcopy_wait(p0.ws, sync_tag(y.e));
            else xcopy_wait(p0.ws, sync_tag(y.e), n & 8 ? 4 : 8, n & 8 ? 3 : 7);
        }
        if (w == 0 && n < 192) {
            sync_ld(y, p0.ws);
            waitvm<0>();
            *LDSP(u32, qslot) = (u32)sync_end(y, p0.ws, phi0, false);
        }
        barrier();
        if (w == 0 && n < 192) flag_add(p0.ws + OFF_ST, F_ACK, 1);
        if (n < 192) sdw = uni((int)*LDSP(u32, qslot));
        barrier();
    }
    while (true) {
        typedef char* const __attribute__((address_space(4)))* KP;
        KP kp = (KP)__builtin_amdgcn_kernarg_segment_ptr();
        asm volatile("" : "+s"(kp), "+s"(lds), "+s"(w), "+s"(gr), "+s"(n));
        __builtin_assume(w >= 0 && w < 4 && gr >= 0 && gr < 8);
        const Ptrs p{kp[0], kp[1], kp[2], kp[3], kp[4], kp[5], kp[6], kp[7], kp[8], kp[9], kp[10]};
        const int ty = en & 7, gate = en >> 9;
        const bool fu = ty == Q_PX && (en >> 3 & 63) < 8;
        const int ix = fu ? (en & 504) + gr : en >> 3 & 63, sf = ((KI)kp)[QT_OFF + QN / 2 + 1] & 255;
        char* const fl = p.ws + OFF_ST;
        const bool raw = sdw < 0;
        int t0 = gate - 1, f1 = F_ONE, t1 = 0, f2 = F_ONE, t2 = 0, f3 = F_ONE, t3 = 0, dt = cfg.T2;
        const bool o2 = ty == Q_OUT && ix < 16;
        const int os = o2 ? sf + 8 * (ix >> 2) + gr : (ix >= 40 ? 44 + (8 * ix + gr) / 20 : (8 * ix + gr) / 5);
        const int bb = ty == Q_BO ? (8 * ix + gr) >> 2 : os;
        if (ty == Q_BA) { t0 += gr * BAS >> 3; f1 = F_B + (gr & RM) | ix << 16; t1 = 5; f2 = F_B + (gr & RM) | max(ix - 1, 0) << 16; t2 = 5; }
        if (ty == Q_BO) { f1 = F_B + (gr & RM) | (8 + bb) << 16; t1 = 8; }
        if (ty == Q_PX) { f1 = F_Q + pblk(ix) * 8 + (fu ? gr & RM : gr) | fu << 16; t1 = fu ? 32 : 4; if (fu) t0 += gr * (((KI)kp)[QT_OFF + QN / 2 + 1] >> 8) >> 3; if (ix < PXT) t0 = max(t0, ix + 1); }
        if (ty == Q_OUT) { f3 = F_B + (gr & RM) | (16 + (os >> 3)) << 16; t3 = 4; if (os < 60) { f1 = F_PX + os; t1 = 8; t0 = max(t0, os + (os < PXT ? OLAG : 1)); if (os >= PXT) dt = TDT; } else { f1 = F_VD + 64 + (gr & RM); t1 = 32; } }
        const bool skip = fu ? ix >= sf : ty == Q_OUT && ix < 40 && !o2 && (os < sf + 8 * NO2 || os >= 60);
        if ((gate || ty >= Q_BA) && !skip) wg_wait(fl, w, F_CUR + gr, t0, f1, t1, f2, t2, f3, t3, dt, false);
        int sig = F_END, nrep = 1;
        if (skip) {}
        else if (ty <= Q_QKVB || ty == Q_BO) {
            const bool ab = ty <= Q_KQ;
            sig = ty == Q_VD ? F_VD + (ix >> 2) * 8 : (ty == Q_KQ ? F_Q + (ix >> 2) * 8 : (ix & 4 ? F_Q : F_K + 8 * ix)) + gr;
            if (!ab) sig = F_B | (bb + (ty == Q_BO ? 16 : 0)) << 16;
            nrep = ty == Q_VD || !ab ? RM + 1 : 1;
            if (ty == Q_VD && ix < 4) { sig += n & 32 ? 4 : 0; nrep = 4; }
            run_gemm(p, ty, ab ? ix >> 2 : bb, ab ? gr + 8 * (ix & 3) : 8 * ix + gr - (ty == Q_BO ? 4 : 5) * bb, (n >> 5) & 1, !XFW && raw && n < 160, y, lds, w);
            if (raw) sdw = sync_end(y, p.ws, phi0, w == 0);
        } else {
            if (ty == Q_BA) {
                battn_task(p, (8 * ix + gr) * 32, lds, w);
                sig = F_B | (8 + ix) << 16; nrep = RM + 1;
            } else if (ty == Q_PX) {
                if (ix < PXT) {
#pragma nounroll
                    for (int i = fu ? 0 : gr; i < (fu ? 8 : gr + 1); ++i) px_wave<false>(p, cfg, ix, i + 8 * w, ix - cfg.fast, w, lds, fu);
                } else px_wave<true>(p, cfg, ix, gr + 8 * w, -1, w, lds, false);
                sig = F_PX + ix;
                if (fu) wg_wait(fl, w, F_ONE, 0, F_B + (gr & RM) | (16 + (ix >> 3)) << 16, 4, F_ONE, 0, F_ONE, 0, dt, false);
            }
            if (ty == Q_OUT || fu) {
                const int cg = 8 * ix + gr - 5 * os;
                if (ix >= 40 && !fu) out_tail(p, cfg, os, 8 * ix + gr - 20 * (os - 44), lds, w, sdw, gr);
                else out_task(p, fu ? ix : os, fu ? 0 : (o2 ? OW * (ix & 3) : cg), fu ? 5 : (o2 ? min(OW * (ix & 3) + OW, 5) : cg + 1), fu, lds, (fu ? ix : os) + 1 - cfg.fast, w, sdw);
                sig = F_END;
            }
            waitvm<0>();
        }
        barrier();
        if (w == 0 && sig != F_END) flag_add(fl, sig, nrep);
        if (w == 1 && sig >= F_Q && sig < F_N) flag_add(fl, sig - gr | 1 << 16, RM + 1);
        if (raw && !isv && w == 0) xcopy_wait(p.ws, sync_tag(y.e));
        if (isv) break;
        if (w == 0) *LDSP(int, qslot) = pick_task(fl, (KI)kp, gr);
        barrier();
        en = uni(*LDSP(int, qslot));
        if (en < 0) return;
        barrier();
    }
    value_role(p0, cfg, n - NS, sdw, phi0, lds, w);
}

#define QCFG 14, 11, 3, 5, 0, 50, 36
#define LVDEF "00?>>=<<;;;<<<<;;::98888999:::::999999:::99888998888888888888886"
constexpr int DX = 380, DUA = 470, DUB = 0, DUC = 4;
static ChainCfg dev_cfg(ChainCfg c) { c.T = PDL; c.cR = ODL; c.s1 = 190 << 8 | 185 << 16; c.T2 = WSLEEP; c.cR2 = DX; c.dU = DUA; c.s2 = DUB; c.T3 = DUC; c.dV |= 4 << 8 | 8 << 16 | 0 << 24; c.cR3 = 4 | 4 << 8 | 0 << 16; return c; }
static QTab set_sched(QTab t, const ChainCfg c, const char* lv) {
    for (int s = 0; s < 64; ++s) {
        const int d = lv[s] - '0';
        t.tt[s] = (u32)((s < c.s1 ? c.T : (s < c.s2 ? c.T2 : c.T3)) + 4 * d) | (u32)((s < c.s1 ? c.cR : (s < c.s2 ? c.cR2 : c.cR3)) + 5 * d / 2) << 16;
    }
    t.tt[0] = (u32)236 | (u32)165 << 16;
    t.tt[1] = (u32)212 | (u32)149 << 16;
    return t;
}
#define QORD "bbV3qbK3s41x40bx51bx60x71s80"
#define GATES "11 13 15 18 25 28 41 43 21 26 36 49 14 18 22 27 33 41"
static QTab make_tab(int lv, int lk, int b0, int d1, int d2, int lg, int nf) {
    QTab t{};
    int n = 0, dl[QN], nq = 0;
    auto add = [&](int ty, int ix) { t.e[n] = (u16)(ty | ix << 3); dl[n++] = ty == Q_VD || ty == Q_KQ ? 8 * (ix / 4 - 1) - (ty == Q_VD ? lv : lk) - (ix >= 32 ? 0 : 0) : 99; };
    for (int i = 1; i < 4; ++i) add(Q_VD, i);
    for (int i = 0; i < 16; ++i) add(i < 4 ? Q_H : ((i & 8) ? Q_VD : Q_KQ), i < 8 ? i : 8 - (i & 1) * 4 + ((i - 8) >> 1));
    for (int i = 0; i < 4; ++i) add(Q_KQ, 8 + i);
    for (const char* c = QORD; *c; ++c) {
        if (*c == 'b') add(Q_QKVB, nq++);
        else if (*c == 'q') for (int i = 4; i < 8; ++i) add(Q_H, i);
        else if (*c == '.') dl[n - 1] |= 1 << 20;
        else if (*c == 'w') { const int b = *++c - '0'; for (int i = 0; i < 16; ++i) add(i & 2 ? Q_KQ : Q_VD, 4 * (b + (i & 1)) + (i >> 2)); }
        else if (*c == 'x') { const int b = c[1] - '0', o = 2 * (c[2] - '0'); c += 2; for (int i = 0; i < 8; ++i) add(i & 2 ? Q_KQ : Q_VD, 4 * (b + (i & 1)) + o + (i >> 2)); }
        else if (*c == 's') { const int b = c[1] - '0', o = 2 * (c[2] - '0'); c += 2; for (int i = 0; i < 4; ++i) add(i & 1 ? Q_KQ : Q_VD, 4 * b + o + (i >> 1)); }
        else if (*c == 'a' || *c == 'c') { add(*c == 'a' ? Q_VD : Q_KQ, 10 * (c[1] - '0') + c[2] - '0'); c += 2; }
        else if (*c == 'n') { const int b = *++c - '0'; for (int i = 0; i < 8; ++i) add(i & 1 ? Q_KQ : Q_VD, 4 * b + (i >> 1) - (i & 1) * 2); }
        else if (*c == 'h' || *c == 'e') { const int b = 4 * (c[1] - '0') + (*c == 'e') * 2; ++c; add(Q_KQ, b); add(Q_KQ, b + 1); }
        else if (*c == 'm') { const int b = c[1] - '0', b2 = c[2] - '0'; c += 2; for (int i = 0; i < 8; ++i) add(i & 1 ? Q_KQ : Q_VD, 4 * (i & 1 ? b2 : b) + ((i >> 1) ^ (i & 2 ? 0 : 0))); }
        else {
            const int ty = (*c | 32) == 'v' ? Q_VD : Q_KQ, two = *c >> 5 & 1, b = *++c - '0';
            for (int i = 0; i < (4 << two); ++i) add(ty, 4 * (b + (i & two)) + (i >> two));
        }
    }
    const int gap = 8;
    for (int i = n - 1, m = 999; i >= 0; --i) { const int pz = dl[i] > 999; m = min(8 * (pz ? dl[i] - (1 << 20) : dl[i]), m - gap - 8 * pz); dl[i] = max(m, 0); }
    auto put = [&](int ty, int ix, int g, int pos) {
        int i = n++;
        for (; dl[i - 1] > pos; --i) { t.e[i] = t.e[i - 1]; dl[i] = dl[i - 1]; }
        t.e[i] = (u16)(ty | ix << 3 | (g + 1) << 9); dl[i] = pos;
    };
    for (int i = 0; i < n; ++i) if (dl[i] >= 8) t.e[i] |= (u16)((dl[i] / 8 + 1) << 9);
    const char* gs = GATES;
    auto gt = [&](int i) { return 10 * (gs[3 * i] - '0') + gs[3 * i + 1] - '0'; };
    for (int i = 0; i < 8; ++i) put(Q_BA, i, gt(i), 8 * gt(i));
    for (int i = 0; i < 4; ++i) put(Q_BO, i, gt(8 + i), 8 * gt(8 + i));
    for (int i = 0; 8 * i < nf; ++i) put(Q_PX, i, gt(12 + i), 8 * (max(gt(12 + i), 8 * i + 1) + b0));
    auto pg = [&](int s) { return max(8 * s + 8, 8 * 46 + (s - nf) * 3); };
    for (int s = nf; s < NSEG; ++s) { const int g = s < PXT ? pg(s) : 8 * max(s - 3, lg); put(Q_PX, s, g / 8, g); }
    for (int k = 0; k < NO2; ++k) for (int c = 0; c * OW < 5; ++c) put(Q_OUT, 4 * k + c, pg(nf + 8 * k + 7) / 8 + OLAG - 1, pg(nf + 8 * k + 7) + 8 * (OLAG - 1));
    for (int x = 5 * (nf + 8 * NO2) / 8; x < 38; ++x) { const int os = (8 * x + 4) / 5, g = pg(min((8 * x + 7) / 5, PXT - 1)); put(Q_OUT, x, g > 8 * ((8 * x + 7) / 5) + 8 ? g / 8 + OLAG - 1 : 0, os < PXT ? pg(os) + 8 * (OLAG - 1) : 8 * (os + 1)); }
    for (int x = 40; x < 50; ++x) { const int g = max(44 + 8 * x / 20 - 3, lg); put(Q_OUT, x, g, 8 * g + 4); }
    t.n = n; t.nf = nf | d1 << 8;
    return t;
}

extern "C" void kernel_launch(void* const* d_in, const int* in_sizes, int n_in, void* d_out, int out_size, void* d_ws,
                              size_t ws_size, hipStream_t stream) {
    Ptrs p{(const char*)d_in[0], (const char*)d_in[1], (const char*)d_in[2], (const char*)d_in[3], (const char*)d_in[4], (const char*)d_in[5],
           (const char*)d_in[8], (const char*)d_in[9], (const char*)d_in[10], (char*)d_out, (char*)(((size_t)d_ws + 0x1fffff) & ~(size_t)0x1fffff)};
    (void)hipFuncSetAttribute((const void*)k_main, hipFuncAttributeMaxDynamicSharedMemorySize, S_LDS);
    k_main<<<256, 256, S_LDS, stream>>>(p, dev_cfg(ChainCfg{CFG}), set_sched(make_tab(QCFG), ChainCfg{CFG}, LVDEF));
    static_assert(sizeof(Ptrs) + sizeof(ChainCfg) + sizeof(QTab) == TT_OFF + 256, "");
}
```

```cpp
#include <hip/hip_runtime.h>
#pragma clang diagnostic ignored "-Wint-to-pointer-cast"

typedef unsigned short u16;
typedef unsigned int u32;
typedef unsigned long long u64;
typedef __bf16 bf16x8 __attribute__((ext_vector_type(8)));
typedef __bf16 bf16x16 __attribute__((ext_vector_type(16)));
typedef short s16x4 __attribute__((ext_vector_type(4)));
typedef float f32x4 __attribute__((ext_vector_type(4)));
typedef unsigned u32x4 __attribute__((ext_vector_type(4)));
typedef unsigned u32x2 __attribute__((ext_vector_type(2)));
typedef unsigned u32x8 __attribute__((ext_vector_type(8)));
typedef unsigned u32x16 __attribute__((ext_vector_type(16)));
typedef unsigned u32x32 __attribute__((ext_vector_type(32)));
#define DI __device__ __forceinline__
#define LDSP(T, a) ((__attribute__((address_space(3))) T*)(a))

constexpr int S_LEN = 2048, DM = 640, NH = 32, DH = 192, NSEG = 64, HD = NH * DH;
#define NSAMP 4
constexpr int NREG = 13;
constexpr int NV = 80, NVB = 40;
constexpr int NS = 64;
#define CFG 148, 104, 108, 20, 62, 16, 148, 104, 52, 148, 104, 6

constexpr size_t U_STRIDE = NV * 4096, D_STRIDE = NV * 16384;
constexpr size_t HO_STRIDE = 65536;
constexpr size_t OFF_ST = NSEG * HO_STRIDE;
enum { F_ACK = 0, F_RDY = 1, F_ONE = 3, F_GO = 8, F_VD = 16, F_QH = 96, F_B = 104, F_CUR = 128, F_PROG = 136 - 8 * 62, F_UD = 152, F_PX = 160, F_K = 224, F_END = 512, F_Q = 520, F_N = 592 };
constexpr size_t OFF_LAT = OFF_ST + F_N * 128;
constexpr size_t OFF_KX = 5u << 20;
constexpr size_t OFF_QX = OFF_KX + (size_t)S_LEN * HD * 2;
constexpr size_t OFF_D = OFF_QX + (size_t)S_LEN * HD * 2;
constexpr size_t OFF_U = (OFF_D + NSEG * D_STRIDE + 0x1fffff) & ~(size_t)0x1fffff;
constexpr size_t OFF_KM = OFF_U + (22u << 20);
constexpr size_t OFF_PX = OFF_KM + (size_t)NSEG * NH * 3072;
constexpr size_t OFF_QKVB = OFF_PX + (size_t)NSEG * 81920;
constexpr size_t OFF_OB = OFF_QKVB + (size_t)S_LEN * 1920 * 2;
constexpr size_t OFF_BOUT = OFF_OB + (size_t)S_LEN * DM * 2;
constexpr size_t OFF_GATE = OFF_BOUT + (size_t)S_LEN * DM * 2;
constexpr size_t OFF_XF = OFF_GATE + S_LEN * 4;
constexpr size_t WS_END = OFF_XF + (size_t)S_LEN * DM * 2;
static_assert(WS_END + (2u << 20) <= (256u << 20), "workspace");

typedef __bf16 bf16x2 __attribute__((ext_vector_type(2)));
typedef float f32x2 __attribute__((ext_vector_type(2)));
DI u32 cvtpk(float lo, float hi) { return __builtin_bit_cast(u32, __builtin_convertvector(f32x2{lo, hi}, bf16x2)); }
DI float rbf(float f) { return __uint_as_float(cvtpk(f, f) << 16); }
DI f32x4 mfma16(u32x4 a, u32x4 b, f32x4 c) {
    return __builtin_amdgcn_mfma_f32_16x16x32_bf16(__builtin_bit_cast(bf16x8, a), __builtin_bit_cast(bf16x8, b), c, 0, 0, 0);
}
DI u32x2 trread(u32 a) {
    return __builtin_bit_cast(u32x2, __builtin_amdgcn_ds_read_tr16_b64_v4i16(LDSP(s16x4, a)));
}
DI void glds16c(const char* sbase, u32 voff, u32 lds) {
    u32 sv;
    asm volatile("s_mov_b32 %0, m0\n\ts_mov_b32 m0, %3\n\ts_nop 0\n\tglobal_load_lds_dwordx4 %1, %2 sc1\n\ts_mov_b32 m0, %0"
                 : "=&s"(sv) : "v"(voff), "s"(sbase), "s"(lds) : "memory");
}
template <int IMM>
DI void gld16(u32x4& r, const char* sbase, u32 voff) {
    asm volatile("s_nop 4\n\tglobal_load_dwordx4 %0, %1, %2 offset:%3" : "=v"(r) : "v"(voff), "s"(sbase), "i"(IMM) : "memory");
}
DI void gst16(char* sbase, u32 voff, u32x4 v) {
    asm volatile("global_store_dwordx4 %0, %1, %2 sc1\n\ts_nop 1" : : "v"(voff), "v"(v), "s"(sbase) : "memory");
}
#define OST " nt"
DI void gst16p(char* sbase, u32 voff, u32x4 v) {
    asm volatile("s_nop 4\n\tglobal_store_dwordx4 %0, %1, %2" OST "\n\ts_nop 1" : : "v"(voff), "v"(v), "s"(sbase) : "memory");
}
DI void gst8(char* sbase, u32 voff, u32x2 v) {
    asm volatile("global_store_dwordx2 %0, %1, %2 sc1\n\ts_nop 1" : : "v"(voff), "v"(v), "s"(sbase) : "memory");
}
template <int N>
DI void waitvm() { asm volatile("s_waitcnt vmcnt(%0)" : : "i"(N) : "memory"); }
DI void barrier() { asm volatile("s_waitcnt lgkmcnt(0)\n\ts_barrier" : : : "memory"); }
#define PIN() __builtin_amdgcn_sched_barrier(0)
DI int uni(int v) { return __builtin_amdgcn_readfirstlane(v); }
DI int lanex() {
    int t;
    asm volatile("v_mbcnt_lo_u32_b32 %0, -1, 0\n\tv_mbcnt_hi_u32_b32 %0, -1, %0" : "=v"(t));
    __builtin_assume(t >= 0 && t < 64);
    return t;
}
DI void flag_ld(u32& r, const char* fl, int idx) {
    asm volatile("s_nop 4\n\tglobal_load_dword %0, %1, %2 sc1" : "=v"(r) : "v"((idx & 0xffff) * 128 + (idx >> 14)), "s"(fl) : "memory");
}
DI void flag_ldm(u32& r, const char* fl, int idx, bool on) {
    asm volatile("v_mov_b32 %0, 1000\n\ts_mov_b32 exec_lo, %3\n\ts_mov_b32 exec_hi, 0\n\tglobal_load_dword %0, %1, %2 sc1\n\ts_mov_b64 exec, -1" : "=&v"(r) : "v"(idx * 128), "s"(fl), "s"(uni(on ? 1 : 0)) : "memory");
}
DI void flag_wait(const char* fl, int idx, int target) {
#pragma nounroll
    for (int it = 0; it < (1 << 16); ++it) {
        u32 v;
        flag_ld(v, fl, idx);
        asm volatile("s_waitcnt vmcnt(0)" : "+v"(v) : : "memory");
        if (uni((int)(v + 1u)) >= target) break;
        __builtin_amdgcn_s_sleep(6);
    }
}
DI void flag_add(char* fl, int idx, int nrep) {
    const int lane = lanex();
    if (lane < nrep) asm volatile("s_nop 4\n\tglobal_atomic_add %0, %1, %2 sc1" : : "v"(((idx & 0xffff) + lane) * 128 + (idx >> 14)), "v"(1), "s"(fl) : "memory");
}
DI void acquire() { asm volatile("buffer_inv sc1\n\ts_waitcnt vmcnt(0)" : : : "memory"); }

template <int V> struct IC { static constexpr int v = V; };
template <int B, int E, class Fn>
DI void sfor(Fn&& fn) { if constexpr (B < E) { fn(IC<B>{}); sfor<B + 1, E>(fn); } }
enum { T_QK = 0, T_VD = 1, T_BL = 3 };
constexpr int GBUF = 24576, GRING = 6;
struct GemmArgs {
    const char *w1, *w1b;
    const char* w2;
    const char* x;
    char *out, *outb;
    int ldw;
    char* sig;
    int type, np1, tn;
    int sV, sP, sQ;
    int xb;
};
DI const char* unip(const char* p) {
    const u64 v = (u64)p;
    return (const char*)(((u64)(u32)uni((int)(v >> 32)) << 32) | (u32)uni((int)v));
}
template <int Z>
DI void mf1(f32x4& c, const u32x4& a, const u32x4& b) {
    if constexpr (Z) asm volatile("v_mfma_f32_16x16x32_bf16 %0, %1, %2, 0" : "=&v"(c) : "v"(a), "a"(b));
    else asm volatile("v_mfma_f32_16x16x32_bf16 %0, %1, %2, %0" : "+v"(c) : "v"(a), "a"(b));
}
template <int Z>
DI void mf2(f32x4& c, const u32x4& a, const u32x4& b) {
    if constexpr (Z) asm volatile("v_mfma_f32_16x16x32_bf16 %0, %1, %2, 0" : "=&v"(c) : "v"(a), "v"(b));
    else asm volatile("v_mfma_f32_16x16x32_bf16 %0, %1, %2, %0" : "+v"(c) : "v"(a), "v"(b));
}
template <int IMM>
DI void gld16a(u32x4& r, const char* sbase, u32 voff) {
    asm volatile("global_load_dwordx4 %0, %1, %2 offset:%3 sc1" : "+a"(r) : "v"(voff), "s"(sbase), "i"(IMM) : "memory");
}
DI void setm0(u32 v) { asm volatile("s_mov_b32 m0, %0" : : "s"(v) : "memory"); }
template <int IMM>
DI void gldsm(const char* sbase, u32 voff) {
    asm volatile("global_load_lds_dwordx4 %0, %1 offset:%2" : : "v"(voff), "s"(sbase), "i"(IMM) : "memory");
}
DI void gst16n(char* sbase, u32 voff, u32x4 v) {
    asm volatile("global_store_dwordx4 %0, %1, %2 sc1" : : "v"(voff), "v"(v), "s"(sbase) : "memory");
}
template <int N>
DI void waitbar() { asm volatile("s_waitcnt vmcnt(%0)\n\ts_barrier" : : "i"(N) : "memory"); }
#define LGKM(n) __builtin_amdgcn_s_waitcnt(0xC07F | ((n) << 8))

#define XSL 10
DI int xhalf() { u32 x; asm volatile("s_getreg_b32 %0, hwreg(HW_REG_XCC_ID)" : "=s"(x)); return (int)((x >> 2) & 1); }
struct Sync { u32x2 r, g, e; };
DI void sync_ld(Sync& y, const char* ws) {
    asm volatile("s_nop 4\n\tglobal_load_dwordx2 %0, %3, %5 sc1\n\tglobal_load_dwordx2 %1, %4, %5 offset:512 sc1\n\tglobal_load_dwordx2 %2, %4, %5 offset:1024 sc1"
                 : "=&v"(y.r), "=&v"(y.g), "=&v"(y.e) : "v"((lanex() & 31) * 8), "v"(0), "s"(ws + OFF_LAT) : "memory");
}
DI u64 sync_tag(u32x2 e) { return (((u64)e[1] << 32 | e[0]) + 1) << 1; }
DI int sync_end(Sync& y, const char* ws, u32& phi, bool ack) {
    asm volatile("" : "+v"(y.r), "+v"(y.g), "+v"(y.e));
#pragma nounroll
    for (int it = 0; it < (1 << 16); ++it) {
        const u64 t = sync_tag(y.e);
        if (__builtin_amdgcn_ballot_w64((y.r[0] & ~1u) != (u32)t || y.r[1] != (u32)(t >> 32) || y.g[0] != (u32)t) == 0) break;
        __builtin_amdgcn_s_sleep(XSL);
        sync_ld(y, ws);
        asm volatile("s_waitcnt vmcnt(0)" : "+v"(y.r), "+v"(y.g), "+v"(y.e) : : "memory");
    }
    if (ack) flag_add((char*)ws + OFF_ST, F_ACK, 1);
    phi = (u32)uni((int)y.g[1]);
    const u32 b = (u32)__builtin_amdgcn_ballot_w64(y.r[0] & 1), all = __builtin_popcount(b) > 16;
    u32 q = 0;
#pragma unroll
    for (int r = 0; r < NREG; ++r) {
        const u32 c = r < 2 ? __builtin_popcount(b & (0x55u << r)) + all : 2 * __builtin_popcount((b >> (4 + 2 * r)) & 3) + all;
        q |= ((c > 2) ^ (u32)((r < 2 ? r : r + (OFF_U >> 21)) & 1)) << r;
    }
    return uni((int)(q ^ 3u * xhalf()));
}
DI void pace(int on) {
    asm volatile("s_cmp_ge_i32 %0, 0\n\ts_cbranch_scc1 .Lp%=\n\ts_waitcnt vmcnt(0)\n.Lp%=:" : : "s"(on) : "memory", "scc");
}
#define SMO 48
#define RM 7
#define BLS 1
#define EPG 1
#define PXT 58
#define PDX 40
#define SPL 0
#define HP0 48
#define UPF 5
#define FRD 25
#define KNT 1
#define BAS 3
#define NO2 2
#define OW 2
#define GSL 6
#define UNAP 700
#define HD1 40
#define PRG 0
#define TG1 0
#define TG2 0
#define TGS 62
#define XFW 0
#define SMB 48
#define SMP SMO
template <int C>
DI void gap(int on) {
    if constexpr (C > 0) asm volatile("s_cmp_ge_i32 %0, 0\n\ts_cbranch_scc1 .Lg%=\n\ts_nop %1\n\ts_nop %2\n\ts_nop %3\n.Lg%=:" : : "s"(on), "n"(C / 12 - 1), "n"(C / 12 - 1), "n"(C / 12 - 1) : "memory", "scc");
}
DI void early_add(char* ctr, int c) {
    asm volatile("s_cmp_lg_u32 %1, 1\n\ts_cbranch_scc1 .Le%=\n\ts_mov_b64 exec, 1\n\ts_nop 1\n\tglobal_atomic_add %2, %3, %0 sc1\n\ts_mov_b64 exec, -1\n.Le%=:"
                 : : "s"(ctr), "s"(c), "v"(0), "v"(1) : "memory", "scc");
}
#define XL 2
template <int NT>
DI void gemm_task(const GemmArgs& g, Sync& y, const char* ws, u32 lds, int w) {
    constexpr int FD = 5, NX = 2 * NT, OPS = NX + 6, BI = 24 - FD;
    const int lane = lanex();
    const int n = lane & 15, q = lane >> 4, qq = (lane >> 2) & 3, pp = lane & 3;
    const bool p2 = g.type == T_VD;
    const int rw = lane >> 3, sp = (lane >> 1) & 3;
    const u32 v2b = (u32)(rw * 1280 + 16 * (lane & 1) + 5120);
    auto v2o = [&](int i) { return v2b + (u32)(((sp ^ (rw >> 1)) << 5) + i * 9216); };
    u32 wv[6];
#pragma unroll
    for (int i = 0; i < 6; ++i)
        wv[i] = (u32)((rw + 8 * (i / 3)) * g.ldw + ((sp ^ (((rw >> 1) & 1) + 2 * (i / 3))) << 5) + 16 * (lane & 1) + (i % 3) * 128 + 5120 - 1024 * i);
    const char* const wp2 = g.w2 + (size_t)(48 * w) * 1280 - 2048;
    const char* wp = unip(g.w1 + (size_t)(16 * w) * g.ldw - 2048);
    int wstep = uni(64 * g.ldw);
    u32 wdst = lds + (u32)w * 6144 + 3072;
    const u32 wdlast = wdst + (GRING - 1) * GBUF;
    auto wring = [&]() { wdst = wdst == wdlast ? wdst - (GRING - 1) * GBUF : wdst + GBUF; };
    auto wload = [&](int i) {
        setm0(wdst);
        wring();
        asm volatile("s_nop 0");
        sfor<0, 6>([&](auto I) { gldsm<1024 * (I.v - 3)>(wp, wv[I.v]); });
        wp += wstep;
    };
    u32x4 vf[NT][6];
    u32x4 fr[FD + 1];
    u32x4 xr[5][NX];
    auto xtie = [&](u32x4 (&x)[NX]) {
#pragma unroll
        for (int i = 0; i < NX; i += 4) asm volatile("" : "+a"(x[i]), "+a"(x[i + 1]), "+a"(x[i + 2]), "+a"(x[i + 3]));
    };
#pragma unroll
    for (int i = 0; i < 5 * NX; ++i) asm volatile("" : "=a"(xr[i / NX][i % NX]));
    u32x2 flo;
    u32 c[4], slot = 0;
    int sd = GBUF;
    auto sring = [&]() { sd = slot == (GRING - 1) * GBUF ? -(GRING - 1) * GBUF : GBUF; slot += (u32)sd; };
    {
        u32 xo[NT];
#pragma unroll
        for (int tt = 0; tt < NT; ++tt) {
            const int tok = 16 * (tt & 1) + n;
            xo[tt] = (u32)((NT / 2 * w + (tt >> 1)) * 40960 + (g.xb ? q * 512 + tok * 16 : q * 16 + tok * 1280));
        }
        f32x4 acc[12][NT];
        const bool rm = NT == 4 && g.type == T_BL;
        const int sw = (qq >> 1) + 2 * (q & 1);
#pragma unroll
        for (int t = 0; t < 4; ++t)
            c[t] = lds + q * 3072 + qq * 128 + (rm ? ((((t & 2) + (pp >> 1)) ^ sw) << 5) + 16 * (pp & 1) + 8 * ((pp >> 1) ^ (t & 1)) : ((t ^ sw) << 5) + 8 * pp);
        auto rlo = [&](int f) { flo = trread(c[f & 3] + (f / 12) * 12288 + ((f % 12) >> 2) * 1024); };
        auto rhi = [&](int f) {
            const u32x2 hi = trread(c[f & 3] + (f / 12) * 12288 + ((f % 12) >> 2) * 1024 + 512);
            fr[f % (FD + 1)] = u32x4{flo[0], flo[1], hi[0], hi[1]};
        };
        const int xk = uni(g.xb ? 2048 : 64);
        const char *xp = g.x, *xq = xp + xk;
        sfor<0, 4>([&](auto J) {
            if constexpr (J.v < XL) { sfor<0, NX>([&](auto I) { gld16a<0>(xr[J.v][I.v], I.v & 1 ? xq : xp, xo[I.v >> 1]); }); xp += 2 * xk; xq += 2 * xk; }
            wload(J.v);
            if constexpr (NT == 4 && PRG && J.v < 3) asm volatile("s_sleep %0" : : "n"(PRG));
        });
        waitbar<18 + (XL - 1) * NX>();
        xtie(xr[0]);
#pragma unroll
        for (int i = 0; i < FD; ++i) { rlo(i); rhi(i); }
        char* po = g.out;
#pragma nounroll
        for (int ph = g.np1; ph > 0; --ph) {
            sfor<0, 10>([&](auto J) {
                constexpr int j = J.v;
                u32x4 (&xc)[NX] = xr[j % 5];
                u32x4 (&xn)[NX] = xr[(j + XL) % 5];
                sfor<0, 24>([&](auto I) {
                    constexpr int idx = I.v, kk = idx / 12, T = idx % 12, z = j == 0 && kk == 0, f = (idx + FD) % 24;
                    constexpr int mo = NT == 4 ? (idx < BI && idx % 4 != 2 ? idx - (idx + 1) / 4 : -1) : (idx < BI && idx % 2 == 0 ? idx / 2 : -1);
                    auto mem = [&]() {
                        if constexpr (mo >= NX) gldsm<1024 * (mo - NX - 3)>(wp, wv[mo - NX]);
                        else if constexpr (mo >= 0) gld16a<0>(xn[mo], mo & 1 ? xq : xp, xo[mo >> 1]);
                    };
                    auto misc = [&]() {
                        if constexpr (idx == 1) wring();
                        if constexpr (idx == 13) sring();
                        if constexpr (j == 1 && idx == 22) early_add(g.sig, ph + w);
                        if constexpr (idx >= 15 && idx < BI) { c[idx - 15] += (u32)sd; asm volatile("" : "+v"(c[idx - 15])); }
                        const bool more = ph > 1;
                        if constexpr (j != 5) {
                            if constexpr (idx == 20) wp += wstep;
                        } else {
                            if constexpr (idx == 19) { wp = more ? g.w1b + (size_t)(16 * w) * g.ldw - 2048 : wp2; wstep = more ? wstep : (p2 ? 128 : 0); }
                            if constexpr (idx >= 20 && idx < 23) {
#pragma unroll
                                for (int i = 2 * (idx - 20); i < 2 * (idx - 19); ++i) { wv[i] = more ? wv[i] : (p2 ? v2o(i) : 5120u); asm volatile("" : "+v"(wv[i])); }
                            }
                        }
                        if constexpr (j != 9 - XL) {
                            if constexpr (idx == 21) xp += 2 * xk;
                            if constexpr (idx == 22) xq += 2 * xk;
                        } else if constexpr (idx == 23) {
                            xp = g.x; xq = xp + xk;
#pragma unroll
                            for (int i = 0; i < NT; ++i) { xo[i] = more ? xo[i] : 0u; asm volatile("" : "+v"(xo[i])); }
                        }
                    };
                    if constexpr (idx == BI) { waitbar<6 + (XL - 1) * OPS>(); xtie(xr[(j + 1) % 5]); }
                    if constexpr (idx % 2 == 0) LGKM(6);
                    PIN();
                    const u32x4 a = fr[idx % (FD + 1)];
                    mf1<z>(acc[T][0], a, xc[kk]); PIN();
                    rlo(f);
                    if constexpr (idx == 0) setm0(wdst);
                    if constexpr (NT == 2) rhi(f);
                    PIN();
                    mf1<z>(acc[T][1], a, xc[2 + kk]); PIN();
                    if constexpr (NT == 4) {
                        rhi(f); PIN();
                        mf1<z>(acc[T][2], a, xc[4 + kk]); PIN();
                        mem(); PIN();
                        mf1<z>(acc[T][3], a, xc[6 + kk]); PIN();
                    } else { mem(); PIN(); }
                    misc();
                    PIN();
                });
            });
            asm volatile("s_nop 7\n\ts_nop 1");
            PIN();
#pragma unroll
            for (int tt = 0; tt < NT; ++tt)
#pragma unroll
                for (int ks = 0; ks < 6; ++ks)
                    vf[tt][ks] = u32x4{cvtpk(acc[2 * ks][tt][0], acc[2 * ks][tt][1]), cvtpk(acc[2 * ks][tt][2], acc[2 * ks][tt][3]),
                                       cvtpk(acc[2 * ks + 1][tt][0], acc[2 * ks + 1][tt][1]), cvtpk(acc[2 * ks + 1][tt][2], acc[2 * ks + 1][tt][3])};
            if (g.type == T_QK) {
#pragma unroll
                for (int tt = 0; tt < NT; ++tt)
#pragma unroll
                    for (int ks = 0; ks < 6; ++ks) {
                        gst16(po + (size_t)(NT / 2 * w + (tt >> 1)) * (NH * 12288) + (tt & 1) * 6144 + ks * 1024, lane * 16,
                              u32x4{vf[tt][ks][0], vf[tt][ks][2], vf[tt][ks][1], vf[tt][ks][3]});
                        if (NT == 4 && EPG) asm volatile("s_sleep %0" : : "n"(EPG));
                    }
            } else if (rm) {
                const bool sx = q >= 2;
#pragma unroll
                for (int i = 0; i < 3 * NT; ++i) {
                    const int tp = i / 6, ks = i % 6;
                    u32x4 o[2];
#pragma unroll
                    for (int j = 0; j < 4; ++j) {
                        const u32x4 &v = vf[2 * tp][ks], &u = vf[2 * tp + 1][ks];
                        if (BLS) {
                            const auto r = __builtin_amdgcn_permlane32_swap(v[j], u[j], false, false);
                            o[0][j] = r[0]; o[1][j ^ 2] = r[1];
                        } else { o[0][j] = sx ? v[j ^ 2] : v[j]; o[1][j] = sx ? u[j ^ 2] : u[j]; }
                    }
#pragma unroll
                    for (int j = 0; j < 2; ++j) {
                        gst16(po + (size_t)(2 * w + tp) * g.sV + j * (BLS ? 1024 : 256) + ks * 2048, BLS ? (u32)((q & 1) * 512 + (q >> 1) * 256 + n * 16) : (u32)(q * 512 + n * 16), o[j]);
                        if (EPG) asm volatile("s_sleep %0" : : "n"(EPG));
                    }
                }
            }
            po = g.outb;
            PIN();
        }
    }
    if (p2) {
        constexpr int HI = 2048;
        const int r = 4 * (q & 1) + qq, sw = r >> 1;
#pragma unroll
        for (int t = 0; t < 4; ++t) c[t] = lds + slot + (q >> 1) * 1024 + r * 128 + ((t ^ sw) << 5) + 8 * pp;
        sring();
        u32 so = (u32)((n >> 3) * g.sV + q * g.sQ + (n & 7) * 16);
        char* const o0 = g.out + (size_t)(NT / 2 * w) * g.sP;
        char* const o1 = o0 + g.sP;
        const int sv2 = 2 * g.sV;
        auto rlo = [&](int f) { flo = trread(c[f / 6] + (f % 6) * 4096); };
        auto rhi = [&](int f) {
            const u32x2 hi = trread(c[f / 6] + (f % 6) * 4096 + HI);
            fr[f % (FD + 1)] = u32x4{flo[0], flo[1], hi[0], hi[1]};
        };
        f32x4 a2[2][NT];
        u32 e[NT / 2][4];
        auto cv = [&](f32x4 (&a)[NT], int i) { e[i >> 2][i & 3] = cvtpk(a[i >> 1][2 * (i & 1)], a[i >> 1][2 * (i & 1) + 1]); asm volatile("" : "+v"(e[i >> 2][i & 3])); };
        auto st = [&](int pl) { gst16n(pl ? o1 : o0, so, u32x4{e[pl][0], e[pl][1], e[pl][2], e[pl][3]}); };
        PIN();
#pragma unroll
        for (int i = 0; i < FD; ++i) { rlo(i); rhi(i); }
#pragma unroll
        for (int tt = 0; tt < NT; ++tt) a2[1][tt] = f32x4{0.f, 0.f, 0.f, 0.f};
#pragma nounroll
        for (int t = 0; t < g.tn; ++t) {
            sfor<0, 24>([&](auto I) {
                constexpr int idx = I.v, cb = idx / 6, ks = idx % 6, f = (idx + FD) % 24;
                f32x4 (&ac)[NT] = a2[cb & 1];
                f32x4 (&ap)[NT] = a2[(cb & 1) ^ 1];
                constexpr int mo = cb < 2 ? (ks == 0 ? 3 * cb : (ks >= 4 ? 3 * cb + ks - 3 : -1)) : -1;
                auto mem = [&]() {
                    if constexpr (mo >= 0) gldsm<1024 * (mo - 3)>(wp, wv[mo]);
                    if constexpr (ks == 3 || (NT == 4 && ks == 2)) { if (cb > 0 || t > 0) st(NT == 4 ? ks - 2 : 0); }
                };
                auto misc = [&]() {
                    if constexpr (idx == 12) wring();
                    if constexpr (idx == 23) sring();
                    if constexpr (idx == 16) { wp += t < g.tn - 5 ? 128 : 0; }
                    if constexpr (ks == 0) { c[cb] += (u32)sd; asm volatile("" : "+v"(c[cb])); }
                    if constexpr (ks == 4) { so += (cb > 0 || t > 0) ? (u32)sv2 : 0u; asm volatile("" : "+v"(so)); }
                };
                if constexpr (idx == BI) waitbar<NT == 4 ? 38 : 28>();
                if constexpr (idx % 2 == 0) LGKM(6);
                PIN();
                const u32x4 b = fr[idx % (FD + 1)];
                mf2<ks == 0>(ac[0], vf[0][ks], b); PIN();
                rlo(f);
                if constexpr (idx == 0) setm0(wdst);
                if constexpr (NT == 2) rhi(f);
                if constexpr (NT == 4 && ks >= 1 && ks <= 3) cv(ap, ks == 1 ? 0 : (ks == 2 ? 4 : 7));
                PIN();
                mf2<ks == 0>(ac[1], vf[1][ks], b); PIN();
                if constexpr (NT == 4) {
                    rhi(f);
                    if constexpr (ks == 1 || ks == 2) cv(ap, ks == 1 ? 1 : 5);
                    PIN();
                    mf2<ks == 0>(ac[2], vf[2][ks], b); PIN();
                    mem();
                    if constexpr (ks == 1) { cv(ap, 2); cv(ap, 3); }
                    PIN();
                    mf2<ks == 0>(ac[3], vf[3][ks], b); PIN();
                    if constexpr (ks == 2) cv(ap, 6);
                } else {
                    if constexpr (ks == 1 || ks == 2) { cv(ap, 2 * ks - 2); cv(ap, 2 * ks - 1); }
                    mem(); PIN();
                }
                misc();
                PIN();
            });
            asm volatile("s_nop 7\n\ts_nop 1");
        }
        PIN();
#pragma unroll
        for (int i = 0; i < 2 * NT; ++i) cv(a2[1], i);
#pragma unroll
        for (int pl = 0; pl < NT / 2; ++pl) st(pl);
        asm volatile("s_nop 1");
    }
    if constexpr (NT == 2) asm volatile(".Lkm_g2:"); else asm volatile(".Lkm_g4:");
    sync_ld(y, ws);
    waitvm<0>();
    asm volatile("" : "+v"(y.r), "+v"(y.g), "+v"(y.e));
    sfor<0, 5>([&](auto I) { xtie(xr[I.v]); });
}

struct Ptrs {
    const char *x, *mem0, *wq, *wk, *wv, *wo, *wqkvb, *wob, *gw;
    char* out;
    char* ws;
};

enum { Q_H = 0, Q_VD = 1, Q_KQ = 2, Q_QKVB = 3, Q_BA = 4, Q_BO = 5, Q_PX = 6, Q_OUT = 7 };
DI void run_gemm(const Ptrs& p, int ty, int b, int i, int hf, bool raw, Sync& y, u32 lds, int w) {
    GemmArgs g;
    g.sV = g.sP = g.sQ = 0; g.w2 = p.wo; g.ldw = HD * 2; g.np1 = 1; g.tn = 10;
    g.sig = p.ws + OFF_ST + F_END * 128;
    const char* xf = raw ? p.x : p.ws + OFF_XF;
    g.xb = !raw;
    int s0 = 8 * b;
    if (ty == Q_VD) {
        s0 = b < 2 ? 4 * b : s0 - 8;
        g.type = T_VD; g.w1 = g.w1b = p.wv + i * DH * 2;
        g.w2 = p.wo + (size_t)i * DH * 1280;
        g.out = g.outb = p.ws + OFF_D + (size_t)s0 * D_STRIDE + i * 512;
        g.sV = 16384; g.sP = (int)D_STRIDE; g.sQ = 128;
        if (b == 0) { g.tn = 5; g.w2 += hf * 640; g.out += hf * 40 * 16384; }
    } else if (ty == Q_H) {
        s0 = 0;
        g.type = T_QK; g.w1 = g.w1b = (b ? p.wq : p.wk) + i * DH * 2;
        g.out = g.outb = p.ws + (b ? OFF_QX : OFF_KX) + (size_t)i * 12288;
        b = 0;
    } else if (ty == Q_KQ) {
        s0 = b < 2 ? 4 * b : s0 - 8;
        g.type = T_QK; g.w1 = p.wk + i * DH * 2; g.w1b = p.wq + i * DH * 2;
        g.out = p.ws + OFF_KX + (size_t)(s0 * NH + i) * 12288;
        g.outb = g.out + (OFF_QX - OFF_KX);
        g.np1 = 2;
        g.sig = p.ws + OFF_ST + (F_K + b * 32 + i) * 128;
    } else if (ty == Q_QKVB) {
        g.type = T_BL; g.w1 = p.wqkvb + i * 768; g.w1b = g.w1 + 384; g.ldw = 3840;
        g.sV = 1920 * 64; g.np1 = 2;
        g.out = p.ws + OFF_QKVB + (size_t)s0 * g.sV + i * 48 * 512; g.outb = g.out + 24 * 512;
    } else {
        const int c0 = min(i * DH, DM - DH);
        g.type = T_BL; g.w1 = g.w1b = p.wob + c0 * 2; g.ldw = 1280;
        xf = p.ws + OFF_OB;
        g.sV = 40960;
        g.out = g.outb = p.ws + OFF_BOUT + (size_t)s0 * 40960 + c0 * 64;
    }
    g.x = xf + (size_t)s0 * 40960;
    if (ty <= Q_KQ && b < 2) gemm_task<2>(g, y, p.ws, lds, w);
    else gemm_task<4>(g, y, p.ws, lds, w);
}
DI int pblk(int s) { return s < 8 ? s >> 2 : min(s >> 3, 7) + 1; }

template <int IMM>
DI void gld16c(u32x4& r, const char* sbase, u32 voff) {
    asm volatile("global_load_dwordx4 %0, %1, %2 offset:%3 sc1" : "=v"(r) : "v"(voff), "s"(sbase), "i"(IMM) : "memory");
}
#define XP1 " nt"
template <int IMM>
DI void gld16x(u32x4& r, const char* sbase, u32 voff) {
    asm volatile("global_load_dwordx4 %0, %1, %2 offset:%3 sc1" XP1 : "=v"(r) : "v"(voff), "s"(sbase), "i"(IMM) : "memory");
}
DI void gst4c(char* sbase, u32 voff, u32 v) {
    asm volatile("global_store_dword %0, %1, %2 sc1" : : "v"(voff), "v"(v), "s"(sbase) : "memory");
}
template <int N>
DI void waitv(u32x4& r) { asm volatile("s_waitcnt vmcnt(%1)" : "+v"(r) : "i"(N) : "memory"); }
DI u32 mt() { return (u32)__builtin_amdgcn_s_memtime(); }
DI u32 rt() { return (u32)__builtin_amdgcn_s_memrealtime(); }
DI void spin_to(u32 tgt) { while ((int)(mt() - tgt) < 0) {} }
DI u32 max3u(u32 a, u32 b, u32 c) { return max(max(a, b), c); }
DI u32x4 ldsr16(u32 a) { return *LDSP(u32x4, a); }
DI void clocks(u32& r, u32& m) {
    u64 a, b;
    asm volatile("s_memrealtime %0\n\ts_memtime %1\n\ts_waitcnt lgkmcnt(0)" : "=s"(a), "=s"(b) : : "memory");
    r = (u32)a; m = (u32)b;
}
#define SLATE 20
DI void edge(u32& r, u32& m) {
    u64 a, t[9];
    u32 n = 0;
#define RT(i) "s_memrealtime %" #i "\n\ts_nop 1\n\t"
#define RC(i) "\n\ts_cmp_eq_u64 %" #i ", %1\n\ts_addc_u32 %10, %10, 0"
    asm volatile("s_memtime %0\n\t" RT(1) RT(2) RT(3) RT(4) RT(5) RT(6) RT(7) RT(8) "s_memrealtime %9\n\ts_waitcnt lgkmcnt(0)" RC(2) RC(3) RC(4) RC(5) RC(6) RC(7) RC(8) RC(9)
                 : "=&s"(a), "=&s"(t[0]), "=&s"(t[1]), "=&s"(t[2]), "=&s"(t[3]), "=&s"(t[4]), "=&s"(t[5]), "=&s"(t[6]), "=&s"(t[7]), "=&s"(t[8]), "+s"(n) : : "scc", "memory");
    m = (u32)a + 12 * n;
    r = (u32)t[0] + 4;
}
#define SPB 90
struct Clock {
    u32 r0, m0, r1, m1; int cpt;
    DI void init() { edge(r0, m0); r1 = r0; m1 = m0; cpt = 24 * 64; }
    DI u32 sched(u32& phi, int ofs, int G, int mode) {
        if (mode == 0) edge(r1, m1);
        u32 r = r1, m = m1;
        if (mode == 2) clocks(r, m);
        int rem = (int)(phi + (u32)ofs - r);
        if (__builtin_expect(rem < -SLATE, 0)) { const int k = (-rem / G + 1) * G; phi += (u32)k; rem += k; }
        return m + (u32)((min(rem, 3000) * cpt) >> 6) - SPB;
    }
    DI u32 at(u32 t) { u32 r, m; clocks(r, m); return m + (u32)((min(max((int)(t - r), 0), 3000) * cpt) >> 6); }
    DI void calib() {
        const int dr = (int)(r1 - r0);
        if (dr >= 60) { int c = (int)fminf(fmaxf((float)(int)(m1 - m0) * 64.f * __builtin_amdgcn_rcpf((float)dr), 256.f), 2560.f); asm volatile("" : "+v"(c)); cpt = uni(c); r0 = r1; m0 = m1; }
    }
};
#define VISS 0
DI void follow(u32& phi, int ofs, u32 t, int G) { const int late = (int)(t - phi) - ofs; if (__builtin_expect(late > 0, 0)) phi += (u32)((late / G + 1) * G); }
DI bool unwritten(const char* base, u32 off) {
#pragma nounroll
    for (int i = 0; i < 10; ++i) {
        u32x4 t;
        gld16c<-4096>(t, base + (i + (i & 8)) * 1024, off); waitv<0>(t);
        if (__builtin_amdgcn_ballot_w64(max3u(max(t[0], t[1]), t[2], t[3]) == 0xffffffffu) != 0) return true;
    }
    return false;
}

DI size_t uslot(int sd, int s) { const int r = (s * 43) >> 8; return OFF_U + (u32)uni(s * (int)U_STRIDE + r * (int)((2u << 20) - 6 * U_STRIDE) + ((sd >> (2 + r)) & 1) * 8192); }
DI int uchunk(int v) { return (2 * (v & 3) + ((v >> 2) & 1) + (v & ~7)) * 8192; }
DI size_t mslot(int sd, int s) { return (u32)uni(s * (int)HO_STRIDE + ((sd >> (s >> 5)) & 1) * 8192 + 4096); }
struct ChainCfg { int T, cR, G, dV, fast, s1, T2, cR2, s2, T3, cR3, dU; };
constexpr int TT_OFF = 88 + 48 + 360;
DI void karg_warm() {
    u32 a[13];
    asm volatile("s_load_dword %0, %13, 0\n\t" "s_load_dword %1, %13, 64\n\t" "s_load_dword %2, %13, 128\n\t" "s_load_dword %3, %13, 192\n\t" "s_load_dword %4, %13, 256\n\t" "s_load_dword %5, %13, 320\n\t" "s_load_dword %6, %13, 384\n\t" "s_load_dword %7, %13, 448\n\t" "s_load_dword %8, %13, 512\n\t" "s_load_dword %9, %13, 576\n\t" "s_load_dword %10, %13, 640\n\t" "s_load_dword %11, %13, 704\n\t" "s_load_dword %12, %13, 748\n\t" "s_waitcnt lgkmcnt(0)"
                 : "=&s"(a[0]), "=&s"(a[1]), "=&s"(a[2]), "=&s"(a[3]), "=&s"(a[4]), "=&s"(a[5]), "=&s"(a[6]), "=&s"(a[7]), "=&s"(a[8]), "=&s"(a[9]), "=&s"(a[10]), "=&s"(a[11]), "=&s"(a[12])
                 : "s"(__builtin_amdgcn_kernarg_segment_ptr()) : "memory");
}
DI u32 sched_ld0(int s) {
    u32 r;
    asm volatile("s_load_dword %0, %1, %2 offset:%3\n\ts_waitcnt lgkmcnt(0)" : "=s"(r) : "s"(__builtin_amdgcn_kernarg_segment_ptr()), "s"(4 * s), "n"(TT_OFF) : "memory");
    return r;
}
#define HP 60
#define HQ 96

constexpr int SL_W = 0;
constexpr int SL_IMG = 131072;
constexpr int SL_KX = SL_IMG + 6656;
constexpr int SL_PF = SL_KX + 12288 + 1024;
constexpr int S_LDS = SL_PF + 256;
DI void nap(int t) { const u32 wake = rt() + (u32)t; while ((int)(rt() - wake) < 0) __builtin_amdgcn_s_sleep(4); }
DI void until(u32 t) { while ((int)(rt() - t) < 0) __builtin_amdgcn_s_sleep(1); }
DI void dly(int n) {
    asm volatile(".Ld%=:\n\ts_sub_u32 %0, %0, 1\n\ts_nop 0\n\ts_cbranch_scc0 .Ld%=" : "+s"(n) : : "scc", "memory");
}
DI void rdy_add(u32& r, char* fl) {
    asm volatile("s_mov_b64 exec, 1\n\ts_nop 3\n\tglobal_atomic_add %0, %1, %2, %3 sc0 sc1\n\ts_mov_b64 exec, -1" : "=&v"(r) : "v"(F_RDY * 128), "v"(1), "s"(fl) : "memory");
}
DI void rdy_end(u32 r, char* fl, int dt) {
    if (uni((int)r) == NS + NVB - 2 && lanex() < 8) gst4c(fl, (F_GO + lanex()) * 128, (rt() + (u32)dt) & ~1u);
}
DI u32 go_wait(const char* fl, int i) {
    u32 v;
#pragma nounroll
    for (int it = 0; it < (1 << 16); ++it) {
        flag_ld(v, fl, F_GO + (i & 7));
        asm volatile("s_waitcnt vmcnt(0)" : "+v"(v) : : "memory");
        if (uni((int)v) != -1) break;
        __builtin_amdgcn_s_sleep(GSL);
    }
    return (u32)uni((int)v);
}
DI u32 go_wg(const char* fl, int i, int w, u32 la) {
    if (w == 0) *LDSP(u32, la) = go_wait(fl, i);
    barrier();
    return (u32)uni((int)*LDSP(u32, la));
}
DI void pfl(const char* sbase, u32 voff, u32 lds) {
    asm volatile("s_mov_b32 m0, %2\n\ts_nop 3\n\tglobal_load_lds_dword %0, %1" : : "v"(voff), "s"(sbase), "s"(lds + SL_PF) : "memory");
}
DI void score_role(const Ptrs& p, const ChainCfg cfg, int h, u32 lds, int w) {
    const int lane = lanex(), tid = 64 * w + lane;
    const int n = lane & 15, g = lane >> 4, qq = (lane >> 2) & 3, pp = lane & 3;
    u32x32 wa[8], wv[3];
    const u32 wreg = lds + SL_W + (u32)w * 32768;
    const u32 lfa = wreg + lane * 16;
    const char* kxp = p.ws + OFF_KX + (size_t)h * 12288 + w * 3072;
    const u32 kxl = lds + SL_KX;
    char* const fl = p.ws + OFF_ST;
    const u32 kxw = kxl + w * 3072 - (w < 2 ? 16 : 0);
    auto kxdma = [&]() {
        asm volatile("s_mov_b32 m0, %2\n\ts_nop 3\n\tglobal_load_lds_dwordx4 %0, %1 sc1\n\tglobal_load_lds_dwordx4 %0, %1 offset:1024 sc1\n\tglobal_load_lds_dwordx4 %0, %1 offset:2048 sc1"
                     : : "v"(lanex() * 16), "s"(kxp), "s"(kxw) : "memory");
    };
    u32 kfl, phi;
    int sd;
    {
        constexpr int GS = 3104;
        Sync y;
        if (w < 3) {
#pragma unroll
            for (int i = PXT; i < NSEG; ++i) gst16(p.ws + OFF_KM + (i * NH + h) * 3072 + w * 1024, lane * 16, u32x4{~0u, ~0u, ~0u, ~0u});
#pragma unroll
            for (int i = NSEG - 4; i < NSEG; ++i) gst16(p.ws + OFF_PX + i * 81920 + (w < 2 ? h * 2048 + w * 1024 : 65536 + h * 512), w < 2 ? lane * 16 : (lane * 16) & 511, u32x4{~0u, ~0u, ~0u, ~0u});
        }
        auto dvo = [&](int i) { const int ix = i * 64 + lane; return (u32)((ix / 24) * (HD * 2) + (ix % 24) * 16); };
        const u32 tra = lds + (2 * (g >> 1) + (qq >> 1)) * GS + (4 * (g & 1) + (qq & 1)) * 384 + 8 * pp;
        u32x8 pa = u32x8{dvo(0), dvo(1), dvo(2), tra + 96 * w + 32 * (w & 1), tra + 96 * w - 64 * (w & 1), tra + 32 * w, lfa, (u32)(lane & 31) * 8};
        asm volatile("s_nop 4\n\t" "s_add_u32 s42, %21, 0x0\n\t" "s_add_u32 s42, s46, s42\n\t" "s_addc_u32 s43, s47, 0\n\t" "s_add_u32 m0, %18, 0x0\n\t" "s_nop 0\n\t" "global_load_lds_dwordx4 v224, s[42:43]\n\t"
                     "s_add_u32 m0, %18, 0x400\n\t" "s_nop 0\n\t" "global_load_lds_dwordx4 v225, s[42:43]\n\t" "s_add_u32 m0, %18, 0x800\n\t" "s_nop 0\n\t" "global_load_lds_dwordx4 v226, s[42:43]\n\t"
                     "s_add_u32 s42, %21, 0x18000\n\t" "s_add_u32 s42, s46, s42\n\t" "s_addc_u32 s43, s47, 0\n\t" "s_add_u32 m0, %18, 0xc20\n\t" "s_nop 0\n\t" "global_load_lds_dwordx4 v224, s[42:43]\n\t"
                     "s_add_u32 m0, %18, 0x1020\n\t" "s_nop 0\n\t" "global_load_lds_dwordx4 v225, s[42:43]\n\t" "s_add_u32 m0, %18, 0x1420\n\t" "s_nop 0\n\t" "global_load_lds_dwordx4 v226, s[42:43]\n\t"
                     "s_sub_u32 s42, 0xc0000, %21\n\t" "s_add_u32 s42, s46, s42\n\t" "s_addc_u32 s43, s47, 0\n\t" "s_add_u32 m0, %18, 0x6100\n\t" "s_nop 0\n\t" "global_load_lds_dwordx4 v224, s[42:43]\n\t"
                     "s_add_u32 m0, %18, 0x6500\n\t" "s_nop 0\n\t" "global_load_lds_dwordx4 v225, s[42:43]\n\t" "s_add_u32 m0, %18, 0x6900\n\t" "s_nop 0\n\t" "global_load_lds_dwordx4 v226, s[42:43]\n\t"
                     "s_sub_u32 s42, 0xd8000, %21\n\t" "s_add_u32 s42, s46, s42\n\t" "s_addc_u32 s43, s47, 0\n\t" "s_add_u32 m0, %18, 0x6d20\n\t" "s_nop 0\n\t" "global_load_lds_dwordx4 v224, s[42:43]\n\t"
                     "s_add_u32 m0, %18, 0x7120\n\t" "s_nop 0\n\t" "global_load_lds_dwordx4 v225, s[42:43]\n\t" "s_add_u32 m0, %18, 0x7520\n\t" "s_nop 0\n\t" "global_load_lds_dwordx4 v226, s[42:43]\n\t"
                     "s_add_u32 s42, %21, 0x180000\n\t" "s_add_u32 s42, s46, s42\n\t" "s_addc_u32 s43, s47, 0\n\t" "s_add_u32 m0, %18, 0xc200\n\t" "s_nop 0\n\t" "global_load_lds_dwordx4 v224, s[42:43]\n\t"
                     "s_add_u32 m0, %18, 0xc600\n\t" "s_nop 0\n\t" "global_load_lds_dwordx4 v225, s[42:43]\n\t" "s_add_u32 m0, %18, 0xca00\n\t" "s_nop 0\n\t" "global_load_lds_dwordx4 v226, s[42:43]\n\t"
                     "s_add_u32 s42, %21, 0x198000\n\t" "s_add_u32 s42, s46, s42\n\t" "s_addc_u32 s43, s47, 0\n\t" "s_add_u32 m0, %18, 0xce20\n\t" "s_nop 0\n\t" "global_load_lds_dwordx4 v224, s[42:43]\n\t"
                     "s_add_u32 m0, %18, 0xd220\n\t" "s_nop 0\n\t" "global_load_lds_dwordx4 v225, s[42:43]\n\t" "s_add_u32 m0, %18, 0xd620\n\t" "s_nop 0\n\t" "global_load_lds_dwordx4 v226, s[42:43]\n\t"
                     "s_sub_u32 s42, 0x240000, %21\n\t" "s_add_u32 s42, s46, s42\n\t" "s_addc_u32 s43, s47, 0\n\t" "s_add_u32 m0, %18, 0x12300\n\t" "s_nop 0\n\t" "global_load_lds_dwordx4 v224, s[42:43]\n\t"
                     "s_add_u32 m0, %18, 0x12700\n\t" "s_nop 0\n\t" "global_load_lds_dwordx4 v225, s[42:43]\n\t" "s_add_u32 m0, %18, 0x12b00\n\t" "s_nop 0\n\t" "global_load_lds_dwordx4 v226, s[42:43]\n\t"
                     "s_sub_u32 s42, 0x258000, %21\n\t" "s_add_u32 s42, s46, s42\n\t" "s_addc_u32 s43, s47, 0\n\t" "s_add_u32 m0, %18, 0x12f20\n\t" "s_nop 0\n\t" "global_load_lds_dwordx4 v224, s[42:43]\n\t"
                     "s_add_u32 m0, %18, 0x13320\n\t" "s_nop 0\n\t" "global_load_lds_dwordx4 v225, s[42:43]\n\t" "s_add_u32 m0, %18, 0x13720\n\t" "s_nop 0\n\t" "global_load_lds_dwordx4 v226, s[42:43]\n\t"
                     "s_waitcnt vmcnt(12) lgkmcnt(0)\n\t" "s_barrier\n\t" "s_add_u32 s42, %21, 0x300000\n\t" "s_add_u32 s42, s46, s42\n\t" "s_addc_u32 s43, s47, 0\n\t" "s_add_u32 m0, %18, 0x18400\n\t" "s_nop 0\n\t"
                     "global_load_lds_dwordx4 v224, s[42:43]\n\t" "s_add_u32 m0, %18, 0x18800\n\t" "s_nop 0\n\t" "global_load_lds_dwordx4 v225, s[42:43]\n\t" "s_add_u32 m0, %18, 0x18c00\n\t" "s_nop 0\n\t"
                     "global_load_lds_dwordx4 v226, s[42:43]\n\t" "s_add_u32 s42, %21, 0x318000\n\t" "s_add_u32 s42, s46, s42\n\t" "s_addc_u32 s43, s47, 0\n\t" "s_add_u32 m0, %18, 0x19020\n\t" "s_nop 0\n\t"
                     "global_load_lds_dwordx4 v224, s[42:43]\n\t" "s_add_u32 m0, %18, 0x19420\n\t" "s_nop 0\n\t" "global_load_lds_dwordx4 v225, s[42:43]\n\t" "s_add_u32 m0, %18, 0x19820\n\t" "s_nop 0\n\t"
                     "global_load_lds_dwordx4 v226, s[42:43]\n\t" "s_sub_u32 s42, 0x3c0000, %21\n\t" "s_add_u32 s42, s46, s42\n\t" "s_addc_u32 s43, s47, 0\n\t" "s_add_u32 m0, %18, 0x1e500\n\t" "s_nop 0\n\t"
                     "global_load_lds_dwordx4 v224, s[42:43]\n\t" "s_add_u32 m0, %18, 0x1e900\n\t" "s_nop 0\n\t" "global_load_lds_dwordx4 v225, s[42:43]\n\t" "s_add_u32 m0, %18, 0x1ed00\n\t" "s_nop 0\n\t"
                     "global_load_lds_dwordx4 v226, s[42:43]\n\t" "s_sub_u32 s42, 0x3d8000, %21\n\t" "s_add_u32 s42, s46, s42\n\t" "s_addc_u32 s43, s47, 0\n\t" "s_add_u32 m0, %18, 0x1f120\n\t" "s_nop 0\n\t"
                     "global_load_lds_dwordx4 v224, s[42:43]\n\t" "s_add_u32 m0, %18, 0x1f520\n\t" "s_nop 0\n\t" "global_load_lds_dwordx4 v225, s[42:43]\n\t" "s_add_u32 m0, %18, 0x1f920\n\t" "s_nop 0\n\t"
                     "global_load_lds_dwordx4 v226, s[42:43]\n\t" "v_add_u32 v232, 0x0, v229\n\t" "ds_read_b64_tr_b16 v[8:9], v232\n\t" "ds_read_b64_tr_b16 v[10:11], v232 offset:768\n\t"
                     "ds_read_b64_tr_b16 v[12:13], v232 offset:12416\n\t" "ds_read_b64_tr_b16 v[14:15], v232 offset:13184\n\t" "ds_read_b64_tr_b16 a[96:97], v232 offset:128\n\t"
                     "ds_read_b64_tr_b16 a[98:99], v232 offset:896\n\t" "ds_read_b64_tr_b16 a[100:101], v232 offset:12544\n\t" "ds_read_b64_tr_b16 a[102:103], v232 offset:13312\n\t"
                     "ds_read_b64_tr_b16 a[176:177], v232 offset:256\n\t" "ds_read_b64_tr_b16 a[178:179], v232 offset:1024\n\t" "ds_read_b64_tr_b16 a[180:181], v232 offset:12672\n\t"
                     "ds_read_b64_tr_b16 a[182:183], v232 offset:13440\n\t" "ds_read_b64_tr_b16 v[40:41], v232 offset:24832\n\t" "ds_read_b64_tr_b16 v[42:43], v232 offset:25600\n\t"
                     "ds_read_b64_tr_b16 v[44:45], v232 offset:37248\n\t" "ds_read_b64_tr_b16 v[46:47], v232 offset:38016\n\t" "ds_read_b64_tr_b16 a[112:113], v232 offset:24960\n\t"
                     "ds_read_b64_tr_b16 a[114:115], v232 offset:25728\n\t" "ds_read_b64_tr_b16 a[116:117], v232 offset:37376\n\t" "ds_read_b64_tr_b16 a[118:119], v232 offset:38144\n\t"
                     "ds_read_b64_tr_b16 a[192:193], v232 offset:25088\n\t" "ds_read_b64_tr_b16 a[194:195], v232 offset:25856\n\t" "ds_read_b64_tr_b16 a[196:197], v232 offset:37504\n\t"
                     "ds_read_b64_tr_b16 a[198:199], v232 offset:38272\n\t" "s_waitcnt vmcnt(12) lgkmcnt(0)\n\t" "s_barrier\n\t" "s_add_u32 s42, %21, 0x480000\n\t" "s_add_u32 s42, s46, s42\n\t" "s_addc_u32 s43, s47, 0\n\t"
                     "s_add_u32 m0, %18, 0x0\n\t" "s_nop 0\n\t" "global_load_lds_dwordx4 v224, s[42:43]\n\t" "s_add_u32 m0, %18, 0x400\n\t" "s_nop 0\n\t" "global_load_lds_dwordx4 v225, s[42:43]\n\t"
                     "s_add_u32 m0, %18, 0x800\n\t" "s_nop 0\n\t" "global_load_lds_dwordx4 v226, s[42:43]\n\t" "s_add_u32 s42, %21, 0x498000\n\t" "s_add_u32 s42, s46, s42\n\t" "s_addc_u32 s43, s47, 0\n\t"
                     "s_add_u32 m0, %18, 0xc20\n\t" "s_nop 0\n\t" "global_load_lds_dwordx4 v224, s[42:43]\n\t" "s_add_u32 m0, %18, 0x1020\n\t" "s_nop 0\n\t" "global_load_lds_dwordx4 v225, s[42:43]\n\t"
                     "s_add_u32 m0, %18, 0x1420\n\t" "s_nop 0\n\t" "global_load_lds_dwordx4 v226, s[42:43]\n\t" "s_sub_u32 s42, 0x540000, %21\n\t" "s_add_u32 s42, s46, s42\n\t" "s_addc_u32 s43, s47, 0\n\t"
                     "s_add_u32 m0, %18, 0x6100\n\t" "s_nop 0\n\t" "global_load_lds_dwordx4 v224, s[42:43]\n\t" "s_add_u32 m0, %18, 0x6500\n\t" "s_nop 0\n\t" "global_load_lds_dwordx4 v225, s[42:43]\n\t"
                     "s_add_u32 m0, %18, 0x6900\n\t" "s_nop 0\n\t" "global_load_lds_dwordx4 v226, s[42:43]\n\t" "s_sub_u32 s42, 0x558000, %21\n\t" "s_add_u32 s42, s46, s42\n\t" "s_addc_u32 s43, s47, 0\n\t"
                     "s_add_u32 m0, %18, 0x6d20\n\t" "s_nop 0\n\t" "global_load_lds_dwordx4 v224, s[42:43]\n\t" "s_add_u32 m0, %18, 0x7120\n\t" "s_nop 0\n\t" "global_load_lds_dwordx4 v225, s[42:43]\n\t"
                     "s_add_u32 m0, %18, 0x7520\n\t" "s_nop 0\n\t" "global_load_lds_dwordx4 v226, s[42:43]\n\t" "v_add_u32 v232, 0xc200, v229\n\t" "ds_read_b64_tr_b16 v[24:25], v232\n\t"
                     "ds_read_b64_tr_b16 v[26:27], v232 offset:768\n\t" "ds_read_b64_tr_b16 v[28:29], v232 offset:12416\n\t" "ds_read_b64_tr_b16 v[30:31], v232 offset:13184\n\t"
                     "ds_read_b64_tr_b16 a[104:105], v232 offset:128\n\t" "ds_read_b64_tr_b16 a[106:107], v232 offset:896\n\t" "ds_read_b64_tr_b16 a[108:109], v232 offset:12544\n\t"
                     "ds_read_b64_tr_b16 a[110:111], v232 offset:13312\n\t" "ds_read_b64_tr_b16 a[184:185], v232 offset:256\n\t" "ds_read_b64_tr_b16 a[186:187], v232 offset:1024\n\t"
                     "ds_read_b64_tr_b16 a[188:189], v232 offset:12672\n\t" "ds_read_b64_tr_b16 a[190:191], v232 offset:13440\n\t" "ds_read_b64_tr_b16 v[56:57], v232 offset:24832\n\t"
                     "ds_read_b64_tr_b16 v[58:59], v232 offset:25600\n\t" "ds_read_b64_tr_b16 v[60:61], v232 offset:37248\n\t" "ds_read_b64_tr_b16 v[62:63], v232 offset:38016\n\t"
                     "ds_read_b64_tr_b16 a[120:121], v232 offset:24960\n\t" "ds_read_b64_tr_b16 a[122:123], v232 offset:25728\n\t" "ds_read_b64_tr_b16 a[124:125], v232 offset:37376\n\t"
                     "ds_read_b64_tr_b16 a[126:127], v232 offset:38144\n\t" "ds_read_b64_tr_b16 a[200:201], v232 offset:25088\n\t" "ds_read_b64_tr_b16 a[202:203], v232 offset:25856\n\t"
                     "ds_read_b64_tr_b16 a[204:205], v232 offset:37504\n\t" "ds_read_b64_tr_b16 a[206:207], v232 offset:38272\n\t" "s_waitcnt vmcnt(12) lgkmcnt(0)\n\t" "s_barrier\n\t" "s_add_u32 s42, %21, 0x600000\n\t"
                     "s_add_u32 s42, s46, s42\n\t" "s_addc_u32 s43, s47, 0\n\t" "s_add_u32 m0, %18, 0xc200\n\t" "s_nop 0\n\t" "global_load_lds_dwordx4 v224, s[42:43]\n\t" "s_add_u32 m0, %18, 0xc600\n\t" "s_nop 0\n\t"
                     "global_load_lds_dwordx4 v225, s[42:43]\n\t" "s_add_u32 m0, %18, 0xca00\n\t" "s_nop 0\n\t" "global_load_lds_dwordx4 v226, s[42:43]\n\t" "s_add_u32 s42, %21, 0x618000\n\t" "s_add_u32 s42, s46, s42\n\t"
                     "s_addc_u32 s43, s47, 0\n\t" "s_add_u32 m0, %18, 0xce20\n\t" "s_nop 0\n\t" "global_load_lds_dwordx4 v224, s[42:43]\n\t" "s_add_u32 m0, %18, 0xd220\n\t" "s_nop 0\n\t"
                     "global_load_lds_dwordx4 v225, s[42:43]\n\t" "s_add_u32 m0, %18, 0xd620\n\t" "s_nop 0\n\t" "global_load_lds_dwordx4 v226, s[42:43]\n\t" "s_sub_u32 s42, 0x6c0000, %21\n\t" "s_add_u32 s42, s46, s42\n\t"
                     "s_addc_u32 s43, s47, 0\n\t" "s_add_u32 m0, %18, 0x12300\n\t" "s_nop 0\n\t" "global_load_lds_dwordx4 v224, s[42:43]\n\t" "s_add_u32 m0, %18, 0x12700\n\t" "s_nop 0\n\t"
                     "global_load_lds_dwordx4 v225, s[42:43]\n\t" "s_add_u32 m0, %18, 0x12b00\n\t" "s_nop 0\n\t" "global_load_lds_dwordx4 v226, s[42:43]\n\t" "s_sub_u32 s42, 0x6d8000, %21\n\t" "s_add_u32 s42, s46, s42\n\t"
                     "s_addc_u32 s43, s47, 0\n\t" "s_add_u32 m0, %18, 0x12f20\n\t" "s_nop 0\n\t" "global_load_lds_dwordx4 v224, s[42:43]\n\t" "s_add_u32 m0, %18, 0x13320\n\t" "s_nop 0\n\t"
                     "global_load_lds_dwordx4 v225, s[42:43]\n\t" "s_add_u32 m0, %18, 0x13720\n\t" "s_nop 0\n\t" "global_load_lds_dwordx4 v226, s[42:43]\n\t" "v_add_u32 v232, 0x18400, v229\n\t"
                     "ds_read_b64_tr_b16 v[72:73], v232\n\t" "ds_read_b64_tr_b16 v[74:75], v232 offset:768\n\t" "ds_read_b64_tr_b16 v[76:77], v232 offset:12416\n\t" "ds_read_b64_tr_b16 v[78:79], v232 offset:13184\n\t"
                     "ds_read_b64_tr_b16 a[128:129], v232 offset:128\n\t" "ds_read_b64_tr_b16 a[130:131], v232 offset:896\n\t" "ds_read_b64_tr_b16 a[132:133], v232 offset:12544\n\t"
                     "ds_read_b64_tr_b16 a[134:135], v232 offset:13312\n\t" "ds_read_b64_tr_b16 a[208:209], v232 offset:256\n\t" "ds_read_b64_tr_b16 a[210:211], v232 offset:1024\n\t"
                     "ds_read_b64_tr_b16 a[212:213], v232 offset:12672\n\t" "ds_read_b64_tr_b16 a[214:215], v232 offset:13440\n\t" "ds_read_b64_tr_b16 v[88:89], v232 offset:24832\n\t"
                     "ds_read_b64_tr_b16 v[90:91], v232 offset:25600\n\t" "ds_read_b64_tr_b16 v[92:93], v232 offset:37248\n\t" "ds_read_b64_tr_b16 v[94:95], v232 offset:38016\n\t"
                     "ds_read_b64_tr_b16 a[136:137], v232 offset:24960\n\t" "ds_read_b64_tr_b16 a[138:139], v232 offset:25728\n\t" "ds_read_b64_tr_b16 a[140:141], v232 offset:37376\n\t"
                     "ds_read_b64_tr_b16 a[142:143], v232 offset:38144\n\t" "ds_read_b64_tr_b16 a[216:217], v232 offset:25088\n\t" "ds_read_b64_tr_b16 a[218:219], v232 offset:25856\n\t"
                     "ds_read_b64_tr_b16 a[220:221], v232 offset:37504\n\t" "ds_read_b64_tr_b16 a[222:223], v232 offset:38272\n\t" "s_waitcnt vmcnt(12) lgkmcnt(0)\n\t" "s_barrier\n\t" "s_add_u32 s42, %21, 0x0\n\t"
                     "s_add_u32 s42, s44, s42\n\t" "s_addc_u32 s43, s45, 0\n\t" "s_add_u32 m0, %18, 0x18400\n\t" "s_nop 0\n\t" "global_load_lds_dwordx4 v224, s[42:43]\n\t" "s_add_u32 m0, %18, 0x18800\n\t" "s_nop 0\n\t"
                     "global_load_lds_dwordx4 v225, s[42:43]\n\t" "s_add_u32 m0, %18, 0x18c00\n\t" "s_nop 0\n\t" "global_load_lds_dwordx4 v226, s[42:43]\n\t" "s_add_u32 s42, %21, 0x18000\n\t" "s_add_u32 s42, s44, s42\n\t"
                     "s_addc_u32 s43, s45, 0\n\t" "s_add_u32 m0, %18, 0x19020\n\t" "s_nop 0\n\t" "global_load_lds_dwordx4 v224, s[42:43]\n\t" "s_add_u32 m0, %18, 0x19420\n\t" "s_nop 0\n\t"
                     "global_load_lds_dwordx4 v225, s[42:43]\n\t" "s_add_u32 m0, %18, 0x19820\n\t" "s_nop 0\n\t" "global_load_lds_dwordx4 v226, s[42:43]\n\t" "s_sub_u32 s42, 0xc0000, %21\n\t" "s_add_u32 s42, s44, s42\n\t"
                     "s_addc_u32 s43, s45, 0\n\t" "s_add_u32 m0, %18, 0x1e500\n\t" "s_nop 0\n\t" "global_load_lds_dwordx4 v224, s[42:43]\n\t" "s_add_u32 m0, %18, 0x1e900\n\t" "s_nop 0\n\t"
                     "global_load_lds_dwordx4 v225, s[42:43]\n\t" "s_add_u32 m0, %18, 0x1ed00\n\t" "s_nop 0\n\t" "global_load_lds_dwordx4 v226, s[42:43]\n\t" "s_sub_u32 s42, 0xd8000, %21\n\t" "s_add_u32 s42, s44, s42\n\t"
                     "s_addc_u32 s43, s45, 0\n\t" "s_add_u32 m0, %18, 0x1f120\n\t" "s_nop 0\n\t" "global_load_lds_dwordx4 v224, s[42:43]\n\t" "s_add_u32 m0, %18, 0x1f520\n\t" "s_nop 0\n\t"
                     "global_load_lds_dwordx4 v225, s[42:43]\n\t" "s_add_u32 m0, %18, 0x1f920\n\t" "s_nop 0\n\t" "global_load_lds_dwordx4 v226, s[42:43]\n\t" "v_add_u32 v232, 0x0, v229\n\t"
                     "ds_read_b64_tr_b16 v[104:105], v232\n\t" "ds_read_b64_tr_b16 v[106:107], v232 offset:768\n\t" "ds_read_b64_tr_b16 v[108:109], v232 offset:12416\n\t"
                     "ds_read_b64_tr_b16 v[110:111], v232 offset:13184\n\t" "ds_read_b64_tr_b16 a[144:145], v232 offset:128\n\t" "ds_read_b64_tr_b16 a[146:147], v232 offset:896\n\t"
                     "ds_read_b64_tr_b16 a[148:149], v232 offset:12544\n\t" "ds_read_b64_tr_b16 a[150:151], v232 offset:13312\n\t" "ds_read_b64_tr_b16 a[224:225], v232 offset:256\n\t"
                     "ds_read_b64_tr_b16 a[226:227], v232 offset:1024\n\t" "ds_read_b64_tr_b16 a[228:229], v232 offset:12672\n\t" "ds_read_b64_tr_b16 a[230:231], v232 offset:13440\n\t"
                     "ds_read_b64_tr_b16 v[120:121], v232 offset:24832\n\t" "ds_read_b64_tr_b16 v[122:123], v232 offset:25600\n\t" "ds_read_b64_tr_b16 v[124:125], v232 offset:37248\n\t"
                     "ds_read_b64_tr_b16 v[126:127], v232 offset:38016\n\t" "ds_read_b64_tr_b16 a[152:153], v232 offset:24960\n\t" "ds_read_b64_tr_b16 a[154:155], v232 offset:25728\n\t"
                     "ds_read_b64_tr_b16 a[156:157], v232 offset:37376\n\t" "ds_read_b64_tr_b16 a[158:159], v232 offset:38144\n\t" "ds_read_b64_tr_b16 a[232:233], v232 offset:25088\n\t"
                     "ds_read_b64_tr_b16 a[234:235], v232 offset:25856\n\t" "ds_read_b64_tr_b16 a[236:237], v232 offset:37504\n\t" "ds_read_b64_tr_b16 a[238:239], v232 offset:38272\n\t" "s_waitcnt vmcnt(12) lgkmcnt(0)\n\t"
                     "s_barrier\n\t" "s_add_u32 s42, %21, 0x180000\n\t" "s_add_u32 s42, s44, s42\n\t" "s_addc_u32 s43, s45, 0\n\t" "s_add_u32 m0, %18, 0x0\n\t" "s_nop 0\n\t" "global_load_lds_dwordx4 v224, s[42:43]\n\t"
                     "s_add_u32 m0, %18, 0x400\n\t" "s_nop 0\n\t" "global_load_lds_dwordx4 v225, s[42:43]\n\t" "s_add_u32 m0, %18, 0x800\n\t" "s_nop 0\n\t" "global_load_lds_dwordx4 v226, s[42:43]\n\t"
                     "s_add_u32 s42, %21, 0x198000\n\t" "s_add_u32 s42, s44, s42\n\t" "s_addc_u32 s43, s45, 0\n\t" "s_add_u32 m0, %18, 0xc20\n\t" "s_nop 0\n\t" "global_load_lds_dwordx4 v224, s[42:43]\n\t"
                     "s_add_u32 m0, %18, 0x1020\n\t" "s_nop 0\n\t" "global_load_lds_dwordx4 v225, s[42:43]\n\t" "s_add_u32 m0, %18, 0x1420\n\t" "s_nop 0\n\t" "global_load_lds_dwordx4 v226, s[42:43]\n\t"
                     "s_sub_u32 s42, 0x240000, %21\n\t" "s_add_u32 s42, s44, s42\n\t" "s_addc_u32 s43, s45, 0\n\t" "s_add_u32 m0, %18, 0x6100\n\t" "s_nop 0\n\t" "global_load_lds_dwordx4 v224, s[42:43]\n\t"
                     "s_add_u32 m0, %18, 0x6500\n\t" "s_nop 0\n\t" "global_load_lds_dwordx4 v225, s[42:43]\n\t" "s_add_u32 m0, %18, 0x6900\n\t" "s_nop 0\n\t" "global_load_lds_dwordx4 v226, s[42:43]\n\t"
                     "s_sub_u32 s42, 0x258000, %21\n\t" "s_add_u32 s42, s44, s42\n\t" "s_addc_u32 s43, s45, 0\n\t" "s_add_u32 m0, %18, 0x6d20\n\t" "s_nop 0\n\t" "global_load_lds_dwordx4 v224, s[42:43]\n\t"
                     "s_add_u32 m0, %18, 0x7120\n\t" "s_nop 0\n\t" "global_load_lds_dwordx4 v225, s[42:43]\n\t" "s_add_u32 m0, %18, 0x7520\n\t" "s_nop 0\n\t" "global_load_lds_dwordx4 v226, s[42:43]\n\t"
                     "v_add_u32 v232, 0xc200, v229\n\t" "ds_read_b64_tr_b16 a[56:57], v232\n\t" "ds_read_b64_tr_b16 a[58:59], v232 offset:768\n\t" "ds_read_b64_tr_b16 a[60:61], v232 offset:12416\n\t"
                     "ds_read_b64_tr_b16 a[62:63], v232 offset:13184\n\t" "ds_read_b64_tr_b16 a[160:161], v232 offset:128\n\t" "ds_read_b64_tr_b16 a[162:163], v232 offset:896\n\t"
                     "ds_read_b64_tr_b16 a[164:165], v232 offset:12544\n\t" "ds_read_b64_tr_b16 a[166:167], v232 offset:13312\n\t" "ds_read_b64_tr_b16 a[240:241], v232 offset:256\n\t"
                     "ds_read_b64_tr_b16 a[242:243], v232 offset:1024\n\t" "ds_read_b64_tr_b16 a[244:245], v232 offset:12672\n\t" "ds_read_b64_tr_b16 a[246:247], v232 offset:13440\n\t"
                     "ds_read_b64_tr_b16 a[88:89], v232 offset:24832\n\t" "ds_read_b64_tr_b16 a[90:91], v232 offset:25600\n\t" "ds_read_b64_tr_b16 a[92:93], v232 offset:37248\n\t"
                     "ds_read_b64_tr_b16 a[94:95], v232 offset:38016\n\t" "ds_read_b64_tr_b16 a[168:169], v232 offset:24960\n\t" "ds_read_b64_tr_b16 a[170:171], v232 offset:25728\n\t"
                     "ds_read_b64_tr_b16 a[172:173], v232 offset:37376\n\t" "ds_read_b64_tr_b16 a[174:175], v232 offset:38144\n\t" "ds_read_b64_tr_b16 a[248:249], v232 offset:25088\n\t"
                     "ds_read_b64_tr_b16 a[250:251], v232 offset:25856\n\t" "ds_read_b64_tr_b16 a[252:253], v232 offset:37504\n\t" "ds_read_b64_tr_b16 a[254:255], v232 offset:38272\n\t" "s_waitcnt vmcnt(12) lgkmcnt(0)\n\t"
                     "s_barrier\n\t" "s_add_u32 s42, %21, 0x300000\n\t" "s_add_u32 s42, s44, s42\n\t" "s_addc_u32 s43, s45, 0\n\t" "s_add_u32 m0, %18, 0xc200\n\t" "s_nop 0\n\t" "global_load_lds_dwordx4 v224, s[42:43]\n\t"
                     "s_add_u32 m0, %18, 0xc600\n\t" "s_nop 0\n\t" "global_load_lds_dwordx4 v225, s[42:43]\n\t" "s_add_u32 m0, %18, 0xca00\n\t" "s_nop 0\n\t" "global_load_lds_dwordx4 v226, s[42:43]\n\t"
                     "s_add_u32 s42, %21, 0x318000\n\t" "s_add_u32 s42, s44, s42\n\t" "s_addc_u32 s43, s45, 0\n\t" "s_add_u32 m0, %18, 0xce20\n\t" "s_nop 0\n\t" "global_load_lds_dwordx4 v224, s[42:43]\n\t"
                     "s_add_u32 m0, %18, 0xd220\n\t" "s_nop 0\n\t" "global_load_lds_dwordx4 v225, s[42:43]\n\t" "s_add_u32 m0, %18, 0xd620\n\t" "s_nop 0\n\t" "global_load_lds_dwordx4 v226, s[42:43]\n\t"
                     "s_sub_u32 s42, 0x3c0000, %21\n\t" "s_add_u32 s42, s44, s42\n\t" "s_addc_u32 s43, s45, 0\n\t" "s_add_u32 m0, %18, 0x12300\n\t" "s_nop 0\n\t" "global_load_lds_dwordx4 v224, s[42:43]\n\t"
                     "s_add_u32 m0, %18, 0x12700\n\t" "s_nop 0\n\t" "global_load_lds_dwordx4 v225, s[42:43]\n\t" "s_add_u32 m0, %18, 0x12b00\n\t" "s_nop 0\n\t" "global_load_lds_dwordx4 v226, s[42:43]\n\t"
                     "s_sub_u32 s42, 0x3d8000, %21\n\t" "s_add_u32 s42, s44, s42\n\t" "s_addc_u32 s43, s45, 0\n\t" "s_add_u32 m0, %18, 0x12f20\n\t" "s_nop 0\n\t" "global_load_lds_dwordx4 v224, s[42:43]\n\t"
                     "s_add_u32 m0, %18, 0x13320\n\t" "s_nop 0\n\t" "global_load_lds_dwordx4 v225, s[42:43]\n\t" "s_add_u32 m0, %18, 0x13720\n\t" "s_nop 0\n\t" "global_load_lds_dwordx4 v226, s[42:43]\n\t"
                     "v_add_u32 v232, 0x18400, v227\n\t" "v_add_u32 v233, 0x18400, v228\n\t" "ds_read_b64_tr_b16 v[128:129], v232\n\t" "ds_read_b64_tr_b16 v[130:131], v232 offset:768\n\t"
                     "ds_read_b64_tr_b16 v[132:133], v232 offset:12416\n\t" "ds_read_b64_tr_b16 v[134:135], v232 offset:13184\n\t" "ds_read_b64_tr_b16 v[0:1], v232 offset:32\n\t"
                     "ds_read_b64_tr_b16 v[2:3], v232 offset:800\n\t" "ds_read_b64_tr_b16 v[4:5], v232 offset:12448\n\t" "ds_read_b64_tr_b16 v[6:7], v232 offset:13216\n\t"
                     "ds_read_b64_tr_b16 v[136:137], v233 offset:64\n\t" "ds_read_b64_tr_b16 v[138:139], v233 offset:832\n\t" "ds_read_b64_tr_b16 v[140:141], v233 offset:12480\n\t"
                     "ds_read_b64_tr_b16 v[142:143], v233 offset:13248\n\t" "ds_read_b64_tr_b16 v[160:161], v232 offset:24832\n\t" "ds_read_b64_tr_b16 v[162:163], v232 offset:25600\n\t"
                     "ds_read_b64_tr_b16 v[164:165], v232 offset:37248\n\t" "ds_read_b64_tr_b16 v[166:167], v232 offset:38016\n\t" "ds_read_b64_tr_b16 v[32:33], v232 offset:24864\n\t"
                     "ds_read_b64_tr_b16 v[34:35], v232 offset:25632\n\t" "ds_read_b64_tr_b16 v[36:37], v232 offset:37280\n\t" "ds_read_b64_tr_b16 v[38:39], v232 offset:38048\n\t"
                     "ds_read_b64_tr_b16 v[168:169], v233 offset:24896\n\t" "ds_read_b64_tr_b16 v[170:171], v233 offset:25664\n\t" "ds_read_b64_tr_b16 v[172:173], v233 offset:37312\n\t"
                     "ds_read_b64_tr_b16 v[174:175], v233 offset:38080\n\t" "s_waitcnt vmcnt(12) lgkmcnt(0)\n\t" "s_barrier\n\t" "v_mov_b32 %14, 0\n\t" "global_load_dwordx2 %11, v231, %19 sc1\n\t"
                     "global_load_dwordx2 %12, %14, %19 offset:512 sc1\n\t" "global_load_dwordx2 %13, %14, %19 offset:1024 sc1\n\t" "s_add_u32 s42, %21, 0x480000\n\t" "s_add_u32 s42, s44, s42\n\t"
                     "s_addc_u32 s43, s45, 0\n\t" "s_add_u32 m0, %18, 0x18400\n\t" "s_nop 0\n\t" "global_load_lds_dwordx4 v224, s[42:43]\n\t" "s_add_u32 m0, %18, 0x18800\n\t" "s_nop 0\n\t"
                     "global_load_lds_dwordx4 v225, s[42:43]\n\t" "s_add_u32 m0, %18, 0x18c00\n\t" "s_nop 0\n\t" "global_load_lds_dwordx4 v226, s[42:43]\n\t" "s_add_u32 s42, %21, 0x498000\n\t" "s_add_u32 s42, s44, s42\n\t"
                     "s_addc_u32 s43, s45, 0\n\t" "s_add_u32 m0, %18, 0x19020\n\t" "s_nop 0\n\t" "global_load_lds_dwordx4 v224, s[42:43]\n\t" "s_add_u32 m0, %18, 0x19420\n\t" "s_nop 0\n\t"
                     "global_load_lds_dwordx4 v225, s[42:43]\n\t" "s_add_u32 m0, %18, 0x19820\n\t" "s_nop 0\n\t" "global_load_lds_dwordx4 v226, s[42:43]\n\t" "s_sub_u32 s42, 0x540000, %21\n\t" "s_add_u32 s42, s44, s42\n\t"
                     "s_addc_u32 s43, s45, 0\n\t" "s_add_u32 m0, %18, 0x1e500\n\t" "s_nop 0\n\t" "global_load_lds_dwordx4 v224, s[42:43]\n\t" "s_add_u32 m0, %18, 0x1e900\n\t" "s_nop 0\n\t"
                     "global_load_lds_dwordx4 v225, s[42:43]\n\t" "s_add_u32 m0, %18, 0x1ed00\n\t" "s_nop 0\n\t" "global_load_lds_dwordx4 v226, s[42:43]\n\t" "s_sub_u32 s42, 0x558000, %21\n\t" "s_add_u32 s42, s44, s42\n\t"
                     "s_addc_u32 s43, s45, 0\n\t" "s_add_u32 m0, %18, 0x1f120\n\t" "s_nop 0\n\t" "global_load_lds_dwordx4 v224, s[42:43]\n\t" "s_add_u32 m0, %18, 0x1f520\n\t" "s_nop 0\n\t"
                     "global_load_lds_dwordx4 v225, s[42:43]\n\t" "s_add_u32 m0, %18, 0x1f920\n\t" "s_nop 0\n\t" "global_load_lds_dwordx4 v226, s[42:43]\n\t" "v_add_u32 v232, 0x0, v227\n\t" "v_add_u32 v233, 0x0, v228\n\t"
                     "ds_read_b64_tr_b16 v[144:145], v232\n\t" "ds_read_b64_tr_b16 v[146:147], v232 offset:768\n\t" "ds_read_b64_tr_b16 v[148:149], v232 offset:12416\n\t"
                     "ds_read_b64_tr_b16 v[150:151], v232 offset:13184\n\t" "ds_read_b64_tr_b16 v[16:17], v232 offset:32\n\t" "ds_read_b64_tr_b16 v[18:19], v232 offset:800\n\t"
                     "ds_read_b64_tr_b16 v[20:21], v232 offset:12448\n\t" "ds_read_b64_tr_b16 v[22:23], v232 offset:13216\n\t" "ds_read_b64_tr_b16 v[152:153], v233 offset:64\n\t"
                     "ds_read_b64_tr_b16 v[154:155], v233 offset:832\n\t" "ds_read_b64_tr_b16 v[156:157], v233 offset:12480\n\t" "ds_read_b64_tr_b16 v[158:159], v233 offset:13248\n\t"
                     "ds_read_b64_tr_b16 v[176:177], v232 offset:24832\n\t" "ds_read_b64_tr_b16 v[178:179], v232 offset:25600\n\t" "ds_read_b64_tr_b16 v[180:181], v232 offset:37248\n\t"
                     "ds_read_b64_tr_b16 v[182:183], v232 offset:38016\n\t" "ds_read_b64_tr_b16 v[48:49], v232 offset:24864\n\t" "ds_read_b64_tr_b16 v[50:51], v232 offset:25632\n\t"
                     "ds_read_b64_tr_b16 v[52:53], v232 offset:37280\n\t" "ds_read_b64_tr_b16 v[54:55], v232 offset:38048\n\t" "ds_read_b64_tr_b16 v[184:185], v233 offset:24896\n\t"
                     "ds_read_b64_tr_b16 v[186:187], v233 offset:25664\n\t" "ds_read_b64_tr_b16 v[188:189], v233 offset:37312\n\t" "ds_read_b64_tr_b16 v[190:191], v233 offset:38080\n\t" "s_waitcnt vmcnt(12) lgkmcnt(0)\n\t"
                     "s_barrier\n\t" "s_add_u32 s42, %21, 0x600000\n\t" "s_add_u32 s42, s44, s42\n\t" "s_addc_u32 s43, s45, 0\n\t" "s_add_u32 m0, %18, 0x0\n\t" "s_nop 0\n\t" "global_load_lds_dwordx4 v224, s[42:43]\n\t"
                     "s_add_u32 m0, %18, 0x400\n\t" "s_nop 0\n\t" "global_load_lds_dwordx4 v225, s[42:43]\n\t" "s_add_u32 m0, %18, 0x800\n\t" "s_nop 0\n\t" "global_load_lds_dwordx4 v226, s[42:43]\n\t"
                     "s_add_u32 s42, %21, 0x618000\n\t" "s_add_u32 s42, s44, s42\n\t" "s_addc_u32 s43, s45, 0\n\t" "s_add_u32 m0, %18, 0xc20\n\t" "s_nop 0\n\t" "global_load_lds_dwordx4 v224, s[42:43]\n\t"
                     "s_add_u32 m0, %18, 0x1020\n\t" "s_nop 0\n\t" "global_load_lds_dwordx4 v225, s[42:43]\n\t" "s_add_u32 m0, %18, 0x1420\n\t" "s_nop 0\n\t" "global_load_lds_dwordx4 v226, s[42:43]\n\t"
                     "s_sub_u32 s42, 0x6c0000, %21\n\t" "s_add_u32 s42, s44, s42\n\t" "s_addc_u32 s43, s45, 0\n\t" "s_add_u32 m0, %18, 0x6100\n\t" "s_nop 0\n\t" "global_load_lds_dwordx4 v224, s[42:43]\n\t"
                     "s_add_u32 m0, %18, 0x6500\n\t" "s_nop 0\n\t" "global_load_lds_dwordx4 v225, s[42:43]\n\t" "s_add_u32 m0, %18, 0x6900\n\t" "s_nop 0\n\t" "global_load_lds_dwordx4 v226, s[42:43]\n\t"
                     "s_sub_u32 s42, 0x6d8000, %21\n\t" "s_add_u32 s42, s44, s42\n\t" "s_addc_u32 s43, s45, 0\n\t" "s_add_u32 m0, %18, 0x6d20\n\t" "s_nop 0\n\t" "global_load_lds_dwordx4 v224, s[42:43]\n\t"
                     "s_add_u32 m0, %18, 0x7120\n\t" "s_nop 0\n\t" "global_load_lds_dwordx4 v225, s[42:43]\n\t" "s_add_u32 m0, %18, 0x7520\n\t" "s_nop 0\n\t" "global_load_lds_dwordx4 v226, s[42:43]\n\t"
                     "global_load_dword %14, %14, %20 sc1\n\t" "v_add_u32 v232, 0xc200, v227\n\t" "v_add_u32 v233, 0xc200, v228\n\t" "ds_read_b64_tr_b16 v[192:193], v232\n\t"
                     "ds_read_b64_tr_b16 v[194:195], v232 offset:768\n\t" "ds_read_b64_tr_b16 v[196:197], v232 offset:12416\n\t" "ds_read_b64_tr_b16 v[198:199], v232 offset:13184\n\t"
                     "ds_read_b64_tr_b16 v[64:65], v232 offset:32\n\t" "ds_read_b64_tr_b16 v[66:67], v232 offset:800\n\t" "ds_read_b64_tr_b16 v[68:69], v232 offset:12448\n\t"
                     "ds_read_b64_tr_b16 v[70:71], v232 offset:13216\n\t" "ds_read_b64_tr_b16 v[200:201], v233 offset:64\n\t" "ds_read_b64_tr_b16 v[202:203], v233 offset:832\n\t"
                     "ds_read_b64_tr_b16 v[204:205], v233 offset:12480\n\t" "ds_read_b64_tr_b16 v[206:207], v233 offset:13248\n\t" "ds_read_b64_tr_b16 v[208:209], v232 offset:24832\n\t"
                     "ds_read_b64_tr_b16 v[210:211], v232 offset:25600\n\t" "ds_read_b64_tr_b16 v[212:213], v232 offset:37248\n\t" "ds_read_b64_tr_b16 v[214:215], v232 offset:38016\n\t"
                     "ds_read_b64_tr_b16 v[80:81], v232 offset:24864\n\t" "ds_read_b64_tr_b16 v[82:83], v232 offset:25632\n\t" "ds_read_b64_tr_b16 v[84:85], v232 offset:37280\n\t"
                     "ds_read_b64_tr_b16 v[86:87], v232 offset:38048\n\t" "ds_read_b64_tr_b16 v[216:217], v233 offset:24896\n\t" "ds_read_b64_tr_b16 v[218:219], v233 offset:25664\n\t"
                     "ds_read_b64_tr_b16 v[220:221], v233 offset:37312\n\t" "ds_read_b64_tr_b16 v[222:223], v233 offset:38080\n\t" "s_waitcnt vmcnt(13) lgkmcnt(0)\n\t" "s_barrier\n\t" "v_add_u32 v232, 0x18400, v227\n\t"
                     "v_add_u32 v233, 0x18400, v228\n\t" "ds_read_b64_tr_b16 a[0:1], v232\n\t" "ds_read_b64_tr_b16 a[2:3], v232 offset:768\n\t" "ds_read_b64_tr_b16 a[4:5], v232 offset:12416\n\t"
                     "ds_read_b64_tr_b16 a[6:7], v232 offset:13184\n\t" "ds_read_b64_tr_b16 v[96:97], v232 offset:32\n\t" "ds_read_b64_tr_b16 v[98:99], v232 offset:800\n\t"
                     "ds_read_b64_tr_b16 v[100:101], v232 offset:12448\n\t" "ds_read_b64_tr_b16 v[102:103], v232 offset:13216\n\t" "ds_read_b64_tr_b16 a[8:9], v233 offset:64\n\t"
                     "ds_read_b64_tr_b16 a[10:11], v233 offset:832\n\t" "ds_read_b64_tr_b16 a[12:13], v233 offset:12480\n\t" "ds_read_b64_tr_b16 a[14:15], v233 offset:13248\n\t"
                     "ds_read_b64_tr_b16 a[16:17], v232 offset:24832\n\t" "ds_read_b64_tr_b16 a[18:19], v232 offset:25600\n\t" "ds_read_b64_tr_b16 a[20:21], v232 offset:37248\n\t"
                     "ds_read_b64_tr_b16 a[22:23], v232 offset:38016\n\t" "ds_read_b64_tr_b16 v[112:113], v232 offset:24864\n\t" "ds_read_b64_tr_b16 v[114:115], v232 offset:25632\n\t"
                     "ds_read_b64_tr_b16 v[116:117], v232 offset:37280\n\t" "ds_read_b64_tr_b16 v[118:119], v232 offset:38048\n\t" "ds_read_b64_tr_b16 a[24:25], v233 offset:24896\n\t"
                     "ds_read_b64_tr_b16 a[26:27], v233 offset:25664\n\t" "ds_read_b64_tr_b16 a[28:29], v233 offset:37312\n\t" "ds_read_b64_tr_b16 a[30:31], v233 offset:38080\n\t" "s_waitcnt vmcnt(0) lgkmcnt(0)\n\t"
                     "s_barrier\n\t" "v_add_u32 v232, 0x0, v227\n\t" "v_add_u32 v233, 0x0, v228\n\t" "ds_read_b64_tr_b16 a[32:33], v232\n\t" "ds_read_b64_tr_b16 a[34:35], v232 offset:768\n\t"
                     "ds_read_b64_tr_b16 a[36:37], v232 offset:12416\n\t" "ds_read_b64_tr_b16 a[38:39], v232 offset:13184\n\t" "ds_read_b64_tr_b16 a[40:41], v232 offset:32\n\t"
                     "ds_read_b64_tr_b16 a[42:43], v232 offset:800\n\t" "ds_read_b64_tr_b16 a[44:45], v232 offset:12448\n\t" "ds_read_b64_tr_b16 a[46:47], v232 offset:13216\n\t"
                     "ds_read_b64_tr_b16 a[48:49], v233 offset:64\n\t" "ds_read_b64_tr_b16 a[50:51], v233 offset:832\n\t" "ds_read_b64_tr_b16 a[52:53], v233 offset:12480\n\t"
                     "ds_read_b64_tr_b16 a[54:55], v233 offset:13248\n\t" "ds_read_b64_tr_b16 a[64:65], v232 offset:24832\n\t" "ds_read_b64_tr_b16 a[66:67], v232 offset:25600\n\t"
                     "ds_read_b64_tr_b16 a[68:69], v232 offset:37248\n\t" "ds_read_b64_tr_b16 a[70:71], v232 offset:38016\n\t" "ds_read_b64_tr_b16 a[72:73], v232 offset:24864\n\t"
                     "ds_read_b64_tr_b16 a[74:75], v232 offset:25632\n\t" "ds_read_b64_tr_b16 a[76:77], v232 offset:37280\n\t" "ds_read_b64_tr_b16 a[78:79], v232 offset:38048\n\t"
                     "ds_read_b64_tr_b16 a[80:81], v233 offset:24896\n\t" "ds_read_b64_tr_b16 a[82:83], v233 offset:25664\n\t" "ds_read_b64_tr_b16 a[84:85], v233 offset:37312\n\t"
                     "ds_read_b64_tr_b16 a[86:87], v233 offset:38080\n\t" "s_waitcnt lgkmcnt(0)\n\t" "s_barrier\n\t" "ds_write_b128 v230, v[0:3]\n\t" "ds_write_b128 v230, v[4:7] offset:1024\n\t"
                     "ds_write_b128 v230, v[8:11] offset:2048\n\t" "ds_write_b128 v230, v[12:15] offset:3072\n\t" "ds_write_b128 v230, v[16:19] offset:4096\n\t" "ds_write_b128 v230, v[20:23] offset:5120\n\t"
                     "ds_write_b128 v230, v[24:27] offset:6144\n\t" "ds_write_b128 v230, v[28:31] offset:7168\n\t" "ds_write_b128 v230, v[32:35] offset:8192\n\t" "ds_write_b128 v230, v[36:39] offset:9216\n\t"
                     "ds_write_b128 v230, v[40:43] offset:10240\n\t" "ds_write_b128 v230, v[44:47] offset:11264\n\t" "ds_write_b128 v230, v[48:51] offset:12288\n\t" "ds_write_b128 v230, v[52:55] offset:13312\n\t"
                     "ds_write_b128 v230, v[56:59] offset:14336\n\t" "ds_write_b128 v230, v[60:63] offset:15360\n\t" "ds_write_b128 v230, v[64:67] offset:16384\n\t" "ds_write_b128 v230, v[68:71] offset:17408\n\t"
                     "ds_write_b128 v230, v[72:75] offset:18432\n\t" "ds_write_b128 v230, v[76:79] offset:19456\n\t" "ds_write_b128 v230, v[80:83] offset:20480\n\t" "ds_write_b128 v230, v[84:87] offset:21504\n\t"
                     "ds_write_b128 v230, v[88:91] offset:22528\n\t" "ds_write_b128 v230, v[92:95] offset:23552\n\t" "ds_write_b128 v230, v[96:99] offset:24576\n\t" "ds_write_b128 v230, v[100:103] offset:25600\n\t"
                     "ds_write_b128 v230, v[104:107] offset:26624\n\t" "ds_write_b128 v230, v[108:111] offset:27648\n\t" "ds_write_b128 v230, v[112:115] offset:28672\n\t" "ds_write_b128 v230, v[116:119] offset:29696\n\t"
                     "ds_write_b128 v230, v[120:123] offset:30720\n\t" "ds_write_b128 v230, v[124:127] offset:31744\n\t"
                     : "=&{a[0:31]}"(wa[0]), "=&{a[32:63]}"(wa[1]), "=&{a[64:95]}"(wa[2]), "=&{a[96:127]}"(wa[3]), "=&{a[128:159]}"(wa[4]), "=&{a[160:191]}"(wa[5]), "=&{a[192:223]}"(wa[6]), "=&{a[224:255]}"(wa[7]), "=&{v[128:159]}"(wv[0]), "=&{v[160:191]}"(wv[1]), "=&{v[192:223]}"(wv[2]), "=&v"(y.r), "=&v"(y.g), "=&v"(y.e), "=&v"(kfl)
                     : "{v[224:231]}"(pa), "{s[44:45]}"(p.wq + h * DH * 2 + (size_t)(16 * w) * (HD * 2)), "{s[46:47]}"(p.wk + h * DH * 2 + (size_t)(16 * w) * (HD * 2)),
                       "s"(lds + 2 * w * GS), "s"(p.ws + OFF_LAT), "s"(fl + (F_K + h) * 128), "s"((h & 4) * 16 * HD * 2)
                     : "v0", "v1", "v2", "v3", "v4", "v5", "v6", "v7", "v8", "v9", "v10", "v11", "v12", "v13", "v14", "v15", "v16", "v17", "v18", "v19", "v20", "v21", "v22", "v23", "v24", "v25", "v26", "v27", "v28", "v29", "v30", "v31", "v32", "v33", "v34", "v35", "v36", "v37", "v38", "v39", "v40", "v41", "v42", "v43", "v44", "v45", "v46", "v47", "v48", "v49", "v50", "v51", "v52", "v53", "v54", "v55", "v56", "v57", "v58", "v59", "v60", "v61", "v62", "v63", "v64", "v65", "v66", "v67", "v68", "v69", "v70", "v71", "v72", "v73", "v74", "v75", "v76", "v77", "v78", "v79", "v80", "v81", "v82", "v83", "v84", "v85", "v86", "v87", "v88", "v89", "v90", "v91", "v92", "v93", "v94", "v95", "v96", "v97", "v98", "v99", "v100", "v101", "v102", "v103", "v104", "v105", "v106", "v107", "v108", "v109", "v110", "v111", "v112", "v113", "v114", "v115", "v116", "v117", "v118", "v119", "v120", "v121", "v122", "v123", "v124", "v125", "v126", "v127", "v232", "v233", "s42", "s43", "scc", "memory");
        const u64 t = sync_tag(y.e);
        const bool rdy = __builtin_amdgcn_ballot_w64((y.r[0] & ~1u) != (u32)t || y.r[1] != (u32)(t >> 32) || y.g[0] != (u32)t) == 0 && uni((int)kfl) != -1;
        if (rdy) kxdma();
        sd = sync_end(y, p.ws, phi, w == 0);
        if (!rdy) { flag_wait(fl, F_K + h, 1); kxdma(); }
        if (w == 3) rdy_add(kfl, fl);
        kxp += NH * 12288;
    }
    const u32 sidx = (lane & 2) ? 0xEEEEu : 0x4444u;
    const u32 moff = lane * 16;
    u32 kxa = kxl - 16 + (n & 1) * 6160 + (16 * g + (n & 8) + 2 * (n & 2) + ((n >> 1) & 2)) * 16;
    const u64 mb0 = 0xffff0000ffff0000ull;
    const int tp = (3 * w + (w & 1)) >> 1, ts = 1 + 3 * (w >> 1);
    u32 qw0 = lds + SL_IMG + tp * 512 + lane * 8, qw1 = lds + SL_IMG + ts * 512 + lane * 8 + 4 * (w & 1);
    const u32 kw0 = lds + SL_IMG + 3072 + w * 288 + n * 16 + 4 * g;
    const u32 qr0 = lds + SL_IMG + (g >> 1) * 512 + w * 128 + 64 * (g & 1) + 8 * qq;
    const u32 kr0 = lds + SL_IMG + 3072 + (qq >> 1) * 288 + g * 64 + (qq & 1) * 16 + 8 * (pp & 1);
    const u32 xso = 32768 + (h >> 4) * 16384 + (h & 15) * 512 + (w >> 1) * 256 + (n >> 3) * 64 + (n & 4) * 32 + (w & 1) * 32 + (g & 1) * 16 + (n & 3) * 4 + (g >> 1) * 8192;
    const u32 pso = (n < 8 ? 20480 + (h >> 1) * 256 + (n & 2) * 16 + (h & 1) * 8 + (n & 4) : 18432 + (h >> 2) * 256 + (h & 1) * 8 + (h & 2) * 2) + w * 64 + (g & 1) * 16 + (g >> 1) * 8192;
    const u64 psm = 0x01aa01aa01aa01aaull;
    const u32 kmo = (u32)(((n >> 3) * 4 + g) * 128 + (n & 7) * 16);
    u32x16 ad = u32x16{moff, sidx, lfa, kxa, qw0, qw1, kw0, kr0 + 1152 * min(w, 2) + (pp >> 1) * 576, qr0, kr0, xso, pso, 0x3dd53b94u, kmo, kmo, kmo};
    asm volatile("" : "+{v[224:239]}"(ad));
    const char* memp = p.ws + mslot(sd, 0);
    char* kmp = p.ws + OFF_KM + h * 3072 + min(w, 2) * 1024;
    const float kscale = 0.07216878364870322f;
    int budget = 30000;
    Clock clk; clk.init();
    if (w == 3) { asm volatile("s_waitcnt vmcnt(0)" : "+v"(kfl) : : "memory"); rdy_end(kfl, fl, cfg.s1 >> 8 & 255); }
    u32x32 wk[3];
    asm volatile("" : "={v[64:95]}"(wk[0]), "={v[96:127]}"(wk[1]), "={v[32:63]}"(wk[2]));
    const int kfo = (F_K + 32 + h) * 128;
    for (int s = 0; s < NSEG; ++s) {
        int tries = 0;
        u64 bad, tp8, en = ~0ull;
        u32 tt;
        int kf;
        while (true) {
            const u32 tgt = s ? clk.sched(phi, 0, cfg.G, tries ? 2 : 0) : mt();
            asm volatile("s_or_b32 s42, %12, %13\n\t" "s_cbranch_scc1 .Lsp_s\n\t" "s_mov_b64 vcc, 0\n\t" ".Lrt_s:\n\t" "s_nop 7\n\t" "ds_read_b128 v[64:67], v226\n\t" "v_mov_b64 v[40:41], 0\n\t"
                         "ds_read_b128 v[68:71], v226 offset:1024\n\t" "v_mov_b64 v[42:43], 0\n\t" "ds_read_b128 v[72:75], v226 offset:2048\n\t" "v_mov_b64 v[44:45], 0\n\t" "ds_read_b128 v[76:79], v226 offset:3072\n\t"
                         "v_mov_b64 v[46:47], 0\n\t" "ds_read_b128 v[80:83], v226 offset:4096\n\t" "v_mov_b64 v[48:49], 0\n\t" "ds_read_b128 v[84:87], v226 offset:5120\n\t" "v_mov_b64 v[50:51], 0\n\t"
                         "ds_read_b128 v[88:91], v226 offset:6144\n\t" "v_mov_b64 v[52:53], 0\n\t" "ds_read_b128 v[92:95], v226 offset:7168\n\t" "v_mov_b64 v[54:55], 0\n\t" "ds_read_b128 v[96:99], v226 offset:8192\n\t"
                         "v_mov_b64 v[56:57], 0\n\t" "ds_read_b128 v[100:103], v226 offset:9216\n\t" "v_mov_b64 v[58:59], 0\n\t" "ds_read_b128 v[104:107], v226 offset:10240\n\t" "v_mov_b64 v[60:61], 0\n\t"
                         "ds_read_b128 v[108:111], v226 offset:11264\n\t" "v_mov_b64 v[62:63], 0\n\t" "ds_read_b128 v[112:115], v226 offset:12288\n\t" "ds_read_b128 v[116:119], v226 offset:13312\n\t"
                         "ds_read_b128 v[120:123], v226 offset:14336\n\t" "ds_read_b128 v[124:127], v226 offset:15360\n\t" "s_cbranch_vccnz .Lsk_s\n\t" ".p2align 5\n\t" "s_nop 0\n\t" "s_nop 0\n\t" "s_nop 0\n\t" "s_nop 0\n\t"
                         "s_nop 0\n\t" ".Lsp_s:\n\t" "s_memtime vcc\n\t" "s_waitcnt lgkmcnt(0)\n\t" "s_sub_i32 s42, %4, vcc_lo\n\t" "s_cmp_gt_i32 s42, 80\n\t" "s_cbranch_scc1 .Lsp_s\n\t" "s_max_i32 s42, s42, 0\n\t"
                         "s_sub_i32 s42, 92, s42\n\t" "s_and_b32 s42, s42, -4\n\t" "s_getpc_b64 vcc\n\t" "s_add_u32 vcc_lo, vcc_lo, s42\n\t" "s_addc_u32 vcc_hi, vcc_hi, 0\n\t" "s_setpc_b64 vcc\n\t" "s_nop 0\n\t" "s_nop 0\n\t"
                         "s_nop 0\n\t" "s_nop 0\n\t" "s_nop 0\n\t" "s_nop 0\n\t" "s_nop 0\n\t" "s_nop 0\n\t" "s_nop 0\n\t" "s_nop 0\n\t" "s_nop 0\n\t" "s_nop 0\n\t" "s_nop 0\n\t" "s_nop 0\n\t" "s_nop 0\n\t" "s_nop 0\n\t"
                         "s_nop 0\n\t" "s_nop 0\n\t" "s_nop 0\n\t" "s_nop 0\n\t" "global_load_dwordx4 v[0:3], v224, %5 offset:-4096 sc1\n\t" "v_add_u32 v238, 0x4000, v224\n\t" "s_nop 9\n\t"
                         "global_load_dwordx4 v[4:7], v224, %5 offset:-2048 sc1\n\t" "s_nop 15\n\t" "global_load_dwordx4 v[8:11], v224, %6 offset:-3072 sc1\n\t" "s_nop 10\n\t"
                         "global_load_dwordx4 v[12:15], v224, %6 offset:-1024 sc1\n\t" "s_nop 10\n\t" "global_load_dwordx4 v[16:19], v224, %5 sc1\n\t" "s_nop 10\n\t" "global_load_dwordx4 v[20:23], v224, %6 offset:1024 sc1\n\t"
                         "s_nop 10\n\t" "global_load_dwordx4 v[24:27], v224, %5 offset:2048 sc1\n\t" "s_nop 10\n\t" "global_load_dwordx4 v[28:31], v224, %6 offset:3072 sc1\n\t" "s_nop 10\n\t"
                         "global_load_dwordx4 v[32:35], v238, %5 offset:-4096 sc1\n\t" "s_nop 10\n\t" "global_load_dwordx4 v[36:39], v238, %6 offset:-3072 sc1\n\t" "s_cmp_lg_u32 %12, 0\n\t" "s_cbranch_scc1 .Lnb_s\n\t"
                         "s_waitcnt vmcnt(10)\n\t" "s_barrier\n\t" ".Lnb_s:\n\t" "s_waitcnt vmcnt(9)\n\t" "v_smfmac_f32_16x16x64_bf16 v[40:43], v[0:3], v[128:135], v225\n\t"
                         "v_smfmac_f32_16x16x64_bf16 v[44:47], v[0:3], v[64:71], v225\n\t" "ds_read_b128 v[64:67], v226 offset:16384\n\t" "v_smfmac_f32_16x16x64_bf16 v[48:51], v[0:3], v[136:143], v225\n\t"
                         "ds_read_b128 v[68:71], v226 offset:17408\n\t" "v_smfmac_f32_16x16x64_bf16 v[52:55], v[0:3], v[72:79], v225\n\t" "ds_read_b128 v[72:75], v226 offset:18432\n\t" "s_waitcnt vmcnt(8)\n\t"
                         "v_smfmac_f32_16x16x64_bf16 v[40:43], v[4:7], v[144:151], v225\n\t" "ds_read_b128 v[76:79], v226 offset:19456\n\t" "v_smfmac_f32_16x16x64_bf16 v[44:47], v[4:7], v[80:87], v225\n\t"
                         "ds_read_b128 v[80:83], v226 offset:20480\n\t" "v_smfmac_f32_16x16x64_bf16 v[48:51], v[4:7], v[152:159], v225\n\t" "ds_read_b128 v[84:87], v226 offset:21504\n\t"
                         "v_smfmac_f32_16x16x64_bf16 v[52:55], v[4:7], v[88:95], v225\n\t" "ds_read_b128 v[88:91], v226 offset:22528\n\t" "s_waitcnt vmcnt(7)\n\t"
                         "v_smfmac_f32_16x16x64_bf16 v[40:43], v[8:11], v[160:167], v225\n\t" "ds_read_b128 v[92:95], v226 offset:23552\n\t" "v_smfmac_f32_16x16x64_bf16 v[44:47], v[8:11], v[96:103], v225\n\t"
                         "ds_read_b128 v[96:99], v226 offset:24576\n\t" "v_smfmac_f32_16x16x64_bf16 v[48:51], v[8:11], v[168:175], v225\n\t" "ds_read_b128 v[100:103], v226 offset:25600\n\t"
                         "v_smfmac_f32_16x16x64_bf16 v[52:55], v[8:11], v[104:111], v225\n\t" "ds_read_b128 v[104:107], v226 offset:26624\n\t" "s_waitcnt vmcnt(6)\n\t"
                         "v_smfmac_f32_16x16x64_bf16 v[40:43], v[12:15], v[176:183], v225\n\t" "ds_read_b128 v[108:111], v226 offset:27648\n\t" "v_smfmac_f32_16x16x64_bf16 v[44:47], v[12:15], v[112:119], v225\n\t"
                         "ds_read_b128 v[112:115], v226 offset:28672\n\t" "v_smfmac_f32_16x16x64_bf16 v[48:51], v[12:15], v[184:191], v225\n\t" "ds_read_b128 v[116:119], v226 offset:29696\n\t"
                         "v_smfmac_f32_16x16x64_bf16 v[52:55], v[12:15], v[120:127], v225\n\t" "ds_read_b128 v[120:123], v226 offset:30720\n\t" "s_waitcnt vmcnt(5)\n\t"
                         "v_smfmac_f32_16x16x64_bf16 v[40:43], v[16:19], v[192:199], v225\n\t" "ds_read_b128 v[124:127], v226 offset:31744\n\t" "s_waitcnt lgkmcnt(14)\n\t"
                         "v_smfmac_f32_16x16x64_bf16 v[44:47], v[16:19], v[64:71], v225\n\t" "ds_read_b128 v[64:67], v227\n\t" "v_smfmac_f32_16x16x64_bf16 v[48:51], v[16:19], v[200:207], v225\n\t"
                         "ds_read_b128 v[68:71], v227 offset:1024\n\t" "s_waitcnt lgkmcnt(14)\n\t" "v_smfmac_f32_16x16x64_bf16 v[52:55], v[16:19], v[72:79], v225\n\t" "ds_read_b128 v[72:75], v227 offset:16\n\t"
                         "s_waitcnt vmcnt(4)\n\t" "v_smfmac_f32_16x16x64_bf16 v[40:43], v[20:23], v[208:215], v225\n\t" "ds_read_b128 v[76:79], v227 offset:1040\n\t" "s_waitcnt lgkmcnt(14)\n\t"
                         "v_smfmac_f32_16x16x64_bf16 v[44:47], v[20:23], v[80:87], v225\n\t" "ds_read_b128 v[80:83], v227 offset:2048\n\t" "v_smfmac_f32_16x16x64_bf16 v[48:51], v[20:23], v[216:223], v225\n\t"
                         "ds_read_b128 v[84:87], v227 offset:3072\n\t" "s_waitcnt lgkmcnt(14)\n\t" "v_smfmac_f32_16x16x64_bf16 v[52:55], v[20:23], v[88:95], v225\n\t" "ds_read_b128 v[88:91], v227 offset:2064\n\t"
                         "s_waitcnt vmcnt(3)\n\t" "v_smfmac_f32_16x16x64_bf16 v[40:43], v[24:27], a[0:7], v225\n\t" "ds_read_b128 v[92:95], v227 offset:3088\n\t" "s_waitcnt lgkmcnt(14)\n\t"
                         "v_smfmac_f32_16x16x64_bf16 v[44:47], v[24:27], v[96:103], v225\n\t" "ds_read_b128 v[96:99], v227 offset:4096\n\t" "v_smfmac_f32_16x16x64_bf16 v[48:51], v[24:27], a[8:15], v225\n\t"
                         "ds_read_b128 v[100:103], v227 offset:5120\n\t" "s_waitcnt lgkmcnt(14)\n\t" "v_smfmac_f32_16x16x64_bf16 v[52:55], v[24:27], v[104:111], v225\n\t" "ds_read_b128 v[104:107], v227 offset:4112\n\t"
                         "s_waitcnt vmcnt(2)\n\t" "v_smfmac_f32_16x16x64_bf16 v[40:43], v[28:31], a[16:23], v225\n\t" "ds_read_b128 v[108:111], v227 offset:5136\n\t" "s_waitcnt lgkmcnt(14)\n\t"
                         "v_smfmac_f32_16x16x64_bf16 v[44:47], v[28:31], v[112:119], v225\n\t" "v_smfmac_f32_16x16x64_bf16 v[48:51], v[28:31], a[24:31], v225\n\t" "s_waitcnt lgkmcnt(12)\n\t"
                         "v_smfmac_f32_16x16x64_bf16 v[52:55], v[28:31], v[120:127], v225\n\t" "s_waitcnt vmcnt(1)\n\t" "v_smfmac_f32_16x16x64_bf16 v[40:43], v[32:35], a[32:39], v225\n\t" "v_mov_b64 v[112:113], 0\n\t"
                         "v_smfmac_f32_16x16x64_bf16 v[44:47], v[32:35], a[40:47], v225\n\t" "v_mov_b64 v[114:115], 0\n\t" "v_smfmac_f32_16x16x64_bf16 v[48:51], v[32:35], a[48:55], v225\n\t" "v_mov_b64 v[116:117], 0\n\t"
                         "v_smfmac_f32_16x16x64_bf16 v[52:55], v[32:35], a[56:63], v225\n\t" "v_mov_b64 v[118:119], 0\n\t" "s_waitcnt vmcnt(0)\n\t" "v_smfmac_f32_16x16x64_bf16 v[40:43], v[36:39], a[64:71], v225\n\t"
                         "v_mov_b64 v[120:121], 0\n\t" "v_smfmac_f32_16x16x64_bf16 v[44:47], v[36:39], a[72:79], v225\n\t" "v_mov_b64 v[122:123], 0\n\t" "v_smfmac_f32_16x16x64_bf16 v[48:51], v[36:39], a[80:87], v225\n\t"
                         "v_mov_b64 v[124:125], 0\n\t" "v_smfmac_f32_16x16x64_bf16 v[52:55], v[36:39], a[88:95], v225\n\t" "v_add_f32 v40, v40, v42\n\t" "v_add_f32 v41, v41, v43\n\t"
                         "v_smfmac_f32_16x16x64_bf16 v[56:59], v[0:3], a[96:103], v225\n\t" "v_cmp_u_f32 vcc, v40, v41\n\t" "v_smfmac_f32_16x16x64_bf16 v[56:59], v[4:7], a[104:111], v225\n\t"
                         "v_cvt_pk_bf16_f32 v42, v40, v41\n\t" "v_add_f32 v44, v44, v46\n\t" "v_smfmac_f32_16x16x64_bf16 v[56:59], v[8:11], a[112:119], v225\n\t" "s_and_b64 vcc, vcc, %11\n\t" "s_cbranch_scc1 .Lrt_s\n\t"
                         "v_smfmac_f32_16x16x64_bf16 v[56:59], v[12:15], a[120:127], v225\n\t" "v_add_f32 v45, v45, v47\n\t" "v_cvt_pk_bf16_f32 v43, v44, v45\n\t"
                         "v_smfmac_f32_16x16x64_bf16 v[56:59], v[16:19], a[128:135], v225\n\t" "ds_write_b64 v228, v[42:43]\n\t" "v_add_f32 v48, v48, v50\n\t"
                         "v_smfmac_f32_16x16x64_bf16 v[56:59], v[20:23], a[136:143], v225\n\t" "v_add_f32 v49, v49, v51\n\t" "v_cvt_pk_bf16_f32 v46, v48, v49\n\t"
                         "v_smfmac_f32_16x16x64_bf16 v[56:59], v[24:27], a[144:151], v225\n\t" "ds_write_b32 v229, v46\n\t" "v_add_f32 v52, v52, v54\n\t" "v_smfmac_f32_16x16x64_bf16 v[56:59], v[28:31], a[152:159], v225\n\t"
                         "v_add_f32 v53, v53, v55\n\t" "v_cvt_pk_bf16_f32 v54, v52, v53\n\t" "v_smfmac_f32_16x16x64_bf16 v[56:59], v[32:35], a[160:167], v225\n\t" "ds_write_b32 v230, v54\n\t"
                         "v_smfmac_f32_16x16x64_bf16 v[56:59], v[36:39], a[168:175], v225\n\t" "v_smfmac_f32_16x16x64_bf16 v[60:63], v[0:3], a[176:183], v225\n\t"
                         "v_smfmac_f32_16x16x64_bf16 v[60:63], v[4:7], a[184:191], v225\n\t" "v_add_f32 v56, v56, v58\n\t" "v_smfmac_f32_16x16x64_bf16 v[60:63], v[8:11], a[192:199], v225\n\t" "s_waitcnt lgkmcnt(0)\n\t"
                         "s_barrier\n\t" "v_smfmac_f32_16x16x64_bf16 v[60:63], v[12:15], a[200:207], v225\n\t" "ds_read_b64_tr_b16 v[40:41], v232\n\t" "v_add_f32 v57, v57, v59\n\t"
                         "v_smfmac_f32_16x16x64_bf16 v[60:63], v[16:19], a[208:215], v225\n\t" "ds_read_b64_tr_b16 v[42:43], v232 offset:32\n\t" "v_cvt_pk_bf16_f32 v58, v56, v57\n\t"
                         "v_smfmac_f32_16x16x64_bf16 v[60:63], v[20:23], a[216:223], v225\n\t" "ds_write_b32 v230, v58 offset:1152\n\t" "v_smfmac_f32_16x16x64_bf16 v[60:63], v[24:27], a[224:231], v225\n\t"
                         "ds_read_b64_tr_b16 v[44:45], v232 offset:1024\n\t" "ds_read_b64_tr_b16 v[46:47], v232 offset:1056\n\t" "v_smfmac_f32_16x16x64_bf16 v[60:63], v[28:31], a[232:239], v225\n\t"
                         "ds_read_b64_tr_b16 v[48:49], v232 offset:2048\n\t" "ds_read_b64_tr_b16 v[50:51], v232 offset:2080\n\t" "v_smfmac_f32_16x16x64_bf16 v[60:63], v[32:35], a[240:247], v225\n\t"
                         "ds_read_b64_tr_b16 v[0:1], v233\n\t" "ds_read_b64_tr_b16 v[2:3], v233 offset:32\n\t" "v_smfmac_f32_16x16x64_bf16 v[60:63], v[36:39], a[248:255], v225\n\t"
                         "ds_read_b64_tr_b16 v[4:5], v233 offset:576\n\t" "ds_read_b64_tr_b16 v[6:7], v233 offset:608\n\t" "s_waitcnt lgkmcnt(9)\n\t" "v_smfmac_f32_16x16x64_bf16 v[112:115], v[40:43], v[64:71], v225\n\t"
                         "s_waitcnt lgkmcnt(8)\n\t" "s_barrier\n\t" "v_smfmac_f32_16x16x64_bf16 v[116:119], v[40:43], v[72:79], v225\n\t" "ds_read_b64_tr_b16 v[8:9], v233 offset:1152\n\t" "s_waitcnt lgkmcnt(7)\n\t"
                         "v_add_f32 v60, v60, v62\n\t" "v_add_f32 v61, v61, v63\n\t" "v_smfmac_f32_16x16x64_bf16 v[112:115], v[44:47], v[80:87], v225\n\t" "ds_read_b64_tr_b16 v[10:11], v233 offset:1184\n\t"
                         "v_cvt_pk_bf16_f32 v62, v60, v61\n\t" "v_smfmac_f32_16x16x64_bf16 v[116:119], v[44:47], v[88:95], v225\n\t" "ds_write_b32 v230, v62 offset:2304\n\t" "s_waitcnt lgkmcnt(7)\n\t"
                         "v_smfmac_f32_16x16x64_bf16 v[112:115], v[48:51], v[96:103], v225\n\t" "ds_read_b64_tr_b16 v[12:13], v233 offset:1728\n\t" "v_smfmac_f32_16x16x64_bf16 v[116:119], v[48:51], v[104:111], v225\n\t"
                         "ds_read_b64_tr_b16 v[14:15], v233 offset:1760\n\t" "s_waitcnt lgkmcnt(5)\n\t" "v_smfmac_f32_16x16x64_bf16 v[120:123], v[40:43], v[0:7], v225\n\t" "v_cndmask_b32 v24, v112, v113, %8\n\t"
                         "v_cndmask_b32 v25, v114, v115, %8\n\t" "v_add_f32 v24, v24, v25\n\t" "v_pk_add_f32 v[26:27], v[116:117], v[118:119]\n\t" "v_cndmask_b32 v25, v26, v27, %8\n\t" "v_max_f32 v26, v24, v25\n\t"
                         "s_waitcnt lgkmcnt(2)\n\t" "s_barrier\n\t" "ds_read_b64_tr_b16 v[16:17], v233 offset:2304\n\t" "v_max_f32_dpp v27, v26, v26 row_ror:4 row_mask:0xf bank_mask:0xf\n\t"
                         "ds_read_b64_tr_b16 v[18:19], v233 offset:2336\n\t" "v_max_f32_dpp v27, v26, v27 row_ror:8 row_mask:0xf bank_mask:0xf\n\t" "v_max_f32_dpp v27, v26, v27 row_ror:12 row_mask:0xf bank_mask:0xf\n\t"
                         "ds_read_b64_tr_b16 v[20:21], v233 offset:2880\n\t" "ds_read_b64_tr_b16 v[22:23], v233 offset:2912\n\t" "v_max_f32_dpp v30, v27, v27 row_ror:1 row_mask:0xf bank_mask:0xf\n\t"
                         "v_max_f32_dpp v30, v27, v30 row_ror:2 row_mask:0xf bank_mask:0xf\n\t" "v_max_f32_dpp v30, v27, v30 row_ror:3 row_mask:0xf bank_mask:0xf\n\t" "s_waitcnt lgkmcnt(4)\n\t"
                         "v_pk_add_f32 v[24:25], v[24:25], v[30:31] op_sel_hi:[1,0] neg_lo:[0,1] neg_hi:[0,1]\n\t" "v_pk_mul_f32 v[24:25], v[24:25], v[236:237] op_sel_hi:[1,0]\n\t" "v_exp_f32 v24, v24\n\t"
                         "v_exp_f32 v25, v25\n\t" "v_mul_f32 v31, %10, v30\n\t" "v_cvt_pk_bf16_f32 v28, |v24|, |v25|\n\t" "v_smfmac_f32_16x16x64_bf16 v[120:123], v[44:47], v[8:15], v225\n\t"
                         "global_store_dword v234, v28, %7 offset:-4096 sc1\n\t" "s_waitcnt lgkmcnt(0)\n\t" "v_add_f32 v26, v24, v25\n\t" "v_smfmac_f32_16x16x64_bf16 v[120:123], v[48:51], v[16:23], v225\n\t"
                         "s_nop 0\n\t" "v_add_f32_dpp v27, v26, v26 row_ror:4 row_mask:0xf bank_mask:0xf\n\t" "v_add_f32_dpp v27, v26, v27 row_ror:8 row_mask:0xf bank_mask:0xf\n\t"
                         "v_add_f32_dpp v27, v26, v27 row_ror:12 row_mask:0xf bank_mask:0xf\n\t" "s_nop 1\n\t" "v_add_f32_dpp v29, v27, v27 row_ror:1 row_mask:0xf bank_mask:0xf\n\t"
                         "v_pk_add_f32 v[24:25], v[120:121], v[122:123]\n\t" "v_cndmask_b32 v24, v24, v25, %8\n\t" "v_max_f32 v26, v24, v30\n\t" "v_add_f32_dpp v29, v27, v29 row_ror:2 row_mask:0xf bank_mask:0xf\n\t"
                         "v_add_f32_dpp v29, v27, v29 row_ror:3 row_mask:0xf bank_mask:0xf\n\t" "v_max_f32_dpp v27, v26, v26 row_ror:1 row_mask:0xf bank_mask:0xf\n\t"
                         "v_max_f32_dpp v27, v26, v27 row_ror:2 row_mask:0xf bank_mask:0xf\n\t" "v_max_f32_dpp v27, v26, v27 row_ror:3 row_mask:0xf bank_mask:0xf\n\t" "v_mul_f32 v24, %10, v24\n\t"
                         "v_mul_f32 v29, 0x3e000000, v29\n\t" "v_max_f32_dpp v26, v27, v27 row_ror:4 row_mask:0xf bank_mask:0xf\n\t" "v_fma_f32 v24, -v26, %10, v24\n\t" "v_fma_f32 v25, -v26, %10, v31\n\t"
                         "v_exp_f32 v24, v24\n\t" "v_exp_f32 v25, v25\n\t" "s_nop 0\n\t" "v_fma_f32 v26, v25, v29, v24\n\t" "v_mov_b32_dpp v124, v24 quad_perm:[0,1,2,3] row_mask:0xf bank_mask:0x3\n\t"
                         "v_mov_b32_dpp v125, v24 quad_perm:[1,0,3,2] row_mask:0xf bank_mask:0x3\n\t" "v_add_f32_dpp v27, v26, v26 row_ror:1 row_mask:0xf bank_mask:0xf\n\t"
                         "v_add_f32_dpp v27, v26, v27 row_ror:2 row_mask:0xf bank_mask:0xf\n\t" "v_add_f32_dpp v27, v26, v27 row_ror:3 row_mask:0xf bank_mask:0xf\n\t"
                         "v_mov_b32_dpp v124, v25 quad_perm:[0,1,2,3] row_mask:0x5 bank_mask:0x4\n\t" "v_mov_b32_dpp v125, v25 quad_perm:[0,1,2,3] row_mask:0xa bank_mask:0x4\n\t"
                         "v_add_f32_dpp v26, v27, v27 row_ror:4 row_mask:0xf bank_mask:0xf\n\t" "v_rcp_f32 v26, v26\n\t" "s_mov_b64 exec, %9\n\t" "v_pk_mul_f32 v[124:125], v[124:125], v[26:27] op_sel_hi:[1,0]\n\t"
                         "v_cvt_pk_bf16_f32 v24, |v124|, |v125|\n\t" "global_store_dword v235, v24, %7 offset:-4096 sc1\n\t" "s_mov_b64 exec, -1\n\t" "s_memrealtime %1\n\t" "s_load_dword %2, %14, %13 offset:%15\n\t"
                         "ds_read_b64_tr_b16 v[36:37], v231\n\t" "ds_read_b64_tr_b16 v[38:39], v231 offset:32\n\t" "v_readfirstlane_b32 %3, v237\n\t" "s_mov_b32 m0, %17\n\t" "v_mov_b64 v[40:41], 0\n\t"
                         "v_mov_b64 v[42:43], 0\n\t" "v_mov_b64 v[44:45], 0\n\t" "v_mov_b64 v[46:47], 0\n\t" "v_mov_b64 v[48:49], 0\n\t" "v_mov_b64 v[50:51], 0\n\t" "v_mov_b64 v[52:53], 0\n\t" "v_mov_b64 v[54:55], 0\n\t"
                         "v_mov_b64 v[56:57], 0\n\t" "v_mov_b64 v[58:59], 0\n\t" "v_mov_b64 v[60:61], 0\n\t" "v_mov_b64 v[62:63], 0\n\t" "v_mov_b32 v237, %18\n\t" "s_waitcnt lgkmcnt(0)\n\t" "ds_read_b128 v[64:67], v226\n\t"
                         "ds_read_b128 v[68:71], v226 offset:1024\n\t" "ds_read_b128 v[72:75], v226 offset:2048\n\t" "ds_read_b128 v[76:79], v226 offset:3072\n\t" "ds_read_b128 v[80:83], v226 offset:4096\n\t"
                         "ds_read_b128 v[84:87], v226 offset:5120\n\t" "s_cmp_eq_u32 %3, -1\n\t" "s_cbranch_scc1 .Lnk_s\n\t" "global_load_lds_dwordx4 v224, %16 sc1\n\t" "global_load_lds_dwordx4 v224, %16 offset:1024 sc1\n\t"
                         "global_load_lds_dwordx4 v224, %16 offset:2048 sc1\n\t" ".Lnk_s:\n\t" "global_store_dwordx4 v239, v[36:39], %21 sc1\n\t" "ds_read_b128 v[88:91], v226 offset:6144\n\t"
                         "ds_read_b128 v[92:95], v226 offset:7168\n\t" "ds_read_b128 v[96:99], v226 offset:8192\n\t" "ds_read_b128 v[100:103], v226 offset:9216\n\t" "ds_read_b128 v[104:107], v226 offset:10240\n\t"
                         "ds_read_b128 v[108:111], v226 offset:11264\n\t" "ds_read_b128 v[112:115], v226 offset:12288\n\t" "ds_read_b128 v[116:119], v226 offset:13312\n\t" "ds_read_b128 v[120:123], v226 offset:14336\n\t"
                         "ds_read_b128 v[124:127], v226 offset:15360\n\t" "s_and_b64 exec, %19, 1\n\t" "s_cbranch_execz .Lnf_s\n\t" "s_sleep 4\n\t" "global_load_dword v237, v237, %20 sc1\n\t" ".Lnf_s:\n\t"
                         "s_mov_b64 exec, -1\n\t" ".Lsk_s:\n\t" "s_mov_b64 %0, vcc\n\t"
                         : "=&s"(bad), "=&s"(tp8), "=&s"(tt), "=&s"(kf)
                         : "s"(uni((int)tgt)), "s"(memp + (h & 4) * 256), "s"(memp - (h & 4) * 256), "s"((u64)memp & ~8192ull), "s"(mb0), "s"(psm), "s"(kscale * 1.4426950408889634f), "s"(en), "s"(tries), "s"(4 * s),
                           "s"(__builtin_amdgcn_kernarg_segment_ptr()), "n"(TT_OFF), "s"(kxp), "s"(kxw), "s"(kfo + (s < 6 ? 0 : (s + 2) >> 3) * 4096), "s"(0x0040404040404044ull >> s), "s"(fl), "s"(kmp),
                           "{v[224:239]}"(ad), "{v[64:95]}"(wk[0]), "{v[96:127]}"(wk[1]), "{v[32:63]}"(wk[2]),
                           "{a[0:31]}"(wa[0]), "{a[32:63]}"(wa[1]), "{a[64:95]}"(wa[2]), "{a[96:127]}"(wa[3]), "{a[128:159]}"(wa[4]), "{a[160:191]}"(wa[5]), "{a[192:223]}"(wa[6]), "{a[224:255]}"(wa[7]), "{v[128:159]}"(wv[0]), "{v[160:191]}"(wv[1]), "{v[192:223]}"(wv[2])
                         : "v0", "v1", "v2", "v3", "v4", "v5", "v6", "v7", "v8", "v9", "v10", "v11", "v12", "v13", "v14", "v15", "v16", "v17", "v18", "v19", "v20", "v21", "v22", "v23", "v24", "v25", "v26", "v27", "v28", "v29", "v30", "v31", "vcc", "scc", "s42", "memory");
            if (__builtin_expect(bad == 0, 1)) break;
            waitvm<0>();
            if (((++tries & 15) == 0 && !unwritten(memp, ad[0])) || --budget < 0) en = 0;
            phi += (u32)cfg.G;
        }
        if (__builtin_expect(kf == -1, 0)) { flag_wait(fl, F_K + pblk(s + 1) * 32 + h, 1); kxdma(); }
        kxp += s < NSEG - 2 ? NH * 12288 : 0;
        clk.calib();
        if (__builtin_expect(s >= NSEG - 2, 0)) {
            waitvm<0>();
            barrier();
            if (w == 3) flag_add(fl, F_PROG + 8 * s + (h & 7), 1);
        }
        if (s == 0) { phi = go_wg(fl, h, w, lds + SL_PF - 16) - (tt >> 16); edge(clk.r1, clk.m1); clk.calib(); }
        follow(phi, (int)(tt >> 16) - (cfg.dV >> 24), (u32)tp8, cfg.G);
        phi += tt & 0xffffu;
        memp = p.ws + mslot(sd, s + 1); kmp += NH * 3072;
    }
    if (h == 0 && w == 0) {
        flag_wait(fl, F_ACK, 256);
        Sync y;
        sync_ld(y, p.ws);
        asm volatile("s_waitcnt vmcnt(0)" : "+v"(y.r), "+v"(y.g), "+v"(y.e) : : "memory");
        const u64 t = ((u64)y.e[1] << 32 | y.e[0]) + 1;
        if (lanex() == 0) gst8(p.ws + OFF_LAT, 1024, u32x2{(u32)t, (u32)(t >> 32)});
    }
    asm volatile(".Lkm_last:\n\ts_waitcnt vmcnt(0)" : : : "memory");
}

DI void proj_role(const Ptrs& p, const ChainCfg cfg, int h, u32 lds, int w) {
    u32x32 wa[8], wv[4];
    u32 phi;
    int sd;
    {
        const int lane = lanex(), tid = 64 * w + lane;
        const int g = lane >> 4, qq = (lane >> 2) & 3, pp = lane & 3;
        constexpr int GS = 3104;
        auto dvo = [&](int i) { const int ix = i * 64 + lane; return (u32)((ix / 24) * (HD * 2) + (ix % 24) * 16); };
        auto ovo = [&](int i) { const int u = i * 64 + lane, r = u / 80; return (u32)(r * 1280 + (((u % 80) ^ (2 * (r ^ (w & 2)))) << 4)); };
        const u32 tra = lds + (2 * (g >> 1) + (qq >> 1)) * GS + (4 * (g & 1) + (qq & 1)) * 384 + 96 * w + 32 * (w & 1) + 8 * pp;
        const u32 tro = (4 * g + (qq & 1) + 16 * (qq >> 1)) * 1280 + 8 * pp + ((u32)(2 * w ^ 2 * ((4 * g + qq) & 7)) << 4);
        u32x16 pa = u32x16{dvo(0), dvo(1), dvo(2), tra, tra - 96 * (w & 1), tro, lds + (u32)((w & 1) * 24576 + (w >> 1) * 99328 + lane * 16), (u32)(lane & 31) * 8,
                           ovo(0), ovo(1), ovo(2), ovo(3), ovo(4), tro, tro, tro};
        Sync y;
        asm volatile("s_nop 4\n\t" "s_add_u32 s42, %22, 0x0\n\t" "s_add_u32 s42, s44, s42\n\t" "s_addc_u32 s43, s45, 0\n\t" "s_add_u32 m0, %18, 0x0\n\t" "s_nop 0\n\t" "global_load_lds_dwordx4 v224, s[42:43]\n\t"
                     "s_add_u32 m0, %18, 0x400\n\t" "s_nop 0\n\t" "global_load_lds_dwordx4 v225, s[42:43]\n\t" "s_add_u32 m0, %18, 0x800\n\t" "s_nop 0\n\t" "global_load_lds_dwordx4 v226, s[42:43]\n\t"
                     "s_add_u32 s42, %22, 0x18000\n\t" "s_add_u32 s42, s44, s42\n\t" "s_addc_u32 s43, s45, 0\n\t" "s_add_u32 m0, %18, 0xc20\n\t" "s_nop 0\n\t" "global_load_lds_dwordx4 v224, s[42:43]\n\t"
                     "s_add_u32 m0, %18, 0x1020\n\t" "s_nop 0\n\t" "global_load_lds_dwordx4 v225, s[42:43]\n\t" "s_add_u32 m0, %18, 0x1420\n\t" "s_nop 0\n\t" "global_load_lds_dwordx4 v226, s[42:43]\n\t"
                     "s_sub_u32 s42, 0xc0000, %22\n\t" "s_add_u32 s42, s44, s42\n\t" "s_addc_u32 s43, s45, 0\n\t" "s_add_u32 m0, %18, 0x6100\n\t" "s_nop 0\n\t" "global_load_lds_dwordx4 v224, s[42:43]\n\t"
                     "s_add_u32 m0, %18, 0x6500\n\t" "s_nop 0\n\t" "global_load_lds_dwordx4 v225, s[42:43]\n\t" "s_add_u32 m0, %18, 0x6900\n\t" "s_nop 0\n\t" "global_load_lds_dwordx4 v226, s[42:43]\n\t"
                     "s_sub_u32 s42, 0xd8000, %22\n\t" "s_add_u32 s42, s44, s42\n\t" "s_addc_u32 s43, s45, 0\n\t" "s_add_u32 m0, %18, 0x6d20\n\t" "s_nop 0\n\t" "global_load_lds_dwordx4 v224, s[42:43]\n\t"
                     "s_add_u32 m0, %18, 0x7120\n\t" "s_nop 0\n\t" "global_load_lds_dwordx4 v225, s[42:43]\n\t" "s_add_u32 m0, %18, 0x7520\n\t" "s_nop 0\n\t" "global_load_lds_dwordx4 v226, s[42:43]\n\t"
                     "s_add_u32 s42, %22, 0x180000\n\t" "s_add_u32 s42, s44, s42\n\t" "s_addc_u32 s43, s45, 0\n\t" "s_add_u32 m0, %18, 0xc200\n\t" "s_nop 0\n\t" "global_load_lds_dwordx4 v224, s[42:43]\n\t"
                     "s_add_u32 m0, %18, 0xc600\n\t" "s_nop 0\n\t" "global_load_lds_dwordx4 v225, s[42:43]\n\t" "s_add_u32 m0, %18, 0xca00\n\t" "s_nop 0\n\t" "global_load_lds_dwordx4 v226, s[42:43]\n\t"
                     "s_add_u32 s42, %22, 0x198000\n\t" "s_add_u32 s42, s44, s42\n\t" "s_addc_u32 s43, s45, 0\n\t" "s_add_u32 m0, %18, 0xce20\n\t" "s_nop 0\n\t" "global_load_lds_dwordx4 v224, s[42:43]\n\t"
                     "s_add_u32 m0, %18, 0xd220\n\t" "s_nop 0\n\t" "global_load_lds_dwordx4 v225, s[42:43]\n\t" "s_add_u32 m0, %18, 0xd620\n\t" "s_nop 0\n\t" "global_load_lds_dwordx4 v226, s[42:43]\n\t"
                     "s_sub_u32 s42, 0x240000, %22\n\t" "s_add_u32 s42, s44, s42\n\t" "s_addc_u32 s43, s45, 0\n\t" "s_add_u32 m0, %18, 0x12300\n\t" "s_nop 0\n\t" "global_load_lds_dwordx4 v224, s[42:43]\n\t"
                     "s_add_u32 m0, %18, 0x12700\n\t" "s_nop 0\n\t" "global_load_lds_dwordx4 v225, s[42:43]\n\t" "s_add_u32 m0, %18, 0x12b00\n\t" "s_nop 0\n\t" "global_load_lds_dwordx4 v226, s[42:43]\n\t"
                     "s_sub_u32 s42, 0x258000, %22\n\t" "s_add_u32 s42, s44, s42\n\t" "s_addc_u32 s43, s45, 0\n\t" "s_add_u32 m0, %18, 0x12f20\n\t" "s_nop 0\n\t" "global_load_lds_dwordx4 v224, s[42:43]\n\t"
                     "s_add_u32 m0, %18, 0x13320\n\t" "s_nop 0\n\t" "global_load_lds_dwordx4 v225, s[42:43]\n\t" "s_add_u32 m0, %18, 0x13720\n\t" "s_nop 0\n\t" "global_load_lds_dwordx4 v226, s[42:43]\n\t"
                     "s_waitcnt vmcnt(12) lgkmcnt(0)\n\t" "s_barrier\n\t" "s_add_u32 s42, %22, 0x300000\n\t" "s_add_u32 s42, s44, s42\n\t" "s_addc_u32 s43, s45, 0\n\t" "s_add_u32 m0, %18, 0x18400\n\t" "s_nop 0\n\t"
                     "global_load_lds_dwordx4 v224, s[42:43]\n\t" "s_add_u32 m0, %18, 0x18800\n\t" "s_nop 0\n\t" "global_load_lds_dwordx4 v225, s[42:43]\n\t" "s_add_u32 m0, %18, 0x18c00\n\t" "s_nop 0\n\t"
                     "global_load_lds_dwordx4 v226, s[42:43]\n\t" "s_add_u32 s42, %22, 0x318000\n\t" "s_add_u32 s42, s44, s42\n\t" "s_addc_u32 s43, s45, 0\n\t" "s_add_u32 m0, %18, 0x19020\n\t" "s_nop 0\n\t"
                     "global_load_lds_dwordx4 v224, s[42:43]\n\t" "s_add_u32 m0, %18, 0x19420\n\t" "s_nop 0\n\t" "global_load_lds_dwordx4 v225, s[42:43]\n\t" "s_add_u32 m0, %18, 0x19820\n\t" "s_nop 0\n\t"
                     "global_load_lds_dwordx4 v226, s[42:43]\n\t" "s_sub_u32 s42, 0x3c0000, %22\n\t" "s_add_u32 s42, s44, s42\n\t" "s_addc_u32 s43, s45, 0\n\t" "s_add_u32 m0, %18, 0x1e500\n\t" "s_nop 0\n\t"
                     "global_load_lds_dwordx4 v224, s[42:43]\n\t" "s_add_u32 m0, %18, 0x1e900\n\t" "s_nop 0\n\t" "global_load_lds_dwordx4 v225, s[42:43]\n\t" "s_add_u32 m0, %18, 0x1ed00\n\t" "s_nop 0\n\t"
                     "global_load_lds_dwordx4 v226, s[42:43]\n\t" "s_sub_u32 s42, 0x3d8000, %22\n\t" "s_add_u32 s42, s44, s42\n\t" "s_addc_u32 s43, s45, 0\n\t" "s_add_u32 m0, %18, 0x1f120\n\t" "s_nop 0\n\t"
                     "global_load_lds_dwordx4 v224, s[42:43]\n\t" "s_add_u32 m0, %18, 0x1f520\n\t" "s_nop 0\n\t" "global_load_lds_dwordx4 v225, s[42:43]\n\t" "s_add_u32 m0, %18, 0x1f920\n\t" "s_nop 0\n\t"
                     "global_load_lds_dwordx4 v226, s[42:43]\n\t" "v_add_u32 v237, 0x0, v227\n\t" "v_add_u32 v238, 0x0, v228\n\t" "ds_read_b64_tr_b16 a[0:1], v237\n\t" "ds_read_b64_tr_b16 a[2:3], v237 offset:768\n\t"
                     "ds_read_b64_tr_b16 a[4:5], v237 offset:12416\n\t" "ds_read_b64_tr_b16 a[6:7], v237 offset:13184\n\t" "ds_read_b64_tr_b16 v[0:1], v237 offset:32\n\t" "ds_read_b64_tr_b16 v[2:3], v237 offset:800\n\t"
                     "ds_read_b64_tr_b16 v[4:5], v237 offset:12448\n\t" "ds_read_b64_tr_b16 v[6:7], v237 offset:13216\n\t" "ds_read_b64_tr_b16 a[8:9], v238 offset:64\n\t" "ds_read_b64_tr_b16 a[10:11], v238 offset:832\n\t"
                     "ds_read_b64_tr_b16 a[12:13], v238 offset:12480\n\t" "ds_read_b64_tr_b16 a[14:15], v238 offset:13248\n\t" "ds_read_b64_tr_b16 a[32:33], v237 offset:24832\n\t"
                     "ds_read_b64_tr_b16 a[34:35], v237 offset:25600\n\t" "ds_read_b64_tr_b16 a[36:37], v237 offset:37248\n\t" "ds_read_b64_tr_b16 a[38:39], v237 offset:38016\n\t"
                     "ds_read_b64_tr_b16 v[16:17], v237 offset:24864\n\t" "ds_read_b64_tr_b16 v[18:19], v237 offset:25632\n\t" "ds_read_b64_tr_b16 v[20:21], v237 offset:37280\n\t"
                     "ds_read_b64_tr_b16 v[22:23], v237 offset:38048\n\t" "ds_read_b64_tr_b16 a[40:41], v238 offset:24896\n\t" "ds_read_b64_tr_b16 a[42:43], v238 offset:25664\n\t"
                     "ds_read_b64_tr_b16 a[44:45], v238 offset:37312\n\t" "ds_read_b64_tr_b16 a[46:47], v238 offset:38080\n\t" "s_waitcnt vmcnt(12) lgkmcnt(0)\n\t" "s_barrier\n\t" "s_add_u32 s42, %22, 0x480000\n\t"
                     "s_add_u32 s42, s44, s42\n\t" "s_addc_u32 s43, s45, 0\n\t" "s_add_u32 m0, %18, 0x0\n\t" "s_nop 0\n\t" "global_load_lds_dwordx4 v224, s[42:43]\n\t" "s_add_u32 m0, %18, 0x400\n\t" "s_nop 0\n\t"
                     "global_load_lds_dwordx4 v225, s[42:43]\n\t" "s_add_u32 m0, %18, 0x800\n\t" "s_nop 0\n\t" "global_load_lds_dwordx4 v226, s[42:43]\n\t" "s_add_u32 s42, %22, 0x498000\n\t" "s_add_u32 s42, s44, s42\n\t"
                     "s_addc_u32 s43, s45, 0\n\t" "s_add_u32 m0, %18, 0xc20\n\t" "s_nop 0\n\t" "global_load_lds_dwordx4 v224, s[42:43]\n\t" "s_add_u32 m0, %18, 0x1020\n\t" "s_nop 0\n\t"
                     "global_load_lds_dwordx4 v225, s[42:43]\n\t" "s_add_u32 m0, %18, 0x1420\n\t" "s_nop 0\n\t" "global_load_lds_dwordx4 v226, s[42:43]\n\t" "s_sub_u32 s42, 0x540000, %22\n\t" "s_add_u32 s42, s44, s42\n\t"
                     "s_addc_u32 s43, s45, 0\n\t" "s_add_u32 m0, %18, 0x6100\n\t" "s_nop 0\n\t" "global_load_lds_dwordx4 v224, s[42:43]\n\t" "s_add_u32 m0, %18, 0x6500\n\t" "s_nop 0\n\t"
                     "global_load_lds_dwordx4 v225, s[42:43]\n\t" "s_add_u32 m0, %18, 0x6900\n\t" "s_nop 0\n\t" "global_load_lds_dwordx4 v226, s[42:43]\n\t" "s_sub_u32 s42, 0x558000, %22\n\t" "s_add_u32 s42, s44, s42\n\t"
                     "s_addc_u32 s43, s45, 0\n\t" "s_add_u32 m0, %18, 0x6d20\n\t" "s_nop 0\n\t" "global_load_lds_dwordx4 v224, s[42:43]\n\t" "s_add_u32 m0, %18, 0x7120\n\t" "s_nop 0\n\t"
                     "global_load_lds_dwordx4 v225, s[42:43]\n\t" "s_add_u32 m0, %18, 0x7520\n\t" "s_nop 0\n\t" "global_load_lds_dwordx4 v226, s[42:43]\n\t" "v_add_u32 v237, 0xc200, v227\n\t"
                     "v_add_u32 v238, 0xc200, v228\n\t" "ds_read_b64_tr_b16 a[16:17], v237\n\t" "ds_read_b64_tr_b16 a[18:19], v237 offset:768\n\t" "ds_read_b64_tr_b16 a[20:21], v237 offset:12416\n\t"
                     "ds_read_b64_tr_b16 a[22:23], v237 offset:13184\n\t" "ds_read_b64_tr_b16 v[8:9], v237 offset:32\n\t" "ds_read_b64_tr_b16 v[10:11], v237 offset:800\n\t"
                     "ds_read_b64_tr_b16 v[12:13], v237 offset:12448\n\t" "ds_read_b64_tr_b16 v[14:15], v237 offset:13216\n\t" "ds_read_b64_tr_b16 a[24:25], v238 offset:64\n\t"
                     "ds_read_b64_tr_b16 a[26:27], v238 offset:832\n\t" "ds_read_b64_tr_b16 a[28:29], v238 offset:12480\n\t" "ds_read_b64_tr_b16 a[30:31], v238 offset:13248\n\t"
                     "ds_read_b64_tr_b16 a[48:49], v237 offset:24832\n\t" "ds_read_b64_tr_b16 a[50:51], v237 offset:25600\n\t" "ds_read_b64_tr_b16 a[52:53], v237 offset:37248\n\t"
                     "ds_read_b64_tr_b16 a[54:55], v237 offset:38016\n\t" "ds_read_b64_tr_b16 v[24:25], v237 offset:24864\n\t" "ds_read_b64_tr_b16 v[26:27], v237 offset:25632\n\t"
                     "ds_read_b64_tr_b16 v[28:29], v237 offset:37280\n\t" "ds_read_b64_tr_b16 v[30:31], v237 offset:38048\n\t" "ds_read_b64_tr_b16 a[56:57], v238 offset:24896\n\t"
                     "ds_read_b64_tr_b16 a[58:59], v238 offset:25664\n\t" "ds_read_b64_tr_b16 a[60:61], v238 offset:37312\n\t" "ds_read_b64_tr_b16 a[62:63], v238 offset:38080\n\t" "s_waitcnt vmcnt(12) lgkmcnt(0)\n\t"
                     "s_barrier\n\t" "s_add_u32 s42, %22, 0x600000\n\t" "s_add_u32 s42, s44, s42\n\t" "s_addc_u32 s43, s45, 0\n\t" "s_add_u32 m0, %18, 0xc200\n\t" "s_nop 0\n\t" "global_load_lds_dwordx4 v224, s[42:43]\n\t"
                     "s_add_u32 m0, %18, 0xc600\n\t" "s_nop 0\n\t" "global_load_lds_dwordx4 v225, s[42:43]\n\t" "s_add_u32 m0, %18, 0xca00\n\t" "s_nop 0\n\t" "global_load_lds_dwordx4 v226, s[42:43]\n\t"
                     "s_add_u32 s42, %22, 0x618000\n\t" "s_add_u32 s42, s44, s42\n\t" "s_addc_u32 s43, s45, 0\n\t" "s_add_u32 m0, %18, 0xce20\n\t" "s_nop 0\n\t" "global_load_lds_dwordx4 v224, s[42:43]\n\t"
                     "s_add_u32 m0, %18, 0xd220\n\t" "s_nop 0\n\t" "global_load_lds_dwordx4 v225, s[42:43]\n\t" "s_add_u32 m0, %18, 0xd620\n\t" "s_nop 0\n\t" "global_load_lds_dwordx4 v226, s[42:43]\n\t"
                     "s_sub_u32 s42, 0x6c0000, %22\n\t" "s_add_u32 s42, s44, s42\n\t" "s_addc_u32 s43, s45, 0\n\t" "s_add_u32 m0, %18, 0x12300\n\t" "s_nop 0\n\t" "global_load_lds_dwordx4 v224, s[42:43]\n\t"
                     "s_add_u32 m0, %18, 0x12700\n\t" "s_nop 0\n\t" "global_load_lds_dwordx4 v225, s[42:43]\n\t" "s_add_u32 m0, %18, 0x12b00\n\t" "s_nop 0\n\t" "global_load_lds_dwordx4 v226, s[42:43]\n\t"
                     "s_sub_u32 s42, 0x6d8000, %22\n\t" "s_add_u32 s42, s44, s42\n\t" "s_addc_u32 s43, s45, 0\n\t" "s_add_u32 m0, %18, 0x12f20\n\t" "s_nop 0\n\t" "global_load_lds_dwordx4 v224, s[42:43]\n\t"
                     "s_add_u32 m0, %18, 0x13320\n\t" "s_nop 0\n\t" "global_load_lds_dwordx4 v225, s[42:43]\n\t" "s_add_u32 m0, %18, 0x13720\n\t" "s_nop 0\n\t" "global_load_lds_dwordx4 v226, s[42:43]\n\t"
                     "v_add_u32 v237, 0x18400, v227\n\t" "v_add_u32 v238, 0x18400, v228\n\t" "ds_read_b64_tr_b16 a[64:65], v237\n\t" "ds_read_b64_tr_b16 a[66:67], v237 offset:768\n\t"
                     "ds_read_b64_tr_b16 a[68:69], v237 offset:12416\n\t" "ds_read_b64_tr_b16 a[70:71], v237 offset:13184\n\t" "ds_read_b64_tr_b16 v[32:33], v237 offset:32\n\t"
                     "ds_read_b64_tr_b16 v[34:35], v237 offset:800\n\t" "ds_read_b64_tr_b16 v[36:37], v237 offset:12448\n\t" "ds_read_b64_tr_b16 v[38:39], v237 offset:13216\n\t"
                     "ds_read_b64_tr_b16 a[72:73], v238 offset:64\n\t" "ds_read_b64_tr_b16 a[74:75], v238 offset:832\n\t" "ds_read_b64_tr_b16 a[76:77], v238 offset:12480\n\t"
                     "ds_read_b64_tr_b16 a[78:79], v238 offset:13248\n\t" "ds_read_b64_tr_b16 a[80:81], v237 offset:24832\n\t" "ds_read_b64_tr_b16 a[82:83], v237 offset:25600\n\t"
                     "ds_read_b64_tr_b16 a[84:85], v237 offset:37248\n\t" "ds_read_b64_tr_b16 a[86:87], v237 offset:38016\n\t" "ds_read_b64_tr_b16 v[40:41], v237 offset:24864\n\t"
                     "ds_read_b64_tr_b16 v[42:43], v237 offset:25632\n\t" "ds_read_b64_tr_b16 v[44:45], v237 offset:37280\n\t" "ds_read_b64_tr_b16 v[46:47], v237 offset:38048\n\t"
                     "ds_read_b64_tr_b16 a[88:89], v238 offset:24896\n\t" "ds_read_b64_tr_b16 a[90:91], v238 offset:25664\n\t" "ds_read_b64_tr_b16 a[92:93], v238 offset:37312\n\t"
                     "ds_read_b64_tr_b16 a[94:95], v238 offset:38080\n\t" "s_waitcnt vmcnt(12) lgkmcnt(0)\n\t" "s_barrier\n\t" "s_add_u32 s42, s46, 0x0\n\t" "s_addc_u32 s43, s47, 0\n\t" "s_add_u32 m0, %20, 0x18400\n\t"
                     "s_nop 0\n\t" "global_load_lds_dwordx4 v232, s[42:43]\n\t" "s_add_u32 m0, %20, 0x18800\n\t" "s_nop 0\n\t" "global_load_lds_dwordx4 v233, s[42:43]\n\t" "s_add_u32 m0, %20, 0x18c00\n\t" "s_nop 0\n\t"
                     "global_load_lds_dwordx4 v234, s[42:43]\n\t" "s_add_u32 m0, %20, 0x19000\n\t" "s_nop 0\n\t" "global_load_lds_dwordx4 v235, s[42:43]\n\t" "s_add_u32 m0, %20, 0x19400\n\t" "s_nop 0\n\t"
                     "global_load_lds_dwordx4 v236, s[42:43]\n\t" "s_add_u32 s42, s46, 0x1400\n\t" "s_addc_u32 s43, s47, 0\n\t" "s_add_u32 m0, %20, 0x19800\n\t" "v_xor_b32 v224, 0x80, v232\n\t"
                     "global_load_lds_dwordx4 v224, s[42:43]\n\t" "s_add_u32 m0, %20, 0x19c00\n\t" "v_xor_b32 v224, 0x80, v233\n\t" "global_load_lds_dwordx4 v224, s[42:43]\n\t" "s_add_u32 m0, %20, 0x1a000\n\t"
                     "v_xor_b32 v224, 0x80, v234\n\t" "global_load_lds_dwordx4 v224, s[42:43]\n\t" "s_add_u32 m0, %20, 0x1a400\n\t" "v_xor_b32 v224, 0x80, v235\n\t" "global_load_lds_dwordx4 v224, s[42:43]\n\t"
                     "s_add_u32 m0, %20, 0x1a800\n\t" "v_xor_b32 v224, 0x80, v236\n\t" "global_load_lds_dwordx4 v224, s[42:43]\n\t" "v_add_u32 v237, 0x0, v227\n\t" "v_add_u32 v238, 0x0, v228\n\t"
                     "ds_read_b64_tr_b16 a[96:97], v237\n\t" "ds_read_b64_tr_b16 a[98:99], v237 offset:768\n\t" "ds_read_b64_tr_b16 a[100:101], v237 offset:12416\n\t" "ds_read_b64_tr_b16 a[102:103], v237 offset:13184\n\t"
                     "ds_read_b64_tr_b16 v[48:49], v237 offset:32\n\t" "ds_read_b64_tr_b16 v[50:51], v237 offset:800\n\t" "ds_read_b64_tr_b16 v[52:53], v237 offset:12448\n\t"
                     "ds_read_b64_tr_b16 v[54:55], v237 offset:13216\n\t" "ds_read_b64_tr_b16 a[104:105], v238 offset:64\n\t" "ds_read_b64_tr_b16 a[106:107], v238 offset:832\n\t"
                     "ds_read_b64_tr_b16 a[108:109], v238 offset:12480\n\t" "ds_read_b64_tr_b16 a[110:111], v238 offset:13248\n\t" "ds_read_b64_tr_b16 a[112:113], v237 offset:24832\n\t"
                     "ds_read_b64_tr_b16 a[114:115], v237 offset:25600\n\t" "ds_read_b64_tr_b16 a[116:117], v237 offset:37248\n\t" "ds_read_b64_tr_b16 a[118:119], v237 offset:38016\n\t"
                     "ds_read_b64_tr_b16 v[56:57], v237 offset:24864\n\t" "ds_read_b64_tr_b16 v[58:59], v237 offset:25632\n\t" "ds_read_b64_tr_b16 v[60:61], v237 offset:37280\n\t"
                     "ds_read_b64_tr_b16 v[62:63], v237 offset:38048\n\t" "ds_read_b64_tr_b16 a[120:121], v238 offset:24896\n\t" "ds_read_b64_tr_b16 a[122:123], v238 offset:25664\n\t"
                     "ds_read_b64_tr_b16 a[124:125], v238 offset:37312\n\t" "ds_read_b64_tr_b16 a[126:127], v238 offset:38080\n\t" "s_waitcnt vmcnt(10) lgkmcnt(0)\n\t" "s_barrier\n\t" "s_add_u32 s42, s46, 0xa000\n\t"
                     "s_addc_u32 s43, s47, 0\n\t" "s_add_u32 m0, %20, 0x0\n\t" "s_nop 0\n\t" "global_load_lds_dwordx4 v232, s[42:43]\n\t" "s_add_u32 m0, %20, 0x400\n\t" "s_nop 0\n\t"
                     "global_load_lds_dwordx4 v233, s[42:43]\n\t" "s_add_u32 m0, %20, 0x800\n\t" "s_nop 0\n\t" "global_load_lds_dwordx4 v234, s[42:43]\n\t" "s_add_u32 m0, %20, 0xc00\n\t" "s_nop 0\n\t"
                     "global_load_lds_dwordx4 v235, s[42:43]\n\t" "s_add_u32 m0, %20, 0x1000\n\t" "s_nop 0\n\t" "global_load_lds_dwordx4 v236, s[42:43]\n\t" "s_add_u32 s42, s46, 0xb400\n\t" "s_addc_u32 s43, s47, 0\n\t"
                     "s_add_u32 m0, %20, 0x1400\n\t" "v_xor_b32 v224, 0x80, v232\n\t" "global_load_lds_dwordx4 v224, s[42:43]\n\t" "s_add_u32 m0, %20, 0x1800\n\t" "v_xor_b32 v224, 0x80, v233\n\t"
                     "global_load_lds_dwordx4 v224, s[42:43]\n\t" "s_add_u32 m0, %20, 0x1c00\n\t" "v_xor_b32 v224, 0x80, v234\n\t" "global_load_lds_dwordx4 v224, s[42:43]\n\t" "s_add_u32 m0, %20, 0x2000\n\t"
                     "v_xor_b32 v224, 0x80, v235\n\t" "global_load_lds_dwordx4 v224, s[42:43]\n\t" "s_add_u32 m0, %20, 0x2400\n\t" "v_xor_b32 v224, 0x80, v236\n\t" "global_load_lds_dwordx4 v224, s[42:43]\n\t"
                     "v_add_u32 v237, 0xc200, v227\n\t" "v_add_u32 v238, 0xc200, v228\n\t" "ds_read_b64_tr_b16 a[128:129], v237\n\t" "ds_read_b64_tr_b16 a[130:131], v237 offset:768\n\t"
                     "ds_read_b64_tr_b16 a[132:133], v237 offset:12416\n\t" "ds_read_b64_tr_b16 a[134:135], v237 offset:13184\n\t" "ds_read_b64_tr_b16 a[136:137], v237 offset:32\n\t"
                     "ds_read_b64_tr_b16 a[138:139], v237 offset:800\n\t" "ds_read_b64_tr_b16 a[140:141], v237 offset:12448\n\t" "ds_read_b64_tr_b16 a[142:143], v237 offset:13216\n\t"
                     "ds_read_b64_tr_b16 a[144:145], v238 offset:64\n\t" "ds_read_b64_tr_b16 a[146:147], v238 offset:832\n\t" "ds_read_b64_tr_b16 a[148:149], v238 offset:12480\n\t"
                     "ds_read_b64_tr_b16 a[150:151], v238 offset:13248\n\t" "ds_read_b64_tr_b16 a[152:153], v237 offset:24832\n\t" "ds_read_b64_tr_b16 a[154:155], v237 offset:25600\n\t"
                     "ds_read_b64_tr_b16 a[156:157], v237 offset:37248\n\t" "ds_read_b64_tr_b16 a[158:159], v237 offset:38016\n\t" "ds_read_b64_tr_b16 a[160:161], v237 offset:24864\n\t"
                     "ds_read_b64_tr_b16 a[162:163], v237 offset:25632\n\t" "ds_read_b64_tr_b16 a[164:165], v237 offset:37280\n\t" "ds_read_b64_tr_b16 a[166:167], v237 offset:38048\n\t"
                     "ds_read_b64_tr_b16 a[168:169], v238 offset:24896\n\t" "ds_read_b64_tr_b16 a[170:171], v238 offset:25664\n\t" "ds_read_b64_tr_b16 a[172:173], v238 offset:37312\n\t"
                     "ds_read_b64_tr_b16 a[174:175], v238 offset:38080\n\t" "s_waitcnt vmcnt(10) lgkmcnt(0)\n\t" "s_barrier\n\t" "s_add_u32 s42, s46, 0x14000\n\t" "s_addc_u32 s43, s47, 0\n\t"
                     "s_add_u32 m0, %20, 0xc200\n\t" "s_nop 0\n\t" "global_load_lds_dwordx4 v232, s[42:43]\n\t" "s_add_u32 m0, %20, 0xc600\n\t" "s_nop 0\n\t" "global_load_lds_dwordx4 v233, s[42:43]\n\t"
                     "s_add_u32 m0, %20, 0xca00\n\t" "s_nop 0\n\t" "global_load_lds_dwordx4 v234, s[42:43]\n\t" "s_add_u32 m0, %20, 0xce00\n\t" "s_nop 0\n\t" "global_load_lds_dwordx4 v235, s[42:43]\n\t"
                     "s_add_u32 m0, %20, 0xd200\n\t" "s_nop 0\n\t" "global_load_lds_dwordx4 v236, s[42:43]\n\t" "s_add_u32 s42, s46, 0x15400\n\t" "s_addc_u32 s43, s47, 0\n\t" "s_add_u32 m0, %20, 0xd600\n\t"
                     "v_xor_b32 v224, 0x80, v232\n\t" "global_load_lds_dwordx4 v224, s[42:43]\n\t" "s_add_u32 m0, %20, 0xda00\n\t" "v_xor_b32 v224, 0x80, v233\n\t" "global_load_lds_dwordx4 v224, s[42:43]\n\t"
                     "s_add_u32 m0, %20, 0xde00\n\t" "v_xor_b32 v224, 0x80, v234\n\t" "global_load_lds_dwordx4 v224, s[42:43]\n\t" "s_add_u32 m0, %20, 0xe200\n\t" "v_xor_b32 v224, 0x80, v235\n\t"
                     "global_load_lds_dwordx4 v224, s[42:43]\n\t" "s_add_u32 m0, %20, 0xe600\n\t" "v_xor_b32 v224, 0x80, v236\n\t" "global_load_lds_dwordx4 v224, s[42:43]\n\t" "s_add_u32 s42, %21, 0x18400\n\t"
                     "v_xor_b32 v237, 0x0, v229\n\t" "v_add_u32 v237, s42, v237\n\t" "v_xor_b32 v238, 0x40, v229\n\t" "v_add_u32 v238, s42, v238\n\t" "v_xor_b32 v239, 0x80, v229\n\t" "v_add_u32 v239, s42, v239\n\t"
                     "v_xor_b32 v226, 0xc0, v229\n\t" "v_add_u32 v226, s42, v226\n\t" "ds_read_b64_tr_b16 a[176:177], v237\n\t" "ds_read_b64_tr_b16 a[178:179], v238 offset:2560\n\t"
                     "ds_read_b64_tr_b16 a[200:201], v239\n\t" "ds_read_b64_tr_b16 a[202:203], v226 offset:2560\n\t" "ds_read_b64_tr_b16 a[224:225], v237 offset:256\n\t"
                     "ds_read_b64_tr_b16 a[226:227], v238 offset:2816\n\t" "ds_read_b64_tr_b16 a[248:249], v239 offset:256\n\t" "ds_read_b64_tr_b16 a[250:251], v226 offset:2816\n\t"
                     "ds_read_b64_tr_b16 v[112:113], v237 offset:512\n\t" "ds_read_b64_tr_b16 v[114:115], v238 offset:3072\n\t" "ds_read_b64_tr_b16 v[136:137], v239 offset:512\n\t"
                     "ds_read_b64_tr_b16 v[138:139], v226 offset:3072\n\t" "ds_read_b64_tr_b16 v[160:161], v237 offset:768\n\t" "ds_read_b64_tr_b16 v[162:163], v238 offset:3328\n\t"
                     "ds_read_b64_tr_b16 v[184:185], v239 offset:768\n\t" "ds_read_b64_tr_b16 v[186:187], v226 offset:3328\n\t" "ds_read_b64_tr_b16 v[208:209], v237 offset:1024\n\t"
                     "ds_read_b64_tr_b16 v[210:211], v238 offset:3584\n\t" "ds_read_b64_tr_b16 v[72:73], v239 offset:1024\n\t" "ds_read_b64_tr_b16 v[74:75], v226 offset:3584\n\t" "s_waitcnt vmcnt(10) lgkmcnt(0)\n\t"
                     "s_barrier\n\t" "s_add_u32 s42, s46, 0x1e000\n\t" "s_addc_u32 s43, s47, 0\n\t" "s_add_u32 m0, %20, 0x18400\n\t" "s_nop 0\n\t" "global_load_lds_dwordx4 v232, s[42:43]\n\t"
                     "s_add_u32 m0, %20, 0x18800\n\t" "s_nop 0\n\t" "global_load_lds_dwordx4 v233, s[42:43]\n\t" "s_add_u32 m0, %20, 0x18c00\n\t" "s_nop 0\n\t" "global_load_lds_dwordx4 v234, s[42:43]\n\t"
                     "s_add_u32 m0, %20, 0x19000\n\t" "s_nop 0\n\t" "global_load_lds_dwordx4 v235, s[42:43]\n\t" "s_add_u32 m0, %20, 0x19400\n\t" "s_nop 0\n\t" "global_load_lds_dwordx4 v236, s[42:43]\n\t"
                     "s_add_u32 s42, s46, 0x1f400\n\t" "s_addc_u32 s43, s47, 0\n\t" "s_add_u32 m0, %20, 0x19800\n\t" "v_xor_b32 v224, 0x80, v232\n\t" "global_load_lds_dwordx4 v224, s[42:43]\n\t"
                     "s_add_u32 m0, %20, 0x19c00\n\t" "v_xor_b32 v224, 0x80, v233\n\t" "global_load_lds_dwordx4 v224, s[42:43]\n\t" "s_add_u32 m0, %20, 0x1a000\n\t" "v_xor_b32 v224, 0x80, v234\n\t"
                     "global_load_lds_dwordx4 v224, s[42:43]\n\t" "s_add_u32 m0, %20, 0x1a400\n\t" "v_xor_b32 v224, 0x80, v235\n\t" "global_load_lds_dwordx4 v224, s[42:43]\n\t" "s_add_u32 m0, %20, 0x1a800\n\t"
                     "v_xor_b32 v224, 0x80, v236\n\t" "global_load_lds_dwordx4 v224, s[42:43]\n\t" "s_add_u32 s42, %21, 0x0\n\t" "v_xor_b32 v237, 0x0, v229\n\t" "v_add_u32 v237, s42, v237\n\t"
                     "v_xor_b32 v238, 0x40, v229\n\t" "v_add_u32 v238, s42, v238\n\t" "v_xor_b32 v239, 0x80, v229\n\t" "v_add_u32 v239, s42, v239\n\t" "v_xor_b32 v226, 0xc0, v229\n\t" "v_add_u32 v226, s42, v226\n\t"
                     "ds_read_b64_tr_b16 a[180:181], v237\n\t" "ds_read_b64_tr_b16 a[182:183], v238 offset:2560\n\t" "ds_read_b64_tr_b16 a[204:205], v239\n\t" "ds_read_b64_tr_b16 a[206:207], v226 offset:2560\n\t"
                     "ds_read_b64_tr_b16 a[228:229], v237 offset:256\n\t" "ds_read_b64_tr_b16 a[230:231], v238 offset:2816\n\t" "ds_read_b64_tr_b16 a[252:253], v239 offset:256\n\t"
                     "ds_read_b64_tr_b16 a[254:255], v226 offset:2816\n\t" "ds_read_b64_tr_b16 v[116:117], v237 offset:512\n\t" "ds_read_b64_tr_b16 v[118:119], v238 offset:3072\n\t"
                     "ds_read_b64_tr_b16 v[140:141], v239 offset:512\n\t" "ds_read_b64_tr_b16 v[142:143], v226 offset:3072\n\t" "ds_read_b64_tr_b16 v[164:165], v237 offset:768\n\t"
                     "ds_read_b64_tr_b16 v[166:167], v238 offset:3328\n\t" "ds_read_b64_tr_b16 v[188:189], v239 offset:768\n\t" "ds_read_b64_tr_b16 v[190:191], v226 offset:3328\n\t"
                     "ds_read_b64_tr_b16 v[212:213], v237 offset:1024\n\t" "ds_read_b64_tr_b16 v[214:215], v238 offset:3584\n\t" "ds_read_b64_tr_b16 v[76:77], v239 offset:1024\n\t"
                     "ds_read_b64_tr_b16 v[78:79], v226 offset:3584\n\t" "s_waitcnt vmcnt(10) lgkmcnt(0)\n\t" "s_barrier\n\t" "v_mov_b32 v225, 0\n\t" "global_load_dwordx2 %12, v231, %19 sc1\n\t"
                     "global_load_dwordx2 %13, v225, %19 offset:512 sc1\n\t" "global_load_dwordx2 %14, v225, %19 offset:1024 sc1\n\t" "s_add_u32 s42, s46, 0x28000\n\t" "s_addc_u32 s43, s47, 0\n\t"
                     "s_add_u32 m0, %20, 0x0\n\t" "s_nop 0\n\t" "global_load_lds_dwordx4 v232, s[42:43]\n\t" "s_add_u32 m0, %20, 0x400\n\t" "s_nop 0\n\t" "global_load_lds_dwordx4 v233, s[42:43]\n\t"
                     "s_add_u32 m0, %20, 0x800\n\t" "s_nop 0\n\t" "global_load_lds_dwordx4 v234, s[42:43]\n\t" "s_add_u32 m0, %20, 0xc00\n\t" "s_nop 0\n\t" "global_load_lds_dwordx4 v235, s[42:43]\n\t"
                     "s_add_u32 m0, %20, 0x1000\n\t" "s_nop 0\n\t" "global_load_lds_dwordx4 v236, s[42:43]\n\t" "s_add_u32 s42, s46, 0x29400\n\t" "s_addc_u32 s43, s47, 0\n\t" "s_add_u32 m0, %20, 0x1400\n\t"
                     "v_xor_b32 v224, 0x80, v232\n\t" "global_load_lds_dwordx4 v224, s[42:43]\n\t" "s_add_u32 m0, %20, 0x1800\n\t" "v_xor_b32 v224, 0x80, v233\n\t" "global_load_lds_dwordx4 v224, s[42:43]\n\t"
                     "s_add_u32 m0, %20, 0x1c00\n\t" "v_xor_b32 v224, 0x80, v234\n\t" "global_load_lds_dwordx4 v224, s[42:43]\n\t" "s_add_u32 m0, %20, 0x2000\n\t" "v_xor_b32 v224, 0x80, v235\n\t"
                     "global_load_lds_dwordx4 v224, s[42:43]\n\t" "s_add_u32 m0, %20, 0x2400\n\t" "v_xor_b32 v224, 0x80, v236\n\t" "global_load_lds_dwordx4 v224, s[42:43]\n\t" "s_add_u32 s42, %21, 0xc200\n\t"
                     "v_xor_b32 v237, 0x0, v229\n\t" "v_add_u32 v237, s42, v237\n\t" "v_xor_b32 v238, 0x40, v229\n\t" "v_add_u32 v238, s42, v238\n\t" "v_xor_b32 v239, 0x80, v229\n\t" "v_add_u32 v239, s42, v239\n\t"
                     "v_xor_b32 v226, 0xc0, v229\n\t" "v_add_u32 v226, s42, v226\n\t" "ds_read_b64_tr_b16 a[184:185], v237\n\t" "ds_read_b64_tr_b16 a[186:187], v238 offset:2560\n\t"
                     "ds_read_b64_tr_b16 a[208:209], v239\n\t" "ds_read_b64_tr_b16 a[210:211], v226 offset:2560\n\t" "ds_read_b64_tr_b16 a[232:233], v237 offset:256\n\t"
                     "ds_read_b64_tr_b16 a[234:235], v238 offset:2816\n\t" "ds_read_b64_tr_b16 v[96:97], v239 offset:256\n\t" "ds_read_b64_tr_b16 v[98:99], v226 offset:2816\n\t"
                     "ds_read_b64_tr_b16 v[120:121], v237 offset:512\n\t" "ds_read_b64_tr_b16 v[122:123], v238 offset:3072\n\t" "ds_read_b64_tr_b16 v[144:145], v239 offset:512\n\t"
                     "ds_read_b64_tr_b16 v[146:147], v226 offset:3072\n\t" "ds_read_b64_tr_b16 v[168:169], v237 offset:768\n\t" "ds_read_b64_tr_b16 v[170:171], v238 offset:3328\n\t"
                     "ds_read_b64_tr_b16 v[192:193], v239 offset:768\n\t" "ds_read_b64_tr_b16 v[194:195], v226 offset:3328\n\t" "ds_read_b64_tr_b16 v[216:217], v237 offset:1024\n\t"
                     "ds_read_b64_tr_b16 v[218:219], v238 offset:3584\n\t" "ds_read_b64_tr_b16 v[80:81], v239 offset:1024\n\t" "ds_read_b64_tr_b16 v[82:83], v226 offset:3584\n\t" "s_waitcnt vmcnt(10) lgkmcnt(0)\n\t"
                     "s_barrier\n\t" "s_add_u32 s42, s46, 0x32000\n\t" "s_addc_u32 s43, s47, 0\n\t" "s_add_u32 m0, %20, 0xc200\n\t" "s_nop 0\n\t" "global_load_lds_dwordx4 v232, s[42:43]\n\t" "s_add_u32 m0, %20, 0xc600\n\t"
                     "s_nop 0\n\t" "global_load_lds_dwordx4 v233, s[42:43]\n\t" "s_add_u32 m0, %20, 0xca00\n\t" "s_nop 0\n\t" "global_load_lds_dwordx4 v234, s[42:43]\n\t" "s_add_u32 m0, %20, 0xce00\n\t" "s_nop 0\n\t"
                     "global_load_lds_dwordx4 v235, s[42:43]\n\t" "s_add_u32 m0, %20, 0xd200\n\t" "s_nop 0\n\t" "global_load_lds_dwordx4 v236, s[42:43]\n\t" "s_add_u32 s42, s46, 0x33400\n\t" "s_addc_u32 s43, s47, 0\n\t"
                     "s_add_u32 m0, %20, 0xd600\n\t" "v_xor_b32 v224, 0x80, v232\n\t" "global_load_lds_dwordx4 v224, s[42:43]\n\t" "s_add_u32 m0, %20, 0xda00\n\t" "v_xor_b32 v224, 0x80, v233\n\t"
                     "global_load_lds_dwordx4 v224, s[42:43]\n\t" "s_add_u32 m0, %20, 0xde00\n\t" "v_xor_b32 v224, 0x80, v234\n\t" "global_load_lds_dwordx4 v224, s[42:43]\n\t" "s_add_u32 m0, %20, 0xe200\n\t"
                     "v_xor_b32 v224, 0x80, v235\n\t" "global_load_lds_dwordx4 v224, s[42:43]\n\t" "s_add_u32 m0, %20, 0xe600\n\t" "v_xor_b32 v224, 0x80, v236\n\t" "global_load_lds_dwordx4 v224, s[42:43]\n\t"
                     "s_add_u32 s42, %21, 0x18400\n\t" "v_xor_b32 v237, 0x0, v229\n\t" "v_add_u32 v237, s42, v237\n\t" "v_xor_b32 v238, 0x40, v229\n\t" "v_add_u32 v238, s42, v238\n\t" "v_xor_b32 v239, 0x80, v229\n\t"
                     "v_add_u32 v239, s42, v239\n\t" "v_xor_b32 v226, 0xc0, v229\n\t" "v_add_u32 v226, s42, v226\n\t" "ds_read_b64_tr_b16 a[188:189], v237\n\t" "ds_read_b64_tr_b16 a[190:191], v238 offset:2560\n\t"
                     "ds_read_b64_tr_b16 a[212:213], v239\n\t" "ds_read_b64_tr_b16 a[214:215], v226 offset:2560\n\t" "ds_read_b64_tr_b16 a[236:237], v237 offset:256\n\t"
                     "ds_read_b64_tr_b16 a[238:239], v238 offset:2816\n\t" "ds_read_b64_tr_b16 v[100:101], v239 offset:256\n\t" "ds_read_b64_tr_b16 v[102:103], v226 offset:2816\n\t"
                     "ds_read_b64_tr_b16 v[124:125], v237 offset:512\n\t" "ds_read_b64_tr_b16 v[126:127], v238 offset:3072\n\t" "ds_read_b64_tr_b16 v[148:149], v239 offset:512\n\t"
                     "ds_read_b64_tr_b16 v[150:151], v226 offset:3072\n\t" "ds_read_b64_tr_b16 v[172:173], v237 offset:768\n\t" "ds_read_b64_tr_b16 v[174:175], v238 offset:3328\n\t"
                     "ds_read_b64_tr_b16 v[196:197], v239 offset:768\n\t" "ds_read_b64_tr_b16 v[198:199], v226 offset:3328\n\t" "ds_read_b64_tr_b16 v[220:221], v237 offset:1024\n\t"
                     "ds_read_b64_tr_b16 v[222:223], v238 offset:3584\n\t" "ds_read_b64_tr_b16 v[84:85], v239 offset:1024\n\t" "ds_read_b64_tr_b16 v[86:87], v226 offset:3584\n\t" "s_waitcnt vmcnt(10) lgkmcnt(0)\n\t"
                     "s_barrier\n\t" "s_add_u32 s42, %21, 0x0\n\t" "v_xor_b32 v237, 0x0, v229\n\t" "v_add_u32 v237, s42, v237\n\t" "v_xor_b32 v238, 0x40, v229\n\t" "v_add_u32 v238, s42, v238\n\t"
                     "v_xor_b32 v239, 0x80, v229\n\t" "v_add_u32 v239, s42, v239\n\t" "v_xor_b32 v226, 0xc0, v229\n\t" "v_add_u32 v226, s42, v226\n\t" "ds_read_b64_tr_b16 a[192:193], v237\n\t"
                     "ds_read_b64_tr_b16 a[194:195], v238 offset:2560\n\t" "ds_read_b64_tr_b16 a[216:217], v239\n\t" "ds_read_b64_tr_b16 a[218:219], v226 offset:2560\n\t"
                     "ds_read_b64_tr_b16 a[240:241], v237 offset:256\n\t" "ds_read_b64_tr_b16 a[242:243], v238 offset:2816\n\t" "ds_read_b64_tr_b16 v[104:105], v239 offset:256\n\t"
                     "ds_read_b64_tr_b16 v[106:107], v226 offset:2816\n\t" "ds_read_b64_tr_b16 v[128:129], v237 offset:512\n\t" "ds_read_b64_tr_b16 v[130:131], v238 offset:3072\n\t"
                     "ds_read_b64_tr_b16 v[152:153], v239 offset:512\n\t" "ds_read_b64_tr_b16 v[154:155], v226 offset:3072\n\t" "ds_read_b64_tr_b16 v[176:177], v237 offset:768\n\t"
                     "ds_read_b64_tr_b16 v[178:179], v238 offset:3328\n\t" "ds_read_b64_tr_b16 v[200:201], v239 offset:768\n\t" "ds_read_b64_tr_b16 v[202:203], v226 offset:3328\n\t"
                     "ds_read_b64_tr_b16 v[64:65], v237 offset:1024\n\t" "ds_read_b64_tr_b16 v[66:67], v238 offset:3584\n\t" "ds_read_b64_tr_b16 v[88:89], v239 offset:1024\n\t"
                     "ds_read_b64_tr_b16 v[90:91], v226 offset:3584\n\t" "s_waitcnt vmcnt(0) lgkmcnt(0)\n\t" "s_barrier\n\t" "s_add_u32 s42, %21, 0xc200\n\t" "v_xor_b32 v237, 0x0, v229\n\t" "v_add_u32 v237, s42, v237\n\t"
                     "v_xor_b32 v238, 0x40, v229\n\t" "v_add_u32 v238, s42, v238\n\t" "v_xor_b32 v239, 0x80, v229\n\t" "v_add_u32 v239, s42, v239\n\t" "v_xor_b32 v226, 0xc0, v229\n\t" "v_add_u32 v226, s42, v226\n\t"
                     "ds_read_b64_tr_b16 a[196:197], v237\n\t" "ds_read_b64_tr_b16 a[198:199], v238 offset:2560\n\t" "ds_read_b64_tr_b16 a[220:221], v239\n\t" "ds_read_b64_tr_b16 a[222:223], v226 offset:2560\n\t"
                     "ds_read_b64_tr_b16 a[244:245], v237 offset:256\n\t" "ds_read_b64_tr_b16 a[246:247], v238 offset:2816\n\t" "ds_read_b64_tr_b16 v[108:109], v239 offset:256\n\t"
                     "ds_read_b64_tr_b16 v[110:111], v226 offset:2816\n\t" "ds_read_b64_tr_b16 v[132:133], v237 offset:512\n\t" "ds_read_b64_tr_b16 v[134:135], v238 offset:3072\n\t"
                     "ds_read_b64_tr_b16 v[156:157], v239 offset:512\n\t" "ds_read_b64_tr_b16 v[158:159], v226 offset:3072\n\t" "ds_read_b64_tr_b16 v[180:181], v237 offset:768\n\t"
                     "ds_read_b64_tr_b16 v[182:183], v238 offset:3328\n\t" "ds_read_b64_tr_b16 v[204:205], v239 offset:768\n\t" "ds_read_b64_tr_b16 v[206:207], v226 offset:3328\n\t"
                     "ds_read_b64_tr_b16 v[68:69], v237 offset:1024\n\t" "ds_read_b64_tr_b16 v[70:71], v238 offset:3584\n\t" "ds_read_b64_tr_b16 v[92:93], v239 offset:1024\n\t"
                     "ds_read_b64_tr_b16 v[94:95], v226 offset:3584\n\t" "s_waitcnt lgkmcnt(0)\n\t" "ds_write_b128 v230, v[0:3]\n\t" "ds_write_b128 v230, v[4:7] offset:1024\n\t"
                     "ds_write_b128 v230, v[8:11] offset:2048\n\t" "ds_write_b128 v230, v[12:15] offset:3072\n\t" "ds_write_b128 v230, v[16:19] offset:4096\n\t" "ds_write_b128 v230, v[20:23] offset:5120\n\t"
                     "ds_write_b128 v230, v[24:27] offset:6144\n\t" "ds_write_b128 v230, v[28:31] offset:7168\n\t" "ds_write_b128 v230, v[32:35] offset:8192\n\t" "ds_write_b128 v230, v[36:39] offset:9216\n\t"
                     "ds_write_b128 v230, v[40:43] offset:10240\n\t" "ds_write_b128 v230, v[44:47] offset:11264\n\t" "ds_write_b128 v230, v[48:51] offset:12288\n\t" "ds_write_b128 v230, v[52:55] offset:13312\n\t"
                     "ds_write_b128 v230, v[56:59] offset:14336\n\t" "ds_write_b128 v230, v[60:63] offset:15360\n\t" "ds_write_b128 v230, v[64:67] offset:16384\n\t" "ds_write_b128 v230, v[68:71] offset:17408\n\t"
                     "ds_write_b128 v230, v[72:75] offset:18432\n\t" "ds_write_b128 v230, v[76:79] offset:19456\n\t" "ds_write_b128 v230, v[80:83] offset:20480\n\t" "ds_write_b128 v230, v[84:87] offset:21504\n\t"
                     "ds_write_b128 v230, v[88:91] offset:22528\n\t" "ds_write_b128 v230, v[92:95] offset:23552\n\t"
                     : "=&{a[0:31]}"(wa[0]), "=&{a[32:63]}"(wa[1]), "=&{a[64:95]}"(wa[2]), "=&{a[96:127]}"(wa[3]), "=&{a[128:159]}"(wa[4]), "=&{a[160:191]}"(wa[5]), "=&{a[192:223]}"(wa[6]), "=&{a[224:255]}"(wa[7]), "=&{v[96:127]}"(wv[0]), "=&{v[128:159]}"(wv[1]), "=&{v[160:191]}"(wv[2]), "=&{v[192:223]}"(wv[3]), "=&v"(y.r), "=&v"(y.g), "=&v"(y.e), "+{v[224:239]}"(pa)
                     : "{s[44:45]}"(p.wv + h * DH * 2 + (size_t)(16 * w) * (HD * 2)), "{s[46:47]}"(p.wo + (size_t)(h * DH + 8 * w) * 1280),
                       "s"(lds + 2 * w * GS), "s"(p.ws + OFF_LAT), "s"(lds + w * 10240), "s"(lds), "s"((h & 4) * 16 * HD * 2)
                     : "v0", "v1", "v2", "v3", "v4", "v5", "v6", "v7", "v8", "v9", "v10", "v11", "v12", "v13", "v14", "v15", "v16", "v17", "v18", "v19", "v20", "v21", "v22", "v23", "v24", "v25", "v26", "v27", "v28", "v29", "v30", "v31", "v32", "v33", "v34", "v35", "v36", "v37", "v38", "v39", "v40", "v41", "v42", "v43", "v44", "v45", "v46", "v47", "v48", "v49", "v50", "v51", "v52", "v53", "v54", "v55", "v56", "v57", "v58", "v59", "v60", "v61", "v62", "v63", "v64", "v65", "v66", "v67", "v68", "v69", "v70", "v71", "v72", "v73", "v74", "v75", "v76", "v77", "v78", "v79", "v80", "v81", "v82", "v83", "v84", "v85", "v86", "v87", "v88", "v89", "v90", "v91", "v92", "v93", "v94", "v95", "s42", "s43", "scc", "memory");
        sd = sync_end(y, p.ws, phi, w == 0);
        if (w == 3) {
            u32 r;
            rdy_add(r, p.ws + OFF_ST);
            asm volatile("s_waitcnt vmcnt(0)" : "+v"(r) : : "memory");
            rdy_end(r, p.ws + OFF_ST, cfg.s1 >> 8 & 255);
        }
        barrier();
    }
    const int lane = lanex(), tid = 64 * w + lane;
    const int n = lane & 15, g = lane >> 4, qq = (lane >> 2) & 3, pp = lane & 3;
    char* const fl = p.ws + OFF_ST;
    const u32 iw = lds + 65536 + ((lane * 8) ^ (lane & 32));
    const u32 qr0 = lds + 65536 + (g >> 1) * 512 + pp * 128 + 64 * (g & 1) + 8 * qq + 32 * (pp >> 1);
    const u32 uo = (u32)((4 * w + (n >> 3)) * 4096 + h * 128 + (n & 7) * 16);
    u32x16 ad = u32x16{(u32)lane * 16 + 1024 + (h & 4) * 256, (lane & 2) ? 0xEEEEu : 0x4444u, lds + (u32)((w & 1) * 24576 + (w >> 1) * 99328 + lane * 16), iw + ((3 * w + (w & 1)) >> 1) * 512,
                       iw + (1 + 3 * (w >> 1)) * 512 + 4 * (w & 1), qr0, qr0 ^ 32, uo + (g & 1) * 8192 + (1 + (g >> 1)) * 65536, uo + (g & 1) * 8192 + (g >> 1) * 8, uo, (u32)lane * 16 + 1024 - (h & 4) * 256, uo, uo, uo, uo, uo};
    asm volatile("" : "+{v[224:239]}"(ad));
    const char* memp = p.ws + mslot(sd, 0);
    int budget = 30000;
    Clock clk; clk.init();
    for (int s = 0; s < NSEG; ++s) {
        int tries = 0;
        u64 bad, tp8, en = ~0ull;
        while (true) {
            const u32 tgt = s ? clk.sched(phi, 0, cfg.G, tries ? 2 : 0) : mt();
            char* up = p.ws + uslot(sd, s);
            asm volatile("ds_read_b128 v[64:67], v226\n\t" "ds_read_b128 v[68:71], v226 offset:1024\n\t" "ds_read_b128 v[72:75], v226 offset:2048\n\t" "ds_read_b128 v[76:79], v226 offset:3072\n\t"
                         "ds_read_b128 v[80:83], v226 offset:4096\n\t" "ds_read_b128 v[84:87], v226 offset:5120\n\t" "ds_read_b128 v[88:91], v226 offset:6144\n\t" "ds_read_b128 v[92:95], v226 offset:7168\n\t"
                         "v_mov_b64 v[40:41], 0\n\t" "v_mov_b64 v[42:43], 0\n\t" "v_mov_b64 v[44:45], 0\n\t" "v_mov_b64 v[46:47], 0\n\t" "v_mov_b64 v[48:49], 0\n\t" "v_mov_b64 v[50:51], 0\n\t" ".p2align 5\n\t" ".Lsp_u:\n\t"
                         "s_memtime vcc\n\t" "s_waitcnt lgkmcnt(0)\n\t" "s_sub_i32 s42, %3, vcc_lo\n\t" "s_cmp_gt_i32 s42, 80\n\t" "s_cbranch_scc1 .Lsp_u\n\t" "s_max_i32 s42, s42, 0\n\t" "s_sub_i32 s42, 92, s42\n\t"
                         "s_and_b32 s42, s42, -4\n\t" "s_getpc_b64 vcc\n\t" "s_add_u32 vcc_lo, vcc_lo, s42\n\t" "s_addc_u32 vcc_hi, vcc_hi, 0\n\t" "s_setpc_b64 vcc\n\t" "s_nop 0\n\t" "s_nop 0\n\t" "s_nop 0\n\t" "s_nop 0\n\t"
                         "s_nop 0\n\t" "s_nop 0\n\t" "s_nop 0\n\t" "s_nop 0\n\t" "s_nop 0\n\t" "s_nop 0\n\t" "s_nop 0\n\t" "s_nop 0\n\t" "s_nop 0\n\t" "s_nop 0\n\t" "s_nop 0\n\t" "s_nop 0\n\t" "s_nop 0\n\t" "s_nop 0\n\t"
                         "s_nop 0\n\t" "s_nop 0\n\t" "global_load_dwordx4 v[0:3], v224, %4 offset:-4096 sc1\n\t" "s_nop 10\n\t" "global_load_dwordx4 v[4:7], v224, %4 offset:-2048 sc1\n\t" "s_nop 15\n\t"
                         "global_load_dwordx4 v[8:11], v234, %4 offset:-3072 sc1\n\t" "s_nop 10\n\t" "global_load_dwordx4 v[12:15], v234, %4 offset:-1024 sc1\n\t" "s_nop 10\n\t" "global_load_dwordx4 v[16:19], v224, %4 sc1\n\t"
                         "s_nop 10\n\t" "global_load_dwordx4 v[20:23], v234, %4 offset:1024 sc1\n\t" "s_nop 10\n\t" "global_load_dwordx4 v[24:27], v224, %4 offset:2048 sc1\n\t" "s_nop 10\n\t"
                         "global_load_dwordx4 v[28:31], v234, %4 offset:3072 sc1\n\t" "s_nop 10\n\t" "global_load_dwordx4 v[32:35], v224, %5 offset:-4096 sc1\n\t" "s_nop 10\n\t"
                         "global_load_dwordx4 v[36:39], v234, %5 offset:-3072 sc1\n\t" "s_waitcnt vmcnt(9)\n\t" "v_smfmac_f32_16x16x64_bf16 v[40:43], v[0:3], a[0:7], v225\n\t"
                         "v_smfmac_f32_16x16x64_bf16 v[44:47], v[0:3], v[64:71], v225\n\t" "ds_read_b128 v[64:67], v226 offset:8192\n\t" "v_smfmac_f32_16x16x64_bf16 v[48:51], v[0:3], a[8:15], v225\n\t"
                         "ds_read_b128 v[68:71], v226 offset:9216\n\t" "v_cmp_eq_u32 vcc, -1, v0\n\t" "s_waitcnt vmcnt(8)\n\t" "v_smfmac_f32_16x16x64_bf16 v[40:43], v[4:7], a[16:23], v225\n\t" "v_mov_b64 v[0:1], 0\n\t"
                         "v_smfmac_f32_16x16x64_bf16 v[44:47], v[4:7], v[72:79], v225\n\t" "s_and_b64 vcc, vcc, %6\n\t" "s_cbranch_scc1 .Lsk_u\n\t" "ds_read_b128 v[72:75], v226 offset:10240\n\t" "v_mov_b64 v[2:3], 0\n\t"
                         "v_smfmac_f32_16x16x64_bf16 v[48:51], v[4:7], a[24:31], v225\n\t" "ds_read_b128 v[76:79], v226 offset:11264\n\t" "s_waitcnt vmcnt(7)\n\t"
                         "v_smfmac_f32_16x16x64_bf16 v[40:43], v[8:11], a[32:39], v225\n\t" "v_mov_b64 v[4:5], 0\n\t" "v_smfmac_f32_16x16x64_bf16 v[44:47], v[8:11], v[80:87], v225\n\t"
                         "ds_read_b128 v[80:83], v226 offset:12288\n\t" "v_mov_b64 v[6:7], 0\n\t" "v_smfmac_f32_16x16x64_bf16 v[48:51], v[8:11], a[40:47], v225\n\t" "ds_read_b128 v[84:87], v226 offset:13312\n\t"
                         "s_waitcnt vmcnt(6)\n\t" "v_smfmac_f32_16x16x64_bf16 v[40:43], v[12:15], a[48:55], v225\n\t" "v_mov_b64 v[8:9], 0\n\t" "v_smfmac_f32_16x16x64_bf16 v[44:47], v[12:15], v[88:95], v225\n\t"
                         "ds_read_b128 v[88:91], v226 offset:14336\n\t" "v_mov_b64 v[10:11], 0\n\t" "v_smfmac_f32_16x16x64_bf16 v[48:51], v[12:15], a[56:63], v225\n\t" "ds_read_b128 v[92:95], v226 offset:15360\n\t"
                         "s_waitcnt vmcnt(5)\n\t" "v_smfmac_f32_16x16x64_bf16 v[40:43], v[16:19], a[64:71], v225\n\t" "v_mov_b64 v[12:13], 0\n\t" "s_waitcnt lgkmcnt(6)\n\t"
                         "v_smfmac_f32_16x16x64_bf16 v[44:47], v[16:19], v[64:71], v225\n\t" "ds_read_b128 v[64:67], v226 offset:16384\n\t" "v_mov_b64 v[14:15], 0\n\t"
                         "v_smfmac_f32_16x16x64_bf16 v[48:51], v[16:19], a[72:79], v225\n\t" "ds_read_b128 v[68:71], v226 offset:17408\n\t" "s_waitcnt vmcnt(4)\n\t"
                         "v_smfmac_f32_16x16x64_bf16 v[40:43], v[20:23], a[80:87], v225\n\t" "v_mov_b64 v[16:17], 0\n\t" "s_waitcnt lgkmcnt(6)\n\t" "v_smfmac_f32_16x16x64_bf16 v[44:47], v[20:23], v[72:79], v225\n\t"
                         "ds_read_b128 v[72:75], v226 offset:18432\n\t" "v_mov_b64 v[18:19], 0\n\t" "v_smfmac_f32_16x16x64_bf16 v[48:51], v[20:23], a[88:95], v225\n\t" "ds_read_b128 v[76:79], v226 offset:19456\n\t"
                         "s_waitcnt vmcnt(3)\n\t" "v_smfmac_f32_16x16x64_bf16 v[40:43], v[24:27], a[96:103], v225\n\t" "v_mov_b64 v[20:21], 0\n\t" "s_waitcnt lgkmcnt(6)\n\t"
                         "v_smfmac_f32_16x16x64_bf16 v[44:47], v[24:27], v[80:87], v225\n\t" "ds_read_b128 v[80:83], v226 offset:20480\n\t" "v_mov_b64 v[22:23], 0\n\t"
                         "v_smfmac_f32_16x16x64_bf16 v[48:51], v[24:27], a[104:111], v225\n\t" "ds_read_b128 v[84:87], v226 offset:21504\n\t" "s_waitcnt vmcnt(2)\n\t"
                         "v_smfmac_f32_16x16x64_bf16 v[40:43], v[28:31], a[112:119], v225\n\t" "v_mov_b64 v[24:25], 0\n\t" "s_waitcnt lgkmcnt(6)\n\t" "v_smfmac_f32_16x16x64_bf16 v[44:47], v[28:31], v[88:95], v225\n\t"
                         "ds_read_b128 v[88:91], v226 offset:22528\n\t" "v_mov_b64 v[26:27], 0\n\t" "v_smfmac_f32_16x16x64_bf16 v[48:51], v[28:31], a[120:127], v225\n\t" "ds_read_b128 v[92:95], v226 offset:23552\n\t"
                         "s_waitcnt vmcnt(1)\n\t" "v_smfmac_f32_16x16x64_bf16 v[40:43], v[32:35], a[128:135], v225\n\t" "v_mov_b64 v[28:29], 0\n\t" "v_smfmac_f32_16x16x64_bf16 v[44:47], v[32:35], a[136:143], v225\n\t"
                         "v_mov_b64 v[30:31], 0\n\t" "v_smfmac_f32_16x16x64_bf16 v[48:51], v[32:35], a[144:151], v225\n\t" "s_waitcnt vmcnt(0)\n\t" "v_smfmac_f32_16x16x64_bf16 v[40:43], v[36:39], a[152:159], v225\n\t"
                         "v_mov_b64 v[32:33], 0\n\t" "v_smfmac_f32_16x16x64_bf16 v[44:47], v[36:39], a[160:167], v225\n\t" "v_mov_b64 v[34:35], 0\n\t" "v_smfmac_f32_16x16x64_bf16 v[48:51], v[36:39], a[168:175], v225\n\t"
                         "v_add_f32 v52, v40, v42\n\t" "v_add_f32 v53, v41, v43\n\t" "v_cmp_u_f32 vcc, v52, v53\n\t" "v_add_f32 v54, v44, v46\n\t" "v_add_f32 v55, v45, v47\n\t" "v_cvt_pk_bf16_f32 v58, v52, v53\n\t"
                         "v_pk_add_f32 v[56:57], v[48:49], v[50:51]\n\t" "v_cvt_pk_bf16_f32 v59, v54, v55\n\t" "v_cvt_pk_bf16_f32 v60, v56, v57\n\t" "ds_write_b64 v227, v[58:59]\n\t" "ds_write_b32 v228, v60\n\t"
                         "v_mov_b64 v[36:37], 0\n\t" "v_mov_b64 v[38:39], 0\n\t" "s_and_b64 vcc, vcc, %6\n\t" "v_mov_b32 v233, 0\n\t" "s_cbranch_scc1 .Lsk_u\n\t" "v_mov_b32 v233, 0\n\t" "v_mov_b32 v233, 0\n\t"
                         "v_mov_b32 v233, 0\n\t" "s_waitcnt lgkmcnt(0)\n\t" "s_barrier\n\t" "ds_read_b64_tr_b16 v[40:41], v229\n\t" "ds_read_b64_tr_b16 v[42:43], v230\n\t" "v_mov_b32 v233, 0\n\t"
                         "ds_read_b64_tr_b16 v[44:45], v229 offset:1024\n\t" "v_mov_b32 v233, 0\n\t" "ds_read_b64_tr_b16 v[46:47], v230 offset:1024\n\t" "v_mov_b32 v233, 0\n\t"
                         "ds_read_b64_tr_b16 v[48:49], v229 offset:2048\n\t" "v_mov_b32 v233, 0\n\t" "ds_read_b64_tr_b16 v[50:51], v230 offset:2048\n\t" "v_mov_b32 v233, 0\n\t" "s_waitcnt lgkmcnt(4)\n\t"
                         "v_smfmac_f32_16x16x64_bf16 v[0:3], v[40:43], a[176:183], v225\n\t" "v_smfmac_f32_16x16x64_bf16 v[4:7], v[40:43], a[200:207], v225\n\t" "s_waitcnt lgkmcnt(2)\n\t"
                         "v_smfmac_f32_16x16x64_bf16 v[0:3], v[44:47], a[184:191], v225\n\t" "v_smfmac_f32_16x16x64_bf16 v[4:7], v[44:47], a[208:215], v225\n\t" "s_waitcnt lgkmcnt(0)\n\t"
                         "v_smfmac_f32_16x16x64_bf16 v[0:3], v[48:51], a[192:199], v225\n\t" "v_smfmac_f32_16x16x64_bf16 v[4:7], v[48:51], a[216:223], v225\n\t"
                         "v_smfmac_f32_16x16x64_bf16 v[8:11], v[40:43], a[224:231], v225\n\t" "v_add_f32 v52, v0, v2\n\t" "v_add_f32 v53, v1, v3\n\t" "v_cvt_pk_bf16_f32 v54, v53, v52\n\t"
                         "v_smfmac_f32_16x16x64_bf16 v[8:11], v[44:47], a[232:239], v225\n\t" "v_add_f32 v52, v4, v6\n\t" "v_add_f32 v53, v5, v7\n\t" "v_cvt_pk_bf16_f32 v55, v53, v52\n\t" "s_nop 1\n\t"
                         "v_permlane16_swap_b32 v54, v55\n\t" "v_smfmac_f32_16x16x64_bf16 v[8:11], v[48:51], a[240:247], v225\n\t" "global_store_dwordx2 v232, v[54:55], %2 sc1\n\t" "s_add_u32 s40, s40, 0x10000\n\t"
                         "s_addc_u32 s41, s41, 0\n\t" "v_smfmac_f32_16x16x64_bf16 v[12:15], v[40:43], a[248:255], v225\n\t" "v_smfmac_f32_16x16x64_bf16 v[12:15], v[44:47], v[96:103], v225\n\t" "v_add_f32 v52, v8, v10\n\t"
                         "v_smfmac_f32_16x16x64_bf16 v[12:15], v[48:51], v[104:111], v225\n\t" "v_add_f32 v53, v9, v11\n\t" "v_smfmac_f32_16x16x64_bf16 v[16:19], v[40:43], v[112:119], v225\n\t"
                         "v_cvt_pk_bf16_f32 v56, v53, v52\n\t" "v_smfmac_f32_16x16x64_bf16 v[16:19], v[44:47], v[120:127], v225\n\t" "v_add_f32 v52, v12, v14\n\t" "v_add_f32 v53, v13, v15\n\t"
                         "v_cvt_pk_bf16_f32 v57, v53, v52\n\t" "s_nop 1\n\t" "v_permlane16_swap_b32 v56, v57\n\t" "v_smfmac_f32_16x16x64_bf16 v[16:19], v[48:51], v[128:135], v225\n\t"
                         "global_store_dwordx2 v232, v[56:57], %2 sc1\n\t" "s_add_u32 s40, s40, 0x10000\n\t" "s_addc_u32 s41, s41, 0\n\t" "v_smfmac_f32_16x16x64_bf16 v[20:23], v[40:43], v[136:143], v225\n\t"
                         "v_smfmac_f32_16x16x64_bf16 v[20:23], v[44:47], v[144:151], v225\n\t" "v_add_f32 v52, v16, v18\n\t" "v_smfmac_f32_16x16x64_bf16 v[20:23], v[48:51], v[152:159], v225\n\t" "v_add_f32 v53, v17, v19\n\t"
                         "v_smfmac_f32_16x16x64_bf16 v[24:27], v[40:43], v[160:167], v225\n\t" "v_cvt_pk_bf16_f32 v58, v53, v52\n\t" "v_smfmac_f32_16x16x64_bf16 v[24:27], v[44:47], v[168:175], v225\n\t"
                         "v_add_f32 v52, v20, v22\n\t" "v_add_f32 v53, v21, v23\n\t" "v_smfmac_f32_16x16x64_bf16 v[24:27], v[48:51], v[176:183], v225\n\t" "v_cvt_pk_bf16_f32 v59, v53, v52\n\t"
                         "v_smfmac_f32_16x16x64_bf16 v[28:31], v[40:43], v[184:191], v225\n\t" "s_nop 0\n\t" "v_permlane16_swap_b32 v58, v59\n\t" "v_smfmac_f32_16x16x64_bf16 v[28:31], v[44:47], v[192:199], v225\n\t"
                         "global_store_dwordx2 v232, v[58:59], %2 sc1\n\t" "v_add_f32 v52, v24, v26\n\t" "v_smfmac_f32_16x16x64_bf16 v[28:31], v[48:51], v[200:207], v225\n\t" "v_add_f32 v53, v25, v27\n\t"
                         "v_smfmac_f32_16x16x64_bf16 v[32:35], v[40:43], v[208:215], v225\n\t" "v_cvt_pk_bf16_f32 v60, v53, v52\n\t" "v_smfmac_f32_16x16x64_bf16 v[32:35], v[44:47], v[216:223], v225\n\t"
                         "v_add_f32 v52, v28, v30\n\t" "v_smfmac_f32_16x16x64_bf16 v[32:35], v[48:51], v[64:71], v225\n\t" "v_add_f32 v53, v29, v31\n\t" "v_smfmac_f32_16x16x64_bf16 v[36:39], v[40:43], v[72:79], v225\n\t"
                         "v_cvt_pk_bf16_f32 v61, v53, v52\n\t" "v_smfmac_f32_16x16x64_bf16 v[36:39], v[44:47], v[80:87], v225\n\t" "v_add_f32 v52, v32, v34\n\t"
                         "v_smfmac_f32_16x16x64_bf16 v[36:39], v[48:51], v[88:95], v225\n\t" "v_add_f32 v53, v33, v35\n\t" "v_cvt_pk_bf16_f32 v62, v53, v52\n\t" "v_mov_b32 v233, 0\n\t" "v_mov_b32 v233, 0\n\t"
                         "v_mov_b32 v233, 0\n\t" "v_mov_b32 v233, 0\n\t" "v_mov_b32 v233, 0\n\t" "v_pk_add_f32 v[52:53], v[36:37], v[38:39]\n\t" "v_cvt_pk_bf16_f32 v63, v53, v52\n\t" "v_permlane32_swap_b32 v60, v62\n\t"
                         "s_nop 0\n\t" "v_permlane32_swap_b32 v61, v63\n\t" "s_nop 1\n\t" "v_permlane16_swap_b32 v60, v61\n\t" "v_permlane16_swap_b32 v62, v63\n\t" "global_store_dwordx4 v231, v[60:63], %2 sc1\n\t"
                         ".Lsk_u:\n\t" "s_memrealtime %1\n\t" "s_mov_b64 %0, vcc\n\t" "s_waitcnt lgkmcnt(0)\n\t"
                         : "=&s"(bad), "=&s"(tp8), "+{s[40:41]}"(up)
                         : "s"(uni((int)tgt)), "s"(memp - 1024), "s"(memp + 15360), "s"(en), "{v[224:239]}"(ad),
                           "{a[0:31]}"(wa[0]), "{a[32:63]}"(wa[1]), "{a[64:95]}"(wa[2]), "{a[96:127]}"(wa[3]), "{a[128:159]}"(wa[4]), "{a[160:191]}"(wa[5]), "{a[192:223]}"(wa[6]), "{a[224:255]}"(wa[7]), "{v[96:127]}"(wv[0]), "{v[128:159]}"(wv[1]), "{v[160:191]}"(wv[2]), "{v[192:223]}"(wv[3])
                         : "v0", "v1", "v2", "v3", "v4", "v5", "v6", "v7", "v8", "v9", "v10", "v11", "v12", "v13", "v14", "v15", "v16", "v17", "v18", "v19", "v20", "v21", "v22", "v23", "v24", "v25", "v26", "v27", "v28", "v29", "v30", "v31", "v32", "v33", "v34", "v35", "v36", "v37", "v38", "v39", "v40", "v41", "v42", "v43", "v44", "v45", "v46", "v47", "v48", "v49", "v50", "v51", "v52", "v53", "v54", "v55", "v56", "v57", "v58", "v59", "v60", "v61", "v62", "v63", "v64", "v65", "v66", "v67", "v68", "v69", "v70", "v71", "v72", "v73", "v74", "v75", "v76", "v77", "v78", "v79", "v80", "v81", "v82", "v83", "v84", "v85", "v86", "v87", "v88", "v89", "v90", "v91", "v92", "v93", "v94", "v95", "vcc", "scc", "s42", "memory");
            if (__builtin_expect(bad == 0, 1)) break;
            waitvm<0>();
            if (--budget < 0 || ((++tries & 15) == 0 && !unwritten(memp, lanex() * 16))) en = 0;
            phi += (u32)cfg.G;
        }
        clk.calib();
        const u32 tt = sched_ld0(s);
        if (s >= NSEG - 2) waitvm<0>();
        barrier();
        if (s >= NSEG - 2 && w == 3) flag_add(fl, F_PROG + 8 * s + (h & 7), 1);
        if (s >= NSEG - 2 && w == 2) flag_add(fl, F_UD + 2 * (s & 1) + (h & 1), 1);
        if (s == 0) {
#pragma unroll
            for (int i = UPF; i < 8; ++i) {
                const int l = 64 * min(i * 4 + w, 29) + lanex();
                pfl(p.wq + h * 384, (u32)((l / 3) * 12288 + (l % 3) * 128), lds);
            }
        }
        if (s == 0) { until(phi + UNAP); phi = go_wg(fl, h, w, lds + SL_PF - 16) - (tt >> 16); edge(clk.r1, clk.m1); clk.calib(); }
        follow(phi, (int)(tt >> 16) + 4 + cfg.T3 - VISS, (u32)tp8, cfg.G);
        phi += tt & 0xffffu;
        memp = p.ws + mslot(sd, s + 1);
    }
    asm volatile(".Lkm_lu:\n\ts_waitcnt vmcnt(0)" : : : "memory");
}

#define VALL "{v[0:3]}"(o), "{v[16:47]}"(rv[0]), "{v[48:79]}"(rv[1]), "{v[80:111]}"(rv[2]), "{v[112:143]}"(rv[3]), "{v[144:175]}"(rv[4]), "{v[176:207]}"(rv[5]), "{v[208:239]}"(rv[6]), "{a[0:31]}"(ra[0]), "{a[32:63]}"(ra[1]), "{a[64:95]}"(ra[2]), "{a[96:127]}"(ra[3])
DI void value_role(const Ptrs& p, const ChainCfg cfg, int vb, int sd, u32 phi, u32 lds, int w) {
    const int lane = lanex(), tid = 64 * w + lane, n = lane & 15, g = lane >> 4;
    const int v = vb;
    const u32 moff = lane * 16;
    char* const fl = p.ws + OFF_ST;
    char* const ub = p.ws + uchunk(v);
    const int xh = (vb >> 2) & 1;
    if (w == 3) {
        const u32x4 ff = u32x4{~0u, ~0u, ~0u, ~0u};
        if (SPL) barrier();
#pragma nounroll
        for (int s = 0; s < NSEG - 2; ++s) {
            dly(s == 1 ? HD1 : cfg.cR3 >> 8 & 255);
            char* const mk = ub + uslot(sd, s + 2);
#pragma unroll
            for (int i = 0; i < 8; ++i) { gst16(mk + i * 1024, moff, ff); gap<HQ>(-1); }
            if (vb < 32 && s) {
                gst16(p.ws + (size_t)(s + 2) * HO_STRIDE + vb * 2048, moff, ff);
                gst16(p.ws + (size_t)(s + 2) * HO_STRIDE + vb * 2048 + 1024, moff, ff);
            }
            if (s == 0) {
                u32 r;
                flag_wait(fl, F_VD + (vb & 3) + (v < 20 ? 0 : 4), 32);
                rdy_add(r, fl);
                asm volatile("s_waitcnt vmcnt(0)" : "+v"(r) : : "memory");
                rdy_end(r, fl, cfg.s1 >> 16);
            }
            waitvm<0>();
            if (SPL && s) barrier();
            barrier();
        }
        return;
    }
    if (w) {
        const u32 dof = (u32)((n >> 3) * 16384 + g * 128 + (n & 7) * 16);
        const char* dp = p.ws + OFF_D + (size_t)v * 32768 - 4096 + w * 8192;
        if (SPL) barrier();
        flag_wait(fl, F_VD + (vb & 3) + (v < 20 ? 0 : 4), 32);
#pragma nounroll
        for (int s = 0; s < NSEG - 1; ++s) {
            u32x4 df[16];
            u32 dfl;
            const int on = uni(1 - s);
            sfor<0, SPL>([&](auto I) { gld16c<I.v * 512 - 4096>(df[I.v], dp, dof); gap<HP0>(on); });
            if (SPL) asm volatile("s_cmp_ge_i32 %0, 0\n\ts_cbranch_scc1 .Lhb\n\ts_barrier\n.Lhb:" : : "s"(on) : "memory", "scc");
            dly(s == 1 ? HD1 : cfg.cR3 & 255);
            sfor<SPL, 16>([&](auto I) { gld16c<I.v * 512 - 4096>(df[I.v], dp, dof); gap<HP>(on); });
            dp += D_STRIDE;
            flag_ldm(dfl, fl, F_VD + pblk(s + 1) * 8 + (vb & RM), pblk(s + 1) != pblk(s));
            const u32 la = lds + moff + (s & 1) * 32768 + (w - 1) * 16384;
            sfor<0, 8>([&](auto I) {
                constexpr int i = I.v;
                waitv<15 - 2 * i>(df[2 * i]); waitv<15 - 2 * i>(df[2 * i + 1]);
                const u32x4 x = df[2 * i], y = df[2 * i + 1];
                const u32 lb = i & 2 ? la - xh * 4096 : la + xh * 4096;
                *LDSP(u32x4, lb + 2048 * i) = u32x4{x[0], y[0], x[2], y[2]};
                *LDSP(u32x4, lb + 2048 * i + 1024) = u32x4{x[1], y[1], x[3], y[3]};
                PIN();
            });
            waitvm<0>();
            asm volatile("" : "+v"(dfl));
            if (uni((int)dfl) < 31) {
                dly(FRD);
                flag_ld(dfl, fl, F_VD + pblk(s + 1) * 8 + (vb & RM));
                asm volatile("s_waitcnt vmcnt(0)" : "+v"(dfl) : : "memory");
            }
            barrier();
            if (uni((int)dfl) < 31) flag_wait(fl, F_VD + pblk(s + 1) * 8 + (vb & RM), 32);
        }
        return;
    }
    u32x32 rv[7], ra[4];
    u32x4 o;
    asm volatile("v_accvgpr_write_b32 a64, 0\n\t" "v_accvgpr_write_b32 a65, 0\n\t" "v_accvgpr_write_b32 a66, 0\n\t" "v_accvgpr_write_b32 a67, 0\n\t" "v_accvgpr_write_b32 a68, 0\n\t" "v_accvgpr_write_b32 a69, 0\n\t"
                 "v_accvgpr_write_b32 a70, 0\n\t" "v_accvgpr_write_b32 a71, 0\n\t" "v_accvgpr_write_b32 a72, 0\n\t" "v_accvgpr_write_b32 a73, 0\n\t" "v_accvgpr_write_b32 a74, 0\n\t" "v_accvgpr_write_b32 a75, 0\n\t"
                 "v_accvgpr_write_b32 a76, 0\n\t" "v_accvgpr_write_b32 a77, 0\n\t" "v_accvgpr_write_b32 a78, 0\n\t" "v_accvgpr_write_b32 a79, 0\n\t" "v_mov_b64 v[112:113], -1\n\t" "v_mov_b64 v[120:121], -1\n\t"
                 "v_mov_b64 v[128:129], -1\n\t" "v_mov_b64 v[136:137], -1\n\t" "v_mov_b64 v[144:145], -1\n\t" "v_mov_b64 v[152:153], -1\n\t" "v_mov_b64 v[160:161], -1\n\t" "v_mov_b64 v[168:169], -1\n\t"
                 "v_mov_b64 v[176:177], -1\n\t" "v_mov_b64 v[184:185], -1\n\t" "v_mov_b64 v[192:193], -1\n\t" "v_mov_b64 v[200:201], -1\n\t" "v_mov_b64 v[208:209], -1\n\t" "v_mov_b64 v[216:217], -1\n\t"
                 "v_mov_b64 v[224:225], -1\n\t" "v_mov_b64 v[232:233], -1\n\t"
                 : "={v[0:3]}"(o), "={v[16:47]}"(rv[0]), "={v[48:79]}"(rv[1]), "={v[80:111]}"(rv[2]), "={v[112:143]}"(rv[3]), "={v[144:175]}"(rv[4]), "={v[176:207]}"(rv[5]), "={v[208:239]}"(rv[6]), "={a[0:31]}"(ra[0]), "={a[32:63]}"(ra[1]), "={a[64:95]}"(ra[2]), "={a[96:127]}"(ra[3]));
    const u32 ho = (u32)(min(lane, 39) * 256 + (lane >> 5) * 8192);
    const u32 pof = (u32)((n & 4) * 128 + (n >> 3) * 256 + g * 64 + (n & 3) * 16 + 2048);
    const u32 uof = (u32)((n >> 3) * 4096 + g * 128 + (n & 7) * 16 + 512);
    const u32 cof = (u32)((g >> 1) * 512 + (n >> 2) * 64 + (n & 1) * 16), pmf = (u32)((g >> 1) * 1024 + (lane & 31) * 16);
    u32x8 ad = u32x8{uof + xh * 512, pof + xh * 2048, cof + xh * 256, pmf + xh * 512,
                     (lane & 2) ? 0xEEEEu : 0x4444u, (lane & 4) ? 0xEEEEu : 0x4444u, (u32)(v * 256 + (v >> 5) * 8192 + g * 64 + (n >> 3) * 32 + (n & 1) * 16 + (n & 6) * 2),
                     (lane & 1) ? 0x07060302u : 0x01000504u};
    u32 pob = pof - xh * 2048, uob = uof - xh * 512, pmo = pmf + (xh ^ 1) * 512, cob = cof + (xh ^ 1) * 256;
    asm volatile("" : "+{v[4:11]}"(ad), "+v"(pob), "+v"(uob), "+v"(pmo), "+v"(cob));
    auto zero = [&]() {
        asm volatile("v_mov_b64 v[0:1], 0\n\t" "v_mov_b64 v[2:3], 0\n\t" "v_mov_b64 v[48:49], 0\n\t" "v_mov_b64 v[50:51], 0\n\t" "v_mov_b64 v[52:53], 0\n\t" "v_mov_b64 v[54:55], 0\n\t" "v_mov_b64 v[56:57], 0\n\t"
                     "v_mov_b64 v[58:59], 0\n\t" "v_mov_b64 v[60:61], 0\n\t" "v_mov_b64 v[62:63], 0\n\t" "v_mov_b64 v[64:65], 0\n\t" "v_mov_b64 v[66:67], 0\n\t" "v_mov_b64 v[68:69], 0\n\t" "v_mov_b64 v[70:71], 0\n\t"
                     "v_mov_b64 v[72:73], 0\n\t" "v_mov_b64 v[74:75], 0\n\t" "v_mov_b64 v[76:77], 0\n\t" "v_mov_b64 v[78:79], 0\n\t" "v_mov_b64 v[80:81], 0\n\t" "v_mov_b64 v[82:83], 0\n\t" "v_mov_b64 v[84:85], 0\n\t"
                     "v_mov_b64 v[86:87], 0\n\t" "v_mov_b64 v[88:89], 0\n\t" "v_mov_b64 v[90:91], 0\n\t" "v_mov_b64 v[92:93], 0\n\t" "v_mov_b64 v[94:95], 0\n\t" "v_mov_b64 v[96:97], 0\n\t" "v_mov_b64 v[98:99], 0\n\t"
                     "v_mov_b64 v[100:101], 0\n\t" "v_mov_b64 v[102:103], 0\n\t" "v_mov_b64 v[104:105], 0\n\t" "v_mov_b64 v[106:107], 0\n\t" "v_mov_b64 v[108:109], 0\n\t" "v_mov_b64 v[110:111], 0\n\t"
                     : : VALL : "memory");
    };
    const char* memp = p.ws + OFF_XF + 4096;
    char* up = ub + OFF_XF;
    int budget = 30000;
    Clock clk; clk.init();
    u32 tt = 0;
    for (int s = -1; s < NSEG - 1; ++s) {
        const char* const mnx = p.ws + mslot(sd, s + 1);
        bool all = false;
        int tries = 0;
        u64 tp8;
        while (true) {
            const u32 tgt = s >= 0 ? clk.sched(phi, (int)(tt >> 16), cfg.G, tries ? 2 : 1) : mt();
            u64 ok;
            asm volatile("s_cmp_lg_u32 %11, 0\n\t" "s_cbranch_scc0 .Lsp_v0\n\t" ".p2align 5\n\t" ".Lsp_v3:\n\t" "s_memtime vcc\n\t" "s_waitcnt lgkmcnt(0)\n\t" "s_sub_i32 s42, %10, vcc_lo\n\t" "s_cmp_gt_i32 s42, 80\n\t"
                         "s_cbranch_scc1 .Lsp_v3\n\t" "s_max_i32 s42, s42, 0\n\t" "s_sub_i32 s42, 92, s42\n\t" "s_and_b32 s42, s42, -4\n\t" "s_getpc_b64 vcc\n\t" "s_add_u32 vcc_lo, vcc_lo, s42\n\t"
                         "s_addc_u32 vcc_hi, vcc_hi, 0\n\t" "s_setpc_b64 vcc\n\t" "s_nop 0\n\t" "s_nop 0\n\t" "s_nop 0\n\t" "s_nop 0\n\t" "s_nop 0\n\t" "s_nop 0\n\t" "s_nop 0\n\t" "s_nop 0\n\t" "s_nop 0\n\t" "s_nop 0\n\t"
                         "s_nop 0\n\t" "s_nop 0\n\t" "s_nop 0\n\t" "s_nop 0\n\t" "s_nop 0\n\t" "s_nop 0\n\t" "s_nop 0\n\t" "s_nop 0\n\t" "s_nop 0\n\t" "s_nop 0\n\t"
                         "global_load_dwordx4 a[0:3], v5, %7 offset:-4096 sc1\n\t" "global_load_dwordx4 a[4:7], v5, %7 offset:-3072 sc1\n\t" "global_load_dwordx4 a[8:11], v5, %7 sc1\n\t"
                         "global_load_dwordx4 a[12:15], v5, %7 offset:1024 sc1\n\t" "global_load_dwordx4 a[16:19], v5, %8 offset:-4096 sc1\n\t" "global_load_dwordx4 a[20:23], v5, %8 offset:-3072 sc1\n\t"
                         "global_load_dwordx4 a[24:27], v5, %8 sc1\n\t" "global_load_dwordx4 a[28:31], v5, %8 offset:1024 sc1\n\t" "s_nop 14\n\t" "global_load_dwordx4 a[32:35], %12, %7 offset:-2048 sc1\n\t"
                         "global_load_dwordx4 a[36:39], %12, %7 offset:-1024 sc1\n\t" "global_load_dwordx4 a[40:43], %12, %7 offset:2048 sc1\n\t" "global_load_dwordx4 a[44:47], %12, %7 offset:3072 sc1\n\t"
                         "global_load_dwordx4 a[48:51], %12, %8 offset:-2048 sc1\n\t" "global_load_dwordx4 a[52:55], %12, %8 offset:-1024 sc1\n\t" "global_load_dwordx4 a[56:59], %12, %8 offset:2048 sc1\n\t"
                         "global_load_dwordx4 a[60:63], %12, %8 offset:3072 sc1\n\t" "s_nop 15\n\t" "s_nop 15\n\t" "global_load_dwordx4 a[80:83], v7, %6 sc1\n\t"
                         "global_load_dwordx4 a[84:87], v7, %6 offset:2048 sc1\n\t" "s_mov_b64 exec, %9\n\t" "global_load_dwordx4 a[64:67], v6, %6 offset:-2048 sc1\n\t"
                         "global_load_dwordx4 a[68:71], v6, %6 offset:-1024 sc1\n\t" "s_mov_b64 exec, -1\n\t" "s_nop 15\n\t" "s_nop 8\n\t" "global_load_dwordx4 a[88:91], %14, %6 sc1\n\t"
                         "global_load_dwordx4 a[92:95], %14, %6 offset:2048 sc1\n\t" "s_mov_b64 exec, %9\n\t" "global_load_dwordx4 a[72:75], %15, %6 offset:-2048 sc1\n\t"
                         "global_load_dwordx4 a[76:79], %15, %6 offset:-1024 sc1\n\t" "s_mov_b64 exec, -1\n\t" "s_mov_b32 s42, %11\n\t" ".Lsp_v5:\n\t" "s_sub_u32 s42, s42, 1\n\t" "s_cmp_gt_i32 s42, 0\n\t"
                         "s_cbranch_scc1 .Lsp_v5\n\t" "global_load_dwordx4 a[96:99], v4, %3 offset:-512 sc1\n\t" "global_load_dwordx4 a[100:103], v4, %3 offset:512 sc1\n\t"
                         "global_load_dwordx4 a[104:107], v4, %3 offset:1536 sc1\n\t" "global_load_dwordx4 a[108:111], v4, %3 offset:2560 sc1\n\t" "global_load_dwordx4 a[112:115], %13, %3 sc1\n\t"
                         "global_load_dwordx4 a[116:119], %13, %3 offset:1024 sc1\n\t" "global_load_dwordx4 a[120:123], %13, %3 offset:2048 sc1\n\t" "global_load_dwordx4 a[124:127], %13, %3 offset:3072 sc1\n\t"
                         "s_waitcnt vmcnt(31)\n\t" "v_smfmac_f32_16x16x64_bf16 v[48:51], a[0:3], v[112:119], v9\n\t" "s_waitcnt vmcnt(30)\n\t" "v_smfmac_f32_16x16x64_bf16 v[52:55], a[4:7], v[120:127], v9\n\t"
                         "s_waitcnt vmcnt(29)\n\t" "v_smfmac_f32_16x16x64_bf16 v[56:59], a[8:11], v[144:151], v9\n\t" "s_waitcnt vmcnt(28)\n\t" "v_cvt_pk_bf16_f32 v16, v48, v49\n\t" "v_cvt_pk_bf16_f32 v17, v50, v51\n\t"
                         "v_smfmac_f32_16x16x64_bf16 v[60:63], a[12:15], v[152:159], v9\n\t" "s_waitcnt vmcnt(27)\n\t" "v_cvt_pk_bf16_f32 v18, v52, v53\n\t" "v_cvt_pk_bf16_f32 v19, v54, v55\n\t"
                         "v_smfmac_f32_16x16x64_bf16 v[64:67], a[16:19], v[176:183], v9\n\t" "s_waitcnt vmcnt(26)\n\t" "v_cvt_pk_bf16_f32 v20, v56, v57\n\t" "v_cvt_pk_bf16_f32 v21, v58, v59\n\t"
                         "v_smfmac_f32_16x16x64_bf16 v[68:71], a[20:23], v[184:191], v9\n\t" "s_waitcnt vmcnt(25)\n\t" "v_cvt_pk_bf16_f32 v22, v60, v61\n\t" "v_cvt_pk_bf16_f32 v23, v62, v63\n\t"
                         "v_smfmac_f32_16x16x64_bf16 v[72:75], a[24:27], v[208:215], v9\n\t" "s_waitcnt vmcnt(24)\n\t" "v_cvt_pk_bf16_f32 v24, v64, v65\n\t" "v_cvt_pk_bf16_f32 v25, v66, v67\n\t"
                         "v_smfmac_f32_16x16x64_bf16 v[76:79], a[28:31], v[216:223], v9\n\t" "s_waitcnt vmcnt(23)\n\t" "v_cvt_pk_bf16_f32 v26, v68, v69\n\t" "v_cvt_pk_bf16_f32 v27, v70, v71\n\t"
                         "v_smfmac_f32_16x16x64_bf16 v[80:83], a[32:35], v[128:135], v9\n\t" "s_waitcnt vmcnt(22)\n\t" "v_cvt_pk_bf16_f32 v28, v72, v73\n\t" "v_cvt_pk_bf16_f32 v29, v74, v75\n\t"
                         "v_smfmac_f32_16x16x64_bf16 v[84:87], a[36:39], v[136:143], v9\n\t" "s_waitcnt vmcnt(21)\n\t" "v_cvt_pk_bf16_f32 v30, v76, v77\n\t" "v_cvt_pk_bf16_f32 v31, v78, v79\n\t"
                         "v_smfmac_f32_16x16x64_bf16 v[88:91], a[40:43], v[160:167], v9\n\t" "s_waitcnt vmcnt(20)\n\t" "v_cvt_pk_bf16_f32 v32, v80, v81\n\t" "v_cvt_pk_bf16_f32 v33, v82, v83\n\t"
                         "v_smfmac_f32_16x16x64_bf16 v[92:95], a[44:47], v[168:175], v9\n\t" "s_waitcnt vmcnt(19)\n\t" "v_cvt_pk_bf16_f32 v34, v84, v85\n\t" "v_cvt_pk_bf16_f32 v35, v86, v87\n\t"
                         "v_smfmac_f32_16x16x64_bf16 v[96:99], a[48:51], v[192:199], v9\n\t" "s_waitcnt vmcnt(18)\n\t" "v_cvt_pk_bf16_f32 v36, v88, v89\n\t" "v_cvt_pk_bf16_f32 v37, v90, v91\n\t"
                         "v_smfmac_f32_16x16x64_bf16 v[100:103], a[52:55], v[200:207], v9\n\t" "s_waitcnt vmcnt(17)\n\t" "v_cvt_pk_bf16_f32 v38, v92, v93\n\t" "v_cvt_pk_bf16_f32 v39, v94, v95\n\t"
                         "v_smfmac_f32_16x16x64_bf16 v[104:107], a[56:59], v[224:231], v9\n\t" "s_waitcnt vmcnt(16)\n\t" "v_cvt_pk_bf16_f32 v40, v96, v97\n\t" "v_cvt_pk_bf16_f32 v41, v98, v99\n\t"
                         "v_smfmac_f32_16x16x64_bf16 v[108:111], a[60:63], v[232:239], v9\n\t" "s_waitcnt vmcnt(13)\n\t" "v_cvt_pk_bf16_f32 v42, v100, v101\n\t" "v_cvt_pk_bf16_f32 v43, v102, v103\n\t"
                         "v_smfmac_f32_16x16x64_bf16 v[0:3], a[64:67], v[16:23], v8\n\t" "s_waitcnt vmcnt(12)\n\t" "v_cvt_pk_bf16_f32 v44, v104, v105\n\t" "v_cvt_pk_bf16_f32 v45, v106, v107\n\t"
                         "v_smfmac_f32_16x16x64_bf16 v[0:3], a[68:71], v[24:31], v8\n\t" "s_waitcnt vmcnt(9)\n\t" "v_cvt_pk_bf16_f32 v46, v108, v109\n\t" "v_cvt_pk_bf16_f32 v47, v110, v111\n\t"
                         "v_smfmac_f32_16x16x64_bf16 v[0:3], a[72:75], v[32:39], v8\n\t" "s_waitcnt vmcnt(8)\n\t" "v_smfmac_f32_16x16x64_bf16 v[0:3], a[76:79], v[40:47], v8\n\t" "s_waitcnt vmcnt(6)\n\t"
                         "v_smfmac_f32_16x16x64_bf16 v[0:3], a[80:83], a[96:103], v8\n\t" "s_waitcnt vmcnt(4)\n\t" "v_smfmac_f32_16x16x64_bf16 v[0:3], a[84:87], a[104:111], v8\n\t" "s_waitcnt vmcnt(2)\n\t"
                         "v_smfmac_f32_16x16x64_bf16 v[0:3], a[88:91], a[112:119], v8\n\t" "s_waitcnt vmcnt(0)\n\t" "v_smfmac_f32_16x16x64_bf16 v[0:3], a[92:95], a[120:127], v8\n\t" "s_branch .Lv_end\n\t"
                         ".p2align 5\n\t" "s_nop 0\n\t" "s_nop 0\n\t" "s_nop 0\n\t" "s_nop 0\n\t" ".Lsp_v0:\n\t" "s_memtime vcc\n\t" "s_waitcnt lgkmcnt(0)\n\t" "s_sub_i32 s42, %10, vcc_lo\n\t" "s_cmp_gt_i32 s42, 80\n\t"
                         "s_cbranch_scc1 .Lsp_v0\n\t" "s_max_i32 s42, s42, 0\n\t" "s_sub_i32 s42, 92, s42\n\t" "s_and_b32 s42, s42, -4\n\t" "s_getpc_b64 vcc\n\t" "s_add_u32 vcc_lo, vcc_lo, s42\n\t"
                         "s_addc_u32 vcc_hi, vcc_hi, 0\n\t" "s_setpc_b64 vcc\n\t" "s_nop 0\n\t" "s_nop 0\n\t" "s_nop 0\n\t" "s_nop 0\n\t" "s_nop 0\n\t" "s_nop 0\n\t" "s_nop 0\n\t" "s_nop 0\n\t" "s_nop 0\n\t" "s_nop 0\n\t"
                         "s_nop 0\n\t" "s_nop 0\n\t" "s_nop 0\n\t" "s_nop 0\n\t" "s_nop 0\n\t" "s_nop 0\n\t" "s_nop 0\n\t" "s_nop 0\n\t" "s_nop 0\n\t" "s_nop 0\n\t"
                         "global_load_dwordx4 a[96:99], v4, %3 offset:-512 sc1\n\t" "global_load_dwordx4 a[100:103], v4, %3 offset:512 sc1\n\t" "global_load_dwordx4 a[104:107], v4, %3 offset:1536 sc1\n\t"
                         "global_load_dwordx4 a[108:111], v4, %3 offset:2560 sc1\n\t" "global_load_dwordx4 a[112:115], %13, %3 sc1\n\t" "global_load_dwordx4 a[116:119], %13, %3 offset:1024 sc1\n\t"
                         "global_load_dwordx4 a[120:123], %13, %3 offset:2048 sc1\n\t" "global_load_dwordx4 a[124:127], %13, %3 offset:3072 sc1\n\t" "s_nop 15\n\t" "s_nop 1\n\t"
                         "global_load_dwordx4 a[0:3], v5, %7 offset:-4096 sc1\n\t" "global_load_dwordx4 a[4:7], v5, %7 offset:-3072 sc1\n\t" "global_load_dwordx4 a[8:11], v5, %7 sc1\n\t"
                         "global_load_dwordx4 a[12:15], v5, %7 offset:1024 sc1\n\t" "global_load_dwordx4 a[16:19], v5, %8 offset:-4096 sc1\n\t" "global_load_dwordx4 a[20:23], v5, %8 offset:-3072 sc1\n\t"
                         "global_load_dwordx4 a[24:27], v5, %8 sc1\n\t" "global_load_dwordx4 a[28:31], v5, %8 offset:1024 sc1\n\t" "s_nop 14\n\t" "global_load_dwordx4 a[32:35], %12, %7 offset:-2048 sc1\n\t"
                         "global_load_dwordx4 a[36:39], %12, %7 offset:-1024 sc1\n\t" "global_load_dwordx4 a[40:43], %12, %7 offset:2048 sc1\n\t" "global_load_dwordx4 a[44:47], %12, %7 offset:3072 sc1\n\t"
                         "global_load_dwordx4 a[48:51], %12, %8 offset:-2048 sc1\n\t" "global_load_dwordx4 a[52:55], %12, %8 offset:-1024 sc1\n\t" "global_load_dwordx4 a[56:59], %12, %8 offset:2048 sc1\n\t"
                         "global_load_dwordx4 a[60:63], %12, %8 offset:3072 sc1\n\t" "s_nop 15\n\t" "s_nop 15\n\t" "global_load_dwordx4 a[80:83], v7, %6 sc1\n\t"
                         "global_load_dwordx4 a[84:87], v7, %6 offset:2048 sc1\n\t" "s_mov_b64 exec, %9\n\t" "global_load_dwordx4 a[64:67], v6, %6 offset:-2048 sc1\n\t"
                         "global_load_dwordx4 a[68:71], v6, %6 offset:-1024 sc1\n\t" "s_mov_b64 exec, -1\n\t" "s_nop 15\n\t" "s_nop 8\n\t" "global_load_dwordx4 a[88:91], %14, %6 sc1\n\t"
                         "global_load_dwordx4 a[92:95], %14, %6 offset:2048 sc1\n\t" "s_mov_b64 exec, %9\n\t" "global_load_dwordx4 a[72:75], %15, %6 offset:-2048 sc1\n\t"
                         "global_load_dwordx4 a[76:79], %15, %6 offset:-1024 sc1\n\t" "s_mov_b64 exec, -1\n\t" "s_waitcnt vmcnt(23)\n\t" "v_smfmac_f32_16x16x64_bf16 v[48:51], a[0:3], v[112:119], v9\n\t"
                         "s_waitcnt vmcnt(22)\n\t" "v_smfmac_f32_16x16x64_bf16 v[52:55], a[4:7], v[120:127], v9\n\t" "s_waitcnt vmcnt(21)\n\t" "v_smfmac_f32_16x16x64_bf16 v[56:59], a[8:11], v[144:151], v9\n\t"
                         "s_waitcnt vmcnt(20)\n\t" "v_cvt_pk_bf16_f32 v16, v48, v49\n\t" "v_cvt_pk_bf16_f32 v17, v50, v51\n\t" "v_smfmac_f32_16x16x64_bf16 v[60:63], a[12:15], v[152:159], v9\n\t" "s_waitcnt vmcnt(19)\n\t"
                         "v_cvt_pk_bf16_f32 v18, v52, v53\n\t" "v_cvt_pk_bf16_f32 v19, v54, v55\n\t" "v_smfmac_f32_16x16x64_bf16 v[64:67], a[16:19], v[176:183], v9\n\t" "s_waitcnt vmcnt(18)\n\t"
                         "v_cvt_pk_bf16_f32 v20, v56, v57\n\t" "v_cvt_pk_bf16_f32 v21, v58, v59\n\t" "v_smfmac_f32_16x16x64_bf16 v[68:71], a[20:23], v[184:191], v9\n\t" "s_waitcnt vmcnt(17)\n\t"
                         "v_cvt_pk_bf16_f32 v22, v60, v61\n\t" "v_cvt_pk_bf16_f32 v23, v62, v63\n\t" "v_smfmac_f32_16x16x64_bf16 v[72:75], a[24:27], v[208:215], v9\n\t" "s_waitcnt vmcnt(16)\n\t"
                         "v_cvt_pk_bf16_f32 v24, v64, v65\n\t" "v_cvt_pk_bf16_f32 v25, v66, v67\n\t" "v_smfmac_f32_16x16x64_bf16 v[76:79], a[28:31], v[216:223], v9\n\t" "s_waitcnt vmcnt(15)\n\t"
                         "v_cvt_pk_bf16_f32 v26, v68, v69\n\t" "v_cvt_pk_bf16_f32 v27, v70, v71\n\t" "v_smfmac_f32_16x16x64_bf16 v[80:83], a[32:35], v[128:135], v9\n\t" "s_waitcnt vmcnt(14)\n\t"
                         "v_cvt_pk_bf16_f32 v28, v72, v73\n\t" "v_cvt_pk_bf16_f32 v29, v74, v75\n\t" "v_smfmac_f32_16x16x64_bf16 v[84:87], a[36:39], v[136:143], v9\n\t" "s_waitcnt vmcnt(13)\n\t"
                         "v_cvt_pk_bf16_f32 v30, v76, v77\n\t" "v_cvt_pk_bf16_f32 v31, v78, v79\n\t" "v_smfmac_f32_16x16x64_bf16 v[88:91], a[40:43], v[160:167], v9\n\t" "s_waitcnt vmcnt(12)\n\t"
                         "v_cvt_pk_bf16_f32 v32, v80, v81\n\t" "v_cvt_pk_bf16_f32 v33, v82, v83\n\t" "v_smfmac_f32_16x16x64_bf16 v[92:95], a[44:47], v[168:175], v9\n\t" "s_waitcnt vmcnt(11)\n\t"
                         "v_cvt_pk_bf16_f32 v34, v84, v85\n\t" "v_cvt_pk_bf16_f32 v35, v86, v87\n\t" "v_smfmac_f32_16x16x64_bf16 v[96:99], a[48:51], v[192:199], v9\n\t" "s_waitcnt vmcnt(10)\n\t"
                         "v_cvt_pk_bf16_f32 v36, v88, v89\n\t" "v_cvt_pk_bf16_f32 v37, v90, v91\n\t" "v_smfmac_f32_16x16x64_bf16 v[100:103], a[52:55], v[200:207], v9\n\t" "s_waitcnt vmcnt(9)\n\t"
                         "v_cvt_pk_bf16_f32 v38, v92, v93\n\t" "v_cvt_pk_bf16_f32 v39, v94, v95\n\t" "v_smfmac_f32_16x16x64_bf16 v[104:107], a[56:59], v[224:231], v9\n\t" "s_waitcnt vmcnt(8)\n\t"
                         "v_cvt_pk_bf16_f32 v40, v96, v97\n\t" "v_smfmac_f32_16x16x64_bf16 v[108:111], a[60:63], v[232:239], v9\n\t" "s_waitcnt vmcnt(7)\n\t" "v_cvt_pk_bf16_f32 v41, v98, v99\n\t"
                         "v_smfmac_f32_16x16x64_bf16 v[0:3], a[80:83], a[96:103], v8\n\t" "s_waitcnt vmcnt(6)\n\t" "v_cvt_pk_bf16_f32 v42, v100, v101\n\t"
                         "v_smfmac_f32_16x16x64_bf16 v[0:3], a[84:87], a[104:111], v8\n\t" "s_waitcnt vmcnt(5)\n\t" "v_cvt_pk_bf16_f32 v43, v102, v103\n\t" "v_smfmac_f32_16x16x64_bf16 v[0:3], a[64:67], v[16:23], v8\n\t"
                         "s_waitcnt vmcnt(4)\n\t" "v_cvt_pk_bf16_f32 v44, v104, v105\n\t" "v_smfmac_f32_16x16x64_bf16 v[0:3], a[68:71], v[24:31], v8\n\t" "s_waitcnt vmcnt(3)\n\t" "v_cvt_pk_bf16_f32 v45, v106, v107\n\t"
                         "v_smfmac_f32_16x16x64_bf16 v[0:3], a[88:91], a[112:119], v8\n\t" "s_waitcnt vmcnt(2)\n\t" "v_cvt_pk_bf16_f32 v46, v108, v109\n\t"
                         "v_smfmac_f32_16x16x64_bf16 v[0:3], a[92:95], a[120:127], v8\n\t" "s_waitcnt vmcnt(1)\n\t" "v_cvt_pk_bf16_f32 v47, v110, v111\n\t" "v_smfmac_f32_16x16x64_bf16 v[0:3], a[72:75], v[32:39], v8\n\t"
                         "s_waitcnt vmcnt(0)\n\t" "v_smfmac_f32_16x16x64_bf16 v[0:3], a[76:79], v[40:47], v8\n\t" ".Lv_end:\n\t" "s_nop 7\n\t" "v_pk_add_f32 v[0:1], v[0:1], v[2:3]\n\t" "v_cvt_pk_bf16_f32 v2, v0, v1\n\t"
                         "v_cmp_u_f32 vcc, v0, v1\n\t" "s_cmp_eq_u64 vcc, 0\n\t" "v_mov_b32_dpp v3, v2 quad_perm:[1,0,3,2] row_mask:0xf bank_mask:0xf\n\t" "s_cselect_b64 %0, -1, 0\n\t" "v_perm_b32 v0, v2, v3, v11\n\t"
                         "s_mov_b64 exec, %0\n\t" "global_store_dword v10, v0, %5 offset:-4096 sc1\n\t" "global_store_dword v10, v0, %4 offset:-4096 sc1\n\t" "s_memrealtime %1\n\t" "s_mov_b64 exec, -1\n\t"
                         "s_waitcnt lgkmcnt(0)\n\t"
                         : "=&s"(ok), "=&s"(tp8)
                         : "{v[4:11]}"(ad), "s"(up), "s"(mnx), "s"((u64)mnx ^ 8192), "s"(memp + 16384), "s"(memp + 32768 - 2048), "s"(memp + 49152 - 2048), "s"(0xc30000c3c30000c3ull),
                           "s"(uni((int)(tgt - (u32)(v < 16 ? cfg.dU - (v >> 3) * cfg.s2 : cfg.cR2)))), "s"(v < 16 ? 0 : (v < 24 ? 1 : cfg.T3)), "v"(pob), "v"(uob), "v"(pmo), "v"(cob), VALL : "memory", "vcc", "scc", "s42");
            if (__builtin_expect(ok != 0, 1) || s < 0) break;
            if (all || --budget < 0) {
                asm volatile("v_min_u32 v0, -2, v0\n\tglobal_store_dword v10, v0, %0 offset:-4096 sc1\n\tglobal_store_dword v10, v0, %1 offset:-4096 sc1"
                             : : "s"(mnx), "s"((u64)mnx ^ 8192), "{v[4:11]}"(ad), VALL : "memory");
                break;
            }
            if ((++tries & 15) == 0) {
                u32 m = 0;
#pragma nounroll
                for (int i = 0; i < 32; ++i) {
                    u32x4 t;
                    gld16c<0>(t, i < 24 ? memp + 12288 : up, i < 20 ? (4 + i + ((4 + i) & 24)) * 1024 + lanex() * 16 : (i < 24 ? 2048 + (i & 1) * 1024 + (i & 2 ? cob : ad[2]) : (i & 7) * 512 - 512 + (i & 1 ? uob : ad[0]))); waitv<0>(t);
                    m = max3u(max3u(m, t[0], t[1]), t[2], t[3]);
                }
                all = __builtin_amdgcn_ballot_w64(m == 0xffffffffu) == 0;
            }
            phi += (u32)cfg.G;
            zero();
        }
        asm volatile("s_barrier\n\t" "ds_read_b128 v[112:115], %0\n\t" "ds_read_b128 v[116:119], %0 offset:1024\n\t" "ds_read_b128 v[120:123], %0 offset:2048\n\t" "ds_read_b128 v[124:127], %0 offset:3072\n\t"
                     "ds_read_b128 v[128:131], %0 offset:4096\n\t" "ds_read_b128 v[132:135], %0 offset:5120\n\t" "ds_read_b128 v[136:139], %0 offset:6144\n\t" "ds_read_b128 v[140:143], %0 offset:7168\n\t"
                     "ds_read_b128 v[144:147], %0 offset:8192\n\t" "ds_read_b128 v[148:151], %0 offset:9216\n\t" "ds_read_b128 v[152:155], %0 offset:10240\n\t" "ds_read_b128 v[156:159], %0 offset:11264\n\t"
                     "ds_read_b128 v[160:163], %0 offset:12288\n\t" "ds_read_b128 v[164:167], %0 offset:13312\n\t" "ds_read_b128 v[168:171], %0 offset:14336\n\t" "ds_read_b128 v[172:175], %0 offset:15360\n\t"
                     "ds_read_b128 v[176:179], %0 offset:16384\n\t" "ds_read_b128 v[180:183], %0 offset:17408\n\t" "ds_read_b128 v[184:187], %0 offset:18432\n\t" "ds_read_b128 v[188:191], %0 offset:19456\n\t"
                     "ds_read_b128 v[192:195], %0 offset:20480\n\t" "ds_read_b128 v[196:199], %0 offset:21504\n\t" "ds_read_b128 v[200:203], %0 offset:22528\n\t" "ds_read_b128 v[204:207], %0 offset:23552\n\t"
                     "ds_read_b128 v[208:211], %0 offset:24576\n\t" "ds_read_b128 v[212:215], %0 offset:25600\n\t" "ds_read_b128 v[216:219], %0 offset:26624\n\t" "ds_read_b128 v[220:223], %0 offset:27648\n\t"
                     "ds_read_b128 v[224:227], %0 offset:28672\n\t" "ds_read_b128 v[228:231], %0 offset:29696\n\t" "ds_read_b128 v[232:235], %0 offset:30720\n\t" "ds_read_b128 v[236:239], %0 offset:31744\n\t"
                     : : "v"(lds + lanex() * 16 + ((s + 1) & 1) * 32768), VALL : "memory");
        if (s >= 0) {
            follow(phi, (int)(tt & 0xffffu) + 5 * (v >> 2) / 2 - (cfg.dV >> 16 & 255), (u32)tp8, cfg.G);
            if (vb == NVB - 1 && lane < 8) gst8(fl, (F_CUR + lane) * 128, u32x2{(u32)s - 1, phi + (tt & 0xffffu)});
        }
        phi += tt & 0xffffu;
        memp = mnx; up = ub + uslot(sd, s + 1);
        zero();
        tt = sched_ld0(s + 1);
        if (s < 0) { phi = go_wait(fl, vb) - (tt >> 16); edge(clk.r1, clk.m1); }
        clk.calib();
        while (s >= 0) {
            u32 h0, h1;
            spin_to(clk.at(phi + (u32)(cfg.dV >> 8 & 255)));
            asm volatile("global_load_dword %0, %1, %2 offset:-4096 sc1" : "=&v"(h1) : "v"(ho & 0xfff), "s"(memp) : "memory");
            spin_to(clk.at(phi + (u32)(cfg.dV & 255)));
            asm volatile("global_load_dword %0, %1, %2 offset:-4096 sc1\n\ts_sleep 5" : "=&v"(h0) : "v"(ho), "s"(memp) : "memory");
            edge(clk.r1, clk.m1);
            asm volatile("s_waitcnt vmcnt(0)" : "+v"(h0), "+v"(h1) : : "memory");
            h0 = max(h0, h1);
            if (__builtin_amdgcn_ballot_w64(h0 == 0xffffffffu) == 0 || --budget < 0) break;
            phi += (u32)cfg.G;
        }
        asm volatile(".Lkm_lv:");
    }
}

DI void xcopy_task(const Ptrs& p, int j, int w) {
    const int tid = 64 * w + lanex();
    Sync y;
    sync_ld(y, p.ws);
    u32x4 xv[10];
    sfor<0, 10>([&](auto I) { gld16<I.v * 128>(xv[I.v], p.x + (size_t)j * 40960, (u32)((tid & 31) * 1280 + (tid >> 5) * 16)); });
    sfor<0, 10>([&](auto I) { waitv<9 - I.v>(xv[I.v]); });
    asm volatile("" : "+v"(y.r), "+v"(y.g), "+v"(y.e));
    sfor<0, 10>([&](auto I) { gst16(p.ws + OFF_XF + (size_t)j * 40960 + I.v * 4096, (u32)tid * 16, xv[I.v]); });
    waitvm<0>();
    barrier();
    const u64 t = sync_tag(y.e);
    if (tid == 0) gst8(p.ws + OFF_LAT, 1536 + (u32)j * 8, u32x2{(u32)t, (u32)(t >> 32)});
}
DI void xcopy_wait(const char* ws, u64 t, int lo = 0, int msk = 63) {
#pragma nounroll
    for (int it = 0; it < (1 << 16); ++it) {
        u32x2 r;
        asm volatile("s_nop 4\n\tglobal_load_dwordx2 %0, %1, %2 offset:1536 sc1\n\ts_waitcnt vmcnt(0)" : "=&v"(r) : "v"((lo + (lanex() & msk)) * 8), "s"(ws + OFF_LAT) : "memory");
        if (__builtin_amdgcn_ballot_w64(r[0] != (u32)t || r[1] != (u32)(t >> 32)) == 0) break;
        __builtin_amdgcn_s_sleep(XSL);
    }
}
DI void init_task(const Ptrs& p, int j, u32 lds, int w) {
    const int lane = lanex(), tid = 64 * w + lane;
    Sync y;
    sync_ld(y, p.ws);
    const u32x4 ff = u32x4{~0u, ~0u, ~0u, ~0u};
    const int i = j * 256 + tid, r = j < 8 ? j & 1 : min(j / 2 - 2, NREG - 1);
    char* const vb = p.ws + (r < 2 ? 0 : OFF_U - (4u << 20)) + ((size_t)r << 21) + 1984 * 1024 + (j < 8 ? j & 6 : (j & 1) * 2 + (j >> 1 == 15) * 4) * 8192;
    const u32 vo = (u32)((w >> 1) * 8192 + (w & 1) * 1024);
    gst16(vb, vo + lane * 16, ff);
    if (i < F_N * 8) gst16(p.ws + OFF_ST, (u32)i * 16, u32x4{i == F_ONE * 8 ? 1000u : ~0u, ~0u, ~0u, ~0u});
    if (i < 512) gst16(p.ws + OFF_GATE, (u32)i * 16, ff);
    gst16(p.ws + 65536, (u32)i * 16, ff);
    if (i < 4096) {
        const int k = (i & 511) + ((i >> 1) & 0x600);
        u32x4 m;
        gld16<0>(m, p.mem0, (u32)((2 * ((k >> 2) & 3) + (k & 1)) * 1280 + min(2 * (k >> 4) + ((k >> 1) & 1), 79) * 16));
        waitv<0>(m);
        gst16(p.ws, (u32)i * 16, k < 640 ? u32x4{min(m[0], 0xfffffffeu), min(m[1], 0xfffffffeu), min(m[2], 0xfffffffeu), min(m[3], 0xfffffffeu)} : ff);
    }
#pragma unroll
    for (int c = 0; c < 6; ++c) gst16(p.ws + OFF_U + c * 131072 - (c == 5) * 122880, (u32)i * 16, ff);
    waitvm<0>();
    asm volatile("" : "+v"(y.r), "+v"(y.g), "+v"(y.e));
    barrier();
    if (w) return;
    u32 dt[2] = {0xffffffu, 0xffffffu};
#pragma unroll
    for (int c = 0; c < 2 * NSAMP; ++c) {
        u32 v;
        const u32 t0 = mt();
        asm volatile("global_load_dword %0, %1, %2 sc1\n\ts_waitcnt vmcnt(0)" : "=&v"(v) : "v"((c & 1) * 8192 + (c >> 1) * 256), "s"(vb) : "memory");
        dt[c & 1] = min(dt[c & 1], mt() - t0);
    }
    const u64 t = sync_tag(y.e);
    const u32 vote = (u32)((dt[0] > dt[1]) ^ xhalf() ^ (int)((vb - p.ws) >> 21)) & 1u;
    if (tid == 0) {
        if (j == 0) gst8(p.ws + OFF_LAT, 512, u32x2{(u32)t, rt()});
        gst8(p.ws + OFF_LAT, (u32)j * 8, u32x2{(u32)t | vote, (u32)(t >> 32)});
    }
}

DI float xmax(float v) {
    const auto a = __builtin_amdgcn_permlane32_swap(__float_as_uint(v), __float_as_uint(v), false, false);
    v = fmaxf(__uint_as_float(a[0]), __uint_as_float(a[1]));
    const auto b = __builtin_amdgcn_permlane16_swap(__float_as_uint(v), __float_as_uint(v), false, false);
    return fmaxf(__uint_as_float(b[0]), __uint_as_float(b[1]));
}
DI float xsum(float v) {
    const auto a = __builtin_amdgcn_permlane32_swap(__float_as_uint(v), __float_as_uint(v), false, false);
    v = __uint_as_float(a[0]) + __uint_as_float(a[1]);
    const auto b = __builtin_amdgcn_permlane16_swap(__float_as_uint(v), __float_as_uint(v), false, false);
    return __uint_as_float(b[0]) + __uint_as_float(b[1]);
}
DI void gst16c(char* sbase, u32 voff, u32x4 v) {
    asm volatile("global_store_dwordx4 %0, %1, %2 sc1\n\ts_nop 1" : : "v"(voff), "v"(v), "s"(sbase) : "memory");
}
#define WAIT6(N, r, o) asm volatile("s_waitcnt vmcnt(" #N ")" : "+v"(r[o]), "+v"(r[o + 1]), "+v"(r[o + 2]), "+v"(r[o + 3]), "+v"(r[o + 4]), "+v"(r[o + 5]) : : "memory")
#define WSLEEP 90
DI void wg_wait(const char* fl, int w, int i0, int t0, int i1, int t1, int i2, int t2, int i3, int t3, int dt, bool acq) {
    if (w == 0) {
        const int lane = lanex(), tg = lane == 1 ? t1 : (lane == 2 ? t2 : (lane == 3 ? t3 : t0));
        const int ix = tg <= 0 ? i0 : (lane == 1 ? i1 : (lane == 2 ? i2 : (lane == 3 ? i3 : i0)));
#pragma nounroll
        for (int it = 0; it < (1 << 14); ++it) {
            u32 v;
            flag_ld(v, fl, ix);
            asm volatile("s_waitcnt vmcnt(0)" : "+v"(v) : : "memory");
            if (__builtin_amdgcn_ballot_w64((int)(v + 1u) < tg) == 0) break;
            const u32 wake = rt() + (u32)(dt + 180 * min(max(t0 - 2 - uni((int)v), 0), 64));
            while ((int)(rt() - wake) < 0) __builtin_amdgcn_s_sleep(4);
        }
        if (acq) acquire();
    }
    barrier();
}
#define TEST 172
DI u32 step_wait(const char* fl, int w, int gr, int t, u32 lds) {
    if (w == 0) {
        u32x2 v;
#pragma nounroll
        for (int it = 0; it < (1 << 14); ++it) {
            asm volatile("s_nop 4\n\tglobal_load_dwordx2 %0, %1, %2 sc1\n\ts_waitcnt vmcnt(0)" : "=&v"(v) : "v"((F_CUR + gr) * 128), "s"(fl) : "memory");
            const int d = t - 1 - uni((int)v[0]);
            if (d <= 0) { v[1] += (u32)(d * TEST); break; }
            const u32 now = rt();
            nap(min(max((int)((u32)uni((int)v[1]) - now) + d * TEST + 10, 40), 2000));
        }
        *LDSP(u32, lds + GRING * GBUF + 4) = v[1];
    }
    barrier();
    return (u32)uni((int)*LDSP(u32, lds + GRING * GBUF + 4));
}
#define PDL 115
#define ODL 240
#define TDT 35
template <bool LATE>
DI void px_wave(const Ptrs& p, const ChainCfg& cfg, int s, int h, int slow, int w, u32 lds, bool keep) {
    const int lane = lanex(), n = lane & 15, g = lane >> 4;
    const char* qb = p.ws + OFF_QX + (size_t)(s * NH + h) * 12288;
    const char* kb = p.ws + OFF_KX + (size_t)(s * NH + h) * 12288;
    const char* mb = p.ws + OFF_KM + (size_t)(s * NH + h) * 3072;
    const u32 mo = (u32)(g * 128 + (n & 7) * 16);
    u32x4 qf[12], kf[12], mf[6];
    sfor<0, 12>([&](auto I) { gld16x<(I.v % 3) * 1024>(qf[I.v], qb + (I.v / 3) * 3072, lane * 16); gap<SMP>(slow); });
    if (!SMP) pace(slow);
    sfor<0, 12>([&](auto I) { if constexpr (LATE || !KNT) gld16c<(I.v % 3) * 1024>(kf[I.v], kb + (I.v / 3) * 3072, lane * 16); else gld16x<(I.v % 3) * 1024>(kf[I.v], kb + (I.v / 3) * 3072, lane * 16); gap<SMP>(slow); });
    if (!SMP) pace(slow);
    if constexpr (LATE) { WAIT6(0, qf, 0); WAIT6(0, qf, 6); WAIT6(0, kf, 0); }
    else {
        sfor<0, 6>([&](auto I) { gld16x<I.v * 512>(mf[I.v], mb, mo); });
        WAIT6(24, qf, 0); WAIT6(18, qf, 6); WAIT6(12, kf, 0);
    }
    f32x4 sc[2][3];
#pragma unroll
    for (int tt = 0; tt < 2; ++tt)
#pragma unroll
        for (int T = 0; T < 3; ++T) sc[tt][T] = f32x4{0.f, 0.f, 0.f, 0.f};
#pragma unroll
    for (int ks = 0; ks < 6; ++ks) { sc[0][0] = mfma16(kf[ks], qf[ks], sc[0][0]); sc[1][0] = mfma16(kf[ks], qf[6 + ks], sc[1][0]); }
    WAIT6(6, kf, 6);
#pragma unroll
    for (int ks = 0; ks < 6; ++ks) sc[1][1] = mfma16(kf[6 + ks], qf[6 + ks], sc[1][1]);
    if constexpr (LATE) {
        until(step_wait(p.ws + OFF_ST, w, h & 7, s - 1, lds) + (u32)(s == NSEG - 1 ? cfg.T : cfg.T + PDX));
        bool ok = false;
#pragma nounroll
        for (int it = 0; it < (1 << 14); ++it) {
            u32 v;
            sfor<0, 6>([&](auto I) { gld16c<I.v * 512>(mf[I.v], mb, mo); });
            flag_ldm(v, p.ws + OFF_ST, (s < NSEG - 2 ? F_CUR : F_PROG + 8 * s) + (h & 7), it > 3);
            WAIT6(0, mf, 0);
            asm volatile("" : "+v"(v));
            u32 m = 0;
#pragma unroll
            for (int i = 0; i < 24; ++i) m = max(m, mf[i >> 2][i & 3]);
            if (ok || __builtin_amdgcn_ballot_w64(m == ~0u) == 0) break;
            ok = it > 3 && uni((int)v) >= (s < NSEG - 2 ? s : 7);
            if (s < NSEG - 1) nap(60);
        }
    }
    WAIT6(0, mf, 0);
#pragma unroll
    for (int ks = 0; ks < 6; ++ks) { sc[0][2] = mfma16(mf[ks], qf[ks], sc[0][2]); sc[1][2] = mfma16(mf[ks], qf[6 + ks], sc[1][2]); }
    const float kscale = 0.07216878364870322f;
    char* pb = p.ws + OFF_PX + (size_t)s * 81920;
#pragma unroll
    for (int tt = 0; tt < 2; ++tt) {
        float v[12], mx = -3.0e38f;
#pragma unroll
        for (int i = 0; i < 12; ++i) {
            const float t = rbf(rbf(sc[tt][i >> 2][i & 3]) * kscale);
            v[i] = (i >= 8 || 4 * g + (i & 3) + 16 * (i >> 2) <= 16 * tt + n) ? t : -3.0e38f;
            mx = fmaxf(mx, v[i]);
        }
        mx = xmax(mx);
        float sx = 0.f, sm = 0.f;
#pragma unroll
        for (int i = 0; i < 12; ++i) {
            v[i] = __builtin_amdgcn_exp2f((v[i] - mx) * 1.4426950408889634f);
            if (i < 8) sx += v[i]; else sm += v[i];
        }
        const float inv = __builtin_amdgcn_rcpf(xsum(sx + 0.5f * sm));
        const u32x4 px = u32x4{cvtpk(v[0] * inv, v[1] * inv), cvtpk(v[2] * inv, v[3] * inv), cvtpk(v[4] * inv, v[5] * inv), cvtpk(v[6] * inv, v[7] * inv)};
        const u32x2 pm = u32x2{cvtpk(v[9] * inv, v[8] * inv), cvtpk(v[11] * inv, v[10] * inv)};
        const int ox = h * 2048 + tt * 1024, om = 65536 + (h >> 2) * 2048 + (h & 3) * 256 + tt * 1024;
        if (!LATE && keep) {
            *LDSP(u32x4, lds + ox + lane * 16) = px;
            *LDSP(u32x2, lds + om + n * 16 + (g & 1) * 8) = pm;
        } else {
            gst16c(pb + ox, lane * 16, px);
            const auto a = __builtin_amdgcn_permlane16_swap(pm[0], pm[0], false, false), b = __builtin_amdgcn_permlane16_swap(pm[1], pm[1], false, false);
            if (g == 0) gst16c(pb + om, (u32)(n * 16), u32x4{a[0], b[0], a[1], b[1]});
        }
    }
}

#define WAIT10(N, r) asm volatile("s_waitcnt vmcnt(" #N ")" : "+v"(r[0]), "+v"(r[1]), "+v"(r[2]), "+v"(r[3]), "+v"(r[4]), "+v"(r[5]), "+v"(r[6]), "+v"(r[7]), "+v"(r[8]), "+v"(r[9]) : : "memory")
DI void out_task(const Ptrs& p, int s, int cg0, int cg1, bool kept, u32 lds, int slow, int w, int sd) {
    u32x4 pf[2][10];
    if (kept) {
        const u32 la = lds + lanex() * 16;
#pragma unroll
        for (int i = 0; i < 20; ++i) pf[i & 1][i >> 1] = ldsr16(la + (i < 16 ? w * 16384 + i * 1024 : 65536 + w * 4096 + (i - 16) * 1024));
    } else {
        const char* pb = p.ws + OFF_PX + (size_t)s * 81920;
        const u32 l16 = lanex() * 16;
        sfor<0, 20>([&](auto I) { gld16c<(I.v & 3) * 1024>(pf[I.v & 1][I.v >> 1], pb + (I.v < 16 ? w * 16384 + (I.v >> 2) * 4096 : 65536 + w * 4096), l16); gap<SMO>(slow); if (!SMO && I.v == 9) pace(slow); });
        WAIT10(0, pf[0]); WAIT10(0, pf[1]);
    }
#pragma nounroll
    for (int cg = cg0; cg < cg1; ++cg) {
    const int lane = lanex(), tid = 64 * w + lane, n = lane & 15, g = lane >> 4;
    const char* db = p.ws + OFF_D + s * D_STRIDE + (size_t)(16 * cg) * 16384 + w * 4096;
    const char* ub = p.ws + uslot(sd, s) + (size_t)(16 * cg) * 4096 + w * 1024;
    const u32 dof = (u32)((n >> 3) * 16384 + g * 128 + (n & 7) * 16), uof = (u32)((n >> 3) * 4096 + g * 128 + (n & 7) * 16);
    u32x4 af[3][10];
    auto aload = [&](u32x4 (&r)[10], int ct) {
        if (!SMO) pace(slow);
        sfor<0, 8>([&](auto I) { gld16x<I.v * 512>(r[I.v], db + ct * 32768, dof); gap<SMO>(slow); });
        gld16x<0>(r[8], ub + uchunk(ct), uof); gap<SMO>(slow); gld16x<512>(r[9], ub + uchunk(ct), uof); gap<SMO>(slow);
    };
    aload(af[0], 0);
    aload(af[1], 1);
    aload(af[2], 2);
    const int tok = 32 * s + (tid >> 3), col = 128 * cg + 8 * (tid & 7);
    u32x4 bb[2]; u32 gv;
    gld16c<0>(bb[0], p.ws + OFF_BOUT, (u32)(s * 40960 + col * 64 + (tid >> 3) * 16)); gld16c<0>(bb[1], p.ws + OFF_BOUT + 4096, (u32)(s * 40960 + col * 64 + (tid >> 3) * 16));
    asm volatile("global_load_dword %0, %1, %2 sc1" : "=v"(gv) : "v"(tok * 4), "s"(p.ws + OFF_GATE) : "memory");
    barrier();
    const u32 wa = lds + (u32)w * 16896 + n * 528 + g * 16;
    sfor<0, 8>([&](auto I) {
        constexpr int ct = I.v;
        u32x4 (&r)[10] = af[ct % 3];
        if constexpr (ct == 0) WAIT10(23, r);
        else if constexpr (ct < 3) WAIT10(23, r);
        else if constexpr (ct < 6) WAIT10(20, r);
        else if constexpr (ct == 6) WAIT10(10, r);
        else WAIT10(0, r);
        f32x4 c0 = f32x4{0.f, 0.f, 0.f, 0.f}, c1 = c0;
#pragma unroll
        for (int i = 0; i < 10; ++i) { c0 = mfma16(r[i], pf[0][i], c0); c1 = mfma16(r[i], pf[1][i], c1); }
        *LDSP(f32x4, wa + ct * 64) = c0;
        *LDSP(f32x4, wa + ct * 64 + 16 * 528) = c1;
        PIN();
        if constexpr (ct + 3 < 8) aload(r, ct + 3);
        PIN();
    });
    asm volatile("" : "+v"(bb[0]), "+v"(bb[1]), "+v"(gv));
    barrier();
    const u32 ra = lds + (u32)(tid >> 3) * 528 + (tid & 7) * 32;
    const float gt = __uint_as_float(gv), g1 = rbf(1.f - gt);
    u32 ov[8];
#pragma unroll
    for (int i = 0; i < 4; ++i) {
        const u32 ri = ra + 16 * (i & 1) + 256 * (i >> 1);
        const f32x4 z = (*LDSP(f32x4, ri) + *LDSP(f32x4, ri + 16896)) + (*LDSP(f32x4, ri + 2 * 16896) + *LDSP(f32x4, ri + 3 * 16896));
#pragma unroll
        for (int j = 0; j < 2; ++j) {
            const u32 b = bb[i >> 1][2 * (i & 1) + j];
            const u32 a = cvtpk(z[2 * j], z[2 * j + 1]);
            const u32 ga = cvtpk(gt * __uint_as_float(a << 16), gt * __uint_as_float(a & 0xffff0000u));
            const u32 gb = cvtpk(g1 * __uint_as_float(b << 16), g1 * __uint_as_float(b & 0xffff0000u));
            ov[2 * i + j] = cvtpk(__uint_as_float(ga << 16) + __uint_as_float(gb << 16), __uint_as_float(ga & 0xffff0000u) + __uint_as_float(gb & 0xffff0000u));
        }
    }
    gst16p(p.out, (u32)(tok * 1280 + col * 2), u32x4{ov[0], ov[1], ov[2], ov[3]});
    gst16p(p.out, (u32)(tok * 1280 + col * 2 + 128), u32x4{ov[4], ov[5], ov[6], ov[7]});
    }
}

DI void out_tail(const Ptrs& p, const ChainCfg& cfg, int s, int cg, u32 lds, int w, int sd, int gr) {
    const int lane = lanex(), tid = 64 * w + lane, n = lane & 15, g = lane >> 4;
    const char* db = p.ws + OFF_D + s * D_STRIDE + (size_t)(4 * cg) * 16384 + w * 4096;
    const char* ub = p.ws + uslot(sd, s) + uchunk(2 * cg) + w * 1024;
    const char* pb = p.ws + OFF_PX + (size_t)s * 81920;
    const u32 dof = (u32)((n >> 3) * 16384 + g * 128 + (n & 7) * 16), uof = (u32)((n >> 3) * 4096 + g * 128 + (n & 7) * 16);
    const int slow = uni(s - TGS);
    u32x4 pf[2][10], af[2][10];
    sfor<0, 16>([&](auto I) { gld16c<(I.v & 7) * 512>(af[I.v >> 3][I.v & 7], db + (I.v >> 3) * 32768, dof); gap<TG1>(-1); });
    const int tok = 32 * s + (tid >> 3), col = 32 * cg + 4 * (tid & 7);
    u32x2 bb; u32 gv;
    asm volatile("global_load_dwordx2 %0, %2, %3 sc1\n\tglobal_load_dword %1, %4, %5 sc1\n\ts_waitcnt vmcnt(0)" : "=&v"(bb), "=&v"(gv)
                 : "v"(s * 40960 + (col >> 3) * 512 + (tid >> 3) * 16 + (col & 7) * 2), "s"(p.ws + OFF_BOUT), "v"(tok * 4), "s"(p.ws + OFF_GATE) : "memory");
    sfor<0, 16>([&](auto I) { waitv<0>(af[I.v >> 3][I.v & 7]); });
    until(step_wait(p.ws + OFF_ST, w, gr, s - 1, lds) + (u32)(s == NSEG - 1 ? cfg.cR : cfg.cR + 2 * PDX));
    const u32 wa = lds + (u32)w * 4608 + n * 144 + g * 16;
#pragma nounroll
    for (int it = 0; it < (1 << 14); ++it) {
        if (it == 4) {
            flag_wait(p.ws + OFF_ST, F_PX + s, 8);
            flag_wait(p.ws + OFF_ST, s < NSEG - 2 ? F_CUR + gr : F_UD + 2 * (s & 1), s < NSEG - 2 ? s + 1 : 16);
            flag_wait(p.ws + OFF_ST, s < NSEG - 2 ? F_ONE : F_UD + 2 * (s & 1) + 1, 16);
        }
        sfor<0, 4>([&](auto I) { gld16c<(I.v & 1) * 512>(af[I.v >> 1][8 + (I.v & 1)], ub + (I.v >> 1) * 16384, uof); });
        sfor<0, 8>([&](auto I) { gld16c<(I.v & 1) * 2048>(pf[0][I.v], pb + w * 16384 + (I.v >> 1) * 4096, lane * 16); gap<TG2>(slow); });
        gld16c<0>(pf[0][8], pb + 65536 + w * 4096, lane * 16); gld16c<2048>(pf[0][9], pb + 65536 + w * 4096, lane * 16);
        sfor<0, 8>([&](auto I) { gld16c<(I.v & 1) * 2048 + 1024>(pf[1][I.v], pb + w * 16384 + (I.v >> 1) * 4096, lane * 16); gap<TG2>(slow); });
        gld16c<1024>(pf[1][8], pb + 65536 + w * 4096, lane * 16); gld16c<3072>(pf[1][9], pb + 65536 + w * 4096, lane * 16);
        WAIT10(10, pf[0]); WAIT10(10, af[0]); WAIT10(10, af[1]);
        float chk = 0.f;
#pragma unroll
        for (int tt = 0; tt < 2; ++tt) {
            if (tt) WAIT10(0, pf[1]);
            f32x4 c0 = f32x4{0.f, 0.f, 0.f, 0.f}, c1 = c0;
#pragma unroll
            for (int i = 0; i < 10; ++i) { c0 = mfma16(af[0][i], pf[tt][i], c0); c1 = mfma16(af[1][i], pf[tt][i], c1); }
            *LDSP(f32x4, wa + tt * 2304) = c0;
            *LDSP(f32x4, wa + tt * 2304 + 64) = c1;
            const f32x4 c = c0 + c1;
            chk += (c[0] + c[1]) + (c[2] + c[3]);
            PIN();
        }
        if (it >= 4 || __builtin_amdgcn_ballot_w64((__float_as_uint(chk) & 0x7fffffffu) > 0x7f800000u) == 0) break;
        if (s < NSEG - 1) nap(60);
    }
    barrier();
    const u32 ra = lds + (u32)(tid >> 3) * 144 + (tid & 7) * 16;
    const float gt = __uint_as_float(gv), g1 = rbf(1.f - gt);
    const f32x4 z = (*LDSP(f32x4, ra) + *LDSP(f32x4, ra + 4608)) + (*LDSP(f32x4, ra + 2 * 4608) + *LDSP(f32x4, ra + 3 * 4608));
    u32x2 ov;
#pragma unroll
    for (int j = 0; j < 2; ++j) {
        const u32 b = bb[j], a = cvtpk(z[2 * j], z[2 * j + 1]);
        const u32 ga = cvtpk(gt * __uint_as_float(a << 16), gt * __uint_as_float(a & 0xffff0000u));
        const u32 gb = cvtpk(g1 * __uint_as_float(b << 16), g1 * __uint_as_float(b & 0xffff0000u));
        ov[j] = cvtpk(__uint_as_float(ga << 16) + __uint_as_float(gb << 16), __uint_as_float(ga & 0xffff0000u) + __uint_as_float(gb & 0xffff0000u));
    }
    *(u32x2*)(p.out + tok * 1280 + col * 2) = ov;
}

DI float dot2bf(u32 a, u32 b, float c) { return __builtin_amdgcn_fdot2_f32_bf16(__builtin_bit_cast(bf16x2, a), __builtin_bit_cast(bf16x2, b), c, false); }
DI void battn_task(const Ptrs& p, int t0, u32 lds, int w) {
    const int lane = lanex(), tid = 64 * w + lane, tl = tid & 31, t = t0 + tl;
    const char* const qkv = p.ws + OFF_QKVB + (size_t)(t0 >> 5) * 122880;
    {
        u32 so[3];
#pragma unroll
        for (int i = 0; i < 3; ++i) {
            const int e = 64 * i + lane, sl = e % 48;
            so[i] = (u32)((e / 48) * 512 + (sl < 16 ? (t0 ? 0 : 122880) + 256 + sl * 16 : 122880 - 256 + sl * 16));
        }
#pragma unroll
        for (int i = 0; i < 30; ++i) { glds16c(qkv - 122880 + 40960 + (10 * w + i / 3) * 2048, so[i % 3], lds + (30 * w + i) * 1024); gap<SMB>(-1); }
    }
    u32x4 q[2][5], xv[10], gv[10];
    sfor<0, 10>([&](auto I) { gld16x<(I.v % 5) * 512>(q[I.v / 5][I.v % 5], qkv + (I.v / 5) * 20480, (u32)((tid >> 5) * 2560 + tl * 16)); gap<SMB>(-1); });
    sfor<0, 10>([&](auto I) { gld16<I.v * 128>(xv[I.v], p.x + (size_t)t0 * 1280, (u32)((tid >> 3) * 1280 + (tid & 7) * 16)); gld16<I.v * 128>(gv[I.v], p.gw, (u32)((tid & 7) * 16)); gap<SMB>(-1); });
    sfor<0, 10>([&](auto I) { waitv<0>(q[I.v / 5][I.v % 5]); waitv<0>(xv[I.v]); waitv<0>(gv[I.v]); });
    barrier();
    {
        float a = 0.f;
#pragma unroll
        for (int i = 0; i < 10; ++i)
#pragma unroll
            for (int j = 0; j < 4; ++j) {
                const u32 r = cvtpk(__uint_as_float(xv[i][j] << 16) * __uint_as_float(gv[i][j] << 16), __uint_as_float(xv[i][j] & 0xffff0000u) * __uint_as_float(gv[i][j] & 0xffff0000u));
                a += __uint_as_float(r << 16) + __uint_as_float(r & 0xffff0000u);
            }
        a += __shfl_xor(a, 1); a += __shfl_xor(a, 2); a += __shfl_xor(a, 4);
        const float z = rbf(a);
        if ((tid & 7) == 0) gst4c(p.ws + OFF_GATE, (u32)((t0 + (tid >> 3)) * 4), __float_as_uint(rbf(1.0f / (1.0f + __expf(-z)))));
    }
#pragma unroll
    for (int it = 0; it < 2; ++it) {
        const int m = (tid >> 5) + 8 * it;
        u32 ka = lds + (u32)(m * 3840 + (2 + tl) * 16), va = ka + 61440;
        asm volatile("" : "+v"(ka), "+v"(va));
        u32x4 k[5][8];
#pragma unroll
        for (int i = 0; i < 40; ++i) k[i >> 3][i & 7] = ldsr16(ka + (i >> 3) * 768 + (7 - (i & 7)) * 32);
        PIN();
        float lg[2][8], mx[2] = {-3.0e38f, -3.0e38f};
#pragma unroll
        for (int i = 0; i < 16; ++i) lg[i >> 3][i & 7] = 0.f;
#pragma unroll
        for (int i = 0; i < 5; ++i) {
#pragma unroll
            for (int j = 0; j < 4; ++j)
#pragma unroll
                for (int wv = 0; wv < 8; ++wv) lg[4 * i + j >= 10][wv] = dot2bf(q[it][i][j], k[i][wv][j], lg[4 * i + j >= 10][wv]);
            PIN();
#pragma unroll
            for (int wv = 0; wv < 8; ++wv) k[i][wv] = ldsr16(va + i * 768 + (7 - wv) * 32);
            PIN();
        }
#pragma unroll
        for (int i = 0; i < 16; ++i) {
            float& l = lg[i >> 3][i & 7];
            l = rbf(rbf(l) * 0.22360679774997896f);
            asm volatile("" : "+v"(l));
            l = t - 2 * (i & 7) >= 0 ? l : -3.0e38f;
            mx[i >> 3] = fmaxf(mx[i >> 3], l);
        }
        u32 pk[2][4];
#pragma unroll
        for (int u = 0; u < 2; ++u) {
            float sum = 0.f;
#pragma unroll
            for (int wv = 0; wv < 8; ++wv) { lg[u][wv] = __builtin_amdgcn_exp2f((lg[u][wv] - mx[u]) * 1.4426950408889634f); sum += lg[u][wv]; }
            const float inv = __builtin_amdgcn_rcpf(sum);
#pragma unroll
            for (int wv = 0; wv < 4; ++wv) pk[u][wv] = cvtpk(lg[u][2 * wv] * inv, lg[u][2 * wv + 1] * inv);
        }
#pragma unroll
        for (int i = 0; i < 5; ++i) {
            float o[8];
#pragma unroll
            for (int d = 0; d < 8; ++d) o[d] = 0.f;
#pragma unroll
            for (int wp = 0; wp < 4; ++wp) {
                const u32x4 v0 = k[i][2 * wp], v1 = k[i][2 * wp + 1];
#pragma unroll
                for (int j = 0; j < 4; ++j) {
                    const u32 pw = pk[4 * i + j >= 10][wp];
                    o[2 * j] = dot2bf(__builtin_amdgcn_perm(v1[j], v0[j], 0x05040100u), pw, o[2 * j]);
                    o[2 * j + 1] = dot2bf(__builtin_amdgcn_perm(v1[j], v0[j], 0x07060302u), pw, o[2 * j + 1]);
                }
            }
            gst16c(p.ws + OFF_OB + (size_t)(t0 >> 5) * 40960 + 512 * i, (u32)(m * 2560 + tl * 16), u32x4{cvtpk(o[0], o[1]), cvtpk(o[2], o[3]), cvtpk(o[4], o[5]), cvtpk(o[6], o[7])});
        }
    }
}

#define XPF 1
#define PFQ 0
#define PFN 8
#define OLAG 3
constexpr int QN = 176, QF = 19;
struct QTab { u16 e[QN]; int n, nf; u32 tt[64]; };
typedef const __attribute__((address_space(4))) int* KI;
constexpr int QT_OFF = (88 + 48) / 4;
DI int pick_task(char* fl, KI k, int gr) {
    u32 r;
    asm volatile("s_mov_b64 exec, 1\n\ts_nop 3\n\tglobal_atomic_add %0, %1, %2, %3 sc0 sc1\n\ts_mov_b64 exec, -1\n\ts_waitcnt vmcnt(0)"
                 : "=&v"(r) : "v"((F_QH + gr) * 128), "v"(1), "s"(fl) : "memory");
    const int x = uni((int)r) + 1 + QF;
    if (x >= k[QT_OFF + QN / 2]) return -1;
    const u32 e2 = (u32)k[QT_OFF + (x >> 1)];
    return (int)(x & 1 ? e2 >> 16 : e2 & 0xffffu);
}
__global__ __launch_bounds__(256, 1) void k_main(Ptrs p0, ChainCfg cfg, QTab tab) {
    extern __shared__ __attribute__((aligned(16))) char smem[];
    u32 lds = (u32)(size_t)LDSP(char, smem);
#define HALF 0
    int n = HALF ? (blockIdx.x & 4) * 32 + (int)(blockIdx.x >> 3) * 4 + (blockIdx.x & 3) : blockIdx.x, gr = n & 7;
    int w = uni(threadIdx.x >> 6);
    {
        u64 pc; int len;
        asm volatile("s_getpc_b64 %0\n.Lkm_first:\n\ts_mov_b32 %1, .Lkm_last-.Lkm_first\n\ts_max_i32 %1, %1, .Lkm_lu-.Lkm_first\n\ts_max_i32 %1, %1, .Lkm_lv-.Lkm_first\n\t"
                     "s_max_i32 %1, %1, .Lkm_g2-.Lkm_first\n\ts_max_i32 %1, %1, .Lkm_g4-.Lkm_first" : "=s"(pc), "=s"(len) : : "scc");
        if (w == 3) pfl((const char*)pc, (u32)min((int)(blockIdx.x >> 3) * 8192 + (int)(threadIdx.x & 63) * 128, max(len, 0)), lds);
    }
    karg_warm();
    if (n < NH) { score_role(p0, cfg, n, lds, w); return; }
    if (n < NS) { proj_role(p0, cfg, n - NH, lds, w); return; }
    const bool isv = n < NS + NVB;
#define qslot (lds + GRING * GBUF)
    int sdw = -1;
    int en = isv ? Q_VD + (n & 24) : tab.e[(n - NS - NVB) >> 3] & 511;
    u32 phi0 = 0;
    Sync y;
    if (XFW && n < 160) {
        const int h = gr + (en & 24);
#pragma unroll
        for (int i = 0; i < XFW; ++i) { const int l = (i * 4 + w) * 64 + lanex(); pfl(((en & 7) == Q_VD ? p0.wv : p0.wk) + h * 384, (u32)((l / 3) * 12288 + (l % 3) * 128), lds); }
        if (w == 1) {
            sync_ld(y, p0.ws);
            waitvm<0>();
            asm volatile("" : "+v"(y.r), "+v"(y.g), "+v"(y.e));
            xcopy_wait(p0.ws, sync_tag(y.e), 0, 3);
        }
        barrier();
    }
    if (!XFW && XPF && n < 160 && w >= 2) pfl(p0.x, (u32)min(((n - NS) >> 3) * 128 + (w & 1) * 64 + lanex(), 1279) * 128, lds);
    if (n >= 160) {
        const int lane = lanex();
        if (n >= 192 && (n & 8)) {
            const int j = (n & 7) + ((n - 192) >> 4) * 8;
            init_task(p0, j, lds, w);
            barrier();
#pragma unroll
            for (int i = 0; i < 3; ++i) pfl(p0.wqkvb, (u32)min(j * 768 + i * 256 + w * 64 + lane, 19199) * 128, lds);
            pfl(j < 25 ? p0.wob : p0.gw, j < 25 ? (u32)(j * 256 + w * 64 + lane) * 128 : (u32)min(lane, 9) * 128, lds);
        } else {
            const int h = n < 192 ? n & 31 : (n & 7) + ((n - 192) >> 4) * 8;
            xcopy_task(p0, h + (n < 192 ? 0 : 32), w);
            if (n < 192) {
#pragma unroll
                for (int i = 0; i < PFN; ++i) {
                    const int l = 64 * min(i * 4 + w, 29) + lane;
                    pfl(p0.wo + (size_t)h * 245760, (u32)(l * 128), lds);
                    if (i >= 8 - PFQ) pfl(p0.wq + h * 384, (u32)((l / 3) * 12288 + (l % 3) * 128), lds);
                }
            }
        }
        if (w == 1) {
            sync_ld(y, p0.ws);
            waitvm<0>();
            asm volatile("" : "+v"(y.r), "+v"(y.g), "+v"(y.e));
            if (n < 192) xcopy_wait(p0.ws, sync_tag(y.e));
            else xcopy_wait(p0.ws, sync_tag(y.e), n & 8 ? 4 : 8, n & 8 ? 3 : 7);
        }
        if (w == 0 && n < 192) {
            sync_ld(y, p0.ws);
            waitvm<0>();
            *LDSP(u32, qslot) = (u32)sync_end(y, p0.ws, phi0, false);
        }
        barrier();
        if (w == 0 && n < 192) flag_add(p0.ws + OFF_ST, F_ACK, 1);
        if (n < 192) sdw = uni((int)*LDSP(u32, qslot));
        barrier();
    }
    while (true) {
        typedef char* const __attribute__((address_space(4)))* KP;
        KP kp = (KP)__builtin_amdgcn_kernarg_segment_ptr();
        asm volatile("" : "+s"(kp), "+s"(lds), "+s"(w), "+s"(gr), "+s"(n));
        __builtin_assume(w >= 0 && w < 4 && gr >= 0 && gr < 8);
        const Ptrs p{kp[0], kp[1], kp[2], kp[3], kp[4], kp[5], kp[6], kp[7], kp[8], kp[9], kp[10]};
        const int ty = en & 7, gate = en >> 9;
        const bool fu = ty == Q_PX && (en >> 3 & 63) < 8;
        const int ix = fu ? (en & 504) + gr : en >> 3 & 63, sf = ((KI)kp)[QT_OFF + QN / 2 + 1] & 255;
        char* const fl = p.ws + OFF_ST;
        const bool raw = sdw < 0;
        int t0 = gate - 1, f1 = F_ONE, t1 = 0, f2 = F_ONE, t2 = 0, f3 = F_ONE, t3 = 0, dt = cfg.T2;
        const bool o2 = ty == Q_OUT && ix < 16;
        const int os = o2 ? sf + 8 * (ix >> 2) + gr : (ix >= 40 ? 44 + (8 * ix + gr) / 20 : (8 * ix + gr) / 5);
        const int bb = ty == Q_BO ? (8 * ix + gr) >> 2 : os;
        if (ty == Q_BA) { t0 += gr * BAS >> 3; f1 = F_B + (gr & RM) | ix << 16; t1 = 5; f2 = F_B + (gr & RM) | max(ix - 1, 0) << 16; t2 = 5; }
        if (ty == Q_BO) { f1 = F_B + (gr & RM) | (8 + bb) << 16; t1 = 8; }
        if (ty == Q_PX) { f1 = F_Q + pblk(ix) * 8 + (fu ? gr & RM : gr) | fu << 16; t1 = fu ? 32 : 4; if (fu) t0 += gr * (((KI)kp)[QT_OFF + QN / 2 + 1] >> 8) >> 3; if (ix < PXT) t0 = max(t0, ix + 1); }
        if (ty == Q_OUT) { f3 = F_B + (gr & RM) | (16 + (os >> 3)) << 16; t3 = 4; if (os < 60) { f1 = F_PX + os; t1 = 8; t0 = max(t0, os + (os < PXT ? OLAG : 1)); if (os >= PXT) dt = TDT; } else { f1 = F_VD + 64 + (gr & RM); t1 = 32; } }
        const bool skip = fu ? ix >= sf : ty == Q_OUT && ix < 40 && !o2 && (os < sf + 8 * NO2 || os >= 60);
        if ((gate || ty >= Q_BA) && !skip) wg_wait(fl, w, F_CUR + gr, t0, f1, t1, f2, t2, f3, t3, dt, false);
        int sig = F_END, nrep = 1;
        if (skip) {}
        else if (ty <= Q_QKVB || ty == Q_BO) {
            const bool ab = ty <= Q_KQ;
            sig = ty == Q_VD ? F_VD + (ix >> 2) * 8 : (ty == Q_KQ ? F_Q + (ix >> 2) * 8 : (ix & 4 ? F_Q : F_K + 8 * ix)) + gr;
            if (!ab) sig = F_B | (bb + (ty == Q_BO ? 16 : 0)) << 16;
            nrep = ty == Q_VD || !ab ? RM + 1 : 1;
            if (ty == Q_VD && ix < 4) { sig += n & 32 ? 4 : 0; nrep = 4; }
            run_gemm(p, ty, ab ? ix >> 2 : bb, ab ? gr + 8 * (ix & 3) : 8 * ix + gr - (ty == Q_BO ? 4 : 5) * bb, (n >> 5) & 1, !XFW && raw && n < 160, y, lds, w);
            if (raw) sdw = sync_end(y, p.ws, phi0, w == 0);
        } else {
            if (ty == Q_BA) {
                battn_task(p, (8 * ix + gr) * 32, lds, w);
                sig = F_B | (8 + ix) << 16; nrep = RM + 1;
            } else if (ty == Q_PX) {
                if (ix < PXT) {
#pragma nounroll
                    for (int i = fu ? 0 : gr; i < (fu ? 8 : gr + 1); ++i) px_wave<false>(p, cfg, ix, i + 8 * w, ix - cfg.fast, w, lds, fu);
                } else px_wave<true>(p, cfg, ix, gr + 8 * w, -1, w, lds, false);
                sig = F_PX + ix;
                if (fu) wg_wait(fl, w, F_ONE, 0, F_B + (gr & RM) | (16 + (ix >> 3)) << 16, 4, F_ONE, 0, F_ONE, 0, dt, false);
            }
            if (ty == Q_OUT || fu) {
                const int cg = 8 * ix + gr - 5 * os;
                if (ix >= 40 && !fu) out_tail(p, cfg, os, 8 * ix + gr - 20 * (os - 44), lds, w, sdw, gr);
                else out_task(p, fu ? ix : os, fu ? 0 : (o2 ? OW * (ix & 3) : cg), fu ? 5 : (o2 ? min(OW * (ix & 3) + OW, 5) : cg + 1), fu, lds, (fu ? ix : os) + 1 - cfg.fast, w, sdw);
                sig = F_END;
            }
            waitvm<0>();
        }
        barrier();
        if (w == 0 && sig != F_END) flag_add(fl, sig, nrep);
        if (w == 1 && sig >= F_Q && sig < F_N) flag_add(fl, sig - gr | 1 << 16, RM + 1);
        if (raw && !isv && w == 0) xcopy_wait(p.ws, sync_tag(y.e));
        if (isv) break;
        if (w == 0) *LDSP(int, qslot) = pick_task(fl, (KI)kp, gr);
        barrier();
        en = uni(*LDSP(int, qslot));
        if (en < 0) return;
        barrier();
    }
    value_role(p0, cfg, n - NS, sdw, phi0, lds, w);
}

#define QCFG 14, 11, 3, 5, 0, 50, 36
#define LVDEF "00?>>=<<;;;<<<<;;::98888999:::::999999:::99888998888888888888886"
constexpr int DX = 280, DUA = 400, DUB = 30, DUC = 2;
static ChainCfg dev_cfg(ChainCfg c) { c.T = PDL; c.cR = ODL; c.s1 = 190 << 8 | 185 << 16; c.T2 = WSLEEP; c.cR2 = DX; c.dU = DUA; c.s2 = DUB; c.T3 = DUC; c.dV |= 4 << 8 | 8 << 16 | 0 << 24; c.cR3 = 4 | 4 << 8 | 0 << 16; return c; }
static QTab set_sched(QTab t, const ChainCfg c, const char* lv) {
    for (int s = 0; s < 64; ++s) {
        const int d = lv[s] - '0';
        t.tt[s] = (u32)((s < c.s1 ? c.T : (s < c.s2 ? c.T2 : c.T3)) + 4 * d) | (u32)((s < c.s1 ? c.cR : (s < c.s2 ? c.cR2 : c.cR3)) + 5 * d / 2) << 16;
    }
    t.tt[0] = (u32)236 | (u32)165 << 16;
    t.tt[1] = (u32)212 | (u32)146 << 16;
    return t;
}
#define QORD "bbV3qbK3s41x40bx51bx60x71s80"
#define GATES "11 13 15 18 25 28 41 43 21 26 36 49 14 18 22 27 33 41"
static QTab make_tab(int lv, int lk, int b0, int d1, int d2, int lg, int nf) {
    QTab t{};
    int n = 0, dl[QN], nq = 0;
    auto add = [&](int ty, int ix) { t.e[n] = (u16)(ty | ix << 3); dl[n++] = ty == Q_VD || ty == Q_KQ ? 8 * (ix / 4 - 1) - (ty == Q_VD ? lv : lk) - (ix >= 32 ? 0 : 0) : 99; };
    for (int i = 1; i < 4; ++i) add(Q_VD, i);
    for (int i = 0; i < 16; ++i) add(i < 4 ? Q_H : ((i & 8) ? Q_VD : Q_KQ), i < 8 ? i : 8 - (i & 1) * 4 + ((i - 8) >> 1));
    for (int i = 0; i < 4; ++i) add(Q_KQ, 8 + i);
    for (const char* c = QORD; *c; ++c) {
        if (*c == 'b') add(Q_QKVB, nq++);
        else if (*c == 'q') for (int i = 4; i < 8; ++i) add(Q_H, i);
        else if (*c == '.') dl[n - 1] |= 1 << 20;
        else if (*c == 'w') { const int b = *++c - '0'; for (int i = 0; i < 16; ++i) add(i & 2 ? Q_KQ : Q_VD, 4 * (b + (i & 1)) + (i >> 2)); }
        else if (*c == 'x') { const int b = c[1] - '0', o = 2 * (c[2] - '0'); c += 2; for (int i = 0; i < 8; ++i) add(i & 2 ? Q_KQ : Q_VD, 4 * (b + (i & 1)) + o + (i >> 2)); }
        else if (*c == 's') { const int b = c[1] - '0', o = 2 * (c[2] - '0'); c += 2; for (int i = 0; i < 4; ++i) add(i & 1 ? Q_KQ : Q_VD, 4 * b + o + (i >> 1)); }
        else if (*c == 'a' || *c == 'c') { add(*c == 'a' ? Q_VD : Q_KQ, 10 * (c[1] - '0') + c[2] - '0'); c += 2; }
        else if (*c == 'n') { const int b = *++c - '0'; for (int i = 0; i < 8; ++i) add(i & 1 ? Q_KQ : Q_VD, 4 * b + (i >> 1) - (i & 1) * 2); }
        else if (*c == 'h' || *c == 'e') { const int b = 4 * (c[1] - '0') + (*c == 'e') * 2; ++c; add(Q_KQ, b); add(Q_KQ, b + 1); }
        else if (*c == 'm') { const int b = c[1] - '0', b2 = c[2] - '0'; c += 2; for (int i = 0; i < 8; ++i) add(i & 1 ? Q_KQ : Q_VD, 4 * (i & 1 ? b2 : b) + ((i >> 1) ^ (i & 2 ? 0 : 0))); }
        else {
            const int ty = (*c | 32) == 'v' ? Q_VD : Q_KQ, two = *c >> 5 & 1, b = *++c - '0';
            for (int i = 0; i < (4 << two); ++i) add(ty, 4 * (b + (i & two)) + (i >> two));
        }
    }
    const int gap = 8;
    for (int i = n - 1, m = 999; i >= 0; --i) { const int pz = dl[i] > 999; m = min(8 * (pz ? dl[i] - (1 << 20) : dl[i]), m - gap - 8 * pz); dl[i] = max(m, 0); }
    auto put = [&](int ty, int ix, int g, int pos) {
        int i = n++;
        for (; dl[i - 1] > pos; --i) { t.e[i] = t.e[i - 1]; dl[i] = dl[i - 1]; }
        t.e[i] = (u16)(ty | ix << 3 | (g + 1) << 9); dl[i] = pos;
    };
    for (int i = 0; i < n; ++i) if (dl[i] >= 8) t.e[i] |= (u16)((dl[i] / 8 + 1) << 9);
    const char* gs = GATES;
    auto gt = [&](int i) { return 10 * (gs[3 * i] - '0') + gs[3 * i + 1] - '0'; };
    for (int i = 0; i < 8; ++i) put(Q_BA, i, gt(i), 8 * gt(i));
    for (int i = 0; i < 4; ++i) put(Q_BO, i, gt(8 + i), 8 * gt(8 + i));
    for (int i = 0; 8 * i < nf; ++i) put(Q_PX, i, gt(12 + i), 8 * (max(gt(12 + i), 8 * i + 1) + b0));
    auto pg = [&](int s) { return max(8 * s + 8, 8 * 46 + (s - nf) * 3); };
    for (int s = nf; s < NSEG; ++s) { const int g = s < PXT ? pg(s) : 8 * max(s - 3, lg); put(Q_PX, s, g / 8, g); }
    for (int k = 0; k < NO2; ++k) for (int c = 0; c * OW < 5; ++c) put(Q_OUT, 4 * k + c, pg(nf + 8 * k + 7) / 8 + OLAG - 1, pg(nf + 8 * k + 7) + 8 * (OLAG - 1));
    for (int x = 5 * (nf + 8 * NO2) / 8; x < 38; ++x) { const int os = (8 * x + 4) / 5, g = pg(min((8 * x + 7) / 5, PXT - 1)); put(Q_OUT, x, g > 8 * ((8 * x + 7) / 5) + 8 ? g / 8 + OLAG - 1 : 0, os < PXT ? pg(os) + 8 * (OLAG - 1) : 8 * (os + 1)); }
    for (int x = 40; x < 50; ++x) { const int g = max(44 + 8 * x / 20 - 3, lg); put(Q_OUT, x, g, 8 * g + 4); }
    t.n = n; t.nf = nf | d1 << 8;
    return t;
}

extern "C" void kernel_launch(void* const* d_in, const int* in_sizes, int n_in, void* d_out, int out_size, void* d_ws,
                              size_t ws_size, hipStream_t stream) {
    Ptrs p{(const char*)d_in[0], (const char*)d_in[1], (const char*)d_in[2], (const char*)d_in[3], (const char*)d_in[4], (const char*)d_in[5],
           (const char*)d_in[8], (const char*)d_in[9], (const char*)d_in[10], (char*)d_out, (char*)(((size_t)d_ws + 0x1fffff) & ~(size_t)0x1fffff)};
    (void)hipFuncSetAttribute((const void*)k_main, hipFuncAttributeMaxDynamicSharedMemorySize, S_LDS);
    k_main<<<256, 256, S_LDS, stream>>>(p, dev_cfg(ChainCfg{CFG}), set_sched(make_tab(QCFG), ChainCfg{CFG}, LVDEF));
    static_assert(sizeof(Ptrs) + sizeof(ChainCfg) + sizeof(QTab) == TT_OFF + 256, "");
}
```

```cpp
#include <hip/hip_runtime.h>
#pragma clang diagnostic ignored "-Wint-to-pointer-cast"

typedef unsigned short u16;
typedef unsigned int u32;
typedef unsigned long long u64;
typedef __bf16 bf16x8 __attribute__((ext_vector_type(8)));
typedef __bf16 bf16x16 __attribute__((ext_vector_type(16)));
typedef short s16x4 __attribute__((ext_vector_type(4)));
typedef float f32x4 __attribute__((ext_vector_type(4)));
typedef unsigned u32x4 __attribute__((ext_vector_type(4)));
typedef unsigned u32x2 __attribute__((ext_vector_type(2)));
typedef unsigned u32x8 __attribute__((ext_vector_type(8)));
typedef unsigned u32x16 __attribute__((ext_vector_type(16)));
typedef unsigned u32x32 __attribute__((ext_vector_type(32)));
#define DI __device__ __forceinline__
#define LDSP(T, a) ((__attribute__((address_space(3))) T*)(a))

constexpr int S_LEN = 2048, DM = 640, NH = 32, DH = 192, NSEG = 64, HD = NH * DH;
#define NSAMP 4
constexpr int NREG = 13;
constexpr int NV = 80, NVB = 40;
constexpr int NS = 64;
#define CFG 147, 103, 98, 20, 62, 16, 147, 103, 52, 147, 103, 6

constexpr size_t U_STRIDE = NV * 4096, D_STRIDE = NV * 16384;
constexpr size_t HO_STRIDE = 65536;
constexpr size_t OFF_ST = NSEG * HO_STRIDE;
enum { F_ACK = 0, F_RDY = 1, F_ONE = 3, F_GO = 8, F_VD = 16, F_QH = 96, F_B = 104, F_CUR = 128, F_PROG = 136 - 8 * 62, F_UD = 152, F_PX = 160, F_K = 224, F_END = 512, F_Q = 520, F_N = 592 };
constexpr size_t OFF_LAT = OFF_ST + F_N * 128;
constexpr size_t OFF_KX = 5u << 20;
constexpr size_t OFF_QX = OFF_KX + (size_t)S_LEN * HD * 2;
constexpr size_t OFF_D = OFF_QX + (size_t)S_LEN * HD * 2;
constexpr size_t OFF_U = (OFF_D + NSEG * D_STRIDE + 0x1fffff) & ~(size_t)0x1fffff;
constexpr size_t OFF_KM = OFF_U + (22u << 20);
constexpr size_t OFF_PX = OFF_KM + (size_t)NSEG * NH * 3072;
constexpr size_t OFF_QKVB = OFF_PX + (size_t)NSEG * 81920;
constexpr size_t OFF_OB = OFF_QKVB + (size_t)S_LEN * 1920 * 2;
constexpr size_t OFF_BOUT = OFF_OB + (size_t)S_LEN * DM * 2;
constexpr size_t OFF_GATE = OFF_BOUT + (size_t)S_LEN * DM * 2;
constexpr size_t OFF_XF = OFF_GATE + S_LEN * 4;
constexpr size_t WS_END = OFF_XF + (size_t)S_LEN * DM * 2;
static_assert(WS_END + (2u << 20) <= (256u << 20), "workspace");

typedef __bf16 bf16x2 __attribute__((ext_vector_type(2)));
typedef float f32x2 __attribute__((ext_vector_type(2)));
DI u32 cvtpk(float lo, float hi) { return __builtin_bit_cast(u32, __builtin_convertvector(f32x2{lo, hi}, bf16x2)); }
DI float rbf(float f) { return __uint_as_float(cvtpk(f, f) << 16); }
DI f32x4 mfma16(u32x4 a, u32x4 b, f32x4 c) {
    return __builtin_amdgcn_mfma_f32_16x16x32_bf16(__builtin_bit_cast(bf16x8, a), __builtin_bit_cast(bf16x8, b), c, 0, 0, 0);
}
DI u32x2 trread(u32 a) {
    return __builtin_bit_cast(u32x2, __builtin_amdgcn_ds_read_tr16_b64_v4i16(LDSP(s16x4, a)));
}
DI void glds16c(const char* sbase, u32 voff, u32 lds) {
    u32 sv;
    asm volatile("s_mov_b32 %0, m0\n\ts_mov_b32 m0, %3\n\ts_nop 0\n\tglobal_load_lds_dwordx4 %1, %2 sc1\n\ts_mov_b32 m0, %0"
                 : "=&s"(sv) : "v"(voff), "s"(sbase), "s"(lds) : "memory");
}
template <int IMM>
DI void gld16(u32x4& r, const char* sbase, u32 voff) {
    asm volatile("s_nop 4\n\tglobal_load_dwordx4 %0, %1, %2 offset:%3" : "=v"(r) : "v"(voff), "s"(sbase), "i"(IMM) : "memory");
}
DI void gst16(char* sbase, u32 voff, u32x4 v) {
    asm volatile("global_store_dwordx4 %0, %1, %2 sc1\n\ts_nop 1" : : "v"(voff), "v"(v), "s"(sbase) : "memory");
}
#define OST " nt"
DI void gst16p(char* sbase, u32 voff, u32x4 v) {
    asm volatile("s_nop 4\n\tglobal_store_dwordx4 %0, %1, %2" OST "\n\ts_nop 1" : : "v"(voff), "v"(v), "s"(sbase) : "memory");
}
DI void gst8(char* sbase, u32 voff, u32x2 v) {
    asm volatile("global_store_dwordx2 %0, %1, %2 sc1\n\ts_nop 1" : : "v"(voff), "v"(v), "s"(sbase) : "memory");
}
template <int N>
DI void waitvm() { asm volatile("s_waitcnt vmcnt(%0)" : : "i"(N) : "memory"); }
DI void barrier() { asm volatile("s_waitcnt lgkmcnt(0)\n\ts_barrier" : : : "memory"); }
#define PIN() __builtin_amdgcn_sched_barrier(0)
DI int uni(int v) { return __builtin_amdgcn_readfirstlane(v); }
DI int lanex() {
    int t;
    asm volatile("v_mbcnt_lo_u32_b32 %0, -1, 0\n\tv_mbcnt_hi_u32_b32 %0, -1, %0" : "=v"(t));
    __builtin_assume(t >= 0 && t < 64);
    return t;
}
DI void flag_ld(u32& r, const char* fl, int idx) {
    asm volatile("s_nop 4\n\tglobal_load_dword %0, %1, %2 sc1" : "=v"(r) : "v"((idx & 0xffff) * 128 + (idx >> 14)), "s"(fl) : "memory");
}
DI void flag_ldm(u32& r, const char* fl, int idx, bool on) {
    asm volatile("v_mov_b32 %0, 1000\n\ts_mov_b32 exec_lo, %3\n\ts_mov_b32 exec_hi, 0\n\tglobal_load_dword %0, %1, %2 sc1\n\ts_mov_b64 exec, -1" : "=&v"(r) : "v"(idx * 128), "s"(fl), "s"(uni(on ? 1 : 0)) : "memory");
}
DI void flag_wait(const char* fl, int idx, int target) {
#pragma nounroll
    for (int it = 0; it < (1 << 16); ++it) {
        u32 v;
        flag_ld(v, fl, idx);
        asm volatile("s_waitcnt vmcnt(0)" : "+v"(v) : : "memory");
        if (uni((int)(v + 1u)) >= target) break;
        __builtin_amdgcn_s_sleep(6);
    }
}
DI void flag_add(char* fl, int idx, int nrep) {
    const int lane = lanex();
    if (lane < nrep) asm volatile("s_nop 4\n\tglobal_atomic_add %0, %1, %2 sc1" : : "v"(((idx & 0xffff) + lane) * 128 + (idx >> 14)), "v"(1), "s"(fl) : "memory");
}
DI void acquire() { asm volatile("buffer_inv sc1\n\ts_waitcnt vmcnt(0)" : : : "memory"); }

template <int V> struct IC { static constexpr int v = V; };
template <int B, int E, class Fn>
DI void sfor(Fn&& fn) { if constexpr (B < E) { fn(IC<B>{}); sfor<B + 1, E>(fn); } }
enum { T_QK = 0, T_VD = 1, T_BL = 3 };
constexpr int GBUF = 24576, GRING = 6;
struct GemmArgs {
    const char *w1, *w1b;
    const char* w2;
    const char* x;
    char *out, *outb;
    int ldw;
    char* sig;
    int type, np1, tn;
    int sV, sP, sQ;
    int xb;
};
DI const char* unip(const char* p) {
    const u64 v = (u64)p;
    return (const char*)(((u64)(u32)uni((int)(v >> 32)) << 32) | (u32)uni((int)v));
}
template <int Z>
DI void mf1(f32x4& c, const u32x4& a, const u32x4& b) {
    if constexpr (Z) asm volatile("v_mfma_f32_16x16x32_bf16 %0, %1, %2, 0" : "=&v"(c) : "v"(a), "a"(b));
    else asm volatile("v_mfma_f32_16x16x32_bf16 %0, %1, %2, %0" : "+v"(c) : "v"(a), "a"(b));
}
template <int Z>
DI void mf2(f32x4& c, const u32x4& a, const u32x4& b) {
    if constexpr (Z) asm volatile("v_mfma_f32_16x16x32_bf16 %0, %1, %2, 0" : "=&v"(c) : "v"(a), "v"(b));
    else asm volatile("v_mfma_f32_16x16x32_bf16 %0, %1, %2, %0" : "+v"(c) : "v"(a), "v"(b));
}
template <int IMM>
DI void gld16a(u32x4& r, const char* sbase, u32 voff) {
    asm volatile("global_load_dwordx4 %0, %1, %2 offset:%3 sc1" : "+a"(r) : "v"(voff), "s"(sbase), "i"(IMM) : "memory");
}
DI void setm0(u32 v) { asm volatile("s_mov_b32 m0, %0" : : "s"(v) : "memory"); }
template <int IMM>
DI void gldsm(const char* sbase, u32 voff) {
    asm volatile("global_load_lds_dwordx4 %0, %1 offset:%2" : : "v"(voff), "s"(sbase), "i"(IMM) : "memory");
}
DI void gst16n(char* sbase, u32 voff, u32x4 v) {
    asm volatile("global_store_dwordx4 %0, %1, %2 sc1" : : "v"(voff), "v"(v), "s"(sbase) : "memory");
}
template <int N>
DI void waitbar() { asm volatile("s_waitcnt vmcnt(%0)\n\ts_barrier" : : "i"(N) : "memory"); }
#define LGKM(n) __builtin_amdgcn_s_waitcnt(0xC07F | ((n) << 8))

#define XSL 10
DI int xhalf() { u32 x; asm volatile("s_getreg_b32 %0, hwreg(HW_REG_XCC_ID)" : "=s"(x)); return (int)((x >> 2) & 1); }
struct Sync { u32x2 r, g, e; };
DI void sync_ld(Sync& y, const char* ws) {
    asm volatile("s_nop 4\n\tglobal_load_dwordx2 %0, %3, %5 sc1\n\tglobal_load_dwordx2 %1, %4, %5 offset:512 sc1\n\tglobal_load_dwordx2 %2, %4, %5 offset:1024 sc1"
                 : "=&v"(y.r), "=&v"(y.g), "=&v"(y.e) : "v"((lanex() & 31) * 8), "v"(0), "s"(ws + OFF_LAT) : "memory");
}
DI u64 sync_tag(u32x2 e) { return (((u64)e[1] << 32 | e[0]) + 1) << 1; }
DI int sync_end(Sync& y, const char* ws, u32& phi, bool ack) {
    asm volatile("" : "+v"(y.r), "+v"(y.g), "+v"(y.e));
#pragma nounroll
    for (int it = 0; it < (1 << 16); ++it) {
        const u64 t = sync_tag(y.e);
        if (__builtin_amdgcn_ballot_w64((y.r[0] & ~1u) != (u32)t || y.r[1] != (u32)(t >> 32) || y.g[0] != (u32)t) == 0) break;
        __builtin_amdgcn_s_sleep(XSL);
        sync_ld(y, ws);
        asm volatile("s_waitcnt vmcnt(0)" : "+v"(y.r), "+v"(y.g), "+v"(y.e) : : "memory");
    }
    if (ack) flag_add((char*)ws + OFF_ST, F_ACK, 1);
    phi = (u32)uni((int)y.g[1]);
    const u32 b = (u32)__builtin_amdgcn_ballot_w64(y.r[0] & 1), all = __builtin_popcount(b) > 16;
    u32 q = 0;
#pragma unroll
    for (int r = 0; r < NREG; ++r) {
        const u32 c = r < 2 ? __builtin_popcount(b & (0x55u << r)) + all : 2 * __builtin_popcount((b >> (4 + 2 * r)) & 3) + all;
        q |= ((c > 2) ^ (u32)((r < 2 ? r : r + (OFF_U >> 21)) & 1)) << r;
    }
    return uni((int)(q ^ 3u * xhalf()));
}
DI void pace(int on) {
    asm volatile("s_cmp_ge_i32 %0, 0\n\ts_cbranch_scc1 .Lp%=\n\ts_waitcnt vmcnt(0)\n.Lp%=:" : : "s"(on) : "memory", "scc");
}
#define SMO 48
#define RM 7
#define BLS 1
#define EPG 1
#define PXT 58
#define PDX 40
#define SPL 0
#define HP0 48
#define UPF 5
#define FRD 25
#define KNT 1
#define BAS 3
#define NO2 2
#define OW 2
#define GSL 6
#define UNAP 700
#define HD1 40
#define PRG 0
#define TG1 0
#define TG2 0
#define TGS 62
#define XFW 0
#define SMB 48
#define SMP SMO
template <int C>
DI void gap(int on) {
    if constexpr (C > 0) asm volatile("s_cmp_ge_i32 %0, 0\n\ts_cbranch_scc1 .Lg%=\n\ts_nop %1\n\ts_nop %2\n\ts_nop %3\n.Lg%=:" : : "s"(on), "n"(C / 12 - 1), "n"(C / 12 - 1), "n"(C / 12 - 1) : "memory", "scc");
}
DI void early_add(char* ctr, int c) {
    asm volatile("s_cmp_lg_u32 %1, 1\n\ts_cbranch_scc1 .Le%=\n\ts_mov_b64 exec, 1\n\ts_nop 1\n\tglobal_atomic_add %2, %3, %0 sc1\n\ts_mov_b64 exec, -1\n.Le%=:"
                 : : "s"(ctr), "s"(c), "v"(0), "v"(1) : "memory", "scc");
}
#define XL 2
template <int NT>
DI void gemm_task(const GemmArgs& g, Sync& y, const char* ws, u32 lds, int w) {
    constexpr int FD = 5, NX = 2 * NT, OPS = NX + 6, BI = 24 - FD;
    const int lane = lanex();
    const int n = lane & 15, q = lane >> 4, qq = (lane >> 2) & 3, pp = lane & 3;
    const bool p2 = g.type == T_VD;
    const int rw = lane >> 3, sp = (lane >> 1) & 3;
    const u32 v2b = (u32)(rw * 1280 + 16 * (lane & 1) + 5120);
    auto v2o = [&](int i) { return v2b + (u32)(((sp ^ (rw >> 1)) << 5) + i * 9216); };
    u32 wv[6];
#pragma unroll
    for (int i = 0; i < 6; ++i)
        wv[i] = (u32)((rw + 8 * (i / 3)) * g.ldw + ((sp ^ (((rw >> 1) & 1) + 2 * (i / 3))) << 5) + 16 * (lane & 1) + (i % 3) * 128 + 5120 - 1024 * i);
    const char* const wp2 = g.w2 + (size_t)(48 * w) * 1280 - 2048;
    const char* wp = unip(g.w1 + (size_t)(16 * w) * g.ldw - 2048);
    int wstep = uni(64 * g.ldw);
    u32 wdst = lds + (u32)w * 6144 + 3072;
    const u32 wdlast = wdst + (GRING - 1) * GBUF;
    auto wring = [&]() { wdst = wdst == wdlast ? wdst - (GRING - 1) * GBUF : wdst + GBUF; };
    auto wload = [&](int i) {
        setm0(wdst);
        wring();
        asm volatile("s_nop 0");
        sfor<0, 6>([&](auto I) { gldsm<1024 * (I.v - 3)>(wp, wv[I.v]); });
        wp += wstep;
    };
    u32x4 vf[NT][6];
    u32x4 fr[FD + 1];
    u32x4 xr[5][NX];
    auto xtie = [&](u32x4 (&x)[NX]) {
#pragma unroll
        for (int i = 0; i < NX; i += 4) asm volatile("" : "+a"(x[i]), "+a"(x[i + 1]), "+a"(x[i + 2]), "+a"(x[i + 3]));
    };
#pragma unroll
    for (int i = 0; i < 5 * NX; ++i) asm volatile("" : "=a"(xr[i / NX][i % NX]));
    u32x2 flo;
    u32 c[4], slot = 0;
    int sd = GBUF;
    auto sring = [&]() { sd = slot == (GRING - 1) * GBUF ? -(GRING - 1) * GBUF : GBUF; slot += (u32)sd; };
    {
        u32 xo[NT];
#pragma unroll
        for (int tt = 0; tt < NT; ++tt) {
            const int tok = 16 * (tt & 1) + n;
            xo[tt] = (u32)((NT / 2 * w + (tt >> 1)) * 40960 + (g.xb ? q * 512 + tok * 16 : q * 16 + tok * 1280));
        }
        f32x4 acc[12][NT];
        const bool rm = NT == 4 && g.type == T_BL;
        const int sw = (qq >> 1) + 2 * (q & 1);
#pragma unroll
        for (int t = 0; t < 4; ++t)
            c[t] = lds + q * 3072 + qq * 128 + (rm ? ((((t & 2) + (pp >> 1)) ^ sw) << 5) + 16 * (pp & 1) + 8 * ((pp >> 1) ^ (t & 1)) : ((t ^ sw) << 5) + 8 * pp);
        auto rlo = [&](int f) { flo = trread(c[f & 3] + (f / 12) * 12288 + ((f % 12) >> 2) * 1024); };
        auto rhi = [&](int f) {
            const u32x2 hi = trread(c[f & 3] + (f / 12) * 12288 + ((f % 12) >> 2) * 1024 + 512);
            fr[f % (FD + 1)] = u32x4{flo[0], flo[1], hi[0], hi[1]};
        };
        const int xk = uni(g.xb ? 2048 : 64);
        const char *xp = g.x, *xq = xp + xk;
        sfor<0, 4>([&](auto J) {
            if constexpr (J.v < XL) { sfor<0, NX>([&](auto I) { gld16a<0>(xr[J.v][I.v], I.v & 1 ? xq : xp, xo[I.v >> 1]); }); xp += 2 * xk; xq += 2 * xk; }
            wload(J.v);
            if constexpr (NT == 4 && PRG && J.v < 3) asm volatile("s_sleep %0" : : "n"(PRG));
        });
        waitbar<18 + (XL - 1) * NX>();
        xtie(xr[0]);
#pragma unroll
        for (int i = 0; i < FD; ++i) { rlo(i); rhi(i); }
        char* po = g.out;
#pragma nounroll
        for (int ph = g.np1; ph > 0; --ph) {
            sfor<0, 10>([&](auto J) {
                constexpr int j = J.v;
                u32x4 (&xc)[NX] = xr[j % 5];
                u32x4 (&xn)[NX] = xr[(j + XL) % 5];
                sfor<0, 24>([&](auto I) {
                    constexpr int idx = I.v, kk = idx / 12, T = idx % 12, z = j == 0 && kk == 0, f = (idx + FD) % 24;
                    constexpr int mo = NT == 4 ? (idx < BI && idx % 4 != 2 ? idx - (idx + 1) / 4 : -1) : (idx < BI && idx % 2 == 0 ? idx / 2 : -1);
                    auto mem = [&]() {
                        if constexpr (mo >= NX) gldsm<1024 * (mo - NX - 3)>(wp, wv[mo - NX]);
                        else if constexpr (mo >= 0) gld16a<0>(xn[mo], mo & 1 ? xq : xp, xo[mo >> 1]);
                    };
                    auto misc = [&]() {
                        if constexpr (idx == 1) wring();
                        if constexpr (idx == 13) sring();
                        if constexpr (j == 1 && idx == 22) early_add(g.sig, ph + w);
                        if constexpr (idx >= 15 && idx < BI) { c[idx - 15] += (u32)sd; asm volatile("" : "+v"(c[idx - 15])); }
                        const bool more = ph > 1;
                        if constexpr (j != 5) {
                            if constexpr (idx == 20) wp += wstep;
                        } else {
                            if constexpr (idx == 19) { wp = more ? g.w1b + (size_t)(16 * w) * g.ldw - 2048 : wp2; wstep = more ? wstep : (p2 ? 128 : 0); }
                            if constexpr (idx >= 20 && idx < 23) {
#pragma unroll
                                for (int i = 2 * (idx - 20); i < 2 * (idx - 19); ++i) { wv[i] = more ? wv[i] : (p2 ? v2o(i) : 5120u); asm volatile("" : "+v"(wv[i])); }
                            }
                        }
                        if constexpr (j != 9 - XL) {
                            if constexpr (idx == 21) xp += 2 * xk;
                            if constexpr (idx == 22) xq += 2 * xk;
                        } else if constexpr (idx == 23) {
                            xp = g.x; xq = xp + xk;
#pragma unroll
                            for (int i = 0; i < NT; ++i) { xo[i] = more ? xo[i] : 0u; asm volatile("" : "+v"(xo[i])); }
                        }
                    };
                    if constexpr (idx == BI) { waitbar<6 + (XL - 1) * OPS>(); xtie(xr[(j + 1) % 5]); }
                    if constexpr (idx % 2 == 0) LGKM(6);
                    PIN();
                    const u32x4 a = fr[idx % (FD + 1)];
                    mf1<z>(acc[T][0], a, xc[kk]); PIN();
                    rlo(f);
                    if constexpr (idx == 0) setm0(wdst);
                    if constexpr (NT == 2) rhi(f);
                    PIN();
                    mf1<z>(acc[T][1], a, xc[2 + kk]); PIN();
                    if constexpr (NT == 4) {
                        rhi(f); PIN();
                        mf1<z>(acc[T][2], a, xc[4 + kk]); PIN();
                        mem(); PIN();
                        mf1<z>(acc[T][3], a, xc[6 + kk]); PIN();
                    } else { mem(); PIN(); }
                    misc();
                    PIN();
                });
            });
            asm volatile("s_nop 7\n\ts_nop 1");
            PIN();
#pragma unroll
            for (int tt = 0; tt < NT; ++tt)
#pragma unroll
                for (int ks = 0; ks < 6; ++ks)
                    vf[tt][ks] = u32x4{cvtpk(acc[2 * ks][tt][0], acc[2 * ks][tt][1]), cvtpk(acc[2 * ks][tt][2], acc[2 * ks][tt][3]),
                                       cvtpk(acc[2 * ks + 1][tt][0], acc[2 * ks + 1][tt][1]), cvtpk(acc[2 * ks + 1][tt][2], acc[2 * ks + 1][tt][3])};
            if (g.type == T_QK) {
#pragma unroll
                for (int tt = 0; tt < NT; ++tt)
#pragma unroll
                    for (int ks = 0; ks < 6; ++ks) {
                        gst16(po + (size_t)(NT / 2 * w + (tt >> 1)) * (NH * 12288) + (tt & 1) * 6144 + ks * 1024, lane * 16,
                              u32x4{vf[tt][ks][0], vf[tt][ks][2], vf[tt][ks][1], vf[tt][ks][3]});
                        if (NT == 4 && EPG) asm volatile("s_sleep %0" : : "n"(EPG));
                    }
            } else if (rm) {
                const bool sx = q >= 2;
#pragma unroll
                for (int i = 0; i < 3 * NT; ++i) {
                    const int tp = i / 6, ks = i % 6;
                    u32x4 o[2];
#pragma unroll
                    for (int j = 0; j < 4; ++j) {
                        const u32x4 &v = vf[2 * tp][ks], &u = vf[2 * tp + 1][ks];
                        if (BLS) {
                            const auto r = __builtin_amdgcn_permlane32_swap(v[j], u[j], false, false);
                            o[0][j] = r[0]; o[1][j ^ 2] = r[1];
                        } else { o[0][j] = sx ? v[j ^ 2] : v[j]; o[1][j] = sx ? u[j ^ 2] : u[j]; }
                    }
#pragma unroll
                    for (int j = 0; j < 2; ++j) {
                        gst16(po + (size_t)(2 * w + tp) * g.sV + j * (BLS ? 1024 : 256) + ks * 2048, BLS ? (u32)((q & 1) * 512 + (q >> 1) * 256 + n * 16) : (u32)(q * 512 + n * 16), o[j]);
                        if (EPG) asm volatile("s_sleep %0" : : "n"(EPG));
                    }
                }
            }
            po = g.outb;
            PIN();
        }
    }
    if (p2) {
        constexpr int HI = 2048;
        const int r = 4 * (q & 1) + qq, sw = r >> 1;
#pragma unroll
        for (int t = 0; t < 4; ++t) c[t] = lds + slot + (q >> 1) * 1024 + r * 128 + ((t ^ sw) << 5) + 8 * pp;
        sring();
        u32 so = (u32)((n >> 3) * g.sV + q * g.sQ + (n & 7) * 16);
        char* const o0 = g.out + (size_t)(NT / 2 * w) * g.sP;
        char* const o1 = o0 + g.sP;
        const int sv2 = 2 * g.sV;
        auto rlo = [&](int f) { flo = trread(c[f / 6] + (f % 6) * 4096); };
        auto rhi = [&](int f) {
            const u32x2 hi = trread(c[f / 6] + (f % 6) * 4096 + HI);
            fr[f % (FD + 1)] = u32x4{flo[0], flo[1], hi[0], hi[1]};
        };
        f32x4 a2[2][NT];
        u32 e[NT / 2][4];
        auto cv = [&](f32x4 (&a)[NT], int i) { e[i >> 2][i & 3] = cvtpk(a[i >> 1][2 * (i & 1)], a[i >> 1][2 * (i & 1) + 1]); asm volatile("" : "+v"(e[i >> 2][i & 3])); };
        auto st = [&](int pl) { gst16n(pl ? o1 : o0, so, u32x4{e[pl][0], e[pl][1], e[pl][2], e[pl][3]}); };
        PIN();
#pragma unroll
        for (int i = 0; i < FD; ++i) { rlo(i); rhi(i); }
#pragma unroll
        for (int tt = 0; tt < NT; ++tt) a2[1][tt] = f32x4{0.f, 0.f, 0.f, 0.f};
#pragma nounroll
        for (int t = 0; t < g.tn; ++t) {
            sfor<0, 24>([&](auto I) {
                constexpr int idx = I.v, cb = idx / 6, ks = idx % 6, f = (idx + FD) % 24;
                f32x4 (&ac)[NT] = a2[cb & 1];
                f32x4 (&ap)[NT] = a2[(cb & 1) ^ 1];
                constexpr int mo = cb < 2 ? (ks == 0 ? 3 * cb : (ks >= 4 ? 3 * cb + ks - 3 : -1)) : -1;
                auto mem = [&]() {
                    if constexpr (mo >= 0) gldsm<1024 * (mo - 3)>(wp, wv[mo]);
                    if constexpr (ks == 3 || (NT == 4 && ks == 2)) { if (cb > 0 || t > 0) st(NT == 4 ? ks - 2 : 0); }
                };
                auto misc = [&]() {
                    if constexpr (idx == 12) wring();
                    if constexpr (idx == 23) sring();
                    if constexpr (idx == 16) { wp += t < g.tn - 5 ? 128 : 0; }
                    if constexpr (ks == 0) { c[cb] += (u32)sd; asm volatile("" : "+v"(c[cb])); }
                    if constexpr (ks == 4) { so += (cb > 0 || t > 0) ? (u32)sv2 : 0u; asm volatile("" : "+v"(so)); }
                };
                if constexpr (idx == BI) waitbar<NT == 4 ? 38 : 28>();
                if constexpr (idx % 2 == 0) LGKM(6);
                PIN();
                const u32x4 b = fr[idx % (FD + 1)];
                mf2<ks == 0>(ac[0], vf[0][ks], b); PIN();
                rlo(f);
                if constexpr (idx == 0) setm0(wdst);
                if constexpr (NT == 2) rhi(f);
                if constexpr (NT == 4 && ks >= 1 && ks <= 3) cv(ap, ks == 1 ? 0 : (ks == 2 ? 4 : 7));
                PIN();
                mf2<ks == 0>(ac[1], vf[1][ks], b); PIN();
                if constexpr (NT == 4) {
                    rhi(f);
                    if constexpr (ks == 1 || ks == 2) cv(ap, ks == 1 ? 1 : 5);
                    PIN();
                    mf2<ks == 0>(ac[2], vf[2][ks], b); PIN();
                    mem();
                    if constexpr (ks == 1) { cv(ap, 2); cv(ap, 3); }
                    PIN();
                    mf2<ks == 0>(ac[3], vf[3][ks], b); PIN();
                    if constexpr (ks == 2) cv(ap, 6);
                } else {
                    if constexpr (ks == 1 || ks == 2) { cv(ap, 2 * ks - 2); cv(ap, 2 * ks - 1); }
                    mem(); PIN();
                }
                misc();
                PIN();
            });
            asm volatile("s_nop 7\n\ts_nop 1");
        }
        PIN();
#pragma unroll
        for (int i = 0; i < 2 * NT; ++i) cv(a2[1], i);
#pragma unroll
        for (int pl = 0; pl < NT / 2; ++pl) st(pl);
        asm volatile("s_nop 1");
    }
    if constexpr (NT == 2) asm volatile(".Lkm_g2:"); else asm volatile(".Lkm_g4:");
    sync_ld(y, ws);
    waitvm<0>();
    asm volatile("" : "+v"(y.r), "+v"(y.g), "+v"(y.e));
    sfor<0, 5>([&](auto I) { xtie(xr[I.v]); });
}

struct Ptrs {
    const char *x, *mem0, *wq, *wk, *wv, *wo, *wqkvb, *wob, *gw;
    char* out;
    char* ws;
};

enum { Q_H = 0, Q_VD = 1, Q_KQ = 2, Q_QKVB = 3, Q_BA = 4, Q_BO = 5, Q_PX = 6, Q_OUT = 7 };
DI void run_gemm(const Ptrs& p, int ty, int b, int i, int hf, bool raw, Sync& y, u32 lds, int w) {
    GemmArgs g;
    g.sV = g.sP = g.sQ = 0; g.w2 = p.wo; g.ldw = HD * 2; g.np1 = 1; g.tn = 10;
    g.sig = p.ws + OFF_ST + F_END * 128;
    const char* xf = raw ? p.x : p.ws + OFF_XF;
    g.xb = !raw;
    int s0 = 8 * b;
    if (ty == Q_VD) {
        s0 = b < 2 ? 4 * b : s0 - 8;
        g.type = T_VD; g.w1 = g.w1b = p.wv + i * DH * 2;
        g.w2 = p.wo + (size_t)i * DH * 1280;
        g.out = g.outb = p.ws + OFF_D + (size_t)s0 * D_STRIDE + i * 512;
        g.sV = 16384; g.sP = (int)D_STRIDE; g.sQ = 128;
        if (b == 0) { g.tn = 5; g.w2 += hf * 640; g.out += hf * 40 * 16384; }
    } else if (ty == Q_H) {
        s0 = 0;
        g.type = T_QK; g.w1 = g.w1b = (b ? p.wq : p.wk) + i * DH * 2;
        g.out = g.outb = p.ws + (b ? OFF_QX : OFF_KX) + (size_t)i * 12288;
        b = 0;
    } else if (ty == Q_KQ) {
        s0 = b < 2 ? 4 * b : s0 - 8;
        g.type = T_QK; g.w1 = p.wk + i * DH * 2; g.w1b = p.wq + i * DH * 2;
        g.out = p.ws + OFF_KX + (size_t)(s0 * NH + i) * 12288;
        g.outb = g.out + (OFF_QX - OFF_KX);
        g.np1 = 2;
        g.sig = p.ws + OFF_ST + (F_K + b * 32 + i) * 128;
    } else if (ty == Q_QKVB) {
        g.type = T_BL; g.w1 = p.wqkvb + i * 768; g.w1b = g.w1 + 384; g.ldw = 3840;
        g.sV = 1920 * 64; g.np1 = 2;
        g.out = p.ws + OFF_QKVB + (size_t)s0 * g.sV + i * 48 * 512; g.outb = g.out + 24 * 512;
    } else {
        const int c0 = min(i * DH, DM - DH);
        g.type = T_BL; g.w1 = g.w1b = p.wob + c0 * 2; g.ldw = 1280;
        xf = p.ws + OFF_OB;
        g.sV = 40960;
        g.out = g.outb = p.ws + OFF_BOUT + (size_t)s0 * 40960 + c0 * 64;
    }
    g.x = xf + (size_t)s0 * 40960;
    if (ty <= Q_KQ && b < 2) gemm_task<2>(g, y, p.ws, lds, w);
    else gemm_task<4>(g, y, p.ws, lds, w);
}
DI int pblk(int s) { return s < 8 ? s >> 2 : min(s >> 3, 7) + 1; }

template <int IMM>
DI void gld16c(u32x4& r, const char* sbase, u32 voff) {
    asm volatile("global_load_dwordx4 %0, %1, %2 offset:%3 sc1" : "=v"(r) : "v"(voff), "s"(sbase), "i"(IMM) : "memory");
}
#define XP1 " nt"
template <int IMM>
DI void gld16x(u32x4& r, const char* sbase, u32 voff) {
    asm volatile("global_load_dwordx4 %0, %1, %2 offset:%3 sc1" XP1 : "=v"(r) : "v"(voff), "s"(sbase), "i"(IMM) : "memory");
}
DI void gst4c(char* sbase, u32 voff, u32 v) {
    asm volatile("global_store_dword %0, %1, %2 sc1" : : "v"(voff), "v"(v), "s"(sbase) : "memory");
}
template <int N>
DI void waitv(u32x4& r) { asm volatile("s_waitcnt vmcnt(%1)" : "+v"(r) : "i"(N) : "memory"); }
DI u32 mt() { return (u32)__builtin_amdgcn_s_memtime(); }
DI u32 rt() { return (u32)__builtin_amdgcn_s_memrealtime(); }
DI void spin_to(u32 tgt) { while ((int)(mt() - tgt) < 0) {} }
DI u32 max3u(u32 a, u32 b, u32 c) { return max(max(a, b), c); }
DI u32x4 ldsr16(u32 a) { return *LDSP(u32x4, a); }
DI void clocks(u32& r, u32& m) {
    u64 a, b;
    asm volatile("s_memrealtime %0\n\ts_memtime %1\n\ts_waitcnt lgkmcnt(0)" : "=s"(a), "=s"(b) : : "memory");
    r = (u32)a; m = (u32)b;
}
#define SLATE 20
DI void edge(u32& r, u32& m) {
    u64 a, t[9];
    u32 n = 0;
#define RT(i) "s_memrealtime %" #i "\n\ts_nop 1\n\t"
#define RC(i) "\n\ts_cmp_eq_u64 %" #i ", %1\n\ts_addc_u32 %10, %10, 0"
    asm volatile("s_memtime %0\n\t" RT(1) RT(2) RT(3) RT(4) RT(5) RT(6) RT(7) RT(8) "s_memrealtime %9\n\ts_waitcnt lgkmcnt(0)" RC(2) RC(3) RC(4) RC(5) RC(6) RC(7) RC(8) RC(9)
                 : "=&s"(a), "=&s"(t[0]), "=&s"(t[1]), "=&s"(t[2]), "=&s"(t[3]), "=&s"(t[4]), "=&s"(t[5]), "=&s"(t[6]), "=&s"(t[7]), "=&s"(t[8]), "+s"(n) : : "scc", "memory");
    m = (u32)a + 12 * n;
    r = (u32)t[0] + 4;
}
#define SPB 90
struct Clock {
    u32 r0, m0, r1, m1; int cpt;
    DI void init() { edge(r0, m0); r1 = r0; m1 = m0; cpt = 24 * 64; }
    DI u32 sched(u32& phi, int ofs, int G, int mode) {
        if (mode == 0) edge(r1, m1);
        u32 r = r1, m = m1;
        if (mode == 2) clocks(r, m);
        int rem = (int)(phi + (u32)ofs - r);
        if (__builtin_expect(rem <= -G, 0)) { const int k = -rem / G * G; phi += (u32)k; rem += k; }
        const int k = rem < -SLATE ? G : 0;
        phi += (u32)k; rem += k;
        return m + (u32)((min(rem, 3000) * cpt) >> 6) - SPB;
    }
    DI u32 at(u32 t) { u32 r, m; clocks(r, m); return m + (u32)((min(max((int)(t - r), 0), 3000) * cpt) >> 6); }
    DI void calib() {
        const int dr = (int)(r1 - r0);
        if (dr >= 60) { int c = (int)fminf(fmaxf((float)(int)(m1 - m0) * 64.f * __builtin_amdgcn_rcpf((float)dr), 256.f), 2560.f); asm volatile("" : "+v"(c)); cpt = uni(c); r0 = r1; m0 = m1; }
    }
};
#define VISS 0
DI void follow(u32& phi, int ofs, u32 t, int G) {
    const int late = (int)(t - phi) - ofs;
    phi += late > 0 ? (u32)G : 0u;
    if (__builtin_expect(late >= G, 0)) phi += (u32)(late / G * G);
}
DI bool unwritten(const char* base, u32 off) {
#pragma nounroll
    for (int i = 0; i < 10; ++i) {
        u32x4 t;
        gld16c<-4096>(t, base + (i + (i & 8)) * 1024, off); waitv<0>(t);
        if (__builtin_amdgcn_ballot_w64(max3u(max(t[0], t[1]), t[2], t[3]) == 0xffffffffu) != 0) return true;
    }
    return false;
}

DI size_t uslot(int sd, int s) { const int r = (s * 43) >> 8; return OFF_U + (u32)uni(s * (int)U_STRIDE + r * (int)((2u << 20) - 6 * U_STRIDE) + ((sd >> (2 + r)) & 1) * 8192); }
DI int uchunk(int v) { return (2 * (v & 3) + ((v >> 2) & 1) + (v & ~7)) * 8192; }
DI size_t mslot(int sd, int s) { return (u32)uni(s * (int)HO_STRIDE + ((sd >> (s >> 5)) & 1) * 8192 + 4096); }
struct ChainCfg { int T, cR, G, dV, fast, s1, T2, cR2, s2, T3, cR3, dU; };
constexpr int TT_OFF = 88 + 48 + 360;
DI void karg_warm() {
    u32 a[13];
    asm volatile("s_load_dword %0, %13, 0\n\t" "s_load_dword %1, %13, 64\n\t" "s_load_dword %2, %13, 128\n\t" "s_load_dword %3, %13, 192\n\t" "s_load_dword %4, %13, 256\n\t" "s_load_dword %5, %13, 320\n\t" "s_load_dword %6, %13, 384\n\t" "s_load_dword %7, %13, 448\n\t" "s_load_dword %8, %13, 512\n\t" "s_load_dword %9, %13, 576\n\t" "s_load_dword %10, %13, 640\n\t" "s_load_dword %11, %13, 704\n\t" "s_load_dword %12, %13, 748\n\t" "s_waitcnt lgkmcnt(0)"
                 : "=&s"(a[0]), "=&s"(a[1]), "=&s"(a[2]), "=&s"(a[3]), "=&s"(a[4]), "=&s"(a[5]), "=&s"(a[6]), "=&s"(a[7]), "=&s"(a[8]), "=&s"(a[9]), "=&s"(a[10]), "=&s"(a[11]), "=&s"(a[12])
                 : "s"(__builtin_amdgcn_kernarg_segment_ptr()) : "memory");
}
DI u32 sched_ld0(int s) {
    u32 r;
    asm volatile("s_load_dword %0, %1, %2 offset:%3\n\ts_waitcnt lgkmcnt(0)" : "=s"(r) : "s"(__builtin_amdgcn_kernarg_segment_ptr()), "s"(4 * s), "n"(TT_OFF) : "memory");
    return r;
}
#define HP 60
#define HQ 96

constexpr int SL_W = 0;
constexpr int SL_IMG = 131072;
constexpr int SL_KX = SL_IMG + 6656;
constexpr int SL_PF = SL_KX + 12288 + 1024;
constexpr int S_LDS = SL_PF + 256;
DI void nap(int t) { const u32 wake = rt() + (u32)t; while ((int)(rt() - wake) < 0) __builtin_amdgcn_s_sleep(4); }
DI void until(u32 t) { while ((int)(rt() - t) < 0) __builtin_amdgcn_s_sleep(1); }
DI void dly(int n) {
    asm volatile(".Ld%=:\n\ts_sub_u32 %0, %0, 1\n\ts_nop 0\n\ts_cbranch_scc0 .Ld%=" : "+s"(n) : : "scc", "memory");
}
DI void rdy_add(u32& r, char* fl) {
    asm volatile("s_mov_b64 exec, 1\n\ts_nop 3\n\tglobal_atomic_add %0, %1, %2, %3 sc0 sc1\n\ts_mov_b64 exec, -1" : "=&v"(r) : "v"(F_RDY * 128), "v"(1), "s"(fl) : "memory");
}
DI void rdy_end(u32 r, char* fl, int dt) {
    if (uni((int)r) == NS + NVB - 2 && lanex() < 8) gst4c(fl, (F_GO + lanex()) * 128, (rt() + (u32)dt) & ~1u);
}
DI u32 go_wait(const char* fl, int i) {
    u32 v;
#pragma nounroll
    for (int it = 0; it < (1 << 16); ++it) {
        flag_ld(v, fl, F_GO + (i & 7));
        asm volatile("s_waitcnt vmcnt(0)" : "+v"(v) : : "memory");
        if (uni((int)v) != -1) break;
        __builtin_amdgcn_s_sleep(GSL);
    }
    return (u32)uni((int)v);
}
DI u32 go_wg(const char* fl, int i, int w, u32 la) {
    if (w == 0) *LDSP(u32, la) = go_wait(fl, i);
    barrier();
    return (u32)uni((int)*LDSP(u32, la));
}
DI void pfl(const char* sbase, u32 voff, u32 lds) {
    asm volatile("s_mov_b32 m0, %2\n\ts_nop 3\n\tglobal_load_lds_dword %0, %1" : : "v"(voff), "s"(sbase), "s"(lds + SL_PF) : "memory");
}
DI void score_role(const Ptrs& p, const ChainCfg cfg, int h, u32 lds, int w) {
    const int lane = lanex(), tid = 64 * w + lane;
    const int n = lane & 15, g = lane >> 4, qq = (lane >> 2) & 3, pp = lane & 3;
    u32x32 wa[8], wv[3];
    const u32 wreg = lds + SL_W + (u32)w * 32768;
    const u32 lfa = wreg + lane * 16;
    const char* kxp = p.ws + OFF_KX + (size_t)h * 12288 + w * 3072;
    const u32 kxl = lds + SL_KX;
    char* const fl = p.ws + OFF_ST;
    const u32 kxw = kxl + w * 3072 - (w < 2 ? 16 : 0);
    auto kxdma = [&]() {
        asm volatile("s_mov_b32 m0, %2\n\ts_nop 3\n\tglobal_load_lds_dwordx4 %0, %1 sc1\n\tglobal_load_lds_dwordx4 %0, %1 offset:1024 sc1\n\tglobal_load_lds_dwordx4 %0, %1 offset:2048 sc1"
                     : : "v"(lanex() * 16), "s"(kxp), "s"(kxw) : "memory");
    };
    u32 kfl, phi;
    int sd;
    {
        constexpr int GS = 3104;
        Sync y;
        if (w < 3) {
#pragma unroll
            for (int i = PXT; i < NSEG; ++i) gst16(p.ws + OFF_KM + (i * NH + h) * 3072 + w * 1024, lane * 16, u32x4{~0u, ~0u, ~0u, ~0u});
#pragma unroll
            for (int i = NSEG - 4; i < NSEG; ++i) gst16(p.ws + OFF_PX + i * 81920 + (w < 2 ? h * 2048 + w * 1024 : 65536 + h * 512), w < 2 ? lane * 16 : (lane * 16) & 511, u32x4{~0u, ~0u, ~0u, ~0u});
        }
        auto dvo = [&](int i) { const int ix = i * 64 + lane; return (u32)((ix / 24) * (HD * 2) + (ix % 24) * 16); };
        const u32 tra = lds + (2 * (g >> 1) + (qq >> 1)) * GS + (4 * (g & 1) + (qq & 1)) * 384 + 8 * pp;
        u32x8 pa = u32x8{dvo(0), dvo(1), dvo(2), tra + 96 * w + 32 * (w & 1), tra + 96 * w - 64 * (w & 1), tra + 32 * w, lfa, (u32)(lane & 31) * 8};
        asm volatile("s_nop 4\n\t" "s_add_u32 s42, %21, 0x0\n\t" "s_add_u32 s42, s46, s42\n\t" "s_addc_u32 s43, s47, 0\n\t" "s_add_u32 m0, %18, 0x0\n\t" "s_nop 0\n\t" "global_load_lds_dwordx4 v224, s[42:43]\n\t"
                     "s_add_u32 m0, %18, 0x400\n\t" "s_nop 0\n\t" "global_load_lds_dwordx4 v225, s[42:43]\n\t" "s_add_u32 m0, %18, 0x800\n\t" "s_nop 0\n\t" "global_load_lds_dwordx4 v226, s[42:43]\n\t"
                     "s_add_u32 s42, %21, 0x18000\n\t" "s_add_u32 s42, s46, s42\n\t" "s_addc_u32 s43, s47, 0\n\t" "s_add_u32 m0, %18, 0xc20\n\t" "s_nop 0\n\t" "global_load_lds_dwordx4 v224, s[42:43]\n\t"
                     "s_add_u32 m0, %18, 0x1020\n\t" "s_nop 0\n\t" "global_load_lds_dwordx4 v225, s[42:43]\n\t" "s_add_u32 m0, %18, 0x1420\n\t" "s_nop 0\n\t" "global_load_lds_dwordx4 v226, s[42:43]\n\t"
                     "s_sub_u32 s42, 0xc0000, %21\n\t" "s_add_u32 s42, s46, s42\n\t" "s_addc_u32 s43, s47, 0\n\t" "s_add_u32 m0, %18, 0x6100\n\t" "s_nop 0\n\t" "global_load_lds_dwordx4 v224, s[42:43]\n\t"
                     "s_add_u32 m0, %18, 0x6500\n\t" "s_nop 0\n\t" "global_load_lds_dwordx4 v225, s[42:43]\n\t" "s_add_u32 m0, %18, 0x6900\n\t" "s_nop 0\n\t" "global_load_lds_dwordx4 v226, s[42:43]\n\t"
                     "s_sub_u32 s42, 0xd8000, %21\n\t" "s_add_u32 s42, s46, s42\n\t" "s_addc_u32 s43, s47, 0\n\t" "s_add_u32 m0, %18, 0x6d20\n\t" "s_nop 0\n\t" "global_load_lds_dwordx4 v224, s[42:43]\n\t"
                     "s_add_u32 m0, %18, 0x7120\n\t" "s_nop 0\n\t" "global_load_lds_dwordx4 v225, s[42:43]\n\t" "s_add_u32 m0, %18, 0x7520\n\t" "s_nop 0\n\t" "global_load_lds_dwordx4 v226, s[42:43]\n\t"
                     "s_add_u32 s42, %21, 0x180000\n\t" "s_add_u32 s42, s46, s42\n\t" "s_addc_u32 s43, s47, 0\n\t" "s_add_u32 m0, %18, 0xc200\n\t" "s_nop 0\n\t" "global_load_lds_dwordx4 v224, s[42:43]\n\t"
                     "s_add_u32 m0, %18, 0xc600\n\t" "s_nop 0\n\t" "global_load_lds_dwordx4 v225, s[42:43]\n\t" "s_add_u32 m0, %18, 0xca00\n\t" "s_nop 0\n\t" "global_load_lds_dwordx4 v226, s[42:43]\n\t"
                     "s_add_u32 s42, %21, 0x198000\n\t" "s_add_u32 s42, s46, s42\n\t" "s_addc_u32 s43, s47, 0\n\t" "s_add_u32 m0, %18, 0xce20\n\t" "s_nop 0\n\t" "global_load_lds_dwordx4 v224, s[42:43]\n\t"
                     "s_add_u32 m0, %18, 0xd220\n\t" "s_nop 0\n\t" "global_load_lds_dwordx4 v225, s[42:43]\n\t" "s_add_u32 m0, %18, 0xd620\n\t" "s_nop 0\n\t" "global_load_lds_dwordx4 v226, s[42:43]\n\t"
                     "s_sub_u32 s42, 0x240000, %21\n\t" "s_add_u32 s42, s46, s42\n\t" "s_addc_u32 s43, s47, 0\n\t" "s_add_u32 m0, %18, 0x12300\n\t" "s_nop 0\n\t" "global_load_lds_dwordx4 v224, s[42:43]\n\t"
                     "s_add_u32 m0, %18, 0x12700\n\t" "s_nop 0\n\t" "global_load_lds_dwordx4 v225, s[42:43]\n\t" "s_add_u32 m0, %18, 0x12b00\n\t" "s_nop 0\n\t" "global_load_lds_dwordx4 v226, s[42:43]\n\t"
                     "s_sub_u32 s42, 0x258000, %21\n\t" "s_add_u32 s42, s46, s42\n\t" "s_addc_u32 s43, s47, 0\n\t" "s_add_u32 m0, %18, 0x12f20\n\t" "s_nop 0\n\t" "global_load_lds_dwordx4 v224, s[42:43]\n\t"
                     "s_add_u32 m0, %18, 0x13320\n\t" "s_nop 0\n\t" "global_load_lds_dwordx4 v225, s[42:43]\n\t" "s_add_u32 m0, %18, 0x13720\n\t" "s_nop 0\n\t" "global_load_lds_dwordx4 v226, s[42:43]\n\t"
                     "s_waitcnt vmcnt(12) lgkmcnt(0)\n\t" "s_barrier\n\t" "s_add_u32 s42, %21, 0x300000\n\t" "s_add_u32 s42, s46, s42\n\t" "s_addc_u32 s43, s47, 0\n\t" "s_add_u32 m0, %18, 0x18400\n\t" "s_nop 0\n\t"
                     "global_load_lds_dwordx4 v224, s[42:43]\n\t" "s_add_u32 m0, %18, 0x18800\n\t" "s_nop 0\n\t" "global_load_lds_dwordx4 v225, s[42:43]\n\t" "s_add_u32 m0, %18, 0x18c00\n\t" "s_nop 0\n\t"
                     "global_load_lds_dwordx4 v226, s[42:43]\n\t" "s_add_u32 s42, %21, 0x318000\n\t" "s_add_u32 s42, s46, s42\n\t" "s_addc_u32 s43, s47, 0\n\t" "s_add_u32 m0, %18, 0x19020\n\t" "s_nop 0\n\t"
                     "global_load_lds_dwordx4 v224, s[42:43]\n\t" "s_add_u32 m0, %18, 0x19420\n\t" "s_nop 0\n\t" "global_load_lds_dwordx4 v225, s[42:43]\n\t" "s_add_u32 m0, %18, 0x19820\n\t" "s_nop 0\n\t"
                     "global_load_lds_dwordx4 v226, s[42:43]\n\t" "s_sub_u32 s42, 0x3c0000, %21\n\t" "s_add_u32 s42, s46, s42\n\t" "s_addc_u32 s43, s47, 0\n\t" "s_add_u32 m0, %18, 0x1e500\n\t" "s_nop 0\n\t"
                     "global_load_lds_dwordx4 v224, s[42:43]\n\t" "s_add_u32 m0, %18, 0x1e900\n\t" "s_nop 0\n\t" "global_load_lds_dwordx4 v225, s[42:43]\n\t" "s_add_u32 m0, %18, 0x1ed00\n\t" "s_nop 0\n\t"
                     "global_load_lds_dwordx4 v226, s[42:43]\n\t" "s_sub_u32 s42, 0x3d8000, %21\n\t" "s_add_u32 s42, s46, s42\n\t" "s_addc_u32 s43, s47, 0\n\t" "s_add_u32 m0, %18, 0x1f120\n\t" "s_nop 0\n\t"
                     "global_load_lds_dwordx4 v224, s[42:43]\n\t" "s_add_u32 m0, %18, 0x1f520\n\t" "s_nop 0\n\t" "global_load_lds_dwordx4 v225, s[42:43]\n\t" "s_add_u32 m0, %18, 0x1f920\n\t" "s_nop 0\n\t"
                     "global_load_lds_dwordx4 v226, s[42:43]\n\t" "v_add_u32 v232, 0x0, v229\n\t" "ds_read_b64_tr_b16 v[8:9], v232\n\t" "ds_read_b64_tr_b16 v[10:11], v232 offset:768\n\t"
                     "ds_read_b64_tr_b16 v[12:13], v232 offset:12416\n\t" "ds_read_b64_tr_b16 v[14:15], v232 offset:13184\n\t" "ds_read_b64_tr_b16 a[96:97], v232 offset:128\n\t"
                     "ds_read_b64_tr_b16 a[98:99], v232 offset:896\n\t" "ds_read_b64_tr_b16 a[100:101], v232 offset:12544\n\t" "ds_read_b64_tr_b16 a[102:103], v232 offset:13312\n\t"
                     "ds_read_b64_tr_b16 a[176:177], v232 offset:256\n\t" "ds_read_b64_tr_b16 a[178:179], v232 offset:1024\n\t" "ds_read_b64_tr_b16 a[180:181], v232 offset:12672\n\t"
                     "ds_read_b64_tr_b16 a[182:183], v232 offset:13440\n\t" "ds_read_b64_tr_b16 v[40:41], v232 offset:24832\n\t" "ds_read_b64_tr_b16 v[42:43], v232 offset:25600\n\t"
                     "ds_read_b64_tr_b16 v[44:45], v232 offset:37248\n\t" "ds_read_b64_tr_b16 v[46:47], v232 offset:38016\n\t" "ds_read_b64_tr_b16 a[112:113], v232 offset:24960\n\t"
                     "ds_read_b64_tr_b16 a[114:115], v232 offset:25728\n\t" "ds_read_b64_tr_b16 a[116:117], v232 offset:37376\n\t" "ds_read_b64_tr_b16 a[118:119], v232 offset:38144\n\t"
                     "ds_read_b64_tr_b16 a[192:193], v232 offset:25088\n\t" "ds_read_b64_tr_b16 a[194:195], v232 offset:25856\n\t" "ds_read_b64_tr_b16 a[196:197], v232 offset:37504\n\t"
                     "ds_read_b64_tr_b16 a[198:199], v232 offset:38272\n\t" "s_waitcnt vmcnt(12) lgkmcnt(0)\n\t" "s_barrier\n\t" "s_add_u32 s42, %21, 0x480000\n\t" "s_add_u32 s42, s46, s42\n\t" "s_addc_u32 s43, s47, 0\n\t"
                     "s_add_u32 m0, %18, 0x0\n\t" "s_nop 0\n\t" "global_load_lds_dwordx4 v224, s[42:43]\n\t" "s_add_u32 m0, %18, 0x400\n\t" "s_nop 0\n\t" "global_load_lds_dwordx4 v225, s[42:43]\n\t"
                     "s_add_u32 m0, %18, 0x800\n\t" "s_nop 0\n\t" "global_load_lds_dwordx4 v226, s[42:43]\n\t" "s_add_u32 s42, %21, 0x498000\n\t" "s_add_u32 s42, s46, s42\n\t" "s_addc_u32 s43, s47, 0\n\t"
                     "s_add_u32 m0, %18, 0xc20\n\t" "s_nop 0\n\t" "global_load_lds_dwordx4 v224, s[42:43]\n\t" "s_add_u32 m0, %18, 0x1020\n\t" "s_nop 0\n\t" "global_load_lds_dwordx4 v225, s[42:43]\n\t"
                     "s_add_u32 m0, %18, 0x1420\n\t" "s_nop 0\n\t" "global_load_lds_dwordx4 v226, s[42:43]\n\t" "s_sub_u32 s42, 0x540000, %21\n\t" "s_add_u32 s42, s46, s42\n\t" "s_addc_u32 s43, s47, 0\n\t"
                     "s_add_u32 m0, %18, 0x6100\n\t" "s_nop 0\n\t" "global_load_lds_dwordx4 v224, s[42:43]\n\t" "s_add_u32 m0, %18, 0x6500\n\t" "s_nop 0\n\t" "global_load_lds_dwordx4 v225, s[42:43]\n\t"
                     "s_add_u32 m0, %18, 0x6900\n\t" "s_nop 0\n\t" "global_load_lds_dwordx4 v226, s[42:43]\n\t" "s_sub_u32 s42, 0x558000, %21\n\t" "s_add_u32 s42, s46, s42\n\t" "s_addc_u32 s43, s47, 0\n\t"
                     "s_add_u32 m0, %18, 0x6d20\n\t" "s_nop 0\n\t" "global_load_lds_dwordx4 v224, s[42:43]\n\t" "s_add_u32 m0, %18, 0x7120\n\t" "s_nop 0\n\t" "global_load_lds_dwordx4 v225, s[42:43]\n\t"
                     "s_add_u32 m0, %18, 0x7520\n\t" "s_nop 0\n\t" "global_load_lds_dwordx4 v226, s[42:43]\n\t" "v_add_u32 v232, 0xc200, v229\n\t" "ds_read_b64_tr_b16 v[24:25], v232\n\t"
                     "ds_read_b64_tr_b16 v[26:27], v232 offset:768\n\t" "ds_read_b64_tr_b16 v[28:29], v232 offset:12416\n\t" "ds_read_b64_tr_b16 v[30:31], v232 offset:13184\n\t"
                     "ds_read_b64_tr_b16 a[104:105], v232 offset:128\n\t" "ds_read_b64_tr_b16 a[106:107], v232 offset:896\n\t" "ds_read_b64_tr_b16 a[108:109], v232 offset:12544\n\t"
                     "ds_read_b64_tr_b16 a[110:111], v232 offset:13312\n\t" "ds_read_b64_tr_b16 a[184:185], v232 offset:256\n\t" "ds_read_b64_tr_b16 a[186:187], v232 offset:1024\n\t"
                     "ds_read_b64_tr_b16 a[188:189], v232 offset:12672\n\t" "ds_read_b64_tr_b16 a[190:191], v232 offset:13440\n\t" "ds_read_b64_tr_b16 v[56:57], v232 offset:24832\n\t"
                     "ds_read_b64_tr_b16 v[58:59], v232 offset:25600\n\t" "ds_read_b64_tr_b16 v[60:61], v232 offset:37248\n\t" "ds_read_b64_tr_b16 v[62:63], v232 offset:38016\n\t"
                     "ds_read_b64_tr_b16 a[120:121], v232 offset:24960\n\t" "ds_read_b64_tr_b16 a[122:123], v232 offset:25728\n\t" "ds_read_b64_tr_b16 a[124:125], v232 offset:37376\n\t"
                     "ds_read_b64_tr_b16 a[126:127], v232 offset:38144\n\t" "ds_read_b64_tr_b16 a[200:201], v232 offset:25088\n\t" "ds_read_b64_tr_b16 a[202:203], v232 offset:25856\n\t"
                     "ds_read_b64_tr_b16 a[204:205], v232 offset:37504\n\t" "ds_read_b64_tr_b16 a[206:207], v232 offset:38272\n\t" "s_waitcnt vmcnt(12) lgkmcnt(0)\n\t" "s_barrier\n\t" "s_add_u32 s42, %21, 0x600000\n\t"
                     "s_add_u32 s42, s46, s42\n\t" "s_addc_u32 s43, s47, 0\n\t" "s_add_u32 m0, %18, 0xc200\n\t" "s_nop 0\n\t" "global_load_lds_dwordx4 v224, s[42:43]\n\t" "s_add_u32 m0, %18, 0xc600\n\t" "s_nop 0\n\t"
                     "global_load_lds_dwordx4 v225, s[42:43]\n\t" "s_add_u32 m0, %18, 0xca00\n\t" "s_nop 0\n\t" "global_load_lds_dwordx4 v226, s[42:43]\n\t" "s_add_u32 s42, %21, 0x618000\n\t" "s_add_u32 s42, s46, s42\n\t"
                     "s_addc_u32 s43, s47, 0\n\t" "s_add_u32 m0, %18, 0xce20\n\t" "s_nop 0\n\t" "global_load_lds_dwordx4 v224, s[42:43]\n\t" "s_add_u32 m0, %18, 0xd220\n\t" "s_nop 0\n\t"
                     "global_load_lds_dwordx4 v225, s[42:43]\n\t" "s_add_u32 m0, %18, 0xd620\n\t" "s_nop 0\n\t" "global_load_lds_dwordx4 v226, s[42:43]\n\t" "s_sub_u32 s42, 0x6c0000, %21\n\t" "s_add_u32 s42, s46, s42\n\t"
                     "s_addc_u32 s43, s47, 0\n\t" "s_add_u32 m0, %18, 0x12300\n\t" "s_nop 0\n\t" "global_load_lds_dwordx4 v224, s[42:43]\n\t" "s_add_u32 m0, %18, 0x12700\n\t" "s_nop 0\n\t"
                     "global_load_lds_dwordx4 v225, s[42:43]\n\t" "s_add_u32 m0, %18, 0x12b00\n\t" "s_nop 0\n\t" "global_load_lds_dwordx4 v226, s[42:43]\n\t" "s_sub_u32 s42, 0x6d8000, %21\n\t" "s_add_u32 s42, s46, s42\n\t"
                     "s_addc_u32 s43, s47, 0\n\t" "s_add_u32 m0, %18, 0x12f20\n\t" "s_nop 0\n\t" "global_load_lds_dwordx4 v224, s[42:43]\n\t" "s_add_u32 m0, %18, 0x13320\n\t" "s_nop 0\n\t"
                     "global_load_lds_dwordx4 v225, s[42:43]\n\t" "s_add_u32 m0, %18, 0x13720\n\t" "s_nop 0\n\t" "global_load_lds_dwordx4 v226, s[42:43]\n\t" "v_add_u32 v232, 0x18400, v229\n\t"
                     "ds_read_b64_tr_b16 v[72:73], v232\n\t" "ds_read_b64_tr_b16 v[74:75], v232 offset:768\n\t" "ds_read_b64_tr_b16 v[76:77], v232 offset:12416\n\t" "ds_read_b64_tr_b16 v[78:79], v232 offset:13184\n\t"
                     "ds_read_b64_tr_b16 a[128:129], v232 offset:128\n\t" "ds_read_b64_tr_b16 a[130:131], v232 offset:896\n\t" "ds_read_b64_tr_b16 a[132:133], v232 offset:12544\n\t"
                     "ds_read_b64_tr_b16 a[134:135], v232 offset:13312\n\t" "ds_read_b64_tr_b16 a[208:209], v232 offset:256\n\t" "ds_read_b64_tr_b16 a[210:211], v232 offset:1024\n\t"
                     "ds_read_b64_tr_b16 a[212:213], v232 offset:12672\n\t" "ds_read_b64_tr_b16 a[214:215], v232 offset:13440\n\t" "ds_read_b64_tr_b16 v[88:89], v232 offset:24832\n\t"
                     "ds_read_b64_tr_b16 v[90:91], v232 offset:25600\n\t" "ds_read_b64_tr_b16 v[92:93], v232 offset:37248\n\t" "ds_read_b64_tr_b16 v[94:95], v232 offset:38016\n\t"
                     "ds_read_b64_tr_b16 a[136:137], v232 offset:24960\n\t" "ds_read_b64_tr_b16 a[138:139], v232 offset:25728\n\t" "ds_read_b64_tr_b16 a[140:141], v232 offset:37376\n\t"
                     "ds_read_b64_tr_b16 a[142:143], v232 offset:38144\n\t" "ds_read_b64_tr_b16 a[216:217], v232 offset:25088\n\t" "ds_read_b64_tr_b16 a[218:219], v232 offset:25856\n\t"
                     "ds_read_b64_tr_b16 a[220:221], v232 offset:37504\n\t" "ds_read_b64_tr_b16 a[222:223], v232 offset:38272\n\t" "s_waitcnt vmcnt(12) lgkmcnt(0)\n\t" "s_barrier\n\t" "s_add_u32 s42, %21, 0x0\n\t"
                     "s_add_u32 s42, s44, s42\n\t" "s_addc_u32 s43, s45, 0\n\t" "s_add_u32 m0, %18, 0x18400\n\t" "s_nop 0\n\t" "global_load_lds_dwordx4 v224, s[42:43]\n\t" "s_add_u32 m0, %18, 0x18800\n\t" "s_nop 0\n\t"
                     "global_load_lds_dwordx4 v225, s[42:43]\n\t" "s_add_u32 m0, %18, 0x18c00\n\t" "s_nop 0\n\t" "global_load_lds_dwordx4 v226, s[42:43]\n\t" "s_add_u32 s42, %21, 0x18000\n\t" "s_add_u32 s42, s44, s42\n\t"
                     "s_addc_u32 s43, s45, 0\n\t" "s_add_u32 m0, %18, 0x19020\n\t" "s_nop 0\n\t" "global_load_lds_dwordx4 v224, s[42:43]\n\t" "s_add_u32 m0, %18, 0x19420\n\t" "s_nop 0\n\t"
                     "global_load_lds_dwordx4 v225, s[42:43]\n\t" "s_add_u32 m0, %18, 0x19820\n\t" "s_nop 0\n\t" "global_load_lds_dwordx4 v226, s[42:43]\n\t" "s_sub_u32 s42, 0xc0000, %21\n\t" "s_add_u32 s42, s44, s42\n\t"
                     "s_addc_u32 s43, s45, 0\n\t" "s_add_u32 m0, %18, 0x1e500\n\t" "s_nop 0\n\t" "global_load_lds_dwordx4 v224, s[42:43]\n\t" "s_add_u32 m0, %18, 0x1e900\n\t" "s_nop 0\n\t"
                     "global_load_lds_dwordx4 v225, s[42:43]\n\t" "s_add_u32 m0, %18, 0x1ed00\n\t" "s_nop 0\n\t" "global_load_lds_dwordx4 v226, s[42:43]\n\t" "s_sub_u32 s42, 0xd8000, %21\n\t" "s_add_u32 s42, s44, s42\n\t"
                     "s_addc_u32 s43, s45, 0\n\t" "s_add_u32 m0, %18, 0x1f120\n\t" "s_nop 0\n\t" "global_load_lds_dwordx4 v224, s[42:43]\n\t" "s_add_u32 m0, %18, 0x1f520\n\t" "s_nop 0\n\t"
                     "global_load_lds_dwordx4 v225, s[42:43]\n\t" "s_add_u32 m0, %18, 0x1f920\n\t" "s_nop 0\n\t" "global_load_lds_dwordx4 v226, s[42:43]\n\t" "v_add_u32 v232, 0x0, v229\n\t"
                     "ds_read_b64_tr_b16 v[104:105], v232\n\t" "ds_read_b64_tr_b16 v[106:107], v232 offset:768\n\t" "ds_read_b64_tr_b16 v[108:109], v232 offset:12416\n\t"
                     "ds_read_b64_tr_b16 v[110:111], v232 offset:13184\n\t" "ds_read_b64_tr_b16 a[144:145], v232 offset:128\n\t" "ds_read_b64_tr_b16 a[146:147], v232 offset:896\n\t"
                     "ds_read_b64_tr_b16 a[148:149], v232 offset:12544\n\t" "ds_read_b64_tr_b16 a[150:151], v232 offset:13312\n\t" "ds_read_b64_tr_b16 a[224:225], v232 offset:256\n\t"
                     "ds_read_b64_tr_b16 a[226:227], v232 offset:1024\n\t" "ds_read_b64_tr_b16 a[228:229], v232 offset:12672\n\t" "ds_read_b64_tr_b16 a[230:231], v232 offset:13440\n\t"
                     "ds_read_b64_tr_b16 v[120:121], v232 offset:24832\n\t" "ds_read_b64_tr_b16 v[122:123], v232 offset:25600\n\t" "ds_read_b64_tr_b16 v[124:125], v232 offset:37248\n\t"
                     "ds_read_b64_tr_b16 v[126:127], v232 offset:38016\n\t" "ds_read_b64_tr_b16 a[152:153], v232 offset:24960\n\t" "ds_read_b64_tr_b16 a[154:155], v232 offset:25728\n\t"
                     "ds_read_b64_tr_b16 a[156:157], v232 offset:37376\n\t" "ds_read_b64_tr_b16 a[158:159], v232 offset:38144\n\t" "ds_read_b64_tr_b16 a[232:233], v232 offset:25088\n\t"
                     "ds_read_b64_tr_b16 a[234:235], v232 offset:25856\n\t" "ds_read_b64_tr_b16 a[236:237], v232 offset:37504\n\t" "ds_read_b64_tr_b16 a[238:239], v232 offset:38272\n\t" "s_waitcnt vmcnt(12) lgkmcnt(0)\n\t"
                     "s_barrier\n\t" "s_add_u32 s42, %21, 0x180000\n\t" "s_add_u32 s42, s44, s42\n\t" "s_addc_u32 s43, s45, 0\n\t" "s_add_u32 m0, %18, 0x0\n\t" "s_nop 0\n\t" "global_load_lds_dwordx4 v224, s[42:43]\n\t"
                     "s_add_u32 m0, %18, 0x400\n\t" "s_nop 0\n\t" "global_load_lds_dwordx4 v225, s[42:43]\n\t" "s_add_u32 m0, %18, 0x800\n\t" "s_nop 0\n\t" "global_load_lds_dwordx4 v226, s[42:43]\n\t"
                     "s_add_u32 s42, %21, 0x198000\n\t" "s_add_u32 s42, s44, s42\n\t" "s_addc_u32 s43, s45, 0\n\t" "s_add_u32 m0, %18, 0xc20\n\t" "s_nop 0\n\t" "global_load_lds_dwordx4 v224, s[42:43]\n\t"
                     "s_add_u32 m0, %18, 0x1020\n\t" "s_nop 0\n\t" "global_load_lds_dwordx4 v225, s[42:43]\n\t" "s_add_u32 m0, %18, 0x1420\n\t" "s_nop 0\n\t" "global_load_lds_dwordx4 v226, s[42:43]\n\t"
                     "s_sub_u32 s42, 0x240000, %21\n\t" "s_add_u32 s42, s44, s42\n\t" "s_addc_u32 s43, s45, 0\n\t" "s_add_u32 m0, %18, 0x6100\n\t" "s_nop 0\n\t" "global_load_lds_dwordx4 v224, s[42:43]\n\t"
                     "s_add_u32 m0, %18, 0x6500\n\t" "s_nop 0\n\t" "global_load_lds_dwordx4 v225, s[42:43]\n\t" "s_add_u32 m0, %18, 0x6900\n\t" "s_nop 0\n\t" "global_load_lds_dwordx4 v226, s[42:43]\n\t"
                     "s_sub_u32 s42, 0x258000, %21\n\t" "s_add_u32 s42, s44, s42\n\t" "s_addc_u32 s43, s45, 0\n\t" "s_add_u32 m0, %18, 0x6d20\n\t" "s_nop 0\n\t" "global_load_lds_dwordx4 v224, s[42:43]\n\t"
                     "s_add_u32 m0, %18, 0x7120\n\t" "s_nop 0\n\t" "global_load_lds_dwordx4 v225, s[42:43]\n\t" "s_add_u32 m0, %18, 0x7520\n\t" "s_nop 0\n\t" "global_load_lds_dwordx4 v226, s[42:43]\n\t"
                     "v_add_u32 v232, 0xc200, v229\n\t" "ds_read_b64_tr_b16 a[56:57], v232\n\t" "ds_read_b64_tr_b16 a[58:59], v232 offset:768\n\t" "ds_read_b64_tr_b16 a[60:61], v232 offset:12416\n\t"
                     "ds_read_b64_tr_b16 a[62:63], v232 offset:13184\n\t" "ds_read_b64_tr_b16 a[160:161], v232 offset:128\n\t" "ds_read_b64_tr_b16 a[162:163], v232 offset:896\n\t"
                     "ds_read_b64_tr_b16 a[164:165], v232 offset:12544\n\t" "ds_read_b64_tr_b16 a[166:167], v232 offset:13312\n\t" "ds_read_b64_tr_b16 a[240:241], v232 offset:256\n\t"
                     "ds_read_b64_tr_b16 a[242:243], v232 offset:1024\n\t" "ds_read_b64_tr_b16 a[244:245], v232 offset:12672\n\t" "ds_read_b64_tr_b16 a[246:247], v232 offset:13440\n\t"
                     "ds_read_b64_tr_b16 a[88:89], v232 offset:24832\n\t" "ds_read_b64_tr_b16 a[90:91], v232 offset:25600\n\t" "ds_read_b64_tr_b16 a[92:93], v232 offset:37248\n\t"
                     "ds_read_b64_tr_b16 a[94:95], v232 offset:38016\n\t" "ds_read_b64_tr_b16 a[168:169], v232 offset:24960\n\t" "ds_read_b64_tr_b16 a[170:171], v232 offset:25728\n\t"
                     "ds_read_b64_tr_b16 a[172:173], v232 offset:37376\n\t" "ds_read_b64_tr_b16 a[174:175], v232 offset:38144\n\t" "ds_read_b64_tr_b16 a[248:249], v232 offset:25088\n\t"
                     "ds_read_b64_tr_b16 a[250:251], v232 offset:25856\n\t" "ds_read_b64_tr_b16 a[252:253], v232 offset:37504\n\t" "ds_read_b64_tr_b16 a[254:255], v232 offset:38272\n\t" "s_waitcnt vmcnt(12) lgkmcnt(0)\n\t"
                     "s_barrier\n\t" "s_add_u32 s42, %21, 0x300000\n\t" "s_add_u32 s42, s44, s42\n\t" "s_addc_u32 s43, s45, 0\n\t" "s_add_u32 m0, %18, 0xc200\n\t" "s_nop 0\n\t" "global_load_lds_dwordx4 v224, s[42:43]\n\t"
                     "s_add_u32 m0, %18, 0xc600\n\t" "s_nop 0\n\t" "global_load_lds_dwordx4 v225, s[42:43]\n\t" "s_add_u32 m0, %18, 0xca00\n\t" "s_nop 0\n\t" "global_load_lds_dwordx4 v226, s[42:43]\n\t"
                     "s_add_u32 s42, %21, 0x318000\n\t" "s_add_u32 s42, s44, s42\n\t" "s_addc_u32 s43, s45, 0\n\t" "s_add_u32 m0, %18, 0xce20\n\t" "s_nop 0\n\t" "global_load_lds_dwordx4 v224, s[42:43]\n\t"
                     "s_add_u32 m0, %18, 0xd220\n\t" "s_nop 0\n\t" "global_load_lds_dwordx4 v225, s[42:43]\n\t" "s_add_u32 m0, %18, 0xd620\n\t" "s_nop 0\n\t" "global_load_lds_dwordx4 v226, s[42:43]\n\t"
                     "s_sub_u32 s42, 0x3c0000, %21\n\t" "s_add_u32 s42, s44, s42\n\t" "s_addc_u32 s43, s45, 0\n\t" "s_add_u32 m0, %18, 0x12300\n\t" "s_nop 0\n\t" "global_load_lds_dwordx4 v224, s[42:43]\n\t"
                     "s_add_u32 m0, %18, 0x12700\n\t" "s_nop 0\n\t" "global_load_lds_dwordx4 v225, s[42:43]\n\t" "s_add_u32 m0, %18, 0x12b00\n\t" "s_nop 0\n\t" "global_load_lds_dwordx4 v226, s[42:43]\n\t"
                     "s_sub_u32 s42, 0x3d8000, %21\n\t" "s_add_u32 s42, s44, s42\n\t" "s_addc_u32 s43, s45, 0\n\t" "s_add_u32 m0, %18, 0x12f20\n\t" "s_nop 0\n\t" "global_load_lds_dwordx4 v224, s[42:43]\n\t"
                     "s_add_u32 m0, %18, 0x13320\n\t" "s_nop 0\n\t" "global_load_lds_dwordx4 v225, s[42:43]\n\t" "s_add_u32 m0, %18, 0x13720\n\t" "s_nop 0\n\t" "global_load_lds_dwordx4 v226, s[42:43]\n\t"
                     "v_add_u32 v232, 0x18400, v227\n\t" "v_add_u32 v233, 0x18400, v228\n\t" "ds_read_b64_tr_b16 v[128:129], v232\n\t" "ds_read_b64_tr_b16 v[130:131], v232 offset:768\n\t"
                     "ds_read_b64_tr_b16 v[132:133], v232 offset:12416\n\t" "ds_read_b64_tr_b16 v[134:135], v232 offset:13184\n\t" "ds_read_b64_tr_b16 v[0:1], v232 offset:32\n\t"
                     "ds_read_b64_tr_b16 v[2:3], v232 offset:800\n\t" "ds_read_b64_tr_b16 v[4:5], v232 offset:12448\n\t" "ds_read_b64_tr_b16 v[6:7], v232 offset:13216\n\t"
                     "ds_read_b64_tr_b16 v[136:137], v233 offset:64\n\t" "ds_read_b64_tr_b16 v[138:139], v233 offset:832\n\t" "ds_read_b64_tr_b16 v[140:141], v233 offset:12480\n\t"
                     "ds_read_b64_tr_b16 v[142:143], v233 offset:13248\n\t" "ds_read_b64_tr_b16 v[160:161], v232 offset:24832\n\t" "ds_read_b64_tr_b16 v[162:163], v232 offset:25600\n\t"
                     "ds_read_b64_tr_b16 v[164:165], v232 offset:37248\n\t" "ds_read_b64_tr_b16 v[166:167], v232 offset:38016\n\t" "ds_read_b64_tr_b16 v[32:33], v232 offset:24864\n\t"
                     "ds_read_b64_tr_b16 v[34:35], v232 offset:25632\n\t" "ds_read_b64_tr_b16 v[36:37], v232 offset:37280\n\t" "ds_read_b64_tr_b16 v[38:39], v232 offset:38048\n\t"
                     "ds_read_b64_tr_b16 v[168:169], v233 offset:24896\n\t" "ds_read_b64_tr_b16 v[170:171], v233 offset:25664\n\t" "ds_read_b64_tr_b16 v[172:173], v233 offset:37312\n\t"
                     "ds_read_b64_tr_b16 v[174:175], v233 offset:38080\n\t" "s_waitcnt vmcnt(12) lgkmcnt(0)\n\t" "s_barrier\n\t" "v_mov_b32 %14, 0\n\t" "global_load_dwordx2 %11, v231, %19 sc1\n\t"
                     "global_load_dwordx2 %12, %14, %19 offset:512 sc1\n\t" "global_load_dwordx2 %13, %14, %19 offset:1024 sc1\n\t" "s_add_u32 s42, %21, 0x480000\n\t" "s_add_u32 s42, s44, s42\n\t"
                     "s_addc_u32 s43, s45, 0\n\t" "s_add_u32 m0, %18, 0x18400\n\t" "s_nop 0\n\t" "global_load_lds_dwordx4 v224, s[42:43]\n\t" "s_add_u32 m0, %18, 0x18800\n\t" "s_nop 0\n\t"
                     "global_load_lds_dwordx4 v225, s[42:43]\n\t" "s_add_u32 m0, %18, 0x18c00\n\t" "s_nop 0\n\t" "global_load_lds_dwordx4 v226, s[42:43]\n\t" "s_add_u32 s42, %21, 0x498000\n\t" "s_add_u32 s42, s44, s42\n\t"
                     "s_addc_u32 s43, s45, 0\n\t" "s_add_u32 m0, %18, 0x19020\n\t" "s_nop 0\n\t" "global_load_lds_dwordx4 v224, s[42:43]\n\t" "s_add_u32 m0, %18, 0x19420\n\t" "s_nop 0\n\t"
                     "global_load_lds_dwordx4 v225, s[42:43]\n\t" "s_add_u32 m0, %18, 0x19820\n\t" "s_nop 0\n\t" "global_load_lds_dwordx4 v226, s[42:43]\n\t" "s_sub_u32 s42, 0x540000, %21\n\t" "s_add_u32 s42, s44, s42\n\t"
                     "s_addc_u32 s43, s45, 0\n\t" "s_add_u32 m0, %18, 0x1e500\n\t" "s_nop 0\n\t" "global_load_lds_dwordx4 v224, s[42:43]\n\t" "s_add_u32 m0, %18, 0x1e900\n\t" "s_nop 0\n\t"
                     "global_load_lds_dwordx4 v225, s[42:43]\n\t" "s_add_u32 m0, %18, 0x1ed00\n\t" "s_nop 0\n\t" "global_load_lds_dwordx4 v226, s[42:43]\n\t" "s_sub_u32 s42, 0x558000, %21\n\t" "s_add_u32 s42, s44, s42\n\t"
                     "s_addc_u32 s43, s45, 0\n\t" "s_add_u32 m0, %18, 0x1f120\n\t" "s_nop 0\n\t" "global_load_lds_dwordx4 v224, s[42:43]\n\t" "s_add_u32 m0, %18, 0x1f520\n\t" "s_nop 0\n\t"
                     "global_load_lds_dwordx4 v225, s[42:43]\n\t" "s_add_u32 m0, %18, 0x1f920\n\t" "s_nop 0\n\t" "global_load_lds_dwordx4 v226, s[42:43]\n\t" "v_add_u32 v232, 0x0, v227\n\t" "v_add_u32 v233, 0x0, v228\n\t"
                     "ds_read_b64_tr_b16 v[144:145], v232\n\t" "ds_read_b64_tr_b16 v[146:147], v232 offset:768\n\t" "ds_read_b64_tr_b16 v[148:149], v232 offset:12416\n\t"
                     "ds_read_b64_tr_b16 v[150:151], v232 offset:13184\n\t" "ds_read_b64_tr_b16 v[16:17], v232 offset:32\n\t" "ds_read_b64_tr_b16 v[18:19], v232 offset:800\n\t"
                     "ds_read_b64_tr_b16 v[20:21], v232 offset:12448\n\t" "ds_read_b64_tr_b16 v[22:23], v232 offset:13216\n\t" "ds_read_b64_tr_b16 v[152:153], v233 offset:64\n\t"
                     "ds_read_b64_tr_b16 v[154:155], v233 offset:832\n\t" "ds_read_b64_tr_b16 v[156:157], v233 offset:12480\n\t" "ds_read_b64_tr_b16 v[158:159], v233 offset:13248\n\t"
                     "ds_read_b64_tr_b16 v[176:177], v232 offset:24832\n\t" "ds_read_b64_tr_b16 v[178:179], v232 offset:25600\n\t" "ds_read_b64_tr_b16 v[180:181], v232 offset:37248\n\t"
                     "ds_read_b64_tr_b16 v[182:183], v232 offset:38016\n\t" "ds_read_b64_tr_b16 v[48:49], v232 offset:24864\n\t" "ds_read_b64_tr_b16 v[50:51], v232 offset:25632\n\t"
                     "ds_read_b64_tr_b16 v[52:53], v232 offset:37280\n\t" "ds_read_b64_tr_b16 v[54:55], v232 offset:38048\n\t" "ds_read_b64_tr_b16 v[184:185], v233 offset:24896\n\t"
                     "ds_read_b64_tr_b16 v[186:187], v233 offset:25664\n\t" "ds_read_b64_tr_b16 v[188:189], v233 offset:37312\n\t" "ds_read_b64_tr_b16 v[190:191], v233 offset:38080\n\t" "s_waitcnt vmcnt(12) lgkmcnt(0)\n\t"
                     "s_barrier\n\t" "s_add_u32 s42, %21, 0x600000\n\t" "s_add_u32 s42, s44, s42\n\t" "s_addc_u32 s43, s45, 0\n\t" "s_add_u32 m0, %18, 0x0\n\t" "s_nop 0\n\t" "global_load_lds_dwordx4 v224, s[42:43]\n\t"
                     "s_add_u32 m0, %18, 0x400\n\t" "s_nop 0\n\t" "global_load_lds_dwordx4 v225, s[42:43]\n\t" "s_add_u32 m0, %18, 0x800\n\t" "s_nop 0\n\t" "global_load_lds_dwordx4 v226, s[42:43]\n\t"
                     "s_add_u32 s42, %21, 0x618000\n\t" "s_add_u32 s42, s44, s42\n\t" "s_addc_u32 s43, s45, 0\n\t" "s_add_u32 m0, %18, 0xc20\n\t" "s_nop 0\n\t" "global_load_lds_dwordx4 v224, s[42:43]\n\t"
                     "s_add_u32 m0, %18, 0x1020\n\t" "s_nop 0\n\t" "global_load_lds_dwordx4 v225, s[42:43]\n\t" "s_add_u32 m0, %18, 0x1420\n\t" "s_nop 0\n\t" "global_load_lds_dwordx4 v226, s[42:43]\n\t"
                     "s_sub_u32 s42, 0x6c0000, %21\n\t" "s_add_u32 s42, s44, s42\n\t" "s_addc_u32 s43, s45, 0\n\t" "s_add_u32 m0, %18, 0x6100\n\t" "s_nop 0\n\t" "global_load_lds_dwordx4 v224, s[42:43]\n\t"
                     "s_add_u32 m0, %18, 0x6500\n\t" "s_nop 0\n\t" "global_load_lds_dwordx4 v225, s[42:43]\n\t" "s_add_u32 m0, %18, 0x6900\n\t" "s_nop 0\n\t" "global_load_lds_dwordx4 v226, s[42:43]\n\t"
                     "s_sub_u32 s42, 0x6d8000, %21\n\t" "s_add_u32 s42, s44, s42\n\t" "s_addc_u32 s43, s45, 0\n\t" "s_add_u32 m0, %18, 0x6d20\n\t" "s_nop 0\n\t" "global_load_lds_dwordx4 v224, s[42:43]\n\t"
                     "s_add_u32 m0, %18, 0x7120\n\t" "s_nop 0\n\t" "global_load_lds_dwordx4 v225, s[42:43]\n\t" "s_add_u32 m0, %18, 0x7520\n\t" "s_nop 0\n\t" "global_load_lds_dwordx4 v226, s[42:43]\n\t"
                     "global_load_dword %14, %14, %20 sc1\n\t" "v_add_u32 v232, 0xc200, v227\n\t" "v_add_u32 v233, 0xc200, v228\n\t" "ds_read_b64_tr_b16 v[192:193], v232\n\t"
                     "ds_read_b64_tr_b16 v[194:195], v232 offset:768\n\t" "ds_read_b64_tr_b16 v[196:197], v232 offset:12416\n\t" "ds_read_b64_tr_b16 v[198:199], v232 offset:13184\n\t"
                     "ds_read_b64_tr_b16 v[64:65], v232 offset:32\n\t" "ds_read_b64_tr_b16 v[66:67], v232 offset:800\n\t" "ds_read_b64_tr_b16 v[68:69], v232 offset:12448\n\t"
                     "ds_read_b64_tr_b16 v[70:71], v232 offset:13216\n\t" "ds_read_b64_tr_b16 v[200:201], v233 offset:64\n\t" "ds_read_b64_tr_b16 v[202:203], v233 offset:832\n\t"
                     "ds_read_b64_tr_b16 v[204:205], v233 offset:12480\n\t" "ds_read_b64_tr_b16 v[206:207], v233 offset:13248\n\t" "ds_read_b64_tr_b16 v[208:209], v232 offset:24832\n\t"
                     "ds_read_b64_tr_b16 v[210:211], v232 offset:25600\n\t" "ds_read_b64_tr_b16 v[212:213], v232 offset:37248\n\t" "ds_read_b64_tr_b16 v[214:215], v232 offset:38016\n\t"
                     "ds_read_b64_tr_b16 v[80:81], v232 offset:24864\n\t" "ds_read_b64_tr_b16 v[82:83], v232 offset:25632\n\t" "ds_read_b64_tr_b16 v[84:85], v232 offset:37280\n\t"
                     "ds_read_b64_tr_b16 v[86:87], v232 offset:38048\n\t" "ds_read_b64_tr_b16 v[216:217], v233 offset:24896\n\t" "ds_read_b64_tr_b16 v[218:219], v233 offset:25664\n\t"
                     "ds_read_b64_tr_b16 v[220:221], v233 offset:37312\n\t" "ds_read_b64_tr_b16 v[222:223], v233 offset:38080\n\t" "s_waitcnt vmcnt(13) lgkmcnt(0)\n\t" "s_barrier\n\t" "v_add_u32 v232, 0x18400, v227\n\t"
                     "v_add_u32 v233, 0x18400, v228\n\t" "ds_read_b64_tr_b16 a[0:1], v232\n\t" "ds_read_b64_tr_b16 a[2:3], v232 offset:768\n\t" "ds_read_b64_tr_b16 a[4:5], v232 offset:12416\n\t"
                     "ds_read_b64_tr_b16 a[6:7], v232 offset:13184\n\t" "ds_read_b64_tr_b16 v[96:97], v232 offset:32\n\t" "ds_read_b64_tr_b16 v[98:99], v232 offset:800\n\t"
                     "ds_read_b64_tr_b16 v[100:101], v232 offset:12448\n\t" "ds_read_b64_tr_b16 v[102:103], v232 offset:13216\n\t" "ds_read_b64_tr_b16 a[8:9], v233 offset:64\n\t"
                     "ds_read_b64_tr_b16 a[10:11], v233 offset:832\n\t" "ds_read_b64_tr_b16 a[12:13], v233 offset:12480\n\t" "ds_read_b64_tr_b16 a[14:15], v233 offset:13248\n\t"
                     "ds_read_b64_tr_b16 a[16:17], v232 offset:24832\n\t" "ds_read_b64_tr_b16 a[18:19], v232 offset:25600\n\t" "ds_read_b64_tr_b16 a[20:21], v232 offset:37248\n\t"
                     "ds_read_b64_tr_b16 a[22:23], v232 offset:38016\n\t" "ds_read_b64_tr_b16 v[112:113], v232 offset:24864\n\t" "ds_read_b64_tr_b16 v[114:115], v232 offset:25632\n\t"
                     "ds_read_b64_tr_b16 v[116:117], v232 offset:37280\n\t" "ds_read_b64_tr_b16 v[118:119], v232 offset:38048\n\t" "ds_read_b64_tr_b16 a[24:25], v233 offset:24896\n\t"
                     "ds_read_b64_tr_b16 a[26:27], v233 offset:25664\n\t" "ds_read_b64_tr_b16 a[28:29], v233 offset:37312\n\t" "ds_read_b64_tr_b16 a[30:31], v233 offset:38080\n\t" "s_waitcnt vmcnt(0) lgkmcnt(0)\n\t"
                     "s_barrier\n\t" "v_add_u32 v232, 0x0, v227\n\t" "v_add_u32 v233, 0x0, v228\n\t" "ds_read_b64_tr_b16 a[32:33], v232\n\t" "ds_read_b64_tr_b16 a[34:35], v232 offset:768\n\t"
                     "ds_read_b64_tr_b16 a[36:37], v232 offset:12416\n\t" "ds_read_b64_tr_b16 a[38:39], v232 offset:13184\n\t" "ds_read_b64_tr_b16 a[40:41], v232 offset:32\n\t"
                     "ds_read_b64_tr_b16 a[42:43], v232 offset:800\n\t" "ds_read_b64_tr_b16 a[44:45], v232 offset:12448\n\t" "ds_read_b64_tr_b16 a[46:47], v232 offset:13216\n\t"
                     "ds_read_b64_tr_b16 a[48:49], v233 offset:64\n\t" "ds_read_b64_tr_b16 a[50:51], v233 offset:832\n\t" "ds_read_b64_tr_b16 a[52:53], v233 offset:12480\n\t"
                     "ds_read_b64_tr_b16 a[54:55], v233 offset:13248\n\t" "ds_read_b64_tr_b16 a[64:65], v232 offset:24832\n\t" "ds_read_b64_tr_b16 a[66:67], v232 offset:25600\n\t"
                     "ds_read_b64_tr_b16 a[68:69], v232 offset:37248\n\t" "ds_read_b64_tr_b16 a[70:71], v232 offset:38016\n\t" "ds_read_b64_tr_b16 a[72:73], v232 offset:24864\n\t"
                     "ds_read_b64_tr_b16 a[74:75], v232 offset:25632\n\t" "ds_read_b64_tr_b16 a[76:77], v232 offset:37280\n\t" "ds_read_b64_tr_b16 a[78:79], v232 offset:38048\n\t"
                     "ds_read_b64_tr_b16 a[80:81], v233 offset:24896\n\t" "ds_read_b64_tr_b16 a[82:83], v233 offset:25664\n\t" "ds_read_b64_tr_b16 a[84:85], v233 offset:37312\n\t"
                     "ds_read_b64_tr_b16 a[86:87], v233 offset:38080\n\t" "s_waitcnt lgkmcnt(0)\n\t" "s_barrier\n\t" "ds_write_b128 v230, v[0:3]\n\t" "ds_write_b128 v230, v[4:7] offset:1024\n\t"
                     "ds_write_b128 v230, v[8:11] offset:2048\n\t" "ds_write_b128 v230, v[12:15] offset:3072\n\t" "ds_write_b128 v230, v[16:19] offset:4096\n\t" "ds_write_b128 v230, v[20:23] offset:5120\n\t"
                     "ds_write_b128 v230, v[24:27] offset:6144\n\t" "ds_write_b128 v230, v[28:31] offset:7168\n\t" "ds_write_b128 v230, v[32:35] offset:8192\n\t" "ds_write_b128 v230, v[36:39] offset:9216\n\t"
                     "ds_write_b128 v230, v[40:43] offset:10240\n\t" "ds_write_b128 v230, v[44:47] offset:11264\n\t" "ds_write_b128 v230, v[48:51] offset:12288\n\t" "ds_write_b128 v230, v[52:55] offset:13312\n\t"
                     "ds_write_b128 v230, v[56:59] offset:14336\n\t" "ds_write_b128 v230, v[60:63] offset:15360\n\t" "ds_write_b128 v230, v[64:67] offset:16384\n\t" "ds_write_b128 v230, v[68:71] offset:17408\n\t"
                     "ds_write_b128 v230, v[72:75] offset:18432\n\t" "ds_write_b128 v230, v[76:79] offset:19456\n\t" "ds_write_b128 v230, v[80:83] offset:20480\n\t" "ds_write_b128 v230, v[84:87] offset:21504\n\t"
                     "ds_write_b128 v230, v[88:91] offset:22528\n\t" "ds_write_b128 v230, v[92:95] offset:23552\n\t" "ds_write_b128 v230, v[96:99] offset:24576\n\t" "ds_write_b128 v230, v[100:103] offset:25600\n\t"
                     "ds_write_b128 v230, v[104:107] offset:26624\n\t" "ds_write_b128 v230, v[108:111] offset:27648\n\t" "ds_write_b128 v230, v[112:115] offset:28672\n\t" "ds_write_b128 v230, v[116:119] offset:29696\n\t"
                     "ds_write_b128 v230, v[120:123] offset:30720\n\t" "ds_write_b128 v230, v[124:127] offset:31744\n\t"
                     : "=&{a[0:31]}"(wa[0]), "=&{a[32:63]}"(wa[1]), "=&{a[64:95]}"(wa[2]), "=&{a[96:127]}"(wa[3]), "=&{a[128:159]}"(wa[4]), "=&{a[160:191]}"(wa[5]), "=&{a[192:223]}"(wa[6]), "=&{a[224:255]}"(wa[7]), "=&{v[128:159]}"(wv[0]), "=&{v[160:191]}"(wv[1]), "=&{v[192:223]}"(wv[2]), "=&v"(y.r), "=&v"(y.g), "=&v"(y.e), "=&v"(kfl)
                     : "{v[224:231]}"(pa), "{s[44:45]}"(p.wq + h * DH * 2 + (size_t)(16 * w) * (HD * 2)), "{s[46:47]}"(p.wk + h * DH * 2 + (size_t)(16 * w) * (HD * 2)),
                       "s"(lds + 2 * w * GS), "s"(p.ws + OFF_LAT), "s"(fl + (F_K + h) * 128), "s"((h & 4) * 16 * HD * 2)
                     : "v0", "v1", "v2", "v3", "v4", "v5", "v6", "v7", "v8", "v9", "v10", "v11", "v12", "v13", "v14", "v15", "v16", "v17", "v18", "v19", "v20", "v21", "v22", "v23", "v24", "v25", "v26", "v27", "v28", "v29", "v30", "v31", "v32", "v33", "v34", "v35", "v36", "v37", "v38", "v39", "v40", "v41", "v42", "v43", "v44", "v45", "v46", "v47", "v48", "v49", "v50", "v51", "v52", "v53", "v54", "v55", "v56", "v57", "v58", "v59", "v60", "v61", "v62", "v63", "v64", "v65", "v66", "v67", "v68", "v69", "v70", "v71", "v72", "v73", "v74", "v75", "v76", "v77", "v78", "v79", "v80", "v81", "v82", "v83", "v84", "v85", "v86", "v87", "v88", "v89", "v90", "v91", "v92", "v93", "v94", "v95", "v96", "v97", "v98", "v99", "v100", "v101", "v102", "v103", "v104", "v105", "v106", "v107", "v108", "v109", "v110", "v111", "v112", "v113", "v114", "v115", "v116", "v117", "v118", "v119", "v120", "v121", "v122", "v123", "v124", "v125", "v126", "v127", "v232", "v233", "s42", "s43", "scc", "memory");
        const u64 t = sync_tag(y.e);
        const bool rdy = __builtin_amdgcn_ballot_w64((y.r[0] & ~1u) != (u32)t || y.r[1] != (u32)(t >> 32) || y.g[0] != (u32)t) == 0 && uni((int)kfl) != -1;
        if (rdy) kxdma();
        sd = sync_end(y, p.ws, phi, w == 0);
        if (!rdy) { flag_wait(fl, F_K + h, 1); kxdma(); }
        if (w == 3) rdy_add(kfl, fl);
        kxp += NH * 12288;
    }
    const u32 sidx = (lane & 2) ? 0xEEEEu : 0x4444u;
    const u32 moff = lane * 16;
    u32 kxa = kxl - 16 + (n & 1) * 6160 + (16 * g + (n & 8) + 2 * (n & 2) + ((n >> 1) & 2)) * 16;
    const u64 mb0 = 0xffff0000ffff0000ull;
    const int tp = (3 * w + (w & 1)) >> 1, ts = 1 + 3 * (w >> 1);
    u32 qw0 = lds + SL_IMG + tp * 512 + lane * 8, qw1 = lds + SL_IMG + ts * 512 + lane * 8 + 4 * (w & 1);
    const u32 kw0 = lds + SL_IMG + 3072 + w * 288 + n * 16 + 4 * g;
    const u32 qr0 = lds + SL_IMG + (g >> 1) * 512 + w * 128 + 64 * (g & 1) + 8 * qq;
    const u32 kr0 = lds + SL_IMG + 3072 + (qq >> 1) * 288 + g * 64 + (qq & 1) * 16 + 8 * (pp & 1);
    const u32 xso = 32768 + (h >> 4) * 16384 + (h & 15) * 512 + (w >> 1) * 256 + (n >> 3) * 64 + (n & 4) * 32 + (w & 1) * 32 + (g & 1) * 16 + (n & 3) * 4 + (g >> 1) * 8192;
    const u32 pso = (n < 8 ? 20480 + (h >> 1) * 256 + (n & 2) * 16 + (h & 1) * 8 + (n & 4) : 18432 + (h >> 2) * 256 + (h & 1) * 8 + (h & 2) * 2) + w * 64 + (g & 1) * 16 + (g >> 1) * 8192;
    const u64 psm = 0x01aa01aa01aa01aaull;
    const u32 kmo = (u32)(((n >> 3) * 4 + g) * 128 + (n & 7) * 16);
    u32x16 ad = u32x16{moff, sidx, lfa, kxa, qw0, qw1, kw0, kr0 + 1152 * min(w, 2) + (pp >> 1) * 576, qr0, kr0, xso, pso, 0x3dd53b94u, kmo, kmo, kmo};
    asm volatile("" : "+{v[224:239]}"(ad));
    const char* memp = p.ws + mslot(sd, 0);
    char* kmp = p.ws + OFF_KM + h * 3072 + min(w, 2) * 1024;
    const float kscale = 0.07216878364870322f;
    int budget = 30000;
    Clock clk; clk.init();
    if (w == 3) { asm volatile("s_waitcnt vmcnt(0)" : "+v"(kfl) : : "memory"); rdy_end(kfl, fl, cfg.s1 >> 8 & 255); }
    u32x32 wk[3];
    asm volatile("" : "={v[64:95]}"(wk[0]), "={v[96:127]}"(wk[1]), "={v[32:63]}"(wk[2]));
    const int kfo = (F_K + 32 + h) * 128;
    for (int s = 0; s < NSEG; ++s) {
        int tries = 0;
        u64 bad, tp8, en = ~0ull;
        u32 tt;
        int kf;
        while (true) {
            int tgt = uni((int)(s ? clk.sched(phi, 0, cfg.G, tries ? 2 : 0) : mt()));
            asm volatile("s_or_b32 s42, %5, %14\n\t" "s_cbranch_scc1 .Lsp_s\n\t" "ds_read_b128 v[64:67], v226\n\t" "v_mov_b64 v[40:41], 0\n\t" "ds_read_b128 v[68:71], v226 offset:1024\n\t" "v_mov_b64 v[42:43], 0\n\t"
                         "ds_read_b128 v[72:75], v226 offset:2048\n\t" "v_mov_b64 v[44:45], 0\n\t" "ds_read_b128 v[76:79], v226 offset:3072\n\t" "v_mov_b64 v[46:47], 0\n\t" "ds_read_b128 v[80:83], v226 offset:4096\n\t"
                         "v_mov_b64 v[48:49], 0\n\t" "ds_read_b128 v[84:87], v226 offset:5120\n\t" "v_mov_b64 v[50:51], 0\n\t" "ds_read_b128 v[88:91], v226 offset:6144\n\t" "v_mov_b64 v[52:53], 0\n\t"
                         "ds_read_b128 v[92:95], v226 offset:7168\n\t" "v_mov_b64 v[54:55], 0\n\t" "ds_read_b128 v[96:99], v226 offset:8192\n\t" "v_mov_b64 v[56:57], 0\n\t" "ds_read_b128 v[100:103], v226 offset:9216\n\t"
                         "v_mov_b64 v[58:59], 0\n\t" "ds_read_b128 v[104:107], v226 offset:10240\n\t" "v_mov_b64 v[60:61], 0\n\t" "ds_read_b128 v[108:111], v226 offset:11264\n\t" "v_mov_b64 v[62:63], 0\n\t"
                         "ds_read_b128 v[112:115], v226 offset:12288\n\t" "ds_read_b128 v[116:119], v226 offset:13312\n\t" "ds_read_b128 v[120:123], v226 offset:14336\n\t" "ds_read_b128 v[124:127], v226 offset:15360\n\t"
                         ".p2align 5\n\t" "s_nop 0\n\t" "s_nop 0\n\t" "s_nop 0\n\t" "s_nop 0\n\t" "s_nop 0\n\t" ".Lsp_s:\n\t" "s_memtime vcc\n\t" "s_waitcnt lgkmcnt(0)\n\t" "s_sub_i32 s42, %4, vcc_lo\n\t"
                         "s_cmp_gt_i32 s42, 80\n\t" "s_cbranch_scc1 .Lsp_s\n\t" "s_max_i32 s42, s42, 0\n\t" "s_sub_i32 s42, 92, s42\n\t" "s_and_b32 s42, s42, -4\n\t" "s_getpc_b64 vcc\n\t" "s_add_u32 vcc_lo, vcc_lo, s42\n\t"
                         "s_addc_u32 vcc_hi, vcc_hi, 0\n\t" "s_setpc_b64 vcc\n\t" "s_nop 0\n\t" "s_nop 0\n\t" "s_nop 0\n\t" "s_nop 0\n\t" "s_nop 0\n\t" "s_nop 0\n\t" "s_nop 0\n\t" "s_nop 0\n\t" "s_nop 0\n\t" "s_nop 0\n\t"
                         "s_nop 0\n\t" "s_nop 0\n\t" "s_nop 0\n\t" "s_nop 0\n\t" "s_nop 0\n\t" "s_nop 0\n\t" "s_nop 0\n\t" "s_nop 0\n\t" "s_nop 0\n\t" "s_nop 0\n\t" "global_load_dwordx4 v[0:3], v224, %7 offset:-4096 sc1\n\t"
                         "v_add_u32 v238, 0x4000, v224\n\t" "s_nop 9\n\t" "global_load_dwordx4 v[4:7], v224, %7 offset:-2048 sc1\n\t" "s_nop 15\n\t" "global_load_dwordx4 v[8:11], v224, %8 offset:-3072 sc1\n\t" "s_nop 10\n\t"
                         "global_load_dwordx4 v[12:15], v224, %8 offset:-1024 sc1\n\t" "s_nop 10\n\t" "global_load_dwordx4 v[16:19], v224, %7 sc1\n\t" "s_nop 10\n\t" "global_load_dwordx4 v[20:23], v224, %8 offset:1024 sc1\n\t"
                         "s_nop 10\n\t" "global_load_dwordx4 v[24:27], v224, %7 offset:2048 sc1\n\t" "s_nop 10\n\t" "global_load_dwordx4 v[28:31], v224, %8 offset:3072 sc1\n\t" "s_nop 10\n\t"
                         "global_load_dwordx4 v[32:35], v238, %7 offset:-4096 sc1\n\t" "s_nop 10\n\t" "global_load_dwordx4 v[36:39], v238, %8 offset:-3072 sc1\n\t" "s_cmp_lg_u32 %5, 0\n\t" "s_cbranch_scc1 .Lnb_s\n\t"
                         "s_waitcnt vmcnt(10)\n\t" "s_barrier\n\t" ".Lnb_s:\n\t" "s_waitcnt vmcnt(9)\n\t" "v_smfmac_f32_16x16x64_bf16 v[40:43], v[0:3], v[128:135], v225\n\t"
                         "v_smfmac_f32_16x16x64_bf16 v[44:47], v[0:3], v[64:71], v225\n\t" "ds_read_b128 v[64:67], v226 offset:16384\n\t" "v_smfmac_f32_16x16x64_bf16 v[48:51], v[0:3], v[136:143], v225\n\t"
                         "ds_read_b128 v[68:71], v226 offset:17408\n\t" "v_smfmac_f32_16x16x64_bf16 v[52:55], v[0:3], v[72:79], v225\n\t" "ds_read_b128 v[72:75], v226 offset:18432\n\t" "s_waitcnt vmcnt(8)\n\t"
                         "v_smfmac_f32_16x16x64_bf16 v[40:43], v[4:7], v[144:151], v225\n\t" "ds_read_b128 v[76:79], v226 offset:19456\n\t" "v_smfmac_f32_16x16x64_bf16 v[44:47], v[4:7], v[80:87], v225\n\t"
                         "ds_read_b128 v[80:83], v226 offset:20480\n\t" "v_smfmac_f32_16x16x64_bf16 v[48:51], v[4:7], v[152:159], v225\n\t" "ds_read_b128 v[84:87], v226 offset:21504\n\t"
                         "v_smfmac_f32_16x16x64_bf16 v[52:55], v[4:7], v[88:95], v225\n\t" "ds_read_b128 v[88:91], v226 offset:22528\n\t" "s_waitcnt vmcnt(7)\n\t"
                         "v_smfmac_f32_16x16x64_bf16 v[40:43], v[8:11], v[160:167], v225\n\t" "ds_read_b128 v[92:95], v226 offset:23552\n\t" "v_smfmac_f32_16x16x64_bf16 v[44:47], v[8:11], v[96:103], v225\n\t"
                         "ds_read_b128 v[96:99], v226 offset:24576\n\t" "v_smfmac_f32_16x16x64_bf16 v[48:51], v[8:11], v[168:175], v225\n\t" "ds_read_b128 v[100:103], v226 offset:25600\n\t"
                         "v_smfmac_f32_16x16x64_bf16 v[52:55], v[8:11], v[104:111], v225\n\t" "ds_read_b128 v[104:107], v226 offset:26624\n\t" "s_waitcnt vmcnt(6)\n\t"
                         "v_smfmac_f32_16x16x64_bf16 v[40:43], v[12:15], v[176:183], v225\n\t" "ds_read_b128 v[108:111], v226 offset:27648\n\t" "v_smfmac_f32_16x16x64_bf16 v[44:47], v[12:15], v[112:119], v225\n\t"
                         "ds_read_b128 v[112:115], v226 offset:28672\n\t" "v_smfmac_f32_16x16x64_bf16 v[48:51], v[12:15], v[184:191], v225\n\t" "ds_read_b128 v[116:119], v226 offset:29696\n\t"
                         "v_smfmac_f32_16x16x64_bf16 v[52:55], v[12:15], v[120:127], v225\n\t" "ds_read_b128 v[120:123], v226 offset:30720\n\t" "s_waitcnt vmcnt(5)\n\t"
                         "v_smfmac_f32_16x16x64_bf16 v[40:43], v[16:19], v[192:199], v225\n\t" "ds_read_b128 v[124:127], v226 offset:31744\n\t" "s_waitcnt lgkmcnt(14)\n\t"
                         "v_smfmac_f32_16x16x64_bf16 v[44:47], v[16:19], v[64:71], v225\n\t" "ds_read_b128 v[64:67], v227\n\t" "v_smfmac_f32_16x16x64_bf16 v[48:51], v[16:19], v[200:207], v225\n\t"
                         "ds_read_b128 v[68:71], v227 offset:1024\n\t" "s_waitcnt lgkmcnt(14)\n\t" "v_smfmac_f32_16x16x64_bf16 v[52:55], v[16:19], v[72:79], v225\n\t" "ds_read_b128 v[72:75], v227 offset:16\n\t"
                         "s_waitcnt vmcnt(4)\n\t" "v_smfmac_f32_16x16x64_bf16 v[40:43], v[20:23], v[208:215], v225\n\t" "ds_read_b128 v[76:79], v227 offset:1040\n\t" "s_waitcnt lgkmcnt(14)\n\t"
                         "v_smfmac_f32_16x16x64_bf16 v[44:47], v[20:23], v[80:87], v225\n\t" "ds_read_b128 v[80:83], v227 offset:2048\n\t" "v_smfmac_f32_16x16x64_bf16 v[48:51], v[20:23], v[216:223], v225\n\t"
                         "ds_read_b128 v[84:87], v227 offset:3072\n\t" "s_waitcnt lgkmcnt(14)\n\t" "v_smfmac_f32_16x16x64_bf16 v[52:55], v[20:23], v[88:95], v225\n\t" "ds_read_b128 v[88:91], v227 offset:2064\n\t"
                         "s_waitcnt vmcnt(3)\n\t" "v_smfmac_f32_16x16x64_bf16 v[40:43], v[24:27], a[0:7], v225\n\t" "ds_read_b128 v[92:95], v227 offset:3088\n\t" "s_waitcnt lgkmcnt(14)\n\t"
                         "v_smfmac_f32_16x16x64_bf16 v[44:47], v[24:27], v[96:103], v225\n\t" "ds_read_b128 v[96:99], v227 offset:4096\n\t" "v_smfmac_f32_16x16x64_bf16 v[48:51], v[24:27], a[8:15], v225\n\t"
                         "ds_read_b128 v[100:103], v227 offset:5120\n\t" "s_waitcnt lgkmcnt(14)\n\t" "v_smfmac_f32_16x16x64_bf16 v[52:55], v[24:27], v[104:111], v225\n\t" "ds_read_b128 v[104:107], v227 offset:4112\n\t"
                         "s_waitcnt vmcnt(2)\n\t" "v_smfmac_f32_16x16x64_bf16 v[40:43], v[28:31], a[16:23], v225\n\t" "ds_read_b128 v[108:111], v227 offset:5136\n\t" "s_waitcnt lgkmcnt(14)\n\t"
                         "v_smfmac_f32_16x16x64_bf16 v[44:47], v[28:31], v[112:119], v225\n\t" "v_smfmac_f32_16x16x64_bf16 v[48:51], v[28:31], a[24:31], v225\n\t" "s_waitcnt lgkmcnt(12)\n\t"
                         "v_smfmac_f32_16x16x64_bf16 v[52:55], v[28:31], v[120:127], v225\n\t" "s_waitcnt vmcnt(1)\n\t" "v_smfmac_f32_16x16x64_bf16 v[40:43], v[32:35], a[32:39], v225\n\t" "v_mov_b64 v[112:113], 0\n\t"
                         "v_smfmac_f32_16x16x64_bf16 v[44:47], v[32:35], a[40:47], v225\n\t" "v_mov_b64 v[114:115], 0\n\t" "v_smfmac_f32_16x16x64_bf16 v[48:51], v[32:35], a[48:55], v225\n\t" "v_mov_b64 v[116:117], 0\n\t"
                         "v_smfmac_f32_16x16x64_bf16 v[52:55], v[32:35], a[56:63], v225\n\t" "v_mov_b64 v[118:119], 0\n\t" "s_waitcnt vmcnt(0)\n\t" "v_smfmac_f32_16x16x64_bf16 v[40:43], v[36:39], a[64:71], v225\n\t"
                         "v_mov_b64 v[120:121], 0\n\t" "v_smfmac_f32_16x16x64_bf16 v[44:47], v[36:39], a[72:79], v225\n\t" "v_mov_b64 v[122:123], 0\n\t" "v_smfmac_f32_16x16x64_bf16 v[48:51], v[36:39], a[80:87], v225\n\t"
                         "v_mov_b64 v[124:125], 0\n\t" "v_smfmac_f32_16x16x64_bf16 v[52:55], v[36:39], a[88:95], v225\n\t" "v_add_f32 v40, v40, v42\n\t" "v_add_f32 v41, v41, v43\n\t"
                         "v_smfmac_f32_16x16x64_bf16 v[56:59], v[0:3], a[96:103], v225\n\t" "v_cmp_u_f32 vcc, v40, v41\n\t" "v_smfmac_f32_16x16x64_bf16 v[56:59], v[4:7], a[104:111], v225\n\t"
                         "v_cvt_pk_bf16_f32 v42, v40, v41\n\t" "v_add_f32 v44, v44, v46\n\t" "v_smfmac_f32_16x16x64_bf16 v[56:59], v[8:11], a[112:119], v225\n\t" "s_and_b64 vcc, vcc, %13\n\t" "s_cbranch_scc1 .Lrt_s\n\t"
                         "v_smfmac_f32_16x16x64_bf16 v[56:59], v[12:15], a[120:127], v225\n\t" "v_add_f32 v45, v45, v47\n\t" "v_cvt_pk_bf16_f32 v43, v44, v45\n\t"
                         "v_smfmac_f32_16x16x64_bf16 v[56:59], v[16:19], a[128:135], v225\n\t" "ds_write_b64 v228, v[42:43]\n\t" "v_add_f32 v48, v48, v50\n\t"
                         "v_smfmac_f32_16x16x64_bf16 v[56:59], v[20:23], a[136:143], v225\n\t" "v_add_f32 v49, v49, v51\n\t" "v_cvt_pk_bf16_f32 v46, v48, v49\n\t"
                         "v_smfmac_f32_16x16x64_bf16 v[56:59], v[24:27], a[144:151], v225\n\t" "ds_write_b32 v229, v46\n\t" "v_add_f32 v52, v52, v54\n\t" "v_smfmac_f32_16x16x64_bf16 v[56:59], v[28:31], a[152:159], v225\n\t"
                         "v_add_f32 v53, v53, v55\n\t" "v_cvt_pk_bf16_f32 v54, v52, v53\n\t" "v_smfmac_f32_16x16x64_bf16 v[56:59], v[32:35], a[160:167], v225\n\t" "ds_write_b32 v230, v54\n\t"
                         "v_smfmac_f32_16x16x64_bf16 v[56:59], v[36:39], a[168:175], v225\n\t" "v_smfmac_f32_16x16x64_bf16 v[60:63], v[0:3], a[176:183], v225\n\t"
                         "v_smfmac_f32_16x16x64_bf16 v[60:63], v[4:7], a[184:191], v225\n\t" "v_add_f32 v56, v56, v58\n\t" "v_smfmac_f32_16x16x64_bf16 v[60:63], v[8:11], a[192:199], v225\n\t" "s_waitcnt lgkmcnt(0)\n\t"
                         "s_barrier\n\t" "v_smfmac_f32_16x16x64_bf16 v[60:63], v[12:15], a[200:207], v225\n\t" "ds_read_b64_tr_b16 v[40:41], v232\n\t" "v_add_f32 v57, v57, v59\n\t"
                         "v_smfmac_f32_16x16x64_bf16 v[60:63], v[16:19], a[208:215], v225\n\t" "ds_read_b64_tr_b16 v[42:43], v232 offset:32\n\t" "v_cvt_pk_bf16_f32 v58, v56, v57\n\t"
                         "v_smfmac_f32_16x16x64_bf16 v[60:63], v[20:23], a[216:223], v225\n\t" "ds_write_b32 v230, v58 offset:1152\n\t" "v_smfmac_f32_16x16x64_bf16 v[60:63], v[24:27], a[224:231], v225\n\t"
                         "ds_read_b64_tr_b16 v[44:45], v232 offset:1024\n\t" "ds_read_b64_tr_b16 v[46:47], v232 offset:1056\n\t" "v_smfmac_f32_16x16x64_bf16 v[60:63], v[28:31], a[232:239], v225\n\t"
                         "ds_read_b64_tr_b16 v[48:49], v232 offset:2048\n\t" "ds_read_b64_tr_b16 v[50:51], v232 offset:2080\n\t" "v_smfmac_f32_16x16x64_bf16 v[60:63], v[32:35], a[240:247], v225\n\t"
                         "ds_read_b64_tr_b16 v[0:1], v233\n\t" "ds_read_b64_tr_b16 v[2:3], v233 offset:32\n\t" "v_smfmac_f32_16x16x64_bf16 v[60:63], v[36:39], a[248:255], v225\n\t"
                         "ds_read_b64_tr_b16 v[4:5], v233 offset:576\n\t" "ds_read_b64_tr_b16 v[6:7], v233 offset:608\n\t" "s_waitcnt lgkmcnt(9)\n\t" "v_smfmac_f32_16x16x64_bf16 v[112:115], v[40:43], v[64:71], v225\n\t"
                         "s_waitcnt lgkmcnt(8)\n\t" "s_barrier\n\t" "v_smfmac_f32_16x16x64_bf16 v[116:119], v[40:43], v[72:79], v225\n\t" "ds_read_b64_tr_b16 v[8:9], v233 offset:1152\n\t" "s_waitcnt lgkmcnt(7)\n\t"
                         "v_add_f32 v60, v60, v62\n\t" "v_add_f32 v61, v61, v63\n\t" "v_smfmac_f32_16x16x64_bf16 v[112:115], v[44:47], v[80:87], v225\n\t" "ds_read_b64_tr_b16 v[10:11], v233 offset:1184\n\t"
                         "v_cvt_pk_bf16_f32 v62, v60, v61\n\t" "v_smfmac_f32_16x16x64_bf16 v[116:119], v[44:47], v[88:95], v225\n\t" "ds_write_b32 v230, v62 offset:2304\n\t" "s_waitcnt lgkmcnt(7)\n\t"
                         "v_smfmac_f32_16x16x64_bf16 v[112:115], v[48:51], v[96:103], v225\n\t" "ds_read_b64_tr_b16 v[12:13], v233 offset:1728\n\t" "v_smfmac_f32_16x16x64_bf16 v[116:119], v[48:51], v[104:111], v225\n\t"
                         "ds_read_b64_tr_b16 v[14:15], v233 offset:1760\n\t" "s_waitcnt lgkmcnt(5)\n\t" "v_smfmac_f32_16x16x64_bf16 v[120:123], v[40:43], v[0:7], v225\n\t" "v_cndmask_b32 v24, v112, v113, %10\n\t"
                         "v_cndmask_b32 v25, v114, v115, %10\n\t" "v_add_f32 v24, v24, v25\n\t" "v_pk_add_f32 v[26:27], v[116:117], v[118:119]\n\t" "v_cndmask_b32 v25, v26, v27, %10\n\t" "v_max_f32 v26, v24, v25\n\t"
                         "s_waitcnt lgkmcnt(2)\n\t" "s_barrier\n\t" "ds_read_b64_tr_b16 v[16:17], v233 offset:2304\n\t" "v_max_f32_dpp v27, v26, v26 row_ror:4 row_mask:0xf bank_mask:0xf\n\t"
                         "ds_read_b64_tr_b16 v[18:19], v233 offset:2336\n\t" "v_max_f32_dpp v27, v26, v27 row_ror:8 row_mask:0xf bank_mask:0xf\n\t" "v_max_f32_dpp v27, v26, v27 row_ror:12 row_mask:0xf bank_mask:0xf\n\t"
                         "ds_read_b64_tr_b16 v[20:21], v233 offset:2880\n\t" "ds_read_b64_tr_b16 v[22:23], v233 offset:2912\n\t" "v_max_f32_dpp v30, v27, v27 row_ror:1 row_mask:0xf bank_mask:0xf\n\t"
                         "v_max_f32_dpp v30, v27, v30 row_ror:2 row_mask:0xf bank_mask:0xf\n\t" "v_max_f32_dpp v30, v27, v30 row_ror:3 row_mask:0xf bank_mask:0xf\n\t" "s_waitcnt lgkmcnt(4)\n\t"
                         "v_pk_add_f32 v[24:25], v[24:25], v[30:31] op_sel_hi:[1,0] neg_lo:[0,1] neg_hi:[0,1]\n\t" "v_pk_mul_f32 v[24:25], v[24:25], v[236:237] op_sel_hi:[1,0]\n\t" "v_exp_f32 v24, v24\n\t"
                         "v_exp_f32 v25, v25\n\t" "v_mul_f32 v31, %12, v30\n\t" "v_cvt_pk_bf16_f32 v28, |v24|, |v25|\n\t" "v_smfmac_f32_16x16x64_bf16 v[120:123], v[44:47], v[8:15], v225\n\t"
                         "global_store_dword v234, v28, %9 offset:-4096 sc1\n\t" "s_waitcnt lgkmcnt(0)\n\t" "v_add_f32 v26, v24, v25\n\t" "v_smfmac_f32_16x16x64_bf16 v[120:123], v[48:51], v[16:23], v225\n\t"
                         "s_nop 0\n\t" "v_add_f32_dpp v27, v26, v26 row_ror:4 row_mask:0xf bank_mask:0xf\n\t" "v_add_f32_dpp v27, v26, v27 row_ror:8 row_mask:0xf bank_mask:0xf\n\t"
                         "v_add_f32_dpp v27, v26, v27 row_ror:12 row_mask:0xf bank_mask:0xf\n\t" "s_nop 1\n\t" "v_add_f32_dpp v29, v27, v27 row_ror:1 row_mask:0xf bank_mask:0xf\n\t"
                         "v_pk_add_f32 v[24:25], v[120:121], v[122:123]\n\t" "v_cndmask_b32 v24, v24, v25, %10\n\t" "v_max_f32 v26, v24, v30\n\t" "v_add_f32_dpp v29, v27, v29 row_ror:2 row_mask:0xf bank_mask:0xf\n\t"
                         "v_add_f32_dpp v29, v27, v29 row_ror:3 row_mask:0xf bank_mask:0xf\n\t" "v_max_f32_dpp v27, v26, v26 row_ror:1 row_mask:0xf bank_mask:0xf\n\t"
                         "v_max_f32_dpp v27, v26, v27 row_ror:2 row_mask:0xf bank_mask:0xf\n\t" "v_max_f32_dpp v27, v26, v27 row_ror:3 row_mask:0xf bank_mask:0xf\n\t" "v_mul_f32 v24, %12, v24\n\t"
                         "v_mul_f32 v29, 0x3e000000, v29\n\t" "v_max_f32_dpp v26, v27, v27 row_ror:4 row_mask:0xf bank_mask:0xf\n\t" "v_fma_f32 v24, -v26, %12, v24\n\t" "v_fma_f32 v25, -v26, %12, v31\n\t"
                         "v_exp_f32 v24, v24\n\t" "v_exp_f32 v25, v25\n\t" "s_nop 0\n\t" "v_fma_f32 v26, v25, v29, v24\n\t" "v_mov_b32_dpp v124, v24 quad_perm:[0,1,2,3] row_mask:0xf bank_mask:0x3\n\t"
                         "v_mov_b32_dpp v125, v24 quad_perm:[1,0,3,2] row_mask:0xf bank_mask:0x3\n\t" "v_add_f32_dpp v27, v26, v26 row_ror:1 row_mask:0xf bank_mask:0xf\n\t"
                         "v_add_f32_dpp v27, v26, v27 row_ror:2 row_mask:0xf bank_mask:0xf\n\t" "v_add_f32_dpp v27, v26, v27 row_ror:3 row_mask:0xf bank_mask:0xf\n\t"
                         "v_mov_b32_dpp v124, v25 quad_perm:[0,1,2,3] row_mask:0x5 bank_mask:0x4\n\t" "v_mov_b32_dpp v125, v25 quad_perm:[0,1,2,3] row_mask:0xa bank_mask:0x4\n\t"
                         "v_add_f32_dpp v26, v27, v27 row_ror:4 row_mask:0xf bank_mask:0xf\n\t" "v_rcp_f32 v26, v26\n\t" "s_mov_b64 exec, %11\n\t" "v_pk_mul_f32 v[124:125], v[124:125], v[26:27] op_sel_hi:[1,0]\n\t"
                         "v_cvt_pk_bf16_f32 v24, |v124|, |v125|\n\t" "global_store_dword v235, v24, %9 offset:-4096 sc1\n\t" "s_mov_b64 exec, -1\n\t" "s_memrealtime %1\n\t" "s_load_dword %2, %15, %14 offset:%16\n\t"
                         "ds_read_b64_tr_b16 v[36:37], v231\n\t" "ds_read_b64_tr_b16 v[38:39], v231 offset:32\n\t" "v_readfirstlane_b32 %3, v237\n\t" "s_mov_b32 m0, %18\n\t" "s_nop 0\n\t" "v_mov_b32 v237, %19\n\t"
                         "s_waitcnt lgkmcnt(0)\n\t" ".Lrt_s:\n\t" "s_nop 3\n\t" "ds_read_b128 v[64:67], v226\n\t" "v_mov_b64 v[40:41], 0\n\t" "ds_read_b128 v[68:71], v226 offset:1024\n\t" "v_mov_b64 v[42:43], 0\n\t"
                         "ds_read_b128 v[72:75], v226 offset:2048\n\t" "v_mov_b64 v[44:45], 0\n\t" "ds_read_b128 v[76:79], v226 offset:3072\n\t" "v_mov_b64 v[46:47], 0\n\t" "ds_read_b128 v[80:83], v226 offset:4096\n\t"
                         "v_mov_b64 v[48:49], 0\n\t" "ds_read_b128 v[84:87], v226 offset:5120\n\t" "v_mov_b64 v[50:51], 0\n\t" "ds_read_b128 v[88:91], v226 offset:6144\n\t" "v_mov_b64 v[52:53], 0\n\t"
                         "ds_read_b128 v[92:95], v226 offset:7168\n\t" "v_mov_b64 v[54:55], 0\n\t" "s_cbranch_vccnz .Lnd_s\n\t" "s_cmp_eq_u32 %3, -1\n\t" "s_cbranch_scc1 .Lnk_s\n\t" "global_load_lds_dwordx4 v224, %17 sc1\n\t"
                         "global_load_lds_dwordx4 v224, %17 offset:1024 sc1\n\t" "global_load_lds_dwordx4 v224, %17 offset:2048 sc1\n\t" ".Lnk_s:\n\t" "global_store_dwordx4 v239, v[36:39], %22 sc1\n\t" ".Lnd_s:\n\t"
                         "ds_read_b128 v[96:99], v226 offset:8192\n\t" "v_mov_b64 v[56:57], 0\n\t" "ds_read_b128 v[100:103], v226 offset:9216\n\t" "v_mov_b64 v[58:59], 0\n\t" "ds_read_b128 v[104:107], v226 offset:10240\n\t"
                         "v_mov_b64 v[60:61], 0\n\t" "ds_read_b128 v[108:111], v226 offset:11264\n\t" "v_mov_b64 v[62:63], 0\n\t" "ds_read_b128 v[112:115], v226 offset:12288\n\t"
                         "ds_read_b128 v[116:119], v226 offset:13312\n\t" "ds_read_b128 v[120:123], v226 offset:14336\n\t" "ds_read_b128 v[124:127], v226 offset:15360\n\t" "s_cbranch_vccz .Lok_s\n\t" "s_cmp_ge_u32 %5, 3\n\t"
                         "s_cbranch_scc1 .Lsk_s\n\t" "s_waitcnt vmcnt(0)\n\t" "s_memrealtime s[44:45]\n\t" "s_memtime s[46:47]\n\t" "s_add_u32 s42, %6, %23\n\t" "s_waitcnt lgkmcnt(0)\n\t" "s_sub_i32 s44, s42, s44\n\t"
                         "s_cmp_lt_i32 s44, -16\n\t" "s_cbranch_scc1 .Lsk_s\n\t" "s_mov_b32 %6, s42\n\t" "s_add_u32 %5, %5, 1\n\t" "s_mul_i32 s44, s44, %24\n\t" "s_ashr_i32 s44, s44, 6\n\t" "s_add_u32 s46, s46, s44\n\t"
                         "s_sub_u32 %4, s46, 0x5a\n\t" "s_branch .Lsp_s\n\t" ".Lok_s:\n\t" "s_and_b64 exec, %20, 1\n\t" "s_cbranch_execz .Lnf_s\n\t" "s_sleep 2\n\t" "global_load_dword v237, v237, %21 sc1\n\t" ".Lnf_s:\n\t"
                         "s_mov_b64 exec, -1\n\t" ".Lsk_s:\n\t" "s_mov_b64 %0, vcc\n\t"
                         : "=&s"(bad), "=&s"(tp8), "=&s"(tt), "=&s"(kf), "+s"(tgt), "+s"(tries), "+s"(phi)
                         : "s"(memp + (h & 4) * 256), "s"(memp - (h & 4) * 256), "s"((u64)memp & ~8192ull), "s"(mb0), "s"(psm), "s"(kscale * 1.4426950408889634f), "s"(en), "s"(4 * s),
                           "s"(__builtin_amdgcn_kernarg_segment_ptr()), "n"(TT_OFF), "s"(kxp), "s"(kxw), "s"(kfo + (s < 6 ? 0 : (s + 2) >> 3) * 4096), "s"(0x0040404040404044ull >> s), "s"(fl), "s"(kmp), "s"(cfg.G), "s"(clk.cpt),
                           "{v[224:239]}"(ad), "{v[64:95]}"(wk[0]), "{v[96:127]}"(wk[1]), "{v[32:63]}"(wk[2]),
                           "{a[0:31]}"(wa[0]), "{a[32:63]}"(wa[1]), "{a[64:95]}"(wa[2]), "{a[96:127]}"(wa[3]), "{a[128:159]}"(wa[4]), "{a[160:191]}"(wa[5]), "{a[192:223]}"(wa[6]), "{a[224:255]}"(wa[7]), "{v[128:159]}"(wv[0]), "{v[160:191]}"(wv[1]), "{v[192:223]}"(wv[2])
                         : "v0", "v1", "v2", "v3", "v4", "v5", "v6", "v7", "v8", "v9", "v10", "v11", "v12", "v13", "v14", "v15", "v16", "v17", "v18", "v19", "v20", "v21", "v22", "v23", "v24", "v25", "v26", "v27", "v28", "v29", "v30", "v31", "vcc", "scc", "s42", "s44", "s45", "s46", "s47", "memory");
            if (__builtin_expect(bad == 0, 1)) break;
            waitvm<0>();
            if (((++tries & 15) == 0 && !unwritten(memp, ad[0])) || --budget < 0) en = 0;
            phi += (u32)cfg.G;
        }
        if (__builtin_expect(kf == -1, 0)) { flag_wait(fl, F_K + pblk(s + 1) * 32 + h, 1); kxdma(); }
        kxp += s < NSEG - 2 ? NH * 12288 : 0;
        clk.calib();
        if (__builtin_expect(s >= NSEG - 2, 0)) {
            waitvm<0>();
            barrier();
            if (w == 3) flag_add(fl, F_PROG + 8 * s + (h & 7), 1);
        }
        if (s == 0) { phi = go_wg(fl, h, w, lds + SL_PF - 16) - (tt >> 16); edge(clk.r1, clk.m1); clk.calib(); }
        follow(phi, (int)(tt >> 16) - (cfg.dV >> 24), (u32)tp8, cfg.G);
        phi += tt & 0xffffu;
        memp = p.ws + mslot(sd, s + 1); kmp += NH * 3072;
    }
    if (h == 0 && w == 0) {
        flag_wait(fl, F_ACK, 256);
        Sync y;
        sync_ld(y, p.ws);
        asm volatile("s_waitcnt vmcnt(0)" : "+v"(y.r), "+v"(y.g), "+v"(y.e) : : "memory");
        const u64 t = ((u64)y.e[1] << 32 | y.e[0]) + 1;
        if (lanex() == 0) gst8(p.ws + OFF_LAT, 1024, u32x2{(u32)t, (u32)(t >> 32)});
    }
    asm volatile(".Lkm_last:\n\ts_waitcnt vmcnt(0)" : : : "memory");
}

DI void proj_role(const Ptrs& p, const ChainCfg cfg, int h, u32 lds, int w) {
    u32x32 wa[8], wv[4];
    u32 phi;
    int sd;
    {
        const int lane = lanex(), tid = 64 * w + lane;
        const int g = lane >> 4, qq = (lane >> 2) & 3, pp = lane & 3;
        constexpr int GS = 3104;
        auto dvo = [&](int i) { const int ix = i * 64 + lane; return (u32)((ix / 24) * (HD * 2) + (ix % 24) * 16); };
        auto ovo = [&](int i) { const int u = i * 64 + lane, r = u / 80; return (u32)(r * 1280 + (((u % 80) ^ (2 * (r ^ (w & 2)))) << 4)); };
        const u32 tra = lds + (2 * (g >> 1) + (qq >> 1)) * GS + (4 * (g & 1) + (qq & 1)) * 384 + 96 * w + 32 * (w & 1) + 8 * pp;
        const u32 tro = (4 * g + (qq & 1) + 16 * (qq >> 1)) * 1280 + 8 * pp + ((u32)(2 * w ^ 2 * ((4 * g + qq) & 7)) << 4);
        u32x16 pa = u32x16{dvo(0), dvo(1), dvo(2), tra, tra - 96 * (w & 1), tro, lds + (u32)((w & 1) * 24576 + (w >> 1) * 99328 + lane * 16), (u32)(lane & 31) * 8,
                           ovo(0), ovo(1), ovo(2), ovo(3), ovo(4), tro, tro, tro};
        Sync y;
        asm volatile("s_nop 4\n\t" "s_add_u32 s42, %22, 0x0\n\t" "s_add_u32 s42, s44, s42\n\t" "s_addc_u32 s43, s45, 0\n\t" "s_add_u32 m0, %18, 0x0\n\t" "s_nop 0\n\t" "global_load_lds_dwordx4 v224, s[42:43]\n\t"
                     "s_add_u32 m0, %18, 0x400\n\t" "s_nop 0\n\t" "global_load_lds_dwordx4 v225, s[42:43]\n\t" "s_add_u32 m0, %18, 0x800\n\t" "s_nop 0\n\t" "global_load_lds_dwordx4 v226, s[42:43]\n\t"
                     "s_add_u32 s42, %22, 0x18000\n\t" "s_add_u32 s42, s44, s42\n\t" "s_addc_u32 s43, s45, 0\n\t" "s_add_u32 m0, %18, 0xc20\n\t" "s_nop 0\n\t" "global_load_lds_dwordx4 v224, s[42:43]\n\t"
                     "s_add_u32 m0, %18, 0x1020\n\t" "s_nop 0\n\t" "global_load_lds_dwordx4 v225, s[42:43]\n\t" "s_add_u32 m0, %18, 0x1420\n\t" "s_nop 0\n\t" "global_load_lds_dwordx4 v226, s[42:43]\n\t"
                     "s_sub_u32 s42, 0xc0000, %22\n\t" "s_add_u32 s42, s44, s42\n\t" "s_addc_u32 s43, s45, 0\n\t" "s_add_u32 m0, %18, 0x6100\n\t" "s_nop 0\n\t" "global_load_lds_dwordx4 v224, s[42:43]\n\t"
                     "s_add_u32 m0, %18, 0x6500\n\t" "s_nop 0\n\t" "global_load_lds_dwordx4 v225, s[42:43]\n\t" "s_add_u32 m0, %18, 0x6900\n\t" "s_nop 0\n\t" "global_load_lds_dwordx4 v226, s[42:43]\n\t"
                     "s_sub_u32 s42, 0xd8000, %22\n\t" "s_add_u32 s42, s44, s42\n\t" "s_addc_u32 s43, s45, 0\n\t" "s_add_u32 m0, %18, 0x6d20\n\t" "s_nop 0\n\t" "global_load_lds_dwordx4 v224, s[42:43]\n\t"
                     "s_add_u32 m0, %18, 0x7120\n\t" "s_nop 0\n\t" "global_load_lds_dwordx4 v225, s[42:43]\n\t" "s_add_u32 m0, %18, 0x7520\n\t" "s_nop 0\n\t" "global_load_lds_dwordx4 v226, s[42:43]\n\t"
                     "s_add_u32 s42, %22, 0x180000\n\t" "s_add_u32 s42, s44, s42\n\t" "s_addc_u32 s43, s45, 0\n\t" "s_add_u32 m0, %18, 0xc200\n\t" "s_nop 0\n\t" "global_load_lds_dwordx4 v224, s[42:43]\n\t"
                     "s_add_u32 m0, %18, 0xc600\n\t" "s_nop 0\n\t" "global_load_lds_dwordx4 v225, s[42:43]\n\t" "s_add_u32 m0, %18, 0xca00\n\t" "s_nop 0\n\t" "global_load_lds_dwordx4 v226, s[42:43]\n\t"
                     "s_add_u32 s42, %22, 0x198000\n\t" "s_add_u32 s42, s44, s42\n\t" "s_addc_u32 s43, s45, 0\n\t" "s_add_u32 m0, %18, 0xce20\n\t" "s_nop 0\n\t" "global_load_lds_dwordx4 v224, s[42:43]\n\t"
                     "s_add_u32 m0, %18, 0xd220\n\t" "s_nop 0\n\t" "global_load_lds_dwordx4 v225, s[42:43]\n\t" "s_add_u32 m0, %18, 0xd620\n\t" "s_nop 0\n\t" "global_load_lds_dwordx4 v226, s[42:43]\n\t"
                     "s_sub_u32 s42, 0x240000, %22\n\t" "s_add_u32 s42, s44, s42\n\t" "s_addc_u32 s43, s45, 0\n\t" "s_add_u32 m0, %18, 0x12300\n\t" "s_nop 0\n\t" "global_load_lds_dwordx4 v224, s[42:43]\n\t"
                     "s_add_u32 m0, %18, 0x12700\n\t" "s_nop 0\n\t" "global_load_lds_dwordx4 v225, s[42:43]\n\t" "s_add_u32 m0, %18, 0x12b00\n\t" "s_nop 0\n\t" "global_load_lds_dwordx4 v226, s[42:43]\n\t"
                     "s_sub_u32 s42, 0x258000, %22\n\t" "s_add_u32 s42, s44, s42\n\t" "s_addc_u32 s43, s45, 0\n\t" "s_add_u32 m0, %18, 0x12f20\n\t" "s_nop 0\n\t" "global_load_lds_dwordx4 v224, s[42:43]\n\t"
                     "s_add_u32 m0, %18, 0x13320\n\t" "s_nop 0\n\t" "global_load_lds_dwordx4 v225, s[42:43]\n\t" "s_add_u32 m0, %18, 0x13720\n\t" "s_nop 0\n\t" "global_load_lds_dwordx4 v226, s[42:43]\n\t"
                     "s_waitcnt vmcnt(12) lgkmcnt(0)\n\t" "s_barrier\n\t" "s_add_u32 s42, %22, 0x300000\n\t" "s_add_u32 s42, s44, s42\n\t" "s_addc_u32 s43, s45, 0\n\t" "s_add_u32 m0, %18, 0x18400\n\t" "s_nop 0\n\t"
                     "global_load_lds_dwordx4 v224, s[42:43]\n\t" "s_add_u32 m0, %18, 0x18800\n\t" "s_nop 0\n\t" "global_load_lds_dwordx4 v225, s[42:43]\n\t" "s_add_u32 m0, %18, 0x18c00\n\t" "s_nop 0\n\t"
                     "global_load_lds_dwordx4 v226, s[42:43]\n\t" "s_add_u32 s42, %22, 0x318000\n\t" "s_add_u32 s42, s44, s42\n\t" "s_addc_u32 s43, s45, 0\n\t" "s_add_u32 m0, %18, 0x19020\n\t" "s_nop 0\n\t"
                     "global_load_lds_dwordx4 v224, s[42:43]\n\t" "s_add_u32 m0, %18, 0x19420\n\t" "s_nop 0\n\t" "global_load_lds_dwordx4 v225, s[42:43]\n\t" "s_add_u32 m0, %18, 0x19820\n\t" "s_nop 0\n\t"
                     "global_load_lds_dwordx4 v226, s[42:43]\n\t" "s_sub_u32 s42, 0x3c0000, %22\n\t" "s_add_u32 s42, s44, s42\n\t" "s_addc_u32 s43, s45, 0\n\t" "s_add_u32 m0, %18, 0x1e500\n\t" "s_nop 0\n\t"
                     "global_load_lds_dwordx4 v224, s[42:43]\n\t" "s_add_u32 m0, %18, 0x1e900\n\t" "s_nop 0\n\t" "global_load_lds_dwordx4 v225, s[42:43]\n\t" "s_add_u32 m0, %18, 0x1ed00\n\t" "s_nop 0\n\t"
                     "global_load_lds_dwordx4 v226, s[42:43]\n\t" "s_sub_u32 s42, 0x3d8000, %22\n\t" "s_add_u32 s42, s44, s42\n\t" "s_addc_u32 s43, s45, 0\n\t" "s_add_u32 m0, %18, 0x1f120\n\t" "s_nop 0\n\t"
                     "global_load_lds_dwordx4 v224, s[42:43]\n\t" "s_add_u32 m0, %18, 0x1f520\n\t" "s_nop 0\n\t" "global_load_lds_dwordx4 v225, s[42:43]\n\t" "s_add_u32 m0, %18, 0x1f920\n\t" "s_nop 0\n\t"
                     "global_load_lds_dwordx4 v226, s[42:43]\n\t" "v_add_u32 v237, 0x0, v227\n\t" "v_add_u32 v238, 0x0, v228\n\t" "ds_read_b64_tr_b16 a[0:1], v237\n\t" "ds_read_b64_tr_b16 a[2:3], v237 offset:768\n\t"
                     "ds_read_b64_tr_b16 a[4:5], v237 offset:12416\n\t" "ds_read_b64_tr_b16 a[6:7], v237 offset:13184\n\t" "ds_read_b64_tr_b16 v[0:1], v237 offset:32\n\t" "ds_read_b64_tr_b16 v[2:3], v237 offset:800\n\t"
                     "ds_read_b64_tr_b16 v[4:5], v237 offset:12448\n\t" "ds_read_b64_tr_b16 v[6:7], v237 offset:13216\n\t" "ds_read_b64_tr_b16 a[8:9], v238 offset:64\n\t" "ds_read_b64_tr_b16 a[10:11], v238 offset:832\n\t"
                     "ds_read_b64_tr_b16 a[12:13], v238 offset:12480\n\t" "ds_read_b64_tr_b16 a[14:15], v238 offset:13248\n\t" "ds_read_b64_tr_b16 a[32:33], v237 offset:24832\n\t"
                     "ds_read_b64_tr_b16 a[34:35], v237 offset:25600\n\t" "ds_read_b64_tr_b16 a[36:37], v237 offset:37248\n\t" "ds_read_b64_tr_b16 a[38:39], v237 offset:38016\n\t"
                     "ds_read_b64_tr_b16 v[16:17], v237 offset:24864\n\t" "ds_read_b64_tr_b16 v[18:19], v237 offset:25632\n\t" "ds_read_b64_tr_b16 v[20:21], v237 offset:37280\n\t"
                     "ds_read_b64_tr_b16 v[22:23], v237 offset:38048\n\t" "ds_read_b64_tr_b16 a[40:41], v238 offset:24896\n\t" "ds_read_b64_tr_b16 a[42:43], v238 offset:25664\n\t"
                     "ds_read_b64_tr_b16 a[44:45], v238 offset:37312\n\t" "ds_read_b64_tr_b16 a[46:47], v238 offset:38080\n\t" "s_waitcnt vmcnt(12) lgkmcnt(0)\n\t" "s_barrier\n\t" "s_add_u32 s42, %22, 0x480000\n\t"
                     "s_add_u32 s42, s44, s42\n\t" "s_addc_u32 s43, s45, 0\n\t" "s_add_u32 m0, %18, 0x0\n\t" "s_nop 0\n\t" "global_load_lds_dwordx4 v224, s[42:43]\n\t" "s_add_u32 m0, %18, 0x400\n\t" "s_nop 0\n\t"
                     "global_load_lds_dwordx4 v225, s[42:43]\n\t" "s_add_u32 m0, %18, 0x800\n\t" "s_nop 0\n\t" "global_load_lds_dwordx4 v226, s[42:43]\n\t" "s_add_u32 s42, %22, 0x498000\n\t" "s_add_u32 s42, s44, s42\n\t"
                     "s_addc_u32 s43, s45, 0\n\t" "s_add_u32 m0, %18, 0xc20\n\t" "s_nop 0\n\t" "global_load_lds_dwordx4 v224, s[42:43]\n\t" "s_add_u32 m0, %18, 0x1020\n\t" "s_nop 0\n\t"
                     "global_load_lds_dwordx4 v225, s[42:43]\n\t" "s_add_u32 m0, %18, 0x1420\n\t" "s_nop 0\n\t" "global_load_lds_dwordx4 v226, s[42:43]\n\t" "s_sub_u32 s42, 0x540000, %22\n\t" "s_add_u32 s42, s44, s42\n\t"
                     "s_addc_u32 s43, s45, 0\n\t" "s_add_u32 m0, %18, 0x6100\n\t" "s_nop 0\n\t" "global_load_lds_dwordx4 v224, s[42:43]\n\t" "s_add_u32 m0, %18, 0x6500\n\t" "s_nop 0\n\t"
                     "global_load_lds_dwordx4 v225, s[42:43]\n\t" "s_add_u32 m0, %18, 0x6900\n\t" "s_nop 0\n\t" "global_load_lds_dwordx4 v226, s[42:43]\n\t" "s_sub_u32 s42, 0x558000, %22\n\t" "s_add_u32 s42, s44, s42\n\t"
                     "s_addc_u32 s43, s45, 0\n\t" "s_add_u32 m0, %18, 0x6d20\n\t" "s_nop 0\n\t" "global_load_lds_dwordx4 v224, s[42:43]\n\t" "s_add_u32 m0, %18, 0x7120\n\t" "s_nop 0\n\t"
                     "global_load_lds_dwordx4 v225, s[42:43]\n\t" "s_add_u32 m0, %18, 0x7520\n\t" "s_nop 0\n\t" "global_load_lds_dwordx4 v226, s[42:43]\n\t" "v_add_u32 v237, 0xc200, v227\n\t"
                     "v_add_u32 v238, 0xc200, v228\n\t" "ds_read_b64_tr_b16 a[16:17], v237\n\t" "ds_read_b64_tr_b16 a[18:19], v237 offset:768\n\t" "ds_read_b64_tr_b16 a[20:21], v237 offset:12416\n\t"
                     "ds_read_b64_tr_b16 a[22:23], v237 offset:13184\n\t" "ds_read_b64_tr_b16 v[8:9], v237 offset:32\n\t" "ds_read_b64_tr_b16 v[10:11], v237 offset:800\n\t"
                     "ds_read_b64_tr_b16 v[12:13], v237 offset:12448\n\t" "ds_read_b64_tr_b16 v[14:15], v237 offset:13216\n\t" "ds_read_b64_tr_b16 a[24:25], v238 offset:64\n\t"
                     "ds_read_b64_tr_b16 a[26:27], v238 offset:832\n\t" "ds_read_b64_tr_b16 a[28:29], v238 offset:12480\n\t" "ds_read_b64_tr_b16 a[30:31], v238 offset:13248\n\t"
                     "ds_read_b64_tr_b16 a[48:49], v237 offset:24832\n\t" "ds_read_b64_tr_b16 a[50:51], v237 offset:25600\n\t" "ds_read_b64_tr_b16 a[52:53], v237 offset:37248\n\t"
                     "ds_read_b64_tr_b16 a[54:55], v237 offset:38016\n\t" "ds_read_b64_tr_b16 v[24:25], v237 offset:24864\n\t" "ds_read_b64_tr_b16 v[26:27], v237 offset:25632\n\t"
                     "ds_read_b64_tr_b16 v[28:29], v237 offset:37280\n\t" "ds_read_b64_tr_b16 v[30:31], v237 offset:38048\n\t" "ds_read_b64_tr_b16 a[56:57], v238 offset:24896\n\t"
                     "ds_read_b64_tr_b16 a[58:59], v238 offset:25664\n\t" "ds_read_b64_tr_b16 a[60:61], v238 offset:37312\n\t" "ds_read_b64_tr_b16 a[62:63], v238 offset:38080\n\t" "s_waitcnt vmcnt(12) lgkmcnt(0)\n\t"
                     "s_barrier\n\t" "s_add_u32 s42, %22, 0x600000\n\t" "s_add_u32 s42, s44, s42\n\t" "s_addc_u32 s43, s45, 0\n\t" "s_add_u32 m0, %18, 0xc200\n\t" "s_nop 0\n\t" "global_load_lds_dwordx4 v224, s[42:43]\n\t"
                     "s_add_u32 m0, %18, 0xc600\n\t" "s_nop 0\n\t" "global_load_lds_dwordx4 v225, s[42:43]\n\t" "s_add_u32 m0, %18, 0xca00\n\t" "s_nop 0\n\t" "global_load_lds_dwordx4 v226, s[42:43]\n\t"
                     "s_add_u32 s42, %22, 0x618000\n\t" "s_add_u32 s42, s44, s42\n\t" "s_addc_u32 s43, s45, 0\n\t" "s_add_u32 m0, %18, 0xce20\n\t" "s_nop 0\n\t" "global_load_lds_dwordx4 v224, s[42:43]\n\t"
                     "s_add_u32 m0, %18, 0xd220\n\t" "s_nop 0\n\t" "global_load_lds_dwordx4 v225, s[42:43]\n\t" "s_add_u32 m0, %18, 0xd620\n\t" "s_nop 0\n\t" "global_load_lds_dwordx4 v226, s[42:43]\n\t"
                     "s_sub_u32 s42, 0x6c0000, %22\n\t" "s_add_u32 s42, s44, s42\n\t" "s_addc_u32 s43, s45, 0\n\t" "s_add_u32 m0, %18, 0x12300\n\t" "s_nop 0\n\t" "global_load_lds_dwordx4 v224, s[42:43]\n\t"
                     "s_add_u32 m0, %18, 0x12700\n\t" "s_nop 0\n\t" "global_load_lds_dwordx4 v225, s[42:43]\n\t" "s_add_u32 m0, %18, 0x12b00\n\t" "s_nop 0\n\t" "global_load_lds_dwordx4 v226, s[42:43]\n\t"
                     "s_sub_u32 s42, 0x6d8000, %22\n\t" "s_add_u32 s42, s44, s42\n\t" "s_addc_u32 s43, s45, 0\n\t" "s_add_u32 m0, %18, 0x12f20\n\t" "s_nop 0\n\t" "global_load_lds_dwordx4 v224, s[42:43]\n\t"
                     "s_add_u32 m0, %18, 0x13320\n\t" "s_nop 0\n\t" "global_load_lds_dwordx4 v225, s[42:43]\n\t" "s_add_u32 m0, %18, 0x13720\n\t" "s_nop 0\n\t" "global_load_lds_dwordx4 v226, s[42:43]\n\t"
                     "v_add_u32 v237, 0x18400, v227\n\t" "v_add_u32 v238, 0x18400, v228\n\t" "ds_read_b64_tr_b16 a[64:65], v237\n\t" "ds_read_b64_tr_b16 a[66:67], v237 offset:768\n\t"
                     "ds_read_b64_tr_b16 a[68:69], v237 offset:12416\n\t" "ds_read_b64_tr_b16 a[70:71], v237 offset:13184\n\t" "ds_read_b64_tr_b16 v[32:33], v237 offset:32\n\t"
                     "ds_read_b64_tr_b16 v[34:35], v237 offset:800\n\t" "ds_read_b64_tr_b16 v[36:37], v237 offset:12448\n\t" "ds_read_b64_tr_b16 v[38:39], v237 offset:13216\n\t"
                     "ds_read_b64_tr_b16 a[72:73], v238 offset:64\n\t" "ds_read_b64_tr_b16 a[74:75], v238 offset:832\n\t" "ds_read_b64_tr_b16 a[76:77], v238 offset:12480\n\t"
                     "ds_read_b64_tr_b16 a[78:79], v238 offset:13248\n\t" "ds_read_b64_tr_b16 a[80:81], v237 offset:24832\n\t" "ds_read_b64_tr_b16 a[82:83], v237 offset:25600\n\t"
                     "ds_read_b64_tr_b16 a[84:85], v237 offset:37248\n\t" "ds_read_b64_tr_b16 a[86:87], v237 offset:38016\n\t" "ds_read_b64_tr_b16 v[40:41], v237 offset:24864\n\t"
                     "ds_read_b64_tr_b16 v[42:43], v237 offset:25632\n\t" "ds_read_b64_tr_b16 v[44:45], v237 offset:37280\n\t" "ds_read_b64_tr_b16 v[46:47], v237 offset:38048\n\t"
                     "ds_read_b64_tr_b16 a[88:89], v238 offset:24896\n\t" "ds_read_b64_tr_b16 a[90:91], v238 offset:25664\n\t" "ds_read_b64_tr_b16 a[92:93], v238 offset:37312\n\t"
                     "ds_read_b64_tr_b16 a[94:95], v238 offset:38080\n\t" "s_waitcnt vmcnt(12) lgkmcnt(0)\n\t" "s_barrier\n\t" "s_add_u32 s42, s46, 0x0\n\t" "s_addc_u32 s43, s47, 0\n\t" "s_add_u32 m0, %20, 0x18400\n\t"
                     "s_nop 0\n\t" "global_load_lds_dwordx4 v232, s[42:43]\n\t" "s_add_u32 m0, %20, 0x18800\n\t" "s_nop 0\n\t" "global_load_lds_dwordx4 v233, s[42:43]\n\t" "s_add_u32 m0, %20, 0x18c00\n\t" "s_nop 0\n\t"
                     "global_load_lds_dwordx4 v234, s[42:43]\n\t" "s_add_u32 m0, %20, 0x19000\n\t" "s_nop 0\n\t" "global_load_lds_dwordx4 v235, s[42:43]\n\t" "s_add_u32 m0, %20, 0x19400\n\t" "s_nop 0\n\t"
                     "global_load_lds_dwordx4 v236, s[42:43]\n\t" "s_add_u32 s42, s46, 0x1400\n\t" "s_addc_u32 s43, s47, 0\n\t" "s_add_u32 m0, %20, 0x19800\n\t" "v_xor_b32 v224, 0x80, v232\n\t"
                     "global_load_lds_dwordx4 v224, s[42:43]\n\t" "s_add_u32 m0, %20, 0x19c00\n\t" "v_xor_b32 v224, 0x80, v233\n\t" "global_load_lds_dwordx4 v224, s[42:43]\n\t" "s_add_u32 m0, %20, 0x1a000\n\t"
                     "v_xor_b32 v224, 0x80, v234\n\t" "global_load_lds_dwordx4 v224, s[42:43]\n\t" "s_add_u32 m0, %20, 0x1a400\n\t" "v_xor_b32 v224, 0x80, v235\n\t" "global_load_lds_dwordx4 v224, s[42:43]\n\t"
                     "s_add_u32 m0, %20, 0x1a800\n\t" "v_xor_b32 v224, 0x80, v236\n\t" "global_load_lds_dwordx4 v224, s[42:43]\n\t" "v_add_u32 v237, 0x0, v227\n\t" "v_add_u32 v238, 0x0, v228\n\t"
                     "ds_read_b64_tr_b16 a[96:97], v237\n\t" "ds_read_b64_tr_b16 a[98:99], v237 offset:768\n\t" "ds_read_b64_tr_b16 a[100:101], v237 offset:12416\n\t" "ds_read_b64_tr_b16 a[102:103], v237 offset:13184\n\t"
                     "ds_read_b64_tr_b16 v[48:49], v237 offset:32\n\t" "ds_read_b64_tr_b16 v[50:51], v237 offset:800\n\t" "ds_read_b64_tr_b16 v[52:53], v237 offset:12448\n\t"
                     "ds_read_b64_tr_b16 v[54:55], v237 offset:13216\n\t" "ds_read_b64_tr_b16 a[104:105], v238 offset:64\n\t" "ds_read_b64_tr_b16 a[106:107], v238 offset:832\n\t"
                     "ds_read_b64_tr_b16 a[108:109], v238 offset:12480\n\t" "ds_read_b64_tr_b16 a[110:111], v238 offset:13248\n\t" "ds_read_b64_tr_b16 a[112:113], v237 offset:24832\n\t"
                     "ds_read_b64_tr_b16 a[114:115], v237 offset:25600\n\t" "ds_read_b64_tr_b16 a[116:117], v237 offset:37248\n\t" "ds_read_b64_tr_b16 a[118:119], v237 offset:38016\n\t"
                     "ds_read_b64_tr_b16 v[56:57], v237 offset:24864\n\t" "ds_read_b64_tr_b16 v[58:59], v237 offset:25632\n\t" "ds_read_b64_tr_b16 v[60:61], v237 offset:37280\n\t"
                     "ds_read_b64_tr_b16 v[62:63], v237 offset:38048\n\t" "ds_read_b64_tr_b16 a[120:121], v238 offset:24896\n\t" "ds_read_b64_tr_b16 a[122:123], v238 offset:25664\n\t"
                     "ds_read_b64_tr_b16 a[124:125], v238 offset:37312\n\t" "ds_read_b64_tr_b16 a[126:127], v238 offset:38080\n\t" "s_waitcnt vmcnt(10) lgkmcnt(0)\n\t" "s_barrier\n\t" "s_add_u32 s42, s46, 0xa000\n\t"
                     "s_addc_u32 s43, s47, 0\n\t" "s_add_u32 m0, %20, 0x0\n\t" "s_nop 0\n\t" "global_load_lds_dwordx4 v232, s[42:43]\n\t" "s_add_u32 m0, %20, 0x400\n\t" "s_nop 0\n\t"
                     "global_load_lds_dwordx4 v233, s[42:43]\n\t" "s_add_u32 m0, %20, 0x800\n\t" "s_nop 0\n\t" "global_load_lds_dwordx4 v234, s[42:43]\n\t" "s_add_u32 m0, %20, 0xc00\n\t" "s_nop 0\n\t"
                     "global_load_lds_dwordx4 v235, s[42:43]\n\t" "s_add_u32 m0, %20, 0x1000\n\t" "s_nop 0\n\t" "global_load_lds_dwordx4 v236, s[42:43]\n\t" "s_add_u32 s42, s46, 0xb400\n\t" "s_addc_u32 s43, s47, 0\n\t"
                     "s_add_u32 m0, %20, 0x1400\n\t" "v_xor_b32 v224, 0x80, v232\n\t" "global_load_lds_dwordx4 v224, s[42:43]\n\t" "s_add_u32 m0, %20, 0x1800\n\t" "v_xor_b32 v224, 0x80, v233\n\t"
                     "global_load_lds_dwordx4 v224, s[42:43]\n\t" "s_add_u32 m0, %20, 0x1c00\n\t" "v_xor_b32 v224, 0x80, v234\n\t" "global_load_lds_dwordx4 v224, s[42:43]\n\t" "s_add_u32 m0, %20, 0x2000\n\t"
                     "v_xor_b32 v224, 0x80, v235\n\t" "global_load_lds_dwordx4 v224, s[42:43]\n\t" "s_add_u32 m0, %20, 0x2400\n\t" "v_xor_b32 v224, 0x80, v236\n\t" "global_load_lds_dwordx4 v224, s[42:43]\n\t"
                     "v_add_u32 v237, 0xc200, v227\n\t" "v_add_u32 v238, 0xc200, v228\n\t" "ds_read_b64_tr_b16 a[128:129], v237\n\t" "ds_read_b64_tr_b16 a[130:131], v237 offset:768\n\t"
                     "ds_read_b64_tr_b16 a[132:133], v237 offset:12416\n\t" "ds_read_b64_tr_b16 a[134:135], v237 offset:13184\n\t" "ds_read_b64_tr_b16 a[136:137], v237 offset:32\n\t"
                     "ds_read_b64_tr_b16 a[138:139], v237 offset:800\n\t" "ds_read_b64_tr_b16 a[140:141], v237 offset:12448\n\t" "ds_read_b64_tr_b16 a[142:143], v237 offset:13216\n\t"
                     "ds_read_b64_tr_b16 a[144:145], v238 offset:64\n\t" "ds_read_b64_tr_b16 a[146:147], v238 offset:832\n\t" "ds_read_b64_tr_b16 a[148:149], v238 offset:12480\n\t"
                     "ds_read_b64_tr_b16 a[150:151], v238 offset:13248\n\t" "ds_read_b64_tr_b16 a[152:153], v237 offset:24832\n\t" "ds_read_b64_tr_b16 a[154:155], v237 offset:25600\n\t"
                     "ds_read_b64_tr_b16 a[156:157], v237 offset:37248\n\t" "ds_read_b64_tr_b16 a[158:159], v237 offset:38016\n\t" "ds_read_b64_tr_b16 a[160:161], v237 offset:24864\n\t"
                     "ds_read_b64_tr_b16 a[162:163], v237 offset:25632\n\t" "ds_read_b64_tr_b16 a[164:165], v237 offset:37280\n\t" "ds_read_b64_tr_b16 a[166:167], v237 offset:38048\n\t"
                     "ds_read_b64_tr_b16 a[168:169], v238 offset:24896\n\t" "ds_read_b64_tr_b16 a[170:171], v238 offset:25664\n\t" "ds_read_b64_tr_b16 a[172:173], v238 offset:37312\n\t"
                     "ds_read_b64_tr_b16 a[174:175], v238 offset:38080\n\t" "s_waitcnt vmcnt(10) lgkmcnt(0)\n\t" "s_barrier\n\t" "s_add_u32 s42, s46, 0x14000\n\t" "s_addc_u32 s43, s47, 0\n\t"
                     "s_add_u32 m0, %20, 0xc200\n\t" "s_nop 0\n\t" "global_load_lds_dwordx4 v232, s[42:43]\n\t" "s_add_u32 m0, %20, 0xc600\n\t" "s_nop 0\n\t" "global_load_lds_dwordx4 v233, s[42:43]\n\t"
                     "s_add_u32 m0, %20, 0xca00\n\t" "s_nop 0\n\t" "global_load_lds_dwordx4 v234, s[42:43]\n\t" "s_add_u32 m0, %20, 0xce00\n\t" "s_nop 0\n\t" "global_load_lds_dwordx4 v235, s[42:43]\n\t"
                     "s_add_u32 m0, %20, 0xd200\n\t" "s_nop 0\n\t" "global_load_lds_dwordx4 v236, s[42:43]\n\t" "s_add_u32 s42, s46, 0x15400\n\t" "s_addc_u32 s43, s47, 0\n\t" "s_add_u32 m0, %20, 0xd600\n\t"
                     "v_xor_b32 v224, 0x80, v232\n\t" "global_load_lds_dwordx4 v224, s[42:43]\n\t" "s_add_u32 m0, %20, 0xda00\n\t" "v_xor_b32 v224, 0x80, v233\n\t" "global_load_lds_dwordx4 v224, s[42:43]\n\t"
                     "s_add_u32 m0, %20, 0xde00\n\t" "v_xor_b32 v224, 0x80, v234\n\t" "global_load_lds_dwordx4 v224, s[42:43]\n\t" "s_add_u32 m0, %20, 0xe200\n\t" "v_xor_b32 v224, 0x80, v235\n\t"
                     "global_load_lds_dwordx4 v224, s[42:43]\n\t" "s_add_u32 m0, %20, 0xe600\n\t" "v_xor_b32 v224, 0x80, v236\n\t" "global_load_lds_dwordx4 v224, s[42:43]\n\t" "s_add_u32 s42, %21, 0x18400\n\t"
                     "v_xor_b32 v237, 0x0, v229\n\t" "v_add_u32 v237, s42, v237\n\t" "v_xor_b32 v238, 0x40, v229\n\t" "v_add_u32 v238, s42, v238\n\t" "v_xor_b32 v239, 0x80, v229\n\t" "v_add_u32 v239, s42, v239\n\t"
                     "v_xor_b32 v226, 0xc0, v229\n\t" "v_add_u32 v226, s42, v226\n\t" "ds_read_b64_tr_b16 a[176:177], v237\n\t" "ds_read_b64_tr_b16 a[178:179], v238 offset:2560\n\t"
                     "ds_read_b64_tr_b16 a[200:201], v239\n\t" "ds_read_b64_tr_b16 a[202:203], v226 offset:2560\n\t" "ds_read_b64_tr_b16 a[224:225], v237 offset:256\n\t"
                     "ds_read_b64_tr_b16 a[226:227], v238 offset:2816\n\t" "ds_read_b64_tr_b16 a[248:249], v239 offset:256\n\t" "ds_read_b64_tr_b16 a[250:251], v226 offset:2816\n\t"
                     "ds_read_b64_tr_b16 v[112:113], v237 offset:512\n\t" "ds_read_b64_tr_b16 v[114:115], v238 offset:3072\n\t" "ds_read_b64_tr_b16 v[136:137], v239 offset:512\n\t"
                     "ds_read_b64_tr_b16 v[138:139], v226 offset:3072\n\t" "ds_read_b64_tr_b16 v[160:161], v237 offset:768\n\t" "ds_read_b64_tr_b16 v[162:163], v238 offset:3328\n\t"
                     "ds_read_b64_tr_b16 v[184:185], v239 offset:768\n\t" "ds_read_b64_tr_b16 v[186:187], v226 offset:3328\n\t" "ds_read_b64_tr_b16 v[208:209], v237 offset:1024\n\t"
                     "ds_read_b64_tr_b16 v[210:211], v238 offset:3584\n\t" "ds_read_b64_tr_b16 v[72:73], v239 offset:1024\n\t" "ds_read_b64_tr_b16 v[74:75], v226 offset:3584\n\t" "s_waitcnt vmcnt(10) lgkmcnt(0)\n\t"
                     "s_barrier\n\t" "s_add_u32 s42, s46, 0x1e000\n\t" "s_addc_u32 s43, s47, 0\n\t" "s_add_u32 m0, %20, 0x18400\n\t" "s_nop 0\n\t" "global_load_lds_dwordx4 v232, s[42:43]\n\t"
                     "s_add_u32 m0, %20, 0x18800\n\t" "s_nop 0\n\t" "global_load_lds_dwordx4 v233, s[42:43]\n\t" "s_add_u32 m0, %20, 0x18c00\n\t" "s_nop 0\n\t" "global_load_lds_dwordx4 v234, s[42:43]\n\t"
                     "s_add_u32 m0, %20, 0x19000\n\t" "s_nop 0\n\t" "global_load_lds_dwordx4 v235, s[42:43]\n\t" "s_add_u32 m0, %20, 0x19400\n\t" "s_nop 0\n\t" "global_load_lds_dwordx4 v236, s[42:43]\n\t"
                     "s_add_u32 s42, s46, 0x1f400\n\t" "s_addc_u32 s43, s47, 0\n\t" "s_add_u32 m0, %20, 0x19800\n\t" "v_xor_b32 v224, 0x80, v232\n\t" "global_load_lds_dwordx4 v224, s[42:43]\n\t"
                     "s_add_u32 m0, %20, 0x19c00\n\t" "v_xor_b32 v224, 0x80, v233\n\t" "global_load_lds_dwordx4 v224, s[42:43]\n\t" "s_add_u32 m0, %20, 0x1a000\n\t" "v_xor_b32 v224, 0x80, v234\n\t"
                     "global_load_lds_dwordx4 v224, s[42:43]\n\t" "s_add_u32 m0, %20, 0x1a400\n\t" "v_xor_b32 v224, 0x80, v235\n\t" "global_load_lds_dwordx4 v224, s[42:43]\n\t" "s_add_u32 m0, %20, 0x1a800\n\t"
                     "v_xor_b32 v224, 0x80, v236\n\t" "global_load_lds_dwordx4 v224, s[42:43]\n\t" "s_add_u32 s42, %21, 0x0\n\t" "v_xor_b32 v237, 0x0, v229\n\t" "v_add_u32 v237, s42, v237\n\t"
                     "v_xor_b32 v238, 0x40, v229\n\t" "v_add_u32 v238, s42, v238\n\t" "v_xor_b32 v239, 0x80, v229\n\t" "v_add_u32 v239, s42, v239\n\t" "v_xor_b32 v226, 0xc0, v229\n\t" "v_add_u32 v226, s42, v226\n\t"
                     "ds_read_b64_tr_b16 a[180:181], v237\n\t" "ds_read_b64_tr_b16 a[182:183], v238 offset:2560\n\t" "ds_read_b64_tr_b16 a[204:205], v239\n\t" "ds_read_b64_tr_b16 a[206:207], v226 offset:2560\n\t"
                     "ds_read_b64_tr_b16 a[228:229], v237 offset:256\n\t" "ds_read_b64_tr_b16 a[230:231], v238 offset:2816\n\t" "ds_read_b64_tr_b16 a[252:253], v239 offset:256\n\t"
                     "ds_read_b64_tr_b16 a[254:255], v226 offset:2816\n\t" "ds_read_b64_tr_b16 v[116:117], v237 offset:512\n\t" "ds_read_b64_tr_b16 v[118:119], v238 offset:3072\n\t"
                     "ds_read_b64_tr_b16 v[140:141], v239 offset:512\n\t" "ds_read_b64_tr_b16 v[142:143], v226 offset:3072\n\t" "ds_read_b64_tr_b16 v[164:165], v237 offset:768\n\t"
                     "ds_read_b64_tr_b16 v[166:167], v238 offset:3328\n\t" "ds_read_b64_tr_b16 v[188:189], v239 offset:768\n\t" "ds_read_b64_tr_b16 v[190:191], v226 offset:3328\n\t"
                     "ds_read_b64_tr_b16 v[212:213], v237 offset:1024\n\t" "ds_read_b64_tr_b16 v[214:215], v238 offset:3584\n\t" "ds_read_b64_tr_b16 v[76:77], v239 offset:1024\n\t"
                     "ds_read_b64_tr_b16 v[78:79], v226 offset:3584\n\t" "s_waitcnt vmcnt(10) lgkmcnt(0)\n\t" "s_barrier\n\t" "v_mov_b32 v225, 0\n\t" "global_load_dwordx2 %12, v231, %19 sc1\n\t"
                     "global_load_dwordx2 %13, v225, %19 offset:512 sc1\n\t" "global_load_dwordx2 %14, v225, %19 offset:1024 sc1\n\t" "s_add_u32 s42, s46, 0x28000\n\t" "s_addc_u32 s43, s47, 0\n\t"
                     "s_add_u32 m0, %20, 0x0\n\t" "s_nop 0\n\t" "global_load_lds_dwordx4 v232, s[42:43]\n\t" "s_add_u32 m0, %20, 0x400\n\t" "s_nop 0\n\t" "global_load_lds_dwordx4 v233, s[42:43]\n\t"
                     "s_add_u32 m0, %20, 0x800\n\t" "s_nop 0\n\t" "global_load_lds_dwordx4 v234, s[42:43]\n\t" "s_add_u32 m0, %20, 0xc00\n\t" "s_nop 0\n\t" "global_load_lds_dwordx4 v235, s[42:43]\n\t"
                     "s_add_u32 m0, %20, 0x1000\n\t" "s_nop 0\n\t" "global_load_lds_dwordx4 v236, s[42:43]\n\t" "s_add_u32 s42, s46, 0x29400\n\t" "s_addc_u32 s43, s47, 0\n\t" "s_add_u32 m0, %20, 0x1400\n\t"
                     "v_xor_b32 v224, 0x80, v232\n\t" "global_load_lds_dwordx4 v224, s[42:43]\n\t" "s_add_u32 m0, %20, 0x1800\n\t" "v_xor_b32 v224, 0x80, v233\n\t" "global_load_lds_dwordx4 v224, s[42:43]\n\t"
                     "s_add_u32 m0, %20, 0x1c00\n\t" "v_xor_b32 v224, 0x80, v234\n\t" "global_load_lds_dwordx4 v224, s[42:43]\n\t" "s_add_u32 m0, %20, 0x2000\n\t" "v_xor_b32 v224, 0x80, v235\n\t"
                     "global_load_lds_dwordx4 v224, s[42:43]\n\t" "s_add_u32 m0, %20, 0x2400\n\t" "v_xor_b32 v224, 0x80, v236\n\t" "global_load_lds_dwordx4 v224, s[42:43]\n\t" "s_add_u32 s42, %21, 0xc200\n\t"
                     "v_xor_b32 v237, 0x0, v229\n\t" "v_add_u32 v237, s42, v237\n\t" "v_xor_b32 v238, 0x40, v229\n\t" "v_add_u32 v238, s42, v238\n\t" "v_xor_b32 v239, 0x80, v229\n\t" "v_add_u32 v239, s42, v239\n\t"
                     "v_xor_b32 v226, 0xc0, v229\n\t" "v_add_u32 v226, s42, v226\n\t" "ds_read_b64_tr_b16 a[184:185], v237\n\t" "ds_read_b64_tr_b16 a[186:187], v238 offset:2560\n\t"
                     "ds_read_b64_tr_b16 a[208:209], v239\n\t" "ds_read_b64_tr_b16 a[210:211], v226 offset:2560\n\t" "ds_read_b64_tr_b16 a[232:233], v237 offset:256\n\t"
                     "ds_read_b64_tr_b16 a[234:235], v238 offset:2816\n\t" "ds_read_b64_tr_b16 v[96:97], v239 offset:256\n\t" "ds_read_b64_tr_b16 v[98:99], v226 offset:2816\n\t"
                     "ds_read_b64_tr_b16 v[120:121], v237 offset:512\n\t" "ds_read_b64_tr_b16 v[122:123], v238 offset:3072\n\t" "ds_read_b64_tr_b16 v[144:145], v239 offset:512\n\t"
                     "ds_read_b64_tr_b16 v[146:147], v226 offset:3072\n\t" "ds_read_b64_tr_b16 v[168:169], v237 offset:768\n\t" "ds_read_b64_tr_b16 v[170:171], v238 offset:3328\n\t"
                     "ds_read_b64_tr_b16 v[192:193], v239 offset:768\n\t" "ds_read_b64_tr_b16 v[194:195], v226 offset:3328\n\t" "ds_read_b64_tr_b16 v[216:217], v237 offset:1024\n\t"
                     "ds_read_b64_tr_b16 v[218:219], v238 offset:3584\n\t" "ds_read_b64_tr_b16 v[80:81], v239 offset:1024\n\t" "ds_read_b64_tr_b16 v[82:83], v226 offset:3584\n\t" "s_waitcnt vmcnt(10) lgkmcnt(0)\n\t"
                     "s_barrier\n\t" "s_add_u32 s42, s46, 0x32000\n\t" "s_addc_u32 s43, s47, 0\n\t" "s_add_u32 m0, %20, 0xc200\n\t" "s_nop 0\n\t" "global_load_lds_dwordx4 v232, s[42:43]\n\t" "s_add_u32 m0, %20, 0xc600\n\t"
                     "s_nop 0\n\t" "global_load_lds_dwordx4 v233, s[42:43]\n\t" "s_add_u32 m0, %20, 0xca00\n\t" "s_nop 0\n\t" "global_load_lds_dwordx4 v234, s[42:43]\n\t" "s_add_u32 m0, %20, 0xce00\n\t" "s_nop 0\n\t"
                     "global_load_lds_dwordx4 v235, s[42:43]\n\t" "s_add_u32 m0, %20, 0xd200\n\t" "s_nop 0\n\t" "global_load_lds_dwordx4 v236, s[42:43]\n\t" "s_add_u32 s42, s46, 0x33400\n\t" "s_addc_u32 s43, s47, 0\n\t"
                     "s_add_u32 m0, %20, 0xd600\n\t" "v_xor_b32 v224, 0x80, v232\n\t" "global_load_lds_dwordx4 v224, s[42:43]\n\t" "s_add_u32 m0, %20, 0xda00\n\t" "v_xor_b32 v224, 0x80, v233\n\t"
                     "global_load_lds_dwordx4 v224, s[42:43]\n\t" "s_add_u32 m0, %20, 0xde00\n\t" "v_xor_b32 v224, 0x80, v234\n\t" "global_load_lds_dwordx4 v224, s[42:43]\n\t" "s_add_u32 m0, %20, 0xe200\n\t"
                     "v_xor_b32 v224, 0x80, v235\n\t" "global_load_lds_dwordx4 v224, s[42:43]\n\t" "s_add_u32 m0, %20, 0xe600\n\t" "v_xor_b32 v224, 0x80, v236\n\t" "global_load_lds_dwordx4 v224, s[42:43]\n\t"
                     "s_add_u32 s42, %21, 0x18400\n\t" "v_xor_b32 v237, 0x0, v229\n\t" "v_add_u32 v237, s42, v237\n\t" "v_xor_b32 v238, 0x40, v229\n\t" "v_add_u32 v238, s42, v238\n\t" "v_xor_b32 v239, 0x80, v229\n\t"
                     "v_add_u32 v239, s42, v239\n\t" "v_xor_b32 v226, 0xc0, v229\n\t" "v_add_u32 v226, s42, v226\n\t" "ds_read_b64_tr_b16 a[188:189], v237\n\t" "ds_read_b64_tr_b16 a[190:191], v238 offset:2560\n\t"
                     "ds_read_b64_tr_b16 a[212:213], v239\n\t" "ds_read_b64_tr_b16 a[214:215], v226 offset:2560\n\t" "ds_read_b64_tr_b16 a[236:237], v237 offset:256\n\t"
                     "ds_read_b64_tr_b16 a[238:239], v238 offset:2816\n\t" "ds_read_b64_tr_b16 v[100:101], v239 offset:256\n\t" "ds_read_b64_tr_b16 v[102:103], v226 offset:2816\n\t"
                     "ds_read_b64_tr_b16 v[124:125], v237 offset:512\n\t" "ds_read_b64_tr_b16 v[126:127], v238 offset:3072\n\t" "ds_read_b64_tr_b16 v[148:149], v239 offset:512\n\t"
                     "ds_read_b64_tr_b16 v[150:151], v226 offset:3072\n\t" "ds_read_b64_tr_b16 v[172:173], v237 offset:768\n\t" "ds_read_b64_tr_b16 v[174:175], v238 offset:3328\n\t"
                     "ds_read_b64_tr_b16 v[196:197], v239 offset:768\n\t" "ds_read_b64_tr_b16 v[198:199], v226 offset:3328\n\t" "ds_read_b64_tr_b16 v[220:221], v237 offset:1024\n\t"
                     "ds_read_b64_tr_b16 v[222:223], v238 offset:3584\n\t" "ds_read_b64_tr_b16 v[84:85], v239 offset:1024\n\t" "ds_read_b64_tr_b16 v[86:87], v226 offset:3584\n\t" "s_waitcnt vmcnt(10) lgkmcnt(0)\n\t"
                     "s_barrier\n\t" "s_add_u32 s42, %21, 0x0\n\t" "v_xor_b32 v237, 0x0, v229\n\t" "v_add_u32 v237, s42, v237\n\t" "v_xor_b32 v238, 0x40, v229\n\t" "v_add_u32 v238, s42, v238\n\t"
                     "v_xor_b32 v239, 0x80, v229\n\t" "v_add_u32 v239, s42, v239\n\t" "v_xor_b32 v226, 0xc0, v229\n\t" "v_add_u32 v226, s42, v226\n\t" "ds_read_b64_tr_b16 a[192:193], v237\n\t"
                     "ds_read_b64_tr_b16 a[194:195], v238 offset:2560\n\t" "ds_read_b64_tr_b16 a[216:217], v239\n\t" "ds_read_b64_tr_b16 a[218:219], v226 offset:2560\n\t"
                     "ds_read_b64_tr_b16 a[240:241], v237 offset:256\n\t" "ds_read_b64_tr_b16 a[242:243], v238 offset:2816\n\t" "ds_read_b64_tr_b16 v[104:105], v239 offset:256\n\t"
                     "ds_read_b64_tr_b16 v[106:107], v226 offset:2816\n\t" "ds_read_b64_tr_b16 v[128:129], v237 offset:512\n\t" "ds_read_b64_tr_b16 v[130:131], v238 offset:3072\n\t"
                     "ds_read_b64_tr_b16 v[152:153], v239 offset:512\n\t" "ds_read_b64_tr_b16 v[154:155], v226 offset:3072\n\t" "ds_read_b64_tr_b16 v[176:177], v237 offset:768\n\t"
                     "ds_read_b64_tr_b16 v[178:179], v238 offset:3328\n\t" "ds_read_b64_tr_b16 v[200:201], v239 offset:768\n\t" "ds_read_b64_tr_b16 v[202:203], v226 offset:3328\n\t"
                     "ds_read_b64_tr_b16 v[64:65], v237 offset:1024\n\t" "ds_read_b64_tr_b16 v[66:67], v238 offset:3584\n\t" "ds_read_b64_tr_b16 v[88:89], v239 offset:1024\n\t"
                     "ds_read_b64_tr_b16 v[90:91], v226 offset:3584\n\t" "s_waitcnt vmcnt(0) lgkmcnt(0)\n\t" "s_barrier\n\t" "s_add_u32 s42, %21, 0xc200\n\t" "v_xor_b32 v237, 0x0, v229\n\t" "v_add_u32 v237, s42, v237\n\t"
                     "v_xor_b32 v238, 0x40, v229\n\t" "v_add_u32 v238, s42, v238\n\t" "v_xor_b32 v239, 0x80, v229\n\t" "v_add_u32 v239, s42, v239\n\t" "v_xor_b32 v226, 0xc0, v229\n\t" "v_add_u32 v226, s42, v226\n\t"
                     "ds_read_b64_tr_b16 a[196:197], v237\n\t" "ds_read_b64_tr_b16 a[198:199], v238 offset:2560\n\t" "ds_read_b64_tr_b16 a[220:221], v239\n\t" "ds_read_b64_tr_b16 a[222:223], v226 offset:2560\n\t"
                     "ds_read_b64_tr_b16 a[244:245], v237 offset:256\n\t" "ds_read_b64_tr_b16 a[246:247], v238 offset:2816\n\t" "ds_read_b64_tr_b16 v[108:109], v239 offset:256\n\t"
                     "ds_read_b64_tr_b16 v[110:111], v226 offset:2816\n\t" "ds_read_b64_tr_b16 v[132:133], v237 offset:512\n\t" "ds_read_b64_tr_b16 v[134:135], v238 offset:3072\n\t"
                     "ds_read_b64_tr_b16 v[156:157], v239 offset:512\n\t" "ds_read_b64_tr_b16 v[158:159], v226 offset:3072\n\t" "ds_read_b64_tr_b16 v[180:181], v237 offset:768\n\t"
                     "ds_read_b64_tr_b16 v[182:183], v238 offset:3328\n\t" "ds_read_b64_tr_b16 v[204:205], v239 offset:768\n\t" "ds_read_b64_tr_b16 v[206:207], v226 offset:3328\n\t"
                     "ds_read_b64_tr_b16 v[68:69], v237 offset:1024\n\t" "ds_read_b64_tr_b16 v[70:71], v238 offset:3584\n\t" "ds_read_b64_tr_b16 v[92:93], v239 offset:1024\n\t"
                     "ds_read_b64_tr_b16 v[94:95], v226 offset:3584\n\t" "s_waitcnt lgkmcnt(0)\n\t" "ds_write_b128 v230, v[0:3]\n\t" "ds_write_b128 v230, v[4:7] offset:1024\n\t"
                     "ds_write_b128 v230, v[8:11] offset:2048\n\t" "ds_write_b128 v230, v[12:15] offset:3072\n\t" "ds_write_b128 v230, v[16:19] offset:4096\n\t" "ds_write_b128 v230, v[20:23] offset:5120\n\t"
                     "ds_write_b128 v230, v[24:27] offset:6144\n\t" "ds_write_b128 v230, v[28:31] offset:7168\n\t" "ds_write_b128 v230, v[32:35] offset:8192\n\t" "ds_write_b128 v230, v[36:39] offset:9216\n\t"
                     "ds_write_b128 v230, v[40:43] offset:10240\n\t" "ds_write_b128 v230, v[44:47] offset:11264\n\t" "ds_write_b128 v230, v[48:51] offset:12288\n\t" "ds_write_b128 v230, v[52:55] offset:13312\n\t"
                     "ds_write_b128 v230, v[56:59] offset:14336\n\t" "ds_write_b128 v230, v[60:63] offset:15360\n\t" "ds_write_b128 v230, v[64:67] offset:16384\n\t" "ds_write_b128 v230, v[68:71] offset:17408\n\t"
                     "ds_write_b128 v230, v[72:75] offset:18432\n\t" "ds_write_b128 v230, v[76:79] offset:19456\n\t" "ds_write_b128 v230, v[80:83] offset:20480\n\t" "ds_write_b128 v230, v[84:87] offset:21504\n\t"
                     "ds_write_b128 v230, v[88:91] offset:22528\n\t" "ds_write_b128 v230, v[92:95] offset:23552\n\t"
                     : "=&{a[0:31]}"(wa[0]), "=&{a[32:63]}"(wa[1]), "=&{a[64:95]}"(wa[2]), "=&{a[96:127]}"(wa[3]), "=&{a[128:159]}"(wa[4]), "=&{a[160:191]}"(wa[5]), "=&{a[192:223]}"(wa[6]), "=&{a[224:255]}"(wa[7]), "=&{v[96:127]}"(wv[0]), "=&{v[128:159]}"(wv[1]), "=&{v[160:191]}"(wv[2]), "=&{v[192:223]}"(wv[3]), "=&v"(y.r), "=&v"(y.g), "=&v"(y.e), "+{v[224:239]}"(pa)
                     : "{s[44:45]}"(p.wv + h * DH * 2 + (size_t)(16 * w) * (HD * 2)), "{s[46:47]}"(p.wo + (size_t)(h * DH + 8 * w) * 1280),
                       "s"(lds + 2 * w * GS), "s"(p.ws + OFF_LAT), "s"(lds + w * 10240), "s"(lds), "s"((h & 4) * 16 * HD * 2)
                     : "v0", "v1", "v2", "v3", "v4", "v5", "v6", "v7", "v8", "v9", "v10", "v11", "v12", "v13", "v14", "v15", "v16", "v17", "v18", "v19", "v20", "v21", "v22", "v23", "v24", "v25", "v26", "v27", "v28", "v29", "v30", "v31", "v32", "v33", "v34", "v35", "v36", "v37", "v38", "v39", "v40", "v41", "v42", "v43", "v44", "v45", "v46", "v47", "v48", "v49", "v50", "v51", "v52", "v53", "v54", "v55", "v56", "v57", "v58", "v59", "v60", "v61", "v62", "v63", "v64", "v65", "v66", "v67", "v68", "v69", "v70", "v71", "v72", "v73", "v74", "v75", "v76", "v77", "v78", "v79", "v80", "v81", "v82", "v83", "v84", "v85", "v86", "v87", "v88", "v89", "v90", "v91", "v92", "v93", "v94", "v95", "s42", "s43", "scc", "memory");
        sd = sync_end(y, p.ws, phi, w == 0);
        if (w == 3) {
            u32 r;
            rdy_add(r, p.ws + OFF_ST);
            asm volatile("s_waitcnt vmcnt(0)" : "+v"(r) : : "memory");
            rdy_end(r, p.ws + OFF_ST, cfg.s1 >> 8 & 255);
        }
        barrier();
    }
    const int lane = lanex(), tid = 64 * w + lane;
    const int n = lane & 15, g = lane >> 4, qq = (lane >> 2) & 3, pp = lane & 3;
    char* const fl = p.ws + OFF_ST;
    const u32 iw = lds + 65536 + ((lane * 8) ^ (lane & 32));
    const u32 qr0 = lds + 65536 + (g >> 1) * 512 + pp * 128 + 64 * (g & 1) + 8 * qq + 32 * (pp >> 1);
    const u32 uo = (u32)((4 * w + (n >> 3)) * 4096 + h * 128 + (n & 7) * 16);
    u32x16 ad = u32x16{(u32)lane * 16 + 1024 + (h & 4) * 256, (lane & 2) ? 0xEEEEu : 0x4444u, lds + (u32)((w & 1) * 24576 + (w >> 1) * 99328 + lane * 16), iw + ((3 * w + (w & 1)) >> 1) * 512,
                       iw + (1 + 3 * (w >> 1)) * 512 + 4 * (w & 1), qr0, qr0 ^ 32, uo + (g & 1) * 8192 + (1 + (g >> 1)) * 65536, uo + (g & 1) * 8192 + (g >> 1) * 8, uo, (u32)lane * 16 + 1024 - (h & 4) * 256, uo, uo, uo, uo, uo};
    asm volatile("" : "+{v[224:239]}"(ad));
    const char* memp = p.ws + mslot(sd, 0);
    int budget = 30000;
    Clock clk; clk.init();
    for (int s = 0; s < NSEG; ++s) {
        int tries = 0;
        u64 bad, tp8, en = ~0ull;
        while (true) {
            int tgt = uni((int)(s ? clk.sched(phi, 0, cfg.G, tries ? 2 : 0) : mt()));
            char* up = p.ws + uslot(sd, s);
            asm volatile(".Lst_u:\n\t" "ds_read_b128 v[64:67], v226\n\t" "ds_read_b128 v[68:71], v226 offset:1024\n\t" "ds_read_b128 v[72:75], v226 offset:2048\n\t" "ds_read_b128 v[76:79], v226 offset:3072\n\t"
                         "ds_read_b128 v[80:83], v226 offset:4096\n\t" "ds_read_b128 v[84:87], v226 offset:5120\n\t" "ds_read_b128 v[88:91], v226 offset:6144\n\t" "ds_read_b128 v[92:95], v226 offset:7168\n\t"
                         "v_mov_b64 v[40:41], 0\n\t" "v_mov_b64 v[42:43], 0\n\t" "v_mov_b64 v[44:45], 0\n\t" "v_mov_b64 v[46:47], 0\n\t" "v_mov_b64 v[48:49], 0\n\t" "v_mov_b64 v[50:51], 0\n\t" ".p2align 5\n\t" ".Lsp_u:\n\t"
                         "s_memtime vcc\n\t" "s_waitcnt lgkmcnt(0)\n\t" "s_sub_i32 s42, %3, vcc_lo\n\t" "s_cmp_gt_i32 s42, 80\n\t" "s_cbranch_scc1 .Lsp_u\n\t" "s_max_i32 s42, s42, 0\n\t" "s_sub_i32 s42, 92, s42\n\t"
                         "s_and_b32 s42, s42, -4\n\t" "s_getpc_b64 vcc\n\t" "s_add_u32 vcc_lo, vcc_lo, s42\n\t" "s_addc_u32 vcc_hi, vcc_hi, 0\n\t" "s_setpc_b64 vcc\n\t" "s_nop 0\n\t" "s_nop 0\n\t" "s_nop 0\n\t" "s_nop 0\n\t"
                         "s_nop 0\n\t" "s_nop 0\n\t" "s_nop 0\n\t" "s_nop 0\n\t" "s_nop 0\n\t" "s_nop 0\n\t" "s_nop 0\n\t" "s_nop 0\n\t" "s_nop 0\n\t" "s_nop 0\n\t" "s_nop 0\n\t" "s_nop 0\n\t" "s_nop 0\n\t" "s_nop 0\n\t"
                         "s_nop 0\n\t" "s_nop 0\n\t" "global_load_dwordx4 v[0:3], v224, %6 offset:-4096 sc1\n\t" "s_nop 10\n\t" "global_load_dwordx4 v[4:7], v224, %6 offset:-2048 sc1\n\t" "s_nop 15\n\t"
                         "global_load_dwordx4 v[8:11], v234, %6 offset:-3072 sc1\n\t" "s_nop 10\n\t" "global_load_dwordx4 v[12:15], v234, %6 offset:-1024 sc1\n\t" "s_nop 10\n\t" "global_load_dwordx4 v[16:19], v224, %6 sc1\n\t"
                         "s_nop 10\n\t" "global_load_dwordx4 v[20:23], v234, %6 offset:1024 sc1\n\t" "s_nop 10\n\t" "global_load_dwordx4 v[24:27], v224, %6 offset:2048 sc1\n\t" "s_nop 10\n\t"
                         "global_load_dwordx4 v[28:31], v234, %6 offset:3072 sc1\n\t" "s_nop 10\n\t" "global_load_dwordx4 v[32:35], v224, %7 offset:-4096 sc1\n\t" "s_nop 10\n\t"
                         "global_load_dwordx4 v[36:39], v234, %7 offset:-3072 sc1\n\t" "s_waitcnt vmcnt(9)\n\t" "v_smfmac_f32_16x16x64_bf16 v[40:43], v[0:3], a[0:7], v225\n\t"
                         "v_smfmac_f32_16x16x64_bf16 v[44:47], v[0:3], v[64:71], v225\n\t" "ds_read_b128 v[64:67], v226 offset:8192\n\t" "v_smfmac_f32_16x16x64_bf16 v[48:51], v[0:3], a[8:15], v225\n\t"
                         "ds_read_b128 v[68:71], v226 offset:9216\n\t" "v_cmp_eq_u32 vcc, -1, v0\n\t" "s_waitcnt vmcnt(8)\n\t" "v_smfmac_f32_16x16x64_bf16 v[40:43], v[4:7], a[16:23], v225\n\t" "v_mov_b64 v[0:1], 0\n\t"
                         "v_smfmac_f32_16x16x64_bf16 v[44:47], v[4:7], v[72:79], v225\n\t" "s_and_b64 vcc, vcc, %8\n\t" "s_cbranch_scc1 .Lrt_u\n\t" "ds_read_b128 v[72:75], v226 offset:10240\n\t" "v_mov_b64 v[2:3], 0\n\t"
                         "v_smfmac_f32_16x16x64_bf16 v[48:51], v[4:7], a[24:31], v225\n\t" "ds_read_b128 v[76:79], v226 offset:11264\n\t" "s_waitcnt vmcnt(7)\n\t"
                         "v_smfmac_f32_16x16x64_bf16 v[40:43], v[8:11], a[32:39], v225\n\t" "v_mov_b64 v[4:5], 0\n\t" "v_smfmac_f32_16x16x64_bf16 v[44:47], v[8:11], v[80:87], v225\n\t"
                         "ds_read_b128 v[80:83], v226 offset:12288\n\t" "v_mov_b64 v[6:7], 0\n\t" "v_smfmac_f32_16x16x64_bf16 v[48:51], v[8:11], a[40:47], v225\n\t" "ds_read_b128 v[84:87], v226 offset:13312\n\t"
                         "s_waitcnt vmcnt(6)\n\t" "v_smfmac_f32_16x16x64_bf16 v[40:43], v[12:15], a[48:55], v225\n\t" "v_mov_b64 v[8:9], 0\n\t" "v_smfmac_f32_16x16x64_bf16 v[44:47], v[12:15], v[88:95], v225\n\t"
                         "ds_read_b128 v[88:91], v226 offset:14336\n\t" "v_mov_b64 v[10:11], 0\n\t" "v_smfmac_f32_16x16x64_bf16 v[48:51], v[12:15], a[56:63], v225\n\t" "ds_read_b128 v[92:95], v226 offset:15360\n\t"
                         "s_waitcnt vmcnt(5)\n\t" "v_smfmac_f32_16x16x64_bf16 v[40:43], v[16:19], a[64:71], v225\n\t" "v_mov_b64 v[12:13], 0\n\t" "s_waitcnt lgkmcnt(6)\n\t"
                         "v_smfmac_f32_16x16x64_bf16 v[44:47], v[16:19], v[64:71], v225\n\t" "ds_read_b128 v[64:67], v226 offset:16384\n\t" "v_mov_b64 v[14:15], 0\n\t"
                         "v_smfmac_f32_16x16x64_bf16 v[48:51], v[16:19], a[72:79], v225\n\t" "ds_read_b128 v[68:71], v226 offset:17408\n\t" "s_waitcnt vmcnt(4)\n\t"
                         "v_smfmac_f32_16x16x64_bf16 v[40:43], v[20:23], a[80:87], v225\n\t" "v_mov_b64 v[16:17], 0\n\t" "s_waitcnt lgkmcnt(6)\n\t" "v_smfmac_f32_16x16x64_bf16 v[44:47], v[20:23], v[72:79], v225\n\t"
                         "ds_read_b128 v[72:75], v226 offset:18432\n\t" "v_mov_b64 v[18:19], 0\n\t" "v_smfmac_f32_16x16x64_bf16 v[48:51], v[20:23], a[88:95], v225\n\t" "ds_read_b128 v[76:79], v226 offset:19456\n\t"
                         "s_waitcnt vmcnt(3)\n\t" "v_smfmac_f32_16x16x64_bf16 v[40:43], v[24:27], a[96:103], v225\n\t" "v_mov_b64 v[20:21], 0\n\t" "s_waitcnt lgkmcnt(6)\n\t"
                         "v_smfmac_f32_16x16x64_bf16 v[44:47], v[24:27], v[80:87], v225\n\t" "ds_read_b128 v[80:83], v226 offset:20480\n\t" "v_mov_b64 v[22:23], 0\n\t"
                         "v_smfmac_f32_16x16x64_bf16 v[48:51], v[24:27], a[104:111], v225\n\t" "ds_read_b128 v[84:87], v226 offset:21504\n\t" "s_waitcnt vmcnt(2)\n\t"
                         "v_smfmac_f32_16x16x64_bf16 v[40:43], v[28:31], a[112:119], v225\n\t" "v_mov_b64 v[24:25], 0\n\t" "s_waitcnt lgkmcnt(6)\n\t" "v_smfmac_f32_16x16x64_bf16 v[44:47], v[28:31], v[88:95], v225\n\t"
                         "ds_read_b128 v[88:91], v226 offset:22528\n\t" "v_mov_b64 v[26:27], 0\n\t" "v_smfmac_f32_16x16x64_bf16 v[48:51], v[28:31], a[120:127], v225\n\t" "ds_read_b128 v[92:95], v226 offset:23552\n\t"
                         "s_waitcnt vmcnt(1)\n\t" "v_smfmac_f32_16x16x64_bf16 v[40:43], v[32:35], a[128:135], v225\n\t" "v_mov_b64 v[28:29], 0\n\t" "v_smfmac_f32_16x16x64_bf16 v[44:47], v[32:35], a[136:143], v225\n\t"
                         "v_mov_b64 v[30:31], 0\n\t" "v_smfmac_f32_16x16x64_bf16 v[48:51], v[32:35], a[144:151], v225\n\t" "s_waitcnt vmcnt(0)\n\t" "v_smfmac_f32_16x16x64_bf16 v[40:43], v[36:39], a[152:159], v225\n\t"
                         "v_mov_b64 v[32:33], 0\n\t" "v_smfmac_f32_16x16x64_bf16 v[44:47], v[36:39], a[160:167], v225\n\t" "v_mov_b64 v[34:35], 0\n\t" "v_smfmac_f32_16x16x64_bf16 v[48:51], v[36:39], a[168:175], v225\n\t"
                         "v_add_f32 v52, v40, v42\n\t" "v_add_f32 v53, v41, v43\n\t" "v_cmp_u_f32 vcc, v52, v53\n\t" "v_add_f32 v54, v44, v46\n\t" "v_add_f32 v55, v45, v47\n\t" "v_cvt_pk_bf16_f32 v58, v52, v53\n\t"
                         "v_pk_add_f32 v[56:57], v[48:49], v[50:51]\n\t" "v_cvt_pk_bf16_f32 v59, v54, v55\n\t" "v_cvt_pk_bf16_f32 v60, v56, v57\n\t" "ds_write_b64 v227, v[58:59]\n\t" "ds_write_b32 v228, v60\n\t"
                         "v_mov_b64 v[36:37], 0\n\t" "v_mov_b64 v[38:39], 0\n\t" "s_and_b64 vcc, vcc, %8\n\t" "v_mov_b32 v233, 0\n\t" "s_cbranch_scc1 .Lrt_u\n\t" "v_mov_b32 v233, 0\n\t" "v_mov_b32 v233, 0\n\t"
                         "v_mov_b32 v233, 0\n\t" "s_waitcnt lgkmcnt(0)\n\t" "s_barrier\n\t" "ds_read_b64_tr_b16 v[40:41], v229\n\t" "ds_read_b64_tr_b16 v[42:43], v230\n\t" "v_mov_b32 v233, 0\n\t"
                         "ds_read_b64_tr_b16 v[44:45], v229 offset:1024\n\t" "v_mov_b32 v233, 0\n\t" "ds_read_b64_tr_b16 v[46:47], v230 offset:1024\n\t" "v_mov_b32 v233, 0\n\t"
                         "ds_read_b64_tr_b16 v[48:49], v229 offset:2048\n\t" "v_mov_b32 v233, 0\n\t" "ds_read_b64_tr_b16 v[50:51], v230 offset:2048\n\t" "v_mov_b32 v233, 0\n\t" "s_waitcnt lgkmcnt(4)\n\t"
                         "v_smfmac_f32_16x16x64_bf16 v[0:3], v[40:43], a[176:183], v225\n\t" "v_smfmac_f32_16x16x64_bf16 v[4:7], v[40:43], a[200:207], v225\n\t" "s_waitcnt lgkmcnt(2)\n\t"
                         "v_smfmac_f32_16x16x64_bf16 v[0:3], v[44:47], a[184:191], v225\n\t" "v_smfmac_f32_16x16x64_bf16 v[4:7], v[44:47], a[208:215], v225\n\t" "s_waitcnt lgkmcnt(0)\n\t"
                         "v_smfmac_f32_16x16x64_bf16 v[0:3], v[48:51], a[192:199], v225\n\t" "v_smfmac_f32_16x16x64_bf16 v[4:7], v[48:51], a[216:223], v225\n\t"
                         "v_smfmac_f32_16x16x64_bf16 v[8:11], v[40:43], a[224:231], v225\n\t" "v_add_f32 v52, v0, v2\n\t" "v_add_f32 v53, v1, v3\n\t" "v_cvt_pk_bf16_f32 v54, v53, v52\n\t"
                         "v_smfmac_f32_16x16x64_bf16 v[8:11], v[44:47], a[232:239], v225\n\t" "v_add_f32 v52, v4, v6\n\t" "v_add_f32 v53, v5, v7\n\t" "v_cvt_pk_bf16_f32 v55, v53, v52\n\t" "s_nop 1\n\t"
                         "v_permlane16_swap_b32 v54, v55\n\t" "v_smfmac_f32_16x16x64_bf16 v[8:11], v[48:51], a[240:247], v225\n\t" "global_store_dwordx2 v232, v[54:55], %2 sc1\n\t" "s_add_u32 s40, s40, 0x10000\n\t"
                         "s_addc_u32 s41, s41, 0\n\t" "v_smfmac_f32_16x16x64_bf16 v[12:15], v[40:43], a[248:255], v225\n\t" "v_smfmac_f32_16x16x64_bf16 v[12:15], v[44:47], v[96:103], v225\n\t" "v_add_f32 v52, v8, v10\n\t"
                         "v_smfmac_f32_16x16x64_bf16 v[12:15], v[48:51], v[104:111], v225\n\t" "v_add_f32 v53, v9, v11\n\t" "v_smfmac_f32_16x16x64_bf16 v[16:19], v[40:43], v[112:119], v225\n\t"
                         "v_cvt_pk_bf16_f32 v56, v53, v52\n\t" "v_smfmac_f32_16x16x64_bf16 v[16:19], v[44:47], v[120:127], v225\n\t" "v_add_f32 v52, v12, v14\n\t" "v_add_f32 v53, v13, v15\n\t"
                         "v_cvt_pk_bf16_f32 v57, v53, v52\n\t" "s_nop 1\n\t" "v_permlane16_swap_b32 v56, v57\n\t" "v_smfmac_f32_16x16x64_bf16 v[16:19], v[48:51], v[128:135], v225\n\t"
                         "global_store_dwordx2 v232, v[56:57], %2 sc1\n\t" "s_add_u32 s40, s40, 0x10000\n\t" "s_addc_u32 s41, s41, 0\n\t" "v_smfmac_f32_16x16x64_bf16 v[20:23], v[40:43], v[136:143], v225\n\t"
                         "v_smfmac_f32_16x16x64_bf16 v[20:23], v[44:47], v[144:151], v225\n\t" "v_add_f32 v52, v16, v18\n\t" "v_smfmac_f32_16x16x64_bf16 v[20:23], v[48:51], v[152:159], v225\n\t" "v_add_f32 v53, v17, v19\n\t"
                         "v_smfmac_f32_16x16x64_bf16 v[24:27], v[40:43], v[160:167], v225\n\t" "v_cvt_pk_bf16_f32 v58, v53, v52\n\t" "v_smfmac_f32_16x16x64_bf16 v[24:27], v[44:47], v[168:175], v225\n\t"
                         "v_add_f32 v52, v20, v22\n\t" "v_add_f32 v53, v21, v23\n\t" "v_smfmac_f32_16x16x64_bf16 v[24:27], v[48:51], v[176:183], v225\n\t" "v_cvt_pk_bf16_f32 v59, v53, v52\n\t"
                         "v_smfmac_f32_16x16x64_bf16 v[28:31], v[40:43], v[184:191], v225\n\t" "s_nop 0\n\t" "v_permlane16_swap_b32 v58, v59\n\t" "v_smfmac_f32_16x16x64_bf16 v[28:31], v[44:47], v[192:199], v225\n\t"
                         "global_store_dwordx2 v232, v[58:59], %2 sc1\n\t" "v_add_f32 v52, v24, v26\n\t" "v_smfmac_f32_16x16x64_bf16 v[28:31], v[48:51], v[200:207], v225\n\t" "v_add_f32 v53, v25, v27\n\t"
                         "v_smfmac_f32_16x16x64_bf16 v[32:35], v[40:43], v[208:215], v225\n\t" "v_cvt_pk_bf16_f32 v60, v53, v52\n\t" "v_smfmac_f32_16x16x64_bf16 v[32:35], v[44:47], v[216:223], v225\n\t"
                         "v_add_f32 v52, v28, v30\n\t" "v_smfmac_f32_16x16x64_bf16 v[32:35], v[48:51], v[64:71], v225\n\t" "v_add_f32 v53, v29, v31\n\t" "v_smfmac_f32_16x16x64_bf16 v[36:39], v[40:43], v[72:79], v225\n\t"
                         "v_cvt_pk_bf16_f32 v61, v53, v52\n\t" "v_smfmac_f32_16x16x64_bf16 v[36:39], v[44:47], v[80:87], v225\n\t" "v_add_f32 v52, v32, v34\n\t"
                         "v_smfmac_f32_16x16x64_bf16 v[36:39], v[48:51], v[88:95], v225\n\t" "v_add_f32 v53, v33, v35\n\t" "v_cvt_pk_bf16_f32 v62, v53, v52\n\t" "v_mov_b32 v233, 0\n\t" "v_mov_b32 v233, 0\n\t"
                         "v_mov_b32 v233, 0\n\t" "v_mov_b32 v233, 0\n\t" "v_mov_b32 v233, 0\n\t" "v_pk_add_f32 v[52:53], v[36:37], v[38:39]\n\t" "v_cvt_pk_bf16_f32 v63, v53, v52\n\t" "v_permlane32_swap_b32 v60, v62\n\t"
                         "s_nop 0\n\t" "v_permlane32_swap_b32 v61, v63\n\t" "s_nop 1\n\t" "v_permlane16_swap_b32 v60, v61\n\t" "v_permlane16_swap_b32 v62, v63\n\t" "global_store_dwordx4 v231, v[60:63], %2 sc1\n\t"
                         "s_memrealtime %1\n\t" "s_branch .Lsk_u\n\t" ".Lrt_u:\n\t" "s_cmp_ge_u32 %4, 3\n\t" "s_cbranch_scc1 .Lsk_u\n\t" "s_waitcnt vmcnt(0)\n\t" "s_memrealtime s[44:45]\n\t" "s_memtime s[46:47]\n\t"
                         "s_add_u32 s42, %5, %9\n\t" "s_waitcnt lgkmcnt(0)\n\t" "s_sub_i32 s44, s42, s44\n\t" "s_cmp_lt_i32 s44, -16\n\t" "s_cbranch_scc1 .Lsk_u\n\t" "s_mov_b32 %5, s42\n\t" "s_add_u32 %4, %4, 1\n\t"
                         "s_mul_i32 s44, s44, %10\n\t" "s_ashr_i32 s44, s44, 6\n\t" "s_add_u32 s46, s46, s44\n\t" "s_sub_u32 %3, s46, 0x5a\n\t" "s_branch .Lst_u\n\t" ".Lsk_u:\n\t" "s_mov_b64 %0, vcc\n\t"
                         "s_waitcnt lgkmcnt(0)\n\t"
                         : "=&s"(bad), "=&s"(tp8), "+{s[40:41]}"(up), "+s"(tgt), "+s"(tries), "+s"(phi)
                         : "s"(memp - 1024), "s"(memp + 15360), "s"(en), "s"(cfg.G), "s"(clk.cpt), "{v[224:239]}"(ad),
                           "{a[0:31]}"(wa[0]), "{a[32:63]}"(wa[1]), "{a[64:95]}"(wa[2]), "{a[96:127]}"(wa[3]), "{a[128:159]}"(wa[4]), "{a[160:191]}"(wa[5]), "{a[192:223]}"(wa[6]), "{a[224:255]}"(wa[7]), "{v[96:127]}"(wv[0]), "{v[128:159]}"(wv[1]), "{v[160:191]}"(wv[2]), "{v[192:223]}"(wv[3])
                         : "v0", "v1", "v2", "v3", "v4", "v5", "v6", "v7", "v8", "v9", "v10", "v11", "v12", "v13", "v14", "v15", "v16", "v17", "v18", "v19", "v20", "v21", "v22", "v23", "v24", "v25", "v26", "v27", "v28", "v29", "v30", "v31", "v32", "v33", "v34", "v35", "v36", "v37", "v38", "v39", "v40", "v41", "v42", "v43", "v44", "v45", "v46", "v47", "v48", "v49", "v50", "v51", "v52", "v53", "v54", "v55", "v56", "v57", "v58", "v59", "v60", "v61", "v62", "v63", "v64", "v65", "v66", "v67", "v68", "v69", "v70", "v71", "v72", "v73", "v74", "v75", "v76", "v77", "v78", "v79", "v80", "v81", "v82", "v83", "v84", "v85", "v86", "v87", "v88", "v89", "v90", "v91", "v92", "v93", "v94", "v95", "vcc", "scc", "s42", "s44", "s45", "s46", "s47", "memory");
            if (__builtin_expect(bad == 0, 1)) break;
            waitvm<0>();
            if (--budget < 0 || ((++tries & 15) == 0 && !unwritten(memp, lanex() * 16))) en = 0;
            phi += (u32)cfg.G;
        }
        clk.calib();
        const u32 tt = sched_ld0(s);
        if (s >= NSEG - 2) waitvm<0>();
        barrier();
        if (s >= NSEG - 2 && w == 3) flag_add(fl, F_PROG + 8 * s + (h & 7), 1);
        if (s >= NSEG - 2 && w == 2) flag_add(fl, F_UD + 2 * (s & 1) + (h & 1), 1);
        if (s == 0) {
#pragma unroll
            for (int i = UPF; i < 8; ++i) {
                const int l = 64 * min(i * 4 + w, 29) + lanex();
                pfl(p.wq + h * 384, (u32)((l / 3) * 12288 + (l % 3) * 128), lds);
            }
        }
        if (s == 0) { until(phi + UNAP); phi = go_wg(fl, h, w, lds + SL_PF - 16) - (tt >> 16); edge(clk.r1, clk.m1); clk.calib(); }
        follow(phi, (int)(tt >> 16) + 4 + cfg.T3 - VISS, (u32)tp8, cfg.G);
        phi += tt & 0xffffu;
        memp = p.ws + mslot(sd, s + 1);
    }
    asm volatile(".Lkm_lu:\n\ts_waitcnt vmcnt(0)" : : : "memory");
}

#define VALL "{v[0:3]}"(o), "{v[16:47]}"(rv[0]), "{v[48:79]}"(rv[1]), "{v[80:111]}"(rv[2]), "{v[112:143]}"(rv[3]), "{v[144:175]}"(rv[4]), "{v[176:207]}"(rv[5]), "{v[208:239]}"(rv[6]), "{a[0:31]}"(ra[0]), "{a[32:63]}"(ra[1]), "{a[64:95]}"(ra[2]), "{a[96:127]}"(ra[3])
DI void value_role(const Ptrs& p, const ChainCfg cfg, int vb, int sd, u32 phi, u32 lds, int w) {
    const int lane = lanex(), tid = 64 * w + lane, n = lane & 15, g = lane >> 4;
    const int v = vb;
    const u32 moff = lane * 16;
    char* const fl = p.ws + OFF_ST;
    char* const ub = p.ws + uchunk(v);
    const int xh = (vb >> 2) & 1;
    if (w == 3) {
        const u32x4 ff = u32x4{~0u, ~0u, ~0u, ~0u};
        if (SPL) barrier();
#pragma nounroll
        for (int s = 0; s < NSEG - 2; ++s) {
            dly(s == 1 ? HD1 : cfg.cR3 >> 8 & 255);
            char* const mk = ub + uslot(sd, s + 2);
#pragma unroll
            for (int i = 0; i < 8; ++i) { gst16(mk + i * 1024, moff, ff); gap<HQ>(-1); }
            if (vb < 32 && s) {
                gst16(p.ws + (size_t)(s + 2) * HO_STRIDE + vb * 2048, moff, ff);
                gst16(p.ws + (size_t)(s + 2) * HO_STRIDE + vb * 2048 + 1024, moff, ff);
            }
            if (s == 0) {
                u32 r;
                flag_wait(fl, F_VD + (vb & 3) + (v < 20 ? 0 : 4), 32);
                rdy_add(r, fl);
                asm volatile("s_waitcnt vmcnt(0)" : "+v"(r) : : "memory");
                rdy_end(r, fl, cfg.s1 >> 16);
            }
            waitvm<0>();
            if (SPL && s) barrier();
            barrier();
        }
        return;
    }
    if (w) {
        const u32 dof = (u32)((n >> 3) * 16384 + g * 128 + (n & 7) * 16);
        const char* dp = p.ws + OFF_D + (size_t)v * 32768 - 4096 + w * 8192;
        if (SPL) barrier();
        flag_wait(fl, F_VD + (vb & 3) + (v < 20 ? 0 : 4), 32);
#pragma nounroll
        for (int s = 0; s < NSEG - 1; ++s) {
            u32x4 df[16];
            u32 dfl;
            const int on = uni(1 - s);
            sfor<0, SPL>([&](auto I) { gld16c<I.v * 512 - 4096>(df[I.v], dp, dof); gap<HP0>(on); });
            if (SPL) asm volatile("s_cmp_ge_i32 %0, 0\n\ts_cbranch_scc1 .Lhb\n\ts_barrier\n.Lhb:" : : "s"(on) : "memory", "scc");
            dly(s == 1 ? HD1 : cfg.cR3 & 255);
            sfor<SPL, 16>([&](auto I) { gld16c<I.v * 512 - 4096>(df[I.v], dp, dof); gap<HP>(on); });
            dp += D_STRIDE;
            flag_ldm(dfl, fl, F_VD + pblk(s + 1) * 8 + (vb & RM), pblk(s + 1) != pblk(s));
            const u32 la = lds + moff + (s & 1) * 32768 + (w - 1) * 16384;
            sfor<0, 8>([&](auto I) {
                constexpr int i = I.v;
                waitv<15 - 2 * i>(df[2 * i]); waitv<15 - 2 * i>(df[2 * i + 1]);
                const u32x4 x = df[2 * i], y = df[2 * i + 1];
                const u32 lb = i & 2 ? la - xh * 4096 : la + xh * 4096;
                *LDSP(u32x4, lb + 2048 * i) = u32x4{x[0], y[0], x[2], y[2]};
                *LDSP(u32x4, lb + 2048 * i + 1024) = u32x4{x[1], y[1], x[3], y[3]};
                PIN();
            });
            waitvm<0>();
            asm volatile("" : "+v"(dfl));
            if (uni((int)dfl) < 31) {
                dly(FRD);
                flag_ld(dfl, fl, F_VD + pblk(s + 1) * 8 + (vb & RM));
                asm volatile("s_waitcnt vmcnt(0)" : "+v"(dfl) : : "memory");
            }
            barrier();
            if (uni((int)dfl) < 31) flag_wait(fl, F_VD + pblk(s + 1) * 8 + (vb & RM), 32);
        }
        return;
    }
    u32x32 rv[7], ra[4];
    u32x4 o;
    asm volatile("v_accvgpr_write_b32 a64, 0\n\t" "v_accvgpr_write_b32 a65, 0\n\t" "v_accvgpr_write_b32 a66, 0\n\t" "v_accvgpr_write_b32 a67, 0\n\t" "v_accvgpr_write_b32 a68, 0\n\t" "v_accvgpr_write_b32 a69, 0\n\t"
                 "v_accvgpr_write_b32 a70, 0\n\t" "v_accvgpr_write_b32 a71, 0\n\t" "v_accvgpr_write_b32 a72, 0\n\t" "v_accvgpr_write_b32 a73, 0\n\t" "v_accvgpr_write_b32 a74, 0\n\t" "v_accvgpr_write_b32 a75, 0\n\t"
                 "v_accvgpr_write_b32 a76, 0\n\t" "v_accvgpr_write_b32 a77, 0\n\t" "v_accvgpr_write_b32 a78, 0\n\t" "v_accvgpr_write_b32 a79, 0\n\t" "v_mov_b64 v[112:113], -1\n\t" "v_mov_b64 v[120:121], -1\n\t"
                 "v_mov_b64 v[128:129], -1\n\t" "v_mov_b64 v[136:137], -1\n\t" "v_mov_b64 v[144:145], -1\n\t" "v_mov_b64 v[152:153], -1\n\t" "v_mov_b64 v[160:161], -1\n\t" "v_mov_b64 v[168:169], -1\n\t"
                 "v_mov_b64 v[176:177], -1\n\t" "v_mov_b64 v[184:185], -1\n\t" "v_mov_b64 v[192:193], -1\n\t" "v_mov_b64 v[200:201], -1\n\t" "v_mov_b64 v[208:209], -1\n\t" "v_mov_b64 v[216:217], -1\n\t"
                 "v_mov_b64 v[224:225], -1\n\t" "v_mov_b64 v[232:233], -1\n\t"
                 : "={v[0:3]}"(o), "={v[16:47]}"(rv[0]), "={v[48:79]}"(rv[1]), "={v[80:111]}"(rv[2]), "={v[112:143]}"(rv[3]), "={v[144:175]}"(rv[4]), "={v[176:207]}"(rv[5]), "={v[208:239]}"(rv[6]), "={a[0:31]}"(ra[0]), "={a[32:63]}"(ra[1]), "={a[64:95]}"(ra[2]), "={a[96:127]}"(ra[3]));
    const u32 ho = (u32)(min(lane, 39) * 256 + (lane >> 5) * 8192);
    const u32 pof = (u32)((n & 4) * 128 + (n >> 3) * 256 + g * 64 + (n & 3) * 16 + 2048);
    const u32 uof = (u32)((n >> 3) * 4096 + g * 128 + (n & 7) * 16 + 512);
    const u32 cof = (u32)((g >> 1) * 512 + (n >> 2) * 64 + (n & 1) * 16), pmf = (u32)((g >> 1) * 1024 + (lane & 31) * 16);
    u32x8 ad = u32x8{uof + xh * 512, pof + xh * 2048, cof + xh * 256, pmf + xh * 512,
                     (lane & 2) ? 0xEEEEu : 0x4444u, (lane & 4) ? 0xEEEEu : 0x4444u, (u32)(v * 256 + (v >> 5) * 8192 + g * 64 + (n >> 3) * 32 + (n & 1) * 16 + (n & 6) * 2),
                     (lane & 1) ? 0x07060302u : 0x01000504u};
    u32 pob = pof - xh * 2048, uob = uof - xh * 512, pmo = pmf + (xh ^ 1) * 512, cob = cof + (xh ^ 1) * 256;
    asm volatile("" : "+{v[4:11]}"(ad), "+v"(pob), "+v"(uob), "+v"(pmo), "+v"(cob));
    const int lead = v < 16 ? cfg.dU - (v >> 3) * cfg.s2 : cfg.cR2;
    const char* memp = p.ws + OFF_XF + 4096;
    char* up = ub + OFF_XF;
    int budget = 30000;
    Clock clk; clk.init();
    u32 tt = 0;
    for (int s = -1; s < NSEG - 1; ++s) {
        const char* const mnx = p.ws + mslot(sd, s + 1);
        bool all = false;
        int tries = 0;
        u64 tp8;
        while (true) {
            int tgt = uni((int)(s >= 0 ? clk.sched(phi, (int)(tt >> 16), cfg.G, tries ? 2 : 1) : mt())) - lead;
            u32 pht = phi + (tt >> 16);
            int nr = s < 0 ? 9 : tries;
            u64 ok;
            asm volatile(".Lst_v:\n\t" "v_mov_b64 v[0:1], 0\n\t" "v_mov_b64 v[2:3], 0\n\t" "s_cmp_lg_u32 %13, 0\n\t" "s_cbranch_scc0 .Lsp_v0\n\t" ".p2align 5\n\t" ".Lsp_v3:\n\t" "s_memtime vcc\n\t" "s_waitcnt lgkmcnt(0)\n\t"
                         "s_sub_i32 s42, %2, vcc_lo\n\t" "s_cmp_gt_i32 s42, 80\n\t" "s_cbranch_scc1 .Lsp_v3\n\t" "s_max_i32 s42, s42, 0\n\t" "s_sub_i32 s42, 92, s42\n\t" "s_and_b32 s42, s42, -4\n\t" "s_getpc_b64 vcc\n\t"
                         "s_add_u32 vcc_lo, vcc_lo, s42\n\t" "s_addc_u32 vcc_hi, vcc_hi, 0\n\t" "s_setpc_b64 vcc\n\t" "s_nop 0\n\t" "s_nop 0\n\t" "s_nop 0\n\t" "s_nop 0\n\t" "s_nop 0\n\t" "s_nop 0\n\t" "s_nop 0\n\t"
                         "s_nop 0\n\t" "s_nop 0\n\t" "s_nop 0\n\t" "s_nop 0\n\t" "s_nop 0\n\t" "s_nop 0\n\t" "s_nop 0\n\t" "s_nop 0\n\t" "s_nop 0\n\t" "s_nop 0\n\t" "s_nop 0\n\t" "s_nop 0\n\t" "s_nop 0\n\t"
                         "global_load_dwordx4 a[0:3], v5, %10 offset:-4096 sc1\n\t" "global_load_dwordx4 a[4:7], v5, %10 offset:-3072 sc1\n\t" "global_load_dwordx4 a[8:11], v5, %10 sc1\n\t"
                         "global_load_dwordx4 a[12:15], v5, %10 offset:1024 sc1\n\t" "global_load_dwordx4 a[16:19], v5, %11 offset:-4096 sc1\n\t" "global_load_dwordx4 a[20:23], v5, %11 offset:-3072 sc1\n\t"
                         "global_load_dwordx4 a[24:27], v5, %11 sc1\n\t" "global_load_dwordx4 a[28:31], v5, %11 offset:1024 sc1\n\t" "s_nop 14\n\t" "global_load_dwordx4 a[32:35], %14, %10 offset:-2048 sc1\n\t"
                         "global_load_dwordx4 a[36:39], %14, %10 offset:-1024 sc1\n\t" "global_load_dwordx4 a[40:43], %14, %10 offset:2048 sc1\n\t" "global_load_dwordx4 a[44:47], %14, %10 offset:3072 sc1\n\t"
                         "global_load_dwordx4 a[48:51], %14, %11 offset:-2048 sc1\n\t" "global_load_dwordx4 a[52:55], %14, %11 offset:-1024 sc1\n\t" "global_load_dwordx4 a[56:59], %14, %11 offset:2048 sc1\n\t"
                         "global_load_dwordx4 a[60:63], %14, %11 offset:3072 sc1\n\t" "s_nop 15\n\t" "s_nop 15\n\t" "global_load_dwordx4 a[80:83], v7, %9 sc1\n\t"
                         "global_load_dwordx4 a[84:87], v7, %9 offset:2048 sc1\n\t" "s_mov_b64 exec, %12\n\t" "global_load_dwordx4 a[64:67], v6, %9 offset:-2048 sc1\n\t"
                         "global_load_dwordx4 a[68:71], v6, %9 offset:-1024 sc1\n\t" "s_mov_b64 exec, -1\n\t" "s_nop 15\n\t" "s_nop 8\n\t" "global_load_dwordx4 a[88:91], %16, %9 sc1\n\t"
                         "global_load_dwordx4 a[92:95], %16, %9 offset:2048 sc1\n\t" "s_mov_b64 exec, %12\n\t" "global_load_dwordx4 a[72:75], %17, %9 offset:-2048 sc1\n\t"
                         "global_load_dwordx4 a[76:79], %17, %9 offset:-1024 sc1\n\t" "s_mov_b64 exec, -1\n\t" "s_mov_b32 s42, %13\n\t" ".Lsp_v5:\n\t" "s_sub_u32 s42, s42, 1\n\t" "s_cmp_gt_i32 s42, 0\n\t"
                         "s_cbranch_scc1 .Lsp_v5\n\t" "global_load_dwordx4 a[96:99], v4, %6 offset:-512 sc1\n\t" "global_load_dwordx4 a[100:103], v4, %6 offset:512 sc1\n\t"
                         "global_load_dwordx4 a[104:107], v4, %6 offset:1536 sc1\n\t" "global_load_dwordx4 a[108:111], v4, %6 offset:2560 sc1\n\t" "global_load_dwordx4 a[112:115], %15, %6 sc1\n\t"
                         "global_load_dwordx4 a[116:119], %15, %6 offset:1024 sc1\n\t" "global_load_dwordx4 a[120:123], %15, %6 offset:2048 sc1\n\t" "global_load_dwordx4 a[124:127], %15, %6 offset:3072 sc1\n\t"
                         "s_waitcnt vmcnt(31)\n\t" "v_smfmac_f32_16x16x64_bf16 v[48:51], a[0:3], v[112:119], v9\n\t" "s_waitcnt vmcnt(30)\n\t" "v_smfmac_f32_16x16x64_bf16 v[52:55], a[4:7], v[120:127], v9\n\t"
                         "s_waitcnt vmcnt(29)\n\t" "v_smfmac_f32_16x16x64_bf16 v[56:59], a[8:11], v[144:151], v9\n\t" "s_waitcnt vmcnt(28)\n\t" "v_cvt_pk_bf16_f32 v16, v48, v49\n\t" "v_cvt_pk_bf16_f32 v17, v50, v51\n\t"
                         "v_smfmac_f32_16x16x64_bf16 v[60:63], a[12:15], v[152:159], v9\n\t" "s_waitcnt vmcnt(27)\n\t" "v_cvt_pk_bf16_f32 v18, v52, v53\n\t" "v_cvt_pk_bf16_f32 v19, v54, v55\n\t"
                         "v_smfmac_f32_16x16x64_bf16 v[64:67], a[16:19], v[176:183], v9\n\t" "s_waitcnt vmcnt(26)\n\t" "v_cvt_pk_bf16_f32 v20, v56, v57\n\t" "v_cvt_pk_bf16_f32 v21, v58, v59\n\t"
                         "v_smfmac_f32_16x16x64_bf16 v[68:71], a[20:23], v[184:191], v9\n\t" "s_waitcnt vmcnt(25)\n\t" "v_cvt_pk_bf16_f32 v22, v60, v61\n\t" "v_cvt_pk_bf16_f32 v23, v62, v63\n\t"
                         "v_smfmac_f32_16x16x64_bf16 v[72:75], a[24:27], v[208:215], v9\n\t" "s_waitcnt vmcnt(24)\n\t" "v_cvt_pk_bf16_f32 v24, v64, v65\n\t" "v_cvt_pk_bf16_f32 v25, v66, v67\n\t"
                         "v_smfmac_f32_16x16x64_bf16 v[76:79], a[28:31], v[216:223], v9\n\t" "s_waitcnt vmcnt(23)\n\t" "v_cvt_pk_bf16_f32 v26, v68, v69\n\t" "v_cvt_pk_bf16_f32 v27, v70, v71\n\t"
                         "v_smfmac_f32_16x16x64_bf16 v[80:83], a[32:35], v[128:135], v9\n\t" "s_waitcnt vmcnt(22)\n\t" "v_cvt_pk_bf16_f32 v28, v72, v73\n\t" "v_cvt_pk_bf16_f32 v29, v74, v75\n\t"
                         "v_smfmac_f32_16x16x64_bf16 v[84:87], a[36:39], v[136:143], v9\n\t" "s_waitcnt vmcnt(21)\n\t" "v_cvt_pk_bf16_f32 v30, v76, v77\n\t" "v_cvt_pk_bf16_f32 v31, v78, v79\n\t"
                         "v_smfmac_f32_16x16x64_bf16 v[88:91], a[40:43], v[160:167], v9\n\t" "s_waitcnt vmcnt(20)\n\t" "v_cvt_pk_bf16_f32 v32, v80, v81\n\t" "v_cvt_pk_bf16_f32 v33, v82, v83\n\t"
                         "v_smfmac_f32_16x16x64_bf16 v[92:95], a[44:47], v[168:175], v9\n\t" "s_waitcnt vmcnt(19)\n\t" "v_cvt_pk_bf16_f32 v34, v84, v85\n\t" "v_cvt_pk_bf16_f32 v35, v86, v87\n\t"
                         "v_smfmac_f32_16x16x64_bf16 v[96:99], a[48:51], v[192:199], v9\n\t" "s_waitcnt vmcnt(18)\n\t" "v_cvt_pk_bf16_f32 v36, v88, v89\n\t" "v_cvt_pk_bf16_f32 v37, v90, v91\n\t"
                         "v_smfmac_f32_16x16x64_bf16 v[100:103], a[52:55], v[200:207], v9\n\t" "s_waitcnt vmcnt(17)\n\t" "v_cvt_pk_bf16_f32 v38, v92, v93\n\t" "v_cvt_pk_bf16_f32 v39, v94, v95\n\t"
                         "v_smfmac_f32_16x16x64_bf16 v[104:107], a[56:59], v[224:231], v9\n\t" "s_waitcnt vmcnt(16)\n\t" "v_cvt_pk_bf16_f32 v40, v96, v97\n\t" "v_cvt_pk_bf16_f32 v41, v98, v99\n\t"
                         "v_smfmac_f32_16x16x64_bf16 v[108:111], a[60:63], v[232:239], v9\n\t" "s_waitcnt vmcnt(13)\n\t" "v_cvt_pk_bf16_f32 v42, v100, v101\n\t" "v_cvt_pk_bf16_f32 v43, v102, v103\n\t"
                         "v_smfmac_f32_16x16x64_bf16 v[0:3], a[64:67], v[16:23], v8\n\t" "s_waitcnt vmcnt(12)\n\t" "v_cvt_pk_bf16_f32 v44, v104, v105\n\t" "v_cvt_pk_bf16_f32 v45, v106, v107\n\t"
                         "v_smfmac_f32_16x16x64_bf16 v[0:3], a[68:71], v[24:31], v8\n\t" "s_waitcnt vmcnt(9)\n\t" "v_cvt_pk_bf16_f32 v46, v108, v109\n\t" "v_cvt_pk_bf16_f32 v47, v110, v111\n\t"
                         "v_smfmac_f32_16x16x64_bf16 v[0:3], a[72:75], v[32:39], v8\n\t" "s_waitcnt vmcnt(8)\n\t" "v_smfmac_f32_16x16x64_bf16 v[0:3], a[76:79], v[40:47], v8\n\t" "s_waitcnt vmcnt(6)\n\t"
                         "v_smfmac_f32_16x16x64_bf16 v[0:3], a[80:83], a[96:103], v8\n\t" "s_waitcnt vmcnt(4)\n\t" "v_smfmac_f32_16x16x64_bf16 v[0:3], a[84:87], a[104:111], v8\n\t" "s_waitcnt vmcnt(2)\n\t"
                         "v_smfmac_f32_16x16x64_bf16 v[0:3], a[88:91], a[112:119], v8\n\t" "s_waitcnt vmcnt(0)\n\t" "v_smfmac_f32_16x16x64_bf16 v[0:3], a[92:95], a[120:127], v8\n\t" "s_branch .Lv_end\n\t"
                         ".p2align 5\n\t" "s_nop 0\n\t" "s_nop 0\n\t" "s_nop 0\n\t" "s_nop 0\n\t" ".Lsp_v0:\n\t" "s_memtime vcc\n\t" "s_waitcnt lgkmcnt(0)\n\t" "s_sub_i32 s42, %2, vcc_lo\n\t" "s_cmp_gt_i32 s42, 80\n\t"
                         "s_cbranch_scc1 .Lsp_v0\n\t" "s_max_i32 s42, s42, 0\n\t" "s_sub_i32 s42, 92, s42\n\t" "s_and_b32 s42, s42, -4\n\t" "s_getpc_b64 vcc\n\t" "s_add_u32 vcc_lo, vcc_lo, s42\n\t"
                         "s_addc_u32 vcc_hi, vcc_hi, 0\n\t" "s_setpc_b64 vcc\n\t" "s_nop 0\n\t" "s_nop 0\n\t" "s_nop 0\n\t" "s_nop 0\n\t" "s_nop 0\n\t" "s_nop 0\n\t" "s_nop 0\n\t" "s_nop 0\n\t" "s_nop 0\n\t" "s_nop 0\n\t"
                         "s_nop 0\n\t" "s_nop 0\n\t" "s_nop 0\n\t" "s_nop 0\n\t" "s_nop 0\n\t" "s_nop 0\n\t" "s_nop 0\n\t" "s_nop 0\n\t" "s_nop 0\n\t" "s_nop 0\n\t"
                         "global_load_dwordx4 a[96:99], v4, %6 offset:-512 sc1\n\t" "global_load_dwordx4 a[100:103], v4, %6 offset:512 sc1\n\t" "global_load_dwordx4 a[104:107], v4, %6 offset:1536 sc1\n\t"
                         "global_load_dwordx4 a[108:111], v4, %6 offset:2560 sc1\n\t" "global_load_dwordx4 a[112:115], %15, %6 sc1\n\t" "global_load_dwordx4 a[116:119], %15, %6 offset:1024 sc1\n\t"
                         "global_load_dwordx4 a[120:123], %15, %6 offset:2048 sc1\n\t" "global_load_dwordx4 a[124:127], %15, %6 offset:3072 sc1\n\t" "s_nop 15\n\t" "s_nop 1\n\t"
                         "global_load_dwordx4 a[0:3], v5, %10 offset:-4096 sc1\n\t" "global_load_dwordx4 a[4:7], v5, %10 offset:-3072 sc1\n\t" "global_load_dwordx4 a[8:11], v5, %10 sc1\n\t"
                         "global_load_dwordx4 a[12:15], v5, %10 offset:1024 sc1\n\t" "global_load_dwordx4 a[16:19], v5, %11 offset:-4096 sc1\n\t" "global_load_dwordx4 a[20:23], v5, %11 offset:-3072 sc1\n\t"
                         "global_load_dwordx4 a[24:27], v5, %11 sc1\n\t" "global_load_dwordx4 a[28:31], v5, %11 offset:1024 sc1\n\t" "s_nop 14\n\t" "global_load_dwordx4 a[32:35], %14, %10 offset:-2048 sc1\n\t"
                         "global_load_dwordx4 a[36:39], %14, %10 offset:-1024 sc1\n\t" "global_load_dwordx4 a[40:43], %14, %10 offset:2048 sc1\n\t" "global_load_dwordx4 a[44:47], %14, %10 offset:3072 sc1\n\t"
                         "global_load_dwordx4 a[48:51], %14, %11 offset:-2048 sc1\n\t" "global_load_dwordx4 a[52:55], %14, %11 offset:-1024 sc1\n\t" "global_load_dwordx4 a[56:59], %14, %11 offset:2048 sc1\n\t"
                         "global_load_dwordx4 a[60:63], %14, %11 offset:3072 sc1\n\t" "s_nop 15\n\t" "s_nop 15\n\t" "global_load_dwordx4 a[80:83], v7, %9 sc1\n\t"
                         "global_load_dwordx4 a[84:87], v7, %9 offset:2048 sc1\n\t" "s_mov_b64 exec, %12\n\t" "global_load_dwordx4 a[64:67], v6, %9 offset:-2048 sc1\n\t"
                         "global_load_dwordx4 a[68:71], v6, %9 offset:-1024 sc1\n\t" "s_mov_b64 exec, -1\n\t" "s_nop 15\n\t" "s_nop 8\n\t" "global_load_dwordx4 a[88:91], %16, %9 sc1\n\t"
                         "global_load_dwordx4 a[92:95], %16, %9 offset:2048 sc1\n\t" "s_mov_b64 exec, %12\n\t" "global_load_dwordx4 a[72:75], %17, %9 offset:-2048 sc1\n\t"
                         "global_load_dwordx4 a[76:79], %17, %9 offset:-1024 sc1\n\t" "s_mov_b64 exec, -1\n\t" "s_waitcnt vmcnt(23)\n\t" "v_smfmac_f32_16x16x64_bf16 v[48:51], a[0:3], v[112:119], v9\n\t"
                         "s_waitcnt vmcnt(22)\n\t" "v_smfmac_f32_16x16x64_bf16 v[52:55], a[4:7], v[120:127], v9\n\t" "s_waitcnt vmcnt(21)\n\t" "v_smfmac_f32_16x16x64_bf16 v[56:59], a[8:11], v[144:151], v9\n\t"
                         "s_waitcnt vmcnt(20)\n\t" "v_cvt_pk_bf16_f32 v16, v48, v49\n\t" "v_cvt_pk_bf16_f32 v17, v50, v51\n\t" "v_smfmac_f32_16x16x64_bf16 v[60:63], a[12:15], v[152:159], v9\n\t" "s_waitcnt vmcnt(19)\n\t"
                         "v_cvt_pk_bf16_f32 v18, v52, v53\n\t" "v_cvt_pk_bf16_f32 v19, v54, v55\n\t" "v_smfmac_f32_16x16x64_bf16 v[64:67], a[16:19], v[176:183], v9\n\t" "s_waitcnt vmcnt(18)\n\t"
                         "v_cvt_pk_bf16_f32 v20, v56, v57\n\t" "v_cvt_pk_bf16_f32 v21, v58, v59\n\t" "v_smfmac_f32_16x16x64_bf16 v[68:71], a[20:23], v[184:191], v9\n\t" "s_waitcnt vmcnt(17)\n\t"
                         "v_cvt_pk_bf16_f32 v22, v60, v61\n\t" "v_cvt_pk_bf16_f32 v23, v62, v63\n\t" "v_smfmac_f32_16x16x64_bf16 v[72:75], a[24:27], v[208:215], v9\n\t" "s_waitcnt vmcnt(16)\n\t"
                         "v_cvt_pk_bf16_f32 v24, v64, v65\n\t" "v_cvt_pk_bf16_f32 v25, v66, v67\n\t" "v_smfmac_f32_16x16x64_bf16 v[76:79], a[28:31], v[216:223], v9\n\t" "s_waitcnt vmcnt(15)\n\t"
                         "v_cvt_pk_bf16_f32 v26, v68, v69\n\t" "v_cvt_pk_bf16_f32 v27, v70, v71\n\t" "v_smfmac_f32_16x16x64_bf16 v[80:83], a[32:35], v[128:135], v9\n\t" "s_waitcnt vmcnt(14)\n\t"
                         "v_cvt_pk_bf16_f32 v28, v72, v73\n\t" "v_cvt_pk_bf16_f32 v29, v74, v75\n\t" "v_smfmac_f32_16x16x64_bf16 v[84:87], a[36:39], v[136:143], v9\n\t" "s_waitcnt vmcnt(13)\n\t"
                         "v_cvt_pk_bf16_f32 v30, v76, v77\n\t" "v_cvt_pk_bf16_f32 v31, v78, v79\n\t" "v_smfmac_f32_16x16x64_bf16 v[88:91], a[40:43], v[160:167], v9\n\t" "s_waitcnt vmcnt(12)\n\t"
                         "v_cvt_pk_bf16_f32 v32, v80, v81\n\t" "v_cvt_pk_bf16_f32 v33, v82, v83\n\t" "v_smfmac_f32_16x16x64_bf16 v[92:95], a[44:47], v[168:175], v9\n\t" "s_waitcnt vmcnt(11)\n\t"
                         "v_cvt_pk_bf16_f32 v34, v84, v85\n\t" "v_cvt_pk_bf16_f32 v35, v86, v87\n\t" "v_smfmac_f32_16x16x64_bf16 v[96:99], a[48:51], v[192:199], v9\n\t" "s_waitcnt vmcnt(10)\n\t"
                         "v_cvt_pk_bf16_f32 v36, v88, v89\n\t" "v_cvt_pk_bf16_f32 v37, v90, v91\n\t" "v_smfmac_f32_16x16x64_bf16 v[100:103], a[52:55], v[200:207], v9\n\t" "s_waitcnt vmcnt(9)\n\t"
                         "v_cvt_pk_bf16_f32 v38, v92, v93\n\t" "v_cvt_pk_bf16_f32 v39, v94, v95\n\t" "v_smfmac_f32_16x16x64_bf16 v[104:107], a[56:59], v[224:231], v9\n\t" "s_waitcnt vmcnt(8)\n\t"
                         "v_cvt_pk_bf16_f32 v40, v96, v97\n\t" "v_smfmac_f32_16x16x64_bf16 v[108:111], a[60:63], v[232:239], v9\n\t" "s_waitcnt vmcnt(7)\n\t" "v_cvt_pk_bf16_f32 v41, v98, v99\n\t"
                         "v_smfmac_f32_16x16x64_bf16 v[0:3], a[80:83], a[96:103], v8\n\t" "s_waitcnt vmcnt(6)\n\t" "v_cvt_pk_bf16_f32 v42, v100, v101\n\t"
                         "v_smfmac_f32_16x16x64_bf16 v[0:3], a[84:87], a[104:111], v8\n\t" "s_waitcnt vmcnt(5)\n\t" "v_cvt_pk_bf16_f32 v43, v102, v103\n\t" "v_smfmac_f32_16x16x64_bf16 v[0:3], a[64:67], v[16:23], v8\n\t"
                         "s_waitcnt vmcnt(4)\n\t" "v_cvt_pk_bf16_f32 v44, v104, v105\n\t" "v_smfmac_f32_16x16x64_bf16 v[0:3], a[68:71], v[24:31], v8\n\t" "s_waitcnt vmcnt(3)\n\t" "v_cvt_pk_bf16_f32 v45, v106, v107\n\t"
                         "v_smfmac_f32_16x16x64_bf16 v[0:3], a[88:91], a[112:119], v8\n\t" "s_waitcnt vmcnt(2)\n\t" "v_cvt_pk_bf16_f32 v46, v108, v109\n\t"
                         "v_smfmac_f32_16x16x64_bf16 v[0:3], a[92:95], a[120:127], v8\n\t" "s_waitcnt vmcnt(1)\n\t" "v_cvt_pk_bf16_f32 v47, v110, v111\n\t" "v_smfmac_f32_16x16x64_bf16 v[0:3], a[72:75], v[32:39], v8\n\t"
                         "s_waitcnt vmcnt(0)\n\t" "v_smfmac_f32_16x16x64_bf16 v[0:3], a[76:79], v[40:47], v8\n\t" ".Lv_end:\n\t" "s_nop 7\n\t" "v_pk_add_f32 v[0:1], v[0:1], v[2:3]\n\t" "v_cvt_pk_bf16_f32 v2, v0, v1\n\t"
                         "v_cmp_u_f32 vcc, v0, v1\n\t" "s_cmp_eq_u64 vcc, 0\n\t" "v_mov_b32_dpp v3, v2 quad_perm:[1,0,3,2] row_mask:0xf bank_mask:0xf\n\t" "s_cselect_b64 %0, -1, 0\n\t" "v_perm_b32 v0, v2, v3, v11\n\t"
                         "s_mov_b64 exec, %0\n\t" "global_store_dword v10, v0, %8 offset:-4096 sc1\n\t" "global_store_dword v10, v0, %7 offset:-4096 sc1\n\t" "s_memrealtime %1\n\t" "s_mov_b64 exec, -1\n\t"
                         "v_mov_b64 v[48:49], 0\n\t" "v_mov_b64 v[50:51], 0\n\t" "v_mov_b64 v[52:53], 0\n\t" "v_mov_b64 v[54:55], 0\n\t" "v_mov_b64 v[56:57], 0\n\t" "v_mov_b64 v[58:59], 0\n\t" "v_mov_b64 v[60:61], 0\n\t"
                         "v_mov_b64 v[62:63], 0\n\t" "v_mov_b64 v[64:65], 0\n\t" "v_mov_b64 v[66:67], 0\n\t" "v_mov_b64 v[68:69], 0\n\t" "v_mov_b64 v[70:71], 0\n\t" "v_mov_b64 v[72:73], 0\n\t" "v_mov_b64 v[74:75], 0\n\t"
                         "v_mov_b64 v[76:77], 0\n\t" "v_mov_b64 v[78:79], 0\n\t" "v_mov_b64 v[80:81], 0\n\t" "v_mov_b64 v[82:83], 0\n\t" "v_mov_b64 v[84:85], 0\n\t" "v_mov_b64 v[86:87], 0\n\t" "v_mov_b64 v[88:89], 0\n\t"
                         "v_mov_b64 v[90:91], 0\n\t" "v_mov_b64 v[92:93], 0\n\t" "v_mov_b64 v[94:95], 0\n\t" "v_mov_b64 v[96:97], 0\n\t" "v_mov_b64 v[98:99], 0\n\t" "v_mov_b64 v[100:101], 0\n\t" "v_mov_b64 v[102:103], 0\n\t"
                         "v_mov_b64 v[104:105], 0\n\t" "v_mov_b64 v[106:107], 0\n\t" "v_mov_b64 v[108:109], 0\n\t" "v_mov_b64 v[110:111], 0\n\t" "s_waitcnt lgkmcnt(0)\n\t" "s_cbranch_vccz .Lsk_v\n\t" "s_cmp_ge_u32 %3, 3\n\t"
                         "s_cbranch_scc1 .Lsk_v\n\t" "s_waitcnt vmcnt(0)\n\t" "s_memrealtime s[44:45]\n\t" "s_memtime s[46:47]\n\t" "s_add_u32 s42, %4, %18\n\t" "s_waitcnt lgkmcnt(0)\n\t" "s_sub_i32 s44, s42, s44\n\t"
                         "s_cmp_lt_i32 s44, -16\n\t" "s_cbranch_scc1 .Lsk_v\n\t" "s_mov_b32 %4, s42\n\t" "s_add_u32 %3, %3, 1\n\t" "s_mul_i32 s44, s44, %19\n\t" "s_ashr_i32 s44, s44, 6\n\t" "s_add_u32 s46, s46, s44\n\t"
                         "s_sub_u32 %2, s46, %20\n\t" "s_branch .Lst_v\n\t" ".Lsk_v:\n\t"
                         : "=&s"(ok), "=&s"(tp8), "+s"(tgt), "+s"(nr), "+s"(pht)
                         : "{v[4:11]}"(ad), "s"(up), "s"(mnx), "s"((u64)mnx ^ 8192), "s"(memp + 16384), "s"(memp + 32768 - 2048), "s"(memp + 49152 - 2048), "s"(0xc30000c3c30000c3ull),
                           "s"(v < 16 ? 0 : (v < 24 ? 1 : cfg.T3)), "v"(pob), "v"(uob), "v"(pmo), "v"(cob), "s"(cfg.G), "s"(clk.cpt), "s"(lead + SPB), VALL
                         : "memory", "vcc", "scc", "s42", "s44", "s45", "s46", "s47");
            phi = pht - (tt >> 16); tries = nr;
            if (__builtin_expect(ok != 0, 1) || s < 0) break;
            if (all || --budget < 0) {
                asm volatile("v_min_u32 v0, -2, v0\n\tglobal_store_dword v10, v0, %0 offset:-4096 sc1\n\tglobal_store_dword v10, v0, %1 offset:-4096 sc1"
                             : : "s"(mnx), "s"((u64)mnx ^ 8192), "{v[4:11]}"(ad), VALL : "memory");
                break;
            }
            if ((++tries & 15) == 0) {
                u32 m = 0;
#pragma nounroll
                for (int i = 0; i < 32; ++i) {
                    u32x4 t;
                    gld16c<0>(t, i < 24 ? memp + 12288 : up, i < 20 ? (4 + i + ((4 + i) & 24)) * 1024 + lanex() * 16 : (i < 24 ? 2048 + (i & 1) * 1024 + (i & 2 ? cob : ad[2]) : (i & 7) * 512 - 512 + (i & 1 ? uob : ad[0]))); waitv<0>(t);
                    m = max3u(max3u(m, t[0], t[1]), t[2], t[3]);
                }
                all = __builtin_amdgcn_ballot_w64(m == 0xffffffffu) == 0;
            }
            phi += (u32)cfg.G;
        }
        asm volatile("s_barrier\n\t" "ds_read_b128 v[112:115], %0\n\t" "ds_read_b128 v[116:119], %0 offset:1024\n\t" "ds_read_b128 v[120:123], %0 offset:2048\n\t" "ds_read_b128 v[124:127], %0 offset:3072\n\t"
                     "ds_read_b128 v[128:131], %0 offset:4096\n\t" "ds_read_b128 v[132:135], %0 offset:5120\n\t" "ds_read_b128 v[136:139], %0 offset:6144\n\t" "ds_read_b128 v[140:143], %0 offset:7168\n\t"
                     "ds_read_b128 v[144:147], %0 offset:8192\n\t" "ds_read_b128 v[148:151], %0 offset:9216\n\t" "ds_read_b128 v[152:155], %0 offset:10240\n\t" "ds_read_b128 v[156:159], %0 offset:11264\n\t"
                     "ds_read_b128 v[160:163], %0 offset:12288\n\t" "ds_read_b128 v[164:167], %0 offset:13312\n\t" "ds_read_b128 v[168:171], %0 offset:14336\n\t" "ds_read_b128 v[172:175], %0 offset:15360\n\t"
                     "ds_read_b128 v[176:179], %0 offset:16384\n\t" "ds_read_b128 v[180:183], %0 offset:17408\n\t" "ds_read_b128 v[184:187], %0 offset:18432\n\t" "ds_read_b128 v[188:191], %0 offset:19456\n\t"
                     "ds_read_b128 v[192:195], %0 offset:20480\n\t" "ds_read_b128 v[196:199], %0 offset:21504\n\t" "ds_read_b128 v[200:203], %0 offset:22528\n\t" "ds_read_b128 v[204:207], %0 offset:23552\n\t"
                     "ds_read_b128 v[208:211], %0 offset:24576\n\t" "ds_read_b128 v[212:215], %0 offset:25600\n\t" "ds_read_b128 v[216:219], %0 offset:26624\n\t" "ds_read_b128 v[220:223], %0 offset:27648\n\t"
                     "ds_read_b128 v[224:227], %0 offset:28672\n\t" "ds_read_b128 v[228:231], %0 offset:29696\n\t" "ds_read_b128 v[232:235], %0 offset:30720\n\t" "ds_read_b128 v[236:239], %0 offset:31744\n\t"
                     : : "v"(lds + lanex() * 16 + ((s + 1) & 1) * 32768), VALL : "memory");
        if (s >= 0) {
            follow(phi, (int)(tt & 0xffffu) + 5 * (v >> 2) / 2 - (cfg.dV >> 16 & 255), (u32)tp8, cfg.G);
            if (vb == NVB - 1 && lane < 8) gst8(fl, (F_CUR + lane) * 128, u32x2{(u32)s - 1, phi + (tt & 0xffffu)});
        }
        phi += tt & 0xffffu;
        memp = mnx; up = ub + uslot(sd, s + 1);
        tt = sched_ld0(s + 1);
        if (s < 0) { phi = go_wait(fl, vb) - (tt >> 16); edge(clk.r1, clk.m1); }
        clk.calib();
        while (s >= 0) {
            u32 h0, h1;
            spin_to(clk.at(phi + (u32)(cfg.dV >> 8 & 255)));
            asm volatile("global_load_dword %0, %1, %2 offset:-4096 sc1" : "=&v"(h1) : "v"(ho & 0xfff), "s"(memp) : "memory");
            spin_to(clk.at(phi + (u32)(cfg.dV & 255)));
            asm volatile("global_load_dword %0, %1, %2 offset:-4096 sc1\n\ts_sleep 5" : "=&v"(h0) : "v"(ho), "s"(memp) : "memory");
            edge(clk.r1, clk.m1);
            asm volatile("s_waitcnt vmcnt(0)" : "+v"(h0), "+v"(h1) : : "memory");
            h0 = max(h0, h1);
            if (__builtin_amdgcn_ballot_w64(h0 == 0xffffffffu) == 0 || --budget < 0) break;
            phi += (u32)cfg.G;
        }
        asm volatile(".Lkm_lv:");
    }
}

DI void xcopy_task(const Ptrs& p, int j, int w) {
    const int tid = 64 * w + lanex();
    Sync y;
    sync_ld(y, p.ws);
    u32x4 xv[10];
    sfor<0, 10>([&](auto I) { gld16<I.v * 128>(xv[I.v], p.x + (size_t)j * 40960, (u32)((tid & 31) * 1280 + (tid >> 5) * 16)); });
    sfor<0, 10>([&](auto I) { waitv<9 - I.v>(xv[I.v]); });
    asm volatile("" : "+v"(y.r), "+v"(y.g), "+v"(y.e));
    sfor<0, 10>([&](auto I) { gst16(p.ws + OFF_XF + (size_t)j * 40960 + I.v * 4096, (u32)tid * 16, xv[I.v]); });
    waitvm<0>();
    barrier();
    const u64 t = sync_tag(y.e);
    if (tid == 0) gst8(p.ws + OFF_LAT, 1536 + (u32)j * 8, u32x2{(u32)t, (u32)(t >> 32)});
}
DI void xcopy_wait(const char* ws, u64 t, int lo = 0, int msk = 63) {
#pragma nounroll
    for (int it = 0; it < (1 << 16); ++it) {
        u32x2 r;
        asm volatile("s_nop 4\n\tglobal_load_dwordx2 %0, %1, %2 offset:1536 sc1\n\ts_waitcnt vmcnt(0)" : "=&v"(r) : "v"((lo + (lanex() & msk)) * 8), "s"(ws + OFF_LAT) : "memory");
        if (__builtin_amdgcn_ballot_w64(r[0] != (u32)t || r[1] != (u32)(t >> 32)) == 0) break;
        __builtin_amdgcn_s_sleep(XSL);
    }
}
DI void init_task(const Ptrs& p, int j, u32 lds, int w) {
    const int lane = lanex(), tid = 64 * w + lane;
    Sync y;
    sync_ld(y, p.ws);
    const u32x4 ff = u32x4{~0u, ~0u, ~0u, ~0u};
    const int i = j * 256 + tid, r = j < 8 ? j & 1 : min(j / 2 - 2, NREG - 1);
    char* const vb = p.ws + (r < 2 ? 0 : OFF_U - (4u << 20)) + ((size_t)r << 21) + 1984 * 1024 + (j < 8 ? j & 6 : (j & 1) * 2 + (j >> 1 == 15) * 4) * 8192;
    const u32 vo = (u32)((w >> 1) * 8192 + (w & 1) * 1024);
    gst16(vb, vo + lane * 16, ff);
    if (i < F_N * 8) gst16(p.ws + OFF_ST, (u32)i * 16, u32x4{i == F_ONE * 8 ? 1000u : ~0u, ~0u, ~0u, ~0u});
    if (i < 512) gst16(p.ws + OFF_GATE, (u32)i * 16, ff);
    gst16(p.ws + 65536, (u32)i * 16, ff);
    if (i < 4096) {
        const int k = (i & 511) + ((i >> 1) & 0x600);
        u32x4 m;
        gld16<0>(m, p.mem0, (u32)((2 * ((k >> 2) & 3) + (k & 1)) * 1280 + min(2 * (k >> 4) + ((k >> 1) & 1), 79) * 16));
        waitv<0>(m);
        gst16(p.ws, (u32)i * 16, k < 640 ? u32x4{min(m[0], 0xfffffffeu), min(m[1], 0xfffffffeu), min(m[2], 0xfffffffeu), min(m[3], 0xfffffffeu)} : ff);
    }
#pragma unroll
    for (int c = 0; c < 6; ++c) gst16(p.ws + OFF_U + c * 131072 - (c == 5) * 122880, (u32)i * 16, ff);
    waitvm<0>();
    asm volatile("" : "+v"(y.r), "+v"(y.g), "+v"(y.e));
    barrier();
    if (w) return;
    u32 dt[2] = {0xffffffu, 0xffffffu};
#pragma unroll
    for (int c = 0; c < 2 * NSAMP; ++c) {
        u32 v;
        const u32 t0 = mt();
        asm volatile("global_load_dword %0, %1, %2 sc1\n\ts_waitcnt vmcnt(0)" : "=&v"(v) : "v"((c & 1) * 8192 + (c >> 1) * 256), "s"(vb) : "memory");
        dt[c & 1] = min(dt[c & 1], mt() - t0);
    }
    const u64 t = sync_tag(y.e);
    const u32 vote = (u32)((dt[0] > dt[1]) ^ xhalf() ^ (int)((vb - p.ws) >> 21)) & 1u;
    if (tid == 0) {
        if (j == 0) gst8(p.ws + OFF_LAT, 512, u32x2{(u32)t, rt()});
        gst8(p.ws + OFF_LAT, (u32)j * 8, u32x2{(u32)t | vote, (u32)(t >> 32)});
    }
}

DI float xmax(float v) {
    const auto a = __builtin_amdgcn_permlane32_swap(__float_as_uint(v), __float_as_uint(v), false, false);
    v = fmaxf(__uint_as_float(a[0]), __uint_as_float(a[1]));
    const auto b = __builtin_amdgcn_permlane16_swap(__float_as_uint(v), __float_as_uint(v), false, false);
    return fmaxf(__uint_as_float(b[0]), __uint_as_float(b[1]));
}
DI float xsum(float v) {
    const auto a = __builtin_amdgcn_permlane32_swap(__float_as_uint(v), __float_as_uint(v), false, false);
    v = __uint_as_float(a[0]) + __uint_as_float(a[1]);
    const auto b = __builtin_amdgcn_permlane16_swap(__float_as_uint(v), __float_as_uint(v), false, false);
    return __uint_as_float(b[0]) + __uint_as_float(b[1]);
}
DI void gst16c(char* sbase, u32 voff, u32x4 v) {
    asm volatile("global_store_dwordx4 %0, %1, %2 sc1\n\ts_nop 1" : : "v"(voff), "v"(v), "s"(sbase) : "memory");
}
#define WAIT6(N, r, o) asm volatile("s_waitcnt vmcnt(" #N ")" : "+v"(r[o]), "+v"(r[o + 1]), "+v"(r[o + 2]), "+v"(r[o + 3]), "+v"(r[o + 4]), "+v"(r[o + 5]) : : "memory")
#define WSLEEP 90
DI void wg_wait(const char* fl, int w, int i0, int t0, int i1, int t1, int i2, int t2, int i3, int t3, int dt, bool acq) {
    if (w == 0) {
        const int lane = lanex(), tg = lane == 1 ? t1 : (lane == 2 ? t2 : (lane == 3 ? t3 : t0));
        const int ix = tg <= 0 ? i0 : (lane == 1 ? i1 : (lane == 2 ? i2 : (lane == 3 ? i3 : i0)));
#pragma nounroll
        for (int it = 0; it < (1 << 14); ++it) {
            u32 v;
            flag_ld(v, fl, ix);
            asm volatile("s_waitcnt vmcnt(0)" : "+v"(v) : : "memory");
            if (__builtin_amdgcn_ballot_w64((int)(v + 1u) < tg) == 0) break;
            const u32 wake = rt() + (u32)(dt + 180 * min(max(t0 - 2 - uni((int)v), 0), 64));
            while ((int)(rt() - wake) < 0) __builtin_amdgcn_s_sleep(4);
        }
        if (acq) acquire();
    }
    barrier();
}
#define TEST 172
DI u32 step_wait(const char* fl, int w, int gr, int t, u32 lds) {
    if (w == 0) {
        u32x2 v;
#pragma nounroll
        for (int it = 0; it < (1 << 14); ++it) {
            asm volatile("s_nop 4\n\tglobal_load_dwordx2 %0, %1, %2 sc1\n\ts_waitcnt vmcnt(0)" : "=&v"(v) : "v"((F_CUR + gr) * 128), "s"(fl) : "memory");
            const int d = t - 1 - uni((int)v[0]);
            if (d <= 0) { v[1] += (u32)(d * TEST); break; }
            const u32 now = rt();
            nap(min(max((int)((u32)uni((int)v[1]) - now) + d * TEST + 10, 40), 2000));
        }
        *LDSP(u32, lds + GRING * GBUF + 4) = v[1];
    }
    barrier();
    return (u32)uni((int)*LDSP(u32, lds + GRING * GBUF + 4));
}
#define PDL 115
#define ODL 240
#define TDT 35
template <bool LATE>
DI void px_wave(const Ptrs& p, const ChainCfg& cfg, int s, int h, int slow, int w, u32 lds, bool keep) {
    const int lane = lanex(), n = lane & 15, g = lane >> 4;
    const char* qb = p.ws + OFF_QX + (size_t)(s * NH + h) * 12288;
    const char* kb = p.ws + OFF_KX + (size_t)(s * NH + h) * 12288;
    const char* mb = p.ws + OFF_KM + (size_t)(s * NH + h) * 3072;
    const u32 mo = (u32)(g * 128 + (n & 7) * 16);
    u32x4 qf[12], kf[12], mf[6];
    sfor<0, 12>([&](auto I) { gld16x<(I.v % 3) * 1024>(qf[I.v], qb + (I.v / 3) * 3072, lane * 16); gap<SMP>(slow); });
    if (!SMP) pace(slow);
    sfor<0, 12>([&](auto I) { if constexpr (LATE || !KNT) gld16c<(I.v % 3) * 1024>(kf[I.v], kb + (I.v / 3) * 3072, lane * 16); else gld16x<(I.v % 3) * 1024>(kf[I.v], kb + (I.v / 3) * 3072, lane * 16); gap<SMP>(slow); });
    if (!SMP) pace(slow);
    if constexpr (LATE) { WAIT6(0, qf, 0); WAIT6(0, qf, 6); WAIT6(0, kf, 0); }
    else {
        sfor<0, 6>([&](auto I) { gld16x<I.v * 512>(mf[I.v], mb, mo); });
        WAIT6(24, qf, 0); WAIT6(18, qf, 6); WAIT6(12, kf, 0);
    }
    f32x4 sc[2][3];
#pragma unroll
    for (int tt = 0; tt < 2; ++tt)
#pragma unroll
        for (int T = 0; T < 3; ++T) sc[tt][T] = f32x4{0.f, 0.f, 0.f, 0.f};
#pragma unroll
    for (int ks = 0; ks < 6; ++ks) { sc[0][0] = mfma16(kf[ks], qf[ks], sc[0][0]); sc[1][0] = mfma16(kf[ks], qf[6 + ks], sc[1][0]); }
    WAIT6(6, kf, 6);
#pragma unroll
    for (int ks = 0; ks < 6; ++ks) sc[1][1] = mfma16(kf[6 + ks], qf[6 + ks], sc[1][1]);
    if constexpr (LATE) {
        until(step_wait(p.ws + OFF_ST, w, h & 7, s - 1, lds) + (u32)(s == NSEG - 1 ? cfg.T : cfg.T + PDX));
        bool ok = false;
#pragma nounroll
        for (int it = 0; it < (1 << 14); ++it) {
            u32 v;
            sfor<0, 6>([&](auto I) { gld16c<I.v * 512>(mf[I.v], mb, mo); });
            flag_ldm(v, p.ws + OFF_ST, (s < NSEG - 2 ? F_CUR : F_PROG + 8 * s) + (h & 7), it > 3);
            WAIT6(0, mf, 0);
            asm volatile("" : "+v"(v));
            u32 m = 0;
#pragma unroll
            for (int i = 0; i < 24; ++i) m = max(m, mf[i >> 2][i & 3]);
            if (ok || __builtin_amdgcn_ballot_w64(m == ~0u) == 0) break;
            ok = it > 3 && uni((int)v) >= (s < NSEG - 2 ? s : 7);
            if (s < NSEG - 1) nap(60);
        }
    }
    WAIT6(0, mf, 0);
#pragma unroll
    for (int ks = 0; ks < 6; ++ks) { sc[0][2] = mfma16(mf[ks], qf[ks], sc[0][2]); sc[1][2] = mfma16(mf[ks], qf[6 + ks], sc[1][2]); }
    const float kscale = 0.07216878364870322f;
    char* pb = p.ws + OFF_PX + (size_t)s * 81920;
#pragma unroll
    for (int tt = 0; tt < 2; ++tt) {
        float v[12], mx = -3.0e38f;
#pragma unroll
        for (int i = 0; i < 12; ++i) {
            const float t = rbf(rbf(sc[tt][i >> 2][i & 3]) * kscale);
            v[i] = (i >= 8 || 4 * g + (i & 3) + 16 * (i >> 2) <= 16 * tt + n) ? t : -3.0e38f;
            mx = fmaxf(mx, v[i]);
        }
        mx = xmax(mx);
        float sx = 0.f, sm = 0.f;
#pragma unroll
        for (int i = 0; i < 12; ++i) {
            v[i] = __builtin_amdgcn_exp2f((v[i] - mx) * 1.4426950408889634f);
            if (i < 8) sx += v[i]; else sm += v[i];
        }
        const float inv = __builtin_amdgcn_rcpf(xsum(sx + 0.5f * sm));
        const u32x4 px = u32x4{cvtpk(v[0] * inv, v[1] * inv), cvtpk(v[2] * inv, v[3] * inv), cvtpk(v[4] * inv, v[5] * inv), cvtpk(v[6] * inv, v[7] * inv)};
        const u32x2 pm = u32x2{cvtpk(v[9] * inv, v[8] * inv), cvtpk(v[11] * inv, v[10] * inv)};
        const int ox = h * 2048 + tt * 1024, om = 65536 + (h >> 2) * 2048 + (h & 3) * 256 + tt * 1024;
        if (!LATE && keep) {
            *LDSP(u32x4, lds + ox + lane * 16) = px;
            *LDSP(u32x2, lds + om + n * 16 + (g & 1) * 8) = pm;
        } else {
            gst16c(pb + ox, lane * 16, px);
            const auto a = __builtin_amdgcn_permlane16_swap(pm[0], pm[0], false, false), b = __builtin_amdgcn_permlane16_swap(pm[1], pm[1], false, false);
            if (g == 0) gst16c(pb + om, (u32)(n * 16), u32x4{a[0], b[0], a[1], b[1]});
        }
    }
}

#define WAIT10(N, r) asm volatile("s_waitcnt vmcnt(" #N ")" : "+v"(r[0]), "+v"(r[1]), "+v"(r[2]), "+v"(r[3]), "+v"(r[4]), "+v"(r[5]), "+v"(r[6]), "+v"(r[7]), "+v"(r[8]), "+v"(r[9]) : : "memory")
DI void out_task(const Ptrs& p, int s, int cg0, int cg1, bool kept, u32 lds, int slow, int w, int sd) {
    u32x4 pf[2][10];
    if (kept) {
        const u32 la = lds + lanex() * 16;
#pragma unroll
        for (int i = 0; i < 20; ++i) pf[i & 1][i >> 1] = ldsr16(la + (i < 16 ? w * 16384 + i * 1024 : 65536 + w * 4096 + (i - 16) * 1024));
    } else {
        const char* pb = p.ws + OFF_PX + (size_t)s * 81920;
        const u32 l16 = lanex() * 16;
        sfor<0, 20>([&](auto I) { gld16c<(I.v & 3) * 1024>(pf[I.v & 1][I.v >> 1], pb + (I.v < 16 ? w * 16384 + (I.v >> 2) * 4096 : 65536 + w * 4096), l16); gap<SMO>(slow); if (!SMO && I.v == 9) pace(slow); });
        WAIT10(0, pf[0]); WAIT10(0, pf[1]);
    }
#pragma nounroll
    for (int cg = cg0; cg < cg1; ++cg) {
    const int lane = lanex(), tid = 64 * w + lane, n = lane & 15, g = lane >> 4;
    const char* db = p.ws + OFF_D + s * D_STRIDE + (size_t)(16 * cg) * 16384 + w * 4096;
    const char* ub = p.ws + uslot(sd, s) + (size_t)(16 * cg) * 4096 + w * 1024;
    const u32 dof = (u32)((n >> 3) * 16384 + g * 128 + (n & 7) * 16), uof = (u32)((n >> 3) * 4096 + g * 128 + (n & 7) * 16);
    u32x4 af[3][10];
    auto aload = [&](u32x4 (&r)[10], int ct) {
        if (!SMO) pace(slow);
        sfor<0, 8>([&](auto I) { gld16x<I.v * 512>(r[I.v], db + ct * 32768, dof); gap<SMO>(slow); });
        gld16x<0>(r[8], ub + uchunk(ct), uof); gap<SMO>(slow); gld16x<512>(r[9], ub + uchunk(ct), uof); gap<SMO>(slow);
    };
    aload(af[0], 0);
    aload(af[1], 1);
    aload(af[2], 2);
    const int tok = 32 * s + (tid >> 3), col = 128 * cg + 8 * (tid & 7);
    u32x4 bb[2]; u32 gv;
    gld16c<0>(bb[0], p.ws + OFF_BOUT, (u32)(s * 40960 + col * 64 + (tid >> 3) * 16)); gld16c<0>(bb[1], p.ws + OFF_BOUT + 4096, (u32)(s * 40960 + col * 64 + (tid >> 3) * 16));
    asm volatile("global_load_dword %0, %1, %2 sc1" : "=v"(gv) : "v"(tok * 4), "s"(p.ws + OFF_GATE) : "memory");
    barrier();
    const u32 wa = lds + (u32)w * 16896 + n * 528 + g * 16;
    sfor<0, 8>([&](auto I) {
        constexpr int ct = I.v;
        u32x4 (&r)[10] = af[ct % 3];
        if constexpr (ct == 0) WAIT10(23, r);
        else if constexpr (ct < 3) WAIT10(23, r);
        else if constexpr (ct < 6) WAIT10(20, r);
        else if constexpr (ct == 6) WAIT10(10, r);
        else WAIT10(0, r);
        f32x4 c0 = f32x4{0.f, 0.f, 0.f, 0.f}, c1 = c0;
#pragma unroll
        for (int i = 0; i < 10; ++i) { c0 = mfma16(r[i], pf[0][i], c0); c1 = mfma16(r[i], pf[1][i], c1); }
        *LDSP(f32x4, wa + ct * 64) = c0;
        *LDSP(f32x4, wa + ct * 64 + 16 * 528) = c1;
        PIN();
        if constexpr (ct + 3 < 8) aload(r, ct + 3);
        PIN();
    });
    asm volatile("" : "+v"(bb[0]), "+v"(bb[1]), "+v"(gv));
    barrier();
    const u32 ra = lds + (u32)(tid >> 3) * 528 + (tid & 7) * 32;
    const float gt = __uint_as_float(gv), g1 = rbf(1.f - gt);
    u32 ov[8];
#pragma unroll
    for (int i = 0; i < 4; ++i) {
        const u32 ri = ra + 16 * (i & 1) + 256 * (i >> 1);
        const f32x4 z = (*LDSP(f32x4, ri) + *LDSP(f32x4, ri + 16896)) + (*LDSP(f32x4, ri + 2 * 16896) + *LDSP(f32x4, ri + 3 * 16896));
#pragma unroll
        for (int j = 0; j < 2; ++j) {
            const u32 b = bb[i >> 1][2 * (i & 1) + j];
            const u32 a = cvtpk(z[2 * j], z[2 * j + 1]);
            const u32 ga = cvtpk(gt * __uint_as_float(a << 16), gt * __uint_as_float(a & 0xffff0000u));
            const u32 gb = cvtpk(g1 * __uint_as_float(b << 16), g1 * __uint_as_float(b & 0xffff0000u));
            ov[2 * i + j] = cvtpk(__uint_as_float(ga << 16) + __uint_as_float(gb << 16), __uint_as_float(ga & 0xffff0000u) + __uint_as_float(gb & 0xffff0000u));
        }
    }
    gst16p(p.out, (u32)(tok * 1280 + col * 2), u32x4{ov[0], ov[1], ov[2], ov[3]});
    gst16p(p.out, (u32)(tok * 1280 + col * 2 + 128), u32x4{ov[4], ov[5], ov[6], ov[7]});
    }
}

DI void out_tail(const Ptrs& p, const ChainCfg& cfg, int s, int cg, u32 lds, int w, int sd, int gr) {
    const int lane = lanex(), tid = 64 * w + lane, n = lane & 15, g = lane >> 4;
    const char* db = p.ws + OFF_D + s * D_STRIDE + (size_t)(4 * cg) * 16384 + w * 4096;
    const char* ub = p.ws + uslot(sd, s) + uchunk(2 * cg) + w * 1024;
    const char* pb = p.ws + OFF_PX + (size_t)s * 81920;
    const u32 dof = (u32)((n >> 3) * 16384 + g * 128 + (n & 7) * 16), uof = (u32)((n >> 3) * 4096 + g * 128 + (n & 7) * 16);
    const int slow = uni(s - TGS);
    u32x4 pf[2][10], af[2][10];
    sfor<0, 16>([&](auto I) { gld16c<(I.v & 7) * 512>(af[I.v >> 3][I.v & 7], db + (I.v >> 3) * 32768, dof); gap<TG1>(-1); });
    const int tok = 32 * s + (tid >> 3), col = 32 * cg + 4 * (tid & 7);
    u32x2 bb; u32 gv;
    asm volatile("global_load_dwordx2 %0, %2, %3 sc1\n\tglobal_load_dword %1, %4, %5 sc1\n\ts_waitcnt vmcnt(0)" : "=&v"(bb), "=&v"(gv)
                 : "v"(s * 40960 + (col >> 3) * 512 + (tid >> 3) * 16 + (col & 7) * 2), "s"(p.ws + OFF_BOUT), "v"(tok * 4), "s"(p.ws + OFF_GATE) : "memory");
    sfor<0, 16>([&](auto I) { waitv<0>(af[I.v >> 3][I.v & 7]); });
    until(step_wait(p.ws + OFF_ST, w, gr, s - 1, lds) + (u32)(s == NSEG - 1 ? cfg.cR : cfg.cR + 2 * PDX));
    const u32 wa = lds + (u32)w * 4608 + n * 144 + g * 16;
#pragma nounroll
    for (int it = 0; it < (1 << 14); ++it) {
        if (it == 4) {
            flag_wait(p.ws + OFF_ST, F_PX + s, 8);
            flag_wait(p.ws + OFF_ST, s < NSEG - 2 ? F_CUR + gr : F_UD + 2 * (s & 1), s < NSEG - 2 ? s + 1 : 16);
            flag_wait(p.ws + OFF_ST, s < NSEG - 2 ? F_ONE : F_UD + 2 * (s & 1) + 1, 16);
        }
        sfor<0, 4>([&](auto I) { gld16c<(I.v & 1) * 512>(af[I.v >> 1][8 + (I.v & 1)], ub + (I.v >> 1) * 16384, uof); });
        sfor<0, 8>([&](auto I) { gld16c<(I.v & 1) * 2048>(pf[0][I.v], pb + w * 16384 + (I.v >> 1) * 4096, lane * 16); gap<TG2>(slow); });
        gld16c<0>(pf[0][8], pb + 65536 + w * 4096, lane * 16); gld16c<2048>(pf[0][9], pb + 65536 + w * 4096, lane * 16);
        sfor<0, 8>([&](auto I) { gld16c<(I.v & 1) * 2048 + 1024>(pf[1][I.v], pb + w * 16384 + (I.v >> 1) * 4096, lane * 16); gap<TG2>(slow); });
        gld16c<1024>(pf[1][8], pb + 65536 + w * 4096, lane * 16); gld16c<3072>(pf[1][9], pb + 65536 + w * 4096, lane * 16);
        WAIT10(10, pf[0]); WAIT10(10, af[0]); WAIT10(10, af[1]);
        float chk = 0.f;
#pragma unroll
        for (int tt = 0; tt < 2; ++tt) {
            if (tt) WAIT10(0, pf[1]);
            f32x4 c0 = f32x4{0.f, 0.f, 0.f, 0.f}, c1 = c0;
#pragma unroll
            for (int i = 0; i < 10; ++i) { c0 = mfma16(af[0][i], pf[tt][i], c0); c1 = mfma16(af[1][i], pf[tt][i], c1); }
            *LDSP(f32x4, wa + tt * 2304) = c0;
            *LDSP(f32x4, wa + tt * 2304 + 64) = c1;
            const f32x4 c = c0 + c1;
            chk += (c[0] + c[1]) + (c[2] + c[3]);
            PIN();
        }
        if (it >= 4 || __builtin_amdgcn_ballot_w64((__float_as_uint(chk) & 0x7fffffffu) > 0x7f800000u) == 0) break;
        if (s < NSEG - 1) nap(60);
    }
    barrier();
    const u32 ra = lds + (u32)(tid >> 3) * 144 + (tid & 7) * 16;
    const float gt = __uint_as_float(gv), g1 = rbf(1.f - gt);
    const f32x4 z = (*LDSP(f32x4, ra) + *LDSP(f32x4, ra + 4608)) + (*LDSP(f32x4, ra + 2 * 4608) + *LDSP(f32x4, ra + 3 * 4608));
    u32x2 ov;
#pragma unroll
    for (int j = 0; j < 2; ++j) {
        const u32 b = bb[j], a = cvtpk(z[2 * j], z[2 * j + 1]);
        const u32 ga = cvtpk(gt * __uint_as_float(a << 16), gt * __uint_as_float(a & 0xffff0000u));
        const u32 gb = cvtpk(g1 * __uint_as_float(b << 16), g1 * __uint_as_float(b & 0xffff0000u));
        ov[j] = cvtpk(__uint_as_float(ga << 16) + __uint_as_float(gb << 16), __uint_as_float(ga & 0xffff0000u) + __uint_as_float(gb & 0xffff0000u));
    }
    *(u32x2*)(p.out + tok * 1280 + col * 2) = ov;
}

DI float dot2bf(u32 a, u32 b, float c) { return __builtin_amdgcn_fdot2_f32_bf16(__builtin_bit_cast(bf16x2, a), __builtin_bit_cast(bf16x2, b), c, false); }
DI void battn_task(const Ptrs& p, int t0, u32 lds, int w) {
    const int lane = lanex(), tid = 64 * w + lane, tl = tid & 31, t = t0 + tl;
    const char* const qkv = p.ws + OFF_QKVB + (size_t)(t0 >> 5) * 122880;
    {
        u32 so[3];
#pragma unroll
        for (int i = 0; i < 3; ++i) {
            const int e = 64 * i + lane, sl = e % 48;
            so[i] = (u32)((e / 48) * 512 + (sl < 16 ? (t0 ? 0 : 122880) + 256 + sl * 16 : 122880 - 256 + sl * 16));
        }
#pragma unroll
        for (int i = 0; i < 30; ++i) { glds16c(qkv - 122880 + 40960 + (10 * w + i / 3) * 2048, so[i % 3], lds + (30 * w + i) * 1024); gap<SMB>(-1); }
    }
    u32x4 q[2][5], xv[10], gv[10];
    sfor<0, 10>([&](auto I) { gld16x<(I.v % 5) * 512>(q[I.v / 5][I.v % 5], qkv + (I.v / 5) * 20480, (u32)((tid >> 5) * 2560 + tl * 16)); gap<SMB>(-1); });
    sfor<0, 10>([&](auto I) { gld16<I.v * 128>(xv[I.v], p.x + (size_t)t0 * 1280, (u32)((tid >> 3) * 1280 + (tid & 7) * 16)); gld16<I.v * 128>(gv[I.v], p.gw, (u32)((tid & 7) * 16)); gap<SMB>(-1); });
    sfor<0, 10>([&](auto I) { waitv<0>(q[I.v / 5][I.v % 5]); waitv<0>(xv[I.v]); waitv<0>(gv[I.v]); });
    barrier();
    {
        float a = 0.f;
#pragma unroll
        for (int i = 0; i < 10; ++i)
#pragma unroll
            for (int j = 0; j < 4; ++j) {
                const u32 r = cvtpk(__uint_as_float(xv[i][j] << 16) * __uint_as_float(gv[i][j] << 16), __uint_as_float(xv[i][j] & 0xffff0000u) * __uint_as_float(gv[i][j] & 0xffff0000u));
                a += __uint_as_float(r << 16) + __uint_as_float(r & 0xffff0000u);
            }
        a += __shfl_xor(a, 1); a += __shfl_xor(a, 2); a += __shfl_xor(a, 4);
        const float z = rbf(a);
        if ((tid & 7) == 0) gst4c(p.ws + OFF_GATE, (u32)((t0 + (tid >> 3)) * 4), __float_as_uint(rbf(1.0f / (1.0f + __expf(-z)))));
    }
#pragma unroll
    for (int it = 0; it < 2; ++it) {
        const int m = (tid >> 5) + 8 * it;
        u32 ka = lds + (u32)(m * 3840 + (2 + tl) * 16), va = ka + 61440;
        asm volatile("" : "+v"(ka), "+v"(va));
        u32x4 k[5][8];
#pragma unroll
        for (int i = 0; i < 40; ++i) k[i >> 3][i & 7] = ldsr16(ka + (i >> 3) * 768 + (7 - (i & 7)) * 32);
        PIN();
        float lg[2][8], mx[2] = {-3.0e38f, -3.0e38f};
#pragma unroll
        for (int i = 0; i < 16; ++i) lg[i >> 3][i & 7] = 0.f;
#pragma unroll
        for (int i = 0; i < 5; ++i) {
#pragma unroll
            for (int j = 0; j < 4; ++j)
#pragma unroll
                for (int wv = 0; wv < 8; ++wv) lg[4 * i + j >= 10][wv] = dot2bf(q[it][i][j], k[i][wv][j], lg[4 * i + j >= 10][wv]);
            PIN();
#pragma unroll
            for (int wv = 0; wv < 8; ++wv) k[i][wv] = ldsr16(va + i * 768 + (7 - wv) * 32);
            PIN();
        }
#pragma unroll
        for (int i = 0; i < 16; ++i) {
            float& l = lg[i >> 3][i & 7];
            l = rbf(rbf(l) * 0.22360679774997896f);
            asm volatile("" : "+v"(l));
            l = t - 2 * (i & 7) >= 0 ? l : -3.0e38f;
            mx[i >> 3] = fmaxf(mx[i >> 3], l);
        }
        u32 pk[2][4];
#pragma unroll
        for (int u = 0; u < 2; ++u) {
            float sum = 0.f;
#pragma unroll
            for (int wv = 0; wv < 8; ++wv) { lg[u][wv] = __builtin_amdgcn_exp2f((lg[u][wv] - mx[u]) * 1.4426950408889634f); sum += lg[u][wv]; }
            const float inv = __builtin_amdgcn_rcpf(sum);
#pragma unroll
            for (int wv = 0; wv < 4; ++wv) pk[u][wv] = cvtpk(lg[u][2 * wv] * inv, lg[u][2 * wv + 1] * inv);
        }
#pragma unroll
        for (int i = 0; i < 5; ++i) {
            float o[8];
#pragma unroll
            for (int d = 0; d < 8; ++d) o[d] = 0.f;
#pragma unroll
            for (int wp = 0; wp < 4; ++wp) {
                const u32x4 v0 = k[i][2 * wp], v1 = k[i][2 * wp + 1];
#pragma unroll
                for (int j = 0; j < 4; ++j) {
                    const u32 pw = pk[4 * i + j >= 10][wp];
                    o[2 * j] = dot2bf(__builtin_amdgcn_perm(v1[j], v0[j], 0x05040100u), pw, o[2 * j]);
                    o[2 * j + 1] = dot2bf(__builtin_amdgcn_perm(v1[j], v0[j], 0x07060302u), pw, o[2 * j + 1]);
                }
            }
            gst16c(p.ws + OFF_OB + (size_t)(t0 >> 5) * 40960 + 512 * i, (u32)(m * 2560 + tl * 16), u32x4{cvtpk(o[0], o[1]), cvtpk(o[2], o[3]), cvtpk(o[4], o[5]), cvtpk(o[6], o[7])});
        }
    }
}

#define XPF 1
#define PFQ 0
#define PFN 8
#define OLAG 3
constexpr int QN = 176, QF = 19;
struct QTab { u16 e[QN]; int n, nf; u32 tt[64]; };
typedef const __attribute__((address_space(4))) int* KI;
constexpr int QT_OFF = (88 + 48) / 4;
DI int pick_task(char* fl, KI k, int gr) {
    u32 r;
    asm volatile("s_mov_b64 exec, 1\n\ts_nop 3\n\tglobal_atomic_add %0, %1, %2, %3 sc0 sc1\n\ts_mov_b64 exec, -1\n\ts_waitcnt vmcnt(0)"
                 : "=&v"(r) : "v"((F_QH + gr) * 128), "v"(1), "s"(fl) : "memory");
    const int x = uni((int)r) + 1 + QF;
    if (x >= k[QT_OFF + QN / 2]) return -1;
    const u32 e2 = (u32)k[QT_OFF + (x >> 1)];
    return (int)(x & 1 ? e2 >> 16 : e2 & 0xffffu);
}
__global__ __launch_bounds__(256, 1) void k_main(Ptrs p0, ChainCfg cfg, QTab tab) {
    extern __shared__ __attribute__((aligned(16))) char smem[];
    u32 lds = (u32)(size_t)LDSP(char, smem);
#define HALF 0
    int n = HALF ? (blockIdx.x & 4) * 32 + (int)(blockIdx.x >> 3) * 4 + (blockIdx.x & 3) : blockIdx.x, gr = n & 7;
    int w = uni(threadIdx.x >> 6);
    {
        u64 pc; int len;
        asm volatile("s_getpc_b64 %0\n.Lkm_first:\n\ts_mov_b32 %1, .Lkm_last-.Lkm_first\n\ts_max_i32 %1, %1, .Lkm_lu-.Lkm_first\n\ts_max_i32 %1, %1, .Lkm_lv-.Lkm_first\n\t"
                     "s_max_i32 %1, %1, .Lkm_g2-.Lkm_first\n\ts_max_i32 %1, %1, .Lkm_g4-.Lkm_first" : "=s"(pc), "=s"(len) : : "scc");
        if (w == 3) pfl((const char*)pc, (u32)min((int)(blockIdx.x >> 3) * 8192 + (int)(threadIdx.x & 63) * 128, max(len, 0)), lds);
    }
    karg_warm();
    if (n < NH) { score_role(p0, cfg, n, lds, w); return; }
    if (n < NS) { proj_role(p0, cfg, n - NH, lds, w); return; }
    const bool isv = n < NS + NVB;
#define qslot (lds + GRING * GBUF)
    int sdw = -1;
    int en = isv ? Q_VD + (n & 24) : tab.e[(n - NS - NVB) >> 3] & 511;
    u32 phi0 = 0;
    Sync y;
    if (XFW && n < 160) {
        const int h = gr + (en & 24);
#pragma unroll
        for (int i = 0; i < XFW; ++i) { const int l = (i * 4 + w) * 64 + lanex(); pfl(((en & 7) == Q_VD ? p0.wv : p0.wk) + h * 384, (u32)((l / 3) * 12288 + (l % 3) * 128), lds); }
        if (w == 1) {
            sync_ld(y, p0.ws);
            waitvm<0>();
            asm volatile("" : "+v"(y.r), "+v"(y.g), "+v"(y.e));
            xcopy_wait(p0.ws, sync_tag(y.e), 0, 3);
        }
        barrier();
    }
    if (!XFW && XPF && n < 160 && w >= 2) pfl(p0.x, (u32)min(((n - NS) >> 3) * 128 + (w & 1) * 64 + lanex(), 1279) * 128, lds);
    if (n >= 160) {
        const int lane = lanex();
        if (n >= 192 && (n & 8)) {
            const int j = (n & 7) + ((n - 192) >> 4) * 8;
            init_task(p0, j, lds, w);
            barrier();
#pragma unroll
            for (int i = 0; i < 3; ++i) pfl(p0.wqkvb, (u32)min(j * 768 + i * 256 + w * 64 + lane, 19199) * 128, lds);
            pfl(j < 25 ? p0.wob : p0.gw, j < 25 ? (u32)(j * 256 + w * 64 + lane) * 128 : (u32)min(lane, 9) * 128, lds);
        } else {
            const int h = n < 192 ? n & 31 : (n & 7) + ((n - 192) >> 4) * 8;
            xcopy_task(p0, h + (n < 192 ? 0 : 32), w);
            if (n < 192) {
#pragma unroll
                for (int i = 0; i < PFN; ++i) {
                    const int l = 64 * min(i * 4 + w, 29) + lane;
                    pfl(p0.wo + (size_t)h * 245760, (u32)(l * 128), lds);
                    if (i >= 8 - PFQ) pfl(p0.wq + h * 384, (u32)((l / 3) * 12288 + (l % 3) * 128), lds);
                }
            }
        }
        if (w == 1) {
            sync_ld(y, p0.ws);
            waitvm<0>();
            asm volatile("" : "+v"(y.r), "+v"(y.g), "+v"(y.e));
            if (n < 192) xcopy_wait(p0.ws, sync_tag(y.e));
            else xcopy_wait(p0.ws, sync_tag(y.e), n & 8 ? 4 : 8, n & 8 ? 3 : 7);
        }
        if (w == 0 && n < 192) {
            sync_ld(y, p0.ws);
            waitvm<0>();
            *LDSP(u32, qslot) = (u32)sync_end(y, p0.ws, phi0, false);
        }
        barrier();
        if (w == 0 && n < 192) flag_add(p0.ws + OFF_ST, F_ACK, 1);
        if (n < 192) sdw = uni((int)*LDSP(u32, qslot));
        barrier();
    }
    while (true) {
        typedef char* const __attribute__((address_space(4)))* KP;
        KP kp = (KP)__builtin_amdgcn_kernarg_segment_ptr();
        asm volatile("" : "+s"(kp), "+s"(lds), "+s"(w), "+s"(gr), "+s"(n));
        __builtin_assume(w >= 0 && w < 4 && gr >= 0 && gr < 8);
        const Ptrs p{kp[0], kp[1], kp[2], kp[3], kp[4], kp[5], kp[6], kp[7], kp[8], kp[9], kp[10]};
        const int ty = en & 7, gate = en >> 9;
        const bool fu = ty == Q_PX && (en >> 3 & 63) < 8;
        const int ix = fu ? (en & 504) + gr : en >> 3 & 63, sf = ((KI)kp)[QT_OFF + QN / 2 + 1] & 255;
        char* const fl = p.ws + OFF_ST;
        const bool raw = sdw < 0;
        int t0 = gate - 1, f1 = F_ONE, t1 = 0, f2 = F_ONE, t2 = 0, f3 = F_ONE, t3 = 0, dt = cfg.T2;
        const bool o2 = ty == Q_OUT && ix < 16;
        const int os = o2 ? sf + 8 * (ix >> 2) + gr : (ix >= 40 ? 44 + (8 * ix + gr) / 20 : (8 * ix + gr) / 5);
        const int bb = ty == Q_BO ? (8 * ix + gr) >> 2 : os;
        if (ty == Q_BA) { t0 += gr * BAS >> 3; f1 = F_B + (gr & RM) | ix << 16; t1 = 5; f2 = F_B + (gr & RM) | max(ix - 1, 0) << 16; t2 = 5; }
        if (ty == Q_BO) { f1 = F_B + (gr & RM) | (8 + bb) << 16; t1 = 8; }
        if (ty == Q_PX) { f1 = F_Q + pblk(ix) * 8 + (fu ? gr & RM : gr) | fu << 16; t1 = fu ? 32 : 4; if (fu) t0 += gr * (((KI)kp)[QT_OFF + QN / 2 + 1] >> 8) >> 3; if (ix < PXT) t0 = max(t0, ix + 1); }
        if (ty == Q_OUT) { f3 = F_B + (gr & RM) | (16 + (os >> 3)) << 16; t3 = 4; if (os < 60) { f1 = F_PX + os; t1 = 8; t0 = max(t0, os + (os < PXT ? OLAG : 1)); if (os >= PXT) dt = TDT; } else { f1 = F_VD + 64 + (gr & RM); t1 = 32; } }
        const bool skip = fu ? ix >= sf : ty == Q_OUT && ix < 40 && !o2 && (os < sf + 8 * NO2 || os >= 60);
        if ((gate || ty >= Q_BA) && !skip) wg_wait(fl, w, F_CUR + gr, t0, f1, t1, f2, t2, f3, t3, dt, false);
        int sig = F_END, nrep = 1;
        if (skip) {}
        else if (ty <= Q_QKVB || ty == Q_BO) {
            const bool ab = ty <= Q_KQ;
            sig = ty == Q_VD ? F_VD + (ix >> 2) * 8 : (ty == Q_KQ ? F_Q + (ix >> 2) * 8 : (ix & 4 ? F_Q : F_K + 8 * ix)) + gr;
            if (!ab) sig = F_B | (bb + (ty == Q_BO ? 16 : 0)) << 16;
            nrep = ty == Q_VD || !ab ? RM + 1 : 1;
            if (ty == Q_VD && ix < 4) { sig += n & 32 ? 4 : 0; nrep = 4; }
            run_gemm(p, ty, ab ? ix >> 2 : bb, ab ? gr + 8 * (ix & 3) : 8 * ix + gr - (ty == Q_BO ? 4 : 5) * bb, (n >> 5) & 1, !XFW && raw && n < 160, y, lds, w);
            if (raw) sdw = sync_end(y, p.ws, phi0, w == 0);
        } else {
            if (ty == Q_BA) {
                battn_task(p, (8 * ix + gr) * 32, lds, w);
                sig = F_B | (8 + ix) << 16; nrep = RM + 1;
            } else if (ty == Q_PX) {
                if (ix < PXT) {
#pragma nounroll
                    for (int i = fu ? 0 : gr; i < (fu ? 8 : gr + 1); ++i) px_wave<false>(p, cfg, ix, i + 8 * w, ix - cfg.fast, w, lds, fu);
                } else px_wave<true>(p, cfg, ix, gr + 8 * w, -1, w, lds, false);
                sig = F_PX + ix;
                if (fu) wg_wait(fl, w, F_ONE, 0, F_B + (gr & RM) | (16 + (ix >> 3)) << 16, 4, F_ONE, 0, F_ONE, 0, dt, false);
            }
            if (ty == Q_OUT || fu) {
                const int cg = 8 * ix + gr - 5 * os;
                if (ix >= 40 && !fu) out_tail(p, cfg, os, 8 * ix + gr - 20 * (os - 44), lds, w, sdw, gr);
                else out_task(p, fu ? ix : os, fu ? 0 : (o2 ? OW * (ix & 3) : cg), fu ? 5 : (o2 ? min(OW * (ix & 3) + OW, 5) : cg + 1), fu, lds, (fu ? ix : os) + 1 - cfg.fast, w, sdw);
                sig = F_END;
            }
            waitvm<0>();
        }
        barrier();
        if (w == 0 && sig != F_END) flag_add(fl, sig, nrep);
        if (w == 1 && sig >= F_Q && sig < F_N) flag_add(fl, sig - gr | 1 << 16, RM + 1);
        if (raw && !isv && w == 0) xcopy_wait(p.ws, sync_tag(y.e));
        if (isv) break;
        if (w == 0) *LDSP(int, qslot) = pick_task(fl, (KI)kp, gr);
        barrier();
        en = uni(*LDSP(int, qslot));
        if (en < 0) return;
        barrier();
    }
    value_role(p0, cfg, n - NS, sdw, phi0, lds, w);
}

#define QCFG 14, 11, 3, 5, 0, 50, 36
#define LVDEF "00?>>=<<;;;<<<<;;::98888999:::::999999:::99888998888888888888886"
constexpr int DX = 280, DUA = 400, DUB = 30, DUC = 2;
static ChainCfg dev_cfg(ChainCfg c) { c.T = PDL; c.cR = ODL; c.s1 = 190 << 8 | 185 << 16; c.T2 = WSLEEP; c.cR2 = DX; c.dU = DUA; c.s2 = DUB; c.T3 = DUC; c.dV |= 4 << 8 | 8 << 16 | 0 << 24; c.cR3 = 0 | 0 << 8 | 0 << 16; return c; }
static QTab set_sched(QTab t, const ChainCfg c, const char* lv) {
    for (int s = 0; s < 64; ++s) {
        const int d = lv[s] - '0';
        t.tt[s] = (u32)((s < c.s1 ? c.T : (s < c.s2 ? c.T2 : c.T3)) + 4 * d) | (u32)((s < c.s1 ? c.cR : (s < c.s2 ? c.cR2 : c.cR3)) + 5 * d / 2) << 16;
    }
    t.tt[0] = (u32)236 | (u32)165 << 16;
    t.tt[1] = (u32)212 | (u32)146 << 16;
    return t;
}
#define QORD "bbV3qbK3s41x40bx51bx60x71s80"
#define GATES "11 13 15 18 25 28 41 43 21 26 36 49 14 18 22 27 33 41"
static QTab make_tab(int lv, int lk, int b0, int d1, int d2, int lg, int nf) {
    QTab t{};
    int n = 0, dl[QN], nq = 0;
    auto add = [&](int ty, int ix) { t.e[n] = (u16)(ty | ix << 3); dl[n++] = ty == Q_VD || ty == Q_KQ ? 8 * (ix / 4 - 1) - (ty == Q_VD ? lv : lk) - (ix >= 32 ? 0 : 0) : 99; };
    for (int i = 1; i < 4; ++i) add(Q_VD, i);
    for (int i = 0; i < 16; ++i) add(i < 4 ? Q_H : ((i & 8) ? Q_VD : Q_KQ), i < 8 ? i : 8 - (i & 1) * 4 + ((i - 8) >> 1));
    for (int i = 0; i < 4; ++i) add(Q_KQ, 8 + i);
    for (const char* c = QORD; *c; ++c) {
        if (*c == 'b') add(Q_QKVB, nq++);
        else if (*c == 'q') for (int i = 4; i < 8; ++i) add(Q_H, i);
        else if (*c == '.') dl[n - 1] |= 1 << 20;
        else if (*c == 'w') { const int b = *++c - '0'; for (int i = 0; i < 16; ++i) add(i & 2 ? Q_KQ : Q_VD, 4 * (b + (i & 1)) + (i >> 2)); }
        else if (*c == 'x') { const int b = c[1] - '0', o = 2 * (c[2] - '0'); c += 2; for (int i = 0; i < 8; ++i) add(i & 2 ? Q_KQ : Q_VD, 4 * (b + (i & 1)) + o + (i >> 2)); }
        else if (*c == 's') { const int b = c[1] - '0', o = 2 * (c[2] - '0'); c += 2; for (int i = 0; i < 4; ++i) add(i & 1 ? Q_KQ : Q_VD, 4 * b + o + (i >> 1)); }
        else if (*c == 'a' || *c == 'c') { add(*c == 'a' ? Q_VD : Q_KQ, 10 * (c[1] - '0') + c[2] - '0'); c += 2; }
        else if (*c == 'n') { const int b = *++c - '0'; for (int i = 0; i < 8; ++i) add(i & 1 ? Q_KQ : Q_VD, 4 * b + (i >> 1) - (i & 1) * 2); }
        else if (*c == 'h' || *c == 'e') { const int b = 4 * (c[1] - '0') + (*c == 'e') * 2; ++c; add(Q_KQ, b); add(Q_KQ, b + 1); }
        else if (*c == 'm') { const int b = c[1] - '0', b2 = c[2] - '0'; c += 2; for (int i = 0; i < 8; ++i) add(i & 1 ? Q_KQ : Q_VD, 4 * (i & 1 ? b2 : b) + ((i >> 1) ^ (i & 2 ? 0 : 0))); }
        else {
            const int ty = (*c | 32) == 'v' ? Q_VD : Q_KQ, two = *c >> 5 & 1, b = *++c - '0';
            for (int i = 0; i < (4 << two); ++i) add(ty, 4 * (b + (i & two)) + (i >> two));
        }
    }
    const int gap = 8;
    for (int i = n - 1, m = 999; i >= 0; --i) { const int pz = dl[i] > 999; m = min(8 * (pz ? dl[i] - (1 << 20) : dl[i]), m - gap - 8 * pz); dl[i] = max(m, 0); }
    auto put = [&](int ty, int ix, int g, int pos) {
        int i = n++;
        for (; dl[i - 1] > pos; --i) { t.e[i] = t.e[i - 1]; dl[i] = dl[i - 1]; }
        t.e[i] = (u16)(ty | ix << 3 | (g + 1) << 9); dl[i] = pos;
    };
    for (int i = 0; i < n; ++i) if (dl[i] >= 8) t.e[i] |= (u16)((dl[i] / 8 + 1) << 9);
    const char* gs = GATES;
    auto gt = [&](int i) { return 10 * (gs[3 * i] - '0') + gs[3 * i + 1] - '0'; };
    for (int i = 0; i < 8; ++i) put(Q_BA, i, gt(i), 8 * gt(i));
    for (int i = 0; i < 4; ++i) put(Q_BO, i, gt(8 + i), 8 * gt(8 + i));
    for (int i = 0; 8 * i < nf; ++i) put(Q_PX, i, gt(12 + i), 8 * (max(gt(12 + i), 8 * i + 1) + b0));
    auto pg = [&](int s) { return max(8 * s + 8, 8 * 46 + (s - nf) * 3); };
    for (int s = nf; s < NSEG; ++s) { const int g = s < PXT ? pg(s) : 8 * max(s - 3, lg); put(Q_PX, s, g / 8, g); }
    for (int k = 0; k < NO2; ++k) for (int c = 0; c * OW < 5; ++c) put(Q_OUT, 4 * k + c, pg(nf + 8 * k + 7) / 8 + OLAG - 1, pg(nf + 8 * k + 7) + 8 * (OLAG - 1));
    for (int x = 5 * (nf + 8 * NO2) / 8; x < 38; ++x) { const int os = (8 * x + 4) / 5, g = pg(min((8 * x + 7) / 5, PXT - 1)); put(Q_OUT, x, g > 8 * ((8 * x + 7) / 5) + 8 ? g / 8 + OLAG - 1 : 0, os < PXT ? pg(os) + 8 * (OLAG - 1) : 8 * (os + 1)); }
    for (int x = 40; x < 50; ++x) { const int g = max(44 + 8 * x / 20 - 3, lg); put(Q_OUT, x, g, 8 * g + 4); }
    t.n = n; t.nf = nf | d1 << 8;
    return t;
}

extern "C" void kernel_launch(void* const* d_in, const int* in_sizes, int n_in, void* d_out, int out_size, void* d_ws,
                              size_t ws_size, hipStream_t stream) {
    Ptrs p{(const char*)d_in[0], (const char*)d_in[1], (const char*)d_in[2], (const char*)d_in[3], (const char*)d_in[4], (const char*)d_in[5],
           (const char*)d_in[8], (const char*)d_in[9], (const char*)d_in[10], (char*)d_out, (char*)(((size_t)d_ws + 0x1fffff) & ~(size_t)0x1fffff)};
    (void)hipFuncSetAttribute((const void*)k_main, hipFuncAttributeMaxDynamicSharedMemorySize, S_LDS);
    k_main<<<256, 256, S_LDS, stream>>>(p, dev_cfg(ChainCfg{CFG}), set_sched(make_tab(QCFG), ChainCfg{CFG}, LVDEF));
    static_assert(sizeof(Ptrs) + sizeof(ChainCfg) + sizeof(QTab) == TT_OFF + 256, "");
}
```
